```python
import torch

def _softmax(x, axis=-1):
    return torch.softmax(x, dim=axis)

def _gelu(x):
    return torch.nn.functional.gelu(x, approximate='tanh')

def _silu(x):
    return torch.nn.functional.silu(x)

def _sigmoid(x):
    return torch.sigmoid(x)

def _layernorm(x, axis=-1, eps=1e-05):
    m = x.mean(dim=axis, keepdim=True)
    v = x.var(dim=axis, keepdim=True, unbiased=False)
    return (x - m) / torch.sqrt(v + eps)

def _scatter_add(tgt, idx, src, axis=0):
    return tgt.clone().index_add_(axis, idx, src)

def _segment_sum(src, seg, n):
    out = torch.zeros((n,) + src.shape[1:], dtype=src.dtype)
    return out.index_add_(0, seg, src)

def _pad(x, pw):
    flat = tuple((v for pair in reversed(pw) for v in pair))
    return torch.nn.functional.pad(x, flat)
kernel_name = 'recurrent_scan__memory_aug_recurrent_mem_tr__mixer_s_mlp__prompt'

def setup_inputs():

    _w = lambda a: (lambda t: t.to(torch.bfloat16) if t.is_floating_point() else t)(torch.as_tensor(a))

    def _A_setup():
        B, S, Dm = (1, 2048, 1024)
        H, Dh = (32, 128)
        M, seg = (8, 32)
        seg = min(seg, S)
        S = S // seg * seg
        return {'x': _w((lambda *s: torch.randn(*s, dtype=torch.bfloat16) * 0.05)(B, S, Dm)), 'mem0': _w((lambda *s: torch.randn(*s, dtype=torch.bfloat16) * 0.2)(B, M, Dm)), 'Wq': _w((lambda *s: torch.randn(*s, dtype=torch.bfloat16) * 1.6)(Dm, H, Dh) * Dm ** (-0.5)), 'Wk': _w((lambda *s: torch.randn(*s, dtype=torch.bfloat16) * 1.6)(Dm, H, Dh) * Dm ** (-0.5)), 'Wv': _w((lambda *s: torch.randn(*s, dtype=torch.bfloat16) * 0.2)(Dm, H, Dh) * Dm ** (-0.5)), 'Wo': _w((lambda *s: torch.randn(*s, dtype=torch.bfloat16) * 0.2)(H, Dh, Dm) * (H * Dh) ** (-0.5)), 'seg_len': seg}
    _A_in = _A_setup()

    def _B_setup():
        B, S, D, F = (1, 2048, 1024, 3584)
        return {'x': _w((lambda *s: torch.randn(*s, dtype=torch.bfloat16) * 0.05)(B, S, D)), 'w1': _w((lambda *s: torch.randn(*s, dtype=torch.bfloat16) * 3.2)(D, D) * D ** (-0.5)), 'w2': _w((lambda *s: torch.randn(*s, dtype=torch.bfloat16) * 3.2)(D, D) * D ** (-0.5)), 'w3': _w((lambda *s: torch.randn(*s, dtype=torch.bfloat16) * 3.2)(D, F) * D ** (-0.5)), 'w4': _w((lambda *s: torch.randn(*s, dtype=torch.bfloat16) * 3.2)(F, D) * F ** (-0.5))}
    _B_in = _B_setup()
    return {'A_x': _A_in['x'], 'A_mem0': _A_in['mem0'], 'A_Wq': _A_in['Wq'], 'A_Wk': _A_in['Wk'], 'A_Wv': _A_in['Wv'], 'A_Wo': _A_in['Wo'], 'A_seg_len': _A_in['seg_len'], 'B_x': _B_in['x'], 'B_w1': _B_in['w1'], 'B_w2': _B_in['w2'], 'B_w3': _B_in['w3'], 'B_w4': _B_in['w4']}

def reference(A_x, A_mem0, A_Wq, A_Wk, A_Wv, A_Wo, A_seg_len, B_x, B_w1, B_w2, B_w3, B_w4):

    def _A_ref(x, mem0, Wq, Wk, Wv, Wo, seg_len):
        B, S, Dm = x.shape
        H, Dh = (Wq.shape[1], Wq.shape[2])
        M = mem0.shape[1]
        scale = Dh ** (-0.5)
        n_seg = S // seg_len
        L = M + seg_len
        idx = torch.arange(L)
        mask = (idx[:, None] < M) | (idx[:, None] >= idx[None, :])
        mem = mem0
        outs = []
        for s in range(n_seg):
            xs = x[:, s * seg_len:(s + 1) * seg_len, :].clone()
            h = torch.concatenate([mem, xs], axis=1)
            q = torch.einsum('btd,dhe->bhte', h, Wq)
            k = torch.einsum('btd,dhe->bhte', h, Wk)
            v = torch.einsum('btd,dhe->bhte', h, Wv)
            scores = torch.einsum('bhqe,bhke->bhqk', q, k) * scale
            scores = torch.where(mask[None, None], scores, -1000000000.0)
            p = _softmax(scores, axis=-1)
            o = torch.einsum('bhqk,bhke->bhqe', p, v)
            o = torch.einsum('bhte,hed->btd', o, Wo)
            mem = o[:, :M, :].clone()
            outs.append(o[:, M:, :])
        return torch.concatenate(outs, axis=1)

    def _B_ref(x, w1, w2, w3, w4):
        B, S, D = x.shape
        h = _gelu(_layernorm(x, axis=-1) @ w1)
        c = D // 4
        g0 = _pad(h[:, :, 0 * c:1 * c], ((0, 0), (1, 0), (0, 0)))[:, :S, :]
        g1 = _pad(h[:, :, 1 * c:2 * c], ((0, 0), (0, 1), (0, 0)))[:, 1:, :]
        g2 = _pad(h[:, :, 2 * c:3 * c], ((0, 0), (2, 0), (0, 0)))[:, :S, :]
        g3 = _pad(h[:, :, 3 * c:], ((0, 0), (0, 2), (0, 0)))[:, 2:, :]
        hs = torch.concatenate([g0, g1, g2, g3], axis=-1) @ w2
        x1 = x + hs
        h2 = _gelu(_layernorm(x1, axis=-1) @ w3) @ w4
        return x1 + h2
    x = A_x
    xn = _layernorm(x, axis=-1)
    s = _A_ref(xn, A_mem0, A_Wq, A_Wk, A_Wv, A_Wo, A_seg_len)
    g = _silu(_B_ref(xn, B_w1, B_w2, B_w3, B_w4))
    return x + s * g
```

```cpp
#include <hip/hip_runtime.h>
#include <hip/hip_bf16.h>
#include <stdint.h>

#define SEQ 2048
#define DM 1024
#define NH 32
#define DH 128
#define HD (NH * DH)
#define MM 8
#define SEG 32
#define NSEG (SEQ / SEG)
#define FF 3584
#define MIX

typedef unsigned short u16;
typedef __attribute__((ext_vector_type(8))) short bf16x8;
typedef __attribute__((ext_vector_type(4))) short bf16x4;
typedef __attribute__((ext_vector_type(4))) float f32x4;
typedef __attribute__((ext_vector_type(4))) unsigned u32x4;
typedef __attribute__((ext_vector_type(2))) unsigned u32x2;
typedef __attribute__((ext_vector_type(2))) float f32x2;
typedef __attribute__((ext_vector_type(2))) __bf16 bf16v2;
typedef __attribute__((ext_vector_type(8))) unsigned u32x8;
#define LAS __attribute__((address_space(3)))
#define GAS __attribute__((address_space(1)))

__device__ __forceinline__ unsigned pack2(float lo, float hi) {
  return __builtin_bit_cast(unsigned, __builtin_convertvector((f32x2){lo, hi}, bf16v2));
}
__device__ __forceinline__ float rb1(float x) { return __uint_as_float(pack2(0.f, x)); }

typedef GAS unsigned gu32;
#define RLX_AGENT __ATOMIC_RELAXED, __HIP_MEMORY_SCOPE_AGENT
#define SPIN_CAP (1u << 20)
#define AUX_SC1 16
#define RSRC_W3 0x00020000
typedef __amdgpu_buffer_rsrc_t rsrc_t;

__device__ __forceinline__ u32x4 ld16(rsrc_t rs, unsigned off) { return __builtin_amdgcn_raw_buffer_load_b128(rs, off, 0, AUX_SC1); }
__device__ __forceinline__ u32x2 ld8(rsrc_t rs, unsigned off) { return __builtin_amdgcn_raw_buffer_load_b64(rs, off, 0, AUX_SC1); }
__device__ __forceinline__ unsigned ld4(rsrc_t rs, unsigned off) { return __builtin_amdgcn_raw_buffer_load_b32(rs, off, 0, AUX_SC1); }
__device__ __forceinline__ void st16(rsrc_t rs, unsigned off, u32x4 v) { __builtin_amdgcn_raw_buffer_store_b128(v, rs, off, 0, AUX_SC1); }
__device__ __forceinline__ void st8(rsrc_t rs, unsigned off, u32x2 v) { __builtin_amdgcn_raw_buffer_store_b64(v, rs, off, 0, AUX_SC1); }
__device__ __forceinline__ void st4(rsrc_t rs, unsigned off, unsigned v) { __builtin_amdgcn_raw_buffer_store_b32(v, rs, off, 0, AUX_SC1); }
__device__ __forceinline__ float lo16(unsigned w) { return __uint_as_float(w << 16); }
__device__ __forceinline__ float hi16(unsigned w) { return __uint_as_float(w & 0xffff0000u); }

__device__ __forceinline__ float xsum16_32(float v) {
  unsigned u = __float_as_uint(v);
  auto a = __builtin_amdgcn_permlane16_swap(u, u, false, false);
  v = __uint_as_float(a[0]) + __uint_as_float(a[1]);
  u = __float_as_uint(v);
  auto b = __builtin_amdgcn_permlane32_swap(u, u, false, false);
  return __uint_as_float(b[0]) + __uint_as_float(b[1]);
}

__device__ __forceinline__ unsigned canon1(unsigned w) { return min(w, 0xfffffffeu); }
__device__ __forceinline__ unsigned max4(u32x4 v) { return max(max(v.x, v.y), max(v.z, v.w)); }
__device__ __forceinline__ int opq(int) { int v; asm volatile("v_mbcnt_lo_u32_b32 %0, -1, 0\n\tv_mbcnt_hi_u32_b32 %0, -1, %0" : "=v"(v)); return v; }
__device__ __forceinline__ u32x4 poison4() { unsigned m = ~0u; asm volatile("" : "+v"(m)); return (u32x4){m, m, m, m}; }
__device__ __forceinline__ f32x4 zero4() { float m = 0.f; asm volatile("" : "+v"(m)); return (f32x4){m, m, m, m}; }
#define MFMA32(a, b, c) __builtin_amdgcn_mfma_f32_16x16x32_bf16(__builtin_bit_cast(bf16x8, a), __builtin_bit_cast(bf16x8, b), c, 0, 0, 0)
#define MFMA16(a, b, c) __builtin_amdgcn_mfma_f32_16x16x16bf16_1k(__builtin_bit_cast(bf16x4, a), __builtin_bit_cast(bf16x4, b), c, 0, 0, 0)
#define LBARRIER() do { asm volatile("s_waitcnt lgkmcnt(0)" ::: "memory"); __builtin_amdgcn_s_barrier(); asm volatile("" ::: "memory"); } while (0)
#define BARRIER() do { asm volatile("" ::: "memory"); __builtin_amdgcn_s_barrier(); asm volatile("" ::: "memory"); } while (0)
#define VMWAIT() asm volatile("s_waitcnt vmcnt(0)" ::: "memory")
#define VMW(n) asm volatile("s_waitcnt vmcnt(" #n ")" ::: "memory")
#define DELAY(n) do { if ((n) > 0) __builtin_amdgcn_s_sleep(n); } while (0)

#define EPO_OFF 0
#define TMO_OFF 128
#define FLG_OFF 256
#define F_LN0(i) (i)
#define F_LN1(i) (64 + (i))
#define F_CAL(i) (128 + (i))
#define F_PRG(t, h) (144 + (t) * 32 + (h))
#define CNT_OFF 65536
#define C_W1(p2) (p2)
#define C_W2(p2) (16 + (p2))
#define C_LN(pm) (32 + (pm))
#define C_W3(pm, hf) (40 + (pm) * 2 + (hf))
#define C_W4(pm) (56 + (pm))
#define C_HEAD 64
#define C_ARR 72
#define C_P4(pm, pc) (96 + (pm) * 4 + (pc))
#define NCNT 128
#define WS_XN (1u << 20)
#define WS_XN2 (WS_XN + (4u << 20))
#define WS_QF (WS_XN2 + (4u << 20))
#define WS_KF (WS_QF + (16u << 20))
#define WS_VF (WS_KF + (16u << 20))
#define WS_OX (WS_VF + (16u << 20))
#define WS_H1 (WS_OX + (16u << 20) + (1u << 20))
#define WS_X1 (WS_H1 + (4u << 20) + 65536u)
#define WS_X1N (WS_X1 + (4u << 20))
#define WS_H3 (WS_X1N + (4u << 20))
#define WS_P4 (WS_H3 + (14u << 20))
#define WS_G (WS_P4 + (8u << 20))
#define WS_PT (WS_G + (4u << 20))
#define WS_COMM (WS_PT + (32u << 20))
#define WS_END (WS_COMM + (4u << 20))
#define CB_STEP 524288u
#define PD 3
__device__ __forceinline__ unsigned comm_base(const char* ws) { return WS_COMM + ((0u - ((unsigned)(size_t)ws + WS_COMM)) & 0x1fffffu); }
__device__ __forceinline__ unsigned step_base(unsigned cb, unsigned mask, int s) { return cb + (unsigned)(s & 3) * CB_STEP + mask; }
#define MF_AT(ks) (((ks) >> 3) * 16384u + ((ks) & 7) * 1024u)
#define OB_AT(r, h) ((2u + (r) * 4u + ((h) >> 3)) * 16384u + ((h) & 7) * 1024u)
#define VB_AT(h) ((10u + ((h) >> 2)) * 16384u + ((h) & 3) * 2048u)
#define XS1 22
#define XS2 44
#define CSL 8
#define FSL 6
#define SQUICK 3
#define NCHUNK 18
#define FL_O(h) (29u * 16384u + (h) * 128u)
#define FL_B(b) (28u * 16384u + (b) * 128u)
#define FL_T(s) FL_O(32 + ((s) & 3))

struct Ptrs { const u16 *x, *mem0, *Wq, *Wk, *Wv, *Wo, *w1, *w2, *w3, *w4; u16* out; char* ws; unsigned tag; };
#define WS_RSRC(P) __builtin_amdgcn_make_buffer_rsrc((void*)(P).ws, 0, WS_END, RSRC_W3)

__device__ __forceinline__ bool spin_fail(unsigned& spins, const Ptrs& P, int lane) {
  asm volatile("" ::: "memory");
  if (++spins > SPIN_CAP) {
    unsigned tv;
    asm volatile("v_mov_b32 %0, %1" : "=v"(tv) : "s"(P.tag));
    if (lane == 0) __builtin_amdgcn_raw_buffer_store_b32(tv, WS_RSRC(P), 0, TMO_OFF, AUX_SC1);
    return true;
  }
  return (spins & 255u) == 0u && (unsigned)__builtin_amdgcn_readfirstlane(__builtin_amdgcn_raw_buffer_load_b32(WS_RSRC(P), 0, TMO_OFF, AUX_SC1)) == P.tag;
}
__device__ __forceinline__ unsigned cnt_load(char* ws, int idx) {
  return __builtin_amdgcn_readfirstlane(__hip_atomic_load((gu32*)(ws + CNT_OFF + idx * 128), RLX_AGENT));
}
__device__ __forceinline__ void cnt_add(char* ws, int idx, int lane) {
  asm volatile("s_waitcnt vmcnt(0)" ::: "memory");
  if (lane == 0) __hip_atomic_fetch_add((gu32*)(ws + CNT_OFF + idx * 128), 1u, RLX_AGENT);
}
__device__ __forceinline__ void cnt_wait(const Ptrs& P, int idx, unsigned want, int lane) {
  for (unsigned spins = 0;;) {
    if (cnt_load(P.ws, idx) >= want) break;
    __builtin_amdgcn_s_sleep(CSL);
    if (spin_fail(spins, P, lane)) break;
  }
}
__device__ __forceinline__ unsigned flag_wait(const Ptrs& P, int f, unsigned want, int lane) {
  const rsrc_t rs = WS_RSRC(P);
  u32x2 v;
  for (unsigned spins = 0;;) {
    v = ld8(rs, FLG_OFF + f * 128);
    if (!__any(v.y != P.tag || v.x < want)) break;
    __builtin_amdgcn_s_sleep(FSL);
    if (spin_fail(spins, P, lane)) break;
  }
  asm volatile("" ::: "memory");
  return v.x;
}
__device__ __forceinline__ void flag_set(const Ptrs& P, int f, unsigned val, int lane) {
  if (lane == 0) st8(WS_RSRC(P), FLG_OFF + f * 128, (u32x2){val, P.tag});
}
__device__ __forceinline__ void get_tag(Ptrs& P) { P.tag = __builtin_amdgcn_readfirstlane(__hip_atomic_load((gu32*)(P.ws + EPO_OFF), RLX_AGENT)) + 1u; }
__device__ __forceinline__ void setup_wait(const Ptrs& P, int lane) { flag_wait(P, F_LN0(lane), 0, lane); }
__device__ __forceinline__ void clock_wait(unsigned tgt) {
  while ((int)(tgt - (unsigned)__builtin_amdgcn_s_memrealtime()) > 0) {}
}
__device__ __forceinline__ void report(const Ptrs& P, int wid) {
  if (wid == 0 && opq(0) == 0) __hip_atomic_fetch_add((gu32*)(P.ws + CNT_OFF + C_ARR * 128), 1u, RLX_AGENT);
}
__device__ __forceinline__ void kv_wait(const Ptrs& P, int s, int h, int& known, int lane) {
  if (s < known) return;
  const unsigned v = flag_wait(P, F_PRG(min(lane, 2), h), s + 1, lane);
  known = __builtin_amdgcn_readfirstlane(min(min(__builtin_amdgcn_readlane(v, 0), __builtin_amdgcn_readlane(v, 1)), __builtin_amdgcn_readlane(v, 2)));
}
__device__ __forceinline__ unsigned mask_wait(const Ptrs& P, int lane) {
  const unsigned v = flag_wait(P, F_CAL(lane & 15), 0, lane);
  return __builtin_popcountll(__ballot(v != 0)) > 32 ? 8192u : 0u;
}
__device__ __forceinline__ void acquire_wait() {
  __builtin_amdgcn_fence(__ATOMIC_ACQUIRE, "agent");
  asm volatile("s_waitcnt vmcnt(0)" ::: "memory");
}
__device__ __forceinline__ void dmaw(rsrc_t rs, unsigned voff, unsigned soff, unsigned ldsaddr) {
  asm volatile("s_mov_b32 m0, %3\n\ts_nop 0\n\tbuffer_load_dwordx4 %0, %1, %2 offen lds" :: "v"(voff), "s"(rs), "s"(soff), "s"(ldsaddr) : "memory");
}
__device__ __forceinline__ void dman(rsrc_t rs, unsigned voff, unsigned soff, unsigned ldsaddr) {
  asm volatile("s_mov_b32 m0, %3\n\ts_nop 0\n\tbuffer_load_dwordx4 %0, %1, %2 offen nt lds" :: "v"(voff), "s"(rs), "s"(soff), "s"(ldsaddr) : "memory");
}
__device__ __forceinline__ void dmas(rsrc_t rs, unsigned voff, unsigned soff, unsigned ldsaddr) {
  asm volatile("s_mov_b32 m0, %3\n\ts_nop 0\n\tbuffer_load_dwordx4 %0, %1, %2 offen sc1 lds" :: "v"(voff), "s"(rs), "s"(soff), "s"(ldsaddr) : "memory");
}
__device__ __forceinline__ void touch(const void* w, unsigned bytes, unsigned voff, LAS unsigned char* lds) {
  asm volatile("s_mov_b32 m0, %2\n\ts_nop 0\n\tbuffer_load_dword %0, %1, 0 offen lds"
               :: "v"(voff), "s"(__builtin_amdgcn_make_buffer_rsrc((void*)w, 0, bytes, RSRC_W3)), "s"((unsigned)(size_t)lds + 148480) : "memory");
}
__device__ __forceinline__ u32x2 trd(LAS unsigned char* p) {
  return __builtin_bit_cast(u32x2, __builtin_amdgcn_ds_read_tr16_b64_v4i16((LAS bf16x4*)p));
}
template <int BO, int UO>
__device__ __forceinline__ u32x8 rd_frag(LAS unsigned char* p) {
  const u32x2 a = trd(p), b = trd(p + UO), c = trd(p + BO), d = trd(p + BO + UO);
  return (u32x8){a.x, a.y, b.x, b.y, c.x, c.y, d.x, d.y};
}

#define TL(i) do {} while (0)
#define TLW(i) do {} while (0)
#define GSDECL
#define GS(i) do {} while (0)
#define GSFLUSH(k, i) do {} while (0)

__device__ __forceinline__ int lds_byte(int r, int c) {
  const int st = (r >> 4) * 2 + (c >> 5), rr = r & 15, cc = c & 31, ob = rr * 64 + cc * 2;
  return st * 1024 + (ob ^ (((ob >> 9) & 1) << 5));
}
#define HSP 3
#define PFD 3
#define PFH 4
struct Pf { unsigned v, step; bool on, a; };
#define G_PF(t) do { if (pf.on && (t) < nt) asm volatile("s_mov_b32 m0, %3\n\ts_nop 0\n\tbuffer_load_dword %0, %1, %2 offen lds" \
    :: "v"(pf.v), "s"(pf.a ? ra : rb), "s"((t) * pf.step), "s"((unsigned)(size_t)lds + 148480) : "memory"); } while (0)

__device__ __forceinline__ void gemm_loop(LAS unsigned char* lds, rsrc_t ra, rsrc_t rb, unsigned cA, unsigned cB, int lda, int ldb, int nt,
                                          const Pf& pf, f32x4 (&acc)[2][2][4][2]) {
  const int wid = __builtin_amdgcn_readfirstlane(threadIdx.x >> 6), lane = opq(0), tid = wid * 64 + lane;
  const int wr = wid >> 2, wc = wid & 3, fr = lane & 15, fq = lane >> 4;
  unsigned voffA[2], voffB[2];
  #pragma unroll
  for (int i = 0; i < 2; ++i) {
    voffA[i] = (unsigned)(((wid >> 1) + 4 * i) * 16 * lda + (wid & 1) * 1024 + lane * 16);
    const int b = tid * 16 + i * 8192, r = b >> 8, pos = (b >> 4) & 15;
    const int c = pos ^ (((r & 3) << 1) | (((r >> 3) & 1) << 3));
    voffB[i] = (unsigned)(r * ldb + c * 16);
  }
  const unsigned hA = 128 * lda;
  const unsigned kB = 64 * ldb;
  const unsigned ldsw = (unsigned)(size_t)lds + (unsigned)wid * 1024u;
  const int aoff = 65536 + lds_byte(wr * 64 + fr, fq * 8);
  const int q = (lane & 15) >> 2, p = lane & 3;
  const unsigned boff0 = (unsigned)(size_t)lds + (8 * fq + q) * 256 + (((wc * 4 + (p >> 1)) ^ ((q << 1) | ((fq & 1) << 3))) << 4) + 8 * (p & 1);
  const unsigned boff1 = boff0 ^ 32;
#define G_SB(b, h) (((b) * 2 + (h)) * 16384)
#define G_SA(b, h) (65536 + ((b) * 2 + (h)) * 16384)
#define G_ST1(bufoff, r, so, voff, i) dmaw(r, (voff)[i], so, ldsw + (bufoff) + (i) * 8192)
#define G_STAGE(bufoff, so, voff) do { G_ST1(bufoff, G_R##voff, so, voff, 0); G_ST1(bufoff, G_R##voff, so, voff, 1); } while (0)
#define G_RvoffA ra
#define G_RvoffB rb
#define G_LDA(dst, b, h) do { _Pragma("unroll") for (int m = 0; m < 4; ++m) _Pragma("unroll") for (int k = 0; k < 2; ++k) \
    dst[m][k] = *(const LAS bf16x8*)(lds + (G_SA(b, h) - 65536) + aoff + m * 2048 + k * 1024); } while (0)
#define G_TR(dst, base, off) asm volatile("ds_read_b64_tr_b16 %0, %1 offset:%2" : "=v"(dst) : "v"(base), "i"(off))
#define G_LDB(dst, b, h) do { _Pragma("unroll") for (int n = 0; n < 2; ++n) _Pragma("unroll") for (int k = 0; k < 2; ++k) { \
    bf16x4 _v0, _v1; \
    G_TR(_v0, (n ? boff1 : boff0), G_SB(b, h) + k * 8192); \
    G_TR(_v1, (n ? boff1 : boff0), G_SB(b, h) + k * 8192 + 1024); \
    dst[n][k] = __builtin_shufflevector(_v0, _v1, 0, 1, 2, 3, 4, 5, 6, 7); } } while (0)
#define G_WAIT_B(Bt) asm volatile("s_waitcnt lgkmcnt(0)" : "+v"(Bt[0][0]), "+v"(Bt[0][1]), "+v"(Bt[1][0]), "+v"(Bt[1][1]) :: "memory")
#define G_MMA(ai, bj, At, Bt) do { __builtin_amdgcn_s_setprio(1); _Pragma("unroll") for (int m = 0; m < 4; ++m) _Pragma("unroll") for (int n = 0; n < 2; ++n) _Pragma("unroll") for (int k = 0; k < 2; ++k) \
    acc[ai][bj][m][n] = __builtin_amdgcn_mfma_f32_16x16x32_bf16(Bt[n][k], At[m][k], acc[ai][bj][m][n], 0, 0, 0); \
    __builtin_amdgcn_s_setprio(0); } while (0)
#define G_WAIT_V(n) asm volatile("s_waitcnt vmcnt(%0)" :: "n"(n) : "memory")
#define G_WAIT_L(n) asm volatile("s_waitcnt lgkmcnt(" #n ")" ::: "memory")
#define G_BAR __builtin_amdgcn_s_barrier()
#define G_SCHED __builtin_amdgcn_sched_barrier(0)
#define G_APTR(t) (cA + (t) * 2048)
  #pragma unroll
  for (int a = 0; a < 2; ++a)
    #pragma unroll
    for (int b = 0; b < 2; ++b)
      #pragma unroll
      for (int m = 0; m < 4; ++m)
        #pragma unroll
        for (int n = 0; n < 2; ++n) acc[a][b][m][n] = (f32x4){0.f, 0.f, 0.f, 0.f};
  bf16x8 At[4][2], B0[2][2], B1[2][2];
  {
    const unsigned a0 = G_APTR(0);
    #pragma unroll
    for (int t = 2; t < PFD; ++t) G_PF(t);
    G_STAGE(G_SB(0, 0), cB, voffB); G_STAGE(G_SA(0, 0), a0, voffA); G_STAGE(G_SB(0, 1), cB + 256, voffB); G_STAGE(G_SA(0, 1), a0 + hA, voffA);
    if (wr == 1) G_BAR;
    G_WAIT_V(4); G_BAR;
    G_STAGE(G_SB(1, 0), cB + kB, voffB); G_STAGE(G_SA(1, 0), a0 + 2048, voffA); G_STAGE(G_SB(1, 1), cB + kB + 256, voffB);
    G_WAIT_V(6); G_BAR;
  }
  for (int t = 0; t < nt; t += 2) {
    const int t2 = (t == nt - 2) ? 0 : t + 2;
    const unsigned a1 = G_APTR(t + 1);
    const unsigned a2 = G_APTR(t2);
    const unsigned b2 = cB + t2 * kB;
    const unsigned a3 = a2 + 2048;
    const unsigned b3 = b2 + kB;
    G_LDB(B0, 0, 0); G_SCHED; G_LDA(At, 0, 0); G_STAGE(G_SA(1, 1), a1 + hA, voffA);
    G_WAIT_L(8); G_BAR; G_WAIT_B(B0); G_MMA(0, 0, At, B0); G_BAR; G_SCHED;
    G_LDB(B1, 0, 1); G_STAGE(G_SB(0, 0), b2, voffB);
    G_BAR; G_WAIT_B(B1); G_MMA(0, 1, At, B1); G_BAR;
    G_LDA(At, 0, 1); G_STAGE(G_SA(0, 0), a2, voffA);
    G_BAR; G_WAIT_L(0); G_MMA(1, 0, At, B0); G_BAR; G_SCHED;
    G_STAGE(G_SB(0, 1), b2 + 256, voffB);
    G_WAIT_V(6); G_PF(t + PFD); G_BAR; G_MMA(1, 1, At, B1); G_BAR;
    G_LDB(B0, 1, 0); G_SCHED; G_LDA(At, 1, 0); G_STAGE(G_SA(0, 1), a2 + hA, voffA);
    G_WAIT_L(8); G_BAR; G_WAIT_B(B0); G_MMA(0, 0, At, B0); G_BAR; G_SCHED;
    G_LDB(B1, 1, 1); G_STAGE(G_SB(1, 0), b3, voffB);
    G_BAR; G_WAIT_B(B1); G_MMA(0, 1, At, B1); G_BAR;
    G_LDA(At, 1, 1); G_STAGE(G_SA(1, 0), a3, voffA);
    G_BAR; G_WAIT_L(0); G_MMA(1, 0, At, B0); G_BAR; G_SCHED;
    G_STAGE(G_SB(1, 1), b3 + 256, voffB);
    G_WAIT_V(6); G_PF(t + PFD + 1); G_BAR; G_MMA(1, 1, At, B1); G_BAR;
  }
  if (wr == 0) G_BAR;
}

__device__ __forceinline__ void gemm_h(LAS unsigned char* lds, rsrc_t ra, rsrc_t rb, unsigned cA, unsigned cB, int lda, int ldb, int nt,
                                       const Pf& pf, f32x4 (&acc)[1][2][4][2]) {
  const int wid = __builtin_amdgcn_readfirstlane(threadIdx.x >> 6), lane = opq(0), tid = wid * 64 + lane;
  const int wr = wid >> 2, wc = wid & 3, fr = lane & 15, fq = lane >> 4;
  unsigned voffA[2], voffB[2];
  #pragma unroll
  for (int i = 0; i < 2; ++i) {
    voffA[i] = (unsigned)(((wid >> 1) + 4 * i) * 16 * lda + (wid & 1) * 1024 + lane * 16);
    const int b = tid * 16 + i * 8192, r = b >> 8, pos = (b >> 4) & 15;
    const int c = pos ^ (((r & 3) << 1) | (((r >> 3) & 1) << 3));
    voffB[i] = (unsigned)(r * ldb + c * 16);
  }
  const unsigned kB = 64 * ldb;
  const unsigned ldsw = (unsigned)(size_t)lds + (unsigned)wid * 1024u;
  const int q = (lane & 15) >> 2, p = lane & 3;
  const int aoffs = 32768 + lds_byte(wr * 64 + fr, fq * 8);
  const unsigned boffs = (unsigned)(size_t)lds + (8 * fq + q) * 256 + (((wc * 4 + (p >> 1)) ^ ((q << 1) | ((fq & 1) << 3))) << 4) + 8 * (p & 1);
  #pragma unroll
  for (int i = 0; i < 16; ++i) acc[0][i >> 3][(i >> 1) & 3][i & 1] = (f32x4){0.f, 0.f, 0.f, 0.f};
  bf16x8 At[4][2], B0[2][2], B1[2][2];
#define H_STAGE(so, t) do { G_STAGE(so, cB + (t) * kB, voffB); G_STAGE((so) + 32768, G_APTR(t), voffA); G_STAGE((so) + 16384, cB + (t) * kB + 256, voffB); } while (0)
  #pragma unroll
  for (int t = 2; t < PFH; ++t) G_PF(t);
  H_STAGE(0, 0); H_STAGE(49152, 1);
  G_WAIT_V(6); G_BAR;
  if (wr == 1) G_BAR;
  int cur = 0, nxt = 98304;
#define H_M4(bj, m, Bt) do { _Pragma("unroll") for (int n = 0; n < 2; ++n) _Pragma("unroll") for (int k = 0; k < 2; ++k) \
    acc[0][bj][m][n] = __builtin_amdgcn_mfma_f32_16x16x32_bf16(Bt[n][k], At[m][k], acc[0][bj][m][n], 0, 0, 0); G_SCHED; } while (0)
  for (int t = 0; t < nt; ++t) {
    const int aoff = aoffs + cur;
    const unsigned boff0 = boffs + cur, boff1 = boff0 ^ 32;
    const int t2 = t + 2 < nt ? t + 2 : 0;
    const unsigned a2 = G_APTR(t2), b2 = cB + t2 * kB;
    G_LDB(B0, 0, 0); G_ST1(nxt, rb, b2, voffB, 0); G_LDA(At, 0, 0); G_SCHED; G_ST1(nxt, rb, b2, voffB, 1); G_LDB(B1, 0, 1); G_SCHED;
    if (HSP > 2) G_ST1(nxt + 32768, ra, a2, voffA, 0);
    G_WAIT_V(HSP); G_WAIT_B(B0); G_WAIT_B(B1); G_BAR; G_SCHED;
    __builtin_amdgcn_s_setprio(1);
    H_M4(0, 0, B0); if (HSP < 3) G_ST1(nxt + 32768, ra, a2, voffA, 0);
    H_M4(0, 1, B0); G_ST1(nxt + 32768, ra, a2, voffA, 1);
    H_M4(0, 2, B0); G_ST1(nxt + 16384, rb, b2 + 256, voffB, 0);
    H_M4(0, 3, B0); G_ST1(nxt + 16384, rb, b2 + 256, voffB, 1);
    H_M4(1, 0, B1); G_PF(t + PFH);
    H_M4(1, 1, B1); H_M4(1, 2, B1); H_M4(1, 3, B1);
    __builtin_amdgcn_s_setprio(0);
    G_BAR; G_SCHED;
    nxt = cur;
    cur = cur == 98304 ? 0 : cur + 49152;
  }
  if (wr == 0) G_BAR;
}

__device__ __forceinline__ u32x4 epi_pack(const f32x4 a0, const f32x4 a1) {
  const unsigned x0 = pack2(a0[0], a0[1]), x1 = pack2(a0[2], a0[3]);
  const unsigned y0 = pack2(a1[0], a1[1]), y1 = pack2(a1[2], a1[3]);
  auto s0 = __builtin_amdgcn_permlane16_swap(x0, y0, false, false);
  auto s1 = __builtin_amdgcn_permlane16_swap(x1, y1, false, false);
  return (u32x4){s0[0], s1[0], s0[1], s1[1]};
}
__device__ __forceinline__ unsigned gelu2(unsigned w) {
  const f32x2 x = {lo16(w), hi16(w)};
  const float c0 = -2.f * 1.4426950408889634f * 0.7978845608028654f, c1 = c0 * 0.044715f;
  const f32x2 a = x * (x * x * c1 + c0);
  const f32x2 d = (f32x2){__builtin_amdgcn_exp2f(a[0]), __builtin_amdgcn_exp2f(a[1])} + 1.f;
  const f32x2 y = x * (f32x2){__builtin_amdgcn_rcpf(d[0]), __builtin_amdgcn_rcpf(d[1])};
  return pack2(y[0], y[1]);
}
__device__ __forceinline__ float silu_f(float b) { return b * __builtin_amdgcn_rcpf(1.f + __expf(-b)); }

__device__ __forceinline__ unsigned blk_in(int r, int c) { const int ob = r * 64 + c * 2; return ob ^ (((ob >> 9) & 1) << 5); }
__device__ __forceinline__ unsigned xn_blk(int rb, int cb) { return (unsigned)(((rb >> 1) * 32 + cb) * 2 + (rb & 1)) * 1024u; }

template <int EPI, int NA>
__device__ __forceinline__ void epi_rows(const f32x4 (&acc)[NA][2][4][2], rsrc_t rs, unsigned obase, int ldc, int pm2, int pn2, int sh, int wr, int wc, int ln) {
  const int fq = ln >> 4, cb = 16 * (fq & 1) + 8 * (fq >> 1), fr = (ln & 15) + sh;
  const unsigned lo = (unsigned)((fr >> 4) * 16 * ldc) + blk_in(fr & 15, cb);
  u32x4 r[8];
  if (EPI == 2) {
    #pragma unroll
    for (int i = 0; i < 8; ++i) r[i] = ld16(rs, WS_XN + xn_blk(pm2 * 8 + 4 * wr + (i >> 1), pn2 * 4 + 4 * (i & 1) + wc) + lo);
  }
  #pragma unroll
  for (int ai = 0; ai < NA; ++ai)
    #pragma unroll
    for (int m = 0; m < 4; ++m)
      #pragma unroll
      for (int bj = 0; bj < 2; ++bj) {
        u32x4 w = epi_pack(acc[ai][bj][m][0], acc[ai][bj][m][1]);
        #pragma unroll
        for (int i = 0; i < 4; ++i) w[i] = EPI < 2 ? gelu2(w[i]) : pack2(lo16(r[2 * m + bj][i]) + lo16(w[i]), hi16(r[2 * m + bj][i]) + hi16(w[i]));
        st16(rs, obase + (unsigned)(pm2 * 128 + 128 * ai + 64 * wr + 16 * m) * (unsigned)ldc + (unsigned)(pn2 * 4 + 4 * bj + wc) * 1024u + lo, w);
      }
}

__device__ __forceinline__ float row_sum16(float v) {
  v += __uint_as_float(__builtin_amdgcn_update_dpp(0, (int)__float_as_uint(v), 0x128, 0xf, 0xf, false));
  v += __uint_as_float(__builtin_amdgcn_update_dpp(0, (int)__float_as_uint(v), 0x124, 0xf, 0xf, false));
  v += __uint_as_float(__builtin_amdgcn_update_dpp(0, (int)__float_as_uint(v), 0x122, 0xf, 0xf, false));
  v += __uint_as_float(__builtin_amdgcn_update_dpp(0, (int)__float_as_uint(v), 0x121, 0xf, 0xf, false));
  return v;
}
__device__ __forceinline__ void ln_rows(u32x4 (&a)[8]) {
  f32x2 v[32];
  #pragma unroll
  for (int i = 0; i < 32; ++i) v[i] = (f32x2){lo16(a[i >> 2][i & 3]), hi16(a[i >> 2][i & 3])};
  f32x2 s[4] = {v[0], v[1], v[2], v[3]};
  #pragma unroll
  for (int i = 4; i < 32; ++i) s[i & 3] += v[i];
  s[0] = (s[0] + s[1]) + (s[2] + s[3]);
  const float mean = row_sum16(s[0][0] + s[0][1]) * (1.f / DM);
  #pragma unroll
  for (int i = 0; i < 32; ++i) { const f32x2 d = v[i] - mean; s[i & 3] = i < 4 ? d * d : s[i & 3] + d * d; }
  s[0] = (s[0] + s[1]) + (s[2] + s[3]);
  const float var = row_sum16(s[0][0] + s[0][1]) * (1.f / DM);
  const float mb = rb1(mean), vb = rb1(var);
  const float rden = __builtin_amdgcn_rcpf(rb1(__builtin_amdgcn_sqrtf(rb1(vb + 1e-5f))));
  #pragma unroll
  for (int i = 0; i < 32; ++i) {
    const f32x2 d = v[i] - mb;
    const unsigned w = pack2(d[0], d[1]);
    const f32x2 y = (f32x2){lo16(w), hi16(w)} * rden;
    a[i >> 2][i & 3] = pack2(y[0], y[1]);
  }
}

template <int N>
__device__ __forceinline__ void fin_out(rsrc_t rs, const Ptrs& P, unsigned po, unsigned go, unsigned eo) {
  u32x4 pv[N][8], gv[N], xv[N];
  #pragma unroll
  for (int c = 0; c < N; ++c) {
    #pragma unroll
    for (int i = 0; i < 8; ++i) pv[c][i] = ld16(rs, po + c * 16384 + (i >> 1) * 131072 + (i & 1) * 1024);
    gv[c] = ld16(rs, go + c * 4096);
    xv[c] = *(const u32x4*)((const char*)P.x + eo + c * 256);
  }
  #pragma unroll
  for (int c = 0; c < N; ++c) {
#define PF4(i) __builtin_bit_cast(f32x4, pv[c][i])
    u32x4 v = epi_pack((PF4(0) + PF4(2)) + (PF4(4) + PF4(6)), (PF4(1) + PF4(3)) + (PF4(5) + PF4(7)));
    #pragma unroll
    for (int k = 0; k < 4; ++k) {
      v[k] = pack2(lo16(v[k]) * lo16(gv[c][k]), hi16(v[k]) * hi16(gv[c][k]));
      v[k] = pack2(lo16(xv[c][k]) + lo16(v[k]), hi16(xv[c][k]) + hi16(v[k]));
    }
    *(u32x4*)((char*)P.out + eo + c * 256) = v;
  }
}

template <int KH>
__device__ __forceinline__ void epi_w4(const f32x4 (&acc)[2][2][4][2], rsrc_t rs, const Ptrs& P, int pm, int pc, int wid, int ln) {
  char* ws = P.ws;
  const int wr = wid >> 2, wc = wid & 3, fr = ln & 15, fq = ln >> 4;
  const unsigned pb = WS_P4 + (unsigned)(pm * 4 + pc) * 262144u + wid * 16384 + ln * 16;
  #pragma unroll
  for (int i = 0; i < 16; ++i) st16(rs, pb + KH * 131072 + i * 1024, __builtin_bit_cast(u32x4, acc[KH ^ 1][(i >> 1) & 1][i >> 2][i & 1]));
  const unsigned eb = (unsigned)(pm * 256 + 128 * KH + 64 * wr) * 2048u + (unsigned)(pc * 8 + wc) * 1024u + blk_in(fr, 16 * (fq & 1) + 8 * (fq >> 1));
  u32x4 pp[16], r1[8];
  #pragma unroll
  for (int i = 0; i < 8; ++i) r1[i] = ld16(rs, WS_X1 + eb + (i >> 1) * 32768 + (i & 1) * 4096);
  VMWAIT();
  BARRIER();
  if (wid == 0) {
    if (ln == 0) __hip_atomic_fetch_add((gu32*)(ws + CNT_OFF + C_P4(pm, pc) * 128), 1u, RLX_AGENT);
    cnt_wait(P, C_P4(pm, pc), 2, ln);
  }
  BARRIER();
  #pragma unroll
  for (int i = 0; i < 16; ++i) pp[i] = ld16(rs, pb + (KH ^ 1) * 131072 + i * 1024);
  asm volatile("" ::: "memory");
  #pragma unroll
  for (int i = 0; i < 8; ++i) {
    u32x4 v = epi_pack(acc[KH][i & 1][i >> 1][0] + __builtin_bit_cast(f32x4, pp[2 * i]), acc[KH][i & 1][i >> 1][1] + __builtin_bit_cast(f32x4, pp[2 * i + 1]));
    #pragma unroll
    for (int k = 0; k < 4; ++k) {
      v[k] = pack2(lo16(r1[i][k]) + lo16(v[k]), hi16(r1[i][k]) + hi16(v[k]));
      v[k] = pack2(silu_f(lo16(v[k])), silu_f(hi16(v[k])));
    }
    st16(rs, WS_G + eb + (i >> 1) * 32768 + (i & 1) * 4096, v);
  }
}

__device__ __forceinline__ void mlp_role(LAS unsigned char* lds, const Ptrs& P, int hf, int slot, int wid) {
  const int wr = wid >> 2, wc = wid & 3;
  char* ws = P.ws;
  const rsrc_t rs = WS_RSRC(P);
#define M_GO() do { if (wid == 0) acquire_wait(); BARRIER(); } while (0)
#define M_DONE(c) do { VMWAIT(); BARRIER(); if (wid == 0) cnt_add(ws, c, opq(0)); } while (0)
  #pragma unroll 1
  for (int code = 0; code < 2; ++code) {
    const int p2 = hf * 8 + (slot >> 2), pn = slot & 3;
    int ln = opq(0);
    if (wid == 0) {
      if (code == 0) flag_wait(P, F_LN1(p2 * 4 + (ln & 3)), 0, ln);
      else {
        if (p2 > 0) cnt_wait(P, C_W1(p2 - 1), 4, ln);
        cnt_wait(P, C_W1(p2), 4, ln);
        if (p2 < 15) cnt_wait(P, C_W1(p2 + 1), 4, ln);
      }
    }
    M_GO();
    TLW(3 * code);
    const rsrc_t rb = __builtin_amdgcn_make_buffer_rsrc((void*)(code ? P.w2 : P.w1), 0, DM * DM * 2, RSRC_W3);
    const unsigned cA = (code ? WS_H1 : WS_XN2) + p2 * 128 * 2048, cB = pn * 512;
    Pf pf;
    pf.on = wc == 0; pf.a = wr;
    ln &= 31;
    pf.v = wr ? cA + (pn * 2 + (ln >> 4)) * 32768 + (ln & 15) * 128 : cB + ((slot >> 2) * 8 + (ln >> 2)) * 2048 + (ln & 3) * 128;
    pf.step = wr ? 2048 : 64 * 2048;
    f32x4 acq[1][2][4][2];
    gemm_h(lds, rs, rb, cA, cB, 2048, 2048, 16, pf, acq);
    TLW(3 * code + 1);
    ln = opq(0);
    if (code == 0) {
      if (wid < 4) touch(P.w2, DM * DM * 2, ((hf * 32 + slot) * 256 + wid * 64 + ln) * 128, lds);
      epi_rows<0, 1>(acq, rs, WS_H1, 2048, p2, 2 * pn, (pn & 1) ? -1 - (pn >> 1) : 1 + (pn >> 1), wr, wc, ln);
      M_DONE(C_W1(p2));
    } else {
      epi_rows<2, 1>(acq, rs, WS_X1, 2048, p2, 2 * pn, 0, wr, wc, ln);
      M_DONE(C_W2(p2));
      if (wid == 0) cnt_wait(P, C_W2(p2), 4, ln);
      BARRIER();
      const unsigned ro = (unsigned)(p2 * 8 + pn * 2 + wr) * 32768u + ((ln >> 2) & 3) * 1024 + blk_in(wc * 4 + (ln >> 4), (ln & 3) * 8);
      u32x4 v[8];
      #pragma unroll
      for (int i = 0; i < 8; ++i) v[i] = ld16(rs, WS_X1 + ro + i * 4096);
      ln_rows(v);
      #pragma unroll
      for (int i = 0; i < 8; ++i) st16(rs, WS_X1N + ro + i * 4096, v[i]);
      M_DONE(C_LN(p2 >> 1));
    }
    TLW(3 * code + 2);
  }
  #pragma unroll 1
  for (int code = 2; code < 5; ++code) {
    const int pm = hf * 4 + (slot & 3), pn = (slot >> 2) + (code == 3 ? 8 : 0);
    int ln = opq(0);
    if (pn >= 14) {
      #pragma unroll 1
      for (int i = 0; i < 7; ++i) {
        touch(P.w4, DM * FF * 2, ((i * 16 + hf * 8 + (slot & 7)) * 512 + wid * 64 + ln) * 128, lds);
        VMWAIT();
        __builtin_amdgcn_s_sleep(64);
      }
      continue;
    }
    if (code != 3) {
      if (wid == 0) cnt_wait(P, code == 2 ? C_LN(pm) : C_W3(pm, pn >> 2), code == 2 ? 8 : 7, ln);
      M_GO();
    }
    TLW(3 * code);
    const bool w4 = code == 4;
    const int lda = w4 ? 7168 : 2048, ldb = w4 ? 2048 : 7168, nt = w4 ? 28 : 16, na = w4 ? 4 : 8;
    const rsrc_t rb = __builtin_amdgcn_make_buffer_rsrc((void*)(w4 ? P.w4 : P.w3), 0, DM * FF * 2, RSRC_W3);
    const unsigned cA = (w4 ? WS_H3 + (pn >> 2) * 28 * 2048 : WS_X1N) + pm * 256 * lda, cB = w4 ? (pn >> 2) * 28 * 64 * 2048 + (pn & 3) * 512 : pn * 512;
    Pf pf;
    pf.on = wc == 0 && (!wr || code != 3); pf.a = wr;
    pf.v = wr ? cA + ((pn & (na - 1)) * (16 / na) + ((ln >> 4) & (16 / na - 1))) * 16 * lda + (ln & 15) * 128 : cB + ((slot & 3) * 16 + (ln >> 2)) * ldb + (ln & 3) * 128;
    pf.step = wr ? 2048 : 64 * ldb;
    f32x4 acc[2][2][4][2];
    gemm_loop(lds, rs, rb, cA, cB, lda, ldb, nt, pf, acc);
    TLW(3 * code + 1);
    ln = opq(0);
    if (!w4) epi_rows<1, 2>(acc, rs, WS_H3, 7168, 2 * pm, 2 * pn, 0, wr, wc, ln);
    else if (pn >> 2) epi_w4<1>(acc, rs, P, pm, pn & 3, wid, ln);
    else epi_w4<0>(acc, rs, P, pm, pn & 3, wid, ln);
    M_DONE(w4 ? C_W4(pm) : C_W3(pm, pn >= 7));
    TLW(3 * code + 2);
  }
}

#define FINE 52
__device__ __forceinline__ void fin_role(LAS unsigned char* lds, const Ptrs& P, int wid) {
  const int wr = wid >> 2, wc = wid & 3;
  const rsrc_t rs = WS_RSRC(P);
  volatile LAS int* tk = (volatile LAS int*)(lds + 147456);
  unsigned tks = 0;
#define POP() do { if (wid == 0 && opq(0) == 0) tks = __hip_atomic_fetch_add((gu32*)(P.ws + CNT_OFF + C_HEAD * 128), 1u, RLX_AGENT); } while (0)
  POP();
  for (int round = 0;; ++round) {
    if (wid == 0 && opq(0) == 0) tk[round & 1] = tks;
    LBARRIER();
    const int u = __builtin_amdgcn_readfirstlane(tk[round & 1]);
    if (u >= 2 * FINE + 8 * (NSEG - FINE)) break;
    const bool big = u < 2 * FINE;
    const int sg = big ? u >> 1 : FINE + ((u - 2 * FINE) >> 3), cc0 = big ? (u & 1) * 4 : (u - 2 * FINE) & 7;
    const int ln = opq(0), fq = ln >> 4, cb = 16 * (fq & 1) + 8 * (fq >> 1);
    const unsigned eo = (unsigned)(sg * 32 + wr * 16 + (ln & 15)) * 2048u + (unsigned)(cc0 * 128 + wc * 32 + cb) * 2u;
    const unsigned go = WS_G + (unsigned)(sg * 2 + wr) * 32768u + (cc0 * 4 + wc) * 1024 + blk_in(ln & 15, cb);
    const unsigned po = WS_PT + (unsigned)sg * 524288u + cc0 * 16384 + wc * 4096 + wr * 2048 + ln * 16;
    POP();
    if (wid == 1) cnt_wait(P, C_W4(sg >> 3), 8, ln);
    if (wid == 2) { const int xg = (sg >= XS1) + (sg >= XS2); flag_wait(P, F_PRG(5 + xg, ln & 31), sg + 1 - (xg == 2 ? XS2 : xg * XS1), ln); }
    BARRIER();
    if (!big) fin_out<1>(rs, P, po, go, eo);
    else { fin_out<2>(rs, P, po, go, eo); fin_out<2>(rs, P, po + 32768, go + 8192, eo + 512); }
  }
}

#define QPRIO 0
#define VPOS 3
#define VLDI "buffer_load_dword v254, v17, %[rs], %[sb] offen sc1\n\tbuffer_load_dword v255, v17, %[rs], %[sb] offen offset:256 sc1\n\t"
#define VLD(p) VLD##p
#define VLD0
#define VLD1
#define VLD2
#define VLD3 VLDI
#define VLD4
#define DEC1 1
#define DEC2 1
#define DGA
#define DGB
#define DGC
#define DGO
#define DGP
#define DGQ
#define PACE "s_sleep 1\n\t"
#define STR_(x) #x
#define STR(x) STR_(x)
#define GSW
#define GSA(k)
#define GSO(k)
#define A_MAIN_Q() \
  asm volatile( \
    "v_mov_b32 v253, %[tag]\n\t" \
    "buffer_load_dwordx4 v[20:23], v12, %[rs], %[vx] offen sc1\n\t" \
    "buffer_load_dwordx4 v[24:27], v12, %[rs], %[vx] offen offset:1024 sc1\n\t" \
    "ds_read_b128 v[236:239], v9 offset:0\n\t" \
    "ds_read_b128 v[240:243], v9 offset:1024\n\t" \
    "ds_read_b128 v[244:247], v9 offset:2048\n\t" \
    "ds_read_b128 v[248:251], v9 offset:3072\n\t" \
    "v_mov_b32 v0, 0\n\t" \
    "v_mov_b32 v1, 0\n\t" \
    "v_mov_b32 v2, 0\n\t" \
    "v_mov_b32 v3, 0\n\t" \
    "v_mov_b32 v4, 0\n\t" \
    "v_mov_b32 v5, 0\n\t" \
    "v_mov_b32 v6, 0\n\t" \
    "v_mov_b32 v7, 0\n\t" \
    "s_cmp_eq_u32 %[sc], 0\n\t" \
    "s_cbranch_scc1 PB_%=\n\t" \
    "TWA_%=:\n\t" \
    "s_memrealtime s[88:89]\n\t" \
    "s_memtime s[90:91]\n\t" \
    "s_waitcnt lgkmcnt(0)\n\t" \
    "s_lshl_b32 s88, s88, 2\n\t" \
    "s_sub_u32 s89, %[tgt], s88\n\t" \
    "s_cmp_gt_i32 s89, 16\n\t" \
    "s_cbranch_scc1 TWA_%=\n\t" \
    "s_mul_i32 s89, s89, 11\n\t" \
    "s_ashr_i32 s89, s89, 1\n\t" \
    "s_add_u32 s89, s89, s90\n\t" \
    DGB \
    "TCA_%=:\n\t" \
    "s_memtime s[92:93]\n\t" \
    "s_waitcnt lgkmcnt(0)\n\t" \
    "s_sub_u32 s93, s89, s92\n\t" \
    "s_cmp_gt_i32 s93, 0\n\t" \
    "s_cbranch_scc1 TCA_%=\n\t" \
    "PB_%=:\n\t" \
    "s_waitcnt lgkmcnt(0)\n\t" \
    "s_barrier\n\t" \
    "buffer_load_dwordx4 v[220:223], v8, %[rs], %[m0] offen sc1\n\t" \
    "buffer_load_dwordx4 v[224:227], v8, %[rs], %[m1] offen sc1\n\t" \
    "PM_%=:\n\t" \
    "s_waitcnt vmcnt(1)\n\t" \
    "ds_write_b128 v8, v[220:223] offset:0\n\t" \
    "v_max3_u32 v252, v220, v221, v222\n\t" \
    "s_waitcnt vmcnt(0)\n\t" \
    "ds_write_b128 v8, v[224:227] offset:4096\n\t" \
    "v_max3_u32 v252, v252, v223, v224\n\t" \
    "v_max3_u32 v252, v252, v225, v226\n\t" \
    "v_max_u32 v252, v252, v227\n\t" \
    "v_cmp_eq_u32 vcc, -1, v252\n\t" \
    "s_cbranch_vccz PD_%=\n\t" \
    "s_sub_u32 %[lim], %[lim], 1\n\t" \
    "s_cmp_gt_i32 %[lim], 0\n\t" \
    "s_cbranch_scc0 PD_%=\n\t" \
    "s_sleep " STR(CADM) "\n\t" \
    "buffer_load_dwordx4 v[220:223], v8, %[rs], %[m0] offen sc1\n\t" \
    "buffer_load_dwordx4 v[224:227], v8, %[rs], %[m1] offen sc1\n\t" \
    "s_branch PM_%=\n\t" \
    "PD_%=:\n\t" \
    GSA(0) \
    "v_smfmac_f32_16x16x64_bf16 v[0:3], v[220:223], v[236:243], v10\n\t" \
    "v_xor_b32 v9, 16384, v9\n\t" \
    "ds_read_b128 v[236:239], v9 offset:0\n\t" \
    "ds_read_b128 v[240:243], v9 offset:1024\n\t" \
    "v_smfmac_f32_16x16x64_bf16 v[4:7], v[220:223], v[244:251], v10\n\t" \
    "ds_read_b128 v[244:247], v9 offset:2048\n\t" \
    "ds_read_b128 v[248:251], v9 offset:3072\n\t" \
    "s_waitcnt lgkmcnt(4)\n\t" \
    "s_barrier\n\t" \
    "ds_read_b128 v[228:231], v8 offset:8192\n\t" \
    "ds_read_b128 v[232:235], v8 offset:12288\n\t" \
    "v_xor_b32 v8, 1024, v8\n\t" \
    "ds_read_b128 v[220:223], v8 offset:0\n\t" \
    "v_smfmac_f32_16x16x64_bf16 v[0:3], v[224:227], v[28:35], v10\n\t" \
    "v_smfmac_f32_16x16x64_bf16 v[4:7], v[224:227], v[36:43], v10\n\t" \
    "ds_read_b128 v[224:227], v8 offset:4096\n\t" \
    "s_waitcnt lgkmcnt(3)\n\t" \
    "v_smfmac_f32_16x16x64_bf16 v[0:3], v[228:231], v[92:99], v10\n\t" \
    "v_smfmac_f32_16x16x64_bf16 v[4:7], v[228:231], v[100:107], v10\n\t" \
    "ds_read_b128 v[228:231], v8 offset:8192\n\t" \
    "s_waitcnt lgkmcnt(3)\n\t" \
    "v_smfmac_f32_16x16x64_bf16 v[0:3], v[232:235], v[156:163], v10\n\t" \
    "v_smfmac_f32_16x16x64_bf16 v[4:7], v[232:235], v[164:171], v10\n\t" \
    "ds_read_b128 v[232:235], v8 offset:12288\n\t" \
    "s_waitcnt lgkmcnt(3)\n\t" \
    "v_smfmac_f32_16x16x64_bf16 v[0:3], v[220:223], v[236:243], v10\n\t" \
    "v_xor_b32 v9, 49152, v9\n\t" \
    "ds_read_b128 v[236:239], v9 offset:0\n\t" \
    "v_smfmac_f32_16x16x64_bf16 v[4:7], v[220:223], v[244:251], v10\n\t" \
    "ds_read_b128 v[240:243], v9 offset:1024\n\t" \
    "s_waitcnt lgkmcnt(4)\n\t" \
    "v_smfmac_f32_16x16x64_bf16 v[0:3], v[224:227], v[44:51], v10\n\t" \
    "v_xor_b32 v8, 3072, v8\n\t" \
    "ds_read_b128 v[220:223], v8 offset:4096\n\t" \
    "v_smfmac_f32_16x16x64_bf16 v[4:7], v[224:227], v[52:59], v10\n\t" \
    "ds_read_b128 v[244:247], v9 offset:2048\n\t" \
    "s_waitcnt lgkmcnt(5)\n\t" \
    "v_smfmac_f32_16x16x64_bf16 v[0:3], v[228:231], v[108:115], v10\n\t" \
    "ds_read_b128 v[224:227], v8 offset:8192\n\t" \
    "v_smfmac_f32_16x16x64_bf16 v[4:7], v[228:231], v[116:123], v10\n\t" \
    "ds_read_b128 v[248:251], v9 offset:3072\n\t" \
    "s_waitcnt lgkmcnt(6)\n\t" \
    "v_smfmac_f32_16x16x64_bf16 v[0:3], v[232:235], v[172:179], v10\n\t" \
    "ds_read_b128 v[228:231], v8 offset:0\n\t" \
    "v_smfmac_f32_16x16x64_bf16 v[4:7], v[232:235], v[180:187], v10\n\t" \
    "ds_read_b128 v[232:235], v8 offset:12288\n\t" \
    "s_waitcnt lgkmcnt(5)\n\t" \
    "v_smfmac_f32_16x16x64_bf16 v[0:3], v[220:223], v[60:67], v10\n\t" \
    "v_smfmac_f32_16x16x64_bf16 v[4:7], v[220:223], v[68:75], v10\n\t" \
    "v_xor_b32 v8, 1024, v8\n\t" \
    "ds_read_b128 v[220:223], v8 offset:4096\n\t" \
    "s_waitcnt lgkmcnt(4)\n\t" \
    "v_smfmac_f32_16x16x64_bf16 v[0:3], v[224:227], v[124:131], v10\n\t" \
    "v_smfmac_f32_16x16x64_bf16 v[4:7], v[224:227], v[132:139], v10\n\t" \
    "ds_read_b128 v[224:227], v8 offset:8192\n\t" \
    "s_waitcnt lgkmcnt(3)\n\t" \
    "v_smfmac_f32_16x16x64_bf16 v[0:3], v[228:231], v[236:243], v10\n\t" \
    "v_xor_b32 v9, 16384, v9\n\t" \
    "ds_read_b128 v[236:239], v9 offset:0\n\t" \
    "v_smfmac_f32_16x16x64_bf16 v[4:7], v[228:231], v[244:251], v10\n\t" \
    "ds_read_b128 v[240:243], v9 offset:1024\n\t" \
    "s_waitcnt lgkmcnt(4)\n\t" \
    "v_smfmac_f32_16x16x64_bf16 v[0:3], v[232:235], v[188:195], v10\n\t" \
    "ds_read_b128 v[228:231], v8 offset:12288\n\t" \
    "v_smfmac_f32_16x16x64_bf16 v[4:7], v[232:235], v[196:203], v10\n\t" \
    "ds_read_b128 v[244:247], v9 offset:2048\n\t" \
    "s_waitcnt lgkmcnt(5)\n\t" \
    "v_smfmac_f32_16x16x64_bf16 v[0:3], v[220:223], v[76:83], v10\n\t" \
    "ds_read_b128 v[232:235], v8 offset:0\n\t" \
    "v_xor_b32 v8, 3072, v8\n\t" \
    "v_smfmac_f32_16x16x64_bf16 v[4:7], v[220:223], v[84:91], v10\n\t" \
    "ds_read_b128 v[248:251], v9 offset:3072\n\t" \
    "s_waitcnt lgkmcnt(6)\n\t" \
    "v_smfmac_f32_16x16x64_bf16 v[0:3], v[224:227], v[140:147], v10\n\t" \
    "ds_read_b128 v[220:223], v12 offset:24576\n\t" \
    "v_smfmac_f32_16x16x64_bf16 v[4:7], v[224:227], v[148:155], v10\n\t" \
    "ds_read_b128 v[224:227], v12 offset:28672\n\t" \
    "s_waitcnt lgkmcnt(5)\n\t" \
    "v_smfmac_f32_16x16x64_bf16 v[0:3], v[228:231], v[204:211], v10\n\t" \
    "v_smfmac_f32_16x16x64_bf16 v[4:7], v[228:231], v[212:219], v10\n\t" \
    "ds_read_b128 v[228:231], v12 offset:25600\n\t" \
    "s_waitcnt lgkmcnt(3)\n\t" \
    "v_smfmac_f32_16x16x64_bf16 v[0:3], v[232:235], v[236:243], v10\n\t" \
    "v_xor_b32 v9, 49152, v9\n\t" \
    "v_smfmac_f32_16x16x64_bf16 v[4:7], v[232:235], v[244:251], v10\n\t" \
    VLD(4) \
    "s_nop 5\n\t" \
    "v_add_f32 v0, v0, v1\n\t" \
    "v_add_f32 v2, v2, v3\n\t" \
    "v_add_f32 v4, v4, v5\n\t" \
    "v_add_f32 v6, v6, v7\n\t" \
    "v_mul_f32 v0, %[sl], v0\n\t" \
    "v_mul_f32 v2, %[sl], v2\n\t" \
    "v_mul_f32 v4, %[sl], v4\n\t" \
    "v_mul_f32 v6, %[sl], v6\n\t" \
    "v_cvt_pk_bf16_f32 v0, v0, v4\n\t" \
    "v_cvt_pk_bf16_f32 v2, v2, v6\n\t" \
    "ds_write2_b32 v11, v0, v2 offset0:0 offset1:4\n\t" \
    "s_mov_b64 exec, %[onej]\n\t" \
    "ds_write_b32 v16, v253\n\t" \
    "s_mov_b64 exec, -1\n\t" \
    GSA(1) \
    "s_setprio " STR(QPRIO) "\n\t" \
    "ds_read_b128 v[232:235], v12 offset:29696\n\t" \
    "s_nop 5\n\t" \
    "ds_read_b128 v[0:3], v12 offset:26624\n\t" \
    "s_nop 5\n\t" \
    "ds_read_b128 v[4:7], v12 offset:27648\n\t" \
    "s_nop 5\n\t" \
    "ds_read_b32 v252, v16\n\t" \
    "ds_read_b128 v[236:239], v14 offset:0\n\t" \
    "s_nop 5\n\t" \
    "ds_read_b128 v[240:243], v14 offset:512\n\t" \
    "s_nop 5\n\t" \
    "ds_read_b128 v[244:247], v14 offset:1024\n\t" \
    "s_nop 5\n\t" \
    VLD(3) \
    "ds_read_b128 v[248:251], v14 offset:1536\n\t" \
    "s_nop 5\n\t" \
    "s_setprio 0\n\t" \
    "s_waitcnt lgkmcnt(4)\n\t" \
    "v_cmp_ne_u32 vcc, v253, v252\n\t" \
    "s_cbranch_vccnz RP_%=\n\t" \
    "CP_%=:\n\t" \
    VLD(0) \
    "s_waitcnt lgkmcnt(2)\n\t" \
    "v_mfma_f32_16x16x32_bf16 v[220:223], v[220:223], v[236:239], 0\n\t" \
    "v_mfma_f32_16x16x32_bf16 v[224:227], v[224:227], v[236:239], 0\n\t" \
    "v_mfma_f32_16x16x32_bf16 v[220:223], v[228:231], v[240:243], v[220:223]\n\t" \
    "ds_read_b128 v[228:231], v12 offset:30720\n\t" \
    "v_mfma_f32_16x16x32_bf16 v[224:227], v[232:235], v[240:243], v[224:227]\n\t" \
    "ds_read_b128 v[232:235], v12 offset:31744\n\t" \
    "s_setprio " STR(QPRIO) "\n\t" \
    VLD(1) \
    "s_waitcnt lgkmcnt(2)\n\t" \
    "v_mfma_f32_16x16x32_bf16 v[220:223], v[0:3], v[244:247], v[220:223]\n\t" \
    "v_mfma_f32_16x16x32_bf16 v[220:223], v[4:7], v[248:251], v[220:223]\n\t" \
    "s_waitcnt lgkmcnt(1)\n\t" \
    "v_mfma_f32_16x16x32_bf16 v[224:227], v[228:231], v[244:247], v[224:227]\n\t" \
    "s_waitcnt lgkmcnt(0)\n\t" \
    "v_mfma_f32_16x16x32_bf16 v[224:227], v[232:235], v[248:251], v[224:227]\n\t" \
    GSA(2) \
    "s_setprio 2\n\t" \
    "ds_read_b32 v252, v16 offset:16\n\t" \
    "ds_read_b128 v[0:3], v15 offset:0\n\t" \
    "ds_read_b128 v[4:7], v15 offset:512\n\t" \
    "ds_read_b128 v[228:231], v15 offset:1024\n\t" \
    "ds_read_b128 v[232:235], v15 offset:1536\n\t" \
    VLD(2) \
    "v_exp_f32 v220, v220\n\t" \
    "v_exp_f32 v221, v221\n\t" \
    "v_exp_f32 v222, v222\n\t" \
    "v_exp_f32 v223, v223\n\t" \
    "v_exp_f32 v224, v224\n\t" \
    "v_exp_f32 v225, v225\n\t" \
    "v_exp_f32 v226, v226\n\t" \
    "v_exp_f32 v227, v227\n\t" \
    "v_cvt_pk_bf16_f32 v220, v220, v221\n\t" \
    "v_cvt_pk_bf16_f32 v221, v222, v223\n\t" \
    "s_waitcnt lgkmcnt(4)\n\t" \
    "v_cmp_ne_u32 vcc, %[tag], v252\n\t" \
    "s_cbranch_vccnz RK_%=\n\t" \
    "CK_%=:\n\t" \
    "s_waitcnt lgkmcnt(3)\n\t" \
    "v_mfma_f32_16x16x32_bf16 v[0:3], v[0:3], v[236:239], 0\n\t" \
    "v_cvt_pk_bf16_f32 v222, v224, v225\n\t" \
    "v_cvt_pk_bf16_f32 v223, v226, v227\n\t" \
    "s_waitcnt lgkmcnt(2)\n\t" \
    "v_mfma_f32_16x16x32_bf16 v[0:3], v[4:7], v[240:243], v[0:3]\n\t" \
    "v_mov_b32 v224, %[one]\n\t" \
    "v_mov_b32 v225, %[one]\n\t" \
    "s_waitcnt lgkmcnt(1)\n\t" \
    "v_mfma_f32_16x16x32_bf16 v[0:3], v[228:231], v[244:247], v[0:3]\n\t" \
    "v_mov_b32 v226, %[one]\n\t" \
    "v_mov_b32 v227, %[one]\n\t" \
    "s_waitcnt lgkmcnt(0)\n\t" \
    "v_mfma_f32_16x16x32_bf16 v[0:3], v[232:235], v[248:251], v[0:3]\n\t" \
    "v_mfma_f32_16x16x32_bf16 v[244:247], v[224:227], v[220:223], 0\n\t" \
    "v_mov_b32 v229, 0\n\t" \
    "v_mfma_f32_16x16x32_bf16 v[236:239], v[20:23], v[220:223], 0\n\t" \
    "v_mov_b32 v230, 0\n\t" \
    "v_mfma_f32_16x16x32_bf16 v[240:243], v[24:27], v[220:223], 0\n\t" \
    "s_nop 2\n\t" \
    "v_exp_f32 v0, v0\n\t" \
    "v_exp_f32 v1, v1\n\t" \
    "s_nop 0\n\t" \
    "v_cvt_pk_bf16_f32 v228, v0, v1\n\t" \
    "v_cvt_pk_bf16_f32 v231, v0, v1\n\t" \
    GSA(3) \
    "CM_%=:\n\t" \
    "s_nop 0\n\t" \
    "v_mfma_f32_16x16x16_bf16 v[244:247], v[224:225], v[228:229], v[244:247]\n\t" \
    "s_waitcnt vmcnt(0)\n\t" \
    "v_mfma_f32_16x16x16_bf16 v[236:239], v[254:255], v[228:229], v[236:239]\n\t" \
    "v_max_u32 v7, v254, v255\n\t" \
    "v_mfma_f32_16x16x16_bf16 v[240:243], v[254:255], v[230:231], v[240:243]\n\t" \
    "v_cmp_eq_u32 s[88:89], -1, v7\n\t" \
    "s_not_b64 exec, %[hi]\n\t" \
    "s_nop 1\n\t" \
    "v_rcp_f32 v2, v244\n\t" \
    "v_subrev_u32 v253, 0x21800000, v244\n\t" \
    "v_cmp_gt_u32 vcc, v253, %[rg]\n\t" \
    "v_pk_mul_f32 v[236:237], v[236:237], v[2:3] op_sel_hi:[1,0]\n\t" \
    "v_pk_mul_f32 v[238:239], v[238:239], v[2:3] op_sel_hi:[1,0]\n\t" \
    "v_pk_mul_f32 v[240:241], v[240:241], v[2:3] op_sel_hi:[1,0]\n\t" \
    "v_pk_mul_f32 v[242:243], v[242:243], v[2:3] op_sel_hi:[1,0]\n\t" \
    "s_or_b64 vcc, vcc, s[88:89]\n\t" \
    "v_cvt_pk_bf16_f32 v4, v236, v237\n\t" \
    "v_cvt_pk_bf16_f32 v5, v238, v239\n\t" \
    "v_cvt_pk_bf16_f32 v6, v240, v241\n\t" \
    "v_cvt_pk_bf16_f32 v7, v242, v243\n\t" \
    "s_cbranch_scc1 FX_%=\n\t" \
    "buffer_store_dwordx4 v[4:7], v18, %[rs], %[sb] offen sc1\n\t" \
    "s_memrealtime %[tp]\n\t" \
    DGA \
    GSA(4) \
    "v_max3_u32 v252, v4, v5, v6\n\t" \
    "v_max_u32 v252, v252, v7\n\t" \
    "v_cmp_eq_u32 vcc, -1, v252\n\t" \
    "s_cbranch_vccnz RC_%=\n\t" \
    "CC_%=:\n\t" \
    "s_mov_b64 exec, -1\n\t" \
    "v_add_u32 v252, %[vd], v12\n\t" \
    "v_add_u32 v253, %[md], v17\n\t" \
    "ds_write_b128 v252, v[20:23]\n\t" \
    "ds_write_b128 v252, v[24:27] offset:1024\n\t" \
    "ds_write2st64_b32 v253, v254, v255 offset0:0 offset1:1\n\t" \
    "s_waitcnt lgkmcnt(0)\n\t" \
    "s_cmp_eq_u32 %[sc], 0\n\t" \
    "s_cbranch_scc1 NH_%=\n\t" \
    "buffer_load_dwordx2 v[0:1], off, %[rs], %[fl] sc1\n\t" \
    "s_barrier\n\t" \
    "s_waitcnt vmcnt(0)\n\t" \
    "v_readfirstlane_b32 %[pt], v0\n\t" \
    "v_readfirstlane_b32 %[pd], v1\n\t" \
    "s_nop 3\n\t" \
    "s_branch E_%=\n\t" \
    "NH_%=:\n\t" \
    "s_barrier\n\t" \
    "s_branch E_%=\n\t" \
    "RP_%=:\n\t" \
    "s_waitcnt lgkmcnt(0)\n\t" \
    "ds_read_b32 v252, v16\n\t" \
    "ds_read_b128 v[236:239], v14 offset:0\n\t" \
    "ds_read_b128 v[240:243], v14 offset:512\n\t" \
    "ds_read_b128 v[244:247], v14 offset:1024\n\t" \
    "ds_read_b128 v[248:251], v14 offset:1536\n\t" \
    "s_sub_u32 %[lim], %[lim], " STR(DEC1) "\n\t" \
    "s_cmp_gt_i32 %[lim], 0\n\t" \
    "s_waitcnt lgkmcnt(4)\n\t" \
    "s_cbranch_scc0 CP_%=\n\t" \
    "v_cmp_ne_u32 vcc, %[tag], v252\n\t" \
    "s_cbranch_vccnz RP_%=\n\t" \
    "s_branch CP_%=\n\t" \
    "RK_%=:\n\t" \
    "s_waitcnt lgkmcnt(0)\n\t" \
    "ds_read_b32 v252, v16 offset:16\n\t" \
    "ds_read_b128 v[0:3], v15 offset:0\n\t" \
    "ds_read_b128 v[4:7], v15 offset:512\n\t" \
    "ds_read_b128 v[228:231], v15 offset:1024\n\t" \
    "ds_read_b128 v[232:235], v15 offset:1536\n\t" \
    "s_sub_u32 %[lim], %[lim], " STR(DEC1) "\n\t" \
    "s_cmp_gt_i32 %[lim], 0\n\t" \
    "s_waitcnt lgkmcnt(4)\n\t" \
    "s_cbranch_scc0 CK_%=\n\t" \
    "v_cmp_ne_u32 vcc, %[tag], v252\n\t" \
    "s_cbranch_vccnz RK_%=\n\t" \
    "s_branch CK_%=\n\t" \
    "RC_%=:\n\t" \
    "v_min_u32 v4, -2, v4\n\t" \
    "v_min_u32 v5, -2, v5\n\t" \
    "v_min_u32 v6, -2, v6\n\t" \
    "v_min_u32 v7, -2, v7\n\t" \
    "buffer_store_dwordx4 v[4:7], v18, %[rs], %[sb] offen sc1\n\t" \
    "s_branch CC_%=\n\t" \
    "FX_%=:\n\t" \
    "s_mov_b64 exec, -1\n\t" \
    "s_cmp_eq_u64 s[88:89], 0\n\t" \
    "s_cbranch_scc1 RM_%=\n\t" \
    "buffer_load_dword v254, v17, %[rs], %[sb] offen sc1\n\t" \
    "buffer_load_dword v255, v17, %[rs], %[sb] offen offset:256 sc1\n\t" \
    "v_mfma_f32_16x16x32_bf16 v[244:247], v[224:227], v[220:223], 0\n\t" \
    "v_mfma_f32_16x16x32_bf16 v[236:239], v[20:23], v[220:223], 0\n\t" \
    "v_mfma_f32_16x16x32_bf16 v[240:243], v[24:27], v[220:223], 0\n\t" \
    "s_sub_u32 %[lim], %[lim], " STR(DEC2) "\n\t" \
    "s_cmp_gt_i32 %[lim], 0\n\t" \
    "s_cbranch_scc1 CM_%=\n\t" \
    "s_waitcnt vmcnt(0)\n\t" \
    "v_mov_b32 v254, 0\n\t" \
    "v_mov_b32 v255, 0\n\t" \
    "s_branch CM_%=\n\t" \
    "RM_%=:\n\t" \
    "s_mov_b32 %[rg], -1\n\t" \
    "s_nop 7\n\t" \
    "ds_read_b128 v[236:239], v14 offset:0\n\t" \
    "ds_read_b128 v[240:243], v14 offset:512\n\t" \
    "ds_read_b128 v[244:247], v14 offset:1024\n\t" \
    "ds_read_b128 v[248:251], v14 offset:1536\n\t" \
    "ds_read_b128 v[232:235], v12 offset:24576\n\t" \
    "s_waitcnt lgkmcnt(0)\n\t" \
    "v_mfma_f32_16x16x32_bf16 v[220:223], v[232:235], v[236:239], 0\n\t" \
    "ds_read_b128 v[232:235], v12 offset:25600\n\t" \
    "s_waitcnt lgkmcnt(0)\n\t" \
    "v_mfma_f32_16x16x32_bf16 v[220:223], v[232:235], v[240:243], v[220:223]\n\t" \
    "ds_read_b128 v[232:235], v12 offset:26624\n\t" \
    "s_waitcnt lgkmcnt(0)\n\t" \
    "v_mfma_f32_16x16x32_bf16 v[220:223], v[232:235], v[244:247], v[220:223]\n\t" \
    "ds_read_b128 v[232:235], v12 offset:27648\n\t" \
    "s_waitcnt lgkmcnt(0)\n\t" \
    "v_mfma_f32_16x16x32_bf16 v[220:223], v[232:235], v[248:251], v[220:223]\n\t" \
    "ds_read_b128 v[232:235], v12 offset:28672\n\t" \
    "s_waitcnt lgkmcnt(0)\n\t" \
    "v_mfma_f32_16x16x32_bf16 v[224:227], v[232:235], v[236:239], 0\n\t" \
    "ds_read_b128 v[232:235], v12 offset:29696\n\t" \
    "s_waitcnt lgkmcnt(0)\n\t" \
    "v_mfma_f32_16x16x32_bf16 v[224:227], v[232:235], v[240:243], v[224:227]\n\t" \
    "ds_read_b128 v[232:235], v12 offset:30720\n\t" \
    "s_waitcnt lgkmcnt(0)\n\t" \
    "v_mfma_f32_16x16x32_bf16 v[224:227], v[232:235], v[244:247], v[224:227]\n\t" \
    "ds_read_b128 v[232:235], v12 offset:31744\n\t" \
    "s_waitcnt lgkmcnt(0)\n\t" \
    "v_mfma_f32_16x16x32_bf16 v[224:227], v[232:235], v[248:251], v[224:227]\n\t" \
    "ds_read_b128 v[232:235], v15 offset:0\n\t" \
    "s_waitcnt lgkmcnt(0)\n\t" \
    "v_mfma_f32_16x16x32_bf16 v[0:3], v[232:235], v[236:239], 0\n\t" \
    "ds_read_b128 v[232:235], v15 offset:512\n\t" \
    "s_waitcnt lgkmcnt(0)\n\t" \
    "v_mfma_f32_16x16x32_bf16 v[0:3], v[232:235], v[240:243], v[0:3]\n\t" \
    "ds_read_b128 v[232:235], v15 offset:1024\n\t" \
    "s_waitcnt lgkmcnt(0)\n\t" \
    "v_mfma_f32_16x16x32_bf16 v[0:3], v[232:235], v[244:247], v[0:3]\n\t" \
    "ds_read_b128 v[232:235], v15 offset:1536\n\t" \
    "s_waitcnt lgkmcnt(0)\n\t" \
    "v_mfma_f32_16x16x32_bf16 v[0:3], v[232:235], v[248:251], v[0:3]\n\t" \
    "v_max3_f32 v252, v220, v221, v222\n\t" \
    "v_max3_f32 v253, v223, v224, v225\n\t" \
    "v_max3_f32 v252, v252, v226, v227\n\t" \
    "s_nop 4\n\t" \
    "v_max3_f32 v253, v253, v0, v1\n\t" \
    "v_max_f32 v252, v252, v253\n\t" \
    "v_mov_b32 v253, v252\n\t" \
    "v_mov_b32 v2, v252\n\t" \
    "s_nop 1\n\t" \
    "v_permlane16_swap_b32 v253, v2\n\t" \
    "v_max_f32 v252, v253, v2\n\t" \
    "v_max_f32 v253, v253, v2\n\t" \
    "s_nop 1\n\t" \
    "v_permlane32_swap_b32 v252, v253\n\t" \
    "v_max_f32 v252, v252, v253\n\t" \
    "v_sub_f32 v220, v220, v252\n\t" \
    "v_sub_f32 v221, v221, v252\n\t" \
    "v_sub_f32 v222, v222, v252\n\t" \
    "v_sub_f32 v223, v223, v252\n\t" \
    "v_sub_f32 v224, v224, v252\n\t" \
    "v_sub_f32 v225, v225, v252\n\t" \
    "v_sub_f32 v226, v226, v252\n\t" \
    "v_sub_f32 v227, v227, v252\n\t" \
    "v_sub_f32 v0, v0, v252\n\t" \
    "v_sub_f32 v1, v1, v252\n\t" \
    "v_exp_f32 v220, v220\n\t" \
    "v_exp_f32 v221, v221\n\t" \
    "v_exp_f32 v222, v222\n\t" \
    "v_exp_f32 v223, v223\n\t" \
    "v_exp_f32 v224, v224\n\t" \
    "v_exp_f32 v225, v225\n\t" \
    "v_exp_f32 v226, v226\n\t" \
    "v_exp_f32 v227, v227\n\t" \
    "v_cvt_pk_bf16_f32 v220, v220, v221\n\t" \
    "v_cvt_pk_bf16_f32 v221, v222, v223\n\t" \
    "v_cvt_pk_bf16_f32 v222, v224, v225\n\t" \
    "v_cvt_pk_bf16_f32 v223, v226, v227\n\t" \
    "v_mov_b32 v224, %[one]\n\t" \
    "v_mov_b32 v225, %[one]\n\t" \
    "v_mov_b32 v226, %[one]\n\t" \
    "v_mov_b32 v227, %[one]\n\t" \
    "s_nop 1\n\t" \
    "v_mfma_f32_16x16x32_bf16 v[244:247], v[224:227], v[220:223], 0\n\t" \
    "v_mfma_f32_16x16x32_bf16 v[236:239], v[20:23], v[220:223], 0\n\t" \
    "v_mfma_f32_16x16x32_bf16 v[240:243], v[24:27], v[220:223], 0\n\t" \
    "v_exp_f32 v0, v0\n\t" \
    "v_exp_f32 v1, v1\n\t" \
    "s_nop 0\n\t" \
    "v_cvt_pk_bf16_f32 v228, v0, v1\n\t" \
    "v_cvt_pk_bf16_f32 v231, v0, v1\n\t" \
    "s_nop 1\n\t" \
    "s_branch CM_%=\n\t" \
    "E_%=:\n\t" \
    GSW \
    "s_nop 1\n\t" \
    : [lim] "+s"(lim), [rg] "+s"(rg), [tp] "=&s"(tp), [pt] "=&s"(pt), [pd] "=&s"(pd) DGO DGP GSO(0) GSO(1) GSO(2) GSO(3) GSO(4) \
    : "{v8}"(am_a), "{v9}"(t0_a), "{v10}"(idx), "{v11}"(iw_a), "{v12}"(ab_a), "{v14}"(qr_a), "{v15}"(kr_a), "{v16}"(fr_a), "{v17}"(vo_v), "{v18}"(oo_v), [rs] "s"(rs), [sb] "s"(sb), [tag] "s"(0xffffff81u + s), [hi] "s"(0xff00ff00ff00ff00ull), [tgt] "s"(tgt), [sc] "s"(wid / 3), [onej] "s"(onej), [sl] "s"(0x3e0293ee), [one] "s"(0x3f803f80), [m0] "s"(sbm), [m1] "s"(sbm + 4096u), [vx] "s"(vxo), [fl] "s"(fb + FL_T(s + 1)), [vd] "s"(8192 + j * 2048), [md] "s"(md), "{v[28:35]}"(wr[0][0]), "{v[36:43]}"(wr[0][1]), "{v[44:51]}"(wr[1][0]), "{v[52:59]}"(wr[1][1]), "{v[60:67]}"(wr[2][0]), "{v[68:75]}"(wr[2][1]), "{v[76:83]}"(wr[3][0]), "{v[84:91]}"(wr[3][1]), "{v[92:99]}"(wr[4][0]), "{v[100:107]}"(wr[4][1]), "{v[108:115]}"(wr[5][0]), "{v[116:123]}"(wr[5][1]), "{v[124:131]}"(wr[6][0]), "{v[132:139]}"(wr[6][1]), "{v[140:147]}"(wr[7][0]), "{v[148:155]}"(wr[7][1]), "{v[156:163]}"(wr[8][0]), "{v[164:171]}"(wr[8][1]), "{v[172:179]}"(wr[9][0]), "{v[180:187]}"(wr[9][1]), "{v[188:195]}"(wr[10][0]), "{v[196:203]}"(wr[10][1]), "{v[204:211]}"(wr[11][0]), "{v[212:219]}"(wr[11][1]) \
    : "memory", "vcc", "scc", "s88", "s89", "s90", "s91", "s92", "s93", "v0", "v1", "v2", "v3", "v4", "v5", "v6", "v7", "v20", "v21", "v22", "v23", "v24", "v25", "v26", "v27", "v220", "v221", "v222", "v223", "v224", "v225", "v226", "v227", "v228", "v229", "v230", "v231", "v232", "v233", "v234", "v235", "v236", "v237", "v238", "v239", "v240", "v241", "v242", "v243", "v244", "v245", "v246", "v247", "v248", "v249", "v250", "v251", "v252", "v253", "v254", "v255")

#define A_MAIN_K() \
  asm volatile( \
    "ds_read_b128 v[236:239], v9 offset:0\n\t" \
    "ds_read_b128 v[240:243], v9 offset:1024\n\t" \
    "ds_read_b128 v[244:247], v9 offset:2048\n\t" \
    "ds_read_b128 v[248:251], v9 offset:3072\n\t" \
    "v_mov_b32 v0, 0\n\t" \
    "v_mov_b32 v1, 0\n\t" \
    "v_mov_b32 v2, 0\n\t" \
    "v_mov_b32 v3, 0\n\t" \
    "v_mov_b32 v4, 0\n\t" \
    "v_mov_b32 v5, 0\n\t" \
    "v_mov_b32 v6, 0\n\t" \
    "v_mov_b32 v7, 0\n\t" \
    "s_waitcnt lgkmcnt(0)\n\t" \
    "s_barrier\n\t" \
    "buffer_load_dwordx4 v[220:223], v8, %[rs], %[m0] offen sc1\n\t" \
    "buffer_load_dwordx4 v[224:227], v8, %[rs], %[m1] offen sc1\n\t" \
    GSA(3) \
    "v_mov_b32 v252, v252\n\t" \
    "s_nop 2\n\t" \
    "v_mov_b32 v252, v252\n\t" \
    "s_nop 2\n\t" \
    "v_mov_b32 v252, v252\n\t" \
    "s_nop 2\n\t" \
    "v_mov_b32 v252, v252\n\t" \
    "s_nop 2\n\t" \
    "v_mov_b32 v252, v252\n\t" \
    "s_nop 2\n\t" \
    "v_mov_b32 v252, v252\n\t" \
    "s_nop 2\n\t" \
    "v_mov_b32 v252, v252\n\t" \
    "s_nop 2\n\t" \
    "v_mov_b32 v252, v252\n\t" \
    "s_nop 2\n\t" \
    "v_mov_b32 v252, v252\n\t" \
    "s_nop 2\n\t" \
    "v_mov_b32 v252, v252\n\t" \
    "s_nop 2\n\t" \
    "v_mov_b32 v252, v252\n\t" \
    "s_nop 2\n\t" \
    "v_mov_b32 v252, v252\n\t" \
    "s_nop 2\n\t" \
    "v_mov_b32 v252, v252\n\t" \
    "s_nop 2\n\t" \
    "v_mov_b32 v252, v252\n\t" \
    "s_nop 2\n\t" \
    "v_mov_b32 v252, v252\n\t" \
    "s_nop 2\n\t" \
    "v_mov_b32 v252, v252\n\t" \
    "s_nop 2\n\t" \
    "v_mov_b32 v252, v252\n\t" \
    "s_nop 2\n\t" \
    "v_mov_b32 v252, v252\n\t" \
    "s_nop 2\n\t" \
    "v_mov_b32 v252, v252\n\t" \
    "s_nop 2\n\t" \
    "v_mov_b32 v252, v252\n\t" \
    "s_nop 2\n\t" \
    "v_mov_b32 v252, v252\n\t" \
    "s_nop 2\n\t" \
    "v_mov_b32 v252, v252\n\t" \
    "s_nop 2\n\t" \
    "v_mov_b32 v252, v252\n\t" \
    "s_nop 2\n\t" \
    "v_mov_b32 v252, v252\n\t" \
    "s_nop 2\n\t" \
    "v_mov_b32 v252, v252\n\t" \
    "s_nop 2\n\t" \
    "v_mov_b32 v252, v252\n\t" \
    "s_nop 2\n\t" \
    "v_mov_b32 v252, v252\n\t" \
    "s_nop 2\n\t" \
    "v_mov_b32 v252, v252\n\t" \
    "s_nop 2\n\t" \
    "v_mov_b32 v252, v252\n\t" \
    "s_nop 2\n\t" \
    "v_mov_b32 v252, v252\n\t" \
    "s_nop 2\n\t" \
    "PM_%=:\n\t" \
    "s_waitcnt vmcnt(1)\n\t" \
    "ds_write_b128 v8, v[220:223] offset:8192\n\t" \
    "v_max3_u32 v252, v220, v221, v222\n\t" \
    "s_waitcnt vmcnt(0)\n\t" \
    "ds_write_b128 v8, v[224:227] offset:12288\n\t" \
    "v_max3_u32 v252, v252, v223, v224\n\t" \
    "v_max3_u32 v252, v252, v225, v226\n\t" \
    "v_max_u32 v252, v252, v227\n\t" \
    "v_cmp_eq_u32 vcc, -1, v252\n\t" \
    "s_cbranch_vccz PD_%=\n\t" \
    "s_sub_u32 %[lim], %[lim], 1\n\t" \
    "s_cmp_gt_i32 %[lim], 0\n\t" \
    "s_cbranch_scc0 PD_%=\n\t" \
    "s_sleep " STR(CADM) "\n\t" \
    "buffer_load_dwordx4 v[220:223], v8, %[rs], %[m0] offen sc1\n\t" \
    "buffer_load_dwordx4 v[224:227], v8, %[rs], %[m1] offen sc1\n\t" \
    "s_branch PM_%=\n\t" \
    "PD_%=:\n\t" \
    GSA(0) \
    "v_smfmac_f32_16x16x64_bf16 v[0:3], v[220:223], v[236:243], v10\n\t" \
    "v_xor_b32 v9, 16384, v9\n\t" \
    "ds_read_b128 v[236:239], v9 offset:0\n\t" \
    "ds_read_b128 v[240:243], v9 offset:1024\n\t" \
    "v_smfmac_f32_16x16x64_bf16 v[4:7], v[220:223], v[244:251], v10\n\t" \
    "ds_read_b128 v[244:247], v9 offset:2048\n\t" \
    "ds_read_b128 v[248:251], v9 offset:3072\n\t" \
    "s_waitcnt lgkmcnt(4)\n\t" \
    "s_barrier\n\t" \
    DGA \
    "ds_read_b128 v[228:231], v8 offset:0\n\t" \
    "ds_read_b128 v[232:235], v8 offset:4096\n\t" \
    "v_smfmac_f32_16x16x64_bf16 v[0:3], v[224:227], v[148:155], v10\n\t" \
    "v_xor_b32 v8, 1024, v8\n\t" \
    "ds_read_b128 v[220:223], v8 offset:8192\n\t" \
    "v_smfmac_f32_16x16x64_bf16 v[4:7], v[224:227], v[156:163], v10\n\t" \
    "ds_read_b128 v[224:227], v8 offset:12288\n\t" \
    "buffer_store_dwordx4 v[212:215], v14, %[rs], %[ox] offen offset:0 sc1\n\t" \
    "buffer_store_dwordx4 v[216:219], v14, %[rs], %[ox] offen offset:512 sc1\n\t" \
    "s_waitcnt lgkmcnt(3)\n\t" \
    "v_smfmac_f32_16x16x64_bf16 v[0:3], v[228:231], v[20:27], v10\n\t" \
    "v_smfmac_f32_16x16x64_bf16 v[4:7], v[228:231], v[28:35], v10\n\t" \
    "ds_read_b128 v[228:231], v8 offset:0\n\t" \
    "s_waitcnt lgkmcnt(3)\n\t" \
    "v_smfmac_f32_16x16x64_bf16 v[0:3], v[232:235], v[84:91], v10\n\t" \
    "v_smfmac_f32_16x16x64_bf16 v[4:7], v[232:235], v[92:99], v10\n\t" \
    "ds_read_b128 v[232:235], v8 offset:4096\n\t" \
    "s_waitcnt lgkmcnt(3)\n\t" \
    "v_smfmac_f32_16x16x64_bf16 v[0:3], v[220:223], v[236:243], v10\n\t" \
    "v_xor_b32 v9, 49152, v9\n\t" \
    "ds_read_b128 v[236:239], v9 offset:0\n\t" \
    "v_smfmac_f32_16x16x64_bf16 v[4:7], v[220:223], v[244:251], v10\n\t" \
    "ds_read_b128 v[240:243], v9 offset:1024\n\t" \
    "s_waitcnt lgkmcnt(4)\n\t" \
    "v_smfmac_f32_16x16x64_bf16 v[0:3], v[224:227], v[164:171], v10\n\t" \
    "v_xor_b32 v8, 3072, v8\n\t" \
    "ds_read_b128 v[220:223], v8 offset:12288\n\t" \
    "v_smfmac_f32_16x16x64_bf16 v[4:7], v[224:227], v[172:179], v10\n\t" \
    "ds_read_b128 v[244:247], v9 offset:2048\n\t" \
    "s_waitcnt lgkmcnt(5)\n\t" \
    "v_smfmac_f32_16x16x64_bf16 v[0:3], v[228:231], v[36:43], v10\n\t" \
    "ds_read_b128 v[224:227], v8 offset:0\n\t" \
    "v_smfmac_f32_16x16x64_bf16 v[4:7], v[228:231], v[44:51], v10\n\t" \
    "ds_read_b128 v[248:251], v9 offset:3072\n\t" \
    "s_waitcnt lgkmcnt(6)\n\t" \
    "v_smfmac_f32_16x16x64_bf16 v[0:3], v[232:235], v[100:107], v10\n\t" \
    "ds_read_b128 v[228:231], v8 offset:8192\n\t" \
    "v_smfmac_f32_16x16x64_bf16 v[4:7], v[232:235], v[108:115], v10\n\t" \
    "ds_read_b128 v[232:235], v8 offset:4096\n\t" \
    "s_waitcnt lgkmcnt(5)\n\t" \
    "v_smfmac_f32_16x16x64_bf16 v[0:3], v[220:223], v[180:187], v10\n\t" \
    "v_smfmac_f32_16x16x64_bf16 v[4:7], v[220:223], v[188:195], v10\n\t" \
    "v_xor_b32 v8, 1024, v8\n\t" \
    "ds_read_b128 v[220:223], v8 offset:12288\n\t" \
    "s_waitcnt lgkmcnt(4)\n\t" \
    "v_smfmac_f32_16x16x64_bf16 v[0:3], v[224:227], v[52:59], v10\n\t" \
    "v_smfmac_f32_16x16x64_bf16 v[4:7], v[224:227], v[60:67], v10\n\t" \
    "ds_read_b128 v[224:227], v8 offset:0\n\t" \
    "s_waitcnt lgkmcnt(3)\n\t" \
    "v_smfmac_f32_16x16x64_bf16 v[0:3], v[228:231], v[236:243], v10\n\t" \
    "v_xor_b32 v9, 16384, v9\n\t" \
    "ds_read_b128 v[236:239], v9 offset:0\n\t" \
    "v_smfmac_f32_16x16x64_bf16 v[4:7], v[228:231], v[244:251], v10\n\t" \
    "ds_read_b128 v[240:243], v9 offset:1024\n\t" \
    "s_waitcnt lgkmcnt(4)\n\t" \
    "v_smfmac_f32_16x16x64_bf16 v[0:3], v[232:235], v[116:123], v10\n\t" \
    "ds_read_b128 v[228:231], v8 offset:4096\n\t" \
    "v_smfmac_f32_16x16x64_bf16 v[4:7], v[232:235], v[124:131], v10\n\t" \
    "ds_read_b128 v[244:247], v9 offset:2048\n\t" \
    "s_waitcnt lgkmcnt(5)\n\t" \
    "v_smfmac_f32_16x16x64_bf16 v[0:3], v[220:223], v[196:203], v10\n\t" \
    "ds_read_b128 v[232:235], v8 offset:8192\n\t" \
    "v_xor_b32 v8, 3072, v8\n\t" \
    "v_smfmac_f32_16x16x64_bf16 v[4:7], v[220:223], v[204:211], v10\n\t" \
    "ds_read_b128 v[248:251], v9 offset:3072\n\t" \
    "s_waitcnt lgkmcnt(6)\n\t" \
    "v_smfmac_f32_16x16x64_bf16 v[0:3], v[224:227], v[68:75], v10\n\t" \
    "v_smfmac_f32_16x16x64_bf16 v[4:7], v[224:227], v[76:83], v10\n\t" \
    "s_waitcnt lgkmcnt(3)\n\t" \
    "v_smfmac_f32_16x16x64_bf16 v[0:3], v[228:231], v[132:139], v10\n\t" \
    "v_smfmac_f32_16x16x64_bf16 v[4:7], v[228:231], v[140:147], v10\n\t" \
    "s_waitcnt lgkmcnt(0)\n\t" \
    "v_smfmac_f32_16x16x64_bf16 v[0:3], v[232:235], v[236:243], v10\n\t" \
    "v_xor_b32 v9, 49152, v9\n\t" \
    "v_smfmac_f32_16x16x64_bf16 v[4:7], v[232:235], v[244:251], v10\n\t" \
    "v_mov_b32 v253, %[stag]\n\t" \
    "s_nop 4\n\t" \
    "v_add_f32 v0, v0, v1\n\t" \
    "v_add_f32 v2, v2, v3\n\t" \
    "v_add_f32 v4, v4, v5\n\t" \
    "v_add_f32 v6, v6, v7\n\t" \
    "v_cvt_pk_bf16_f32 v0, v0, v4\n\t" \
    "v_cvt_pk_bf16_f32 v2, v2, v6\n\t" \
    "ds_write2_b32 v11, v0, v2 offset0:0 offset1:4\n\t" \
    "s_mov_b64 exec, %[onej]\n\t" \
    "ds_write_b32 v18, v253 offset:16\n\t" \
    "s_mov_b64 exec, -1\n\t" \
    GSA(1) \
    "s_setprio 0\n\t" \
    "buffer_load_dwordx4 v[236:239], v12, %[rs], %[qx] offen offset:0 sc1\n\t" \
    "buffer_load_dwordx4 v[240:243], v12, %[rs], %[qx] offen offset:1024 sc1\n\t" \
    "buffer_load_dwordx4 v[244:247], v12, %[rs], %[qx] offen offset:2048 sc1\n\t" \
    "buffer_load_dwordx4 v[248:251], v12, %[rs], %[qx] offen offset:3072 sc1\n\t" \
    "v_cmp_le_i32 s[72:73], 0, v16\n\t" \
    "v_cmp_le_i32 s[74:75], 1, v16\n\t" \
    "v_cmp_le_i32 s[76:77], 2, v16\n\t" \
    "v_cmp_le_i32 s[78:79], 3, v16\n\t" \
    "v_cmp_le_i32 s[80:81], 16, v16\n\t" \
    "v_cmp_le_i32 s[82:83], 17, v16\n\t" \
    "v_cmp_le_i32 s[84:85], 18, v16\n\t" \
    "v_cmp_le_i32 s[86:87], 19, v16\n\t" \
    "v_mov_b32 v254, v254\n\t" \
    "s_nop 2\n\t" \
    "v_mov_b32 v254, v254\n\t" \
    "s_nop 2\n\t" \
    "v_mov_b32 v254, v254\n\t" \
    "s_nop 2\n\t" \
    "v_mov_b32 v254, v254\n\t" \
    "s_nop 2\n\t" \
    "v_mov_b32 v254, v254\n\t" \
    "s_nop 2\n\t" \
    "v_mov_b32 v254, v254\n\t" \
    "s_nop 2\n\t" \
    "v_mov_b32 v254, v254\n\t" \
    "s_nop 2\n\t" \
    "v_mov_b32 v254, v254\n\t" \
    "s_nop 2\n\t" \
    "v_mov_b32 v254, v254\n\t" \
    "s_nop 2\n\t" \
    "v_mov_b32 v254, v254\n\t" \
    "s_nop 2\n\t" \
    "v_mov_b32 v254, v254\n\t" \
    "s_nop 2\n\t" \
    "v_mov_b32 v254, v254\n\t" \
    "s_nop 2\n\t" \
    "v_mov_b32 v254, v254\n\t" \
    "s_nop 2\n\t" \
    "v_mov_b32 v254, v254\n\t" \
    "s_nop 2\n\t" \
    "v_mov_b32 v254, v254\n\t" \
    "s_nop 2\n\t" \
    "v_mov_b32 v254, v254\n\t" \
    "s_nop 2\n\t" \
    "v_mov_b32 v254, v254\n\t" \
    "s_nop 2\n\t" \
    "v_mov_b32 v254, v254\n\t" \
    "s_nop 2\n\t" \
    "v_mov_b32 v254, v254\n\t" \
    "s_nop 2\n\t" \
    "v_mov_b32 v254, v254\n\t" \
    "s_nop 2\n\t" \
    "LW_%=:\n\t" \
    "ds_read_b32 v0, v18 offset:16\n\t" \
    "s_sub_u32 %[lim], %[lim], " STR(DEC1) "\n\t" \
    "s_cmp_gt_i32 %[lim], 0\n\t" \
    "v_mov_b32 v254, v254\n\t" \
    "s_nop 2\n\t" \
    "v_mov_b32 v254, v254\n\t" \
    "s_nop 2\n\t" \
    "v_mov_b32 v254, v254\n\t" \
    "s_nop 2\n\t" \
    "v_mov_b32 v254, v254\n\t" \
    "s_nop 2\n\t" \
    "v_mov_b32 v254, v254\n\t" \
    "s_nop 2\n\t" \
    "s_waitcnt lgkmcnt(0)\n\t" \
    "s_cbranch_scc0 CW_%=\n\t" \
    "v_cmp_ne_u32 vcc, v0, v253\n\t" \
    "s_cbranch_vccnz LW_%=\n\t" \
    "CW_%=:\n\t" \
    "ds_read_b128 v[212:215], v12 offset:24576\n\t" \
    "ds_read_b128 v[216:219], v12 offset:28672\n\t" \
    "ds_read_b128 v[220:223], v15 offset:0\n\t" \
    "ds_read_b128 v[224:227], v12 offset:25600\n\t" \
    "ds_read_b128 v[228:231], v12 offset:29696\n\t" \
    "v_add_u32 v253, %[se], v12\n\t" \
    "v_mov_b32 v254, v254\n\t" \
    "s_nop 2\n\t" \
    "v_mov_b32 v254, v254\n\t" \
    "s_nop 2\n\t" \
    "v_mov_b32 v254, v254\n\t" \
    "s_nop 2\n\t" \
    "v_mov_b32 v254, v254\n\t" \
    "s_nop 2\n\t" \
    "s_waitcnt vmcnt(0)\n\t" \
    "s_waitcnt lgkmcnt(4)\n\t" \
    "v_mfma_f32_16x16x32_bf16 v[0:3], v[212:215], v[236:239], 0\n\t" \
    "ds_read_b128 v[212:215], v15 offset:512\n\t" \
    "s_waitcnt lgkmcnt(4)\n\t" \
    "v_mfma_f32_16x16x32_bf16 v[4:7], v[216:219], v[236:239], 0\n\t" \
    "ds_read_b128 v[216:219], v12 offset:26624\n\t" \
    "s_waitcnt lgkmcnt(4)\n\t" \
    "v_mfma_f32_16x16x32_bf16 v[232:235], v[220:223], v[236:239], 0\n\t" \
    "ds_read_b128 v[220:223], v12 offset:30720\n\t" \
    "s_waitcnt lgkmcnt(4)\n\t" \
    "v_mfma_f32_16x16x32_bf16 v[0:3], v[224:227], v[240:243], v[0:3]\n\t" \
    "ds_read_b128 v[224:227], v15 offset:1024\n\t" \
    "s_waitcnt lgkmcnt(4)\n\t" \
    "v_mfma_f32_16x16x32_bf16 v[4:7], v[228:231], v[240:243], v[4:7]\n\t" \
    "ds_read_b128 v[228:231], v12 offset:27648\n\t" \
    "s_waitcnt lgkmcnt(4)\n\t" \
    "v_mfma_f32_16x16x32_bf16 v[232:235], v[212:215], v[240:243], v[232:235]\n\t" \
    "ds_read_b128 v[212:215], v12 offset:31744\n\t" \
    "s_waitcnt lgkmcnt(4)\n\t" \
    "v_mfma_f32_16x16x32_bf16 v[0:3], v[216:219], v[244:247], v[0:3]\n\t" \
    "ds_read_b128 v[216:219], v15 offset:1536\n\t" \
    "s_waitcnt lgkmcnt(4)\n\t" \
    "v_mfma_f32_16x16x32_bf16 v[4:7], v[220:223], v[244:247], v[4:7]\n\t" \
    "s_waitcnt lgkmcnt(3)\n\t" \
    "v_mfma_f32_16x16x32_bf16 v[232:235], v[224:227], v[244:247], v[232:235]\n\t" \
    "s_waitcnt lgkmcnt(2)\n\t" \
    "v_mfma_f32_16x16x32_bf16 v[0:3], v[228:231], v[248:251], v[0:3]\n\t" \
    "s_waitcnt lgkmcnt(1)\n\t" \
    "v_mfma_f32_16x16x32_bf16 v[4:7], v[212:215], v[248:251], v[4:7]\n\t" \
    "s_waitcnt lgkmcnt(0)\n\t" \
    "v_mfma_f32_16x16x32_bf16 v[232:235], v[216:219], v[248:251], v[232:235]\n\t" \
    "s_barrier\n\t" \
    GSA(4) \
    "ds_read_b128 v[236:239], v253 offset:8192\n\t" \
    "ds_read_b128 v[240:243], v253 offset:9216\n\t" \
    "ds_read_b128 v[244:247], v253 offset:10240\n\t" \
    "ds_read_b128 v[248:251], v253 offset:11264\n\t" \
    "ds_read2st64_b32 v[228:229], v17 offset0:0 offset1:1\n\t" \
    "ds_read2st64_b32 v[230:231], v17 offset0:2 offset1:3\n\t" \
    "s_mov_b64 exec, %[fm]\n\t" \
    "v_mov_b32 v220, %[st]\n\t" \
    "v_mov_b32 v221, %[tag]\n\t" \
    "v_mov_b32 v222, 0\n\t" \
    "buffer_store_dwordx2 v[220:221], v222, %[rs], %[po] offen sc1\n\t" \
    "s_mov_b64 exec, -1\n\t" \
    "v_mov_b32 v254, 0xff800000\n\t" \
    "v_cvt_pk_bf16_f32 v0, 0, v0\n\t" \
    "v_cvt_pk_bf16_f32 v1, 0, v1\n\t" \
    "v_cvt_pk_bf16_f32 v2, 0, v2\n\t" \
    "v_cvt_pk_bf16_f32 v3, 0, v3\n\t" \
    "v_cvt_pk_bf16_f32 v4, 0, v4\n\t" \
    "v_cvt_pk_bf16_f32 v5, 0, v5\n\t" \
    "v_cvt_pk_bf16_f32 v6, 0, v6\n\t" \
    "v_cvt_pk_bf16_f32 v7, 0, v7\n\t" \
    "v_cvt_pk_bf16_f32 v232, 0, v232\n\t" \
    "v_cvt_pk_bf16_f32 v233, 0, v233\n\t" \
    "v_mul_f32 v0, 0x3db504f3, v0\n\t" \
    "v_mul_f32 v1, 0x3db504f3, v1\n\t" \
    "v_mul_f32 v2, 0x3db504f3, v2\n\t" \
    "v_mul_f32 v3, 0x3db504f3, v3\n\t" \
    "v_mul_f32 v4, 0x3db504f3, v4\n\t" \
    "v_mul_f32 v5, 0x3db504f3, v5\n\t" \
    "v_mul_f32 v6, 0x3db504f3, v6\n\t" \
    "v_mul_f32 v7, 0x3db504f3, v7\n\t" \
    "v_mul_f32 v232, 0x3db504f3, v232\n\t" \
    "v_mul_f32 v233, 0x3db504f3, v233\n\t" \
    "v_cvt_pk_bf16_f32 v0, 0, v0\n\t" \
    "v_cvt_pk_bf16_f32 v1, 0, v1\n\t" \
    "v_cvt_pk_bf16_f32 v2, 0, v2\n\t" \
    "v_cvt_pk_bf16_f32 v3, 0, v3\n\t" \
    "v_cvt_pk_bf16_f32 v4, 0, v4\n\t" \
    "v_cvt_pk_bf16_f32 v5, 0, v5\n\t" \
    "v_cvt_pk_bf16_f32 v6, 0, v6\n\t" \
    "v_cvt_pk_bf16_f32 v7, 0, v7\n\t" \
    "v_cvt_pk_bf16_f32 v232, 0, v232\n\t" \
    "v_cvt_pk_bf16_f32 v233, 0, v233\n\t" \
    "v_cndmask_b32 v0, v254, v0, s[72:73]\n\t" \
    "v_cndmask_b32 v1, v254, v1, s[74:75]\n\t" \
    "v_cndmask_b32 v2, v254, v2, s[76:77]\n\t" \
    "v_cndmask_b32 v3, v254, v3, s[78:79]\n\t" \
    "v_cndmask_b32 v4, v254, v4, s[80:81]\n\t" \
    "v_cndmask_b32 v5, v254, v5, s[82:83]\n\t" \
    "v_cndmask_b32 v6, v254, v6, s[84:85]\n\t" \
    "v_cndmask_b32 v7, v254, v7, s[86:87]\n\t" \
    "v_max3_f32 v252, v0, v1, v2\n\t" \
    "v_max3_f32 v255, v3, v4, v5\n\t" \
    "v_max3_f32 v254, v6, v7, v232\n\t" \
    "v_max3_f32 v252, v252, v255, v254\n\t" \
    "v_max_f32 v252, v252, v233\n\t" \
    "v_mov_b32 v255, v252\n\t" \
    "v_mov_b32 v254, v252\n\t" \
    "s_nop 1\n\t" \
    "v_permlane16_swap_b32 v255, v254\n\t" \
    "v_max_f32 v252, v255, v254\n\t" \
    "v_max_f32 v255, v255, v254\n\t" \
    "s_nop 1\n\t" \
    "v_permlane32_swap_b32 v252, v255\n\t" \
    "v_max_f32 v252, v252, v255\n\t" \
    "v_mul_f32 v252, 0xbfb8aa3b, v252\n\t" \
    "v_fma_f32 v0, v0, %[l2e], v252\n\t" \
    "v_fma_f32 v1, v1, %[l2e], v252\n\t" \
    "v_fma_f32 v2, v2, %[l2e], v252\n\t" \
    "v_fma_f32 v3, v3, %[l2e], v252\n\t" \
    "v_fma_f32 v4, v4, %[l2e], v252\n\t" \
    "v_fma_f32 v5, v5, %[l2e], v252\n\t" \
    "v_fma_f32 v6, v6, %[l2e], v252\n\t" \
    "v_fma_f32 v7, v7, %[l2e], v252\n\t" \
    "v_fma_f32 v232, v232, %[l2e], v252\n\t" \
    "v_fma_f32 v233, v233, %[l2e], v252\n\t" \
    "v_exp_f32 v0, v0\n\t" \
    "v_exp_f32 v1, v1\n\t" \
    "v_exp_f32 v2, v2\n\t" \
    "v_exp_f32 v3, v3\n\t" \
    "v_exp_f32 v4, v4\n\t" \
    "v_exp_f32 v5, v5\n\t" \
    "v_exp_f32 v6, v6\n\t" \
    "v_exp_f32 v7, v7\n\t" \
    "v_exp_f32 v232, v232\n\t" \
    "v_exp_f32 v233, v233\n\t" \
    "v_add_f32 v252, v0, v1\n\t" \
    "v_add_f32 v235, v2, v3\n\t" \
    "v_add_f32 v254, v4, v5\n\t" \
    "v_add_f32 v255, v6, v7\n\t" \
    "v_add_f32 v234, v232, v233\n\t" \
    "v_add_f32 v252, v252, v235\n\t" \
    "v_add_f32 v254, v254, v255\n\t" \
    "v_add_f32 v252, v252, v254\n\t" \
    "v_add_f32 v252, v252, v234\n\t" \
    "v_mov_b32 v255, v252\n\t" \
    "v_mov_b32 v254, v252\n\t" \
    "s_nop 1\n\t" \
    "v_permlane16_swap_b32 v255, v254\n\t" \
    "v_add_f32 v252, v255, v254\n\t" \
    "v_add_f32 v255, v255, v254\n\t" \
    "s_nop 1\n\t" \
    "v_permlane32_swap_b32 v252, v255\n\t" \
    "v_add_f32 v252, v252, v255\n\t" \
    "v_rcp_f32 v252, v252\n\t" \
    "s_nop 0\n\t" \
    "v_mul_f32 v0, v0, v252\n\t" \
    "v_mul_f32 v1, v1, v252\n\t" \
    "v_mul_f32 v2, v2, v252\n\t" \
    "v_mul_f32 v3, v3, v252\n\t" \
    "v_mul_f32 v4, v4, v252\n\t" \
    "v_mul_f32 v5, v5, v252\n\t" \
    "v_mul_f32 v6, v6, v252\n\t" \
    "v_mul_f32 v7, v7, v252\n\t" \
    "v_mul_f32 v232, v232, v252\n\t" \
    "v_mul_f32 v233, v233, v252\n\t" \
    "v_cvt_pk_bf16_f32 v0, v0, v1\n\t" \
    "v_cvt_pk_bf16_f32 v1, v2, v3\n\t" \
    "v_cvt_pk_bf16_f32 v2, v4, v5\n\t" \
    "v_cvt_pk_bf16_f32 v3, v6, v7\n\t" \
    "v_cvt_pk_bf16_f32 v4, v232, v233\n\t" \
    "v_mov_b32 v5, 0\n\t" \
    "v_mov_b32 v6, 0\n\t" \
    "v_cvt_pk_bf16_f32 v7, v232, v233\n\t" \
    "s_waitcnt lgkmcnt(0)\n\t" \
    "v_mfma_f32_16x16x32_bf16 v[236:239], v[236:239], v[0:3], 0\n\t" \
    "v_mfma_f32_16x16x32_bf16 v[240:243], v[240:243], v[0:3], 0\n\t" \
    "v_mfma_f32_16x16x32_bf16 v[244:247], v[244:247], v[0:3], 0\n\t" \
    "v_mfma_f32_16x16x32_bf16 v[248:251], v[248:251], v[0:3], 0\n\t" \
    "v_mfma_f32_16x16x16_bf16 v[236:239], v[228:229], v[4:5], v[236:239]\n\t" \
    "v_mfma_f32_16x16x16_bf16 v[240:243], v[228:229], v[6:7], v[240:243]\n\t" \
    "v_mfma_f32_16x16x16_bf16 v[244:247], v[230:231], v[4:5], v[244:247]\n\t" \
    "v_mfma_f32_16x16x16_bf16 v[248:251], v[230:231], v[6:7], v[248:251]\n\t" \
    "s_nop 4\n\t" \
    "v_cvt_pk_bf16_f32 v212, v236, v237\n\t" \
    "v_cvt_pk_bf16_f32 v213, v240, v241\n\t" \
    "v_cvt_pk_bf16_f32 v214, v238, v239\n\t" \
    "v_cvt_pk_bf16_f32 v215, v242, v243\n\t" \
    "v_permlane16_swap_b32 v212, v213\n\t" \
    "s_nop 0\n\t" \
    "v_permlane16_swap_b32 v214, v215\n\t" \
    "v_cvt_pk_bf16_f32 v216, v244, v245\n\t" \
    "v_cvt_pk_bf16_f32 v217, v248, v249\n\t" \
    "v_cvt_pk_bf16_f32 v218, v246, v247\n\t" \
    "v_cvt_pk_bf16_f32 v219, v250, v251\n\t" \
    "v_permlane16_swap_b32 v216, v217\n\t" \
    "s_nop 0\n\t" \
    "v_permlane16_swap_b32 v218, v219\n\t" \
    "s_setprio 1\n\t" \
    GSA(2) \
    GSW \
    "s_nop 1\n\t" \
    : [lim] "+s"(lim), "+{v[212:219]}"(oxr) DGO GSO(0) GSO(1) GSO(2) GSO(3) GSO(4) \
    : "{v8}"(am_a), "{v9}"(t0_a), "{v10}"(idx), "{v11}"(iw_a), "{v12}"(ab_a), "{v14}"(xo_v), "{v15}"(kr_a), "{v16}"(dq), "{v17}"(vl_a), "{v18}"(fr_a), [stag] "s"(0xffffff81u + s), [onej] "s"(onej), [rs] "s"(rs), [m0] "s"(sbm + 16384u), [m1] "s"(sbm + 20480u), [st] "s"(s), [tag] "s"(P.tag), [po] "s"(FLG_OFF + F_PRG(4, h) * 128), [fm] "s"(fmask), [qx] "s"(qxo), [qd] "s"(lbase + LA_VM), [qh] "s"((j & 1) * 2048), [ox] "s"(oxo), [se] "s"((j & 1) * 4096), [l2e] "s"(0x3fb8aa3b), "{v[20:27]}"(wr[0][0]), "{v[28:35]}"(wr[0][1]), "{v[36:43]}"(wr[1][0]), "{v[44:51]}"(wr[1][1]), "{v[52:59]}"(wr[2][0]), "{v[60:67]}"(wr[2][1]), "{v[68:75]}"(wr[3][0]), "{v[76:83]}"(wr[3][1]), "{v[84:91]}"(wr[4][0]), "{v[92:99]}"(wr[4][1]), "{v[100:107]}"(wr[5][0]), "{v[108:115]}"(wr[5][1]), "{v[116:123]}"(wr[6][0]), "{v[124:131]}"(wr[6][1]), "{v[132:139]}"(wr[7][0]), "{v[140:147]}"(wr[7][1]), "{v[148:155]}"(wr[8][0]), "{v[156:163]}"(wr[8][1]), "{v[164:171]}"(wr[9][0]), "{v[172:179]}"(wr[9][1]), "{v[180:187]}"(wr[10][0]), "{v[188:195]}"(wr[10][1]), "{v[196:203]}"(wr[11][0]), "{v[204:211]}"(wr[11][1]) \
    : "memory", "scc", "vcc", "s90", "s91", "s72", "s73", "s74", "s75", "s76", "s77", "s78", "s79", "s80", "s81", "s82", "s83", "s84", "s85", "s86", "s87", "v0", "v1", "v2", "v3", "v4", "v5", "v6", "v7", "v220", "v221", "v222", "v223", "v224", "v225", "v226", "v227", "v228", "v229", "v230", "v231", "v232", "v233", "v234", "v235", "v236", "v237", "v238", "v239", "v240", "v241", "v242", "v243", "v244", "v245", "v246", "v247", "v248", "v249", "v250", "v251", "v252", "v253", "v254", "v255")

#define B_POLL() \
  asm volatile( \
    "TWB_%=:\n\t" \
    "s_memrealtime s[88:89]\n\t" \
    "s_memtime s[90:91]\n\t" \
    "s_waitcnt lgkmcnt(0)\n\t" \
    "s_lshl_b32 s88, s88, 2\n\t" \
    "s_sub_u32 s89, %[tgt], s88\n\t" \
    "s_cmp_gt_i32 s89, 16\n\t" \
    "s_cbranch_scc1 TWB_%=\n\t" \
    "s_mul_i32 s89, s89, 11\n\t" \
    "s_ashr_i32 s89, s89, 1\n\t" \
    "s_add_u32 s89, s89, s90\n\t" \
    "TCB_%=:\n\t" \
    "s_memtime s[92:93]\n\t" \
    "s_waitcnt lgkmcnt(0)\n\t" \
    "s_sub_u32 s93, s89, s92\n\t" \
    "s_cmp_gt_i32 s93, 0\n\t" \
    "s_cbranch_scc1 TCB_%=\n\t" \
    "buffer_load_dwordx4 v[220:223], %[l16], %[rs], %[m0] offen offset:0 sc1\n\t" \
    "buffer_load_dwordx4 v[224:227], %[l16], %[rs], %[m0] offen offset:1024 sc1\n\t" \
    "buffer_load_dwordx4 v[228:231], %[l16], %[rs], %[m1] offen offset:0 sc1\n\t" \
    "buffer_load_dwordx4 v[232:235], %[l16], %[rs], %[m1] offen offset:1024 sc1\n\t" \
    "v_mov_b32 v252, v252\n\t" \
    "s_nop 2\n\t" \
    "v_mov_b32 v252, v252\n\t" \
    "s_nop 2\n\t" \
    "v_mov_b32 v252, v252\n\t" \
    "s_nop 2\n\t" \
    "v_mov_b32 v252, v252\n\t" \
    "s_nop 2\n\t" \
    "v_mov_b32 v252, v252\n\t" \
    "s_nop 2\n\t" \
    "v_mov_b32 v252, v252\n\t" \
    "s_nop 2\n\t" \
    "v_mov_b32 v252, v252\n\t" \
    "s_nop 2\n\t" \
    "v_mov_b32 v252, v252\n\t" \
    "s_nop 2\n\t" \
    "v_mov_b32 v252, v252\n\t" \
    "s_nop 2\n\t" \
    "v_mov_b32 v252, v252\n\t" \
    "s_nop 2\n\t" \
    "v_mov_b32 v252, v252\n\t" \
    "s_nop 2\n\t" \
    "v_mov_b32 v252, v252\n\t" \
    "s_nop 2\n\t" \
    "v_mov_b32 v252, v252\n\t" \
    "s_nop 2\n\t" \
    "v_mov_b32 v252, v252\n\t" \
    "s_nop 2\n\t" \
    "v_mov_b32 v252, v252\n\t" \
    "s_nop 2\n\t" \
    "v_mov_b32 v252, v252\n\t" \
    "s_nop 2\n\t" \
    "v_mov_b32 v252, v252\n\t" \
    "s_nop 2\n\t" \
    "v_mov_b32 v252, v252\n\t" \
    "s_nop 2\n\t" \
    "v_mov_b32 v252, v252\n\t" \
    "s_nop 2\n\t" \
    "v_mov_b32 v252, v252\n\t" \
    "s_nop 2\n\t" \
    "v_mov_b32 v252, v252\n\t" \
    "s_nop 2\n\t" \
    "v_mov_b32 v252, v252\n\t" \
    "s_nop 2\n\t" \
    "v_mov_b32 v252, v252\n\t" \
    "s_nop 2\n\t" \
    "v_mov_b32 v252, v252\n\t" \
    "s_nop 2\n\t" \
    "PM_%=:\n\t" \
    "s_waitcnt vmcnt(3)\n\t" \
    "ds_write_b128 %[la], v[220:223] offset:0\n\t" \
    "v_max3_u32 v252, v220, v220, v221\n\t" \
    "v_max3_u32 v252, v252, v222, v223\n\t" \
    "s_waitcnt vmcnt(2)\n\t" \
    "ds_write_b128 %[la], v[224:227] offset:1024\n\t" \
    "v_max3_u32 v252, v252, v224, v225\n\t" \
    "v_max3_u32 v252, v252, v226, v227\n\t" \
    "s_waitcnt vmcnt(1)\n\t" \
    "ds_write_b128 %[la], v[228:231] offset:8192\n\t" \
    "v_max3_u32 v252, v252, v228, v229\n\t" \
    "v_max3_u32 v252, v252, v230, v231\n\t" \
    "s_waitcnt vmcnt(0)\n\t" \
    "ds_write_b128 %[la], v[232:235] offset:9216\n\t" \
    "v_max3_u32 v252, v252, v232, v233\n\t" \
    "v_max3_u32 v252, v252, v234, v235\n\t" \
    "v_cmp_eq_u32 vcc, -1, v252\n\t" \
    "s_cbranch_vccz PD_%=\n\t" \
    "s_sub_u32 %[lim], %[lim], 1\n\t" \
    "s_cmp_gt_i32 %[lim], 0\n\t" \
    "s_cbranch_scc0 PD_%=\n\t" \
    "s_sleep " STR(CADM) "\n\t" \
    "buffer_load_dwordx4 v[220:223], %[l16], %[rs], %[m0] offen offset:0 sc1\n\t" \
    "buffer_load_dwordx4 v[224:227], %[l16], %[rs], %[m0] offen offset:1024 sc1\n\t" \
    "buffer_load_dwordx4 v[228:231], %[l16], %[rs], %[m1] offen offset:0 sc1\n\t" \
    "buffer_load_dwordx4 v[232:235], %[l16], %[rs], %[m1] offen offset:1024 sc1\n\t" \
    "s_branch PM_%=\n\t" \
    "PD_%=:\n\t" \
    GSA(3) \
    "s_waitcnt lgkmcnt(0)\n\t" \
    "s_barrier\n\t" \
    : [lim] "+s"(lim) GSO(3) \
    : [l16] "v"(l16), [la] "v"(la), [rs] "s"(rs), [m0] "s"(mo), [m1] "s"(mo + 16384u), [tgt] "s"(tgt) \
    : "memory", "vcc", "scc", "v252", "s88", "s89", "s90", "s91", "s92", "s93", "v220", "v221", "v222", "v223", "v224", "v225", "v226", "v227", "v228", "v229", "v230", "v231", "v232", "v233", "v234", "v235")

#define T_WAIT() \
  asm volatile( \
    "TWC_%=:\n\t" \
    "s_memrealtime s[88:89]\n\t" \
    "s_memtime s[90:91]\n\t" \
    "s_waitcnt lgkmcnt(0)\n\t" \
    "s_lshl_b32 s88, s88, 2\n\t" \
    "s_sub_u32 s89, %[tgt], s88\n\t" \
    "s_cmp_gt_i32 s89, 16\n\t" \
    "s_cbranch_scc1 TWC_%=\n\t" \
    "s_mul_i32 s89, s89, 11\n\t" \
    "s_ashr_i32 s89, s89, 1\n\t" \
    "s_add_u32 s89, s89, s90\n\t" \
    "TCC_%=:\n\t" \
    "s_memtime s[92:93]\n\t" \
    "s_waitcnt lgkmcnt(0)\n\t" \
    "s_sub_u32 s93, s89, s92\n\t" \
    "s_cmp_gt_i32 s93, 0\n\t" \
    "s_cbranch_scc1 TCC_%=\n\t" \
    :  \
    : [tgt] "s"(tgt) \
    : "s88", "s89", "s90", "s91", "s92", "s93", "memory", "scc")

#define V_PROJ() \
  asm volatile( \
    "v_mov_b32 v0, 0\n\t" \
    "v_mov_b32 v1, 0\n\t" \
    "v_mov_b32 v2, 0\n\t" \
    "v_mov_b32 v3, 0\n\t" \
    "v_mov_b32 v4, 0\n\t" \
    "v_mov_b32 v5, 0\n\t" \
    "v_mov_b32 v6, 0\n\t" \
    "v_mov_b32 v7, 0\n\t" \
    "s_barrier\n\t" \
    "ds_read_b128 v[220:223], %[am] offset:0\n\t" \
    "ds_read_b128 v[224:227], %[am] offset:1024\n\t" \
    "ds_read_b128 v[228:231], %[am] offset:2048\n\t" \
    "ds_read_b128 v[232:235], %[am] offset:3072\n\t" \
    "ds_read_b128 v[236:239], %[am] offset:4096\n\t" \
    "ds_read_b128 v[240:243], %[am] offset:5120\n\t" \
    "ds_read_b128 v[244:247], %[am] offset:6144\n\t" \
    "ds_read_b128 v[248:251], %[am] offset:7168\n\t" \
    "s_waitcnt lgkmcnt(7)\n\t" \
    "v_smfmac_f32_16x16x64_bf16 v[0:3], v[220:223], v[28:35], %[idx]\n\t" \
    "ds_read_b128 v[220:223], %[am] offset:8192\n\t" \
    "s_waitcnt lgkmcnt(7)\n\t" \
    "v_smfmac_f32_16x16x64_bf16 v[4:7], v[224:227], v[36:43], %[idx]\n\t" \
    "ds_read_b128 v[224:227], %[am] offset:9216\n\t" \
    "s_waitcnt lgkmcnt(7)\n\t" \
    "v_smfmac_f32_16x16x64_bf16 v[0:3], v[228:231], v[44:51], %[idx]\n\t" \
    "ds_read_b128 v[228:231], %[am] offset:10240\n\t" \
    "s_waitcnt lgkmcnt(7)\n\t" \
    "v_smfmac_f32_16x16x64_bf16 v[4:7], v[232:235], v[52:59], %[idx]\n\t" \
    "ds_read_b128 v[232:235], %[am] offset:11264\n\t" \
    "s_waitcnt lgkmcnt(7)\n\t" \
    "v_smfmac_f32_16x16x64_bf16 v[0:3], v[236:239], v[60:67], %[idx]\n\t" \
    "ds_read_b128 v[236:239], %[am] offset:12288\n\t" \
    "s_waitcnt lgkmcnt(7)\n\t" \
    "v_smfmac_f32_16x16x64_bf16 v[4:7], v[240:243], v[68:75], %[idx]\n\t" \
    "ds_read_b128 v[240:243], %[am] offset:13312\n\t" \
    "s_waitcnt lgkmcnt(7)\n\t" \
    "v_smfmac_f32_16x16x64_bf16 v[0:3], v[244:247], v[76:83], %[idx]\n\t" \
    "ds_read_b128 v[244:247], %[am] offset:14336\n\t" \
    "s_waitcnt lgkmcnt(7)\n\t" \
    "v_smfmac_f32_16x16x64_bf16 v[4:7], v[248:251], v[84:91], %[idx]\n\t" \
    "ds_read_b128 v[248:251], %[am] offset:15360\n\t" \
    "s_waitcnt lgkmcnt(7)\n\t" \
    "v_smfmac_f32_16x16x64_bf16 v[0:3], v[220:223], v[92:99], %[idx]\n\t" \
    "s_waitcnt lgkmcnt(6)\n\t" \
    "v_smfmac_f32_16x16x64_bf16 v[4:7], v[224:227], v[100:107], %[idx]\n\t" \
    "s_waitcnt lgkmcnt(5)\n\t" \
    "v_smfmac_f32_16x16x64_bf16 v[0:3], v[228:231], v[108:115], %[idx]\n\t" \
    "s_waitcnt lgkmcnt(4)\n\t" \
    "v_smfmac_f32_16x16x64_bf16 v[4:7], v[232:235], v[116:123], %[idx]\n\t" \
    "s_waitcnt lgkmcnt(3)\n\t" \
    "v_smfmac_f32_16x16x64_bf16 v[0:3], v[236:239], v[124:131], %[idx]\n\t" \
    "s_waitcnt lgkmcnt(2)\n\t" \
    "v_smfmac_f32_16x16x64_bf16 v[4:7], v[240:243], v[132:139], %[idx]\n\t" \
    "s_waitcnt lgkmcnt(1)\n\t" \
    "v_smfmac_f32_16x16x64_bf16 v[0:3], v[244:247], v[140:147], %[idx]\n\t" \
    "s_waitcnt lgkmcnt(0)\n\t" \
    "v_smfmac_f32_16x16x64_bf16 v[4:7], v[248:251], v[148:155], %[idx]\n\t" \
    "s_nop 5\n\t" \
    "v_add_f32 v0, v0, v1\n\t" \
    "v_add_f32 v2, v2, v3\n\t" \
    "v_add_f32 v4, v4, v5\n\t" \
    "v_add_f32 v6, v6, v7\n\t" \
    "v_add_f32 v0, v0, v4\n\t" \
    "v_add_f32 v2, v2, v6\n\t" \
    "v_cvt_pk_bf16_f32 v0, v0, v2\n\t" \
    "v_min_u32 v0, -2, v0\n\t" \
    "s_nop 1\n\t" \
    "v_mov_b32_dpp v1, v0 quad_perm:[1,1,1,1] row_mask:0xf bank_mask:0xf\n\t" \
    "v_mov_b32_dpp v2, v0 quad_perm:[2,2,2,2] row_mask:0xf bank_mask:0xf\n\t" \
    "v_mov_b32_dpp v3, v0 quad_perm:[3,3,3,3] row_mask:0xf bank_mask:0xf\n\t" \
    "s_mov_b64 exec, %[qm]\n\t" \
    "buffer_store_dwordx4 v[0:3], %[l4], %[rs], %[vo] offen sc1\n\t" \
    "s_mov_b64 exec, -1\n\t" \
    "s_nop 1\n\t" \
    :  \
    : [am] "v"(am_a), [idx] "v"(idx), [l4] "v"(ln * 4), [rs] "s"(rs), [vo] "s"(sb + vo), [qm] "s"(0x1111111111111111ull), "{v[28:35]}"(wv[0]), "{v[36:43]}"(wv[1]), "{v[44:51]}"(wv[2]), "{v[52:59]}"(wv[3]), "{v[60:67]}"(wv[4]), "{v[68:75]}"(wv[5]), "{v[76:83]}"(wv[6]), "{v[84:91]}"(wv[7]), "{v[92:99]}"(wv[8]), "{v[100:107]}"(wv[9]), "{v[108:115]}"(wv[10]), "{v[116:123]}"(wv[11]), "{v[124:131]}"(wv[12]), "{v[132:139]}"(wv[13]), "{v[140:147]}"(wv[14]), "{v[148:155]}"(wv[15]) \
    : "memory", "v0", "v1", "v2", "v3", "v4", "v5", "v6", "v7", "v220", "v221", "v222", "v223", "v224", "v225", "v226", "v227", "v228", "v229", "v230", "v231", "v232", "v233", "v234", "v235", "v236", "v237", "v238", "v239", "v240", "v241", "v242", "v243", "v244", "v245", "v246", "v247", "v248", "v249", "v250", "v251")

#define WO_V() \
  asm volatile( \
    "v_mov_b32 v0, 0\n\t" \
    "v_mov_b32 v1, 0\n\t" \
    "v_mov_b32 v2, 0\n\t" \
    "v_mov_b32 v3, 0\n\t" \
    "v_mov_b32 v4, 0\n\t" \
    "v_mov_b32 v5, 0\n\t" \
    "v_mov_b32 v6, 0\n\t" \
    "v_mov_b32 v7, 0\n\t" \
    "v_mov_b32 v8, 0\n\t" \
    "v_mov_b32 v9, 0\n\t" \
    "v_mov_b32 v10, 0\n\t" \
    "v_mov_b32 v11, 0\n\t" \
    "v_mov_b32 v12, 0\n\t" \
    "v_mov_b32 v13, 0\n\t" \
    "v_mov_b32 v14, 0\n\t" \
    "v_mov_b32 v15, 0\n\t" \
    "v_mov_b32 v252, 0\n\t" \
    "s_barrier\n\t" \
    GSA(4) \
    DGC \
    "buffer_load_dwordx4 v[220:223], %[l16], %[rs], %[s0] offen sc1\n\t" \
    "buffer_load_dwordx4 v[224:227], %[l16], %[rs], %[s1] offen sc1\n\t" \
    "A_%=:\n\t" \
    "s_waitcnt vmcnt(1)\n\t" \
    "v_smfmac_f32_16x16x64_bf16 v[0:3], v[220:223], v[156:163], %[idx]\n\t" \
    "v_max3_u32 v252, v252, v220, v221\n\t" \
    "v_smfmac_f32_16x16x64_bf16 v[4:7], v[220:223], v[164:171], %[idx]\n\t" \
    "v_max3_u32 v252, v252, v222, v223\n\t" \
    "v_smfmac_f32_16x16x64_bf16 v[8:11], v[220:223], v[172:179], %[idx]\n\t" \
    "v_smfmac_f32_16x16x64_bf16 v[12:15], v[220:223], v[180:187], %[idx]\n\t" \
    "s_waitcnt vmcnt(0)\n\t" \
    "v_smfmac_f32_16x16x64_bf16 v[0:3], v[224:227], v[188:195], %[idx]\n\t" \
    "v_max3_u32 v252, v252, v224, v225\n\t" \
    "v_smfmac_f32_16x16x64_bf16 v[4:7], v[224:227], v[196:203], %[idx]\n\t" \
    "v_max3_u32 v252, v252, v226, v227\n\t" \
    "v_smfmac_f32_16x16x64_bf16 v[8:11], v[224:227], v[204:211], %[idx]\n\t" \
    "v_smfmac_f32_16x16x64_bf16 v[12:15], v[224:227], v[212:219], %[idx]\n\t" \
    "v_cmp_eq_u32 vcc, -1, v252\n\t" \
    "s_cbranch_vccnz SL_%=\n\t" \
    "D_%=:\n\t" \
    GSA(1) \
    "s_nop 2\n\t" \
    "v_add_f32 v0, v0, v1\n\t" \
    "v_add_f32 v2, v2, v3\n\t" \
    "v_add_f32 v4, v4, v5\n\t" \
    "v_add_f32 v6, v6, v7\n\t" \
    "v_add_f32 v8, v8, v9\n\t" \
    "v_add_f32 v10, v10, v11\n\t" \
    "v_add_f32 v12, v12, v13\n\t" \
    "v_add_f32 v14, v14, v15\n\t" \
    "v_permlane32_swap_b32 v0, v4\n\t" \
    "v_permlane32_swap_b32 v2, v6\n\t" \
    "v_permlane32_swap_b32 v8, v12\n\t" \
    "v_permlane32_swap_b32 v10, v14\n\t" \
    "v_add_f32 v0, v0, v12\n\t" \
    "v_add_f32 v2, v2, v14\n\t" \
    "ds_write2_b32 %[rw], v0, v2 offset0:0 offset1:32\n\t" \
    "s_waitcnt lgkmcnt(0)\n\t" \
    "s_barrier\n\t" \
    "s_cmp_eq_u32 %[red], 0\n\t" \
    "s_cbranch_scc0 E_%=\n\t" \
    "ds_read2st64_b64 v[0:3], %[rr] offset0:0 offset1:1\n\t" \
    "ds_read2st64_b64 v[4:7], %[rr] offset0:2 offset1:3\n\t" \
    "ds_read2st64_b64 v[8:11], %[rr] offset0:4 offset1:5\n\t" \
    "ds_read2st64_b64 v[12:15], %[rr] offset0:6 offset1:7\n\t" \
    "s_waitcnt lgkmcnt(3)\n\t" \
    "v_pk_add_f32 v[0:1], v[0:1], v[2:3]\n\t" \
    "s_waitcnt lgkmcnt(2)\n\t" \
    "v_pk_add_f32 v[4:5], v[4:5], v[6:7]\n\t" \
    "s_waitcnt lgkmcnt(1)\n\t" \
    "v_pk_add_f32 v[8:9], v[8:9], v[10:11]\n\t" \
    "v_pk_add_f32 v[0:1], v[0:1], v[4:5]\n\t" \
    "s_waitcnt lgkmcnt(0)\n\t" \
    "v_pk_add_f32 v[12:13], v[12:13], v[14:15]\n\t" \
    "s_nop 0\n\t" \
    "v_pk_add_f32 v[8:9], v[8:9], v[12:13]\n\t" \
    "s_nop 0\n\t" \
    "v_pk_add_f32 v[0:1], v[0:1], v[8:9]\n\t" \
    "s_nop 0\n\t" \
    "v_cvt_pk_bf16_f32 v0, v0, v1\n\t" \
    "v_min_u32 v0, -2, v0\n\t" \
    "buffer_store_dword v0, %[mo], %[rs], %[sn] offen sc1\n\t" \
    "s_memrealtime %[t1]\n\t" \
    DGA \
    "s_waitcnt lgkmcnt(0)\n\t" \
    GSA(2) \
    "s_branch E_%=\n\t" \
    "SL_%=:\n\t" \
    "s_sleep 1\n\t" \
    "buffer_load_dwordx4 v[220:223], %[l16], %[rs], %[s0] offen sc1\n\t" \
    "buffer_load_dwordx4 v[224:227], %[l16], %[rs], %[s1] offen sc1\n\t" \
    "s_sub_u32 %[lim], %[lim], " STR(DEC2) "\n\t" \
    "s_cmp_gt_i32 %[lim], 0\n\t" \
    "s_cbranch_scc0 G_%=\n\t" \
    "s_nop 3\n\t" \
    "v_mov_b32 v0, 0\n\t" \
    "v_mov_b32 v1, 0\n\t" \
    "v_mov_b32 v2, 0\n\t" \
    "v_mov_b32 v3, 0\n\t" \
    "v_mov_b32 v4, 0\n\t" \
    "v_mov_b32 v5, 0\n\t" \
    "v_mov_b32 v6, 0\n\t" \
    "v_mov_b32 v7, 0\n\t" \
    "v_mov_b32 v8, 0\n\t" \
    "v_mov_b32 v9, 0\n\t" \
    "v_mov_b32 v10, 0\n\t" \
    "v_mov_b32 v11, 0\n\t" \
    "v_mov_b32 v12, 0\n\t" \
    "v_mov_b32 v13, 0\n\t" \
    "v_mov_b32 v14, 0\n\t" \
    "v_mov_b32 v15, 0\n\t" \
    "v_mov_b32 v252, 0\n\t" \
    "s_branch A_%=\n\t" \
    "G_%=:\n\t" \
    "s_waitcnt vmcnt(0)\n\t" \
    "s_branch D_%=\n\t" \
    "E_%=:\n\t" \
    GSW \
    : [lim] "+s"(lim) GSO(1) GSO(4), [t1] "=&s"(t1) DGO DGQ GSO(2) \
    : [l16] "v"(l16), [rs] "s"(rs), [idx] "v"(idx), [rw] "v"(rw_a), [s0] "s"(so[0]), [s1] "s"(so[1]), [rr] "v"(rr_a), [mo] "v"(mo_v), [sn] "s"(sn), [red] "s"(wid), "{v[156:163]}"(wo[0][0][0]), "{v[164:171]}"(wo[0][0][1]), "{v[172:179]}"(wo[0][1][0]), "{v[180:187]}"(wo[0][1][1]), "{v[188:195]}"(wo[1][0][0]), "{v[196:203]}"(wo[1][0][1]), "{v[204:211]}"(wo[1][1][0]), "{v[212:219]}"(wo[1][1][1]) \
    : "memory", "vcc", "scc", "v252", "v0", "v1", "v2", "v3", "v4", "v5", "v6", "v7", "v8", "v9", "v10", "v11", "v12", "v13", "v14", "v15", "v220", "v221", "v222", "v223", "v224", "v225", "v226", "v227")

#define WO_W() \
  asm volatile( \
    "v_mov_b32 v0, 0\n\t" \
    "v_mov_b32 v1, 0\n\t" \
    "v_mov_b32 v2, 0\n\t" \
    "v_mov_b32 v3, 0\n\t" \
    "v_mov_b32 v4, 0\n\t" \
    "v_mov_b32 v5, 0\n\t" \
    "v_mov_b32 v6, 0\n\t" \
    "v_mov_b32 v7, 0\n\t" \
    "v_mov_b32 v8, 0\n\t" \
    "v_mov_b32 v9, 0\n\t" \
    "v_mov_b32 v10, 0\n\t" \
    "v_mov_b32 v11, 0\n\t" \
    "v_mov_b32 v12, 0\n\t" \
    "v_mov_b32 v13, 0\n\t" \
    "v_mov_b32 v14, 0\n\t" \
    "v_mov_b32 v15, 0\n\t" \
    "v_mov_b32 v252, 0\n\t" \
    "s_barrier\n\t" \
    GSA(4) \
    "buffer_load_dwordx4 v[220:223], %[l16], %[rs], %[s0] offen sc1\n\t" \
    PACE \
    "buffer_load_dwordx4 v[224:227], %[l16], %[rs], %[s1] offen sc1\n\t" \
    PACE \
    "buffer_load_dwordx4 v[228:231], %[l16], %[rs], %[s2] offen sc1\n\t" \
    PACE \
    "buffer_load_dwordx4 v[232:235], %[l16], %[rs], %[s3] offen sc1\n\t" \
    PACE \
    "buffer_load_dwordx4 v[236:239], %[l16], %[rs], %[s4] offen sc1\n\t" \
    PACE \
    "buffer_load_dwordx4 v[240:243], %[l16], %[rs], %[s5] offen sc1\n\t" \
    "A_%=:\n\t" \
    "s_waitcnt vmcnt(5)\n\t" \
    "v_smfmac_f32_16x16x64_bf16 v[0:3], v[220:223], v[28:35], %[idx]\n\t" \
    "v_max3_u32 v252, v252, v220, v221\n\t" \
    "v_smfmac_f32_16x16x64_bf16 v[4:7], v[220:223], v[36:43], %[idx]\n\t" \
    "v_max3_u32 v252, v252, v222, v223\n\t" \
    "v_smfmac_f32_16x16x64_bf16 v[8:11], v[220:223], v[44:51], %[idx]\n\t" \
    "v_smfmac_f32_16x16x64_bf16 v[12:15], v[220:223], v[52:59], %[idx]\n\t" \
    "s_waitcnt vmcnt(4)\n\t" \
    "v_smfmac_f32_16x16x64_bf16 v[0:3], v[224:227], v[60:67], %[idx]\n\t" \
    "v_max3_u32 v252, v252, v224, v225\n\t" \
    "v_smfmac_f32_16x16x64_bf16 v[4:7], v[224:227], v[68:75], %[idx]\n\t" \
    "v_max3_u32 v252, v252, v226, v227\n\t" \
    "v_smfmac_f32_16x16x64_bf16 v[8:11], v[224:227], v[76:83], %[idx]\n\t" \
    "v_smfmac_f32_16x16x64_bf16 v[12:15], v[224:227], v[84:91], %[idx]\n\t" \
    "s_waitcnt vmcnt(3)\n\t" \
    "v_smfmac_f32_16x16x64_bf16 v[0:3], v[228:231], v[92:99], %[idx]\n\t" \
    "v_max3_u32 v252, v252, v228, v229\n\t" \
    "v_smfmac_f32_16x16x64_bf16 v[4:7], v[228:231], v[100:107], %[idx]\n\t" \
    "v_max3_u32 v252, v252, v230, v231\n\t" \
    "v_smfmac_f32_16x16x64_bf16 v[8:11], v[228:231], v[108:115], %[idx]\n\t" \
    "v_smfmac_f32_16x16x64_bf16 v[12:15], v[228:231], v[116:123], %[idx]\n\t" \
    "s_waitcnt vmcnt(2)\n\t" \
    "v_smfmac_f32_16x16x64_bf16 v[0:3], v[232:235], v[124:131], %[idx]\n\t" \
    "v_max3_u32 v252, v252, v232, v233\n\t" \
    "v_smfmac_f32_16x16x64_bf16 v[4:7], v[232:235], v[132:139], %[idx]\n\t" \
    "v_max3_u32 v252, v252, v234, v235\n\t" \
    "v_smfmac_f32_16x16x64_bf16 v[8:11], v[232:235], v[140:147], %[idx]\n\t" \
    "v_smfmac_f32_16x16x64_bf16 v[12:15], v[232:235], v[148:155], %[idx]\n\t" \
    "s_waitcnt vmcnt(1)\n\t" \
    "v_smfmac_f32_16x16x64_bf16 v[0:3], v[236:239], v[156:163], %[idx]\n\t" \
    "v_max3_u32 v252, v252, v236, v237\n\t" \
    "v_smfmac_f32_16x16x64_bf16 v[4:7], v[236:239], v[164:171], %[idx]\n\t" \
    "v_max3_u32 v252, v252, v238, v239\n\t" \
    "v_smfmac_f32_16x16x64_bf16 v[8:11], v[236:239], v[172:179], %[idx]\n\t" \
    "v_smfmac_f32_16x16x64_bf16 v[12:15], v[236:239], v[180:187], %[idx]\n\t" \
    "s_waitcnt vmcnt(0)\n\t" \
    "v_smfmac_f32_16x16x64_bf16 v[0:3], v[240:243], v[188:195], %[idx]\n\t" \
    "v_max3_u32 v252, v252, v240, v241\n\t" \
    "v_smfmac_f32_16x16x64_bf16 v[4:7], v[240:243], v[196:203], %[idx]\n\t" \
    "v_max3_u32 v252, v252, v242, v243\n\t" \
    "v_smfmac_f32_16x16x64_bf16 v[8:11], v[240:243], v[204:211], %[idx]\n\t" \
    "v_smfmac_f32_16x16x64_bf16 v[12:15], v[240:243], v[212:219], %[idx]\n\t" \
    "v_cmp_eq_u32 vcc, -1, v252\n\t" \
    "s_cbranch_vccnz SL_%=\n\t" \
    "D_%=:\n\t" \
    GSA(1) \
    "s_nop 2\n\t" \
    "v_add_f32 v0, v0, v1\n\t" \
    "v_add_f32 v2, v2, v3\n\t" \
    "v_add_f32 v4, v4, v5\n\t" \
    "v_add_f32 v6, v6, v7\n\t" \
    "v_add_f32 v8, v8, v9\n\t" \
    "v_add_f32 v10, v10, v11\n\t" \
    "v_add_f32 v12, v12, v13\n\t" \
    "v_add_f32 v14, v14, v15\n\t" \
    "v_permlane32_swap_b32 v0, v4\n\t" \
    "v_permlane32_swap_b32 v2, v6\n\t" \
    "v_permlane32_swap_b32 v8, v12\n\t" \
    "v_permlane32_swap_b32 v10, v14\n\t" \
    "v_add_f32 v0, v0, v12\n\t" \
    "v_add_f32 v2, v2, v14\n\t" \
    "ds_write2_b32 %[rw], v0, v2 offset0:0 offset1:32\n\t" \
    "s_waitcnt lgkmcnt(0)\n\t" \
    "s_barrier\n\t" \
    "s_branch E_%=\n\t" \
    "SL_%=:\n\t" \
    "s_sleep 1\n\t" \
    "buffer_load_dwordx4 v[220:223], %[l16], %[rs], %[s0] offen sc1\n\t" \
    "buffer_load_dwordx4 v[224:227], %[l16], %[rs], %[s1] offen sc1\n\t" \
    "buffer_load_dwordx4 v[228:231], %[l16], %[rs], %[s2] offen sc1\n\t" \
    "buffer_load_dwordx4 v[232:235], %[l16], %[rs], %[s3] offen sc1\n\t" \
    "buffer_load_dwordx4 v[236:239], %[l16], %[rs], %[s4] offen sc1\n\t" \
    "buffer_load_dwordx4 v[240:243], %[l16], %[rs], %[s5] offen sc1\n\t" \
    "s_sub_u32 %[lim], %[lim], " STR(DEC2) "\n\t" \
    "s_cmp_gt_i32 %[lim], 0\n\t" \
    "s_cbranch_scc0 G_%=\n\t" \
    "s_nop 3\n\t" \
    "v_mov_b32 v0, 0\n\t" \
    "v_mov_b32 v1, 0\n\t" \
    "v_mov_b32 v2, 0\n\t" \
    "v_mov_b32 v3, 0\n\t" \
    "v_mov_b32 v4, 0\n\t" \
    "v_mov_b32 v5, 0\n\t" \
    "v_mov_b32 v6, 0\n\t" \
    "v_mov_b32 v7, 0\n\t" \
    "v_mov_b32 v8, 0\n\t" \
    "v_mov_b32 v9, 0\n\t" \
    "v_mov_b32 v10, 0\n\t" \
    "v_mov_b32 v11, 0\n\t" \
    "v_mov_b32 v12, 0\n\t" \
    "v_mov_b32 v13, 0\n\t" \
    "v_mov_b32 v14, 0\n\t" \
    "v_mov_b32 v15, 0\n\t" \
    "v_mov_b32 v252, 0\n\t" \
    "s_branch A_%=\n\t" \
    "G_%=:\n\t" \
    "s_waitcnt vmcnt(0)\n\t" \
    "s_branch D_%=\n\t" \
    "E_%=:\n\t" \
    GSW \
    : [lim] "+s"(lim) GSO(1) GSO(4) \
    : [l16] "v"(l16), [rs] "s"(rs), [idx] "v"(idx), [rw] "v"(rw_a), [s0] "s"(so[0]), [s1] "s"(so[1]), [s2] "s"(so[2]), [s3] "s"(so[3]), [s4] "s"(so[4]), [s5] "s"(so[5]), "{v[28:35]}"(wo[0][0][0]), "{v[36:43]}"(wo[0][0][1]), "{v[44:51]}"(wo[0][1][0]), "{v[52:59]}"(wo[0][1][1]), "{v[60:67]}"(wo[1][0][0]), "{v[68:75]}"(wo[1][0][1]), "{v[76:83]}"(wo[1][1][0]), "{v[84:91]}"(wo[1][1][1]), "{v[92:99]}"(wo[2][0][0]), "{v[100:107]}"(wo[2][0][1]), "{v[108:115]}"(wo[2][1][0]), "{v[116:123]}"(wo[2][1][1]), "{v[124:131]}"(wo[3][0][0]), "{v[132:139]}"(wo[3][0][1]), "{v[140:147]}"(wo[3][1][0]), "{v[148:155]}"(wo[3][1][1]), "{v[156:163]}"(wo[4][0][0]), "{v[164:171]}"(wo[4][0][1]), "{v[172:179]}"(wo[4][1][0]), "{v[180:187]}"(wo[4][1][1]), "{v[188:195]}"(wo[5][0][0]), "{v[196:203]}"(wo[5][0][1]), "{v[204:211]}"(wo[5][1][0]), "{v[212:219]}"(wo[5][1][1]) \
    : "memory", "vcc", "scc", "v252", "v0", "v1", "v2", "v3", "v4", "v5", "v6", "v7", "v8", "v9", "v10", "v11", "v12", "v13", "v14", "v15", "v220", "v221", "v222", "v223", "v224", "v225", "v226", "v227", "v228", "v229", "v230", "v231", "v232", "v233", "v234", "v235", "v236", "v237", "v238", "v239", "v240", "v241", "v242", "v243")

#define S_LOOP_K(TE_, FIN_) \
  asm volatile( \
    "s_cmp_eq_u32 %[kh], 0\n\t" \
    "s_cbranch_scc0 L1_%=\n\t" \
    "L0_%=:\n\t" \
    "s_setprio 0\n\t" \
    "s_nop 3\n\t" \
    "ds_write_b128 %[pa], v[196:199] offset:0\n\t" \
    "ds_write_b128 %[pa], v[200:203] offset:1024\n\t" \
    "ds_write_b128 %[pa], v[204:207] offset:2048\n\t" \
    "ds_write_b128 %[pa], v[208:211] offset:3072\n\t" \
    "s_sub_u32 %[a], %[t], 1\n\t" \
    "v_mov_b32 v53, %[tag]\n\t" \
    "v_mov_b32 v52, %[a]\n\t" \
    "s_sub_u32 %[b], %[t], 2\n\t" \
    "s_cmp_lt_u32 %[b], " STR(SQUICK) "\n\t" \
    "s_cselect_b64 exec, %[fm], 0\n\t" \
    "buffer_store_dwordx2 v[52:53], %[l16], %[rs], %[fo] offen sc1\n\t" \
    "s_mov_b64 exec, -1\n\t" \
    "s_add_u32 m0, %[sd], 49152\n\t" \
    "s_add_u32 %[a], %[sx], 81920\n\t" \
    "s_waitcnt lgkmcnt(0)\n\t" \
    "buffer_load_dwordx4 %[l16], %[rs], %[a] offen offset:0 sc1 lds\n\t" \
    "buffer_load_dwordx4 %[l16], %[rs], %[a] offen offset:1024 sc1 lds\n\t" \
    "buffer_load_dwordx4 %[l16], %[rs], %[a] offen offset:2048 sc1 lds\n\t" \
    "buffer_load_dwordx4 %[l16], %[rs], %[a] offen offset:3072 sc1 lds\n\t" \
    "v_mfma_f32_16x16x32_bf16 v[0:3], v[68:71], v[20:23], 0\n\t" \
    "ds_read_b128 v[44:47], %[xp] offset:6144\n\t" \
    "v_mfma_f32_16x16x32_bf16 v[4:7], v[76:79], v[20:23], 0\n\t" \
    "ds_read_b128 v[48:51], %[xp] offset:7168\n\t" \
    "v_mfma_f32_16x16x32_bf16 v[8:11], v[68:71], v[24:27], 0\n\t" \
    "v_mfma_f32_16x16x32_bf16 v[12:15], v[76:79], v[24:27], 0\n\t" \
    "v_mfma_f32_16x16x32_bf16 v[0:3], v[72:75], v[28:31], v[0:3]\n\t" \
    "ds_read_b128 v[20:23], %[xp] offset:8192\n\t" \
    "v_mfma_f32_16x16x32_bf16 v[4:7], v[80:83], v[28:31], v[4:7]\n\t" \
    "ds_read_b128 v[24:27], %[xp] offset:9216\n\t" \
    "v_mfma_f32_16x16x32_bf16 v[8:11], v[72:75], v[32:35], v[8:11]\n\t" \
    "v_mfma_f32_16x16x32_bf16 v[12:15], v[80:83], v[32:35], v[12:15]\n\t" \
    "v_mfma_f32_16x16x32_bf16 v[0:3], v[84:87], v[36:39], v[0:3]\n\t" \
    "ds_read_b128 v[28:31], %[xp] offset:10240\n\t" \
    "v_mfma_f32_16x16x32_bf16 v[4:7], v[92:95], v[36:39], v[4:7]\n\t" \
    "ds_read_b128 v[32:35], %[xp] offset:11264\n\t" \
    "v_mfma_f32_16x16x32_bf16 v[8:11], v[84:87], v[40:43], v[8:11]\n\t" \
    "s_waitcnt lgkmcnt(4)\n\t" \
    "v_mfma_f32_16x16x32_bf16 v[12:15], v[92:95], v[40:43], v[12:15]\n\t" \
    "v_mfma_f32_16x16x32_bf16 v[0:3], v[88:91], v[44:47], v[0:3]\n\t" \
    "ds_read_b128 v[36:39], %[xp] offset:12288\n\t" \
    "v_mfma_f32_16x16x32_bf16 v[4:7], v[96:99], v[44:47], v[4:7]\n\t" \
    "ds_read_b128 v[40:43], %[xp] offset:13312\n\t" \
    "v_mfma_f32_16x16x32_bf16 v[8:11], v[88:91], v[48:51], v[8:11]\n\t" \
    "s_waitcnt lgkmcnt(4)\n\t" \
    "v_mfma_f32_16x16x32_bf16 v[12:15], v[96:99], v[48:51], v[12:15]\n\t" \
    "v_mfma_f32_16x16x32_bf16 v[0:3], v[100:103], v[20:23], v[0:3]\n\t" \
    "ds_read_b128 v[44:47], %[xp] offset:14336\n\t" \
    "v_mfma_f32_16x16x32_bf16 v[4:7], v[108:111], v[20:23], v[4:7]\n\t" \
    "ds_read_b128 v[48:51], %[xp] offset:15360\n\t" \
    "v_mfma_f32_16x16x32_bf16 v[8:11], v[100:103], v[24:27], v[8:11]\n\t" \
    "s_waitcnt lgkmcnt(4)\n\t" \
    "v_mfma_f32_16x16x32_bf16 v[12:15], v[108:111], v[24:27], v[12:15]\n\t" \
    "v_mfma_f32_16x16x32_bf16 v[0:3], v[104:107], v[28:31], v[0:3]\n\t" \
    "ds_read_b128 v[20:23], %[xp] offset:16384\n\t" \
    "v_mfma_f32_16x16x32_bf16 v[4:7], v[112:115], v[28:31], v[4:7]\n\t" \
    "ds_read_b128 v[24:27], %[xp] offset:17408\n\t" \
    "v_mfma_f32_16x16x32_bf16 v[8:11], v[104:107], v[32:35], v[8:11]\n\t" \
    "s_waitcnt lgkmcnt(4)\n\t" \
    "v_mfma_f32_16x16x32_bf16 v[12:15], v[112:115], v[32:35], v[12:15]\n\t" \
    "v_mfma_f32_16x16x32_bf16 v[0:3], v[116:119], v[36:39], v[0:3]\n\t" \
    "ds_read_b128 v[28:31], %[xp] offset:18432\n\t" \
    "v_mfma_f32_16x16x32_bf16 v[4:7], v[124:127], v[36:39], v[4:7]\n\t" \
    "ds_read_b128 v[32:35], %[xp] offset:19456\n\t" \
    "v_mfma_f32_16x16x32_bf16 v[8:11], v[116:119], v[40:43], v[8:11]\n\t" \
    "s_waitcnt lgkmcnt(4)\n\t" \
    "v_mfma_f32_16x16x32_bf16 v[12:15], v[124:127], v[40:43], v[12:15]\n\t" \
    "v_mfma_f32_16x16x32_bf16 v[0:3], v[120:123], v[44:47], v[0:3]\n\t" \
    "ds_read_b128 v[36:39], %[xp] offset:20480\n\t" \
    "v_mfma_f32_16x16x32_bf16 v[4:7], v[128:131], v[44:47], v[4:7]\n\t" \
    "ds_read_b128 v[40:43], %[xp] offset:21504\n\t" \
    "v_mfma_f32_16x16x32_bf16 v[8:11], v[120:123], v[48:51], v[8:11]\n\t" \
    "s_waitcnt lgkmcnt(4)\n\t" \
    "v_mfma_f32_16x16x32_bf16 v[12:15], v[128:131], v[48:51], v[12:15]\n\t" \
    "s_waitcnt vmcnt(4)\n\t" \
    "s_barrier\n\t" \
    "s_setprio 0\n\t" \
    "s_sub_u32 %[a], %[t], 1\n\t" \
    "v_mov_b32 v53, %[tag]\n\t" \
    "v_mov_b32 v52, %[a]\n\t" \
    "s_cmp_gt_u32 %[t], 1\n\t" \
    "s_cselect_b64 exec, %[fm], 0\n\t" \
    "buffer_store_dwordx2 v[52:53], %[l16], %[rs], %[fo] offen sc1\n\t" \
    "s_mov_b64 exec, %[pm]\n\t" \
    "s_mov_b32 m0, %[pd]\n\t" \
    "s_add_u32 %[a], %[sx], %[po]\n\t" \
    "buffer_load_dwordx4 %[l16], %[rs], %[a] offen sc1 lds\n\t" \
    "s_mov_b64 exec, -1\n\t" \
    "s_add_u32 m0, %[sd], 0\n\t" \
    "s_add_u32 %[a], %[sx], 131072\n\t" \
    "buffer_load_dwordx4 %[l16], %[rs], %[a] offen offset:0 sc1 lds\n\t" \
    "buffer_load_dwordx4 %[l16], %[rs], %[a] offen offset:1024 sc1 lds\n\t" \
    "buffer_load_dwordx4 %[l16], %[rs], %[a] offen offset:2048 sc1 lds\n\t" \
    "buffer_load_dwordx4 %[l16], %[rs], %[a] offen offset:3072 sc1 lds\n\t" \
    "v_mfma_f32_16x16x32_bf16 v[0:3], v[132:135], v[20:23], v[0:3]\n\t" \
    "ds_read_b128 v[44:47], %[xp] offset:22528\n\t" \
    "v_mfma_f32_16x16x32_bf16 v[4:7], v[140:143], v[20:23], v[4:7]\n\t" \
    "ds_read_b128 v[48:51], %[xp] offset:23552\n\t" \
    "v_mfma_f32_16x16x32_bf16 v[8:11], v[132:135], v[24:27], v[8:11]\n\t" \
    "s_waitcnt lgkmcnt(4)\n\t" \
    "v_mfma_f32_16x16x32_bf16 v[12:15], v[140:143], v[24:27], v[12:15]\n\t" \
    "v_mfma_f32_16x16x32_bf16 v[0:3], v[136:139], v[28:31], v[0:3]\n\t" \
    "ds_read_b128 v[20:23], %[xp] offset:24576\n\t" \
    "v_mfma_f32_16x16x32_bf16 v[4:7], v[144:147], v[28:31], v[4:7]\n\t" \
    "ds_read_b128 v[24:27], %[xp] offset:25600\n\t" \
    "v_mfma_f32_16x16x32_bf16 v[8:11], v[136:139], v[32:35], v[8:11]\n\t" \
    "s_waitcnt lgkmcnt(4)\n\t" \
    "v_mfma_f32_16x16x32_bf16 v[12:15], v[144:147], v[32:35], v[12:15]\n\t" \
    "v_mfma_f32_16x16x32_bf16 v[0:3], v[148:151], v[36:39], v[0:3]\n\t" \
    "ds_read_b128 v[28:31], %[xp] offset:26624\n\t" \
    "v_mfma_f32_16x16x32_bf16 v[4:7], v[156:159], v[36:39], v[4:7]\n\t" \
    "ds_read_b128 v[32:35], %[xp] offset:27648\n\t" \
    "v_mfma_f32_16x16x32_bf16 v[8:11], v[148:151], v[40:43], v[8:11]\n\t" \
    "s_waitcnt lgkmcnt(4)\n\t" \
    "v_mfma_f32_16x16x32_bf16 v[12:15], v[156:159], v[40:43], v[12:15]\n\t" \
    "v_mfma_f32_16x16x32_bf16 v[0:3], v[152:155], v[44:47], v[0:3]\n\t" \
    "ds_read_b128 v[36:39], %[xp] offset:28672\n\t" \
    "v_mfma_f32_16x16x32_bf16 v[4:7], v[160:163], v[44:47], v[4:7]\n\t" \
    "ds_read_b128 v[40:43], %[xp] offset:29696\n\t" \
    "v_mfma_f32_16x16x32_bf16 v[8:11], v[152:155], v[48:51], v[8:11]\n\t" \
    "s_waitcnt lgkmcnt(4)\n\t" \
    "v_mfma_f32_16x16x32_bf16 v[12:15], v[160:163], v[48:51], v[12:15]\n\t" \
    "v_mfma_f32_16x16x32_bf16 v[0:3], v[164:167], v[20:23], v[0:3]\n\t" \
    "ds_read_b128 v[44:47], %[xp] offset:30720\n\t" \
    "v_mfma_f32_16x16x32_bf16 v[4:7], v[172:175], v[20:23], v[4:7]\n\t" \
    "ds_read_b128 v[48:51], %[xp] offset:31744\n\t" \
    "v_mfma_f32_16x16x32_bf16 v[8:11], v[164:167], v[24:27], v[8:11]\n\t" \
    "s_waitcnt lgkmcnt(4)\n\t" \
    "v_mfma_f32_16x16x32_bf16 v[12:15], v[172:175], v[24:27], v[12:15]\n\t" \
    "v_mfma_f32_16x16x32_bf16 v[0:3], v[168:171], v[28:31], v[0:3]\n\t" \
    "ds_read_b128 v[20:23], %[xp] offset:32768\n\t" \
    "v_mfma_f32_16x16x32_bf16 v[4:7], v[176:179], v[28:31], v[4:7]\n\t" \
    "ds_read_b128 v[24:27], %[xp] offset:33792\n\t" \
    "v_mfma_f32_16x16x32_bf16 v[8:11], v[168:171], v[32:35], v[8:11]\n\t" \
    "s_waitcnt lgkmcnt(4)\n\t" \
    "v_mfma_f32_16x16x32_bf16 v[12:15], v[176:179], v[32:35], v[12:15]\n\t" \
    "v_mfma_f32_16x16x32_bf16 v[0:3], v[180:183], v[36:39], v[0:3]\n\t" \
    "ds_read_b128 v[28:31], %[xp] offset:34816\n\t" \
    "v_mfma_f32_16x16x32_bf16 v[4:7], v[188:191], v[36:39], v[4:7]\n\t" \
    "ds_read_b128 v[32:35], %[xp] offset:35840\n\t" \
    "v_mfma_f32_16x16x32_bf16 v[8:11], v[180:183], v[40:43], v[8:11]\n\t" \
    "s_waitcnt lgkmcnt(4)\n\t" \
    "v_mfma_f32_16x16x32_bf16 v[12:15], v[188:191], v[40:43], v[12:15]\n\t" \
    "v_mfma_f32_16x16x32_bf16 v[0:3], v[184:187], v[44:47], v[0:3]\n\t" \
    "ds_read_b128 v[36:39], %[xp] offset:36864\n\t" \
    "v_mfma_f32_16x16x32_bf16 v[4:7], v[192:195], v[44:47], v[4:7]\n\t" \
    "ds_read_b128 v[40:43], %[xp] offset:37888\n\t" \
    "v_mfma_f32_16x16x32_bf16 v[8:11], v[184:187], v[48:51], v[8:11]\n\t" \
    "s_waitcnt lgkmcnt(4)\n\t" \
    "v_mfma_f32_16x16x32_bf16 v[12:15], v[192:195], v[48:51], v[12:15]\n\t" \
    "s_add_u32 %[sx], %[sx], 65536\n\t" \
    "s_add_u32 %[t], %[t], 1\n\t" \
    "s_waitcnt vmcnt(4)\n\t" \
    "s_barrier\n\t" \
    "s_setprio 0\n\t" \
    "s_nop 3\n\t" \
    "ds_write_b128 %[pa], v[0:3] offset:0\n\t" \
    "ds_write_b128 %[pa], v[4:7] offset:1024\n\t" \
    "ds_write_b128 %[pa], v[8:11] offset:2048\n\t" \
    "ds_write_b128 %[pa], v[12:15] offset:3072\n\t" \
    "s_sub_u32 %[a], %[t], 1\n\t" \
    "v_mov_b32 v53, %[tag]\n\t" \
    "v_mov_b32 v52, %[a]\n\t" \
    "s_sub_u32 %[b], %[t], 2\n\t" \
    "s_cmp_lt_u32 %[b], " STR(SQUICK) "\n\t" \
    "s_cselect_b64 exec, %[fm], 0\n\t" \
    "buffer_store_dwordx2 v[52:53], %[l16], %[rs], %[fo] offen sc1\n\t" \
    "s_mov_b64 exec, -1\n\t" \
    "s_add_u32 m0, %[sd], 16384\n\t" \
    "s_add_u32 %[a], %[sx], 81920\n\t" \
    "s_waitcnt lgkmcnt(0)\n\t" \
    "buffer_load_dwordx4 %[l16], %[rs], %[a] offen offset:0 sc1 lds\n\t" \
    "buffer_load_dwordx4 %[l16], %[rs], %[a] offen offset:1024 sc1 lds\n\t" \
    "buffer_load_dwordx4 %[l16], %[rs], %[a] offen offset:2048 sc1 lds\n\t" \
    "buffer_load_dwordx4 %[l16], %[rs], %[a] offen offset:3072 sc1 lds\n\t" \
    "v_mfma_f32_16x16x32_bf16 v[196:199], v[68:71], v[20:23], 0\n\t" \
    "ds_read_b128 v[44:47], %[xp] offset:38912\n\t" \
    "v_mfma_f32_16x16x32_bf16 v[200:203], v[76:79], v[20:23], 0\n\t" \
    "ds_read_b128 v[48:51], %[xp] offset:39936\n\t" \
    "v_mfma_f32_16x16x32_bf16 v[204:207], v[68:71], v[24:27], 0\n\t" \
    "v_mfma_f32_16x16x32_bf16 v[208:211], v[76:79], v[24:27], 0\n\t" \
    "v_mfma_f32_16x16x32_bf16 v[196:199], v[72:75], v[28:31], v[196:199]\n\t" \
    "ds_read_b128 v[20:23], %[xp] offset:40960\n\t" \
    "v_mfma_f32_16x16x32_bf16 v[200:203], v[80:83], v[28:31], v[200:203]\n\t" \
    "ds_read_b128 v[24:27], %[xp] offset:41984\n\t" \
    "v_mfma_f32_16x16x32_bf16 v[204:207], v[72:75], v[32:35], v[204:207]\n\t" \
    "v_mfma_f32_16x16x32_bf16 v[208:211], v[80:83], v[32:35], v[208:211]\n\t" \
    "v_mfma_f32_16x16x32_bf16 v[196:199], v[84:87], v[36:39], v[196:199]\n\t" \
    "ds_read_b128 v[28:31], %[xp] offset:43008\n\t" \
    "v_mfma_f32_16x16x32_bf16 v[200:203], v[92:95], v[36:39], v[200:203]\n\t" \
    "ds_read_b128 v[32:35], %[xp] offset:44032\n\t" \
    "v_mfma_f32_16x16x32_bf16 v[204:207], v[84:87], v[40:43], v[204:207]\n\t" \
    "s_waitcnt lgkmcnt(4)\n\t" \
    "v_mfma_f32_16x16x32_bf16 v[208:211], v[92:95], v[40:43], v[208:211]\n\t" \
    "v_mfma_f32_16x16x32_bf16 v[196:199], v[88:91], v[44:47], v[196:199]\n\t" \
    "ds_read_b128 v[36:39], %[xp] offset:45056\n\t" \
    "v_mfma_f32_16x16x32_bf16 v[200:203], v[96:99], v[44:47], v[200:203]\n\t" \
    "ds_read_b128 v[40:43], %[xp] offset:46080\n\t" \
    "v_mfma_f32_16x16x32_bf16 v[204:207], v[88:91], v[48:51], v[204:207]\n\t" \
    "s_waitcnt lgkmcnt(4)\n\t" \
    "v_mfma_f32_16x16x32_bf16 v[208:211], v[96:99], v[48:51], v[208:211]\n\t" \
    "v_mfma_f32_16x16x32_bf16 v[196:199], v[100:103], v[20:23], v[196:199]\n\t" \
    "ds_read_b128 v[44:47], %[xp] offset:47104\n\t" \
    "v_mfma_f32_16x16x32_bf16 v[200:203], v[108:111], v[20:23], v[200:203]\n\t" \
    "ds_read_b128 v[48:51], %[xp] offset:48128\n\t" \
    "v_mfma_f32_16x16x32_bf16 v[204:207], v[100:103], v[24:27], v[204:207]\n\t" \
    "s_waitcnt lgkmcnt(4)\n\t" \
    "v_mfma_f32_16x16x32_bf16 v[208:211], v[108:111], v[24:27], v[208:211]\n\t" \
    "v_mfma_f32_16x16x32_bf16 v[196:199], v[104:107], v[28:31], v[196:199]\n\t" \
    "ds_read_b128 v[20:23], %[xp] offset:49152\n\t" \
    "v_mfma_f32_16x16x32_bf16 v[200:203], v[112:115], v[28:31], v[200:203]\n\t" \
    "ds_read_b128 v[24:27], %[xp] offset:50176\n\t" \
    "v_mfma_f32_16x16x32_bf16 v[204:207], v[104:107], v[32:35], v[204:207]\n\t" \
    "s_waitcnt lgkmcnt(4)\n\t" \
    "v_mfma_f32_16x16x32_bf16 v[208:211], v[112:115], v[32:35], v[208:211]\n\t" \
    "v_mfma_f32_16x16x32_bf16 v[196:199], v[116:119], v[36:39], v[196:199]\n\t" \
    "ds_read_b128 v[28:31], %[xp] offset:51200\n\t" \
    "v_mfma_f32_16x16x32_bf16 v[200:203], v[124:127], v[36:39], v[200:203]\n\t" \
    "ds_read_b128 v[32:35], %[xp] offset:52224\n\t" \
    "v_mfma_f32_16x16x32_bf16 v[204:207], v[116:119], v[40:43], v[204:207]\n\t" \
    "s_waitcnt lgkmcnt(4)\n\t" \
    "v_mfma_f32_16x16x32_bf16 v[208:211], v[124:127], v[40:43], v[208:211]\n\t" \
    "v_mfma_f32_16x16x32_bf16 v[196:199], v[120:123], v[44:47], v[196:199]\n\t" \
    "ds_read_b128 v[36:39], %[xp] offset:53248\n\t" \
    "v_mfma_f32_16x16x32_bf16 v[200:203], v[128:131], v[44:47], v[200:203]\n\t" \
    "ds_read_b128 v[40:43], %[xp] offset:54272\n\t" \
    "v_mfma_f32_16x16x32_bf16 v[204:207], v[120:123], v[48:51], v[204:207]\n\t" \
    "s_waitcnt lgkmcnt(4)\n\t" \
    "v_mfma_f32_16x16x32_bf16 v[208:211], v[128:131], v[48:51], v[208:211]\n\t" \
    "s_waitcnt vmcnt(4)\n\t" \
    "s_barrier\n\t" \
    "s_setprio 0\n\t" \
    "s_sub_u32 %[a], %[t], 1\n\t" \
    "v_mov_b32 v53, %[tag]\n\t" \
    "v_mov_b32 v52, %[a]\n\t" \
    "s_cmp_gt_u32 %[t], 1\n\t" \
    "s_cselect_b64 exec, %[fm], 0\n\t" \
    "buffer_store_dwordx2 v[52:53], %[l16], %[rs], %[fo] offen sc1\n\t" \
    "s_mov_b64 exec, %[pm]\n\t" \
    "s_mov_b32 m0, %[pd]\n\t" \
    "s_add_u32 %[a], %[sx], %[po]\n\t" \
    "buffer_load_dwordx4 %[l16], %[rs], %[a] offen sc1 lds\n\t" \
    "s_mov_b64 exec, -1\n\t" \
    "s_add_u32 m0, %[sd], 32768\n\t" \
    "s_add_u32 %[a], %[sx], 131072\n\t" \
    "buffer_load_dwordx4 %[l16], %[rs], %[a] offen offset:0 sc1 lds\n\t" \
    "buffer_load_dwordx4 %[l16], %[rs], %[a] offen offset:1024 sc1 lds\n\t" \
    "buffer_load_dwordx4 %[l16], %[rs], %[a] offen offset:2048 sc1 lds\n\t" \
    "buffer_load_dwordx4 %[l16], %[rs], %[a] offen offset:3072 sc1 lds\n\t" \
    "v_mfma_f32_16x16x32_bf16 v[196:199], v[132:135], v[20:23], v[196:199]\n\t" \
    "ds_read_b128 v[44:47], %[xp] offset:55296\n\t" \
    "v_mfma_f32_16x16x32_bf16 v[200:203], v[140:143], v[20:23], v[200:203]\n\t" \
    "ds_read_b128 v[48:51], %[xp] offset:56320\n\t" \
    "v_mfma_f32_16x16x32_bf16 v[204:207], v[132:135], v[24:27], v[204:207]\n\t" \
    "s_waitcnt lgkmcnt(4)\n\t" \
    "v_mfma_f32_16x16x32_bf16 v[208:211], v[140:143], v[24:27], v[208:211]\n\t" \
    "v_mfma_f32_16x16x32_bf16 v[196:199], v[136:139], v[28:31], v[196:199]\n\t" \
    "ds_read_b128 v[20:23], %[xp] offset:57344\n\t" \
    "v_mfma_f32_16x16x32_bf16 v[200:203], v[144:147], v[28:31], v[200:203]\n\t" \
    "ds_read_b128 v[24:27], %[xp] offset:58368\n\t" \
    "v_mfma_f32_16x16x32_bf16 v[204:207], v[136:139], v[32:35], v[204:207]\n\t" \
    "s_waitcnt lgkmcnt(4)\n\t" \
    "v_mfma_f32_16x16x32_bf16 v[208:211], v[144:147], v[32:35], v[208:211]\n\t" \
    "v_mfma_f32_16x16x32_bf16 v[196:199], v[148:151], v[36:39], v[196:199]\n\t" \
    "ds_read_b128 v[28:31], %[xp] offset:59392\n\t" \
    "v_mfma_f32_16x16x32_bf16 v[200:203], v[156:159], v[36:39], v[200:203]\n\t" \
    "ds_read_b128 v[32:35], %[xp] offset:60416\n\t" \
    "v_mfma_f32_16x16x32_bf16 v[204:207], v[148:151], v[40:43], v[204:207]\n\t" \
    "s_waitcnt lgkmcnt(4)\n\t" \
    "v_mfma_f32_16x16x32_bf16 v[208:211], v[156:159], v[40:43], v[208:211]\n\t" \
    "v_mfma_f32_16x16x32_bf16 v[196:199], v[152:155], v[44:47], v[196:199]\n\t" \
    "ds_read_b128 v[36:39], %[xp] offset:61440\n\t" \
    "v_mfma_f32_16x16x32_bf16 v[200:203], v[160:163], v[44:47], v[200:203]\n\t" \
    "ds_read_b128 v[40:43], %[xp] offset:62464\n\t" \
    "v_mfma_f32_16x16x32_bf16 v[204:207], v[152:155], v[48:51], v[204:207]\n\t" \
    "s_waitcnt lgkmcnt(4)\n\t" \
    "v_mfma_f32_16x16x32_bf16 v[208:211], v[160:163], v[48:51], v[208:211]\n\t" \
    "v_mfma_f32_16x16x32_bf16 v[196:199], v[164:167], v[20:23], v[196:199]\n\t" \
    "ds_read_b128 v[44:47], %[xp] offset:63488\n\t" \
    "v_mfma_f32_16x16x32_bf16 v[200:203], v[172:175], v[20:23], v[200:203]\n\t" \
    "ds_read_b128 v[48:51], %[xp] offset:64512\n\t" \
    "v_mfma_f32_16x16x32_bf16 v[204:207], v[164:167], v[24:27], v[204:207]\n\t" \
    "s_waitcnt lgkmcnt(4)\n\t" \
    "v_mfma_f32_16x16x32_bf16 v[208:211], v[172:175], v[24:27], v[208:211]\n\t" \
    "v_mfma_f32_16x16x32_bf16 v[196:199], v[168:171], v[28:31], v[196:199]\n\t" \
    "ds_read_b128 v[20:23], %[xp] offset:0\n\t" \
    "v_mfma_f32_16x16x32_bf16 v[200:203], v[176:179], v[28:31], v[200:203]\n\t" \
    "ds_read_b128 v[24:27], %[xp] offset:1024\n\t" \
    "v_mfma_f32_16x16x32_bf16 v[204:207], v[168:171], v[32:35], v[204:207]\n\t" \
    "s_waitcnt lgkmcnt(4)\n\t" \
    "v_mfma_f32_16x16x32_bf16 v[208:211], v[176:179], v[32:35], v[208:211]\n\t" \
    "v_mfma_f32_16x16x32_bf16 v[196:199], v[180:183], v[36:39], v[196:199]\n\t" \
    "ds_read_b128 v[28:31], %[xp] offset:2048\n\t" \
    "v_mfma_f32_16x16x32_bf16 v[200:203], v[188:191], v[36:39], v[200:203]\n\t" \
    "ds_read_b128 v[32:35], %[xp] offset:3072\n\t" \
    "v_mfma_f32_16x16x32_bf16 v[204:207], v[180:183], v[40:43], v[204:207]\n\t" \
    "s_waitcnt lgkmcnt(4)\n\t" \
    "v_mfma_f32_16x16x32_bf16 v[208:211], v[188:191], v[40:43], v[208:211]\n\t" \
    "v_mfma_f32_16x16x32_bf16 v[196:199], v[184:187], v[44:47], v[196:199]\n\t" \
    "ds_read_b128 v[36:39], %[xp] offset:4096\n\t" \
    "v_mfma_f32_16x16x32_bf16 v[200:203], v[192:195], v[44:47], v[200:203]\n\t" \
    "ds_read_b128 v[40:43], %[xp] offset:5120\n\t" \
    "v_mfma_f32_16x16x32_bf16 v[204:207], v[184:187], v[48:51], v[204:207]\n\t" \
    "s_waitcnt lgkmcnt(4)\n\t" \
    "v_mfma_f32_16x16x32_bf16 v[208:211], v[192:195], v[48:51], v[208:211]\n\t" \
    "s_add_u32 %[sx], %[sx], 65536\n\t" \
    "s_add_u32 %[t], %[t], 1\n\t" \
    "s_waitcnt vmcnt(4)\n\t" \
    "s_barrier\n\t" \
    "s_cmp_lt_u32 %[t], %[te]\n\t" \
    "s_cbranch_scc1 L0_%=\n\t" \
    "s_cmp_lg_u32 %[fin], 0\n\t" \
    "s_cbranch_scc0 E_%=\n\t" \
    "s_nop 4\n\t" \
    "ds_write_b128 %[pa], v[196:199] offset:0\n\t" \
    "ds_write_b128 %[pa], v[200:203] offset:1024\n\t" \
    "ds_write_b128 %[pa], v[204:207] offset:2048\n\t" \
    "ds_write_b128 %[pa], v[208:211] offset:3072\n\t" \
    "s_waitcnt lgkmcnt(0)\n\t" \
    "s_barrier\n\t" \
    "s_branch E_%=\n\t" \
    "L1_%=:\n\t" \
    "s_setprio 1\n\t" \
    "s_add_u32 m0, %[sd], 49152\n\t" \
    "s_add_u32 %[a], %[sx], 81920\n\t" \
    "v_mfma_f32_16x16x32_bf16 v[0:3], v[68:71], v[20:23], 0\n\t" \
    "ds_read_b128 v[44:47], %[xp] offset:6144\n\t" \
    "v_mfma_f32_16x16x32_bf16 v[4:7], v[76:79], v[20:23], 0\n\t" \
    "ds_read_b128 v[48:51], %[xp] offset:7168\n\t" \
    "v_mfma_f32_16x16x32_bf16 v[8:11], v[68:71], v[24:27], 0\n\t" \
    "s_waitcnt lgkmcnt(4)\n\t" \
    "v_mfma_f32_16x16x32_bf16 v[12:15], v[76:79], v[24:27], 0\n\t" \
    "v_mfma_f32_16x16x32_bf16 v[0:3], v[72:75], v[28:31], v[0:3]\n\t" \
    "ds_read_b128 v[20:23], %[xp] offset:8192\n\t" \
    "v_mfma_f32_16x16x32_bf16 v[4:7], v[80:83], v[28:31], v[4:7]\n\t" \
    "ds_read_b128 v[24:27], %[xp] offset:9216\n\t" \
    "v_mfma_f32_16x16x32_bf16 v[8:11], v[72:75], v[32:35], v[8:11]\n\t" \
    "s_waitcnt lgkmcnt(4)\n\t" \
    "v_mfma_f32_16x16x32_bf16 v[12:15], v[80:83], v[32:35], v[12:15]\n\t" \
    "v_mfma_f32_16x16x32_bf16 v[0:3], v[84:87], v[36:39], v[0:3]\n\t" \
    "ds_read_b128 v[28:31], %[xp] offset:10240\n\t" \
    "v_mfma_f32_16x16x32_bf16 v[4:7], v[92:95], v[36:39], v[4:7]\n\t" \
    "ds_read_b128 v[32:35], %[xp] offset:11264\n\t" \
    "v_mfma_f32_16x16x32_bf16 v[8:11], v[84:87], v[40:43], v[8:11]\n\t" \
    "s_waitcnt lgkmcnt(4)\n\t" \
    "v_mfma_f32_16x16x32_bf16 v[12:15], v[92:95], v[40:43], v[12:15]\n\t" \
    "v_mfma_f32_16x16x32_bf16 v[0:3], v[88:91], v[44:47], v[0:3]\n\t" \
    "ds_read_b128 v[36:39], %[xp] offset:12288\n\t" \
    "v_mfma_f32_16x16x32_bf16 v[4:7], v[96:99], v[44:47], v[4:7]\n\t" \
    "ds_read_b128 v[40:43], %[xp] offset:13312\n\t" \
    "v_mfma_f32_16x16x32_bf16 v[8:11], v[88:91], v[48:51], v[8:11]\n\t" \
    "s_waitcnt lgkmcnt(4)\n\t" \
    "v_mfma_f32_16x16x32_bf16 v[12:15], v[96:99], v[48:51], v[12:15]\n\t" \
    "v_mfma_f32_16x16x32_bf16 v[0:3], v[100:103], v[20:23], v[0:3]\n\t" \
    "ds_read_b128 v[44:47], %[xp] offset:14336\n\t" \
    "v_mfma_f32_16x16x32_bf16 v[4:7], v[108:111], v[20:23], v[4:7]\n\t" \
    "ds_read_b128 v[48:51], %[xp] offset:15360\n\t" \
    "v_mfma_f32_16x16x32_bf16 v[8:11], v[100:103], v[24:27], v[8:11]\n\t" \
    "s_waitcnt lgkmcnt(4)\n\t" \
    "v_mfma_f32_16x16x32_bf16 v[12:15], v[108:111], v[24:27], v[12:15]\n\t" \
    "buffer_load_dwordx4 %[l16], %[rs], %[a] offen offset:0 sc1 lds\n\t" \
    "v_mfma_f32_16x16x32_bf16 v[0:3], v[104:107], v[28:31], v[0:3]\n\t" \
    "ds_read_b128 v[20:23], %[xp] offset:16384\n\t" \
    "v_mfma_f32_16x16x32_bf16 v[4:7], v[112:115], v[28:31], v[4:7]\n\t" \
    "ds_read_b128 v[24:27], %[xp] offset:17408\n\t" \
    "v_mfma_f32_16x16x32_bf16 v[8:11], v[104:107], v[32:35], v[8:11]\n\t" \
    "s_waitcnt lgkmcnt(4)\n\t" \
    "v_mfma_f32_16x16x32_bf16 v[12:15], v[112:115], v[32:35], v[12:15]\n\t" \
    "buffer_load_dwordx4 %[l16], %[rs], %[a] offen offset:1024 sc1 lds\n\t" \
    "v_mfma_f32_16x16x32_bf16 v[0:3], v[116:119], v[36:39], v[0:3]\n\t" \
    "ds_read_b128 v[28:31], %[xp] offset:18432\n\t" \
    "v_mfma_f32_16x16x32_bf16 v[4:7], v[124:127], v[36:39], v[4:7]\n\t" \
    "ds_read_b128 v[32:35], %[xp] offset:19456\n\t" \
    "v_mfma_f32_16x16x32_bf16 v[8:11], v[116:119], v[40:43], v[8:11]\n\t" \
    "s_waitcnt lgkmcnt(4)\n\t" \
    "v_mfma_f32_16x16x32_bf16 v[12:15], v[124:127], v[40:43], v[12:15]\n\t" \
    "buffer_load_dwordx4 %[l16], %[rs], %[a] offen offset:2048 sc1 lds\n\t" \
    "v_mfma_f32_16x16x32_bf16 v[0:3], v[120:123], v[44:47], v[0:3]\n\t" \
    "ds_read_b128 v[36:39], %[xp] offset:20480\n\t" \
    "v_mfma_f32_16x16x32_bf16 v[4:7], v[128:131], v[44:47], v[4:7]\n\t" \
    "ds_read_b128 v[40:43], %[xp] offset:21504\n\t" \
    "v_mfma_f32_16x16x32_bf16 v[8:11], v[120:123], v[48:51], v[8:11]\n\t" \
    "s_waitcnt lgkmcnt(4)\n\t" \
    "v_mfma_f32_16x16x32_bf16 v[12:15], v[128:131], v[48:51], v[12:15]\n\t" \
    "buffer_load_dwordx4 %[l16], %[rs], %[a] offen offset:3072 sc1 lds\n\t" \
    "s_waitcnt vmcnt(4)\n\t" \
    "s_barrier\n\t" \
    "s_setprio 1\n\t" \
    "s_add_u32 m0, %[sd], 0\n\t" \
    "s_add_u32 %[a], %[sx], 131072\n\t" \
    "s_add_u32 %[b], %[oo], 0x1000\n\t" \
    "s_cmp_lg_u32 %[t], 0\n\t" \
    "s_cselect_b64 %[em], -1, 0\n\t" \
    "v_mfma_f32_16x16x32_bf16 v[0:3], v[132:135], v[20:23], v[0:3]\n\t" \
    "ds_read_b128 v[44:47], %[xp] offset:22528\n\t" \
    "v_mfma_f32_16x16x32_bf16 v[4:7], v[140:143], v[20:23], v[4:7]\n\t" \
    "ds_read_b128 v[48:51], %[xp] offset:23552\n\t" \
    "v_mfma_f32_16x16x32_bf16 v[8:11], v[132:135], v[24:27], v[8:11]\n\t" \
    "ds_read_b128 v[52:55], %[pa] offset:0\n\t" \
    "s_waitcnt lgkmcnt(5)\n\t" \
    "v_mfma_f32_16x16x32_bf16 v[12:15], v[140:143], v[24:27], v[12:15]\n\t" \
    "buffer_load_dwordx4 %[l16], %[rs], %[a] offen offset:0 sc1 lds\n\t" \
    "v_mfma_f32_16x16x32_bf16 v[0:3], v[136:139], v[28:31], v[0:3]\n\t" \
    "ds_read_b128 v[20:23], %[xp] offset:24576\n\t" \
    "v_mfma_f32_16x16x32_bf16 v[4:7], v[144:147], v[28:31], v[4:7]\n\t" \
    "ds_read_b128 v[24:27], %[xp] offset:25600\n\t" \
    "v_mfma_f32_16x16x32_bf16 v[8:11], v[136:139], v[32:35], v[8:11]\n\t" \
    "ds_read_b128 v[56:59], %[pa] offset:1024\n\t" \
    "s_waitcnt lgkmcnt(6)\n\t" \
    "v_mfma_f32_16x16x32_bf16 v[12:15], v[144:147], v[32:35], v[12:15]\n\t" \
    "buffer_load_dwordx4 %[l16], %[rs], %[a] offen offset:1024 sc1 lds\n\t" \
    "v_mfma_f32_16x16x32_bf16 v[0:3], v[148:151], v[36:39], v[0:3]\n\t" \
    "ds_read_b128 v[28:31], %[xp] offset:26624\n\t" \
    "v_mfma_f32_16x16x32_bf16 v[4:7], v[156:159], v[36:39], v[4:7]\n\t" \
    "ds_read_b128 v[32:35], %[xp] offset:27648\n\t" \
    "v_mfma_f32_16x16x32_bf16 v[8:11], v[148:151], v[40:43], v[8:11]\n\t" \
    "ds_read_b128 v[60:63], %[pa] offset:2048\n\t" \
    "s_waitcnt lgkmcnt(7)\n\t" \
    "v_mfma_f32_16x16x32_bf16 v[12:15], v[156:159], v[40:43], v[12:15]\n\t" \
    "buffer_load_dwordx4 %[l16], %[rs], %[a] offen offset:2048 sc1 lds\n\t" \
    "v_mfma_f32_16x16x32_bf16 v[0:3], v[152:155], v[44:47], v[0:3]\n\t" \
    "ds_read_b128 v[36:39], %[xp] offset:28672\n\t" \
    "s_waitcnt lgkmcnt(7)\n\t" \
    "v_add_f32 v196, v196, v52\n\t" \
    "v_mfma_f32_16x16x32_bf16 v[4:7], v[160:163], v[44:47], v[4:7]\n\t" \
    "ds_read_b128 v[40:43], %[xp] offset:29696\n\t" \
    "v_add_f32 v197, v197, v53\n\t" \
    "v_mfma_f32_16x16x32_bf16 v[8:11], v[152:155], v[48:51], v[8:11]\n\t" \
    "ds_read_b128 v[64:67], %[pa] offset:3072\n\t" \
    "s_waitcnt lgkmcnt(7)\n\t" \
    "v_mfma_f32_16x16x32_bf16 v[12:15], v[160:163], v[48:51], v[12:15]\n\t" \
    "buffer_load_dwordx4 %[l16], %[rs], %[a] offen offset:3072 sc1 lds\n\t" \
    "v_add_f32 v198, v198, v54\n\t" \
    "v_add_f32 v199, v199, v55\n\t" \
    "v_mfma_f32_16x16x32_bf16 v[0:3], v[164:167], v[20:23], v[0:3]\n\t" \
    "ds_read_b128 v[44:47], %[xp] offset:30720\n\t" \
    "s_waitcnt lgkmcnt(7)\n\t" \
    "v_add_f32 v200, v200, v56\n\t" \
    "v_mfma_f32_16x16x32_bf16 v[4:7], v[172:175], v[20:23], v[4:7]\n\t" \
    "ds_read_b128 v[48:51], %[xp] offset:31744\n\t" \
    "v_add_f32 v201, v201, v57\n\t" \
    "v_mfma_f32_16x16x32_bf16 v[8:11], v[164:167], v[24:27], v[8:11]\n\t" \
    "v_add_f32 v202, v202, v58\n\t" \
    "s_waitcnt lgkmcnt(6)\n\t" \
    "v_mfma_f32_16x16x32_bf16 v[12:15], v[172:175], v[24:27], v[12:15]\n\t" \
    "v_add_f32 v203, v203, v59\n\t" \
    "s_waitcnt lgkmcnt(5)\n\t" \
    "v_add_f32 v204, v204, v60\n\t" \
    "v_add_f32 v205, v205, v61\n\t" \
    "v_mfma_f32_16x16x32_bf16 v[0:3], v[168:171], v[28:31], v[0:3]\n\t" \
    "ds_read_b128 v[20:23], %[xp] offset:32768\n\t" \
    "v_add_f32 v206, v206, v62\n\t" \
    "v_mfma_f32_16x16x32_bf16 v[4:7], v[176:179], v[28:31], v[4:7]\n\t" \
    "ds_read_b128 v[24:27], %[xp] offset:33792\n\t" \
    "v_add_f32 v207, v207, v63\n\t" \
    "v_mfma_f32_16x16x32_bf16 v[8:11], v[168:171], v[32:35], v[8:11]\n\t" \
    "s_waitcnt lgkmcnt(4)\n\t" \
    "v_add_f32 v208, v208, v64\n\t" \
    "v_mfma_f32_16x16x32_bf16 v[12:15], v[176:179], v[32:35], v[12:15]\n\t" \
    "v_add_f32 v209, v209, v65\n\t" \
    "v_add_f32 v210, v210, v66\n\t" \
    "v_add_f32 v211, v211, v67\n\t" \
    "v_mfma_f32_16x16x32_bf16 v[0:3], v[180:183], v[36:39], v[0:3]\n\t" \
    "ds_read_b128 v[28:31], %[xp] offset:34816\n\t" \
    "v_cvt_pk_bf16_f32 v52, v196, v200\n\t" \
    "v_mfma_f32_16x16x32_bf16 v[4:7], v[188:191], v[36:39], v[4:7]\n\t" \
    "ds_read_b128 v[32:35], %[xp] offset:35840\n\t" \
    "v_cvt_pk_bf16_f32 v53, v197, v201\n\t" \
    "v_mfma_f32_16x16x32_bf16 v[8:11], v[180:183], v[40:43], v[8:11]\n\t" \
    "v_cvt_pk_bf16_f32 v54, v198, v202\n\t" \
    "s_waitcnt lgkmcnt(4)\n\t" \
    "v_mfma_f32_16x16x32_bf16 v[12:15], v[188:191], v[40:43], v[12:15]\n\t" \
    "v_cvt_pk_bf16_f32 v55, v199, v203\n\t" \
    "v_cvt_pk_bf16_f32 v56, v204, v208\n\t" \
    "v_cvt_pk_bf16_f32 v57, v205, v209\n\t" \
    "v_mfma_f32_16x16x32_bf16 v[0:3], v[184:187], v[44:47], v[0:3]\n\t" \
    "ds_read_b128 v[36:39], %[xp] offset:36864\n\t" \
    "v_cvt_pk_bf16_f32 v58, v206, v210\n\t" \
    "v_mfma_f32_16x16x32_bf16 v[4:7], v[192:195], v[44:47], v[4:7]\n\t" \
    "ds_read_b128 v[40:43], %[xp] offset:37888\n\t" \
    "v_cvt_pk_bf16_f32 v59, v207, v211\n\t" \
    "v_mfma_f32_16x16x32_bf16 v[8:11], v[184:187], v[48:51], v[8:11]\n\t" \
    "s_waitcnt lgkmcnt(4)\n\t" \
    "v_mfma_f32_16x16x32_bf16 v[12:15], v[192:195], v[48:51], v[12:15]\n\t" \
    "s_mov_b64 exec, %[em]\n\t" \
    "buffer_store_dwordx4 v[52:55], %[l16], %[rs], %[oo] offen sc1\n\t" \
    "buffer_store_dwordx4 v[56:59], %[l16], %[rs], %[b] offen sc1\n\t" \
    "s_mov_b64 exec, -1\n\t" \
    "s_add_u32 %[sx], %[sx], 65536\n\t" \
    "s_add_u32 %[t], %[t], 1\n\t" \
    "s_add_u32 %[oo], %[oo], 262144\n\t" \
    "s_waitcnt vmcnt(6)\n\t" \
    "s_cmp_gt_u32 %[t], " STR(SQUICK) " + 1\n\t" \
    "s_cbranch_scc1 B0_%=\n\t" \
    "s_waitcnt vmcnt(0)\n\t" \
    "B0_%=:\n\t" \
    "s_barrier\n\t" \
    "s_setprio 1\n\t" \
    "s_add_u32 m0, %[sd], 16384\n\t" \
    "s_add_u32 %[a], %[sx], 81920\n\t" \
    "v_mfma_f32_16x16x32_bf16 v[196:199], v[68:71], v[20:23], 0\n\t" \
    "ds_read_b128 v[44:47], %[xp] offset:38912\n\t" \
    "v_mfma_f32_16x16x32_bf16 v[200:203], v[76:79], v[20:23], 0\n\t" \
    "ds_read_b128 v[48:51], %[xp] offset:39936\n\t" \
    "v_mfma_f32_16x16x32_bf16 v[204:207], v[68:71], v[24:27], 0\n\t" \
    "s_waitcnt lgkmcnt(4)\n\t" \
    "v_mfma_f32_16x16x32_bf16 v[208:211], v[76:79], v[24:27], 0\n\t" \
    "v_mfma_f32_16x16x32_bf16 v[196:199], v[72:75], v[28:31], v[196:199]\n\t" \
    "ds_read_b128 v[20:23], %[xp] offset:40960\n\t" \
    "v_mfma_f32_16x16x32_bf16 v[200:203], v[80:83], v[28:31], v[200:203]\n\t" \
    "ds_read_b128 v[24:27], %[xp] offset:41984\n\t" \
    "v_mfma_f32_16x16x32_bf16 v[204:207], v[72:75], v[32:35], v[204:207]\n\t" \
    "s_waitcnt lgkmcnt(4)\n\t" \
    "v_mfma_f32_16x16x32_bf16 v[208:211], v[80:83], v[32:35], v[208:211]\n\t" \
    "v_mfma_f32_16x16x32_bf16 v[196:199], v[84:87], v[36:39], v[196:199]\n\t" \
    "ds_read_b128 v[28:31], %[xp] offset:43008\n\t" \
    "v_mfma_f32_16x16x32_bf16 v[200:203], v[92:95], v[36:39], v[200:203]\n\t" \
    "ds_read_b128 v[32:35], %[xp] offset:44032\n\t" \
    "v_mfma_f32_16x16x32_bf16 v[204:207], v[84:87], v[40:43], v[204:207]\n\t" \
    "s_waitcnt lgkmcnt(4)\n\t" \
    "v_mfma_f32_16x16x32_bf16 v[208:211], v[92:95], v[40:43], v[208:211]\n\t" \
    "v_mfma_f32_16x16x32_bf16 v[196:199], v[88:91], v[44:47], v[196:199]\n\t" \
    "ds_read_b128 v[36:39], %[xp] offset:45056\n\t" \
    "v_mfma_f32_16x16x32_bf16 v[200:203], v[96:99], v[44:47], v[200:203]\n\t" \
    "ds_read_b128 v[40:43], %[xp] offset:46080\n\t" \
    "v_mfma_f32_16x16x32_bf16 v[204:207], v[88:91], v[48:51], v[204:207]\n\t" \
    "s_waitcnt lgkmcnt(4)\n\t" \
    "v_mfma_f32_16x16x32_bf16 v[208:211], v[96:99], v[48:51], v[208:211]\n\t" \
    "v_mfma_f32_16x16x32_bf16 v[196:199], v[100:103], v[20:23], v[196:199]\n\t" \
    "ds_read_b128 v[44:47], %[xp] offset:47104\n\t" \
    "v_mfma_f32_16x16x32_bf16 v[200:203], v[108:111], v[20:23], v[200:203]\n\t" \
    "ds_read_b128 v[48:51], %[xp] offset:48128\n\t" \
    "v_mfma_f32_16x16x32_bf16 v[204:207], v[100:103], v[24:27], v[204:207]\n\t" \
    "s_waitcnt lgkmcnt(4)\n\t" \
    "v_mfma_f32_16x16x32_bf16 v[208:211], v[108:111], v[24:27], v[208:211]\n\t" \
    "buffer_load_dwordx4 %[l16], %[rs], %[a] offen offset:0 sc1 lds\n\t" \
    "v_mfma_f32_16x16x32_bf16 v[196:199], v[104:107], v[28:31], v[196:199]\n\t" \
    "ds_read_b128 v[20:23], %[xp] offset:49152\n\t" \
    "v_mfma_f32_16x16x32_bf16 v[200:203], v[112:115], v[28:31], v[200:203]\n\t" \
    "ds_read_b128 v[24:27], %[xp] offset:50176\n\t" \
    "v_mfma_f32_16x16x32_bf16 v[204:207], v[104:107], v[32:35], v[204:207]\n\t" \
    "s_waitcnt lgkmcnt(4)\n\t" \
    "v_mfma_f32_16x16x32_bf16 v[208:211], v[112:115], v[32:35], v[208:211]\n\t" \
    "buffer_load_dwordx4 %[l16], %[rs], %[a] offen offset:1024 sc1 lds\n\t" \
    "v_mfma_f32_16x16x32_bf16 v[196:199], v[116:119], v[36:39], v[196:199]\n\t" \
    "ds_read_b128 v[28:31], %[xp] offset:51200\n\t" \
    "v_mfma_f32_16x16x32_bf16 v[200:203], v[124:127], v[36:39], v[200:203]\n\t" \
    "ds_read_b128 v[32:35], %[xp] offset:52224\n\t" \
    "v_mfma_f32_16x16x32_bf16 v[204:207], v[116:119], v[40:43], v[204:207]\n\t" \
    "s_waitcnt lgkmcnt(4)\n\t" \
    "v_mfma_f32_16x16x32_bf16 v[208:211], v[124:127], v[40:43], v[208:211]\n\t" \
    "buffer_load_dwordx4 %[l16], %[rs], %[a] offen offset:2048 sc1 lds\n\t" \
    "v_mfma_f32_16x16x32_bf16 v[196:199], v[120:123], v[44:47], v[196:199]\n\t" \
    "ds_read_b128 v[36:39], %[xp] offset:53248\n\t" \
    "v_mfma_f32_16x16x32_bf16 v[200:203], v[128:131], v[44:47], v[200:203]\n\t" \
    "ds_read_b128 v[40:43], %[xp] offset:54272\n\t" \
    "v_mfma_f32_16x16x32_bf16 v[204:207], v[120:123], v[48:51], v[204:207]\n\t" \
    "s_waitcnt lgkmcnt(4)\n\t" \
    "v_mfma_f32_16x16x32_bf16 v[208:211], v[128:131], v[48:51], v[208:211]\n\t" \
    "buffer_load_dwordx4 %[l16], %[rs], %[a] offen offset:3072 sc1 lds\n\t" \
    "s_waitcnt vmcnt(4)\n\t" \
    "s_barrier\n\t" \
    "s_setprio 1\n\t" \
    "s_add_u32 m0, %[sd], 32768\n\t" \
    "s_add_u32 %[a], %[sx], 131072\n\t" \
    "s_add_u32 %[b], %[oo], 0x1000\n\t" \
    "s_cmp_lg_u32 %[t], 0\n\t" \
    "s_cselect_b64 %[em], -1, 0\n\t" \
    "v_mfma_f32_16x16x32_bf16 v[196:199], v[132:135], v[20:23], v[196:199]\n\t" \
    "ds_read_b128 v[44:47], %[xp] offset:55296\n\t" \
    "v_mfma_f32_16x16x32_bf16 v[200:203], v[140:143], v[20:23], v[200:203]\n\t" \
    "ds_read_b128 v[48:51], %[xp] offset:56320\n\t" \
    "v_mfma_f32_16x16x32_bf16 v[204:207], v[132:135], v[24:27], v[204:207]\n\t" \
    "ds_read_b128 v[52:55], %[pa] offset:0\n\t" \
    "s_waitcnt lgkmcnt(5)\n\t" \
    "v_mfma_f32_16x16x32_bf16 v[208:211], v[140:143], v[24:27], v[208:211]\n\t" \
    "buffer_load_dwordx4 %[l16], %[rs], %[a] offen offset:0 sc1 lds\n\t" \
    "v_mfma_f32_16x16x32_bf16 v[196:199], v[136:139], v[28:31], v[196:199]\n\t" \
    "ds_read_b128 v[20:23], %[xp] offset:57344\n\t" \
    "v_mfma_f32_16x16x32_bf16 v[200:203], v[144:147], v[28:31], v[200:203]\n\t" \
    "ds_read_b128 v[24:27], %[xp] offset:58368\n\t" \
    "v_mfma_f32_16x16x32_bf16 v[204:207], v[136:139], v[32:35], v[204:207]\n\t" \
    "ds_read_b128 v[56:59], %[pa] offset:1024\n\t" \
    "s_waitcnt lgkmcnt(6)\n\t" \
    "v_mfma_f32_16x16x32_bf16 v[208:211], v[144:147], v[32:35], v[208:211]\n\t" \
    "buffer_load_dwordx4 %[l16], %[rs], %[a] offen offset:1024 sc1 lds\n\t" \
    "v_mfma_f32_16x16x32_bf16 v[196:199], v[148:151], v[36:39], v[196:199]\n\t" \
    "ds_read_b128 v[28:31], %[xp] offset:59392\n\t" \
    "v_mfma_f32_16x16x32_bf16 v[200:203], v[156:159], v[36:39], v[200:203]\n\t" \
    "ds_read_b128 v[32:35], %[xp] offset:60416\n\t" \
    "v_mfma_f32_16x16x32_bf16 v[204:207], v[148:151], v[40:43], v[204:207]\n\t" \
    "ds_read_b128 v[60:63], %[pa] offset:2048\n\t" \
    "s_waitcnt lgkmcnt(7)\n\t" \
    "v_mfma_f32_16x16x32_bf16 v[208:211], v[156:159], v[40:43], v[208:211]\n\t" \
    "buffer_load_dwordx4 %[l16], %[rs], %[a] offen offset:2048 sc1 lds\n\t" \
    "v_mfma_f32_16x16x32_bf16 v[196:199], v[152:155], v[44:47], v[196:199]\n\t" \
    "ds_read_b128 v[36:39], %[xp] offset:61440\n\t" \
    "s_waitcnt lgkmcnt(7)\n\t" \
    "v_add_f32 v0, v0, v52\n\t" \
    "v_mfma_f32_16x16x32_bf16 v[200:203], v[160:163], v[44:47], v[200:203]\n\t" \
    "ds_read_b128 v[40:43], %[xp] offset:62464\n\t" \
    "v_add_f32 v1, v1, v53\n\t" \
    "v_mfma_f32_16x16x32_bf16 v[204:207], v[152:155], v[48:51], v[204:207]\n\t" \
    "ds_read_b128 v[64:67], %[pa] offset:3072\n\t" \
    "s_waitcnt lgkmcnt(7)\n\t" \
    "v_mfma_f32_16x16x32_bf16 v[208:211], v[160:163], v[48:51], v[208:211]\n\t" \
    "buffer_load_dwordx4 %[l16], %[rs], %[a] offen offset:3072 sc1 lds\n\t" \
    "v_add_f32 v2, v2, v54\n\t" \
    "v_add_f32 v3, v3, v55\n\t" \
    "v_mfma_f32_16x16x32_bf16 v[196:199], v[164:167], v[20:23], v[196:199]\n\t" \
    "ds_read_b128 v[44:47], %[xp] offset:63488\n\t" \
    "s_waitcnt lgkmcnt(7)\n\t" \
    "v_add_f32 v4, v4, v56\n\t" \
    "v_mfma_f32_16x16x32_bf16 v[200:203], v[172:175], v[20:23], v[200:203]\n\t" \
    "ds_read_b128 v[48:51], %[xp] offset:64512\n\t" \
    "v_add_f32 v5, v5, v57\n\t" \
    "v_mfma_f32_16x16x32_bf16 v[204:207], v[164:167], v[24:27], v[204:207]\n\t" \
    "v_add_f32 v6, v6, v58\n\t" \
    "s_waitcnt lgkmcnt(6)\n\t" \
    "v_mfma_f32_16x16x32_bf16 v[208:211], v[172:175], v[24:27], v[208:211]\n\t" \
    "v_add_f32 v7, v7, v59\n\t" \
    "s_waitcnt lgkmcnt(5)\n\t" \
    "v_add_f32 v8, v8, v60\n\t" \
    "v_add_f32 v9, v9, v61\n\t" \
    "v_mfma_f32_16x16x32_bf16 v[196:199], v[168:171], v[28:31], v[196:199]\n\t" \
    "ds_read_b128 v[20:23], %[xp] offset:0\n\t" \
    "v_add_f32 v10, v10, v62\n\t" \
    "v_mfma_f32_16x16x32_bf16 v[200:203], v[176:179], v[28:31], v[200:203]\n\t" \
    "ds_read_b128 v[24:27], %[xp] offset:1024\n\t" \
    "v_add_f32 v11, v11, v63\n\t" \
    "v_mfma_f32_16x16x32_bf16 v[204:207], v[168:171], v[32:35], v[204:207]\n\t" \
    "s_waitcnt lgkmcnt(4)\n\t" \
    "v_add_f32 v12, v12, v64\n\t" \
    "v_mfma_f32_16x16x32_bf16 v[208:211], v[176:179], v[32:35], v[208:211]\n\t" \
    "v_add_f32 v13, v13, v65\n\t" \
    "v_add_f32 v14, v14, v66\n\t" \
    "v_add_f32 v15, v15, v67\n\t" \
    "v_mfma_f32_16x16x32_bf16 v[196:199], v[180:183], v[36:39], v[196:199]\n\t" \
    "ds_read_b128 v[28:31], %[xp] offset:2048\n\t" \
    "v_cvt_pk_bf16_f32 v52, v0, v4\n\t" \
    "v_mfma_f32_16x16x32_bf16 v[200:203], v[188:191], v[36:39], v[200:203]\n\t" \
    "ds_read_b128 v[32:35], %[xp] offset:3072\n\t" \
    "v_cvt_pk_bf16_f32 v53, v1, v5\n\t" \
    "v_mfma_f32_16x16x32_bf16 v[204:207], v[180:183], v[40:43], v[204:207]\n\t" \
    "v_cvt_pk_bf16_f32 v54, v2, v6\n\t" \
    "s_waitcnt lgkmcnt(4)\n\t" \
    "v_mfma_f32_16x16x32_bf16 v[208:211], v[188:191], v[40:43], v[208:211]\n\t" \
    "v_cvt_pk_bf16_f32 v55, v3, v7\n\t" \
    "v_cvt_pk_bf16_f32 v56, v8, v12\n\t" \
    "v_cvt_pk_bf16_f32 v57, v9, v13\n\t" \
    "v_mfma_f32_16x16x32_bf16 v[196:199], v[184:187], v[44:47], v[196:199]\n\t" \
    "ds_read_b128 v[36:39], %[xp] offset:4096\n\t" \
    "v_cvt_pk_bf16_f32 v58, v10, v14\n\t" \
    "v_mfma_f32_16x16x32_bf16 v[200:203], v[192:195], v[44:47], v[200:203]\n\t" \
    "ds_read_b128 v[40:43], %[xp] offset:5120\n\t" \
    "v_cvt_pk_bf16_f32 v59, v11, v15\n\t" \
    "v_mfma_f32_16x16x32_bf16 v[204:207], v[184:187], v[48:51], v[204:207]\n\t" \
    "s_waitcnt lgkmcnt(4)\n\t" \
    "v_mfma_f32_16x16x32_bf16 v[208:211], v[192:195], v[48:51], v[208:211]\n\t" \
    "s_mov_b64 exec, %[em]\n\t" \
    "buffer_store_dwordx4 v[52:55], %[l16], %[rs], %[oo] offen sc1\n\t" \
    "buffer_store_dwordx4 v[56:59], %[l16], %[rs], %[b] offen sc1\n\t" \
    "s_mov_b64 exec, -1\n\t" \
    "s_add_u32 %[sx], %[sx], 65536\n\t" \
    "s_add_u32 %[t], %[t], 1\n\t" \
    "s_add_u32 %[oo], %[oo], 262144\n\t" \
    "s_waitcnt vmcnt(6)\n\t" \
    "s_cmp_gt_u32 %[t], " STR(SQUICK) " + 1\n\t" \
    "s_cbranch_scc1 B1_%=\n\t" \
    "s_waitcnt vmcnt(0)\n\t" \
    "B1_%=:\n\t" \
    "s_barrier\n\t" \
    "s_cmp_lt_u32 %[t], %[te]\n\t" \
    "s_cbranch_scc1 L1_%=\n\t" \
    "s_cmp_lg_u32 %[fin], 0\n\t" \
    "s_cbranch_scc0 E_%=\n\t" \
    "s_nop 4\n\t" \
    "s_barrier\n\t" \
    "ds_read_b128 v[52:55], %[pa] offset:0\n\t" \
    "ds_read_b128 v[56:59], %[pa] offset:1024\n\t" \
    "ds_read_b128 v[60:63], %[pa] offset:2048\n\t" \
    "ds_read_b128 v[64:67], %[pa] offset:3072\n\t" \
    "s_add_u32 %[b], %[oo], 0x1000\n\t" \
    "s_waitcnt lgkmcnt(0)\n\t" \
    "v_add_f32 v196, v196, v52\n\t" \
    "v_add_f32 v197, v197, v53\n\t" \
    "v_add_f32 v198, v198, v54\n\t" \
    "v_add_f32 v199, v199, v55\n\t" \
    "v_add_f32 v200, v200, v56\n\t" \
    "v_add_f32 v201, v201, v57\n\t" \
    "v_add_f32 v202, v202, v58\n\t" \
    "v_add_f32 v203, v203, v59\n\t" \
    "v_add_f32 v204, v204, v60\n\t" \
    "v_add_f32 v205, v205, v61\n\t" \
    "v_add_f32 v206, v206, v62\n\t" \
    "v_add_f32 v207, v207, v63\n\t" \
    "v_add_f32 v208, v208, v64\n\t" \
    "v_add_f32 v209, v209, v65\n\t" \
    "v_add_f32 v210, v210, v66\n\t" \
    "v_add_f32 v211, v211, v67\n\t" \
    "v_cvt_pk_bf16_f32 v52, v196, v200\n\t" \
    "v_cvt_pk_bf16_f32 v53, v197, v201\n\t" \
    "v_cvt_pk_bf16_f32 v54, v198, v202\n\t" \
    "v_cvt_pk_bf16_f32 v55, v199, v203\n\t" \
    "v_cvt_pk_bf16_f32 v56, v204, v208\n\t" \
    "v_cvt_pk_bf16_f32 v57, v205, v209\n\t" \
    "v_cvt_pk_bf16_f32 v58, v206, v210\n\t" \
    "v_cvt_pk_bf16_f32 v59, v207, v211\n\t" \
    "buffer_store_dwordx4 v[52:55], %[l16], %[rs], %[oo] offen sc1\n\t" \
    "buffer_store_dwordx4 v[56:59], %[l16], %[rs], %[b] offen sc1\n\t" \
    "E_%=:\n\t" \
    "s_setprio 0\n\t" \
    "s_waitcnt lgkmcnt(0)\n\t" \
    "s_nop 7\n\t" \
    : [t] "+s"(t), [sx] "+s"(sx), [oo] "+s"(oo), [a] "=&s"(tmp), [b] "=&s"(tmp2), [em] "=&s"(em), "+{v[20:27]}"(xf[0]), "+{v[28:35]}"(xf[1]), "+{v[36:43]}"(xf[2]), "+{v[196:203]}"(ac[0]), "+{v[204:211]}"(ac[1]) \
    : [te] "s"(TE_), [fin] "s"(FIN_), [kh] "s"(kh), [sd] "s"(sd), [rs] "s"(rs), [l16] "v"(l16), [xp] "v"(xp), [pa] "v"(pa), [fm] "s"(fm), [pm] "s"(pm), [fo] "s"(fo), [po] "s"(po), [pd] "s"(pd), [tag] "s"(P.tag), "{v[68:75]}"(w[0][0]), "{v[76:83]}"(w[0][1]), "{v[84:91]}"(w[1][0]), "{v[92:99]}"(w[1][1]), "{v[100:107]}"(w[2][0]), "{v[108:115]}"(w[2][1]), "{v[116:123]}"(w[3][0]), "{v[124:131]}"(w[3][1]), "{v[132:139]}"(w[4][0]), "{v[140:147]}"(w[4][1]), "{v[148:155]}"(w[5][0]), "{v[156:163]}"(w[5][1]), "{v[164:171]}"(w[6][0]), "{v[172:179]}"(w[6][1]), "{v[180:187]}"(w[7][0]), "{v[188:195]}"(w[7][1]) \
    : "memory", "scc", "v0", "v1", "v2", "v3", "v4", "v5", "v6", "v7", "v8", "v9", "v10", "v11", "v12", "v13", "v14", "v15", "v44", "v45", "v46", "v47", "v48", "v49", "v50", "v51", "v52", "v53", "v54", "v55", "v56", "v57", "v58", "v59", "v60", "v61", "v62", "v63", "v64", "v65", "v66", "v67")

#define S_LOOP_V(TE_, FIN_) \
  asm volatile( \
    "s_cmp_eq_u32 %[kh], 0\n\t" \
    "s_cbranch_scc0 L1_%=\n\t" \
    "L0_%=:\n\t" \
    "s_setprio 0\n\t" \
    "s_nop 3\n\t" \
    "ds_write_b128 %[pa], v[196:199] offset:0\n\t" \
    "ds_write_b128 %[pa], v[200:203] offset:1024\n\t" \
    "ds_write_b128 %[pa], v[204:207] offset:2048\n\t" \
    "ds_write_b128 %[pa], v[208:211] offset:3072\n\t" \
    "s_sub_u32 %[a], %[t], 1\n\t" \
    "v_mov_b32 v53, %[tag]\n\t" \
    "v_mov_b32 v52, %[a]\n\t" \
    "s_sub_u32 %[b], %[t], 2\n\t" \
    "s_cmp_lt_u32 %[b], " STR(SQUICK) "\n\t" \
    "s_cselect_b64 exec, %[fm], 0\n\t" \
    "buffer_store_dwordx2 v[52:53], %[l16], %[rs], %[fo] offen sc1\n\t" \
    "s_mov_b64 exec, -1\n\t" \
    "s_add_u32 m0, %[sd], 49152\n\t" \
    "s_add_u32 %[a], %[sx], 81920\n\t" \
    "s_waitcnt lgkmcnt(0)\n\t" \
    "buffer_load_dwordx4 %[l16], %[rs], %[a] offen offset:0 sc1 lds\n\t" \
    "buffer_load_dwordx4 %[l16], %[rs], %[a] offen offset:1024 sc1 lds\n\t" \
    "buffer_load_dwordx4 %[l16], %[rs], %[a] offen offset:2048 sc1 lds\n\t" \
    "buffer_load_dwordx4 %[l16], %[rs], %[a] offen offset:3072 sc1 lds\n\t" \
    "v_mfma_f32_16x16x32_bf16 v[0:3], v[20:23], v[68:71], 0\n\t" \
    "ds_read_b128 v[44:47], %[xp] offset:6144\n\t" \
    "v_mfma_f32_16x16x32_bf16 v[4:7], v[20:23], v[76:79], 0\n\t" \
    "ds_read_b128 v[48:51], %[xp] offset:7168\n\t" \
    "v_mfma_f32_16x16x32_bf16 v[8:11], v[24:27], v[68:71], 0\n\t" \
    "v_mfma_f32_16x16x32_bf16 v[12:15], v[24:27], v[76:79], 0\n\t" \
    "v_mfma_f32_16x16x32_bf16 v[0:3], v[28:31], v[72:75], v[0:3]\n\t" \
    "ds_read_b128 v[20:23], %[xp] offset:8192\n\t" \
    "v_mfma_f32_16x16x32_bf16 v[4:7], v[28:31], v[80:83], v[4:7]\n\t" \
    "ds_read_b128 v[24:27], %[xp] offset:9216\n\t" \
    "v_mfma_f32_16x16x32_bf16 v[8:11], v[32:35], v[72:75], v[8:11]\n\t" \
    "v_mfma_f32_16x16x32_bf16 v[12:15], v[32:35], v[80:83], v[12:15]\n\t" \
    "v_mfma_f32_16x16x32_bf16 v[0:3], v[36:39], v[84:87], v[0:3]\n\t" \
    "ds_read_b128 v[28:31], %[xp] offset:10240\n\t" \
    "v_mfma_f32_16x16x32_bf16 v[4:7], v[36:39], v[92:95], v[4:7]\n\t" \
    "ds_read_b128 v[32:35], %[xp] offset:11264\n\t" \
    "v_mfma_f32_16x16x32_bf16 v[8:11], v[40:43], v[84:87], v[8:11]\n\t" \
    "s_waitcnt lgkmcnt(4)\n\t" \
    "v_mfma_f32_16x16x32_bf16 v[12:15], v[40:43], v[92:95], v[12:15]\n\t" \
    "v_mfma_f32_16x16x32_bf16 v[0:3], v[44:47], v[88:91], v[0:3]\n\t" \
    "ds_read_b128 v[36:39], %[xp] offset:12288\n\t" \
    "v_mfma_f32_16x16x32_bf16 v[4:7], v[44:47], v[96:99], v[4:7]\n\t" \
    "ds_read_b128 v[40:43], %[xp] offset:13312\n\t" \
    "v_mfma_f32_16x16x32_bf16 v[8:11], v[48:51], v[88:91], v[8:11]\n\t" \
    "s_waitcnt lgkmcnt(4)\n\t" \
    "v_mfma_f32_16x16x32_bf16 v[12:15], v[48:51], v[96:99], v[12:15]\n\t" \
    "v_mfma_f32_16x16x32_bf16 v[0:3], v[20:23], v[100:103], v[0:3]\n\t" \
    "ds_read_b128 v[44:47], %[xp] offset:14336\n\t" \
    "v_mfma_f32_16x16x32_bf16 v[4:7], v[20:23], v[108:111], v[4:7]\n\t" \
    "ds_read_b128 v[48:51], %[xp] offset:15360\n\t" \
    "v_mfma_f32_16x16x32_bf16 v[8:11], v[24:27], v[100:103], v[8:11]\n\t" \
    "s_waitcnt lgkmcnt(4)\n\t" \
    "v_mfma_f32_16x16x32_bf16 v[12:15], v[24:27], v[108:111], v[12:15]\n\t" \
    "v_mfma_f32_16x16x32_bf16 v[0:3], v[28:31], v[104:107], v[0:3]\n\t" \
    "ds_read_b128 v[20:23], %[xp] offset:16384\n\t" \
    "v_mfma_f32_16x16x32_bf16 v[4:7], v[28:31], v[112:115], v[4:7]\n\t" \
    "ds_read_b128 v[24:27], %[xp] offset:17408\n\t" \
    "v_mfma_f32_16x16x32_bf16 v[8:11], v[32:35], v[104:107], v[8:11]\n\t" \
    "s_waitcnt lgkmcnt(4)\n\t" \
    "v_mfma_f32_16x16x32_bf16 v[12:15], v[32:35], v[112:115], v[12:15]\n\t" \
    "v_mfma_f32_16x16x32_bf16 v[0:3], v[36:39], v[116:119], v[0:3]\n\t" \
    "ds_read_b128 v[28:31], %[xp] offset:18432\n\t" \
    "v_mfma_f32_16x16x32_bf16 v[4:7], v[36:39], v[124:127], v[4:7]\n\t" \
    "ds_read_b128 v[32:35], %[xp] offset:19456\n\t" \
    "v_mfma_f32_16x16x32_bf16 v[8:11], v[40:43], v[116:119], v[8:11]\n\t" \
    "s_waitcnt lgkmcnt(4)\n\t" \
    "v_mfma_f32_16x16x32_bf16 v[12:15], v[40:43], v[124:127], v[12:15]\n\t" \
    "v_mfma_f32_16x16x32_bf16 v[0:3], v[44:47], v[120:123], v[0:3]\n\t" \
    "ds_read_b128 v[36:39], %[xp] offset:20480\n\t" \
    "v_mfma_f32_16x16x32_bf16 v[4:7], v[44:47], v[128:131], v[4:7]\n\t" \
    "ds_read_b128 v[40:43], %[xp] offset:21504\n\t" \
    "v_mfma_f32_16x16x32_bf16 v[8:11], v[48:51], v[120:123], v[8:11]\n\t" \
    "s_waitcnt lgkmcnt(4)\n\t" \
    "v_mfma_f32_16x16x32_bf16 v[12:15], v[48:51], v[128:131], v[12:15]\n\t" \
    "s_waitcnt vmcnt(4)\n\t" \
    "s_barrier\n\t" \
    "s_setprio 0\n\t" \
    "s_sub_u32 %[a], %[t], 1\n\t" \
    "v_mov_b32 v53, %[tag]\n\t" \
    "v_mov_b32 v52, %[a]\n\t" \
    "s_cmp_gt_u32 %[t], 1\n\t" \
    "s_cselect_b64 exec, %[fm], 0\n\t" \
    "buffer_store_dwordx2 v[52:53], %[l16], %[rs], %[fo] offen sc1\n\t" \
    "s_mov_b64 exec, %[pm]\n\t" \
    "s_mov_b32 m0, %[pd]\n\t" \
    "s_add_u32 %[a], %[sx], %[po]\n\t" \
    "buffer_load_dwordx4 %[l16], %[rs], %[a] offen sc1 lds\n\t" \
    "s_mov_b64 exec, -1\n\t" \
    "s_add_u32 m0, %[sd], 0\n\t" \
    "s_add_u32 %[a], %[sx], 131072\n\t" \
    "buffer_load_dwordx4 %[l16], %[rs], %[a] offen offset:0 sc1 lds\n\t" \
    "buffer_load_dwordx4 %[l16], %[rs], %[a] offen offset:1024 sc1 lds\n\t" \
    "buffer_load_dwordx4 %[l16], %[rs], %[a] offen offset:2048 sc1 lds\n\t" \
    "buffer_load_dwordx4 %[l16], %[rs], %[a] offen offset:3072 sc1 lds\n\t" \
    "v_mfma_f32_16x16x32_bf16 v[0:3], v[20:23], v[132:135], v[0:3]\n\t" \
    "ds_read_b128 v[44:47], %[xp] offset:22528\n\t" \
    "v_mfma_f32_16x16x32_bf16 v[4:7], v[20:23], v[140:143], v[4:7]\n\t" \
    "ds_read_b128 v[48:51], %[xp] offset:23552\n\t" \
    "v_mfma_f32_16x16x32_bf16 v[8:11], v[24:27], v[132:135], v[8:11]\n\t" \
    "s_waitcnt lgkmcnt(4)\n\t" \
    "v_mfma_f32_16x16x32_bf16 v[12:15], v[24:27], v[140:143], v[12:15]\n\t" \
    "v_mfma_f32_16x16x32_bf16 v[0:3], v[28:31], v[136:139], v[0:3]\n\t" \
    "ds_read_b128 v[20:23], %[xp] offset:24576\n\t" \
    "v_mfma_f32_16x16x32_bf16 v[4:7], v[28:31], v[144:147], v[4:7]\n\t" \
    "ds_read_b128 v[24:27], %[xp] offset:25600\n\t" \
    "v_mfma_f32_16x16x32_bf16 v[8:11], v[32:35], v[136:139], v[8:11]\n\t" \
    "s_waitcnt lgkmcnt(4)\n\t" \
    "v_mfma_f32_16x16x32_bf16 v[12:15], v[32:35], v[144:147], v[12:15]\n\t" \
    "v_mfma_f32_16x16x32_bf16 v[0:3], v[36:39], v[148:151], v[0:3]\n\t" \
    "ds_read_b128 v[28:31], %[xp] offset:26624\n\t" \
    "v_mfma_f32_16x16x32_bf16 v[4:7], v[36:39], v[156:159], v[4:7]\n\t" \
    "ds_read_b128 v[32:35], %[xp] offset:27648\n\t" \
    "v_mfma_f32_16x16x32_bf16 v[8:11], v[40:43], v[148:151], v[8:11]\n\t" \
    "s_waitcnt lgkmcnt(4)\n\t" \
    "v_mfma_f32_16x16x32_bf16 v[12:15], v[40:43], v[156:159], v[12:15]\n\t" \
    "v_mfma_f32_16x16x32_bf16 v[0:3], v[44:47], v[152:155], v[0:3]\n\t" \
    "ds_read_b128 v[36:39], %[xp] offset:28672\n\t" \
    "v_mfma_f32_16x16x32_bf16 v[4:7], v[44:47], v[160:163], v[4:7]\n\t" \
    "ds_read_b128 v[40:43], %[xp] offset:29696\n\t" \
    "v_mfma_f32_16x16x32_bf16 v[8:11], v[48:51], v[152:155], v[8:11]\n\t" \
    "s_waitcnt lgkmcnt(4)\n\t" \
    "v_mfma_f32_16x16x32_bf16 v[12:15], v[48:51], v[160:163], v[12:15]\n\t" \
    "v_mfma_f32_16x16x32_bf16 v[0:3], v[20:23], v[164:167], v[0:3]\n\t" \
    "ds_read_b128 v[44:47], %[xp] offset:30720\n\t" \
    "v_mfma_f32_16x16x32_bf16 v[4:7], v[20:23], v[172:175], v[4:7]\n\t" \
    "ds_read_b128 v[48:51], %[xp] offset:31744\n\t" \
    "v_mfma_f32_16x16x32_bf16 v[8:11], v[24:27], v[164:167], v[8:11]\n\t" \
    "s_waitcnt lgkmcnt(4)\n\t" \
    "v_mfma_f32_16x16x32_bf16 v[12:15], v[24:27], v[172:175], v[12:15]\n\t" \
    "v_mfma_f32_16x16x32_bf16 v[0:3], v[28:31], v[168:171], v[0:3]\n\t" \
    "ds_read_b128 v[20:23], %[xp] offset:32768\n\t" \
    "v_mfma_f32_16x16x32_bf16 v[4:7], v[28:31], v[176:179], v[4:7]\n\t" \
    "ds_read_b128 v[24:27], %[xp] offset:33792\n\t" \
    "v_mfma_f32_16x16x32_bf16 v[8:11], v[32:35], v[168:171], v[8:11]\n\t" \
    "s_waitcnt lgkmcnt(4)\n\t" \
    "v_mfma_f32_16x16x32_bf16 v[12:15], v[32:35], v[176:179], v[12:15]\n\t" \
    "v_mfma_f32_16x16x32_bf16 v[0:3], v[36:39], v[180:183], v[0:3]\n\t" \
    "ds_read_b128 v[28:31], %[xp] offset:34816\n\t" \
    "v_mfma_f32_16x16x32_bf16 v[4:7], v[36:39], v[188:191], v[4:7]\n\t" \
    "ds_read_b128 v[32:35], %[xp] offset:35840\n\t" \
    "v_mfma_f32_16x16x32_bf16 v[8:11], v[40:43], v[180:183], v[8:11]\n\t" \
    "s_waitcnt lgkmcnt(4)\n\t" \
    "v_mfma_f32_16x16x32_bf16 v[12:15], v[40:43], v[188:191], v[12:15]\n\t" \
    "v_mfma_f32_16x16x32_bf16 v[0:3], v[44:47], v[184:187], v[0:3]\n\t" \
    "ds_read_b128 v[36:39], %[xp] offset:36864\n\t" \
    "v_mfma_f32_16x16x32_bf16 v[4:7], v[44:47], v[192:195], v[4:7]\n\t" \
    "ds_read_b128 v[40:43], %[xp] offset:37888\n\t" \
    "v_mfma_f32_16x16x32_bf16 v[8:11], v[48:51], v[184:187], v[8:11]\n\t" \
    "s_waitcnt lgkmcnt(4)\n\t" \
    "v_mfma_f32_16x16x32_bf16 v[12:15], v[48:51], v[192:195], v[12:15]\n\t" \
    "s_add_u32 %[sx], %[sx], 65536\n\t" \
    "s_add_u32 %[t], %[t], 1\n\t" \
    "s_waitcnt vmcnt(4)\n\t" \
    "s_barrier\n\t" \
    "s_setprio 0\n\t" \
    "s_nop 3\n\t" \
    "ds_write_b128 %[pa], v[0:3] offset:0\n\t" \
    "ds_write_b128 %[pa], v[4:7] offset:1024\n\t" \
    "ds_write_b128 %[pa], v[8:11] offset:2048\n\t" \
    "ds_write_b128 %[pa], v[12:15] offset:3072\n\t" \
    "s_sub_u32 %[a], %[t], 1\n\t" \
    "v_mov_b32 v53, %[tag]\n\t" \
    "v_mov_b32 v52, %[a]\n\t" \
    "s_sub_u32 %[b], %[t], 2\n\t" \
    "s_cmp_lt_u32 %[b], " STR(SQUICK) "\n\t" \
    "s_cselect_b64 exec, %[fm], 0\n\t" \
    "buffer_store_dwordx2 v[52:53], %[l16], %[rs], %[fo] offen sc1\n\t" \
    "s_mov_b64 exec, -1\n\t" \
    "s_add_u32 m0, %[sd], 16384\n\t" \
    "s_add_u32 %[a], %[sx], 81920\n\t" \
    "s_waitcnt lgkmcnt(0)\n\t" \
    "buffer_load_dwordx4 %[l16], %[rs], %[a] offen offset:0 sc1 lds\n\t" \
    "buffer_load_dwordx4 %[l16], %[rs], %[a] offen offset:1024 sc1 lds\n\t" \
    "buffer_load_dwordx4 %[l16], %[rs], %[a] offen offset:2048 sc1 lds\n\t" \
    "buffer_load_dwordx4 %[l16], %[rs], %[a] offen offset:3072 sc1 lds\n\t" \
    "v_mfma_f32_16x16x32_bf16 v[196:199], v[20:23], v[68:71], 0\n\t" \
    "ds_read_b128 v[44:47], %[xp] offset:38912\n\t" \
    "v_mfma_f32_16x16x32_bf16 v[200:203], v[20:23], v[76:79], 0\n\t" \
    "ds_read_b128 v[48:51], %[xp] offset:39936\n\t" \
    "v_mfma_f32_16x16x32_bf16 v[204:207], v[24:27], v[68:71], 0\n\t" \
    "v_mfma_f32_16x16x32_bf16 v[208:211], v[24:27], v[76:79], 0\n\t" \
    "v_mfma_f32_16x16x32_bf16 v[196:199], v[28:31], v[72:75], v[196:199]\n\t" \
    "ds_read_b128 v[20:23], %[xp] offset:40960\n\t" \
    "v_mfma_f32_16x16x32_bf16 v[200:203], v[28:31], v[80:83], v[200:203]\n\t" \
    "ds_read_b128 v[24:27], %[xp] offset:41984\n\t" \
    "v_mfma_f32_16x16x32_bf16 v[204:207], v[32:35], v[72:75], v[204:207]\n\t" \
    "v_mfma_f32_16x16x32_bf16 v[208:211], v[32:35], v[80:83], v[208:211]\n\t" \
    "v_mfma_f32_16x16x32_bf16 v[196:199], v[36:39], v[84:87], v[196:199]\n\t" \
    "ds_read_b128 v[28:31], %[xp] offset:43008\n\t" \
    "v_mfma_f32_16x16x32_bf16 v[200:203], v[36:39], v[92:95], v[200:203]\n\t" \
    "ds_read_b128 v[32:35], %[xp] offset:44032\n\t" \
    "v_mfma_f32_16x16x32_bf16 v[204:207], v[40:43], v[84:87], v[204:207]\n\t" \
    "s_waitcnt lgkmcnt(4)\n\t" \
    "v_mfma_f32_16x16x32_bf16 v[208:211], v[40:43], v[92:95], v[208:211]\n\t" \
    "v_mfma_f32_16x16x32_bf16 v[196:199], v[44:47], v[88:91], v[196:199]\n\t" \
    "ds_read_b128 v[36:39], %[xp] offset:45056\n\t" \
    "v_mfma_f32_16x16x32_bf16 v[200:203], v[44:47], v[96:99], v[200:203]\n\t" \
    "ds_read_b128 v[40:43], %[xp] offset:46080\n\t" \
    "v_mfma_f32_16x16x32_bf16 v[204:207], v[48:51], v[88:91], v[204:207]\n\t" \
    "s_waitcnt lgkmcnt(4)\n\t" \
    "v_mfma_f32_16x16x32_bf16 v[208:211], v[48:51], v[96:99], v[208:211]\n\t" \
    "v_mfma_f32_16x16x32_bf16 v[196:199], v[20:23], v[100:103], v[196:199]\n\t" \
    "ds_read_b128 v[44:47], %[xp] offset:47104\n\t" \
    "v_mfma_f32_16x16x32_bf16 v[200:203], v[20:23], v[108:111], v[200:203]\n\t" \
    "ds_read_b128 v[48:51], %[xp] offset:48128\n\t" \
    "v_mfma_f32_16x16x32_bf16 v[204:207], v[24:27], v[100:103], v[204:207]\n\t" \
    "s_waitcnt lgkmcnt(4)\n\t" \
    "v_mfma_f32_16x16x32_bf16 v[208:211], v[24:27], v[108:111], v[208:211]\n\t" \
    "v_mfma_f32_16x16x32_bf16 v[196:199], v[28:31], v[104:107], v[196:199]\n\t" \
    "ds_read_b128 v[20:23], %[xp] offset:49152\n\t" \
    "v_mfma_f32_16x16x32_bf16 v[200:203], v[28:31], v[112:115], v[200:203]\n\t" \
    "ds_read_b128 v[24:27], %[xp] offset:50176\n\t" \
    "v_mfma_f32_16x16x32_bf16 v[204:207], v[32:35], v[104:107], v[204:207]\n\t" \
    "s_waitcnt lgkmcnt(4)\n\t" \
    "v_mfma_f32_16x16x32_bf16 v[208:211], v[32:35], v[112:115], v[208:211]\n\t" \
    "v_mfma_f32_16x16x32_bf16 v[196:199], v[36:39], v[116:119], v[196:199]\n\t" \
    "ds_read_b128 v[28:31], %[xp] offset:51200\n\t" \
    "v_mfma_f32_16x16x32_bf16 v[200:203], v[36:39], v[124:127], v[200:203]\n\t" \
    "ds_read_b128 v[32:35], %[xp] offset:52224\n\t" \
    "v_mfma_f32_16x16x32_bf16 v[204:207], v[40:43], v[116:119], v[204:207]\n\t" \
    "s_waitcnt lgkmcnt(4)\n\t" \
    "v_mfma_f32_16x16x32_bf16 v[208:211], v[40:43], v[124:127], v[208:211]\n\t" \
    "v_mfma_f32_16x16x32_bf16 v[196:199], v[44:47], v[120:123], v[196:199]\n\t" \
    "ds_read_b128 v[36:39], %[xp] offset:53248\n\t" \
    "v_mfma_f32_16x16x32_bf16 v[200:203], v[44:47], v[128:131], v[200:203]\n\t" \
    "ds_read_b128 v[40:43], %[xp] offset:54272\n\t" \
    "v_mfma_f32_16x16x32_bf16 v[204:207], v[48:51], v[120:123], v[204:207]\n\t" \
    "s_waitcnt lgkmcnt(4)\n\t" \
    "v_mfma_f32_16x16x32_bf16 v[208:211], v[48:51], v[128:131], v[208:211]\n\t" \
    "s_waitcnt vmcnt(4)\n\t" \
    "s_barrier\n\t" \
    "s_setprio 0\n\t" \
    "s_sub_u32 %[a], %[t], 1\n\t" \
    "v_mov_b32 v53, %[tag]\n\t" \
    "v_mov_b32 v52, %[a]\n\t" \
    "s_cmp_gt_u32 %[t], 1\n\t" \
    "s_cselect_b64 exec, %[fm], 0\n\t" \
    "buffer_store_dwordx2 v[52:53], %[l16], %[rs], %[fo] offen sc1\n\t" \
    "s_mov_b64 exec, %[pm]\n\t" \
    "s_mov_b32 m0, %[pd]\n\t" \
    "s_add_u32 %[a], %[sx], %[po]\n\t" \
    "buffer_load_dwordx4 %[l16], %[rs], %[a] offen sc1 lds\n\t" \
    "s_mov_b64 exec, -1\n\t" \
    "s_add_u32 m0, %[sd], 32768\n\t" \
    "s_add_u32 %[a], %[sx], 131072\n\t" \
    "buffer_load_dwordx4 %[l16], %[rs], %[a] offen offset:0 sc1 lds\n\t" \
    "buffer_load_dwordx4 %[l16], %[rs], %[a] offen offset:1024 sc1 lds\n\t" \
    "buffer_load_dwordx4 %[l16], %[rs], %[a] offen offset:2048 sc1 lds\n\t" \
    "buffer_load_dwordx4 %[l16], %[rs], %[a] offen offset:3072 sc1 lds\n\t" \
    "v_mfma_f32_16x16x32_bf16 v[196:199], v[20:23], v[132:135], v[196:199]\n\t" \
    "ds_read_b128 v[44:47], %[xp] offset:55296\n\t" \
    "v_mfma_f32_16x16x32_bf16 v[200:203], v[20:23], v[140:143], v[200:203]\n\t" \
    "ds_read_b128 v[48:51], %[xp] offset:56320\n\t" \
    "v_mfma_f32_16x16x32_bf16 v[204:207], v[24:27], v[132:135], v[204:207]\n\t" \
    "s_waitcnt lgkmcnt(4)\n\t" \
    "v_mfma_f32_16x16x32_bf16 v[208:211], v[24:27], v[140:143], v[208:211]\n\t" \
    "v_mfma_f32_16x16x32_bf16 v[196:199], v[28:31], v[136:139], v[196:199]\n\t" \
    "ds_read_b128 v[20:23], %[xp] offset:57344\n\t" \
    "v_mfma_f32_16x16x32_bf16 v[200:203], v[28:31], v[144:147], v[200:203]\n\t" \
    "ds_read_b128 v[24:27], %[xp] offset:58368\n\t" \
    "v_mfma_f32_16x16x32_bf16 v[204:207], v[32:35], v[136:139], v[204:207]\n\t" \
    "s_waitcnt lgkmcnt(4)\n\t" \
    "v_mfma_f32_16x16x32_bf16 v[208:211], v[32:35], v[144:147], v[208:211]\n\t" \
    "v_mfma_f32_16x16x32_bf16 v[196:199], v[36:39], v[148:151], v[196:199]\n\t" \
    "ds_read_b128 v[28:31], %[xp] offset:59392\n\t" \
    "v_mfma_f32_16x16x32_bf16 v[200:203], v[36:39], v[156:159], v[200:203]\n\t" \
    "ds_read_b128 v[32:35], %[xp] offset:60416\n\t" \
    "v_mfma_f32_16x16x32_bf16 v[204:207], v[40:43], v[148:151], v[204:207]\n\t" \
    "s_waitcnt lgkmcnt(4)\n\t" \
    "v_mfma_f32_16x16x32_bf16 v[208:211], v[40:43], v[156:159], v[208:211]\n\t" \
    "v_mfma_f32_16x16x32_bf16 v[196:199], v[44:47], v[152:155], v[196:199]\n\t" \
    "ds_read_b128 v[36:39], %[xp] offset:61440\n\t" \
    "v_mfma_f32_16x16x32_bf16 v[200:203], v[44:47], v[160:163], v[200:203]\n\t" \
    "ds_read_b128 v[40:43], %[xp] offset:62464\n\t" \
    "v_mfma_f32_16x16x32_bf16 v[204:207], v[48:51], v[152:155], v[204:207]\n\t" \
    "s_waitcnt lgkmcnt(4)\n\t" \
    "v_mfma_f32_16x16x32_bf16 v[208:211], v[48:51], v[160:163], v[208:211]\n\t" \
    "v_mfma_f32_16x16x32_bf16 v[196:199], v[20:23], v[164:167], v[196:199]\n\t" \
    "ds_read_b128 v[44:47], %[xp] offset:63488\n\t" \
    "v_mfma_f32_16x16x32_bf16 v[200:203], v[20:23], v[172:175], v[200:203]\n\t" \
    "ds_read_b128 v[48:51], %[xp] offset:64512\n\t" \
    "v_mfma_f32_16x16x32_bf16 v[204:207], v[24:27], v[164:167], v[204:207]\n\t" \
    "s_waitcnt lgkmcnt(4)\n\t" \
    "v_mfma_f32_16x16x32_bf16 v[208:211], v[24:27], v[172:175], v[208:211]\n\t" \
    "v_mfma_f32_16x16x32_bf16 v[196:199], v[28:31], v[168:171], v[196:199]\n\t" \
    "ds_read_b128 v[20:23], %[xp] offset:0\n\t" \
    "v_mfma_f32_16x16x32_bf16 v[200:203], v[28:31], v[176:179], v[200:203]\n\t" \
    "ds_read_b128 v[24:27], %[xp] offset:1024\n\t" \
    "v_mfma_f32_16x16x32_bf16 v[204:207], v[32:35], v[168:171], v[204:207]\n\t" \
    "s_waitcnt lgkmcnt(4)\n\t" \
    "v_mfma_f32_16x16x32_bf16 v[208:211], v[32:35], v[176:179], v[208:211]\n\t" \
    "v_mfma_f32_16x16x32_bf16 v[196:199], v[36:39], v[180:183], v[196:199]\n\t" \
    "ds_read_b128 v[28:31], %[xp] offset:2048\n\t" \
    "v_mfma_f32_16x16x32_bf16 v[200:203], v[36:39], v[188:191], v[200:203]\n\t" \
    "ds_read_b128 v[32:35], %[xp] offset:3072\n\t" \
    "v_mfma_f32_16x16x32_bf16 v[204:207], v[40:43], v[180:183], v[204:207]\n\t" \
    "s_waitcnt lgkmcnt(4)\n\t" \
    "v_mfma_f32_16x16x32_bf16 v[208:211], v[40:43], v[188:191], v[208:211]\n\t" \
    "v_mfma_f32_16x16x32_bf16 v[196:199], v[44:47], v[184:187], v[196:199]\n\t" \
    "ds_read_b128 v[36:39], %[xp] offset:4096\n\t" \
    "v_mfma_f32_16x16x32_bf16 v[200:203], v[44:47], v[192:195], v[200:203]\n\t" \
    "ds_read_b128 v[40:43], %[xp] offset:5120\n\t" \
    "v_mfma_f32_16x16x32_bf16 v[204:207], v[48:51], v[184:187], v[204:207]\n\t" \
    "s_waitcnt lgkmcnt(4)\n\t" \
    "v_mfma_f32_16x16x32_bf16 v[208:211], v[48:51], v[192:195], v[208:211]\n\t" \
    "s_add_u32 %[sx], %[sx], 65536\n\t" \
    "s_add_u32 %[t], %[t], 1\n\t" \
    "s_waitcnt vmcnt(4)\n\t" \
    "s_barrier\n\t" \
    "s_cmp_lt_u32 %[t], %[te]\n\t" \
    "s_cbranch_scc1 L0_%=\n\t" \
    "s_cmp_lg_u32 %[fin], 0\n\t" \
    "s_cbranch_scc0 E_%=\n\t" \
    "s_nop 4\n\t" \
    "ds_write_b128 %[pa], v[196:199] offset:0\n\t" \
    "ds_write_b128 %[pa], v[200:203] offset:1024\n\t" \
    "ds_write_b128 %[pa], v[204:207] offset:2048\n\t" \
    "ds_write_b128 %[pa], v[208:211] offset:3072\n\t" \
    "s_waitcnt lgkmcnt(0)\n\t" \
    "s_barrier\n\t" \
    "s_branch E_%=\n\t" \
    "L1_%=:\n\t" \
    "s_setprio 1\n\t" \
    "s_add_u32 m0, %[sd], 49152\n\t" \
    "s_add_u32 %[a], %[sx], 81920\n\t" \
    "v_mfma_f32_16x16x32_bf16 v[0:3], v[20:23], v[68:71], 0\n\t" \
    "ds_read_b128 v[44:47], %[xp] offset:6144\n\t" \
    "v_mfma_f32_16x16x32_bf16 v[4:7], v[20:23], v[76:79], 0\n\t" \
    "ds_read_b128 v[48:51], %[xp] offset:7168\n\t" \
    "v_mfma_f32_16x16x32_bf16 v[8:11], v[24:27], v[68:71], 0\n\t" \
    "s_waitcnt lgkmcnt(4)\n\t" \
    "v_mfma_f32_16x16x32_bf16 v[12:15], v[24:27], v[76:79], 0\n\t" \
    "v_mfma_f32_16x16x32_bf16 v[0:3], v[28:31], v[72:75], v[0:3]\n\t" \
    "ds_read_b128 v[20:23], %[xp] offset:8192\n\t" \
    "v_mfma_f32_16x16x32_bf16 v[4:7], v[28:31], v[80:83], v[4:7]\n\t" \
    "ds_read_b128 v[24:27], %[xp] offset:9216\n\t" \
    "v_mfma_f32_16x16x32_bf16 v[8:11], v[32:35], v[72:75], v[8:11]\n\t" \
    "s_waitcnt lgkmcnt(4)\n\t" \
    "v_mfma_f32_16x16x32_bf16 v[12:15], v[32:35], v[80:83], v[12:15]\n\t" \
    "v_mfma_f32_16x16x32_bf16 v[0:3], v[36:39], v[84:87], v[0:3]\n\t" \
    "ds_read_b128 v[28:31], %[xp] offset:10240\n\t" \
    "v_mfma_f32_16x16x32_bf16 v[4:7], v[36:39], v[92:95], v[4:7]\n\t" \
    "ds_read_b128 v[32:35], %[xp] offset:11264\n\t" \
    "v_mfma_f32_16x16x32_bf16 v[8:11], v[40:43], v[84:87], v[8:11]\n\t" \
    "s_waitcnt lgkmcnt(4)\n\t" \
    "v_mfma_f32_16x16x32_bf16 v[12:15], v[40:43], v[92:95], v[12:15]\n\t" \
    "v_mfma_f32_16x16x32_bf16 v[0:3], v[44:47], v[88:91], v[0:3]\n\t" \
    "ds_read_b128 v[36:39], %[xp] offset:12288\n\t" \
    "v_mfma_f32_16x16x32_bf16 v[4:7], v[44:47], v[96:99], v[4:7]\n\t" \
    "ds_read_b128 v[40:43], %[xp] offset:13312\n\t" \
    "v_mfma_f32_16x16x32_bf16 v[8:11], v[48:51], v[88:91], v[8:11]\n\t" \
    "s_waitcnt lgkmcnt(4)\n\t" \
    "v_mfma_f32_16x16x32_bf16 v[12:15], v[48:51], v[96:99], v[12:15]\n\t" \
    "v_mfma_f32_16x16x32_bf16 v[0:3], v[20:23], v[100:103], v[0:3]\n\t" \
    "ds_read_b128 v[44:47], %[xp] offset:14336\n\t" \
    "v_mfma_f32_16x16x32_bf16 v[4:7], v[20:23], v[108:111], v[4:7]\n\t" \
    "ds_read_b128 v[48:51], %[xp] offset:15360\n\t" \
    "v_mfma_f32_16x16x32_bf16 v[8:11], v[24:27], v[100:103], v[8:11]\n\t" \
    "s_waitcnt lgkmcnt(4)\n\t" \
    "v_mfma_f32_16x16x32_bf16 v[12:15], v[24:27], v[108:111], v[12:15]\n\t" \
    "buffer_load_dwordx4 %[l16], %[rs], %[a] offen offset:0 sc1 lds\n\t" \
    "v_mfma_f32_16x16x32_bf16 v[0:3], v[28:31], v[104:107], v[0:3]\n\t" \
    "ds_read_b128 v[20:23], %[xp] offset:16384\n\t" \
    "v_mfma_f32_16x16x32_bf16 v[4:7], v[28:31], v[112:115], v[4:7]\n\t" \
    "ds_read_b128 v[24:27], %[xp] offset:17408\n\t" \
    "v_mfma_f32_16x16x32_bf16 v[8:11], v[32:35], v[104:107], v[8:11]\n\t" \
    "s_waitcnt lgkmcnt(4)\n\t" \
    "v_mfma_f32_16x16x32_bf16 v[12:15], v[32:35], v[112:115], v[12:15]\n\t" \
    "buffer_load_dwordx4 %[l16], %[rs], %[a] offen offset:1024 sc1 lds\n\t" \
    "v_mfma_f32_16x16x32_bf16 v[0:3], v[36:39], v[116:119], v[0:3]\n\t" \
    "ds_read_b128 v[28:31], %[xp] offset:18432\n\t" \
    "v_mfma_f32_16x16x32_bf16 v[4:7], v[36:39], v[124:127], v[4:7]\n\t" \
    "ds_read_b128 v[32:35], %[xp] offset:19456\n\t" \
    "v_mfma_f32_16x16x32_bf16 v[8:11], v[40:43], v[116:119], v[8:11]\n\t" \
    "s_waitcnt lgkmcnt(4)\n\t" \
    "v_mfma_f32_16x16x32_bf16 v[12:15], v[40:43], v[124:127], v[12:15]\n\t" \
    "buffer_load_dwordx4 %[l16], %[rs], %[a] offen offset:2048 sc1 lds\n\t" \
    "v_mfma_f32_16x16x32_bf16 v[0:3], v[44:47], v[120:123], v[0:3]\n\t" \
    "ds_read_b128 v[36:39], %[xp] offset:20480\n\t" \
    "v_mfma_f32_16x16x32_bf16 v[4:7], v[44:47], v[128:131], v[4:7]\n\t" \
    "ds_read_b128 v[40:43], %[xp] offset:21504\n\t" \
    "v_mfma_f32_16x16x32_bf16 v[8:11], v[48:51], v[120:123], v[8:11]\n\t" \
    "s_waitcnt lgkmcnt(4)\n\t" \
    "v_mfma_f32_16x16x32_bf16 v[12:15], v[48:51], v[128:131], v[12:15]\n\t" \
    "buffer_load_dwordx4 %[l16], %[rs], %[a] offen offset:3072 sc1 lds\n\t" \
    "s_waitcnt vmcnt(4)\n\t" \
    "s_barrier\n\t" \
    "s_setprio 1\n\t" \
    "s_add_u32 m0, %[sd], 0\n\t" \
    "s_add_u32 %[a], %[sx], 131072\n\t" \
    "s_add_u32 %[b], %[oo], 0x1000\n\t" \
    "s_cmp_lg_u32 %[t], 0\n\t" \
    "s_cselect_b64 %[em], -1, 0\n\t" \
    "v_mfma_f32_16x16x32_bf16 v[0:3], v[20:23], v[132:135], v[0:3]\n\t" \
    "ds_read_b128 v[44:47], %[xp] offset:22528\n\t" \
    "v_mfma_f32_16x16x32_bf16 v[4:7], v[20:23], v[140:143], v[4:7]\n\t" \
    "ds_read_b128 v[48:51], %[xp] offset:23552\n\t" \
    "v_mfma_f32_16x16x32_bf16 v[8:11], v[24:27], v[132:135], v[8:11]\n\t" \
    "ds_read_b128 v[52:55], %[pa] offset:0\n\t" \
    "s_waitcnt lgkmcnt(5)\n\t" \
    "v_mfma_f32_16x16x32_bf16 v[12:15], v[24:27], v[140:143], v[12:15]\n\t" \
    "buffer_load_dwordx4 %[l16], %[rs], %[a] offen offset:0 sc1 lds\n\t" \
    "v_mfma_f32_16x16x32_bf16 v[0:3], v[28:31], v[136:139], v[0:3]\n\t" \
    "ds_read_b128 v[20:23], %[xp] offset:24576\n\t" \
    "v_mfma_f32_16x16x32_bf16 v[4:7], v[28:31], v[144:147], v[4:7]\n\t" \
    "ds_read_b128 v[24:27], %[xp] offset:25600\n\t" \
    "v_mfma_f32_16x16x32_bf16 v[8:11], v[32:35], v[136:139], v[8:11]\n\t" \
    "ds_read_b128 v[56:59], %[pa] offset:1024\n\t" \
    "s_waitcnt lgkmcnt(6)\n\t" \
    "v_mfma_f32_16x16x32_bf16 v[12:15], v[32:35], v[144:147], v[12:15]\n\t" \
    "buffer_load_dwordx4 %[l16], %[rs], %[a] offen offset:1024 sc1 lds\n\t" \
    "v_mfma_f32_16x16x32_bf16 v[0:3], v[36:39], v[148:151], v[0:3]\n\t" \
    "ds_read_b128 v[28:31], %[xp] offset:26624\n\t" \
    "v_mfma_f32_16x16x32_bf16 v[4:7], v[36:39], v[156:159], v[4:7]\n\t" \
    "ds_read_b128 v[32:35], %[xp] offset:27648\n\t" \
    "v_mfma_f32_16x16x32_bf16 v[8:11], v[40:43], v[148:151], v[8:11]\n\t" \
    "ds_read_b128 v[60:63], %[pa] offset:2048\n\t" \
    "s_waitcnt lgkmcnt(7)\n\t" \
    "v_mfma_f32_16x16x32_bf16 v[12:15], v[40:43], v[156:159], v[12:15]\n\t" \
    "buffer_load_dwordx4 %[l16], %[rs], %[a] offen offset:2048 sc1 lds\n\t" \
    "v_mfma_f32_16x16x32_bf16 v[0:3], v[44:47], v[152:155], v[0:3]\n\t" \
    "ds_read_b128 v[36:39], %[xp] offset:28672\n\t" \
    "s_waitcnt lgkmcnt(7)\n\t" \
    "v_add_f32 v196, v196, v52\n\t" \
    "v_mfma_f32_16x16x32_bf16 v[4:7], v[44:47], v[160:163], v[4:7]\n\t" \
    "ds_read_b128 v[40:43], %[xp] offset:29696\n\t" \
    "v_add_f32 v197, v197, v53\n\t" \
    "v_mfma_f32_16x16x32_bf16 v[8:11], v[48:51], v[152:155], v[8:11]\n\t" \
    "ds_read_b128 v[64:67], %[pa] offset:3072\n\t" \
    "s_waitcnt lgkmcnt(7)\n\t" \
    "v_mfma_f32_16x16x32_bf16 v[12:15], v[48:51], v[160:163], v[12:15]\n\t" \
    "buffer_load_dwordx4 %[l16], %[rs], %[a] offen offset:3072 sc1 lds\n\t" \
    "v_add_f32 v198, v198, v54\n\t" \
    "v_add_f32 v199, v199, v55\n\t" \
    "v_mfma_f32_16x16x32_bf16 v[0:3], v[20:23], v[164:167], v[0:3]\n\t" \
    "ds_read_b128 v[44:47], %[xp] offset:30720\n\t" \
    "s_waitcnt lgkmcnt(7)\n\t" \
    "v_add_f32 v200, v200, v56\n\t" \
    "v_mfma_f32_16x16x32_bf16 v[4:7], v[20:23], v[172:175], v[4:7]\n\t" \
    "ds_read_b128 v[48:51], %[xp] offset:31744\n\t" \
    "v_add_f32 v201, v201, v57\n\t" \
    "v_mfma_f32_16x16x32_bf16 v[8:11], v[24:27], v[164:167], v[8:11]\n\t" \
    "v_add_f32 v202, v202, v58\n\t" \
    "s_waitcnt lgkmcnt(6)\n\t" \
    "v_mfma_f32_16x16x32_bf16 v[12:15], v[24:27], v[172:175], v[12:15]\n\t" \
    "v_add_f32 v203, v203, v59\n\t" \
    "s_waitcnt lgkmcnt(5)\n\t" \
    "v_add_f32 v204, v204, v60\n\t" \
    "v_add_f32 v205, v205, v61\n\t" \
    "v_mfma_f32_16x16x32_bf16 v[0:3], v[28:31], v[168:171], v[0:3]\n\t" \
    "ds_read_b128 v[20:23], %[xp] offset:32768\n\t" \
    "v_add_f32 v206, v206, v62\n\t" \
    "v_mfma_f32_16x16x32_bf16 v[4:7], v[28:31], v[176:179], v[4:7]\n\t" \
    "ds_read_b128 v[24:27], %[xp] offset:33792\n\t" \
    "v_add_f32 v207, v207, v63\n\t" \
    "v_mfma_f32_16x16x32_bf16 v[8:11], v[32:35], v[168:171], v[8:11]\n\t" \
    "s_waitcnt lgkmcnt(4)\n\t" \
    "v_add_f32 v208, v208, v64\n\t" \
    "v_mfma_f32_16x16x32_bf16 v[12:15], v[32:35], v[176:179], v[12:15]\n\t" \
    "v_add_f32 v209, v209, v65\n\t" \
    "v_add_f32 v210, v210, v66\n\t" \
    "v_add_f32 v211, v211, v67\n\t" \
    "v_mfma_f32_16x16x32_bf16 v[0:3], v[36:39], v[180:183], v[0:3]\n\t" \
    "ds_read_b128 v[28:31], %[xp] offset:34816\n\t" \
    "v_cvt_pk_bf16_f32 v52, v196, v197\n\t" \
    "v_mfma_f32_16x16x32_bf16 v[4:7], v[36:39], v[188:191], v[4:7]\n\t" \
    "ds_read_b128 v[32:35], %[xp] offset:35840\n\t" \
    "v_cvt_pk_bf16_f32 v53, v198, v199\n\t" \
    "v_mfma_f32_16x16x32_bf16 v[8:11], v[40:43], v[180:183], v[8:11]\n\t" \
    "v_cvt_pk_bf16_f32 v54, v204, v205\n\t" \
    "s_waitcnt lgkmcnt(4)\n\t" \
    "v_mfma_f32_16x16x32_bf16 v[12:15], v[40:43], v[188:191], v[12:15]\n\t" \
    "v_cvt_pk_bf16_f32 v55, v206, v207\n\t" \
    "v_cvt_pk_bf16_f32 v56, v200, v201\n\t" \
    "v_cvt_pk_bf16_f32 v57, v202, v203\n\t" \
    "v_mfma_f32_16x16x32_bf16 v[0:3], v[44:47], v[184:187], v[0:3]\n\t" \
    "ds_read_b128 v[36:39], %[xp] offset:36864\n\t" \
    "v_cvt_pk_bf16_f32 v58, v208, v209\n\t" \
    "v_mfma_f32_16x16x32_bf16 v[4:7], v[44:47], v[192:195], v[4:7]\n\t" \
    "ds_read_b128 v[40:43], %[xp] offset:37888\n\t" \
    "v_cvt_pk_bf16_f32 v59, v210, v211\n\t" \
    "v_mfma_f32_16x16x32_bf16 v[8:11], v[48:51], v[184:187], v[8:11]\n\t" \
    "s_waitcnt lgkmcnt(4)\n\t" \
    "v_mfma_f32_16x16x32_bf16 v[12:15], v[48:51], v[192:195], v[12:15]\n\t" \
    "s_mov_b64 exec, %[em]\n\t" \
    "buffer_store_dwordx4 v[52:55], %[l16], %[rs], %[oo] offen sc1\n\t" \
    "buffer_store_dwordx4 v[56:59], %[l16], %[rs], %[oo] offen offset:1024 sc1\n\t" \
    "s_mov_b64 exec, -1\n\t" \
    "s_add_u32 %[sx], %[sx], 65536\n\t" \
    "s_add_u32 %[t], %[t], 1\n\t" \
    "s_add_u32 %[oo], %[oo], 262144\n\t" \
    "s_waitcnt vmcnt(6)\n\t" \
    "s_cmp_gt_u32 %[t], " STR(SQUICK) " + 1\n\t" \
    "s_cbranch_scc1 B0_%=\n\t" \
    "s_waitcnt vmcnt(0)\n\t" \
    "B0_%=:\n\t" \
    "s_barrier\n\t" \
    "s_setprio 1\n\t" \
    "s_add_u32 m0, %[sd], 16384\n\t" \
    "s_add_u32 %[a], %[sx], 81920\n\t" \
    "v_mfma_f32_16x16x32_bf16 v[196:199], v[20:23], v[68:71], 0\n\t" \
    "ds_read_b128 v[44:47], %[xp] offset:38912\n\t" \
    "v_mfma_f32_16x16x32_bf16 v[200:203], v[20:23], v[76:79], 0\n\t" \
    "ds_read_b128 v[48:51], %[xp] offset:39936\n\t" \
    "v_mfma_f32_16x16x32_bf16 v[204:207], v[24:27], v[68:71], 0\n\t" \
    "s_waitcnt lgkmcnt(4)\n\t" \
    "v_mfma_f32_16x16x32_bf16 v[208:211], v[24:27], v[76:79], 0\n\t" \
    "v_mfma_f32_16x16x32_bf16 v[196:199], v[28:31], v[72:75], v[196:199]\n\t" \
    "ds_read_b128 v[20:23], %[xp] offset:40960\n\t" \
    "v_mfma_f32_16x16x32_bf16 v[200:203], v[28:31], v[80:83], v[200:203]\n\t" \
    "ds_read_b128 v[24:27], %[xp] offset:41984\n\t" \
    "v_mfma_f32_16x16x32_bf16 v[204:207], v[32:35], v[72:75], v[204:207]\n\t" \
    "s_waitcnt lgkmcnt(4)\n\t" \
    "v_mfma_f32_16x16x32_bf16 v[208:211], v[32:35], v[80:83], v[208:211]\n\t" \
    "v_mfma_f32_16x16x32_bf16 v[196:199], v[36:39], v[84:87], v[196:199]\n\t" \
    "ds_read_b128 v[28:31], %[xp] offset:43008\n\t" \
    "v_mfma_f32_16x16x32_bf16 v[200:203], v[36:39], v[92:95], v[200:203]\n\t" \
    "ds_read_b128 v[32:35], %[xp] offset:44032\n\t" \
    "v_mfma_f32_16x16x32_bf16 v[204:207], v[40:43], v[84:87], v[204:207]\n\t" \
    "s_waitcnt lgkmcnt(4)\n\t" \
    "v_mfma_f32_16x16x32_bf16 v[208:211], v[40:43], v[92:95], v[208:211]\n\t" \
    "v_mfma_f32_16x16x32_bf16 v[196:199], v[44:47], v[88:91], v[196:199]\n\t" \
    "ds_read_b128 v[36:39], %[xp] offset:45056\n\t" \
    "v_mfma_f32_16x16x32_bf16 v[200:203], v[44:47], v[96:99], v[200:203]\n\t" \
    "ds_read_b128 v[40:43], %[xp] offset:46080\n\t" \
    "v_mfma_f32_16x16x32_bf16 v[204:207], v[48:51], v[88:91], v[204:207]\n\t" \
    "s_waitcnt lgkmcnt(4)\n\t" \
    "v_mfma_f32_16x16x32_bf16 v[208:211], v[48:51], v[96:99], v[208:211]\n\t" \
    "v_mfma_f32_16x16x32_bf16 v[196:199], v[20:23], v[100:103], v[196:199]\n\t" \
    "ds_read_b128 v[44:47], %[xp] offset:47104\n\t" \
    "v_mfma_f32_16x16x32_bf16 v[200:203], v[20:23], v[108:111], v[200:203]\n\t" \
    "ds_read_b128 v[48:51], %[xp] offset:48128\n\t" \
    "v_mfma_f32_16x16x32_bf16 v[204:207], v[24:27], v[100:103], v[204:207]\n\t" \
    "s_waitcnt lgkmcnt(4)\n\t" \
    "v_mfma_f32_16x16x32_bf16 v[208:211], v[24:27], v[108:111], v[208:211]\n\t" \
    "buffer_load_dwordx4 %[l16], %[rs], %[a] offen offset:0 sc1 lds\n\t" \
    "v_mfma_f32_16x16x32_bf16 v[196:199], v[28:31], v[104:107], v[196:199]\n\t" \
    "ds_read_b128 v[20:23], %[xp] offset:49152\n\t" \
    "v_mfma_f32_16x16x32_bf16 v[200:203], v[28:31], v[112:115], v[200:203]\n\t" \
    "ds_read_b128 v[24:27], %[xp] offset:50176\n\t" \
    "v_mfma_f32_16x16x32_bf16 v[204:207], v[32:35], v[104:107], v[204:207]\n\t" \
    "s_waitcnt lgkmcnt(4)\n\t" \
    "v_mfma_f32_16x16x32_bf16 v[208:211], v[32:35], v[112:115], v[208:211]\n\t" \
    "buffer_load_dwordx4 %[l16], %[rs], %[a] offen offset:1024 sc1 lds\n\t" \
    "v_mfma_f32_16x16x32_bf16 v[196:199], v[36:39], v[116:119], v[196:199]\n\t" \
    "ds_read_b128 v[28:31], %[xp] offset:51200\n\t" \
    "v_mfma_f32_16x16x32_bf16 v[200:203], v[36:39], v[124:127], v[200:203]\n\t" \
    "ds_read_b128 v[32:35], %[xp] offset:52224\n\t" \
    "v_mfma_f32_16x16x32_bf16 v[204:207], v[40:43], v[116:119], v[204:207]\n\t" \
    "s_waitcnt lgkmcnt(4)\n\t" \
    "v_mfma_f32_16x16x32_bf16 v[208:211], v[40:43], v[124:127], v[208:211]\n\t" \
    "buffer_load_dwordx4 %[l16], %[rs], %[a] offen offset:2048 sc1 lds\n\t" \
    "v_mfma_f32_16x16x32_bf16 v[196:199], v[44:47], v[120:123], v[196:199]\n\t" \
    "ds_read_b128 v[36:39], %[xp] offset:53248\n\t" \
    "v_mfma_f32_16x16x32_bf16 v[200:203], v[44:47], v[128:131], v[200:203]\n\t" \
    "ds_read_b128 v[40:43], %[xp] offset:54272\n\t" \
    "v_mfma_f32_16x16x32_bf16 v[204:207], v[48:51], v[120:123], v[204:207]\n\t" \
    "s_waitcnt lgkmcnt(4)\n\t" \
    "v_mfma_f32_16x16x32_bf16 v[208:211], v[48:51], v[128:131], v[208:211]\n\t" \
    "buffer_load_dwordx4 %[l16], %[rs], %[a] offen offset:3072 sc1 lds\n\t" \
    "s_waitcnt vmcnt(4)\n\t" \
    "s_barrier\n\t" \
    "s_setprio 1\n\t" \
    "s_add_u32 m0, %[sd], 32768\n\t" \
    "s_add_u32 %[a], %[sx], 131072\n\t" \
    "s_add_u32 %[b], %[oo], 0x1000\n\t" \
    "s_cmp_lg_u32 %[t], 0\n\t" \
    "s_cselect_b64 %[em], -1, 0\n\t" \
    "v_mfma_f32_16x16x32_bf16 v[196:199], v[20:23], v[132:135], v[196:199]\n\t" \
    "ds_read_b128 v[44:47], %[xp] offset:55296\n\t" \
    "v_mfma_f32_16x16x32_bf16 v[200:203], v[20:23], v[140:143], v[200:203]\n\t" \
    "ds_read_b128 v[48:51], %[xp] offset:56320\n\t" \
    "v_mfma_f32_16x16x32_bf16 v[204:207], v[24:27], v[132:135], v[204:207]\n\t" \
    "ds_read_b128 v[52:55], %[pa] offset:0\n\t" \
    "s_waitcnt lgkmcnt(5)\n\t" \
    "v_mfma_f32_16x16x32_bf16 v[208:211], v[24:27], v[140:143], v[208:211]\n\t" \
    "buffer_load_dwordx4 %[l16], %[rs], %[a] offen offset:0 sc1 lds\n\t" \
    "v_mfma_f32_16x16x32_bf16 v[196:199], v[28:31], v[136:139], v[196:199]\n\t" \
    "ds_read_b128 v[20:23], %[xp] offset:57344\n\t" \
    "v_mfma_f32_16x16x32_bf16 v[200:203], v[28:31], v[144:147], v[200:203]\n\t" \
    "ds_read_b128 v[24:27], %[xp] offset:58368\n\t" \
    "v_mfma_f32_16x16x32_bf16 v[204:207], v[32:35], v[136:139], v[204:207]\n\t" \
    "ds_read_b128 v[56:59], %[pa] offset:1024\n\t" \
    "s_waitcnt lgkmcnt(6)\n\t" \
    "v_mfma_f32_16x16x32_bf16 v[208:211], v[32:35], v[144:147], v[208:211]\n\t" \
    "buffer_load_dwordx4 %[l16], %[rs], %[a] offen offset:1024 sc1 lds\n\t" \
    "v_mfma_f32_16x16x32_bf16 v[196:199], v[36:39], v[148:151], v[196:199]\n\t" \
    "ds_read_b128 v[28:31], %[xp] offset:59392\n\t" \
    "v_mfma_f32_16x16x32_bf16 v[200:203], v[36:39], v[156:159], v[200:203]\n\t" \
    "ds_read_b128 v[32:35], %[xp] offset:60416\n\t" \
    "v_mfma_f32_16x16x32_bf16 v[204:207], v[40:43], v[148:151], v[204:207]\n\t" \
    "ds_read_b128 v[60:63], %[pa] offset:2048\n\t" \
    "s_waitcnt lgkmcnt(7)\n\t" \
    "v_mfma_f32_16x16x32_bf16 v[208:211], v[40:43], v[156:159], v[208:211]\n\t" \
    "buffer_load_dwordx4 %[l16], %[rs], %[a] offen offset:2048 sc1 lds\n\t" \
    "v_mfma_f32_16x16x32_bf16 v[196:199], v[44:47], v[152:155], v[196:199]\n\t" \
    "ds_read_b128 v[36:39], %[xp] offset:61440\n\t" \
    "s_waitcnt lgkmcnt(7)\n\t" \
    "v_add_f32 v0, v0, v52\n\t" \
    "v_mfma_f32_16x16x32_bf16 v[200:203], v[44:47], v[160:163], v[200:203]\n\t" \
    "ds_read_b128 v[40:43], %[xp] offset:62464\n\t" \
    "v_add_f32 v1, v1, v53\n\t" \
    "v_mfma_f32_16x16x32_bf16 v[204:207], v[48:51], v[152:155], v[204:207]\n\t" \
    "ds_read_b128 v[64:67], %[pa] offset:3072\n\t" \
    "s_waitcnt lgkmcnt(7)\n\t" \
    "v_mfma_f32_16x16x32_bf16 v[208:211], v[48:51], v[160:163], v[208:211]\n\t" \
    "buffer_load_dwordx4 %[l16], %[rs], %[a] offen offset:3072 sc1 lds\n\t" \
    "v_add_f32 v2, v2, v54\n\t" \
    "v_add_f32 v3, v3, v55\n\t" \
    "v_mfma_f32_16x16x32_bf16 v[196:199], v[20:23], v[164:167], v[196:199]\n\t" \
    "ds_read_b128 v[44:47], %[xp] offset:63488\n\t" \
    "s_waitcnt lgkmcnt(7)\n\t" \
    "v_add_f32 v4, v4, v56\n\t" \
    "v_mfma_f32_16x16x32_bf16 v[200:203], v[20:23], v[172:175], v[200:203]\n\t" \
    "ds_read_b128 v[48:51], %[xp] offset:64512\n\t" \
    "v_add_f32 v5, v5, v57\n\t" \
    "v_mfma_f32_16x16x32_bf16 v[204:207], v[24:27], v[164:167], v[204:207]\n\t" \
    "v_add_f32 v6, v6, v58\n\t" \
    "s_waitcnt lgkmcnt(6)\n\t" \
    "v_mfma_f32_16x16x32_bf16 v[208:211], v[24:27], v[172:175], v[208:211]\n\t" \
    "v_add_f32 v7, v7, v59\n\t" \
    "s_waitcnt lgkmcnt(5)\n\t" \
    "v_add_f32 v8, v8, v60\n\t" \
    "v_add_f32 v9, v9, v61\n\t" \
    "v_mfma_f32_16x16x32_bf16 v[196:199], v[28:31], v[168:171], v[196:199]\n\t" \
    "ds_read_b128 v[20:23], %[xp] offset:0\n\t" \
    "v_add_f32 v10, v10, v62\n\t" \
    "v_mfma_f32_16x16x32_bf16 v[200:203], v[28:31], v[176:179], v[200:203]\n\t" \
    "ds_read_b128 v[24:27], %[xp] offset:1024\n\t" \
    "v_add_f32 v11, v11, v63\n\t" \
    "v_mfma_f32_16x16x32_bf16 v[204:207], v[32:35], v[168:171], v[204:207]\n\t" \
    "s_waitcnt lgkmcnt(4)\n\t" \
    "v_add_f32 v12, v12, v64\n\t" \
    "v_mfma_f32_16x16x32_bf16 v[208:211], v[32:35], v[176:179], v[208:211]\n\t" \
    "v_add_f32 v13, v13, v65\n\t" \
    "v_add_f32 v14, v14, v66\n\t" \
    "v_add_f32 v15, v15, v67\n\t" \
    "v_mfma_f32_16x16x32_bf16 v[196:199], v[36:39], v[180:183], v[196:199]\n\t" \
    "ds_read_b128 v[28:31], %[xp] offset:2048\n\t" \
    "v_cvt_pk_bf16_f32 v52, v0, v1\n\t" \
    "v_mfma_f32_16x16x32_bf16 v[200:203], v[36:39], v[188:191], v[200:203]\n\t" \
    "ds_read_b128 v[32:35], %[xp] offset:3072\n\t" \
    "v_cvt_pk_bf16_f32 v53, v2, v3\n\t" \
    "v_mfma_f32_16x16x32_bf16 v[204:207], v[40:43], v[180:183], v[204:207]\n\t" \
    "v_cvt_pk_bf16_f32 v54, v8, v9\n\t" \
    "s_waitcnt lgkmcnt(4)\n\t" \
    "v_mfma_f32_16x16x32_bf16 v[208:211], v[40:43], v[188:191], v[208:211]\n\t" \
    "v_cvt_pk_bf16_f32 v55, v10, v11\n\t" \
    "v_cvt_pk_bf16_f32 v56, v4, v5\n\t" \
    "v_cvt_pk_bf16_f32 v57, v6, v7\n\t" \
    "v_mfma_f32_16x16x32_bf16 v[196:199], v[44:47], v[184:187], v[196:199]\n\t" \
    "ds_read_b128 v[36:39], %[xp] offset:4096\n\t" \
    "v_cvt_pk_bf16_f32 v58, v12, v13\n\t" \
    "v_mfma_f32_16x16x32_bf16 v[200:203], v[44:47], v[192:195], v[200:203]\n\t" \
    "ds_read_b128 v[40:43], %[xp] offset:5120\n\t" \
    "v_cvt_pk_bf16_f32 v59, v14, v15\n\t" \
    "v_mfma_f32_16x16x32_bf16 v[204:207], v[48:51], v[184:187], v[204:207]\n\t" \
    "s_waitcnt lgkmcnt(4)\n\t" \
    "v_mfma_f32_16x16x32_bf16 v[208:211], v[48:51], v[192:195], v[208:211]\n\t" \
    "s_mov_b64 exec, %[em]\n\t" \
    "buffer_store_dwordx4 v[52:55], %[l16], %[rs], %[oo] offen sc1\n\t" \
    "buffer_store_dwordx4 v[56:59], %[l16], %[rs], %[oo] offen offset:1024 sc1\n\t" \
    "s_mov_b64 exec, -1\n\t" \
    "s_add_u32 %[sx], %[sx], 65536\n\t" \
    "s_add_u32 %[t], %[t], 1\n\t" \
    "s_add_u32 %[oo], %[oo], 262144\n\t" \
    "s_waitcnt vmcnt(6)\n\t" \
    "s_cmp_gt_u32 %[t], " STR(SQUICK) " + 1\n\t" \
    "s_cbranch_scc1 B1_%=\n\t" \
    "s_waitcnt vmcnt(0)\n\t" \
    "B1_%=:\n\t" \
    "s_barrier\n\t" \
    "s_cmp_lt_u32 %[t], %[te]\n\t" \
    "s_cbranch_scc1 L1_%=\n\t" \
    "s_cmp_lg_u32 %[fin], 0\n\t" \
    "s_cbranch_scc0 E_%=\n\t" \
    "s_nop 4\n\t" \
    "s_barrier\n\t" \
    "ds_read_b128 v[52:55], %[pa] offset:0\n\t" \
    "ds_read_b128 v[56:59], %[pa] offset:1024\n\t" \
    "ds_read_b128 v[60:63], %[pa] offset:2048\n\t" \
    "ds_read_b128 v[64:67], %[pa] offset:3072\n\t" \
    "s_add_u32 %[b], %[oo], 0x1000\n\t" \
    "s_waitcnt lgkmcnt(0)\n\t" \
    "v_add_f32 v196, v196, v52\n\t" \
    "v_add_f32 v197, v197, v53\n\t" \
    "v_add_f32 v198, v198, v54\n\t" \
    "v_add_f32 v199, v199, v55\n\t" \
    "v_add_f32 v200, v200, v56\n\t" \
    "v_add_f32 v201, v201, v57\n\t" \
    "v_add_f32 v202, v202, v58\n\t" \
    "v_add_f32 v203, v203, v59\n\t" \
    "v_add_f32 v204, v204, v60\n\t" \
    "v_add_f32 v205, v205, v61\n\t" \
    "v_add_f32 v206, v206, v62\n\t" \
    "v_add_f32 v207, v207, v63\n\t" \
    "v_add_f32 v208, v208, v64\n\t" \
    "v_add_f32 v209, v209, v65\n\t" \
    "v_add_f32 v210, v210, v66\n\t" \
    "v_add_f32 v211, v211, v67\n\t" \
    "v_cvt_pk_bf16_f32 v52, v196, v197\n\t" \
    "v_cvt_pk_bf16_f32 v53, v198, v199\n\t" \
    "v_cvt_pk_bf16_f32 v54, v204, v205\n\t" \
    "v_cvt_pk_bf16_f32 v55, v206, v207\n\t" \
    "v_cvt_pk_bf16_f32 v56, v200, v201\n\t" \
    "v_cvt_pk_bf16_f32 v57, v202, v203\n\t" \
    "v_cvt_pk_bf16_f32 v58, v208, v209\n\t" \
    "v_cvt_pk_bf16_f32 v59, v210, v211\n\t" \
    "buffer_store_dwordx4 v[52:55], %[l16], %[rs], %[oo] offen sc1\n\t" \
    "buffer_store_dwordx4 v[56:59], %[l16], %[rs], %[oo] offen offset:1024 sc1\n\t" \
    "E_%=:\n\t" \
    "s_setprio 0\n\t" \
    "s_waitcnt lgkmcnt(0)\n\t" \
    "s_nop 7\n\t" \
    : [t] "+s"(t), [sx] "+s"(sx), [oo] "+s"(oo), [a] "=&s"(tmp), [b] "=&s"(tmp2), [em] "=&s"(em), "+{v[20:27]}"(xf[0]), "+{v[28:35]}"(xf[1]), "+{v[36:43]}"(xf[2]), "+{v[196:203]}"(ac[0]), "+{v[204:211]}"(ac[1]) \
    : [te] "s"(TE_), [fin] "s"(FIN_), [kh] "s"(kh), [sd] "s"(sd), [rs] "s"(rs), [l16] "v"(l16), [xp] "v"(xp), [pa] "v"(pa), [fm] "s"(fm), [pm] "s"(pm), [fo] "s"(fo), [po] "s"(po), [pd] "s"(pd), [tag] "s"(P.tag), "{v[68:75]}"(w[0][0]), "{v[76:83]}"(w[0][1]), "{v[84:91]}"(w[1][0]), "{v[92:99]}"(w[1][1]), "{v[100:107]}"(w[2][0]), "{v[108:115]}"(w[2][1]), "{v[116:123]}"(w[3][0]), "{v[124:131]}"(w[3][1]), "{v[132:139]}"(w[4][0]), "{v[140:147]}"(w[4][1]), "{v[148:155]}"(w[5][0]), "{v[156:163]}"(w[5][1]), "{v[164:171]}"(w[6][0]), "{v[172:179]}"(w[6][1]), "{v[180:187]}"(w[7][0]), "{v[188:195]}"(w[7][1]) \
    : "memory", "scc", "v0", "v1", "v2", "v3", "v4", "v5", "v6", "v7", "v8", "v9", "v10", "v11", "v12", "v13", "v14", "v15", "v44", "v45", "v46", "v47", "v48", "v49", "v50", "v51", "v52", "v53", "v54", "v55", "v56", "v57", "v58", "v59", "v60", "v61", "v62", "v63", "v64", "v65", "v66", "v67")

#define S_LOOP_X(TE_, FIN_) \
  asm volatile( \
    "s_cmp_eq_u32 %[kh], 0\n\t" \
    "s_cbranch_scc0 L1_%=\n\t" \
    "L0_%=:\n\t" \
    "s_setprio 0\n\t" \
    "s_nop 3\n\t" \
    "ds_write_b128 %[pa], v[196:199] offset:0\n\t" \
    "ds_write_b128 %[pa], v[200:203] offset:1024\n\t" \
    "ds_write_b128 %[pa], v[204:207] offset:2048\n\t" \
    "ds_write_b128 %[pa], v[208:211] offset:3072\n\t" \
    "s_sub_u32 %[a], %[t], 1\n\t" \
    "v_mov_b32 v53, %[tag]\n\t" \
    "v_mov_b32 v52, %[a]\n\t" \
    "s_sub_u32 %[b], %[t], 2\n\t" \
    "s_cmp_lt_u32 %[b], " STR(SQUICK) "\n\t" \
    "s_cselect_b64 exec, %[fm], 0\n\t" \
    "buffer_store_dwordx2 v[52:53], %[l16], %[rs], %[fo] offen sc1\n\t" \
    "s_mov_b64 exec, -1\n\t" \
    "s_add_u32 m0, %[sd], 49152\n\t" \
    "s_add_u32 %[a], %[sx], 278528\n\t" \
    "s_waitcnt lgkmcnt(0)\n\t" \
    "buffer_load_dwordx4 %[l16], %[rs], %[a] offen offset:0 sc1 lds\n\t" \
    "buffer_load_dwordx4 %[l16], %[rs], %[a] offen offset:1024 sc1 lds\n\t" \
    "buffer_load_dwordx4 %[l16], %[rs], %[a] offen offset:2048 sc1 lds\n\t" \
    "buffer_load_dwordx4 %[l16], %[rs], %[a] offen offset:3072 sc1 lds\n\t" \
    "v_mfma_f32_16x16x32_bf16 v[0:3], v[68:71], v[20:23], 0\n\t" \
    "ds_read_b128 v[44:47], %[xp] offset:6144\n\t" \
    "v_mfma_f32_16x16x32_bf16 v[4:7], v[76:79], v[20:23], 0\n\t" \
    "ds_read_b128 v[48:51], %[xp] offset:7168\n\t" \
    "v_mfma_f32_16x16x32_bf16 v[8:11], v[68:71], v[24:27], 0\n\t" \
    "v_mfma_f32_16x16x32_bf16 v[12:15], v[76:79], v[24:27], 0\n\t" \
    "v_mfma_f32_16x16x32_bf16 v[0:3], v[72:75], v[28:31], v[0:3]\n\t" \
    "ds_read_b128 v[20:23], %[xp] offset:8192\n\t" \
    "v_mfma_f32_16x16x32_bf16 v[4:7], v[80:83], v[28:31], v[4:7]\n\t" \
    "ds_read_b128 v[24:27], %[xp] offset:9216\n\t" \
    "v_mfma_f32_16x16x32_bf16 v[8:11], v[72:75], v[32:35], v[8:11]\n\t" \
    "v_mfma_f32_16x16x32_bf16 v[12:15], v[80:83], v[32:35], v[12:15]\n\t" \
    "v_mfma_f32_16x16x32_bf16 v[0:3], v[84:87], v[36:39], v[0:3]\n\t" \
    "ds_read_b128 v[28:31], %[xp] offset:10240\n\t" \
    "v_mfma_f32_16x16x32_bf16 v[4:7], v[92:95], v[36:39], v[4:7]\n\t" \
    "ds_read_b128 v[32:35], %[xp] offset:11264\n\t" \
    "v_mfma_f32_16x16x32_bf16 v[8:11], v[84:87], v[40:43], v[8:11]\n\t" \
    "s_waitcnt lgkmcnt(4)\n\t" \
    "v_mfma_f32_16x16x32_bf16 v[12:15], v[92:95], v[40:43], v[12:15]\n\t" \
    "v_mfma_f32_16x16x32_bf16 v[0:3], v[88:91], v[44:47], v[0:3]\n\t" \
    "ds_read_b128 v[36:39], %[xp] offset:12288\n\t" \
    "v_mfma_f32_16x16x32_bf16 v[4:7], v[96:99], v[44:47], v[4:7]\n\t" \
    "ds_read_b128 v[40:43], %[xp] offset:13312\n\t" \
    "v_mfma_f32_16x16x32_bf16 v[8:11], v[88:91], v[48:51], v[8:11]\n\t" \
    "s_waitcnt lgkmcnt(4)\n\t" \
    "v_mfma_f32_16x16x32_bf16 v[12:15], v[96:99], v[48:51], v[12:15]\n\t" \
    "v_mfma_f32_16x16x32_bf16 v[0:3], v[100:103], v[20:23], v[0:3]\n\t" \
    "ds_read_b128 v[44:47], %[xp] offset:14336\n\t" \
    "v_mfma_f32_16x16x32_bf16 v[4:7], v[108:111], v[20:23], v[4:7]\n\t" \
    "ds_read_b128 v[48:51], %[xp] offset:15360\n\t" \
    "v_mfma_f32_16x16x32_bf16 v[8:11], v[100:103], v[24:27], v[8:11]\n\t" \
    "s_waitcnt lgkmcnt(4)\n\t" \
    "v_mfma_f32_16x16x32_bf16 v[12:15], v[108:111], v[24:27], v[12:15]\n\t" \
    "v_mfma_f32_16x16x32_bf16 v[0:3], v[104:107], v[28:31], v[0:3]\n\t" \
    "ds_read_b128 v[20:23], %[xp] offset:16384\n\t" \
    "v_mfma_f32_16x16x32_bf16 v[4:7], v[112:115], v[28:31], v[4:7]\n\t" \
    "ds_read_b128 v[24:27], %[xp] offset:17408\n\t" \
    "v_mfma_f32_16x16x32_bf16 v[8:11], v[104:107], v[32:35], v[8:11]\n\t" \
    "s_waitcnt lgkmcnt(4)\n\t" \
    "v_mfma_f32_16x16x32_bf16 v[12:15], v[112:115], v[32:35], v[12:15]\n\t" \
    "v_mfma_f32_16x16x32_bf16 v[0:3], v[116:119], v[36:39], v[0:3]\n\t" \
    "ds_read_b128 v[28:31], %[xp] offset:18432\n\t" \
    "v_mfma_f32_16x16x32_bf16 v[4:7], v[124:127], v[36:39], v[4:7]\n\t" \
    "ds_read_b128 v[32:35], %[xp] offset:19456\n\t" \
    "v_mfma_f32_16x16x32_bf16 v[8:11], v[116:119], v[40:43], v[8:11]\n\t" \
    "s_waitcnt lgkmcnt(4)\n\t" \
    "v_mfma_f32_16x16x32_bf16 v[12:15], v[124:127], v[40:43], v[12:15]\n\t" \
    "v_mfma_f32_16x16x32_bf16 v[0:3], v[120:123], v[44:47], v[0:3]\n\t" \
    "ds_read_b128 v[36:39], %[xp] offset:20480\n\t" \
    "v_mfma_f32_16x16x32_bf16 v[4:7], v[128:131], v[44:47], v[4:7]\n\t" \
    "ds_read_b128 v[40:43], %[xp] offset:21504\n\t" \
    "v_mfma_f32_16x16x32_bf16 v[8:11], v[120:123], v[48:51], v[8:11]\n\t" \
    "s_waitcnt lgkmcnt(4)\n\t" \
    "v_mfma_f32_16x16x32_bf16 v[12:15], v[128:131], v[48:51], v[12:15]\n\t" \
    "s_waitcnt vmcnt(4)\n\t" \
    "s_barrier\n\t" \
    "s_setprio 0\n\t" \
    "s_sub_u32 %[a], %[t], 1\n\t" \
    "v_mov_b32 v53, %[tag]\n\t" \
    "v_mov_b32 v52, %[a]\n\t" \
    "s_cmp_gt_u32 %[t], 1\n\t" \
    "s_cselect_b64 exec, %[fm], 0\n\t" \
    "buffer_store_dwordx2 v[52:53], %[l16], %[rs], %[fo] offen sc1\n\t" \
    "s_mov_b64 exec, %[pm]\n\t" \
    "s_mov_b32 m0, %[pd]\n\t" \
    "s_add_u32 %[a], %[sx], %[po]\n\t" \
    "buffer_load_dwordx4 %[l16], %[rs], %[a] offen sc1 lds\n\t" \
    "s_mov_b64 exec, -1\n\t" \
    "s_add_u32 m0, %[sd], 0\n\t" \
    "s_add_u32 %[a], %[sx], 524288\n\t" \
    "buffer_load_dwordx4 %[l16], %[rs], %[a] offen offset:0 sc1 lds\n\t" \
    "buffer_load_dwordx4 %[l16], %[rs], %[a] offen offset:1024 sc1 lds\n\t" \
    "buffer_load_dwordx4 %[l16], %[rs], %[a] offen offset:2048 sc1 lds\n\t" \
    "buffer_load_dwordx4 %[l16], %[rs], %[a] offen offset:3072 sc1 lds\n\t" \
    "v_mfma_f32_16x16x32_bf16 v[0:3], v[132:135], v[20:23], v[0:3]\n\t" \
    "ds_read_b128 v[44:47], %[xp] offset:22528\n\t" \
    "v_mfma_f32_16x16x32_bf16 v[4:7], v[140:143], v[20:23], v[4:7]\n\t" \
    "ds_read_b128 v[48:51], %[xp] offset:23552\n\t" \
    "v_mfma_f32_16x16x32_bf16 v[8:11], v[132:135], v[24:27], v[8:11]\n\t" \
    "s_waitcnt lgkmcnt(4)\n\t" \
    "v_mfma_f32_16x16x32_bf16 v[12:15], v[140:143], v[24:27], v[12:15]\n\t" \
    "v_mfma_f32_16x16x32_bf16 v[0:3], v[136:139], v[28:31], v[0:3]\n\t" \
    "ds_read_b128 v[20:23], %[xp] offset:24576\n\t" \
    "v_mfma_f32_16x16x32_bf16 v[4:7], v[144:147], v[28:31], v[4:7]\n\t" \
    "ds_read_b128 v[24:27], %[xp] offset:25600\n\t" \
    "v_mfma_f32_16x16x32_bf16 v[8:11], v[136:139], v[32:35], v[8:11]\n\t" \
    "s_waitcnt lgkmcnt(4)\n\t" \
    "v_mfma_f32_16x16x32_bf16 v[12:15], v[144:147], v[32:35], v[12:15]\n\t" \
    "v_mfma_f32_16x16x32_bf16 v[0:3], v[148:151], v[36:39], v[0:3]\n\t" \
    "ds_read_b128 v[28:31], %[xp] offset:26624\n\t" \
    "v_mfma_f32_16x16x32_bf16 v[4:7], v[156:159], v[36:39], v[4:7]\n\t" \
    "ds_read_b128 v[32:35], %[xp] offset:27648\n\t" \
    "v_mfma_f32_16x16x32_bf16 v[8:11], v[148:151], v[40:43], v[8:11]\n\t" \
    "s_waitcnt lgkmcnt(4)\n\t" \
    "v_mfma_f32_16x16x32_bf16 v[12:15], v[156:159], v[40:43], v[12:15]\n\t" \
    "v_mfma_f32_16x16x32_bf16 v[0:3], v[152:155], v[44:47], v[0:3]\n\t" \
    "ds_read_b128 v[36:39], %[xp] offset:28672\n\t" \
    "v_mfma_f32_16x16x32_bf16 v[4:7], v[160:163], v[44:47], v[4:7]\n\t" \
    "ds_read_b128 v[40:43], %[xp] offset:29696\n\t" \
    "v_mfma_f32_16x16x32_bf16 v[8:11], v[152:155], v[48:51], v[8:11]\n\t" \
    "s_waitcnt lgkmcnt(4)\n\t" \
    "v_mfma_f32_16x16x32_bf16 v[12:15], v[160:163], v[48:51], v[12:15]\n\t" \
    "v_mfma_f32_16x16x32_bf16 v[0:3], v[164:167], v[20:23], v[0:3]\n\t" \
    "ds_read_b128 v[44:47], %[xp] offset:30720\n\t" \
    "v_mfma_f32_16x16x32_bf16 v[4:7], v[172:175], v[20:23], v[4:7]\n\t" \
    "ds_read_b128 v[48:51], %[xp] offset:31744\n\t" \
    "v_mfma_f32_16x16x32_bf16 v[8:11], v[164:167], v[24:27], v[8:11]\n\t" \
    "s_waitcnt lgkmcnt(4)\n\t" \
    "v_mfma_f32_16x16x32_bf16 v[12:15], v[172:175], v[24:27], v[12:15]\n\t" \
    "v_mfma_f32_16x16x32_bf16 v[0:3], v[168:171], v[28:31], v[0:3]\n\t" \
    "ds_read_b128 v[20:23], %[xp] offset:32768\n\t" \
    "v_mfma_f32_16x16x32_bf16 v[4:7], v[176:179], v[28:31], v[4:7]\n\t" \
    "ds_read_b128 v[24:27], %[xp] offset:33792\n\t" \
    "v_mfma_f32_16x16x32_bf16 v[8:11], v[168:171], v[32:35], v[8:11]\n\t" \
    "s_waitcnt lgkmcnt(4)\n\t" \
    "v_mfma_f32_16x16x32_bf16 v[12:15], v[176:179], v[32:35], v[12:15]\n\t" \
    "v_mfma_f32_16x16x32_bf16 v[0:3], v[180:183], v[36:39], v[0:3]\n\t" \
    "ds_read_b128 v[28:31], %[xp] offset:34816\n\t" \
    "v_mfma_f32_16x16x32_bf16 v[4:7], v[188:191], v[36:39], v[4:7]\n\t" \
    "ds_read_b128 v[32:35], %[xp] offset:35840\n\t" \
    "v_mfma_f32_16x16x32_bf16 v[8:11], v[180:183], v[40:43], v[8:11]\n\t" \
    "s_waitcnt lgkmcnt(4)\n\t" \
    "v_mfma_f32_16x16x32_bf16 v[12:15], v[188:191], v[40:43], v[12:15]\n\t" \
    "v_mfma_f32_16x16x32_bf16 v[0:3], v[184:187], v[44:47], v[0:3]\n\t" \
    "ds_read_b128 v[36:39], %[xp] offset:36864\n\t" \
    "v_mfma_f32_16x16x32_bf16 v[4:7], v[192:195], v[44:47], v[4:7]\n\t" \
    "ds_read_b128 v[40:43], %[xp] offset:37888\n\t" \
    "v_mfma_f32_16x16x32_bf16 v[8:11], v[184:187], v[48:51], v[8:11]\n\t" \
    "s_waitcnt lgkmcnt(4)\n\t" \
    "v_mfma_f32_16x16x32_bf16 v[12:15], v[192:195], v[48:51], v[12:15]\n\t" \
    "s_add_u32 %[sx], %[sx], 262144\n\t" \
    "s_add_u32 %[t], %[t], 1\n\t" \
    "s_waitcnt vmcnt(4)\n\t" \
    "s_barrier\n\t" \
    "s_setprio 0\n\t" \
    "s_nop 3\n\t" \
    "ds_write_b128 %[pa], v[0:3] offset:0\n\t" \
    "ds_write_b128 %[pa], v[4:7] offset:1024\n\t" \
    "ds_write_b128 %[pa], v[8:11] offset:2048\n\t" \
    "ds_write_b128 %[pa], v[12:15] offset:3072\n\t" \
    "s_sub_u32 %[a], %[t], 1\n\t" \
    "v_mov_b32 v53, %[tag]\n\t" \
    "v_mov_b32 v52, %[a]\n\t" \
    "s_sub_u32 %[b], %[t], 2\n\t" \
    "s_cmp_lt_u32 %[b], " STR(SQUICK) "\n\t" \
    "s_cselect_b64 exec, %[fm], 0\n\t" \
    "buffer_store_dwordx2 v[52:53], %[l16], %[rs], %[fo] offen sc1\n\t" \
    "s_mov_b64 exec, -1\n\t" \
    "s_add_u32 m0, %[sd], 16384\n\t" \
    "s_add_u32 %[a], %[sx], 278528\n\t" \
    "s_waitcnt lgkmcnt(0)\n\t" \
    "buffer_load_dwordx4 %[l16], %[rs], %[a] offen offset:0 sc1 lds\n\t" \
    "buffer_load_dwordx4 %[l16], %[rs], %[a] offen offset:1024 sc1 lds\n\t" \
    "buffer_load_dwordx4 %[l16], %[rs], %[a] offen offset:2048 sc1 lds\n\t" \
    "buffer_load_dwordx4 %[l16], %[rs], %[a] offen offset:3072 sc1 lds\n\t" \
    "v_mfma_f32_16x16x32_bf16 v[196:199], v[68:71], v[20:23], 0\n\t" \
    "ds_read_b128 v[44:47], %[xp] offset:38912\n\t" \
    "v_mfma_f32_16x16x32_bf16 v[200:203], v[76:79], v[20:23], 0\n\t" \
    "ds_read_b128 v[48:51], %[xp] offset:39936\n\t" \
    "v_mfma_f32_16x16x32_bf16 v[204:207], v[68:71], v[24:27], 0\n\t" \
    "v_mfma_f32_16x16x32_bf16 v[208:211], v[76:79], v[24:27], 0\n\t" \
    "v_mfma_f32_16x16x32_bf16 v[196:199], v[72:75], v[28:31], v[196:199]\n\t" \
    "ds_read_b128 v[20:23], %[xp] offset:40960\n\t" \
    "v_mfma_f32_16x16x32_bf16 v[200:203], v[80:83], v[28:31], v[200:203]\n\t" \
    "ds_read_b128 v[24:27], %[xp] offset:41984\n\t" \
    "v_mfma_f32_16x16x32_bf16 v[204:207], v[72:75], v[32:35], v[204:207]\n\t" \
    "v_mfma_f32_16x16x32_bf16 v[208:211], v[80:83], v[32:35], v[208:211]\n\t" \
    "v_mfma_f32_16x16x32_bf16 v[196:199], v[84:87], v[36:39], v[196:199]\n\t" \
    "ds_read_b128 v[28:31], %[xp] offset:43008\n\t" \
    "v_mfma_f32_16x16x32_bf16 v[200:203], v[92:95], v[36:39], v[200:203]\n\t" \
    "ds_read_b128 v[32:35], %[xp] offset:44032\n\t" \
    "v_mfma_f32_16x16x32_bf16 v[204:207], v[84:87], v[40:43], v[204:207]\n\t" \
    "s_waitcnt lgkmcnt(4)\n\t" \
    "v_mfma_f32_16x16x32_bf16 v[208:211], v[92:95], v[40:43], v[208:211]\n\t" \
    "v_mfma_f32_16x16x32_bf16 v[196:199], v[88:91], v[44:47], v[196:199]\n\t" \
    "ds_read_b128 v[36:39], %[xp] offset:45056\n\t" \
    "v_mfma_f32_16x16x32_bf16 v[200:203], v[96:99], v[44:47], v[200:203]\n\t" \
    "ds_read_b128 v[40:43], %[xp] offset:46080\n\t" \
    "v_mfma_f32_16x16x32_bf16 v[204:207], v[88:91], v[48:51], v[204:207]\n\t" \
    "s_waitcnt lgkmcnt(4)\n\t" \
    "v_mfma_f32_16x16x32_bf16 v[208:211], v[96:99], v[48:51], v[208:211]\n\t" \
    "v_mfma_f32_16x16x32_bf16 v[196:199], v[100:103], v[20:23], v[196:199]\n\t" \
    "ds_read_b128 v[44:47], %[xp] offset:47104\n\t" \
    "v_mfma_f32_16x16x32_bf16 v[200:203], v[108:111], v[20:23], v[200:203]\n\t" \
    "ds_read_b128 v[48:51], %[xp] offset:48128\n\t" \
    "v_mfma_f32_16x16x32_bf16 v[204:207], v[100:103], v[24:27], v[204:207]\n\t" \
    "s_waitcnt lgkmcnt(4)\n\t" \
    "v_mfma_f32_16x16x32_bf16 v[208:211], v[108:111], v[24:27], v[208:211]\n\t" \
    "v_mfma_f32_16x16x32_bf16 v[196:199], v[104:107], v[28:31], v[196:199]\n\t" \
    "ds_read_b128 v[20:23], %[xp] offset:49152\n\t" \
    "v_mfma_f32_16x16x32_bf16 v[200:203], v[112:115], v[28:31], v[200:203]\n\t" \
    "ds_read_b128 v[24:27], %[xp] offset:50176\n\t" \
    "v_mfma_f32_16x16x32_bf16 v[204:207], v[104:107], v[32:35], v[204:207]\n\t" \
    "s_waitcnt lgkmcnt(4)\n\t" \
    "v_mfma_f32_16x16x32_bf16 v[208:211], v[112:115], v[32:35], v[208:211]\n\t" \
    "v_mfma_f32_16x16x32_bf16 v[196:199], v[116:119], v[36:39], v[196:199]\n\t" \
    "ds_read_b128 v[28:31], %[xp] offset:51200\n\t" \
    "v_mfma_f32_16x16x32_bf16 v[200:203], v[124:127], v[36:39], v[200:203]\n\t" \
    "ds_read_b128 v[32:35], %[xp] offset:52224\n\t" \
    "v_mfma_f32_16x16x32_bf16 v[204:207], v[116:119], v[40:43], v[204:207]\n\t" \
    "s_waitcnt lgkmcnt(4)\n\t" \
    "v_mfma_f32_16x16x32_bf16 v[208:211], v[124:127], v[40:43], v[208:211]\n\t" \
    "v_mfma_f32_16x16x32_bf16 v[196:199], v[120:123], v[44:47], v[196:199]\n\t" \
    "ds_read_b128 v[36:39], %[xp] offset:53248\n\t" \
    "v_mfma_f32_16x16x32_bf16 v[200:203], v[128:131], v[44:47], v[200:203]\n\t" \
    "ds_read_b128 v[40:43], %[xp] offset:54272\n\t" \
    "v_mfma_f32_16x16x32_bf16 v[204:207], v[120:123], v[48:51], v[204:207]\n\t" \
    "s_waitcnt lgkmcnt(4)\n\t" \
    "v_mfma_f32_16x16x32_bf16 v[208:211], v[128:131], v[48:51], v[208:211]\n\t" \
    "s_waitcnt vmcnt(4)\n\t" \
    "s_barrier\n\t" \
    "s_setprio 0\n\t" \
    "s_sub_u32 %[a], %[t], 1\n\t" \
    "v_mov_b32 v53, %[tag]\n\t" \
    "v_mov_b32 v52, %[a]\n\t" \
    "s_cmp_gt_u32 %[t], 1\n\t" \
    "s_cselect_b64 exec, %[fm], 0\n\t" \
    "buffer_store_dwordx2 v[52:53], %[l16], %[rs], %[fo] offen sc1\n\t" \
    "s_mov_b64 exec, %[pm]\n\t" \
    "s_mov_b32 m0, %[pd]\n\t" \
    "s_add_u32 %[a], %[sx], %[po]\n\t" \
    "buffer_load_dwordx4 %[l16], %[rs], %[a] offen sc1 lds\n\t" \
    "s_mov_b64 exec, -1\n\t" \
    "s_add_u32 m0, %[sd], 32768\n\t" \
    "s_add_u32 %[a], %[sx], 524288\n\t" \
    "buffer_load_dwordx4 %[l16], %[rs], %[a] offen offset:0 sc1 lds\n\t" \
    "buffer_load_dwordx4 %[l16], %[rs], %[a] offen offset:1024 sc1 lds\n\t" \
    "buffer_load_dwordx4 %[l16], %[rs], %[a] offen offset:2048 sc1 lds\n\t" \
    "buffer_load_dwordx4 %[l16], %[rs], %[a] offen offset:3072 sc1 lds\n\t" \
    "v_mfma_f32_16x16x32_bf16 v[196:199], v[132:135], v[20:23], v[196:199]\n\t" \
    "ds_read_b128 v[44:47], %[xp] offset:55296\n\t" \
    "v_mfma_f32_16x16x32_bf16 v[200:203], v[140:143], v[20:23], v[200:203]\n\t" \
    "ds_read_b128 v[48:51], %[xp] offset:56320\n\t" \
    "v_mfma_f32_16x16x32_bf16 v[204:207], v[132:135], v[24:27], v[204:207]\n\t" \
    "s_waitcnt lgkmcnt(4)\n\t" \
    "v_mfma_f32_16x16x32_bf16 v[208:211], v[140:143], v[24:27], v[208:211]\n\t" \
    "v_mfma_f32_16x16x32_bf16 v[196:199], v[136:139], v[28:31], v[196:199]\n\t" \
    "ds_read_b128 v[20:23], %[xp] offset:57344\n\t" \
    "v_mfma_f32_16x16x32_bf16 v[200:203], v[144:147], v[28:31], v[200:203]\n\t" \
    "ds_read_b128 v[24:27], %[xp] offset:58368\n\t" \
    "v_mfma_f32_16x16x32_bf16 v[204:207], v[136:139], v[32:35], v[204:207]\n\t" \
    "s_waitcnt lgkmcnt(4)\n\t" \
    "v_mfma_f32_16x16x32_bf16 v[208:211], v[144:147], v[32:35], v[208:211]\n\t" \
    "v_mfma_f32_16x16x32_bf16 v[196:199], v[148:151], v[36:39], v[196:199]\n\t" \
    "ds_read_b128 v[28:31], %[xp] offset:59392\n\t" \
    "v_mfma_f32_16x16x32_bf16 v[200:203], v[156:159], v[36:39], v[200:203]\n\t" \
    "ds_read_b128 v[32:35], %[xp] offset:60416\n\t" \
    "v_mfma_f32_16x16x32_bf16 v[204:207], v[148:151], v[40:43], v[204:207]\n\t" \
    "s_waitcnt lgkmcnt(4)\n\t" \
    "v_mfma_f32_16x16x32_bf16 v[208:211], v[156:159], v[40:43], v[208:211]\n\t" \
    "v_mfma_f32_16x16x32_bf16 v[196:199], v[152:155], v[44:47], v[196:199]\n\t" \
    "ds_read_b128 v[36:39], %[xp] offset:61440\n\t" \
    "v_mfma_f32_16x16x32_bf16 v[200:203], v[160:163], v[44:47], v[200:203]\n\t" \
    "ds_read_b128 v[40:43], %[xp] offset:62464\n\t" \
    "v_mfma_f32_16x16x32_bf16 v[204:207], v[152:155], v[48:51], v[204:207]\n\t" \
    "s_waitcnt lgkmcnt(4)\n\t" \
    "v_mfma_f32_16x16x32_bf16 v[208:211], v[160:163], v[48:51], v[208:211]\n\t" \
    "v_mfma_f32_16x16x32_bf16 v[196:199], v[164:167], v[20:23], v[196:199]\n\t" \
    "ds_read_b128 v[44:47], %[xp] offset:63488\n\t" \
    "v_mfma_f32_16x16x32_bf16 v[200:203], v[172:175], v[20:23], v[200:203]\n\t" \
    "ds_read_b128 v[48:51], %[xp] offset:64512\n\t" \
    "v_mfma_f32_16x16x32_bf16 v[204:207], v[164:167], v[24:27], v[204:207]\n\t" \
    "s_waitcnt lgkmcnt(4)\n\t" \
    "v_mfma_f32_16x16x32_bf16 v[208:211], v[172:175], v[24:27], v[208:211]\n\t" \
    "v_mfma_f32_16x16x32_bf16 v[196:199], v[168:171], v[28:31], v[196:199]\n\t" \
    "ds_read_b128 v[20:23], %[xp] offset:0\n\t" \
    "v_mfma_f32_16x16x32_bf16 v[200:203], v[176:179], v[28:31], v[200:203]\n\t" \
    "ds_read_b128 v[24:27], %[xp] offset:1024\n\t" \
    "v_mfma_f32_16x16x32_bf16 v[204:207], v[168:171], v[32:35], v[204:207]\n\t" \
    "s_waitcnt lgkmcnt(4)\n\t" \
    "v_mfma_f32_16x16x32_bf16 v[208:211], v[176:179], v[32:35], v[208:211]\n\t" \
    "v_mfma_f32_16x16x32_bf16 v[196:199], v[180:183], v[36:39], v[196:199]\n\t" \
    "ds_read_b128 v[28:31], %[xp] offset:2048\n\t" \
    "v_mfma_f32_16x16x32_bf16 v[200:203], v[188:191], v[36:39], v[200:203]\n\t" \
    "ds_read_b128 v[32:35], %[xp] offset:3072\n\t" \
    "v_mfma_f32_16x16x32_bf16 v[204:207], v[180:183], v[40:43], v[204:207]\n\t" \
    "s_waitcnt lgkmcnt(4)\n\t" \
    "v_mfma_f32_16x16x32_bf16 v[208:211], v[188:191], v[40:43], v[208:211]\n\t" \
    "v_mfma_f32_16x16x32_bf16 v[196:199], v[184:187], v[44:47], v[196:199]\n\t" \
    "ds_read_b128 v[36:39], %[xp] offset:4096\n\t" \
    "v_mfma_f32_16x16x32_bf16 v[200:203], v[192:195], v[44:47], v[200:203]\n\t" \
    "ds_read_b128 v[40:43], %[xp] offset:5120\n\t" \
    "v_mfma_f32_16x16x32_bf16 v[204:207], v[184:187], v[48:51], v[204:207]\n\t" \
    "s_waitcnt lgkmcnt(4)\n\t" \
    "v_mfma_f32_16x16x32_bf16 v[208:211], v[192:195], v[48:51], v[208:211]\n\t" \
    "s_add_u32 %[sx], %[sx], 262144\n\t" \
    "s_add_u32 %[t], %[t], 1\n\t" \
    "s_waitcnt vmcnt(4)\n\t" \
    "s_barrier\n\t" \
    "s_cmp_lt_u32 %[t], %[te]\n\t" \
    "s_cbranch_scc1 L0_%=\n\t" \
    "s_cmp_lg_u32 %[fin], 0\n\t" \
    "s_cbranch_scc0 E_%=\n\t" \
    "s_nop 4\n\t" \
    "ds_write_b128 %[pa], v[196:199] offset:0\n\t" \
    "ds_write_b128 %[pa], v[200:203] offset:1024\n\t" \
    "ds_write_b128 %[pa], v[204:207] offset:2048\n\t" \
    "ds_write_b128 %[pa], v[208:211] offset:3072\n\t" \
    "s_waitcnt lgkmcnt(0)\n\t" \
    "s_barrier\n\t" \
    "s_branch E_%=\n\t" \
    "L1_%=:\n\t" \
    "s_setprio 1\n\t" \
    "s_add_u32 m0, %[sd], 49152\n\t" \
    "s_add_u32 %[a], %[sx], 278528\n\t" \
    "v_mfma_f32_16x16x32_bf16 v[0:3], v[68:71], v[20:23], 0\n\t" \
    "ds_read_b128 v[44:47], %[xp] offset:6144\n\t" \
    "v_mfma_f32_16x16x32_bf16 v[4:7], v[76:79], v[20:23], 0\n\t" \
    "ds_read_b128 v[48:51], %[xp] offset:7168\n\t" \
    "v_mfma_f32_16x16x32_bf16 v[8:11], v[68:71], v[24:27], 0\n\t" \
    "s_waitcnt lgkmcnt(4)\n\t" \
    "v_mfma_f32_16x16x32_bf16 v[12:15], v[76:79], v[24:27], 0\n\t" \
    "v_mfma_f32_16x16x32_bf16 v[0:3], v[72:75], v[28:31], v[0:3]\n\t" \
    "ds_read_b128 v[20:23], %[xp] offset:8192\n\t" \
    "v_mfma_f32_16x16x32_bf16 v[4:7], v[80:83], v[28:31], v[4:7]\n\t" \
    "ds_read_b128 v[24:27], %[xp] offset:9216\n\t" \
    "v_mfma_f32_16x16x32_bf16 v[8:11], v[72:75], v[32:35], v[8:11]\n\t" \
    "s_waitcnt lgkmcnt(4)\n\t" \
    "v_mfma_f32_16x16x32_bf16 v[12:15], v[80:83], v[32:35], v[12:15]\n\t" \
    "v_mfma_f32_16x16x32_bf16 v[0:3], v[84:87], v[36:39], v[0:3]\n\t" \
    "ds_read_b128 v[28:31], %[xp] offset:10240\n\t" \
    "v_mfma_f32_16x16x32_bf16 v[4:7], v[92:95], v[36:39], v[4:7]\n\t" \
    "ds_read_b128 v[32:35], %[xp] offset:11264\n\t" \
    "v_mfma_f32_16x16x32_bf16 v[8:11], v[84:87], v[40:43], v[8:11]\n\t" \
    "s_waitcnt lgkmcnt(4)\n\t" \
    "v_mfma_f32_16x16x32_bf16 v[12:15], v[92:95], v[40:43], v[12:15]\n\t" \
    "v_mfma_f32_16x16x32_bf16 v[0:3], v[88:91], v[44:47], v[0:3]\n\t" \
    "ds_read_b128 v[36:39], %[xp] offset:12288\n\t" \
    "v_mfma_f32_16x16x32_bf16 v[4:7], v[96:99], v[44:47], v[4:7]\n\t" \
    "ds_read_b128 v[40:43], %[xp] offset:13312\n\t" \
    "v_mfma_f32_16x16x32_bf16 v[8:11], v[88:91], v[48:51], v[8:11]\n\t" \
    "s_waitcnt lgkmcnt(4)\n\t" \
    "v_mfma_f32_16x16x32_bf16 v[12:15], v[96:99], v[48:51], v[12:15]\n\t" \
    "v_mfma_f32_16x16x32_bf16 v[0:3], v[100:103], v[20:23], v[0:3]\n\t" \
    "ds_read_b128 v[44:47], %[xp] offset:14336\n\t" \
    "v_mfma_f32_16x16x32_bf16 v[4:7], v[108:111], v[20:23], v[4:7]\n\t" \
    "ds_read_b128 v[48:51], %[xp] offset:15360\n\t" \
    "v_mfma_f32_16x16x32_bf16 v[8:11], v[100:103], v[24:27], v[8:11]\n\t" \
    "s_waitcnt lgkmcnt(4)\n\t" \
    "v_mfma_f32_16x16x32_bf16 v[12:15], v[108:111], v[24:27], v[12:15]\n\t" \
    "buffer_load_dwordx4 %[l16], %[rs], %[a] offen offset:0 sc1 lds\n\t" \
    "v_mfma_f32_16x16x32_bf16 v[0:3], v[104:107], v[28:31], v[0:3]\n\t" \
    "ds_read_b128 v[20:23], %[xp] offset:16384\n\t" \
    "v_mfma_f32_16x16x32_bf16 v[4:7], v[112:115], v[28:31], v[4:7]\n\t" \
    "ds_read_b128 v[24:27], %[xp] offset:17408\n\t" \
    "v_mfma_f32_16x16x32_bf16 v[8:11], v[104:107], v[32:35], v[8:11]\n\t" \
    "s_waitcnt lgkmcnt(4)\n\t" \
    "v_mfma_f32_16x16x32_bf16 v[12:15], v[112:115], v[32:35], v[12:15]\n\t" \
    "buffer_load_dwordx4 %[l16], %[rs], %[a] offen offset:1024 sc1 lds\n\t" \
    "v_mfma_f32_16x16x32_bf16 v[0:3], v[116:119], v[36:39], v[0:3]\n\t" \
    "ds_read_b128 v[28:31], %[xp] offset:18432\n\t" \
    "v_mfma_f32_16x16x32_bf16 v[4:7], v[124:127], v[36:39], v[4:7]\n\t" \
    "ds_read_b128 v[32:35], %[xp] offset:19456\n\t" \
    "v_mfma_f32_16x16x32_bf16 v[8:11], v[116:119], v[40:43], v[8:11]\n\t" \
    "s_waitcnt lgkmcnt(4)\n\t" \
    "v_mfma_f32_16x16x32_bf16 v[12:15], v[124:127], v[40:43], v[12:15]\n\t" \
    "buffer_load_dwordx4 %[l16], %[rs], %[a] offen offset:2048 sc1 lds\n\t" \
    "v_mfma_f32_16x16x32_bf16 v[0:3], v[120:123], v[44:47], v[0:3]\n\t" \
    "ds_read_b128 v[36:39], %[xp] offset:20480\n\t" \
    "v_mfma_f32_16x16x32_bf16 v[4:7], v[128:131], v[44:47], v[4:7]\n\t" \
    "ds_read_b128 v[40:43], %[xp] offset:21504\n\t" \
    "v_mfma_f32_16x16x32_bf16 v[8:11], v[120:123], v[48:51], v[8:11]\n\t" \
    "s_waitcnt lgkmcnt(4)\n\t" \
    "v_mfma_f32_16x16x32_bf16 v[12:15], v[128:131], v[48:51], v[12:15]\n\t" \
    "buffer_load_dwordx4 %[l16], %[rs], %[a] offen offset:3072 sc1 lds\n\t" \
    "s_waitcnt vmcnt(4)\n\t" \
    "s_barrier\n\t" \
    "s_setprio 1\n\t" \
    "s_add_u32 m0, %[sd], 0\n\t" \
    "s_add_u32 %[a], %[sx], 524288\n\t" \
    "s_add_u32 %[b], %[oo], 0x1000\n\t" \
    "s_cmp_lg_u32 %[t], 0\n\t" \
    "s_cselect_b64 %[em], -1, 0\n\t" \
    "v_mfma_f32_16x16x32_bf16 v[0:3], v[132:135], v[20:23], v[0:3]\n\t" \
    "ds_read_b128 v[44:47], %[xp] offset:22528\n\t" \
    "v_mfma_f32_16x16x32_bf16 v[4:7], v[140:143], v[20:23], v[4:7]\n\t" \
    "ds_read_b128 v[48:51], %[xp] offset:23552\n\t" \
    "v_mfma_f32_16x16x32_bf16 v[8:11], v[132:135], v[24:27], v[8:11]\n\t" \
    "ds_read_b128 v[52:55], %[pa] offset:0\n\t" \
    "s_waitcnt lgkmcnt(5)\n\t" \
    "v_mfma_f32_16x16x32_bf16 v[12:15], v[140:143], v[24:27], v[12:15]\n\t" \
    "buffer_load_dwordx4 %[l16], %[rs], %[a] offen offset:0 sc1 lds\n\t" \
    "v_mfma_f32_16x16x32_bf16 v[0:3], v[136:139], v[28:31], v[0:3]\n\t" \
    "ds_read_b128 v[20:23], %[xp] offset:24576\n\t" \
    "v_mfma_f32_16x16x32_bf16 v[4:7], v[144:147], v[28:31], v[4:7]\n\t" \
    "ds_read_b128 v[24:27], %[xp] offset:25600\n\t" \
    "v_mfma_f32_16x16x32_bf16 v[8:11], v[136:139], v[32:35], v[8:11]\n\t" \
    "ds_read_b128 v[56:59], %[pa] offset:1024\n\t" \
    "s_waitcnt lgkmcnt(6)\n\t" \
    "v_mfma_f32_16x16x32_bf16 v[12:15], v[144:147], v[32:35], v[12:15]\n\t" \
    "buffer_load_dwordx4 %[l16], %[rs], %[a] offen offset:1024 sc1 lds\n\t" \
    "v_mfma_f32_16x16x32_bf16 v[0:3], v[148:151], v[36:39], v[0:3]\n\t" \
    "ds_read_b128 v[28:31], %[xp] offset:26624\n\t" \
    "v_mfma_f32_16x16x32_bf16 v[4:7], v[156:159], v[36:39], v[4:7]\n\t" \
    "ds_read_b128 v[32:35], %[xp] offset:27648\n\t" \
    "v_mfma_f32_16x16x32_bf16 v[8:11], v[148:151], v[40:43], v[8:11]\n\t" \
    "ds_read_b128 v[60:63], %[pa] offset:2048\n\t" \
    "s_waitcnt lgkmcnt(7)\n\t" \
    "v_mfma_f32_16x16x32_bf16 v[12:15], v[156:159], v[40:43], v[12:15]\n\t" \
    "buffer_load_dwordx4 %[l16], %[rs], %[a] offen offset:2048 sc1 lds\n\t" \
    "v_mfma_f32_16x16x32_bf16 v[0:3], v[152:155], v[44:47], v[0:3]\n\t" \
    "ds_read_b128 v[36:39], %[xp] offset:28672\n\t" \
    "s_waitcnt lgkmcnt(7)\n\t" \
    "v_add_f32 v196, v196, v52\n\t" \
    "v_mfma_f32_16x16x32_bf16 v[4:7], v[160:163], v[44:47], v[4:7]\n\t" \
    "ds_read_b128 v[40:43], %[xp] offset:29696\n\t" \
    "v_add_f32 v197, v197, v53\n\t" \
    "v_mfma_f32_16x16x32_bf16 v[8:11], v[152:155], v[48:51], v[8:11]\n\t" \
    "ds_read_b128 v[64:67], %[pa] offset:3072\n\t" \
    "s_waitcnt lgkmcnt(7)\n\t" \
    "v_mfma_f32_16x16x32_bf16 v[12:15], v[160:163], v[48:51], v[12:15]\n\t" \
    "buffer_load_dwordx4 %[l16], %[rs], %[a] offen offset:3072 sc1 lds\n\t" \
    "v_add_f32 v198, v198, v54\n\t" \
    "v_add_f32 v199, v199, v55\n\t" \
    "v_mfma_f32_16x16x32_bf16 v[0:3], v[164:167], v[20:23], v[0:3]\n\t" \
    "ds_read_b128 v[44:47], %[xp] offset:30720\n\t" \
    "s_waitcnt lgkmcnt(7)\n\t" \
    "v_add_f32 v200, v200, v56\n\t" \
    "v_mfma_f32_16x16x32_bf16 v[4:7], v[172:175], v[20:23], v[4:7]\n\t" \
    "ds_read_b128 v[48:51], %[xp] offset:31744\n\t" \
    "v_add_f32 v201, v201, v57\n\t" \
    "v_mfma_f32_16x16x32_bf16 v[8:11], v[164:167], v[24:27], v[8:11]\n\t" \
    "v_add_f32 v202, v202, v58\n\t" \
    "s_waitcnt lgkmcnt(6)\n\t" \
    "v_mfma_f32_16x16x32_bf16 v[12:15], v[172:175], v[24:27], v[12:15]\n\t" \
    "v_add_f32 v203, v203, v59\n\t" \
    "s_waitcnt lgkmcnt(5)\n\t" \
    "v_add_f32 v204, v204, v60\n\t" \
    "v_add_f32 v205, v205, v61\n\t" \
    "v_mfma_f32_16x16x32_bf16 v[0:3], v[168:171], v[28:31], v[0:3]\n\t" \
    "ds_read_b128 v[20:23], %[xp] offset:32768\n\t" \
    "v_add_f32 v206, v206, v62\n\t" \
    "v_mfma_f32_16x16x32_bf16 v[4:7], v[176:179], v[28:31], v[4:7]\n\t" \
    "ds_read_b128 v[24:27], %[xp] offset:33792\n\t" \
    "v_add_f32 v207, v207, v63\n\t" \
    "v_mfma_f32_16x16x32_bf16 v[8:11], v[168:171], v[32:35], v[8:11]\n\t" \
    "s_waitcnt lgkmcnt(4)\n\t" \
    "v_add_f32 v208, v208, v64\n\t" \
    "v_mfma_f32_16x16x32_bf16 v[12:15], v[176:179], v[32:35], v[12:15]\n\t" \
    "v_add_f32 v209, v209, v65\n\t" \
    "v_add_f32 v210, v210, v66\n\t" \
    "v_add_f32 v211, v211, v67\n\t" \
    "v_mfma_f32_16x16x32_bf16 v[0:3], v[180:183], v[36:39], v[0:3]\n\t" \
    "ds_read_b128 v[28:31], %[xp] offset:34816\n\t" \
    "v_mfma_f32_16x16x32_bf16 v[4:7], v[188:191], v[36:39], v[4:7]\n\t" \
    "ds_read_b128 v[32:35], %[xp] offset:35840\n\t" \
    "s_mov_b64 exec, %[em]\n\t" \
    "buffer_store_dwordx4 v[196:199], %[l16], %[rs], %[oo] offen offset:0 sc1\n\t" \
    "s_mov_b64 exec, -1\n\t" \
    "v_mfma_f32_16x16x32_bf16 v[8:11], v[180:183], v[40:43], v[8:11]\n\t" \
    "s_waitcnt lgkmcnt(4)\n\t" \
    "v_mfma_f32_16x16x32_bf16 v[12:15], v[188:191], v[40:43], v[12:15]\n\t" \
    "s_mov_b64 exec, %[em]\n\t" \
    "buffer_store_dwordx4 v[200:203], %[l16], %[rs], %[oo] offen offset:1024 sc1\n\t" \
    "s_mov_b64 exec, -1\n\t" \
    "v_mfma_f32_16x16x32_bf16 v[0:3], v[184:187], v[44:47], v[0:3]\n\t" \
    "ds_read_b128 v[36:39], %[xp] offset:36864\n\t" \
    "v_mfma_f32_16x16x32_bf16 v[4:7], v[192:195], v[44:47], v[4:7]\n\t" \
    "ds_read_b128 v[40:43], %[xp] offset:37888\n\t" \
    "s_mov_b64 exec, %[em]\n\t" \
    "buffer_store_dwordx4 v[204:207], %[l16], %[rs], %[oo] offen offset:2048 sc1\n\t" \
    "s_mov_b64 exec, -1\n\t" \
    "v_mfma_f32_16x16x32_bf16 v[8:11], v[184:187], v[48:51], v[8:11]\n\t" \
    "s_waitcnt lgkmcnt(4)\n\t" \
    "v_mfma_f32_16x16x32_bf16 v[12:15], v[192:195], v[48:51], v[12:15]\n\t" \
    "s_mov_b64 exec, %[em]\n\t" \
    "buffer_store_dwordx4 v[208:211], %[l16], %[rs], %[oo] offen offset:3072 sc1\n\t" \
    "s_mov_b64 exec, -1\n\t" \
    "s_add_u32 %[sx], %[sx], 262144\n\t" \
    "s_add_u32 %[t], %[t], 1\n\t" \
    "s_add_u32 %[oo], %[oo], 524288\n\t" \
    "s_waitcnt vmcnt(8)\n\t" \
    "s_cmp_gt_u32 %[t], " STR(SQUICK) " + 1\n\t" \
    "s_cbranch_scc1 B0_%=\n\t" \
    "s_waitcnt vmcnt(0)\n\t" \
    "B0_%=:\n\t" \
    "s_barrier\n\t" \
    "s_setprio 1\n\t" \
    "s_add_u32 m0, %[sd], 16384\n\t" \
    "s_add_u32 %[a], %[sx], 278528\n\t" \
    "v_mfma_f32_16x16x32_bf16 v[196:199], v[68:71], v[20:23], 0\n\t" \
    "ds_read_b128 v[44:47], %[xp] offset:38912\n\t" \
    "v_mfma_f32_16x16x32_bf16 v[200:203], v[76:79], v[20:23], 0\n\t" \
    "ds_read_b128 v[48:51], %[xp] offset:39936\n\t" \
    "v_mfma_f32_16x16x32_bf16 v[204:207], v[68:71], v[24:27], 0\n\t" \
    "s_waitcnt lgkmcnt(4)\n\t" \
    "v_mfma_f32_16x16x32_bf16 v[208:211], v[76:79], v[24:27], 0\n\t" \
    "v_mfma_f32_16x16x32_bf16 v[196:199], v[72:75], v[28:31], v[196:199]\n\t" \
    "ds_read_b128 v[20:23], %[xp] offset:40960\n\t" \
    "v_mfma_f32_16x16x32_bf16 v[200:203], v[80:83], v[28:31], v[200:203]\n\t" \
    "ds_read_b128 v[24:27], %[xp] offset:41984\n\t" \
    "v_mfma_f32_16x16x32_bf16 v[204:207], v[72:75], v[32:35], v[204:207]\n\t" \
    "s_waitcnt lgkmcnt(4)\n\t" \
    "v_mfma_f32_16x16x32_bf16 v[208:211], v[80:83], v[32:35], v[208:211]\n\t" \
    "v_mfma_f32_16x16x32_bf16 v[196:199], v[84:87], v[36:39], v[196:199]\n\t" \
    "ds_read_b128 v[28:31], %[xp] offset:43008\n\t" \
    "v_mfma_f32_16x16x32_bf16 v[200:203], v[92:95], v[36:39], v[200:203]\n\t" \
    "ds_read_b128 v[32:35], %[xp] offset:44032\n\t" \
    "v_mfma_f32_16x16x32_bf16 v[204:207], v[84:87], v[40:43], v[204:207]\n\t" \
    "s_waitcnt lgkmcnt(4)\n\t" \
    "v_mfma_f32_16x16x32_bf16 v[208:211], v[92:95], v[40:43], v[208:211]\n\t" \
    "v_mfma_f32_16x16x32_bf16 v[196:199], v[88:91], v[44:47], v[196:199]\n\t" \
    "ds_read_b128 v[36:39], %[xp] offset:45056\n\t" \
    "v_mfma_f32_16x16x32_bf16 v[200:203], v[96:99], v[44:47], v[200:203]\n\t" \
    "ds_read_b128 v[40:43], %[xp] offset:46080\n\t" \
    "v_mfma_f32_16x16x32_bf16 v[204:207], v[88:91], v[48:51], v[204:207]\n\t" \
    "s_waitcnt lgkmcnt(4)\n\t" \
    "v_mfma_f32_16x16x32_bf16 v[208:211], v[96:99], v[48:51], v[208:211]\n\t" \
    "v_mfma_f32_16x16x32_bf16 v[196:199], v[100:103], v[20:23], v[196:199]\n\t" \
    "ds_read_b128 v[44:47], %[xp] offset:47104\n\t" \
    "v_mfma_f32_16x16x32_bf16 v[200:203], v[108:111], v[20:23], v[200:203]\n\t" \
    "ds_read_b128 v[48:51], %[xp] offset:48128\n\t" \
    "v_mfma_f32_16x16x32_bf16 v[204:207], v[100:103], v[24:27], v[204:207]\n\t" \
    "s_waitcnt lgkmcnt(4)\n\t" \
    "v_mfma_f32_16x16x32_bf16 v[208:211], v[108:111], v[24:27], v[208:211]\n\t" \
    "buffer_load_dwordx4 %[l16], %[rs], %[a] offen offset:0 sc1 lds\n\t" \
    "v_mfma_f32_16x16x32_bf16 v[196:199], v[104:107], v[28:31], v[196:199]\n\t" \
    "ds_read_b128 v[20:23], %[xp] offset:49152\n\t" \
    "v_mfma_f32_16x16x32_bf16 v[200:203], v[112:115], v[28:31], v[200:203]\n\t" \
    "ds_read_b128 v[24:27], %[xp] offset:50176\n\t" \
    "v_mfma_f32_16x16x32_bf16 v[204:207], v[104:107], v[32:35], v[204:207]\n\t" \
    "s_waitcnt lgkmcnt(4)\n\t" \
    "v_mfma_f32_16x16x32_bf16 v[208:211], v[112:115], v[32:35], v[208:211]\n\t" \
    "buffer_load_dwordx4 %[l16], %[rs], %[a] offen offset:1024 sc1 lds\n\t" \
    "v_mfma_f32_16x16x32_bf16 v[196:199], v[116:119], v[36:39], v[196:199]\n\t" \
    "ds_read_b128 v[28:31], %[xp] offset:51200\n\t" \
    "v_mfma_f32_16x16x32_bf16 v[200:203], v[124:127], v[36:39], v[200:203]\n\t" \
    "ds_read_b128 v[32:35], %[xp] offset:52224\n\t" \
    "v_mfma_f32_16x16x32_bf16 v[204:207], v[116:119], v[40:43], v[204:207]\n\t" \
    "s_waitcnt lgkmcnt(4)\n\t" \
    "v_mfma_f32_16x16x32_bf16 v[208:211], v[124:127], v[40:43], v[208:211]\n\t" \
    "buffer_load_dwordx4 %[l16], %[rs], %[a] offen offset:2048 sc1 lds\n\t" \
    "v_mfma_f32_16x16x32_bf16 v[196:199], v[120:123], v[44:47], v[196:199]\n\t" \
    "ds_read_b128 v[36:39], %[xp] offset:53248\n\t" \
    "v_mfma_f32_16x16x32_bf16 v[200:203], v[128:131], v[44:47], v[200:203]\n\t" \
    "ds_read_b128 v[40:43], %[xp] offset:54272\n\t" \
    "v_mfma_f32_16x16x32_bf16 v[204:207], v[120:123], v[48:51], v[204:207]\n\t" \
    "s_waitcnt lgkmcnt(4)\n\t" \
    "v_mfma_f32_16x16x32_bf16 v[208:211], v[128:131], v[48:51], v[208:211]\n\t" \
    "buffer_load_dwordx4 %[l16], %[rs], %[a] offen offset:3072 sc1 lds\n\t" \
    "s_waitcnt vmcnt(4)\n\t" \
    "s_barrier\n\t" \
    "s_setprio 1\n\t" \
    "s_add_u32 m0, %[sd], 32768\n\t" \
    "s_add_u32 %[a], %[sx], 524288\n\t" \
    "s_add_u32 %[b], %[oo], 0x1000\n\t" \
    "s_cmp_lg_u32 %[t], 0\n\t" \
    "s_cselect_b64 %[em], -1, 0\n\t" \
    "v_mfma_f32_16x16x32_bf16 v[196:199], v[132:135], v[20:23], v[196:199]\n\t" \
    "ds_read_b128 v[44:47], %[xp] offset:55296\n\t" \
    "v_mfma_f32_16x16x32_bf16 v[200:203], v[140:143], v[20:23], v[200:203]\n\t" \
    "ds_read_b128 v[48:51], %[xp] offset:56320\n\t" \
    "v_mfma_f32_16x16x32_bf16 v[204:207], v[132:135], v[24:27], v[204:207]\n\t" \
    "ds_read_b128 v[52:55], %[pa] offset:0\n\t" \
    "s_waitcnt lgkmcnt(5)\n\t" \
    "v_mfma_f32_16x16x32_bf16 v[208:211], v[140:143], v[24:27], v[208:211]\n\t" \
    "buffer_load_dwordx4 %[l16], %[rs], %[a] offen offset:0 sc1 lds\n\t" \
    "v_mfma_f32_16x16x32_bf16 v[196:199], v[136:139], v[28:31], v[196:199]\n\t" \
    "ds_read_b128 v[20:23], %[xp] offset:57344\n\t" \
    "v_mfma_f32_16x16x32_bf16 v[200:203], v[144:147], v[28:31], v[200:203]\n\t" \
    "ds_read_b128 v[24:27], %[xp] offset:58368\n\t" \
    "v_mfma_f32_16x16x32_bf16 v[204:207], v[136:139], v[32:35], v[204:207]\n\t" \
    "ds_read_b128 v[56:59], %[pa] offset:1024\n\t" \
    "s_waitcnt lgkmcnt(6)\n\t" \
    "v_mfma_f32_16x16x32_bf16 v[208:211], v[144:147], v[32:35], v[208:211]\n\t" \
    "buffer_load_dwordx4 %[l16], %[rs], %[a] offen offset:1024 sc1 lds\n\t" \
    "v_mfma_f32_16x16x32_bf16 v[196:199], v[148:151], v[36:39], v[196:199]\n\t" \
    "ds_read_b128 v[28:31], %[xp] offset:59392\n\t" \
    "v_mfma_f32_16x16x32_bf16 v[200:203], v[156:159], v[36:39], v[200:203]\n\t" \
    "ds_read_b128 v[32:35], %[xp] offset:60416\n\t" \
    "v_mfma_f32_16x16x32_bf16 v[204:207], v[148:151], v[40:43], v[204:207]\n\t" \
    "ds_read_b128 v[60:63], %[pa] offset:2048\n\t" \
    "s_waitcnt lgkmcnt(7)\n\t" \
    "v_mfma_f32_16x16x32_bf16 v[208:211], v[156:159], v[40:43], v[208:211]\n\t" \
    "buffer_load_dwordx4 %[l16], %[rs], %[a] offen offset:2048 sc1 lds\n\t" \
    "v_mfma_f32_16x16x32_bf16 v[196:199], v[152:155], v[44:47], v[196:199]\n\t" \
    "ds_read_b128 v[36:39], %[xp] offset:61440\n\t" \
    "s_waitcnt lgkmcnt(7)\n\t" \
    "v_add_f32 v0, v0, v52\n\t" \
    "v_mfma_f32_16x16x32_bf16 v[200:203], v[160:163], v[44:47], v[200:203]\n\t" \
    "ds_read_b128 v[40:43], %[xp] offset:62464\n\t" \
    "v_add_f32 v1, v1, v53\n\t" \
    "v_mfma_f32_16x16x32_bf16 v[204:207], v[152:155], v[48:51], v[204:207]\n\t" \
    "ds_read_b128 v[64:67], %[pa] offset:3072\n\t" \
    "s_waitcnt lgkmcnt(7)\n\t" \
    "v_mfma_f32_16x16x32_bf16 v[208:211], v[160:163], v[48:51], v[208:211]\n\t" \
    "buffer_load_dwordx4 %[l16], %[rs], %[a] offen offset:3072 sc1 lds\n\t" \
    "v_add_f32 v2, v2, v54\n\t" \
    "v_add_f32 v3, v3, v55\n\t" \
    "v_mfma_f32_16x16x32_bf16 v[196:199], v[164:167], v[20:23], v[196:199]\n\t" \
    "ds_read_b128 v[44:47], %[xp] offset:63488\n\t" \
    "s_waitcnt lgkmcnt(7)\n\t" \
    "v_add_f32 v4, v4, v56\n\t" \
    "v_mfma_f32_16x16x32_bf16 v[200:203], v[172:175], v[20:23], v[200:203]\n\t" \
    "ds_read_b128 v[48:51], %[xp] offset:64512\n\t" \
    "v_add_f32 v5, v5, v57\n\t" \
    "v_mfma_f32_16x16x32_bf16 v[204:207], v[164:167], v[24:27], v[204:207]\n\t" \
    "v_add_f32 v6, v6, v58\n\t" \
    "s_waitcnt lgkmcnt(6)\n\t" \
    "v_mfma_f32_16x16x32_bf16 v[208:211], v[172:175], v[24:27], v[208:211]\n\t" \
    "v_add_f32 v7, v7, v59\n\t" \
    "s_waitcnt lgkmcnt(5)\n\t" \
    "v_add_f32 v8, v8, v60\n\t" \
    "v_add_f32 v9, v9, v61\n\t" \
    "v_mfma_f32_16x16x32_bf16 v[196:199], v[168:171], v[28:31], v[196:199]\n\t" \
    "ds_read_b128 v[20:23], %[xp] offset:0\n\t" \
    "v_add_f32 v10, v10, v62\n\t" \
    "v_mfma_f32_16x16x32_bf16 v[200:203], v[176:179], v[28:31], v[200:203]\n\t" \
    "ds_read_b128 v[24:27], %[xp] offset:1024\n\t" \
    "v_add_f32 v11, v11, v63\n\t" \
    "v_mfma_f32_16x16x32_bf16 v[204:207], v[168:171], v[32:35], v[204:207]\n\t" \
    "s_waitcnt lgkmcnt(4)\n\t" \
    "v_add_f32 v12, v12, v64\n\t" \
    "v_mfma_f32_16x16x32_bf16 v[208:211], v[176:179], v[32:35], v[208:211]\n\t" \
    "v_add_f32 v13, v13, v65\n\t" \
    "v_add_f32 v14, v14, v66\n\t" \
    "v_add_f32 v15, v15, v67\n\t" \
    "v_mfma_f32_16x16x32_bf16 v[196:199], v[180:183], v[36:39], v[196:199]\n\t" \
    "ds_read_b128 v[28:31], %[xp] offset:2048\n\t" \
    "v_mfma_f32_16x16x32_bf16 v[200:203], v[188:191], v[36:39], v[200:203]\n\t" \
    "ds_read_b128 v[32:35], %[xp] offset:3072\n\t" \
    "s_mov_b64 exec, %[em]\n\t" \
    "buffer_store_dwordx4 v[0:3], %[l16], %[rs], %[oo] offen offset:0 sc1\n\t" \
    "s_mov_b64 exec, -1\n\t" \
    "v_mfma_f32_16x16x32_bf16 v[204:207], v[180:183], v[40:43], v[204:207]\n\t" \
    "s_waitcnt lgkmcnt(4)\n\t" \
    "v_mfma_f32_16x16x32_bf16 v[208:211], v[188:191], v[40:43], v[208:211]\n\t" \
    "s_mov_b64 exec, %[em]\n\t" \
    "buffer_store_dwordx4 v[4:7], %[l16], %[rs], %[oo] offen offset:1024 sc1\n\t" \
    "s_mov_b64 exec, -1\n\t" \
    "v_mfma_f32_16x16x32_bf16 v[196:199], v[184:187], v[44:47], v[196:199]\n\t" \
    "ds_read_b128 v[36:39], %[xp] offset:4096\n\t" \
    "v_mfma_f32_16x16x32_bf16 v[200:203], v[192:195], v[44:47], v[200:203]\n\t" \
    "ds_read_b128 v[40:43], %[xp] offset:5120\n\t" \
    "s_mov_b64 exec, %[em]\n\t" \
    "buffer_store_dwordx4 v[8:11], %[l16], %[rs], %[oo] offen offset:2048 sc1\n\t" \
    "s_mov_b64 exec, -1\n\t" \
    "v_mfma_f32_16x16x32_bf16 v[204:207], v[184:187], v[48:51], v[204:207]\n\t" \
    "s_waitcnt lgkmcnt(4)\n\t" \
    "v_mfma_f32_16x16x32_bf16 v[208:211], v[192:195], v[48:51], v[208:211]\n\t" \
    "s_mov_b64 exec, %[em]\n\t" \
    "buffer_store_dwordx4 v[12:15], %[l16], %[rs], %[oo] offen offset:3072 sc1\n\t" \
    "s_mov_b64 exec, -1\n\t" \
    "s_add_u32 %[sx], %[sx], 262144\n\t" \
    "s_add_u32 %[t], %[t], 1\n\t" \
    "s_add_u32 %[oo], %[oo], 524288\n\t" \
    "s_waitcnt vmcnt(8)\n\t" \
    "s_cmp_gt_u32 %[t], " STR(SQUICK) " + 1\n\t" \
    "s_cbranch_scc1 B1_%=\n\t" \
    "s_waitcnt vmcnt(0)\n\t" \
    "B1_%=:\n\t" \
    "s_barrier\n\t" \
    "s_cmp_lt_u32 %[t], %[te]\n\t" \
    "s_cbranch_scc1 L1_%=\n\t" \
    "s_cmp_lg_u32 %[fin], 0\n\t" \
    "s_cbranch_scc0 E_%=\n\t" \
    "s_nop 4\n\t" \
    "s_barrier\n\t" \
    "ds_read_b128 v[52:55], %[pa] offset:0\n\t" \
    "ds_read_b128 v[56:59], %[pa] offset:1024\n\t" \
    "ds_read_b128 v[60:63], %[pa] offset:2048\n\t" \
    "ds_read_b128 v[64:67], %[pa] offset:3072\n\t" \
    "s_add_u32 %[b], %[oo], 0x1000\n\t" \
    "s_waitcnt lgkmcnt(0)\n\t" \
    "v_add_f32 v196, v196, v52\n\t" \
    "v_add_f32 v197, v197, v53\n\t" \
    "v_add_f32 v198, v198, v54\n\t" \
    "v_add_f32 v199, v199, v55\n\t" \
    "v_add_f32 v200, v200, v56\n\t" \
    "v_add_f32 v201, v201, v57\n\t" \
    "v_add_f32 v202, v202, v58\n\t" \
    "v_add_f32 v203, v203, v59\n\t" \
    "v_add_f32 v204, v204, v60\n\t" \
    "v_add_f32 v205, v205, v61\n\t" \
    "v_add_f32 v206, v206, v62\n\t" \
    "v_add_f32 v207, v207, v63\n\t" \
    "v_add_f32 v208, v208, v64\n\t" \
    "v_add_f32 v209, v209, v65\n\t" \
    "v_add_f32 v210, v210, v66\n\t" \
    "v_add_f32 v211, v211, v67\n\t" \
    "buffer_store_dwordx4 v[196:199], %[l16], %[rs], %[oo] offen offset:0 sc1\n\t" \
    "buffer_store_dwordx4 v[200:203], %[l16], %[rs], %[oo] offen offset:1024 sc1\n\t" \
    "buffer_store_dwordx4 v[204:207], %[l16], %[rs], %[oo] offen offset:2048 sc1\n\t" \
    "buffer_store_dwordx4 v[208:211], %[l16], %[rs], %[oo] offen offset:3072 sc1\n\t" \
    "E_%=:\n\t" \
    "s_setprio 0\n\t" \
    "s_waitcnt lgkmcnt(0)\n\t" \
    "s_nop 7\n\t" \
    : [t] "+s"(t), [sx] "+s"(sx), [oo] "+s"(oo), [a] "=&s"(tmp), [b] "=&s"(tmp2), [em] "=&s"(em), "+{v[20:27]}"(xf[0]), "+{v[28:35]}"(xf[1]), "+{v[36:43]}"(xf[2]), "+{v[196:203]}"(ac[0]), "+{v[204:211]}"(ac[1]) \
    : [te] "s"(TE_), [fin] "s"(FIN_), [kh] "s"(kh), [sd] "s"(sd), [rs] "s"(rs), [l16] "v"(l16), [xp] "v"(xp), [pa] "v"(pa), [fm] "s"(fm), [pm] "s"(pm), [fo] "s"(fo), [po] "s"(po), [pd] "s"(pd), [tag] "s"(P.tag), "{v[68:75]}"(w[0][0]), "{v[76:83]}"(w[0][1]), "{v[84:91]}"(w[1][0]), "{v[92:99]}"(w[1][1]), "{v[100:107]}"(w[2][0]), "{v[108:115]}"(w[2][1]), "{v[116:123]}"(w[3][0]), "{v[124:131]}"(w[3][1]), "{v[132:139]}"(w[4][0]), "{v[140:147]}"(w[4][1]), "{v[148:155]}"(w[5][0]), "{v[156:163]}"(w[5][1]), "{v[164:171]}"(w[6][0]), "{v[172:179]}"(w[6][1]), "{v[180:187]}"(w[7][0]), "{v[188:195]}"(w[7][1]) \
    : "memory", "scc", "v0", "v1", "v2", "v3", "v4", "v5", "v6", "v7", "v8", "v9", "v10", "v11", "v12", "v13", "v14", "v15", "v44", "v45", "v46", "v47", "v48", "v49", "v50", "v51", "v52", "v53", "v54", "v55", "v56", "v57", "v58", "v59", "v60", "v61", "v62", "v63", "v64", "v65", "v66", "v67")
#define AQ_PIN0() asm volatile("" : "+{v[28:35]}"(wr[0][0]), "+{v[36:43]}"(wr[0][1]))
#define AQ_PIN1() asm volatile("" : "+{v[44:51]}"(wr[1][0]), "+{v[52:59]}"(wr[1][1]))
#define AQ_PIN2() asm volatile("" : "+{v[60:67]}"(wr[2][0]), "+{v[68:75]}"(wr[2][1]))
#define AQ_PIN3() asm volatile("" : "+{v[76:83]}"(wr[3][0]), "+{v[84:91]}"(wr[3][1]))
#define AQ_PIN4() asm volatile("" : "+{v[92:99]}"(wr[4][0]), "+{v[100:107]}"(wr[4][1]))
#define AQ_PIN5() asm volatile("" : "+{v[108:115]}"(wr[5][0]), "+{v[116:123]}"(wr[5][1]))
#define AQ_PIN6() asm volatile("" : "+{v[124:131]}"(wr[6][0]), "+{v[132:139]}"(wr[6][1]))
#define AQ_PIN7() asm volatile("" : "+{v[140:147]}"(wr[7][0]), "+{v[148:155]}"(wr[7][1]))
#define AQ_PIN8() asm volatile("" : "+{v[156:163]}"(wr[8][0]), "+{v[164:171]}"(wr[8][1]))
#define AQ_PIN9() asm volatile("" : "+{v[172:179]}"(wr[9][0]), "+{v[180:187]}"(wr[9][1]))
#define AQ_PIN10() asm volatile("" : "+{v[188:195]}"(wr[10][0]), "+{v[196:203]}"(wr[10][1]))
#define AQ_PIN11() asm volatile("" : "+{v[204:211]}"(wr[11][0]), "+{v[212:219]}"(wr[11][1]))
#define AK_PIN0() asm volatile("" : "+{v[20:27]}"(wr[0][0]), "+{v[28:35]}"(wr[0][1]))
#define AK_PIN1() asm volatile("" : "+{v[36:43]}"(wr[1][0]), "+{v[44:51]}"(wr[1][1]))
#define AK_PIN2() asm volatile("" : "+{v[52:59]}"(wr[2][0]), "+{v[60:67]}"(wr[2][1]))
#define AK_PIN3() asm volatile("" : "+{v[68:75]}"(wr[3][0]), "+{v[76:83]}"(wr[3][1]))
#define AK_PIN4() asm volatile("" : "+{v[84:91]}"(wr[4][0]), "+{v[92:99]}"(wr[4][1]))
#define AK_PIN5() asm volatile("" : "+{v[100:107]}"(wr[5][0]), "+{v[108:115]}"(wr[5][1]))
#define AK_PIN6() asm volatile("" : "+{v[116:123]}"(wr[6][0]), "+{v[124:131]}"(wr[6][1]))
#define AK_PIN7() asm volatile("" : "+{v[132:139]}"(wr[7][0]), "+{v[140:147]}"(wr[7][1]))
#define AK_PIN8() asm volatile("" : "+{v[148:155]}"(wr[8][0]), "+{v[156:163]}"(wr[8][1]))
#define AK_PIN9() asm volatile("" : "+{v[164:171]}"(wr[9][0]), "+{v[172:179]}"(wr[9][1]))
#define AK_PIN10() asm volatile("" : "+{v[180:187]}"(wr[10][0]), "+{v[188:195]}"(wr[10][1]))
#define AK_PIN11() asm volatile("" : "+{v[196:203]}"(wr[11][0]), "+{v[204:211]}"(wr[11][1]))
#define V_PIN() asm volatile("" : "+{v[28:35]}"(wv[0]), "+{v[36:43]}"(wv[1]), "+{v[44:51]}"(wv[2]), "+{v[52:59]}"(wv[3]), "+{v[60:67]}"(wv[4]), "+{v[68:75]}"(wv[5]), "+{v[76:83]}"(wv[6]), "+{v[84:91]}"(wv[7]), "+{v[92:99]}"(wv[8]), "+{v[100:107]}"(wv[9]), "+{v[108:115]}"(wv[10]), "+{v[116:123]}"(wv[11]), "+{v[124:131]}"(wv[12]), "+{v[132:139]}"(wv[13]), "+{v[140:147]}"(wv[14]), "+{v[148:155]}"(wv[15]))
#define WOV_PIN0() asm volatile("" : "+{v[156:163]}"(wo[0][0][0]), "+{v[164:171]}"(wo[0][0][1]), "+{v[172:179]}"(wo[0][1][0]), "+{v[180:187]}"(wo[0][1][1]))
#define WOV_PIN1() asm volatile("" : "+{v[188:195]}"(wo[1][0][0]), "+{v[196:203]}"(wo[1][0][1]), "+{v[204:211]}"(wo[1][1][0]), "+{v[212:219]}"(wo[1][1][1]))
#define WOW_PIN0() asm volatile("" : "+{v[28:35]}"(wo[0][0][0]), "+{v[36:43]}"(wo[0][0][1]), "+{v[44:51]}"(wo[0][1][0]), "+{v[52:59]}"(wo[0][1][1]))
#define WOW_PIN1() asm volatile("" : "+{v[60:67]}"(wo[1][0][0]), "+{v[68:75]}"(wo[1][0][1]), "+{v[76:83]}"(wo[1][1][0]), "+{v[84:91]}"(wo[1][1][1]))
#define WOW_PIN2() asm volatile("" : "+{v[92:99]}"(wo[2][0][0]), "+{v[100:107]}"(wo[2][0][1]), "+{v[108:115]}"(wo[2][1][0]), "+{v[116:123]}"(wo[2][1][1]))
#define WOW_PIN3() asm volatile("" : "+{v[124:131]}"(wo[3][0][0]), "+{v[132:139]}"(wo[3][0][1]), "+{v[140:147]}"(wo[3][1][0]), "+{v[148:155]}"(wo[3][1][1]))
#define WOW_PIN4() asm volatile("" : "+{v[156:163]}"(wo[4][0][0]), "+{v[164:171]}"(wo[4][0][1]), "+{v[172:179]}"(wo[4][1][0]), "+{v[180:187]}"(wo[4][1][1]))
#define WOW_PIN5() asm volatile("" : "+{v[188:195]}"(wo[5][0][0]), "+{v[196:203]}"(wo[5][0][1]), "+{v[204:211]}"(wo[5][1][0]), "+{v[212:219]}"(wo[5][1][1]))
#define A_PIN(QW, i) do { switch (i) { case 0: if (QW) AQ_PIN0(); else AK_PIN0(); break; case 1: if (QW) AQ_PIN1(); else AK_PIN1(); break; case 2: if (QW) AQ_PIN2(); else AK_PIN2(); break; case 3: if (QW) AQ_PIN3(); else AK_PIN3(); break; case 4: if (QW) AQ_PIN4(); else AK_PIN4(); break; case 5: if (QW) AQ_PIN5(); else AK_PIN5(); break; case 6: if (QW) AQ_PIN6(); else AK_PIN6(); break; case 7: if (QW) AQ_PIN7(); else AK_PIN7(); break; case 8: if (QW) AQ_PIN8(); else AK_PIN8(); break; case 9: if (QW) AQ_PIN9(); else AK_PIN9(); break; case 10: if (QW) AQ_PIN10(); else AK_PIN10(); break; case 11: if (QW) AQ_PIN11(); else AK_PIN11(); break; } } while (0)
#define WO_PIN(VW, i) do { switch (i) { case 0: if (VW) WOV_PIN0(); else WOW_PIN0(); break; case 1: if (VW) WOV_PIN1(); else WOW_PIN1(); break; case 2: if (!VW) WOW_PIN2(); break; case 3: if (!VW) WOW_PIN3(); break; case 4: if (!VW) WOW_PIN4(); break; case 5: if (!VW) WOW_PIN5(); break; } } while (0)

__device__ __forceinline__ u32x4 ldsv(LAS unsigned char* p) { return *(volatile LAS u32x4*)p; }
#define DMA_A dman
#define DMA_V dman
#define DMA_O dmaw
#define TOUCH 0
#define TAUX 0
#define POISON4 ((u32x4){~0u, ~0u, ~0u, ~0u})
#define DLYM 0
#define DLYA 8
#define DLYK (DLYA + 8)
#define DLYO 8
#define FLA 0
#define FLB 0
#define CADM 1
#define CADO 1

#define RTC(id)
#define DCA 3
#define DCB 2
#define DL1 28
#define DL2 28
#define DLB 8
#define ADL 0
#define SN0 150
#define HELP 0
#define QT 0
__device__ __forceinline__ void track(int& est, int d, int up, int dn) { est += max(min(d - est, up), min(-dn, (d - est) >> 1)); }
__device__ __forceinline__ unsigned rfl(unsigned v) { unsigned r; asm volatile("s_nop 0\n\tv_readfirstlane_b32 %0, %1\n\ts_nop 4" : "=s"(r) : "v"(v)); return r; }
__device__ __forceinline__ int wmax(int v) {
  v = max(v, __builtin_amdgcn_update_dpp(0, v, 0x128, 0xf, 0xf, false));
  v = max(v, __builtin_amdgcn_update_dpp(0, v, 0x124, 0xf, 0xf, false));
  v = max(v, __builtin_amdgcn_update_dpp(0, v, 0x122, 0xf, 0xf, false));
  v = max(v, __builtin_amdgcn_update_dpp(0, v, 0x121, 0xf, 0xf, false));
  return max(max(__builtin_amdgcn_readlane(v, 0), __builtin_amdgcn_readlane(v, 16)), max(__builtin_amdgcn_readlane(v, 32), __builtin_amdgcn_readlane(v, 48)));
}
__device__ __forceinline__ void plan_wait(rsrc_t rs, unsigned fo, int s, int& lim, unsigned& tm, unsigned& d) {
  while ((d & 255u) != (unsigned)s && (lim -= DEC1) > 0) {
    __builtin_amdgcn_s_sleep(1);
    const u32x2 v = __builtin_amdgcn_raw_buffer_load_b64(rs, 0, fo, AUX_SC1);
    tm = rfl(v.x); d = rfl(v.y);
  }
  const unsigned now = (unsigned)__builtin_amdgcn_s_memrealtime() * 4u;
  tm = now + min((int)(tm - now), 2000);
  d = min(d >> 8, 1000u);
}
#define SPIN_BUDGET (1 << 21)

#define SLOT(i) ((i) * 16384)
#define LA_MEM SLOT(8)
#define LA_Q SLOT(9)
#define LA_K (SLOT(9) + 2048)
#define LA_P (SLOT(9) + 4096)
#define LA_VM (SLOT(9) + 6144)
#define LA_KX (SLOT(9) + 8192)

template <bool QW>
__device__ __forceinline__ void a_wave(LAS unsigned char* lds, Ptrs& P, int h, int wid) {
  char* ws = P.ws;
  const int lane = opq(0), tid = wid * 64 + lane;
  const int j = wid & 3, g = lane >> 4, q4 = (lane & 15) >> 2, p4 = lane & 3;
  const rsrc_t rs = WS_RSRC(P);
  const rsrc_t rq = __builtin_amdgcn_make_buffer_rsrc((void*)P.Wq, 0, HD * DM * 2, RSRC_W3);
  const rsrc_t rk = __builtin_amdgcn_make_buffer_rsrc((void*)P.Wk, 0, HD * DM * 2, RSRC_W3);
  const unsigned lbase = (unsigned)(size_t)lds;
  int lim = SPIN_BUDGET;
  u32x8 wr[12][2];
  const int trow = 16 * (g >> 1) + 4 * (g & 1) + 8 * (q4 >> 1) + (q4 & 1);
  const int tsw = ((q4 & 1) | ((g & 1) << 1) | ((q4 >> 1) << 2)) << 1;
  const int toff = trow * 256 + (((4 * j + (p4 >> 1)) ^ tsw) << 4) + 8 * (p4 & 1);
  {
    unsigned dv[2];
    #pragma unroll
    for (int x = 0; x < 2; ++x) {
      const int row = 8 * wid + 4 * x + (lane >> 4);
      const int sw = ((row & 1) | (((row >> 2) & 1) << 1) | (((row >> 3) & 1) << 2)) << 1;
      dv[x] = (unsigned)(row * (HD * 2) + h * 256 + (((lane & 15) ^ sw) << 4));
    }
#define A_GRP(n) do { _Pragma("unroll") for (int i = 0; i < 8; ++i) \
      DMA_A(((n) & 1) ? rk : rq, dv[i & 1], (4 * (((n) & 1) ? ((n) >> 1) ^ ((n) >> 2) : (((n) >> 1) + 1) & 3) + (i >> 1)) * 64 * HD * 2, lbase + SLOT(4 * ((n) & 1) + (i >> 1)) + wid * 2048 + (i & 1) * 1024); } while (0)
    A_GRP(0); A_GRP(1);
    #pragma unroll
    for (int n = 0; n < 6; ++n) {
      VMW(8);
      BARRIER();
      if ((n & 1) == (QW ? 0 : 1)) {
        #pragma unroll
        for (int b = 0; b < 4; ++b) {
          LAS unsigned char* t = lds + SLOT(QW ? 0 : 4) + ((b ^ j) << 14);
          wr[4 * (n >> 1) + b][0] = rd_frag<8192, 512>(t + toff);
          wr[4 * (n >> 1) + b][1] = rd_frag<8192, 512>(t + (toff ^ 32));
        }
        #pragma unroll
        for (int b = 0; b < 4; ++b) A_PIN(QW, 4 * (n >> 1) + b);
      }
      LBARRIER();
      A_GRP(n + 2);
    }
  }
  const unsigned cb = comm_base(ws);
  get_tag(P);
  VMWAIT();
  BARRIER();
  #pragma unroll
  for (int rd = 0; rd < 2; ++rd) {
    u32x8 f[2][2];
    #pragma unroll
    for (int i = 0; i < 2; ++i) {
      f[i][0] = rd_frag<8192, 512>(lds + SLOT((QW ? 0 : 4) + 2 * rd + i) + toff);
      f[i][1] = rd_frag<8192, 512>(lds + SLOT((QW ? 0 : 4) + 2 * rd + i) + (toff ^ 32));
    }
    asm volatile("" : "+v"(f[0][0]), "+v"(f[0][1]), "+v"(f[1][0]), "+v"(f[1][1]));
    LBARRIER();
    #pragma unroll
    for (int i = 0; i < 2; ++i)
      #pragma unroll
      for (int t = 0; t < 2; ++t) {
        LAS unsigned char* p = lds + SLOT((QW ? 0 : 4) + 2 * rd + i) + j * 4096 + t * 2048 + lane * 16;
        *(LAS u32x4*)p = (u32x4){f[i][t][0], f[i][t][1], f[i][t][2], f[i][t][3]};
        *(LAS u32x4*)(p + 1024) = (u32x4){f[i][t][4], f[i][t][5], f[i][t][6], f[i][t][7]};
      }
  }
  setup_wait(P, lane);
  const unsigned mask = mask_wait(P, lane);
  const int idx = (lane & 1) ? 0xEEEE : 0x4444;
  const unsigned ab_a = lbase + LA_MEM + lane * 16, am_a = ab_a + j * 1024;
  const unsigned t0_a = lbase + SLOT((QW ? 0 : 4) + j) + j * 4096 + lane * 16;
  const unsigned iw_a = lbase + (QW ? LA_Q : LA_K) + j * 512 + q4 * 128 + g * 32 + p4 * 4;
  const unsigned qr_a = lbase + LA_Q + g * 128 + (lane & 7) * 16;
  const unsigned kr_a = lbase + LA_K + g * 128 + (2 * q4 + (lane & 1)) * 16;
  const unsigned fr_a = lbase + LA_P + p4 * 4;
  if (tid < 8) *(LAS unsigned*)(lds + LA_P + tid * 4) = 0u;
  const unsigned vo_v = VB_AT(h) + j * 512 + lane * 4, md = lbase + LA_VM - VB_AT(h);
  const unsigned oo_v = OB_AT((lane >> 2) & 1, h) + (j * 16 + (g >> 1) * 8 + p4 * 2 + (g & 1)) * 16;
  const unsigned long long onej = 1ull << j;
  const unsigned lkx = lbase + LA_KX + j * 2048;
  u32x8 oxr = {};
  const unsigned xo_v = (lane >> 5) * 2048 + (lane & 31) * 16, vl_a = lbase + LA_VM + (j & 1) * 1024 + lane * 4;
  const int dq = 16 * (j >> 1) + (lane & 15) - 4 * g;
  const unsigned fb = step_base(cb, mask, 0);
  unsigned tgt = (unsigned)__builtin_amdgcn_s_memrealtime() * 4u;
  unsigned long long tp = 0;
  unsigned pt = 0, pd = 0;
  const unsigned long long fmask = __builtin_amdgcn_readfirstlane((16u >> wid) & 1u);
  int known = 0;
  __builtin_amdgcn_s_setprio(QW ? 2 : 1);
  LBARRIER();
  report(P, wid);
  TL(72);
  for (int s = 0; s < NSEG; ++s) {
    const unsigned sb = step_base(cb, mask, s);
    const unsigned sbm = sb - (lbase + LA_MEM);
    const unsigned fo = (unsigned)(s * NH + h) * 8192u + j * 2048 - (lbase + LA_MEM);
    GSDECL;
    RTC(h);
    lim = __builtin_amdgcn_readfirstlane(lim);
    if (QW) {
      if (wid == 3 && s > 0) {
        if (opq(0) == 0) __builtin_amdgcn_raw_buffer_store_b32((unsigned)tp * 4u - tgt, rs, 0, fb + FL_O(h), AUX_SC1);
        plan_wait(rs, fb + FL_T(s), s, lim, pt, pd);
        tgt = pt;
      }
      if (s >= known && s >= 1) {
        const u32x2 v = *(volatile LAS u32x2*)(lds + LA_P + 640 + (opq(0) & 3) * 8);
        if (!__any(v.y != P.tag)) known = rfl(min(min(__builtin_amdgcn_readlane(v.x, 0), __builtin_amdgcn_readlane(v.x, 1)), __builtin_amdgcn_readlane(v.x, 2)));
      }
      kv_wait(P, s, h, known, opq(0));
      const unsigned kxo = WS_KF + fo, vxo = WS_VF + fo;
      if (wid < 3) for (int i = 0; i < ADL; ++i) __builtin_amdgcn_s_sleep(1);
      dmas(rs, ab_a, kxo, lkx);
      dmas(rs, ab_a, kxo + 1024, lkx + 1024);
      dmas(rs, ab_a, WS_QF + fo, lbase + LA_P + 1024);
      dmas(rs, ab_a, WS_QF + fo + 1024, lbase + LA_P + 1024);
      if (wid == 2 && s + 1 >= known) {
        const int ln = opq(0);
        const unsigned fv = FLG_OFF + F_PRG(min((ln >> 1) & 3, 2), h) * 128 + (ln & 1) * 4;
        asm volatile("s_mov_b32 m0, %2\n\ts_nop 0\n\tbuffer_load_dword %0, %1, 0 offen sc1 lds" :: "v"(fv), "s"(rs), "s"(lbase + LA_P + 640) : "memory");
      }
      unsigned rg = 0x3c000000u;
      A_MAIN_Q();
      TL(s);
      GSFLUSH(0, 24 + wid); GSFLUSH(1, 4 + wid); GSFLUSH(2, 12 + j); GSFLUSH(3, 16 + j); GSFLUSH(4, 20 + j);
      if (s + PD < NSEG && !(opq(0) & 8)) __builtin_amdgcn_raw_buffer_store_b128(poison4(), rs, oo_v, step_base(cb, mask, s + PD), AUX_SC1);
    } else {
      const unsigned qxo = WS_QF + fo - (j & 1) * 2048, oxo = WS_OX + (unsigned)(max(s - 1, 0) * NH + h) * 8192u + (j & 1) * 4096 + (j >> 1) * 1024;
      A_MAIN_K();
      if (wid == 7) GSFLUSH(0, 28);
      GSFLUSH(1, 4 + wid);
    }
  }
  if (!QW) {
    const unsigned oxo = WS_OX + (unsigned)((NSEG - 1) * NH + h) * 8192u + (j & 1) * 4096 + (j >> 1) * 1024 + xo_v;
    st16(rs, oxo, (u32x4){oxr[0], oxr[1], oxr[2], oxr[3]});
    st16(rs, oxo + 512, (u32x4){oxr[4], oxr[5], oxr[6], oxr[7]});
  }
  VMWAIT();
  BARRIER();
  if (wid == 4) flag_set(P, F_PRG(4, h), NSEG, opq(0));
}

#define CALN 6
__device__ __forceinline__ void calib_role(LAS unsigned char* lds, const Ptrs& P, int reg, int wid) {
  const int lane = opq(0), tid = wid * 64 + lane;
  char* ws = P.ws;
  const rsrc_t rs = WS_RSRC(P);
  const unsigned cb = comm_base(ws);
  volatile LAS unsigned* res = (volatile LAS unsigned*)lds;
  unsigned best[2] = {~0u, ~0u};
  for (int it = 0; it < CALN; ++it) {
    const unsigned off = cb + (reg & 3) * CB_STEP + (30 + (wid & 1)) * 16384u + (it & 1) * 8192u + ((it >> 1) & 1) * 4096u + (wid >> 1) * 1024u + (reg >> 2) * 256u;
    const unsigned long long c0 = __builtin_amdgcn_s_memtime();
    asm volatile("s_waitcnt lgkmcnt(0)" ::: "memory");
    if (lane == 0) st4(rs, off, 0u);
    VMWAIT();
    const unsigned long long c1 = __builtin_amdgcn_s_memtime();
    const unsigned d = (unsigned)(c1 - c0);
    if (it >= 2) { if (it & 1) best[1] = min(best[1], d); else best[0] = min(best[0], d); }
  }
  if (lane == 0) { res[wid] = best[0]; res[8 + wid] = best[1]; }
  LBARRIER();
  if (tid == 0) {
    unsigned a = ~0u, b = ~0u;
    for (int i = 0; i < 8; ++i) { a = min(a, res[i]); b = min(b, res[8 + i]); }
    flag_set(P, F_CAL(reg), b < a ? 1u : 0u, 0);
  }
  LBARRIER();
}

#define LB_MEM 0
#define LB_RED 16384
template <bool VW>
__device__ __forceinline__ void b_wave(LAS unsigned char* lds, Ptrs& P, int r, int c, int wid, int bs) {
  constexpr int NHD = VW ? 2 : 6;
  char* ws = P.ws;
  const int lane = opq(0), tid = wid * 64 + lane;
  const int j = wid & 3, g = lane >> 4, q4 = (lane & 15) >> 2, p4 = lane & 3;
  const int b = r * 32 + c;
  const rsrc_t rs = WS_RSRC(P);
  const rsrc_t rv = __builtin_amdgcn_make_buffer_rsrc((void*)P.Wv, 0, HD * DM * 2, RSRC_W3);
  const rsrc_t ro = __builtin_amdgcn_make_buffer_rsrc((void*)P.Wo, 0, HD * DM * 2, RSRC_W3);
  const unsigned lbase = (unsigned)(size_t)lds;
  int lim = SPIN_BUDGET;
  const int h0 = ((VW ? 2 * j : 8 + 6 * j) + 5 * c) & 31;
  u32x8 wv[16], wo[6][2][2];
  {
    {
      unsigned tch[2];
      const int nl = c >= 20 ? 6 : 5;
      #pragma unroll
      for (int i = 0; i < 2; ++i) {
        const unsigned L = bs * 1024 + wid * 128 + i * 64 + lane, row = L / nl;
        tch[i] = __builtin_amdgcn_raw_buffer_load_b32(ro, row * (DM * 2) + (c - (bs >> 1)) * 64 + (L - row * nl) * 128, 0, TAUX);
      }
      asm volatile("" :: "v"(tch[0]), "v"(tch[1]));
      BARRIER();
    }
    const int pos = lane >> 2;
    const int prow = (pos & 9) | ((pos & 2) << 1) | ((pos & 4) >> 1);
    const unsigned dv0 = (unsigned)(prow * (DM * 2) + c * 64 + ((lane & 3) << 4));
    const unsigned dv1 = (unsigned)(prow * (DM * 2) + c * 64 + (((lane & 3) ^ 2) << 4));
    const int toff = wid * 16384 + (g * 16 + q4) * 64 + (((p4 >> 1) ^ ((g & 1) << 1)) << 4) + 8 * (p4 & 1);
    #pragma unroll
    for (int rd = 0; rd < NHD / 2; ++rd) {
      #pragma unroll
      for (int hh = 0; hh < 2; ++hh) {
        const int h = (h0 + 2 * rd + hh) & 31;
        #pragma unroll
        for (int i = 0; i < 8; ++i)
          DMA_O(ro, (i & 1) ? dv1 : dv0, (unsigned)(h * 128 + i * 16) * (DM * 2), lbase + wid * 16384 + hh * 8192 + i * 1024);
      }
      VMWAIT();
      #pragma unroll
      for (int hh = 0; hh < 2; ++hh)
        #pragma unroll
        for (int ks = 0; ks < 2; ++ks)
          #pragma unroll
          for (int t = 0; t < 2; ++t)
            wo[2 * rd + hh][ks][t] = rd_frag<4096, 256>(lds + (toff ^ (t * 32)) + hh * 8192 + ks * 512);
      #pragma unroll
      for (int hh = 0; hh < 2; ++hh)
        WO_PIN(VW, 2 * rd + hh);
      asm volatile("s_waitcnt lgkmcnt(0)" ::: "memory");
    }
    const int lr = lane >> 3;
    #pragma unroll
    for (int i = 0; i < 16; ++i) {
      const int sw = (((lr >> 2) & 1) | ((i & 1) << 1)) << 1;
      const unsigned dv = (unsigned)(lr * (HD * 2) + b * 128 + (((lane & 7) ^ sw) << 4));
      DMA_V(rv, dv, (unsigned)(wid * 128 + i * 8) * (HD * 2), lbase + wid * 16384 + i * 1024);
    }
    VMWAIT();
    BARRIER();
    if (VW) {
      const int trow = 16 * (g >> 1) + 4 * (g & 1) + 8 * (q4 >> 1) + (q4 & 1);
      const int tsw = ((g & 1) | ((q4 >> 1) << 1)) << 1;
      LAS unsigned char* tv = lds + trow * 128 + (((2 * j + (p4 >> 1)) ^ tsw) << 4) + 8 * (p4 & 1);
      #pragma unroll
      for (int ks = 0; ks < 16; ++ks) wv[ks] = rd_frag<4096, 256>(tv + ks * 8192);
      V_PIN();
    }
    LBARRIER();
  }
  const unsigned cb = comm_base(ws);
  get_tag(P);
  if ((b & 3) == 0) calib_role(lds, P, b >> 2, wid);
  setup_wait(P, lane);
  const unsigned mask = mask_wait(P, lane);
  const int idx = (lane & 1) ? 0xEEEE : 0x4444;
  const unsigned am_a = lbase + LB_MEM + lane * 16;
  const unsigned fb = step_base(cb, mask, 0);
  volatile LAS unsigned* tsl = (volatile LAS unsigned*)(lds + LB_RED + 4096);
  if (tid < 8) tsl[tid] = tid ? 0u : (unsigned)__builtin_amdgcn_s_memrealtime() * 4u;
  unsigned dn = 660;
  int da = 640, gb = 360;
  if (VW) __builtin_amdgcn_s_setprio(2);
  LBARRIER();
  report(P, wid);
  TL(72);
  for (int s = 0; s < NSEG; ++s) {
    const unsigned sb = step_base(cb, mask, s);
    const int ln = opq(lane), gg = ln >> 4;
    GSDECL;
    RTC(32 + b);
    const int l16 = ln * 16;
    const int xc = 32 * (c & 1) + 2 * (ln & 15);
    const unsigned mo_v = MF_AT(c >> 1) + ((xc >> 4) * 16 + (4 * r + gg) * 2 + ((xc >> 3) & 1)) * 16 + (xc & 7) * 2;
    if (!VW) {
      const unsigned tm = __builtin_amdgcn_readfirstlane(*tsl), tgt = tm + DLB, la = lbase + LB_MEM + j * 2048 + l16, mo = sb + j * 2048;
      B_POLL();
      if (wid == 7) {
        const int late = (int)((unsigned)__builtin_amdgcn_s_memrealtime() * 4u - tgt) - SN0;
        const unsigned to = tm + dn + (late > 80 ? late : 0);
        if (ln == 0) tsl[1] = to;
        if (b == 0) {
          dn = da + DL1;
          if (ln == 0) { *tsl = to + gb + DL2; st8(rs, fb + FL_T(s + 1), (u32x2){to + gb + DL2, (dn << 8) | (s + 1)}); }
          const unsigned va = ld4(rs, fb + FL_O(ln & 31)), vb = ld4(rs, fb + FL_B(ln));
          if (s > 0) {
            track(da, rfl(wmax(va)), DCA, DCB);
            track(gb, rfl(wmax(vb)), DCA, DCB);
          }
        }
      }
      if (wid == 7 && s > 0 && s + 2 < NSEG) __builtin_amdgcn_raw_buffer_store_b32(~0u, rs, mo_v, step_base(cb, mask, s + 2), AUX_SC1);
    } else {
      GS(3);
      const unsigned vo = VB_AT(b >> 1) + ((b & 1) * 4 + j) * 256;
      V_PROJ();
      GS(0); GSFLUSH(0, 4 + j);
      if (s + PD < NSEG && ln < 16) st16(rs, step_base(cb, mask, s + PD) + vo + l16, poison4());
    }
    GSFLUSH(3, 24 + wid);
    if (s + 1 == NSEG) break;
    unsigned so[6];
    #pragma unroll
    for (int i = 0; i < NHD; ++i) { const int h = (h0 + i) & 31; so[i] = __builtin_amdgcn_readfirstlane(sb + OB_AT(r, h)); }
    const unsigned sn = __builtin_amdgcn_readfirstlane(step_base(cb, mask, s + 1));
    const unsigned rw_a = lbase + LB_RED + (wid * 128 + (gg & 1) * 64 + (gg >> 1) * 16 + (ln & 15)) * 4;
    const unsigned rr_a = lbase + LB_RED + ln * 8;
    if (wid == 7) {
      if (b != 0) {
        __builtin_amdgcn_s_sleep(4);
        unsigned tm = 0;
        dn = 0;
        plan_wait(rs, fb + FL_T(s + 1), s + 1, lim, tm, dn);
        if (ln == 0) *tsl = tm;
      }
      const unsigned tgt = rfl(tsl[1]);
      T_WAIT();
    }
    lim = __builtin_amdgcn_readfirstlane(lim);
    unsigned long long t1;
    if (VW) WO_V(); else WO_W();
    if (wid == 0) {
      if (ln == 0) st4(rs, fb + FL_B(b), (unsigned)t1 * 4u - tsl[1]);
    }
    if (wid == 0) {
      GSFLUSH(2, 16);
      TL(s);
    }
    GSFLUSH(1, 8 + wid);
    if (wid == 0 || wid == 4) { GSFLUSH(4, 17 + (wid >> 2)); GSFLUSH(0, 19 + (wid >> 2)); }
  }
}

#define DMA_S dman
#define LS_P 131072
template <int MODE>
__device__ __forceinline__ void s_role(LAS unsigned char* lds, Ptrs& P, const u16* W, int h, unsigned obase, int nseg, int fl, int wid, int s0) {
  constexpr bool XM = MODE == 2;
  constexpr unsigned RS = XM ? DM * 2 : HD * 2, SS = XM ? 262144u : 65536u;
  const int lane = opq(0);
  const int kh = wid >> 2, tp = wid & 3;
  const rsrc_t rs = WS_RSRC(P);
  const rsrc_t rw = __builtin_amdgcn_make_buffer_rsrc((void*)W, 0, HD * DM * 2, RSRC_W3);
  const unsigned lbase = (unsigned)(size_t)lds;
  u32x8 w[8][2];
  {
    const int g = lane >> 4, q4 = (lane & 15) >> 2, p4 = lane & 3;
    const int trow = XM ? 16 * g + 4 * (q4 >> 1) + (q4 & 1) : 8 * g + q4;
#define S_SW(row) ((((row) & 1) | ((((row) >> (XM ? 2 : 1)) & 1) << 1) | ((((row) >> (XM ? 4 : 3)) & 1) << 2)) << 1)
    const int toff = trow * 256 + (((4 * tp + (p4 >> 1)) ^ S_SW(trow)) << 4) + 8 * (p4 & 1);
    unsigned dv[2];
    #pragma unroll
    for (int x = 0; x < 2; ++x) {
      const int row = 8 * wid + 4 * x + (lane >> 4);
      dv[x] = (unsigned)row * RS + (XM ? (h >> 3) * 1024 * RS + (h & 7) * 256 : h * 256) + (((lane & 15) ^ S_SW(row)) << 4);
    }
#define S_IMG(i) do { DMA_S(rw, dv[0], (i) * 64 * RS, lbase + ((i) & 7) * 16384 + wid * 2048); \
                      DMA_S(rw, dv[1], (i) * 64 * RS, lbase + ((i) & 7) * 16384 + wid * 2048 + 1024); } while (0)
    #pragma unroll
    for (int i = 0; i < 8; ++i) S_IMG(i);
    #pragma unroll
    for (int b = 0; b < 4; ++b) {
      if (b == 3 && !XM) get_tag(P);
      if (b < 3) VMW(8); else VMWAIT();
      BARRIER();
      if (kh == (b >> 1)) {
        #pragma unroll
        for (int i = 4 * (b & 1); i < 4 * (b & 1) + 4; ++i) {
          w[i][0] = rd_frag<XM ? 2048 : 8192, XM ? 512 : 1024>(lds + toff + i * 16384);
          w[i][1] = rd_frag<XM ? 2048 : 8192, XM ? 512 : 1024>(lds + (toff ^ 32) + i * 16384);
        }
      }
      LBARRIER();
      if (b < 2) {
        #pragma unroll
        for (int i = 4 * b + 8; i < 4 * b + 12; ++i) S_IMG(i);
      }
    }
  }
  TL(XM ? 74 : 72);
  const unsigned l16 = lane * 16;
  volatile LAS int* rd = (volatile LAS int*)(lds + LS_P + 20480);
#define X_READY(want) do { \
    if (wid == 0) { \
      const int n_ = -wmax(-(int)flag_wait(P, F_PRG(4, (h & 24) + (lane & 7)), s0 + (want), lane)) - s0; \
      if (lane == 0) *rd = max(n_, want); \
    } \
    LBARRIER(); } while (0)
  if (XM) X_READY(2);
  else {
    if (wid == 0) setup_wait(P, lane);
    BARRIER();
  }
  unsigned sx = (XM ? WS_OX + s0 * SS + (h >> 3) * 65536 : WS_XN) + kh * 32768 + tp * 4096;
  const unsigned sd = lbase + kh * 65536 + tp * 4096;
  #pragma unroll
  for (int i = 0; i < 12; ++i) dmas(rs, l16, sx + (i >> 3) * SS + ((i >> 2) & 1) * 16384 + (i & 3) * 1024, sd + (i >> 2) * 16384 + (i & 3) * 1024);
  VMWAIT();
  BARRIER();
  const unsigned xl = kh * 65536 + (XM ? l16 : blk_in(lane & 15, (lane >> 4) * 8));
  const unsigned xp = lbase + xl, pa = lbase + LS_P + tp * 4096 + l16;
  u32x8 xf[3], ac[2] = {};
  #pragma unroll
  for (int i = 0; i < 3; ++i) {
    const u32x4 a = *(const LAS u32x4*)(lds + xl + i * 2048), b = *(const LAS u32x4*)(lds + xl + i * 2048 + 1024);
    xf[i] = (u32x8){a[0], a[1], a[2], a[3], b[0], b[1], b[2], b[3]};
  }
  unsigned long long fm, pm;
  asm volatile("s_cmp_eq_u32 %2, 1\n\ts_cselect_b64 %0, 1, 0\n\ts_cmp_eq_u32 %3, 1\n\ts_cselect_b64 %1, -1, 0" : "=&s"(fm), "=&s"(pm) : "s"(wid), "s"(XM ? 0 : wid >> 1) : "scc");
  const unsigned fo = FLG_OFF + fl * 128, pd = lbase + LS_P + 16384;
  const unsigned po = 4 * 65536 + h * 2048 + (wid & 1) * 1024 - tp * 4096 - kh * 32768;
  unsigned oo = XM ? obase + (unsigned)h * 16384u + tp * 4096 + (s0 - 1) * 0x80000u : obase + (unsigned)h * 8192u + tp * (MODE ? 2048 : 1024) - 0x40000u, tmp, tmp2;
  unsigned long long em;
  int t = 0;
  if (MODE == 0) S_LOOP_K(nseg, 1);
  else if (MODE == 1) S_LOOP_V(nseg, 1);
  else {
    #pragma unroll 1
    while (t < nseg) {
      X_READY(min(t + 4, nseg));
      const int n = __builtin_amdgcn_readfirstlane(*rd), te = n >= nseg ? nseg : (n - 2) & ~1;
      TL(t);
      S_LOOP_X(te, (nseg - 1 - te) >> 31);
    }
  }
  TL(XM ? 65 : 64);
  VMWAIT();
  BARRIER();
  if (wid == 1) flag_set(P, fl, nseg, lane);
}

__device__ __forceinline__ void setup_role(LAS unsigned char* lds, Ptrs& P, int cu, int wid) {
  const int lane = opq(0), tid = wid * 64 + lane;
  const rsrc_t rs = WS_RSRC(P);
  u32x4 v[8];
  const u32x4* px = (const u32x4*)(P.x + (size_t)(cu * 32 + wid * 4 + (lane >> 4)) * DM) + (lane & 15);
  #pragma unroll
  for (int i = 0; i < 8; ++i) v[i] = __builtin_nontemporal_load(px + i * 16);
  const unsigned epo = __hip_atomic_load((gu32*)(P.ws + EPO_OFF), RLX_AGENT);
  const unsigned cb = comm_base(P.ws);
  const u32x4 z = {0u, 0u, 0u, 0u};
  if (wid == 0 && cu < 16) st16(rs, CNT_OFF + cu * 1024 + lane * 16, z);
  if (cu < 4) st16(rs, cb + FL_B(0) + cu * 8192 + tid * 16, z);
  if (cu == 4 && tid < 256) {
    const int g4 = tid >> 6, k = (tid >> 5) & 1 & (g4 >> 1);
    st16(rs, WS_H1 + ((g4 & 1) ? 127u * 32768u : 0u) + (g4 * 8 + ((tid >> 2) & 7)) * 1024 + blk_in((g4 & 1) ? 15 - k : k, (tid & 3) * 8), z);
  }
  #pragma unroll
  for (int i = 0; i < 2; ++i) {
    const int f = i * 64 + cu;
    if (f < PD * NCHUNK * 2) {
      u32x4 m = {~0u, ~0u, ~0u, ~0u};
      if (f < 4) {
        const int ks = (f >> 1) * 8 + wid;
        m = *(const u32x4*)(P.mem0 + ((lane >> 1) & 7) * DM + 64 * ks + 16 * (lane >> 4) + 8 * (lane & 1));
        m.x = canon1(m.x); m.y = canon1(m.y); m.z = canon1(m.z); m.w = canon1(m.w);
      }
      st16(rs, cb + (unsigned)(f / (NCHUNK * 2)) * CB_STEP + (f % (NCHUNK * 2)) * 8192u + tid * 16, m);
    }
  }
  const unsigned bi = blk_in((wid & 3) * 4 + (lane >> 4), (lane & 3) * 8);
  const int c0 = (lane >> 2) & 3;
  ln_rows(v);
  TL(69);
  #pragma unroll
  for (int i = 0; i < 8; ++i) st16(rs, WS_XN + xn_blk(cu * 2 + (wid >> 2), 4 * i + c0) + bi, v[i]);
  P.tag = __builtin_amdgcn_readfirstlane(epo) + 1u;
  ln_rows(v);
  asm volatile("" ::: "memory");
  #pragma unroll
  for (int i = 0; i < 8; ++i) st16(rs, WS_XN2 + (unsigned)(cu * 2 + (wid >> 2)) * 32768u + (4 * i + c0) * 1024 + bi, v[i]);
  touch(P.w1, DM * DM * 2, ((cu & 31) * 512 + tid) * 128, lds);
  VMW(9);
  BARRIER();
  if (wid == 0) flag_set(P, F_LN0(cu), 1, lane);
  TL(70);
  if (wid == 0) VMW(2); else VMW(1);
  BARRIER();
  if (wid == 0) flag_set(P, F_LN1(cu), 1, lane);
  TL(71);
}

__global__ __launch_bounds__(512, 2) void mega_kernel(const u16* x, const u16* mem0, const u16* Wq, const u16* Wk, const u16* Wv, const u16* Wo,
                                                     const u16* w1, const u16* w2, const u16* w3, const u16* w4, u16* out, char* ws) {
  __shared__ __attribute__((aligned(16))) unsigned char smem[163840];
  LAS unsigned char* lds = (LAS unsigned char*)smem;
  Ptrs P;
  P.x = x; P.mem0 = mem0; P.Wq = Wq; P.Wk = Wk; P.Wv = Wv; P.Wo = Wo; P.w1 = w1; P.w2 = w2; P.w3 = w3; P.w4 = w4; P.out = out; P.ws = ws;
  const int xcd = blockIdx.x & 7, slot = blockIdx.x >> 3;
  const int wid = __builtin_amdgcn_readfirstlane(threadIdx.x >> 6);
  TL(79);
  P.tag = 0;
  const int na = xcd == 2 ? 8 : 12;
  const bool isa = xcd < 3 && slot < na;
  const int ah = xcd * 12 + slot, bi = xcd * 20 + slot - na, br = bi & 1, bc = bi >> 1, bs = slot - na;
  if (isa) {
    if (wid < 4) a_wave<true>(lds, P, ah, wid); else a_wave<false>(lds, P, ah, wid);
  } else if (xcd < 3) {
    if (wid < 4) b_wave<true>(lds, P, br, bc, wid, bs); else b_wave<false>(lds, P, br, bc, wid, bs);
  } else {
    if ((xcd & 3) == 3) {
      setup_role(lds, P, (xcd >> 2) * 32 + slot, wid);
      if (wid == 0) setup_wait(P, opq(0));
    } else if (xcd == 6) s_role<1>(lds, P, Wv, slot, WS_VF, NSEG, F_PRG(2, slot), wid, 0);
    else s_role<0>(lds, P, xcd == 4 ? Wk : Wq, slot, xcd == 4 ? WS_KF : WS_QF, NSEG, F_PRG(xcd - 4, slot), wid, 0);
    report(P, wid);
    if ((xcd & 3) == 3) mlp_role(lds, P, xcd >> 2, slot, wid);
    else s_role<2>(lds, P, Wo, slot, WS_PT, xcd == 4 ? XS1 : xcd == 5 ? XS2 - XS1 : NSEG - XS2, F_PRG(xcd + 1, slot), wid, xcd == 4 ? 0 : xcd == 5 ? XS1 : XS2);
    fin_role(lds, P, wid);
    if (blockIdx.x == 3 && wid == 0) {
      cnt_wait(P, C_ARR, 256, threadIdx.x);
      if (threadIdx.x == 0) __hip_atomic_store((gu32*)(ws + EPO_OFF), P.tag, RLX_AGENT);
    }
  }
  TL(78);
}

extern "C" void kernel_launch(void* const* d_in, const int* in_sizes, int n_in, void* d_out, int out_size,
                              void* d_ws, size_t ws_size, hipStream_t stream) {
  mega_kernel<<<256, 512, 0, stream>>>((const u16*)d_in[0], (const u16*)d_in[1], (const u16*)d_in[2], (const u16*)d_in[3], (const u16*)d_in[4],
                                       (const u16*)d_in[5], (const u16*)d_in[8], (const u16*)d_in[9], (const u16*)d_in[10], (const u16*)d_in[11],
                                       (u16*)d_out, (char*)d_ws);
}
```

```cpp
#include <hip/hip_runtime.h>
#include <hip/hip_bf16.h>
#include <stdint.h>

#define SEQ 2048
#define DM 1024
#define NH 32
#define DH 128
#define HD (NH * DH)
#define MM 8
#define SEG 32
#define NSEG (SEQ / SEG)
#define FF 3584
#define MIX

typedef unsigned short u16;
typedef __attribute__((ext_vector_type(8))) short bf16x8;
typedef __attribute__((ext_vector_type(4))) short bf16x4;
typedef __attribute__((ext_vector_type(4))) float f32x4;
typedef __attribute__((ext_vector_type(4))) unsigned u32x4;
typedef __attribute__((ext_vector_type(2))) unsigned u32x2;
typedef __attribute__((ext_vector_type(2))) float f32x2;
typedef __attribute__((ext_vector_type(2))) __bf16 bf16v2;
typedef __attribute__((ext_vector_type(8))) unsigned u32x8;
#define LAS __attribute__((address_space(3)))
#define GAS __attribute__((address_space(1)))

__device__ __forceinline__ unsigned pack2(float lo, float hi) {
  return __builtin_bit_cast(unsigned, __builtin_convertvector((f32x2){lo, hi}, bf16v2));
}
__device__ __forceinline__ float rb1(float x) { return __uint_as_float(pack2(0.f, x)); }

typedef GAS unsigned gu32;
#define RLX_AGENT __ATOMIC_RELAXED, __HIP_MEMORY_SCOPE_AGENT
#define SPIN_CAP (1u << 20)
#define AUX_SC1 16
#define RSRC_W3 0x00020000
typedef __amdgpu_buffer_rsrc_t rsrc_t;

__device__ __forceinline__ u32x4 ld16(rsrc_t rs, unsigned off) { return __builtin_amdgcn_raw_buffer_load_b128(rs, off, 0, AUX_SC1); }
__device__ __forceinline__ u32x2 ld8(rsrc_t rs, unsigned off) { return __builtin_amdgcn_raw_buffer_load_b64(rs, off, 0, AUX_SC1); }
__device__ __forceinline__ unsigned ld4(rsrc_t rs, unsigned off) { return __builtin_amdgcn_raw_buffer_load_b32(rs, off, 0, AUX_SC1); }
__device__ __forceinline__ void st16(rsrc_t rs, unsigned off, u32x4 v) { __builtin_amdgcn_raw_buffer_store_b128(v, rs, off, 0, AUX_SC1); }
__device__ __forceinline__ void st8(rsrc_t rs, unsigned off, u32x2 v) { __builtin_amdgcn_raw_buffer_store_b64(v, rs, off, 0, AUX_SC1); }
__device__ __forceinline__ void st4(rsrc_t rs, unsigned off, unsigned v) { __builtin_amdgcn_raw_buffer_store_b32(v, rs, off, 0, AUX_SC1); }
__device__ __forceinline__ float lo16(unsigned w) { return __uint_as_float(w << 16); }
__device__ __forceinline__ float hi16(unsigned w) { return __uint_as_float(w & 0xffff0000u); }

__device__ __forceinline__ float xsum16_32(float v) {
  unsigned u = __float_as_uint(v);
  auto a = __builtin_amdgcn_permlane16_swap(u, u, false, false);
  v = __uint_as_float(a[0]) + __uint_as_float(a[1]);
  u = __float_as_uint(v);
  auto b = __builtin_amdgcn_permlane32_swap(u, u, false, false);
  return __uint_as_float(b[0]) + __uint_as_float(b[1]);
}

__device__ __forceinline__ unsigned canon1(unsigned w) { return min(w, 0xfffffffeu); }
__device__ __forceinline__ unsigned max4(u32x4 v) { return max(max(v.x, v.y), max(v.z, v.w)); }
__device__ __forceinline__ int opq(int) { int v; asm volatile("v_mbcnt_lo_u32_b32 %0, -1, 0\n\tv_mbcnt_hi_u32_b32 %0, -1, %0" : "=v"(v)); return v; }
__device__ __forceinline__ u32x4 poison4() { unsigned m = ~0u; asm volatile("" : "+v"(m)); return (u32x4){m, m, m, m}; }
__device__ __forceinline__ f32x4 zero4() { float m = 0.f; asm volatile("" : "+v"(m)); return (f32x4){m, m, m, m}; }
#define MFMA32(a, b, c) __builtin_amdgcn_mfma_f32_16x16x32_bf16(__builtin_bit_cast(bf16x8, a), __builtin_bit_cast(bf16x8, b), c, 0, 0, 0)
#define MFMA16(a, b, c) __builtin_amdgcn_mfma_f32_16x16x16bf16_1k(__builtin_bit_cast(bf16x4, a), __builtin_bit_cast(bf16x4, b), c, 0, 0, 0)
#define LBARRIER() do { asm volatile("s_waitcnt lgkmcnt(0)" ::: "memory"); __builtin_amdgcn_s_barrier(); asm volatile("" ::: "memory"); } while (0)
#define BARRIER() do { asm volatile("" ::: "memory"); __builtin_amdgcn_s_barrier(); asm volatile("" ::: "memory"); } while (0)
#define VMWAIT() asm volatile("s_waitcnt vmcnt(0)" ::: "memory")
#define VMW(n) asm volatile("s_waitcnt vmcnt(" #n ")" ::: "memory")
#define DELAY(n) do { if ((n) > 0) __builtin_amdgcn_s_sleep(n); } while (0)

#define EPO_OFF 0
#define TMO_OFF 128
#define FLG_OFF 256
#define F_LN0(i) (i)
#define F_LN1(i) (64 + (i))
#define F_CAL(i) (128 + (i))
#define F_PRG(t, h) (192 + (t) * 32 + (h))
#define CNT_OFF 65536
#define C_W1(p2) (p2)
#define C_W2(p2) (16 + (p2))
#define C_LN(pm) (32 + (pm))
#define C_W3(pm, hf) (40 + (pm) * 2 + (hf))
#define C_W4(pm) (56 + (pm))
#define C_HEAD 64
#define C_ARR 72
#define C_P4(pm, pc) (96 + (pm) * 4 + (pc))
#define NCNT 128
#define WS_XN (1u << 20)
#define WS_XN2 (WS_XN + (4u << 20))
#define WS_QF (WS_XN2 + (4u << 20))
#define WS_KF (WS_QF + (16u << 20))
#define WS_VF (WS_KF + (16u << 20))
#define WS_OX (WS_VF + (16u << 20))
#define WS_H1 (WS_OX + (16u << 20) + (1u << 20))
#define WS_X1 (WS_H1 + (4u << 20) + 65536u)
#define WS_X1N (WS_X1 + (4u << 20))
#define WS_H3 (WS_X1N + (4u << 20))
#define WS_P4 (WS_H3 + (14u << 20))
#define WS_G (WS_P4 + (8u << 20))
#define WS_PT (WS_G + (4u << 20))
#define WS_COMM (WS_PT + (32u << 20))
#define WS_END (WS_COMM + (4u << 20))
#define CB_STEP 524288u
#define PD 3
__device__ __forceinline__ unsigned comm_base(const char* ws) { return WS_COMM + ((0u - ((unsigned)(size_t)ws + WS_COMM)) & 0x1fffffu); }
__device__ __forceinline__ unsigned step_base(unsigned cb, unsigned mask, int s) { return cb + (unsigned)(s & 3) * CB_STEP + mask; }
#define MF_AT(ks) (((ks) >> 3) * 16384u + ((ks) & 7) * 1024u)
#define OB_AT(r, h) ((2u + (r) * 4u + ((h) >> 3)) * 16384u + ((h) & 7) * 1024u)
#define VB_AT(h) ((10u + ((h) >> 2)) * 16384u + ((h) & 3) * 2048u)
#define XS1 22
#define XS2 44
#define XS3 60
#define CSL 8
#define FSL 6
#define SQUICK 3
#define NCHUNK 18
#define FL_O(h) (29u * 16384u + (h) * 128u)
#define FL_B(b) (28u * 16384u + (b) * 128u)
#define FL_T(s) FL_O(32 + ((s) & 3))

struct Ptrs { const u16 *x, *mem0, *Wq, *Wk, *Wv, *Wo, *w1, *w2, *w3, *w4; u16* out; char* ws; unsigned tag; };
#define WS_RSRC(P) __builtin_amdgcn_make_buffer_rsrc((void*)(P).ws, 0, WS_END, RSRC_W3)

__device__ __forceinline__ bool spin_fail(unsigned& spins, const Ptrs& P, int lane) {
  asm volatile("" ::: "memory");
  if (++spins > SPIN_CAP) {
    unsigned tv;
    asm volatile("v_mov_b32 %0, %1" : "=v"(tv) : "s"(P.tag));
    if (lane == 0) __builtin_amdgcn_raw_buffer_store_b32(tv, WS_RSRC(P), 0, TMO_OFF, AUX_SC1);
    return true;
  }
  return (spins & 255u) == 0u && (unsigned)__builtin_amdgcn_readfirstlane(__builtin_amdgcn_raw_buffer_load_b32(WS_RSRC(P), 0, TMO_OFF, AUX_SC1)) == P.tag;
}
__device__ __forceinline__ unsigned cnt_load(char* ws, int idx) {
  return __builtin_amdgcn_readfirstlane(__hip_atomic_load((gu32*)(ws + CNT_OFF + idx * 128), RLX_AGENT));
}
__device__ __forceinline__ void cnt_add(char* ws, int idx, int lane) {
  asm volatile("s_waitcnt vmcnt(0)" ::: "memory");
  if (lane == 0) __hip_atomic_fetch_add((gu32*)(ws + CNT_OFF + idx * 128), 1u, RLX_AGENT);
}
__device__ __forceinline__ void cnt_wait(const Ptrs& P, int idx, unsigned want, int lane) {
  for (unsigned spins = 0;;) {
    if (cnt_load(P.ws, idx) >= want) break;
    __builtin_amdgcn_s_sleep(CSL);
    if (spin_fail(spins, P, lane)) break;
  }
}
__device__ __forceinline__ unsigned flag_wait(const Ptrs& P, int f, unsigned want, int lane) {
  const rsrc_t rs = WS_RSRC(P);
  u32x2 v;
  for (unsigned spins = 0;;) {
    v = ld8(rs, FLG_OFF + f * 128);
    if (!__any(v.y != P.tag || v.x < want)) break;
    __builtin_amdgcn_s_sleep(FSL);
    if (spin_fail(spins, P, lane)) break;
  }
  asm volatile("" ::: "memory");
  return v.x;
}
__device__ __forceinline__ void flag_set(const Ptrs& P, int f, unsigned val, int lane) {
  if (lane == 0) st8(WS_RSRC(P), FLG_OFF + f * 128, (u32x2){val, P.tag});
}
__device__ __forceinline__ void get_tag(Ptrs& P) { P.tag = __builtin_amdgcn_readfirstlane(__hip_atomic_load((gu32*)(P.ws + EPO_OFF), RLX_AGENT)) + 1u; }
__device__ __forceinline__ void setup_wait(const Ptrs& P, int lane) { flag_wait(P, F_LN0(lane), 0, lane); }
__device__ __forceinline__ void clock_wait(unsigned tgt) {
  while ((int)(tgt - (unsigned)__builtin_amdgcn_s_memrealtime()) > 0) {}
}
__device__ __forceinline__ void report(const Ptrs& P, int wid) {
  if (wid == 0 && opq(0) == 0) __hip_atomic_fetch_add((gu32*)(P.ws + CNT_OFF + C_ARR * 128), 1u, RLX_AGENT);
}
__device__ __forceinline__ void kv_wait(const Ptrs& P, int s, int h, int& known, int lane) {
  if (s < known) return;
  const unsigned v = flag_wait(P, F_PRG(min(lane, 2), h), s + 1, lane);
  known = __builtin_amdgcn_readfirstlane(min(min(__builtin_amdgcn_readlane(v, 0), __builtin_amdgcn_readlane(v, 1)), __builtin_amdgcn_readlane(v, 2)));
}
__device__ __forceinline__ int wmax(int v) {
  v = max(v, __builtin_amdgcn_update_dpp(0, v, 0x128, 0xf, 0xf, false));
  v = max(v, __builtin_amdgcn_update_dpp(0, v, 0x124, 0xf, 0xf, false));
  v = max(v, __builtin_amdgcn_update_dpp(0, v, 0x122, 0xf, 0xf, false));
  v = max(v, __builtin_amdgcn_update_dpp(0, v, 0x121, 0xf, 0xf, false));
  return max(max(__builtin_amdgcn_readlane(v, 0), __builtin_amdgcn_readlane(v, 16)), max(__builtin_amdgcn_readlane(v, 32), __builtin_amdgcn_readlane(v, 48)));
}
__device__ __forceinline__ unsigned near_half(unsigned v) { return wmax(-(int)(v & 65535u)) > wmax(-(int)(v >> 16)) ? 8192u : 0u; }
__device__ __forceinline__ void dmaw(rsrc_t rs, unsigned voff, unsigned soff, unsigned ldsaddr) {
  asm volatile("s_mov_b32 m0, %3\n\ts_nop 0\n\tbuffer_load_dwordx4 %0, %1, %2 offen lds" :: "v"(voff), "s"(rs), "s"(soff), "s"(ldsaddr) : "memory");
}
__device__ __forceinline__ void dman(rsrc_t rs, unsigned voff, unsigned soff, unsigned ldsaddr) {
  asm volatile("s_mov_b32 m0, %3\n\ts_nop 0\n\tbuffer_load_dwordx4 %0, %1, %2 offen nt lds" :: "v"(voff), "s"(rs), "s"(soff), "s"(ldsaddr) : "memory");
}
__device__ __forceinline__ void dmas(rsrc_t rs, unsigned voff, unsigned soff, unsigned ldsaddr) {
  asm volatile("s_mov_b32 m0, %3\n\ts_nop 0\n\tbuffer_load_dwordx4 %0, %1, %2 offen sc1 lds" :: "v"(voff), "s"(rs), "s"(soff), "s"(ldsaddr) : "memory");
}
__device__ __forceinline__ void touch(const void* w, unsigned bytes, unsigned voff, LAS unsigned char* lds) {
  asm volatile("s_mov_b32 m0, %2\n\ts_nop 0\n\tbuffer_load_dword %0, %1, 0 offen lds"
               :: "v"(voff), "s"(__builtin_amdgcn_make_buffer_rsrc((void*)w, 0, bytes, RSRC_W3)), "s"((unsigned)(size_t)lds + 148480) : "memory");
}
__device__ __forceinline__ u32x2 trd(LAS unsigned char* p) {
  return __builtin_bit_cast(u32x2, __builtin_amdgcn_ds_read_tr16_b64_v4i16((LAS bf16x4*)p));
}
template <int BO, int UO>
__device__ __forceinline__ u32x8 rd_frag(LAS unsigned char* p) {
  const u32x2 a = trd(p), b = trd(p + UO), c = trd(p + BO), d = trd(p + BO + UO);
  return (u32x8){a.x, a.y, b.x, b.y, c.x, c.y, d.x, d.y};
}

#define TL(i) do {} while (0)
#define TLW(i) do {} while (0)
#define GSDECL
#define GS(i) do {} while (0)
#define GSFLUSH(k, i) do {} while (0)

__device__ __forceinline__ int lds_byte(int r, int c) {
  const int st = (r >> 4) * 2 + (c >> 5), rr = r & 15, cc = c & 31, ob = rr * 64 + cc * 2;
  return st * 1024 + (ob ^ (((ob >> 9) & 1) << 5));
}
#define HSP 3
#define PFD 3
#define PFH 4
struct Pf { unsigned v, step; bool on, a; };
#define G_PF(t) do { if (pf.on && (t) < nt) asm volatile("s_mov_b32 m0, %3\n\ts_nop 0\n\tbuffer_load_dword %0, %1, %2 offen sc1 lds" \
    :: "v"(pf.v), "s"(pf.a ? ra : rb), "s"((t) * pf.step), "s"((unsigned)(size_t)lds + 148480) : "memory"); } while (0)

__device__ __forceinline__ void gemm_loop(LAS unsigned char* lds, rsrc_t ra, rsrc_t rb, unsigned cA, unsigned cB, int lda, int ldb, int nt,
                                          const Pf& pf, f32x4 (&acc)[2][2][4][2]) {
  const int wid = __builtin_amdgcn_readfirstlane(threadIdx.x >> 6), lane = opq(0), tid = wid * 64 + lane;
  const int wr = wid >> 2, wc = wid & 3, fr = lane & 15, fq = lane >> 4;
  unsigned voffA[2], voffB[2];
  #pragma unroll
  for (int i = 0; i < 2; ++i) {
    voffA[i] = (unsigned)(((wid >> 1) + 4 * i) * 16 * lda + (wid & 1) * 1024 + lane * 16);
    const int b = tid * 16 + i * 8192, r = b >> 8, pos = (b >> 4) & 15;
    const int c = pos ^ (((r & 3) << 1) | (((r >> 3) & 1) << 3));
    voffB[i] = (unsigned)(r * ldb + c * 16);
  }
  const unsigned hA = 128 * lda;
  const unsigned kB = 64 * ldb;
  const unsigned ldsw = (unsigned)(size_t)lds + (unsigned)wid * 1024u;
  const int aoff = 65536 + lds_byte(wr * 64 + fr, fq * 8);
  const int q = (lane & 15) >> 2, p = lane & 3;
  const unsigned boff0 = (unsigned)(size_t)lds + (8 * fq + q) * 256 + (((wc * 4 + (p >> 1)) ^ ((q << 1) | ((fq & 1) << 3))) << 4) + 8 * (p & 1);
  const unsigned boff1 = boff0 ^ 32;
#define G_SB(b, h) (((b) * 2 + (h)) * 16384)
#define G_SA(b, h) (65536 + ((b) * 2 + (h)) * 16384)
#define G_ST1(bufoff, r, so, voff, i) G_D##voff(r, (voff)[i], so, ldsw + (bufoff) + (i) * 8192)
#define G_DvoffA dmas
#define G_DvoffB dmaw
#define G_STAGE(bufoff, so, voff) do { G_ST1(bufoff, G_R##voff, so, voff, 0); G_ST1(bufoff, G_R##voff, so, voff, 1); } while (0)
#define G_RvoffA ra
#define G_RvoffB rb
#define G_LDA(dst, b, h) do { _Pragma("unroll") for (int m = 0; m < 4; ++m) _Pragma("unroll") for (int k = 0; k < 2; ++k) \
    dst[m][k] = *(const LAS bf16x8*)(lds + (G_SA(b, h) - 65536) + aoff + m * 2048 + k * 1024); } while (0)
#define G_TR(dst, base, off) asm volatile("ds_read_b64_tr_b16 %0, %1 offset:%2" : "=v"(dst) : "v"(base), "i"(off))
#define G_LDB(dst, b, h) do { _Pragma("unroll") for (int n = 0; n < 2; ++n) _Pragma("unroll") for (int k = 0; k < 2; ++k) { \
    bf16x4 _v0, _v1; \
    G_TR(_v0, (n ? boff1 : boff0), G_SB(b, h) + k * 8192); \
    G_TR(_v1, (n ? boff1 : boff0), G_SB(b, h) + k * 8192 + 1024); \
    dst[n][k] = __builtin_shufflevector(_v0, _v1, 0, 1, 2, 3, 4, 5, 6, 7); } } while (0)
#define G_WAIT_B(Bt) asm volatile("s_waitcnt lgkmcnt(0)" : "+v"(Bt[0][0]), "+v"(Bt[0][1]), "+v"(Bt[1][0]), "+v"(Bt[1][1]) :: "memory")
#define G_MMA(ai, bj, At, Bt) do { __builtin_amdgcn_s_setprio(1); _Pragma("unroll") for (int m = 0; m < 4; ++m) _Pragma("unroll") for (int n = 0; n < 2; ++n) _Pragma("unroll") for (int k = 0; k < 2; ++k) \
    acc[ai][bj][m][n] = __builtin_amdgcn_mfma_f32_16x16x32_bf16(Bt[n][k], At[m][k], acc[ai][bj][m][n], 0, 0, 0); \
    __builtin_amdgcn_s_setprio(0); } while (0)
#define G_WAIT_V(n) asm volatile("s_waitcnt vmcnt(%0)" :: "n"(n) : "memory")
#define G_WAIT_L(n) asm volatile("s_waitcnt lgkmcnt(" #n ")" ::: "memory")
#define G_BAR __builtin_amdgcn_s_barrier()
#define G_SCHED __builtin_amdgcn_sched_barrier(0)
#define G_APTR(t) (cA + (t) * 2048)
  #pragma unroll
  for (int a = 0; a < 2; ++a)
    #pragma unroll
    for (int b = 0; b < 2; ++b)
      #pragma unroll
      for (int m = 0; m < 4; ++m)
        #pragma unroll
        for (int n = 0; n < 2; ++n) acc[a][b][m][n] = (f32x4){0.f, 0.f, 0.f, 0.f};
  bf16x8 At[4][2], B0[2][2], B1[2][2];
  {
    const unsigned a0 = G_APTR(0);
    #pragma unroll
    for (int t = 2; t < PFD; ++t) G_PF(t);
    G_STAGE(G_SB(0, 0), cB, voffB); G_STAGE(G_SA(0, 0), a0, voffA); G_STAGE(G_SB(0, 1), cB + 256, voffB); G_STAGE(G_SA(0, 1), a0 + hA, voffA);
    if (wr == 1) G_BAR;
    G_WAIT_V(4); G_BAR;
    G_STAGE(G_SB(1, 0), cB + kB, voffB); G_STAGE(G_SA(1, 0), a0 + 2048, voffA); G_STAGE(G_SB(1, 1), cB + kB + 256, voffB);
    G_WAIT_V(6); G_BAR;
  }
  for (int t = 0; t < nt; t += 2) {
    const int t2 = (t == nt - 2) ? 0 : t + 2;
    const unsigned a1 = G_APTR(t + 1);
    const unsigned a2 = G_APTR(t2);
    const unsigned b2 = cB + t2 * kB;
    const unsigned a3 = a2 + 2048;
    const unsigned b3 = b2 + kB;
    G_LDB(B0, 0, 0); G_SCHED; G_LDA(At, 0, 0); G_STAGE(G_SA(1, 1), a1 + hA, voffA);
    G_WAIT_L(8); G_BAR; G_WAIT_B(B0); G_MMA(0, 0, At, B0); G_BAR; G_SCHED;
    G_LDB(B1, 0, 1); G_STAGE(G_SB(0, 0), b2, voffB);
    G_BAR; G_WAIT_B(B1); G_MMA(0, 1, At, B1); G_BAR;
    G_LDA(At, 0, 1); G_STAGE(G_SA(0, 0), a2, voffA);
    G_BAR; G_WAIT_L(0); G_MMA(1, 0, At, B0); G_BAR; G_SCHED;
    G_STAGE(G_SB(0, 1), b2 + 256, voffB);
    G_WAIT_V(6); G_PF(t + PFD); G_BAR; G_MMA(1, 1, At, B1); G_BAR;
    G_LDB(B0, 1, 0); G_SCHED; G_LDA(At, 1, 0); G_STAGE(G_SA(0, 1), a2 + hA, voffA);
    G_WAIT_L(8); G_BAR; G_WAIT_B(B0); G_MMA(0, 0, At, B0); G_BAR; G_SCHED;
    G_LDB(B1, 1, 1); G_STAGE(G_SB(1, 0), b3, voffB);
    G_BAR; G_WAIT_B(B1); G_MMA(0, 1, At, B1); G_BAR;
    G_LDA(At, 1, 1); G_STAGE(G_SA(1, 0), a3, voffA);
    G_BAR; G_WAIT_L(0); G_MMA(1, 0, At, B0); G_BAR; G_SCHED;
    G_STAGE(G_SB(1, 1), b3 + 256, voffB);
    G_WAIT_V(6); G_PF(t + PFD + 1); G_BAR; G_MMA(1, 1, At, B1); G_BAR;
  }
  if (wr == 0) G_BAR;
}

__device__ __forceinline__ void gemm_h(LAS unsigned char* lds, rsrc_t ra, rsrc_t rb, unsigned cA, unsigned cB, int lda, int ldb, int nt,
                                       const Pf& pf, f32x4 (&acc)[1][2][4][2]) {
  const int wid = __builtin_amdgcn_readfirstlane(threadIdx.x >> 6), lane = opq(0), tid = wid * 64 + lane;
  const int wr = wid >> 2, wc = wid & 3, fr = lane & 15, fq = lane >> 4;
  unsigned voffA[2], voffB[2];
  #pragma unroll
  for (int i = 0; i < 2; ++i) {
    voffA[i] = (unsigned)(((wid >> 1) + 4 * i) * 16 * lda + (wid & 1) * 1024 + lane * 16);
    const int b = tid * 16 + i * 8192, r = b >> 8, pos = (b >> 4) & 15;
    const int c = pos ^ (((r & 3) << 1) | (((r >> 3) & 1) << 3));
    voffB[i] = (unsigned)(r * ldb + c * 16);
  }
  const unsigned kB = 64 * ldb;
  const unsigned ldsw = (unsigned)(size_t)lds + (unsigned)wid * 1024u;
  const int q = (lane & 15) >> 2, p = lane & 3;
  const int aoffs = 32768 + lds_byte(wr * 64 + fr, fq * 8);
  const unsigned boffs = (unsigned)(size_t)lds + (8 * fq + q) * 256 + (((wc * 4 + (p >> 1)) ^ ((q << 1) | ((fq & 1) << 3))) << 4) + 8 * (p & 1);
  #pragma unroll
  for (int i = 0; i < 16; ++i) acc[0][i >> 3][(i >> 1) & 3][i & 1] = (f32x4){0.f, 0.f, 0.f, 0.f};
  bf16x8 At[4][2], B0[2][2], B1[2][2];
#define H_STAGE(so, t) do { G_STAGE(so, cB + (t) * kB, voffB); G_STAGE((so) + 32768, G_APTR(t), voffA); G_STAGE((so) + 16384, cB + (t) * kB + 256, voffB); } while (0)
  #pragma unroll
  for (int t = 2; t < PFH; ++t) G_PF(t);
  H_STAGE(0, 0); H_STAGE(49152, 1);
  G_WAIT_V(6); G_BAR;
  if (wr == 1) G_BAR;
  int cur = 0, nxt = 98304;
#define H_M4(bj, m, Bt) do { _Pragma("unroll") for (int n = 0; n < 2; ++n) _Pragma("unroll") for (int k = 0; k < 2; ++k) \
    acc[0][bj][m][n] = __builtin_amdgcn_mfma_f32_16x16x32_bf16(Bt[n][k], At[m][k], acc[0][bj][m][n], 0, 0, 0); G_SCHED; } while (0)
  for (int t = 0; t < nt; ++t) {
    const int aoff = aoffs + cur;
    const unsigned boff0 = boffs + cur, boff1 = boff0 ^ 32;
    const int t2 = t + 2 < nt ? t + 2 : 0;
    const unsigned a2 = G_APTR(t2), b2 = cB + t2 * kB;
    G_LDB(B0, 0, 0); G_ST1(nxt, rb, b2, voffB, 0); G_LDA(At, 0, 0); G_SCHED; G_ST1(nxt, rb, b2, voffB, 1); G_LDB(B1, 0, 1); G_SCHED;
    if (HSP > 2) G_ST1(nxt + 32768, ra, a2, voffA, 0);
    G_WAIT_V(HSP); G_WAIT_B(B0); G_WAIT_B(B1); G_BAR; G_SCHED;
    __builtin_amdgcn_s_setprio(1);
    H_M4(0, 0, B0); if (HSP < 3) G_ST1(nxt + 32768, ra, a2, voffA, 0);
    H_M4(0, 1, B0); G_ST1(nxt + 32768, ra, a2, voffA, 1);
    H_M4(0, 2, B0); G_ST1(nxt + 16384, rb, b2 + 256, voffB, 0);
    H_M4(0, 3, B0); G_ST1(nxt + 16384, rb, b2 + 256, voffB, 1);
    H_M4(1, 0, B1); G_PF(t + PFH);
    H_M4(1, 1, B1); H_M4(1, 2, B1); H_M4(1, 3, B1);
    __builtin_amdgcn_s_setprio(0);
    G_BAR; G_SCHED;
    nxt = cur;
    cur = cur == 98304 ? 0 : cur + 49152;
  }
  if (wr == 0) G_BAR;
}

__device__ __forceinline__ u32x4 epi_pack(const f32x4 a0, const f32x4 a1) {
  const unsigned x0 = pack2(a0[0], a0[1]), x1 = pack2(a0[2], a0[3]);
  const unsigned y0 = pack2(a1[0], a1[1]), y1 = pack2(a1[2], a1[3]);
  auto s0 = __builtin_amdgcn_permlane16_swap(x0, y0, false, false);
  auto s1 = __builtin_amdgcn_permlane16_swap(x1, y1, false, false);
  return (u32x4){s0[0], s1[0], s0[1], s1[1]};
}
__device__ __forceinline__ unsigned gelu2(unsigned w) {
  const f32x2 x = {lo16(w), hi16(w)};
  const float c0 = -2.f * 1.4426950408889634f * 0.7978845608028654f, c1 = c0 * 0.044715f;
  const f32x2 a = x * (x * x * c1 + c0);
  const f32x2 d = (f32x2){__builtin_amdgcn_exp2f(a[0]), __builtin_amdgcn_exp2f(a[1])} + 1.f;
  const f32x2 y = x * (f32x2){__builtin_amdgcn_rcpf(d[0]), __builtin_amdgcn_rcpf(d[1])};
  return pack2(y[0], y[1]);
}
__device__ __forceinline__ float silu_f(float b) { return b * __builtin_amdgcn_rcpf(1.f + __expf(-b)); }

__device__ __forceinline__ unsigned blk_in(int r, int c) { const int ob = r * 64 + c * 2; return ob ^ (((ob >> 9) & 1) << 5); }
__device__ __forceinline__ unsigned xn_blk(int rb, int cb) { return (unsigned)(((rb >> 1) * 32 + cb) * 2 + (rb & 1)) * 1024u; }

template <int EPI, int NA>
__device__ __forceinline__ void epi_rows(const f32x4 (&acc)[NA][2][4][2], rsrc_t rs, unsigned obase, int ldc, int pm2, int pn2, int sh, int wr, int wc, int ln) {
  const int fq = ln >> 4, cb = 16 * (fq & 1) + 8 * (fq >> 1), fr = (ln & 15) + sh;
  const unsigned lo = (unsigned)((fr >> 4) * 16 * ldc) + blk_in(fr & 15, cb);
  u32x4 r[8];
  if (EPI == 2) {
    #pragma unroll
    for (int i = 0; i < 8; ++i) r[i] = ld16(rs, WS_XN + xn_blk(pm2 * 8 + 4 * wr + (i >> 1), pn2 * 4 + 4 * (i & 1) + wc) + lo);
  }
  #pragma unroll
  for (int ai = 0; ai < NA; ++ai)
    #pragma unroll
    for (int m = 0; m < 4; ++m)
      #pragma unroll
      for (int bj = 0; bj < 2; ++bj) {
        u32x4 w = epi_pack(acc[ai][bj][m][0], acc[ai][bj][m][1]);
        #pragma unroll
        for (int i = 0; i < 4; ++i) w[i] = EPI < 2 ? gelu2(w[i]) : pack2(lo16(r[2 * m + bj][i]) + lo16(w[i]), hi16(r[2 * m + bj][i]) + hi16(w[i]));
        st16(rs, obase + (unsigned)(pm2 * 128 + 128 * ai + 64 * wr + 16 * m) * (unsigned)ldc + (unsigned)(pn2 * 4 + 4 * bj + wc) * 1024u + lo, w);
      }
}

__device__ __forceinline__ float row_sum16(float v) {
  v += __uint_as_float(__builtin_amdgcn_update_dpp(0, (int)__float_as_uint(v), 0x128, 0xf, 0xf, false));
  v += __uint_as_float(__builtin_amdgcn_update_dpp(0, (int)__float_as_uint(v), 0x124, 0xf, 0xf, false));
  v += __uint_as_float(__builtin_amdgcn_update_dpp(0, (int)__float_as_uint(v), 0x122, 0xf, 0xf, false));
  v += __uint_as_float(__builtin_amdgcn_update_dpp(0, (int)__float_as_uint(v), 0x121, 0xf, 0xf, false));
  return v;
}
__device__ __forceinline__ void ln_rows(u32x4 (&a)[8]) {
  f32x2 v[32];
  #pragma unroll
  for (int i = 0; i < 32; ++i) v[i] = (f32x2){lo16(a[i >> 2][i & 3]), hi16(a[i >> 2][i & 3])};
  f32x2 s[4] = {v[0], v[1], v[2], v[3]};
  #pragma unroll
  for (int i = 4; i < 32; ++i) s[i & 3] += v[i];
  s[0] = (s[0] + s[1]) + (s[2] + s[3]);
  const float mean = row_sum16(s[0][0] + s[0][1]) * (1.f / DM);
  #pragma unroll
  for (int i = 0; i < 32; ++i) { const f32x2 d = v[i] - mean; s[i & 3] = i < 4 ? d * d : s[i & 3] + d * d; }
  s[0] = (s[0] + s[1]) + (s[2] + s[3]);
  const float var = row_sum16(s[0][0] + s[0][1]) * (1.f / DM);
  const float mb = rb1(mean), vb = rb1(var);
  const float rden = __builtin_amdgcn_rcpf(rb1(__builtin_amdgcn_sqrtf(rb1(vb + 1e-5f))));
  #pragma unroll
  for (int i = 0; i < 32; ++i) {
    const f32x2 d = v[i] - mb;
    const unsigned w = pack2(d[0], d[1]);
    const f32x2 y = (f32x2){lo16(w), hi16(w)} * rden;
    a[i >> 2][i & 3] = pack2(y[0], y[1]);
  }
}

template <int N>
__device__ __forceinline__ void fin_out(rsrc_t rs, const Ptrs& P, unsigned po, unsigned go, unsigned eo) {
  u32x4 pv[N][8], gv[N], xv[N];
  #pragma unroll
  for (int c = 0; c < N; ++c) {
    #pragma unroll
    for (int i = 0; i < 8; ++i) pv[c][i] = ld16(rs, po + c * 16384 + (i >> 1) * 131072 + (i & 1) * 1024);
    gv[c] = ld16(rs, go + c * 4096);
    xv[c] = *(const u32x4*)((const char*)P.x + eo + c * 256);
  }
  #pragma unroll
  for (int c = 0; c < N; ++c) {
#define PF4(i) __builtin_bit_cast(f32x4, pv[c][i])
    u32x4 v = epi_pack((PF4(0) + PF4(2)) + (PF4(4) + PF4(6)), (PF4(1) + PF4(3)) + (PF4(5) + PF4(7)));
    #pragma unroll
    for (int k = 0; k < 4; ++k) {
      v[k] = pack2(lo16(v[k]) * lo16(gv[c][k]), hi16(v[k]) * hi16(gv[c][k]));
      v[k] = pack2(lo16(xv[c][k]) + lo16(v[k]), hi16(xv[c][k]) + hi16(v[k]));
    }
    *(u32x4*)((char*)P.out + eo + c * 256) = v;
  }
}

template <int KH>
__device__ __forceinline__ void epi_w4(const f32x4 (&acc)[2][2][4][2], rsrc_t rs, const Ptrs& P, int pm, int pc, int wid, int ln) {
  char* ws = P.ws;
  const int wr = wid >> 2, wc = wid & 3, fr = ln & 15, fq = ln >> 4;
  const unsigned pb = WS_P4 + (unsigned)(pm * 4 + pc) * 262144u + wid * 16384 + ln * 16;
  #pragma unroll
  for (int i = 0; i < 16; ++i) st16(rs, pb + KH * 131072 + i * 1024, __builtin_bit_cast(u32x4, acc[KH ^ 1][(i >> 1) & 1][i >> 2][i & 1]));
  const unsigned eb = (unsigned)(pm * 256 + 128 * KH + 64 * wr) * 2048u + (unsigned)(pc * 8 + wc) * 1024u + blk_in(fr, 16 * (fq & 1) + 8 * (fq >> 1));
  u32x4 pp[16], r1[8];
  #pragma unroll
  for (int i = 0; i < 8; ++i) r1[i] = ld16(rs, WS_X1 + eb + (i >> 1) * 32768 + (i & 1) * 4096);
  VMWAIT();
  BARRIER();
  if (wid == 0) {
    if (ln == 0) __hip_atomic_fetch_add((gu32*)(ws + CNT_OFF + C_P4(pm, pc) * 128), 1u, RLX_AGENT);
    cnt_wait(P, C_P4(pm, pc), 2, ln);
  }
  BARRIER();
  #pragma unroll
  for (int i = 0; i < 16; ++i) pp[i] = ld16(rs, pb + (KH ^ 1) * 131072 + i * 1024);
  asm volatile("" ::: "memory");
  #pragma unroll
  for (int i = 0; i < 8; ++i) {
    u32x4 v = epi_pack(acc[KH][i & 1][i >> 1][0] + __builtin_bit_cast(f32x4, pp[2 * i]), acc[KH][i & 1][i >> 1][1] + __builtin_bit_cast(f32x4, pp[2 * i + 1]));
    #pragma unroll
    for (int k = 0; k < 4; ++k) {
      v[k] = pack2(lo16(r1[i][k]) + lo16(v[k]), hi16(r1[i][k]) + hi16(v[k]));
      v[k] = pack2(silu_f(lo16(v[k])), silu_f(hi16(v[k])));
    }
    st16(rs, WS_G + eb + (i >> 1) * 32768 + (i & 1) * 4096, v);
  }
}

__device__ __forceinline__ void mlp_role(LAS unsigned char* lds, const Ptrs& P, int hf, int slot, int wid) {
  const int wr = wid >> 2, wc = wid & 3;
  char* ws = P.ws;
  const rsrc_t rs = WS_RSRC(P);
#define M_DONE(c) do { VMWAIT(); BARRIER(); if (wid == 0) cnt_add(ws, c, opq(0)); } while (0)
  #pragma unroll 1
  for (int code = 0; code < 2; ++code) {
    const int p2 = hf * 8 + (slot >> 2), pn = slot & 3;
    int ln = opq(0);
    if (wid == 0) {
      if (code == 0) flag_wait(P, F_LN1(p2 * 4 + (ln & 3)), 0, ln);
      else {
        const unsigned co = CNT_OFF + C_W1(min(max(p2 - 1 + min(ln, 2), 0), 15)) * 128;
        for (unsigned spins = 0; __any(ld4(rs, co) < 4u);) {
          __builtin_amdgcn_s_sleep(4);
          if (spin_fail(spins, P, ln)) break;
        }
      }
    }
    BARRIER();
    TLW(3 * code);
    const rsrc_t rb = __builtin_amdgcn_make_buffer_rsrc((void*)(code ? P.w2 : P.w1), 0, DM * DM * 2, RSRC_W3);
    const unsigned cA = (code ? WS_H1 : WS_XN2) + p2 * 128 * 2048, cB = pn * 512;
    Pf pf;
    pf.on = wc == 0; pf.a = wr;
    ln &= 31;
    pf.v = wr ? cA + (pn * 2 + (ln >> 4)) * 32768 + (ln & 15) * 128 : cB + ((slot >> 2) * 8 + (ln >> 2)) * 2048 + (ln & 3) * 128;
    pf.step = wr ? 2048 : 64 * 2048;
    f32x4 acq[1][2][4][2];
    gemm_h(lds, rs, rb, cA, cB, 2048, 2048, 16, pf, acq);
    TLW(3 * code + 1);
    ln = opq(0);
    if (code == 0) {
      if (wid < 4) touch(P.w2, DM * DM * 2, ((hf * 32 + slot) * 256 + wid * 64 + ln) * 128, lds);
      epi_rows<0, 1>(acq, rs, WS_H1, 2048, p2, 2 * pn, (pn & 1) ? -1 - (pn >> 1) : 1 + (pn >> 1), wr, wc, ln);
      if (wid == 0) setup_wait(P, ln);
      M_DONE(C_W1(p2));
    } else {
      epi_rows<2, 1>(acq, rs, WS_X1, 2048, p2, 2 * pn, 0, wr, wc, ln);
      M_DONE(C_W2(p2));
      if (wid == 0) cnt_wait(P, C_W2(p2), 4, ln);
      BARRIER();
      const unsigned ro = (unsigned)(p2 * 8 + pn * 2 + wr) * 32768u + ((ln >> 2) & 3) * 1024 + blk_in(wc * 4 + (ln >> 4), (ln & 3) * 8);
      u32x4 v[8];
      #pragma unroll
      for (int i = 0; i < 8; ++i) v[i] = ld16(rs, WS_X1 + ro + i * 4096);
      ln_rows(v);
      #pragma unroll
      for (int i = 0; i < 8; ++i) st16(rs, WS_X1N + ro + i * 4096, v[i]);
      M_DONE(C_LN(p2 >> 1));
    }
    TLW(3 * code + 2);
  }
  #pragma unroll 1
  for (int code = 2; code < 5; ++code) {
    const int pm = hf * 4 + (slot & 3), pn = (slot >> 2) + (code == 3 ? 8 : 0);
    int ln = opq(0);
    if (pn >= 14) {
      #pragma unroll 1
      for (int i = 0; i < 7; ++i) {
        touch(P.w4, DM * FF * 2, ((i * 16 + hf * 8 + (slot & 7)) * 512 + wid * 64 + ln) * 128, lds);
        VMWAIT();
        __builtin_amdgcn_s_sleep(64);
      }
      continue;
    }
    if (code != 3) {
      if (wid == 0) cnt_wait(P, code == 2 ? C_LN(pm) : C_W3(pm, pn >> 2), code == 2 ? 8 : 7, ln);
      BARRIER();
    }
    TLW(3 * code);
    const bool w4 = code == 4;
    const int lda = w4 ? 7168 : 2048, ldb = w4 ? 2048 : 7168, nt = w4 ? 28 : 16, na = w4 ? 4 : 8;
    const rsrc_t rb = __builtin_amdgcn_make_buffer_rsrc((void*)(w4 ? P.w4 : P.w3), 0, DM * FF * 2, RSRC_W3);
    const unsigned cA = (w4 ? WS_H3 + (pn >> 2) * 28 * 2048 : WS_X1N) + pm * 256 * lda, cB = w4 ? (pn >> 2) * 28 * 64 * 2048 + (pn & 3) * 512 : pn * 512;
    Pf pf;
    pf.on = wc == 0 && (!wr || code != 3); pf.a = wr;
    pf.v = wr ? cA + ((pn & (na - 1)) * (16 / na) + ((ln >> 4) & (16 / na - 1))) * 16 * lda + (ln & 15) * 128 : cB + ((slot & 3) * 16 + (ln >> 2)) * ldb + (ln & 3) * 128;
    pf.step = wr ? 2048 : 64 * ldb;
    f32x4 acc[2][2][4][2];
    gemm_loop(lds, rs, rb, cA, cB, lda, ldb, nt, pf, acc);
    TLW(3 * code + 1);
    ln = opq(0);
    if (!w4) epi_rows<1, 2>(acc, rs, WS_H3, 7168, 2 * pm, 2 * pn, 0, wr, wc, ln);
    else if (pn >> 2) epi_w4<1>(acc, rs, P, pm, pn & 3, wid, ln);
    else epi_w4<0>(acc, rs, P, pm, pn & 3, wid, ln);
    M_DONE(w4 ? C_W4(pm) : C_W3(pm, pn >= 7));
    TLW(3 * code + 2);
  }
}

#define FINE 52
__device__ __forceinline__ void fin_role(LAS unsigned char* lds, const Ptrs& P, int wid) {
  const int wr = wid >> 2, wc = wid & 3;
  const rsrc_t rs = WS_RSRC(P);
  volatile LAS int* tk = (volatile LAS int*)(lds + 147456);
  unsigned tks = 0;
#define POP() do { if (wid == 0 && opq(0) == 0) tks = __hip_atomic_fetch_add((gu32*)(P.ws + CNT_OFF + C_HEAD * 128), 1u, RLX_AGENT); } while (0)
  POP();
  for (int round = 0;; ++round) {
    if (wid == 0 && opq(0) == 0) tk[round & 1] = tks;
    LBARRIER();
    const int u = __builtin_amdgcn_readfirstlane(tk[round & 1]);
    if (u >= 2 * FINE + 8 * (XS3 - FINE)) break;
    const bool big = u < 2 * FINE;
    const int sg = big ? u >> 1 : FINE + ((u - 2 * FINE) >> 3), cc0 = big ? (u & 1) * 4 : (u - 2 * FINE) & 7;
    const int ln = opq(0), fq = ln >> 4, cb = 16 * (fq & 1) + 8 * (fq >> 1);
    const unsigned eo = (unsigned)(sg * 32 + wr * 16 + (ln & 15)) * 2048u + (unsigned)(cc0 * 128 + wc * 32 + cb) * 2u;
    const unsigned go = WS_G + (unsigned)(sg * 2 + wr) * 32768u + (cc0 * 4 + wc) * 1024 + blk_in(ln & 15, cb);
    const unsigned po = WS_PT + (unsigned)sg * 524288u + cc0 * 16384 + wc * 4096 + wr * 2048 + ln * 16;
    POP();
    if (wid == 1) cnt_wait(P, C_W4(sg >> 3), 8, ln);
    if (wid == 2) { const int xg = (sg >= XS1) + (sg >= XS2); flag_wait(P, F_PRG(5 + xg, ln & 31), sg + 1 - (xg == 2 ? XS2 : xg * XS1), ln); }
    BARRIER();
    if (!big) fin_out<1>(rs, P, po, go, eo);
    else { fin_out<2>(rs, P, po, go, eo); fin_out<2>(rs, P, po + 32768, go + 8192, eo + 512); }
  }
}

#define QPRIO 0
#define VPOS 3
#define VLDI "buffer_load_dword v254, v17, %[rs], %[sb] offen sc1\n\tbuffer_load_dword v255, v17, %[rs], %[sb] offen offset:256 sc1\n\t"
#define VLD(p) VLD##p
#define VLD0
#define VLD1
#define VLD2
#define VLD3 VLDI
#define VLD4
#define DEC1 1
#define DEC2 1
#define DGA
#define DGB
#define DGC
#define DGO
#define DGP
#define DGQ
#define PACE "s_sleep 1\n\t"
#define STR_(x) #x
#define STR(x) STR_(x)
#define GSW
#define GSA(k)
#define GSO(k)
#define A_MAIN_Q() \
  asm volatile( \
    "v_mov_b32 v253, %[tag]\n\t" \
    "buffer_load_dwordx4 v[20:23], v12, %[rs], %[vx] offen sc1\n\t" \
    "buffer_load_dwordx4 v[24:27], v12, %[rs], %[vx] offen offset:1024 sc1\n\t" \
    "ds_read_b128 v[236:239], v9 offset:0\n\t" \
    "ds_read_b128 v[240:243], v9 offset:1024\n\t" \
    "ds_read_b128 v[244:247], v9 offset:2048\n\t" \
    "ds_read_b128 v[248:251], v9 offset:3072\n\t" \
    "v_mov_b32 v0, 0\n\t" \
    "v_mov_b32 v1, 0\n\t" \
    "v_mov_b32 v2, 0\n\t" \
    "v_mov_b32 v3, 0\n\t" \
    "v_mov_b32 v4, 0\n\t" \
    "v_mov_b32 v5, 0\n\t" \
    "v_mov_b32 v6, 0\n\t" \
    "v_mov_b32 v7, 0\n\t" \
    "s_cmp_lg_u32 %[sc], 3\n\t" \
    "s_cbranch_scc1 PB_%=\n\t" \
    "TWA_%=:\n\t" \
    "s_memrealtime s[88:89]\n\t" \
    "s_memtime s[90:91]\n\t" \
    "s_waitcnt lgkmcnt(0)\n\t" \
    "s_lshl_b32 s88, s88, 2\n\t" \
    "s_sub_u32 s89, %[tgt], s88\n\t" \
    "s_cmp_gt_i32 s89, 16\n\t" \
    "s_cbranch_scc1 TWA_%=\n\t" \
    "s_mul_i32 s89, s89, 11\n\t" \
    "s_ashr_i32 s89, s89, 1\n\t" \
    "s_add_u32 s89, s89, s90\n\t" \
    DGB \
    "TCA_%=:\n\t" \
    "s_memtime s[92:93]\n\t" \
    "s_waitcnt lgkmcnt(0)\n\t" \
    "s_sub_u32 s93, s89, s92\n\t" \
    "s_cmp_gt_i32 s93, 0\n\t" \
    "s_cbranch_scc1 TCA_%=\n\t" \
    "PB_%=:\n\t" \
    "s_waitcnt lgkmcnt(0)\n\t" \
    "s_barrier\n\t" \
    "buffer_load_dwordx4 v[220:223], v8, %[rs], %[m0] offen sc1\n\t" \
    "buffer_load_dwordx4 v[224:227], v8, %[rs], %[m1] offen sc1\n\t" \
    "PM_%=:\n\t" \
    "s_waitcnt vmcnt(1)\n\t" \
    "ds_write_b128 v8, v[220:223] offset:0\n\t" \
    "v_max3_u32 v252, v220, v221, v222\n\t" \
    "s_waitcnt vmcnt(0)\n\t" \
    "ds_write_b128 v8, v[224:227] offset:4096\n\t" \
    "v_max3_u32 v252, v252, v223, v224\n\t" \
    "v_max3_u32 v252, v252, v225, v226\n\t" \
    "v_max_u32 v252, v252, v227\n\t" \
    "v_cmp_eq_u32 vcc, -1, v252\n\t" \
    "s_cbranch_vccz PD_%=\n\t" \
    "s_sub_u32 %[lim], %[lim], 1\n\t" \
    "s_cmp_gt_i32 %[lim], 0\n\t" \
    "s_cbranch_scc0 PD_%=\n\t" \
    "s_sleep " STR(CADM) "\n\t" \
    "buffer_load_dwordx4 v[220:223], v8, %[rs], %[m0] offen sc1\n\t" \
    "buffer_load_dwordx4 v[224:227], v8, %[rs], %[m1] offen sc1\n\t" \
    "s_branch PM_%=\n\t" \
    "PD_%=:\n\t" \
    GSA(0) \
    "v_smfmac_f32_16x16x64_bf16 v[0:3], v[220:223], v[236:243], v10\n\t" \
    "v_xor_b32 v9, 16384, v9\n\t" \
    "ds_read_b128 v[236:239], v9 offset:0\n\t" \
    "ds_read_b128 v[240:243], v9 offset:1024\n\t" \
    "v_smfmac_f32_16x16x64_bf16 v[4:7], v[220:223], v[244:251], v10\n\t" \
    "ds_read_b128 v[244:247], v9 offset:2048\n\t" \
    "ds_read_b128 v[248:251], v9 offset:3072\n\t" \
    "s_waitcnt lgkmcnt(4)\n\t" \
    "s_barrier\n\t" \
    "ds_read_b128 v[228:231], v8 offset:8192\n\t" \
    "ds_read_b128 v[232:235], v8 offset:12288\n\t" \
    "v_xor_b32 v8, 1024, v8\n\t" \
    "ds_read_b128 v[220:223], v8 offset:0\n\t" \
    "v_smfmac_f32_16x16x64_bf16 v[0:3], v[224:227], v[28:35], v10\n\t" \
    "v_smfmac_f32_16x16x64_bf16 v[4:7], v[224:227], v[36:43], v10\n\t" \
    "ds_read_b128 v[224:227], v8 offset:4096\n\t" \
    "s_waitcnt lgkmcnt(3)\n\t" \
    "v_smfmac_f32_16x16x64_bf16 v[0:3], v[228:231], v[92:99], v10\n\t" \
    "v_smfmac_f32_16x16x64_bf16 v[4:7], v[228:231], v[100:107], v10\n\t" \
    "ds_read_b128 v[228:231], v8 offset:8192\n\t" \
    "s_waitcnt lgkmcnt(3)\n\t" \
    "v_smfmac_f32_16x16x64_bf16 v[0:3], v[232:235], v[156:163], v10\n\t" \
    "v_smfmac_f32_16x16x64_bf16 v[4:7], v[232:235], v[164:171], v10\n\t" \
    "ds_read_b128 v[232:235], v8 offset:12288\n\t" \
    "s_waitcnt lgkmcnt(3)\n\t" \
    "v_smfmac_f32_16x16x64_bf16 v[0:3], v[220:223], v[236:243], v10\n\t" \
    "v_xor_b32 v9, 49152, v9\n\t" \
    "ds_read_b128 v[236:239], v9 offset:0\n\t" \
    "v_smfmac_f32_16x16x64_bf16 v[4:7], v[220:223], v[244:251], v10\n\t" \
    "ds_read_b128 v[240:243], v9 offset:1024\n\t" \
    "s_waitcnt lgkmcnt(4)\n\t" \
    "v_smfmac_f32_16x16x64_bf16 v[0:3], v[224:227], v[44:51], v10\n\t" \
    "v_xor_b32 v8, 3072, v8\n\t" \
    "ds_read_b128 v[220:223], v8 offset:4096\n\t" \
    "v_smfmac_f32_16x16x64_bf16 v[4:7], v[224:227], v[52:59], v10\n\t" \
    "ds_read_b128 v[244:247], v9 offset:2048\n\t" \
    "s_waitcnt lgkmcnt(5)\n\t" \
    "v_smfmac_f32_16x16x64_bf16 v[0:3], v[228:231], v[108:115], v10\n\t" \
    "ds_read_b128 v[224:227], v8 offset:8192\n\t" \
    "v_smfmac_f32_16x16x64_bf16 v[4:7], v[228:231], v[116:123], v10\n\t" \
    "ds_read_b128 v[248:251], v9 offset:3072\n\t" \
    "s_waitcnt lgkmcnt(6)\n\t" \
    "v_smfmac_f32_16x16x64_bf16 v[0:3], v[232:235], v[172:179], v10\n\t" \
    "ds_read_b128 v[228:231], v8 offset:0\n\t" \
    "v_smfmac_f32_16x16x64_bf16 v[4:7], v[232:235], v[180:187], v10\n\t" \
    "ds_read_b128 v[232:235], v8 offset:12288\n\t" \
    "s_waitcnt lgkmcnt(5)\n\t" \
    "v_smfmac_f32_16x16x64_bf16 v[0:3], v[220:223], v[60:67], v10\n\t" \
    "v_smfmac_f32_16x16x64_bf16 v[4:7], v[220:223], v[68:75], v10\n\t" \
    "v_xor_b32 v8, 1024, v8\n\t" \
    "ds_read_b128 v[220:223], v8 offset:4096\n\t" \
    "s_waitcnt lgkmcnt(4)\n\t" \
    "v_smfmac_f32_16x16x64_bf16 v[0:3], v[224:227], v[124:131], v10\n\t" \
    "v_smfmac_f32_16x16x64_bf16 v[4:7], v[224:227], v[132:139], v10\n\t" \
    "ds_read_b128 v[224:227], v8 offset:8192\n\t" \
    "s_waitcnt lgkmcnt(3)\n\t" \
    "v_smfmac_f32_16x16x64_bf16 v[0:3], v[228:231], v[236:243], v10\n\t" \
    "v_xor_b32 v9, 16384, v9\n\t" \
    "ds_read_b128 v[236:239], v9 offset:0\n\t" \
    "v_smfmac_f32_16x16x64_bf16 v[4:7], v[228:231], v[244:251], v10\n\t" \
    "ds_read_b128 v[240:243], v9 offset:1024\n\t" \
    "s_waitcnt lgkmcnt(4)\n\t" \
    "v_smfmac_f32_16x16x64_bf16 v[0:3], v[232:235], v[188:195], v10\n\t" \
    "ds_read_b128 v[228:231], v8 offset:12288\n\t" \
    "v_smfmac_f32_16x16x64_bf16 v[4:7], v[232:235], v[196:203], v10\n\t" \
    "ds_read_b128 v[244:247], v9 offset:2048\n\t" \
    "s_waitcnt lgkmcnt(5)\n\t" \
    "v_smfmac_f32_16x16x64_bf16 v[0:3], v[220:223], v[76:83], v10\n\t" \
    "ds_read_b128 v[232:235], v8 offset:0\n\t" \
    "v_xor_b32 v8, 3072, v8\n\t" \
    "v_smfmac_f32_16x16x64_bf16 v[4:7], v[220:223], v[84:91], v10\n\t" \
    "ds_read_b128 v[248:251], v9 offset:3072\n\t" \
    "s_waitcnt lgkmcnt(6)\n\t" \
    "v_smfmac_f32_16x16x64_bf16 v[0:3], v[224:227], v[140:147], v10\n\t" \
    "ds_read_b128 v[220:223], v12 offset:24576\n\t" \
    "v_smfmac_f32_16x16x64_bf16 v[4:7], v[224:227], v[148:155], v10\n\t" \
    "ds_read_b128 v[224:227], v12 offset:28672\n\t" \
    "s_waitcnt lgkmcnt(5)\n\t" \
    "v_smfmac_f32_16x16x64_bf16 v[0:3], v[228:231], v[204:211], v10\n\t" \
    "v_smfmac_f32_16x16x64_bf16 v[4:7], v[228:231], v[212:219], v10\n\t" \
    "ds_read_b128 v[228:231], v12 offset:25600\n\t" \
    "s_waitcnt lgkmcnt(3)\n\t" \
    "v_smfmac_f32_16x16x64_bf16 v[0:3], v[232:235], v[236:243], v10\n\t" \
    "v_xor_b32 v9, 49152, v9\n\t" \
    "v_smfmac_f32_16x16x64_bf16 v[4:7], v[232:235], v[244:251], v10\n\t" \
    VLD(4) \
    "s_nop 5\n\t" \
    "v_add_f32 v0, v0, v1\n\t" \
    "v_add_f32 v2, v2, v3\n\t" \
    "v_add_f32 v4, v4, v5\n\t" \
    "v_add_f32 v6, v6, v7\n\t" \
    "v_mul_f32 v0, %[sl], v0\n\t" \
    "v_mul_f32 v2, %[sl], v2\n\t" \
    "v_mul_f32 v4, %[sl], v4\n\t" \
    "v_mul_f32 v6, %[sl], v6\n\t" \
    "v_cvt_pk_bf16_f32 v0, v0, v4\n\t" \
    "v_cvt_pk_bf16_f32 v2, v2, v6\n\t" \
    "ds_write2_b32 v11, v0, v2 offset0:0 offset1:4\n\t" \
    "s_mov_b64 exec, %[onej]\n\t" \
    "ds_write_b32 v16, v253\n\t" \
    "s_mov_b64 exec, -1\n\t" \
    GSA(1) \
    "s_setprio " STR(QPRIO) "\n\t" \
    "ds_read_b128 v[232:235], v12 offset:29696\n\t" \
    "s_nop 5\n\t" \
    "ds_read_b128 v[0:3], v12 offset:26624\n\t" \
    "s_nop 5\n\t" \
    "ds_read_b128 v[4:7], v12 offset:27648\n\t" \
    "s_nop 5\n\t" \
    "ds_read_b32 v252, v16\n\t" \
    "ds_read_b128 v[236:239], v14 offset:0\n\t" \
    "s_nop 5\n\t" \
    "ds_read_b128 v[240:243], v14 offset:512\n\t" \
    "s_nop 5\n\t" \
    "ds_read_b128 v[244:247], v14 offset:1024\n\t" \
    "s_nop 5\n\t" \
    VLD(3) \
    "ds_read_b128 v[248:251], v14 offset:1536\n\t" \
    "s_nop 5\n\t" \
    "s_setprio 0\n\t" \
    "s_waitcnt lgkmcnt(4)\n\t" \
    "v_cmp_ne_u32 vcc, v253, v252\n\t" \
    "s_cbranch_vccnz RP_%=\n\t" \
    "CP_%=:\n\t" \
    VLD(0) \
    "s_waitcnt lgkmcnt(2)\n\t" \
    "v_mfma_f32_16x16x32_bf16 v[220:223], v[220:223], v[236:239], 0\n\t" \
    "v_mfma_f32_16x16x32_bf16 v[224:227], v[224:227], v[236:239], 0\n\t" \
    "v_mfma_f32_16x16x32_bf16 v[220:223], v[228:231], v[240:243], v[220:223]\n\t" \
    "ds_read_b128 v[228:231], v12 offset:30720\n\t" \
    "v_mfma_f32_16x16x32_bf16 v[224:227], v[232:235], v[240:243], v[224:227]\n\t" \
    "ds_read_b128 v[232:235], v12 offset:31744\n\t" \
    "s_setprio " STR(QPRIO) "\n\t" \
    VLD(1) \
    "s_waitcnt lgkmcnt(2)\n\t" \
    "v_mfma_f32_16x16x32_bf16 v[220:223], v[0:3], v[244:247], v[220:223]\n\t" \
    "v_mfma_f32_16x16x32_bf16 v[220:223], v[4:7], v[248:251], v[220:223]\n\t" \
    "s_waitcnt lgkmcnt(1)\n\t" \
    "v_mfma_f32_16x16x32_bf16 v[224:227], v[228:231], v[244:247], v[224:227]\n\t" \
    "s_waitcnt lgkmcnt(0)\n\t" \
    "v_mfma_f32_16x16x32_bf16 v[224:227], v[232:235], v[248:251], v[224:227]\n\t" \
    GSA(2) \
    "s_setprio 2\n\t" \
    "ds_read_b32 v252, v16 offset:16\n\t" \
    "ds_read_b128 v[0:3], v15 offset:0\n\t" \
    "ds_read_b128 v[4:7], v15 offset:512\n\t" \
    "ds_read_b128 v[228:231], v15 offset:1024\n\t" \
    "ds_read_b128 v[232:235], v15 offset:1536\n\t" \
    VLD(2) \
    "v_exp_f32 v220, v220\n\t" \
    "v_exp_f32 v221, v221\n\t" \
    "v_exp_f32 v222, v222\n\t" \
    "v_exp_f32 v223, v223\n\t" \
    "v_exp_f32 v224, v224\n\t" \
    "v_exp_f32 v225, v225\n\t" \
    "v_exp_f32 v226, v226\n\t" \
    "v_exp_f32 v227, v227\n\t" \
    "v_cvt_pk_bf16_f32 v220, v220, v221\n\t" \
    "v_cvt_pk_bf16_f32 v221, v222, v223\n\t" \
    "s_waitcnt lgkmcnt(4)\n\t" \
    "v_cmp_ne_u32 vcc, %[tag], v252\n\t" \
    "s_cbranch_vccnz RK_%=\n\t" \
    "CK_%=:\n\t" \
    "s_waitcnt lgkmcnt(3)\n\t" \
    "v_mfma_f32_16x16x32_bf16 v[0:3], v[0:3], v[236:239], 0\n\t" \
    "v_cvt_pk_bf16_f32 v222, v224, v225\n\t" \
    "v_cvt_pk_bf16_f32 v223, v226, v227\n\t" \
    "s_waitcnt lgkmcnt(2)\n\t" \
    "v_mfma_f32_16x16x32_bf16 v[0:3], v[4:7], v[240:243], v[0:3]\n\t" \
    "v_mov_b32 v224, %[one]\n\t" \
    "v_mov_b32 v225, %[one]\n\t" \
    "s_waitcnt lgkmcnt(1)\n\t" \
    "v_mfma_f32_16x16x32_bf16 v[0:3], v[228:231], v[244:247], v[0:3]\n\t" \
    "v_mov_b32 v226, %[one]\n\t" \
    "v_mov_b32 v227, %[one]\n\t" \
    "s_waitcnt lgkmcnt(0)\n\t" \
    "v_mfma_f32_16x16x32_bf16 v[0:3], v[232:235], v[248:251], v[0:3]\n\t" \
    "v_mfma_f32_16x16x32_bf16 v[244:247], v[224:227], v[220:223], 0\n\t" \
    "v_mov_b32 v229, 0\n\t" \
    "v_mfma_f32_16x16x32_bf16 v[236:239], v[20:23], v[220:223], 0\n\t" \
    "v_mov_b32 v230, 0\n\t" \
    "v_mfma_f32_16x16x32_bf16 v[240:243], v[24:27], v[220:223], 0\n\t" \
    "s_nop 2\n\t" \
    "v_exp_f32 v0, v0\n\t" \
    "v_exp_f32 v1, v1\n\t" \
    "s_nop 0\n\t" \
    "v_cvt_pk_bf16_f32 v228, v0, v1\n\t" \
    "v_cvt_pk_bf16_f32 v231, v0, v1\n\t" \
    GSA(3) \
    "CM_%=:\n\t" \
    "s_nop 0\n\t" \
    "v_mfma_f32_16x16x16_bf16 v[244:247], v[224:225], v[228:229], v[244:247]\n\t" \
    "s_waitcnt vmcnt(0)\n\t" \
    "v_mfma_f32_16x16x16_bf16 v[236:239], v[254:255], v[228:229], v[236:239]\n\t" \
    "v_max_u32 v7, v254, v255\n\t" \
    "v_mfma_f32_16x16x16_bf16 v[240:243], v[254:255], v[230:231], v[240:243]\n\t" \
    "v_cmp_eq_u32 s[88:89], -1, v7\n\t" \
    "s_not_b64 exec, %[hi]\n\t" \
    "s_nop 1\n\t" \
    "v_rcp_f32 v2, v244\n\t" \
    "v_subrev_u32 v253, 0x21800000, v244\n\t" \
    "v_cmp_gt_u32 vcc, v253, %[rg]\n\t" \
    "v_pk_mul_f32 v[236:237], v[236:237], v[2:3] op_sel_hi:[1,0]\n\t" \
    "v_pk_mul_f32 v[238:239], v[238:239], v[2:3] op_sel_hi:[1,0]\n\t" \
    "v_pk_mul_f32 v[240:241], v[240:241], v[2:3] op_sel_hi:[1,0]\n\t" \
    "v_pk_mul_f32 v[242:243], v[242:243], v[2:3] op_sel_hi:[1,0]\n\t" \
    "s_or_b64 vcc, vcc, s[88:89]\n\t" \
    "v_cvt_pk_bf16_f32 v4, v236, v237\n\t" \
    "v_cvt_pk_bf16_f32 v5, v238, v239\n\t" \
    "v_cvt_pk_bf16_f32 v6, v240, v241\n\t" \
    "v_cvt_pk_bf16_f32 v7, v242, v243\n\t" \
    "s_cbranch_scc1 FX_%=\n\t" \
    "buffer_store_dwordx4 v[4:7], v18, %[rs], %[sb] offen sc1\n\t" \
    "s_memrealtime %[tp]\n\t" \
    DGA \
    GSA(4) \
    "v_max3_u32 v252, v4, v5, v6\n\t" \
    "v_max_u32 v252, v252, v7\n\t" \
    "v_cmp_eq_u32 vcc, -1, v252\n\t" \
    "s_cbranch_vccnz RC_%=\n\t" \
    "CC_%=:\n\t" \
    "s_mov_b64 exec, -1\n\t" \
    "v_add_u32 v252, %[vd], v12\n\t" \
    "v_add_u32 v253, %[md], v17\n\t" \
    "ds_write_b128 v252, v[20:23]\n\t" \
    "ds_write_b128 v252, v[24:27] offset:1024\n\t" \
    "ds_write2st64_b32 v253, v254, v255 offset0:0 offset1:1\n\t" \
    "s_waitcnt lgkmcnt(0)\n\t" \
    "s_cmp_lg_u32 %[sc], 3\n\t" \
    "s_cbranch_scc1 NH_%=\n\t" \
    "buffer_load_dwordx2 v[0:1], off, %[rs], %[fl] sc1\n\t" \
    "s_barrier\n\t" \
    "s_waitcnt vmcnt(0)\n\t" \
    "v_readfirstlane_b32 %[pt], v0\n\t" \
    "v_readfirstlane_b32 %[pd], v1\n\t" \
    "s_nop 3\n\t" \
    "s_branch E_%=\n\t" \
    "NH_%=:\n\t" \
    "s_barrier\n\t" \
    "s_branch E_%=\n\t" \
    "RP_%=:\n\t" \
    "s_waitcnt lgkmcnt(0)\n\t" \
    "ds_read_b32 v252, v16\n\t" \
    "ds_read_b128 v[236:239], v14 offset:0\n\t" \
    "ds_read_b128 v[240:243], v14 offset:512\n\t" \
    "ds_read_b128 v[244:247], v14 offset:1024\n\t" \
    "ds_read_b128 v[248:251], v14 offset:1536\n\t" \
    "s_sub_u32 %[lim], %[lim], " STR(DEC1) "\n\t" \
    "s_cmp_gt_i32 %[lim], 0\n\t" \
    "s_waitcnt lgkmcnt(4)\n\t" \
    "s_cbranch_scc0 CP_%=\n\t" \
    "v_cmp_ne_u32 vcc, %[tag], v252\n\t" \
    "s_cbranch_vccnz RP_%=\n\t" \
    "s_branch CP_%=\n\t" \
    "RK_%=:\n\t" \
    "s_waitcnt lgkmcnt(0)\n\t" \
    "ds_read_b32 v252, v16 offset:16\n\t" \
    "ds_read_b128 v[0:3], v15 offset:0\n\t" \
    "ds_read_b128 v[4:7], v15 offset:512\n\t" \
    "ds_read_b128 v[228:231], v15 offset:1024\n\t" \
    "ds_read_b128 v[232:235], v15 offset:1536\n\t" \
    "s_sub_u32 %[lim], %[lim], " STR(DEC1) "\n\t" \
    "s_cmp_gt_i32 %[lim], 0\n\t" \
    "s_waitcnt lgkmcnt(4)\n\t" \
    "s_cbranch_scc0 CK_%=\n\t" \
    "v_cmp_ne_u32 vcc, %[tag], v252\n\t" \
    "s_cbranch_vccnz RK_%=\n\t" \
    "s_branch CK_%=\n\t" \
    "RC_%=:\n\t" \
    "v_min_u32 v4, -2, v4\n\t" \
    "v_min_u32 v5, -2, v5\n\t" \
    "v_min_u32 v6, -2, v6\n\t" \
    "v_min_u32 v7, -2, v7\n\t" \
    "buffer_store_dwordx4 v[4:7], v18, %[rs], %[sb] offen sc1\n\t" \
    "s_branch CC_%=\n\t" \
    "FX_%=:\n\t" \
    "s_mov_b64 exec, -1\n\t" \
    "s_cmp_eq_u64 s[88:89], 0\n\t" \
    "s_cbranch_scc1 RM_%=\n\t" \
    "buffer_load_dword v254, v17, %[rs], %[sb] offen sc1\n\t" \
    "buffer_load_dword v255, v17, %[rs], %[sb] offen offset:256 sc1\n\t" \
    "v_mfma_f32_16x16x32_bf16 v[244:247], v[224:227], v[220:223], 0\n\t" \
    "v_mfma_f32_16x16x32_bf16 v[236:239], v[20:23], v[220:223], 0\n\t" \
    "v_mfma_f32_16x16x32_bf16 v[240:243], v[24:27], v[220:223], 0\n\t" \
    "s_sub_u32 %[lim], %[lim], " STR(DEC2) "\n\t" \
    "s_cmp_gt_i32 %[lim], 0\n\t" \
    "s_cbranch_scc1 CM_%=\n\t" \
    "s_waitcnt vmcnt(0)\n\t" \
    "v_mov_b32 v254, 0\n\t" \
    "v_mov_b32 v255, 0\n\t" \
    "s_branch CM_%=\n\t" \
    "RM_%=:\n\t" \
    "s_mov_b32 %[rg], -1\n\t" \
    "s_nop 7\n\t" \
    "ds_read_b128 v[236:239], v14 offset:0\n\t" \
    "ds_read_b128 v[240:243], v14 offset:512\n\t" \
    "ds_read_b128 v[244:247], v14 offset:1024\n\t" \
    "ds_read_b128 v[248:251], v14 offset:1536\n\t" \
    "ds_read_b128 v[232:235], v12 offset:24576\n\t" \
    "s_waitcnt lgkmcnt(0)\n\t" \
    "v_mfma_f32_16x16x32_bf16 v[220:223], v[232:235], v[236:239], 0\n\t" \
    "ds_read_b128 v[232:235], v12 offset:25600\n\t" \
    "s_waitcnt lgkmcnt(0)\n\t" \
    "v_mfma_f32_16x16x32_bf16 v[220:223], v[232:235], v[240:243], v[220:223]\n\t" \
    "ds_read_b128 v[232:235], v12 offset:26624\n\t" \
    "s_waitcnt lgkmcnt(0)\n\t" \
    "v_mfma_f32_16x16x32_bf16 v[220:223], v[232:235], v[244:247], v[220:223]\n\t" \
    "ds_read_b128 v[232:235], v12 offset:27648\n\t" \
    "s_waitcnt lgkmcnt(0)\n\t" \
    "v_mfma_f32_16x16x32_bf16 v[220:223], v[232:235], v[248:251], v[220:223]\n\t" \
    "ds_read_b128 v[232:235], v12 offset:28672\n\t" \
    "s_waitcnt lgkmcnt(0)\n\t" \
    "v_mfma_f32_16x16x32_bf16 v[224:227], v[232:235], v[236:239], 0\n\t" \
    "ds_read_b128 v[232:235], v12 offset:29696\n\t" \
    "s_waitcnt lgkmcnt(0)\n\t" \
    "v_mfma_f32_16x16x32_bf16 v[224:227], v[232:235], v[240:243], v[224:227]\n\t" \
    "ds_read_b128 v[232:235], v12 offset:30720\n\t" \
    "s_waitcnt lgkmcnt(0)\n\t" \
    "v_mfma_f32_16x16x32_bf16 v[224:227], v[232:235], v[244:247], v[224:227]\n\t" \
    "ds_read_b128 v[232:235], v12 offset:31744\n\t" \
    "s_waitcnt lgkmcnt(0)\n\t" \
    "v_mfma_f32_16x16x32_bf16 v[224:227], v[232:235], v[248:251], v[224:227]\n\t" \
    "ds_read_b128 v[232:235], v15 offset:0\n\t" \
    "s_waitcnt lgkmcnt(0)\n\t" \
    "v_mfma_f32_16x16x32_bf16 v[0:3], v[232:235], v[236:239], 0\n\t" \
    "ds_read_b128 v[232:235], v15 offset:512\n\t" \
    "s_waitcnt lgkmcnt(0)\n\t" \
    "v_mfma_f32_16x16x32_bf16 v[0:3], v[232:235], v[240:243], v[0:3]\n\t" \
    "ds_read_b128 v[232:235], v15 offset:1024\n\t" \
    "s_waitcnt lgkmcnt(0)\n\t" \
    "v_mfma_f32_16x16x32_bf16 v[0:3], v[232:235], v[244:247], v[0:3]\n\t" \
    "ds_read_b128 v[232:235], v15 offset:1536\n\t" \
    "s_waitcnt lgkmcnt(0)\n\t" \
    "v_mfma_f32_16x16x32_bf16 v[0:3], v[232:235], v[248:251], v[0:3]\n\t" \
    "v_max3_f32 v252, v220, v221, v222\n\t" \
    "v_max3_f32 v253, v223, v224, v225\n\t" \
    "v_max3_f32 v252, v252, v226, v227\n\t" \
    "s_nop 4\n\t" \
    "v_max3_f32 v253, v253, v0, v1\n\t" \
    "v_max_f32 v252, v252, v253\n\t" \
    "v_mov_b32 v253, v252\n\t" \
    "v_mov_b32 v2, v252\n\t" \
    "s_nop 1\n\t" \
    "v_permlane16_swap_b32 v253, v2\n\t" \
    "v_max_f32 v252, v253, v2\n\t" \
    "v_max_f32 v253, v253, v2\n\t" \
    "s_nop 1\n\t" \
    "v_permlane32_swap_b32 v252, v253\n\t" \
    "v_max_f32 v252, v252, v253\n\t" \
    "v_sub_f32 v220, v220, v252\n\t" \
    "v_sub_f32 v221, v221, v252\n\t" \
    "v_sub_f32 v222, v222, v252\n\t" \
    "v_sub_f32 v223, v223, v252\n\t" \
    "v_sub_f32 v224, v224, v252\n\t" \
    "v_sub_f32 v225, v225, v252\n\t" \
    "v_sub_f32 v226, v226, v252\n\t" \
    "v_sub_f32 v227, v227, v252\n\t" \
    "v_sub_f32 v0, v0, v252\n\t" \
    "v_sub_f32 v1, v1, v252\n\t" \
    "v_exp_f32 v220, v220\n\t" \
    "v_exp_f32 v221, v221\n\t" \
    "v_exp_f32 v222, v222\n\t" \
    "v_exp_f32 v223, v223\n\t" \
    "v_exp_f32 v224, v224\n\t" \
    "v_exp_f32 v225, v225\n\t" \
    "v_exp_f32 v226, v226\n\t" \
    "v_exp_f32 v227, v227\n\t" \
    "v_cvt_pk_bf16_f32 v220, v220, v221\n\t" \
    "v_cvt_pk_bf16_f32 v221, v222, v223\n\t" \
    "v_cvt_pk_bf16_f32 v222, v224, v225\n\t" \
    "v_cvt_pk_bf16_f32 v223, v226, v227\n\t" \
    "v_mov_b32 v224, %[one]\n\t" \
    "v_mov_b32 v225, %[one]\n\t" \
    "v_mov_b32 v226, %[one]\n\t" \
    "v_mov_b32 v227, %[one]\n\t" \
    "s_nop 1\n\t" \
    "v_mfma_f32_16x16x32_bf16 v[244:247], v[224:227], v[220:223], 0\n\t" \
    "v_mfma_f32_16x16x32_bf16 v[236:239], v[20:23], v[220:223], 0\n\t" \
    "v_mfma_f32_16x16x32_bf16 v[240:243], v[24:27], v[220:223], 0\n\t" \
    "v_exp_f32 v0, v0\n\t" \
    "v_exp_f32 v1, v1\n\t" \
    "s_nop 0\n\t" \
    "v_cvt_pk_bf16_f32 v228, v0, v1\n\t" \
    "v_cvt_pk_bf16_f32 v231, v0, v1\n\t" \
    "s_nop 1\n\t" \
    "s_branch CM_%=\n\t" \
    "E_%=:\n\t" \
    GSW \
    "s_nop 1\n\t" \
    : [lim] "+s"(lim), [rg] "+s"(rg), [tp] "=&s"(tp), [pt] "=&s"(pt), [pd] "=&s"(pd) DGO DGP GSO(0) GSO(1) GSO(2) GSO(3) GSO(4) \
    : "{v8}"(am_a), "{v9}"(t0_a), "{v10}"(idx), "{v11}"(iw_a), "{v12}"(ab_a), "{v14}"(qr_a), "{v15}"(kr_a), "{v16}"(fr_a), "{v17}"(vo_v), "{v18}"(oo_v), [rs] "s"(rs), [sb] "s"(sb), [tag] "s"(0xffffff81u + s), [hi] "s"(0xff00ff00ff00ff00ull), [tgt] "s"(tgt), [sc] "s"(wid), [onej] "s"(onej), [sl] "s"(0x3e0293ee), [one] "s"(0x3f803f80), [m0] "s"(sbm), [m1] "s"(sbm + 4096u), [vx] "s"(vxo), [fl] "s"(fb + FL_T(s + 1)), [vd] "s"(8192 + j * 2048), [md] "s"(md), "{v[28:35]}"(wr[0][0]), "{v[36:43]}"(wr[0][1]), "{v[44:51]}"(wr[1][0]), "{v[52:59]}"(wr[1][1]), "{v[60:67]}"(wr[2][0]), "{v[68:75]}"(wr[2][1]), "{v[76:83]}"(wr[3][0]), "{v[84:91]}"(wr[3][1]), "{v[92:99]}"(wr[4][0]), "{v[100:107]}"(wr[4][1]), "{v[108:115]}"(wr[5][0]), "{v[116:123]}"(wr[5][1]), "{v[124:131]}"(wr[6][0]), "{v[132:139]}"(wr[6][1]), "{v[140:147]}"(wr[7][0]), "{v[148:155]}"(wr[7][1]), "{v[156:163]}"(wr[8][0]), "{v[164:171]}"(wr[8][1]), "{v[172:179]}"(wr[9][0]), "{v[180:187]}"(wr[9][1]), "{v[188:195]}"(wr[10][0]), "{v[196:203]}"(wr[10][1]), "{v[204:211]}"(wr[11][0]), "{v[212:219]}"(wr[11][1]) \
    : "memory", "vcc", "scc", "s88", "s89", "s90", "s91", "s92", "s93", "v0", "v1", "v2", "v3", "v4", "v5", "v6", "v7", "v20", "v21", "v22", "v23", "v24", "v25", "v26", "v27", "v220", "v221", "v222", "v223", "v224", "v225", "v226", "v227", "v228", "v229", "v230", "v231", "v232", "v233", "v234", "v235", "v236", "v237", "v238", "v239", "v240", "v241", "v242", "v243", "v244", "v245", "v246", "v247", "v248", "v249", "v250", "v251", "v252", "v253", "v254", "v255")

#define A_MAIN_K() \
  asm volatile( \
    "ds_read_b128 v[236:239], v9 offset:0\n\t" \
    "ds_read_b128 v[240:243], v9 offset:1024\n\t" \
    "ds_read_b128 v[244:247], v9 offset:2048\n\t" \
    "ds_read_b128 v[248:251], v9 offset:3072\n\t" \
    "v_mov_b32 v0, 0\n\t" \
    "v_mov_b32 v1, 0\n\t" \
    "v_mov_b32 v2, 0\n\t" \
    "v_mov_b32 v3, 0\n\t" \
    "v_mov_b32 v4, 0\n\t" \
    "v_mov_b32 v5, 0\n\t" \
    "v_mov_b32 v6, 0\n\t" \
    "v_mov_b32 v7, 0\n\t" \
    "s_waitcnt lgkmcnt(0)\n\t" \
    "s_barrier\n\t" \
    "buffer_load_dwordx4 v[220:223], v8, %[rs], %[m0] offen sc1\n\t" \
    "buffer_load_dwordx4 v[224:227], v8, %[rs], %[m1] offen sc1\n\t" \
    GSA(3) \
    "v_mov_b32 v252, v252\n\t" \
    "s_nop 2\n\t" \
    "v_mov_b32 v252, v252\n\t" \
    "s_nop 2\n\t" \
    "v_mov_b32 v252, v252\n\t" \
    "s_nop 2\n\t" \
    "v_mov_b32 v252, v252\n\t" \
    "s_nop 2\n\t" \
    "v_mov_b32 v252, v252\n\t" \
    "s_nop 2\n\t" \
    "v_mov_b32 v252, v252\n\t" \
    "s_nop 2\n\t" \
    "v_mov_b32 v252, v252\n\t" \
    "s_nop 2\n\t" \
    "v_mov_b32 v252, v252\n\t" \
    "s_nop 2\n\t" \
    "v_mov_b32 v252, v252\n\t" \
    "s_nop 2\n\t" \
    "v_mov_b32 v252, v252\n\t" \
    "s_nop 2\n\t" \
    "v_mov_b32 v252, v252\n\t" \
    "s_nop 2\n\t" \
    "v_mov_b32 v252, v252\n\t" \
    "s_nop 2\n\t" \
    "v_mov_b32 v252, v252\n\t" \
    "s_nop 2\n\t" \
    "v_mov_b32 v252, v252\n\t" \
    "s_nop 2\n\t" \
    "v_mov_b32 v252, v252\n\t" \
    "s_nop 2\n\t" \
    "v_mov_b32 v252, v252\n\t" \
    "s_nop 2\n\t" \
    "v_mov_b32 v252, v252\n\t" \
    "s_nop 2\n\t" \
    "v_mov_b32 v252, v252\n\t" \
    "s_nop 2\n\t" \
    "v_mov_b32 v252, v252\n\t" \
    "s_nop 2\n\t" \
    "v_mov_b32 v252, v252\n\t" \
    "s_nop 2\n\t" \
    "v_mov_b32 v252, v252\n\t" \
    "s_nop 2\n\t" \
    "v_mov_b32 v252, v252\n\t" \
    "s_nop 2\n\t" \
    "v_mov_b32 v252, v252\n\t" \
    "s_nop 2\n\t" \
    "v_mov_b32 v252, v252\n\t" \
    "s_nop 2\n\t" \
    "v_mov_b32 v252, v252\n\t" \
    "s_nop 2\n\t" \
    "v_mov_b32 v252, v252\n\t" \
    "s_nop 2\n\t" \
    "v_mov_b32 v252, v252\n\t" \
    "s_nop 2\n\t" \
    "v_mov_b32 v252, v252\n\t" \
    "s_nop 2\n\t" \
    "v_mov_b32 v252, v252\n\t" \
    "s_nop 2\n\t" \
    "v_mov_b32 v252, v252\n\t" \
    "s_nop 2\n\t" \
    "PM_%=:\n\t" \
    "s_waitcnt vmcnt(1)\n\t" \
    "ds_write_b128 v8, v[220:223] offset:8192\n\t" \
    "v_max3_u32 v252, v220, v221, v222\n\t" \
    "s_waitcnt vmcnt(0)\n\t" \
    "ds_write_b128 v8, v[224:227] offset:12288\n\t" \
    "v_max3_u32 v252, v252, v223, v224\n\t" \
    "v_max3_u32 v252, v252, v225, v226\n\t" \
    "v_max_u32 v252, v252, v227\n\t" \
    "v_cmp_eq_u32 vcc, -1, v252\n\t" \
    "s_cbranch_vccz PD_%=\n\t" \
    "s_sub_u32 %[lim], %[lim], 1\n\t" \
    "s_cmp_gt_i32 %[lim], 0\n\t" \
    "s_cbranch_scc0 PD_%=\n\t" \
    "s_sleep " STR(CADM) "\n\t" \
    "buffer_load_dwordx4 v[220:223], v8, %[rs], %[m0] offen sc1\n\t" \
    "buffer_load_dwordx4 v[224:227], v8, %[rs], %[m1] offen sc1\n\t" \
    "s_branch PM_%=\n\t" \
    "PD_%=:\n\t" \
    GSA(0) \
    "v_smfmac_f32_16x16x64_bf16 v[0:3], v[220:223], v[236:243], v10\n\t" \
    "v_xor_b32 v9, 16384, v9\n\t" \
    "ds_read_b128 v[236:239], v9 offset:0\n\t" \
    "ds_read_b128 v[240:243], v9 offset:1024\n\t" \
    "v_smfmac_f32_16x16x64_bf16 v[4:7], v[220:223], v[244:251], v10\n\t" \
    "ds_read_b128 v[244:247], v9 offset:2048\n\t" \
    "ds_read_b128 v[248:251], v9 offset:3072\n\t" \
    "s_waitcnt lgkmcnt(4)\n\t" \
    "s_barrier\n\t" \
    DGA \
    "ds_read_b128 v[228:231], v8 offset:0\n\t" \
    "ds_read_b128 v[232:235], v8 offset:4096\n\t" \
    "v_smfmac_f32_16x16x64_bf16 v[0:3], v[224:227], v[148:155], v10\n\t" \
    "v_xor_b32 v8, 1024, v8\n\t" \
    "ds_read_b128 v[220:223], v8 offset:8192\n\t" \
    "v_smfmac_f32_16x16x64_bf16 v[4:7], v[224:227], v[156:163], v10\n\t" \
    "ds_read_b128 v[224:227], v8 offset:12288\n\t" \
    "buffer_store_dwordx4 v[212:215], v14, %[rs], %[ox] offen offset:0 sc1\n\t" \
    "buffer_store_dwordx4 v[216:219], v14, %[rs], %[ox] offen offset:512 sc1\n\t" \
    "s_waitcnt lgkmcnt(3)\n\t" \
    "v_smfmac_f32_16x16x64_bf16 v[0:3], v[228:231], v[20:27], v10\n\t" \
    "v_smfmac_f32_16x16x64_bf16 v[4:7], v[228:231], v[28:35], v10\n\t" \
    "ds_read_b128 v[228:231], v8 offset:0\n\t" \
    "s_waitcnt lgkmcnt(3)\n\t" \
    "v_smfmac_f32_16x16x64_bf16 v[0:3], v[232:235], v[84:91], v10\n\t" \
    "v_smfmac_f32_16x16x64_bf16 v[4:7], v[232:235], v[92:99], v10\n\t" \
    "ds_read_b128 v[232:235], v8 offset:4096\n\t" \
    "s_waitcnt lgkmcnt(3)\n\t" \
    "v_smfmac_f32_16x16x64_bf16 v[0:3], v[220:223], v[236:243], v10\n\t" \
    "v_xor_b32 v9, 49152, v9\n\t" \
    "ds_read_b128 v[236:239], v9 offset:0\n\t" \
    "v_smfmac_f32_16x16x64_bf16 v[4:7], v[220:223], v[244:251], v10\n\t" \
    "ds_read_b128 v[240:243], v9 offset:1024\n\t" \
    "s_waitcnt lgkmcnt(4)\n\t" \
    "v_smfmac_f32_16x16x64_bf16 v[0:3], v[224:227], v[164:171], v10\n\t" \
    "v_xor_b32 v8, 3072, v8\n\t" \
    "ds_read_b128 v[220:223], v8 offset:12288\n\t" \
    "v_smfmac_f32_16x16x64_bf16 v[4:7], v[224:227], v[172:179], v10\n\t" \
    "ds_read_b128 v[244:247], v9 offset:2048\n\t" \
    "s_waitcnt lgkmcnt(5)\n\t" \
    "v_smfmac_f32_16x16x64_bf16 v[0:3], v[228:231], v[36:43], v10\n\t" \
    "ds_read_b128 v[224:227], v8 offset:0\n\t" \
    "v_smfmac_f32_16x16x64_bf16 v[4:7], v[228:231], v[44:51], v10\n\t" \
    "ds_read_b128 v[248:251], v9 offset:3072\n\t" \
    "s_waitcnt lgkmcnt(6)\n\t" \
    "v_smfmac_f32_16x16x64_bf16 v[0:3], v[232:235], v[100:107], v10\n\t" \
    "ds_read_b128 v[228:231], v8 offset:8192\n\t" \
    "v_smfmac_f32_16x16x64_bf16 v[4:7], v[232:235], v[108:115], v10\n\t" \
    "ds_read_b128 v[232:235], v8 offset:4096\n\t" \
    "s_waitcnt lgkmcnt(5)\n\t" \
    "v_smfmac_f32_16x16x64_bf16 v[0:3], v[220:223], v[180:187], v10\n\t" \
    "v_smfmac_f32_16x16x64_bf16 v[4:7], v[220:223], v[188:195], v10\n\t" \
    "v_xor_b32 v8, 1024, v8\n\t" \
    "ds_read_b128 v[220:223], v8 offset:12288\n\t" \
    "s_waitcnt lgkmcnt(4)\n\t" \
    "v_smfmac_f32_16x16x64_bf16 v[0:3], v[224:227], v[52:59], v10\n\t" \
    "v_smfmac_f32_16x16x64_bf16 v[4:7], v[224:227], v[60:67], v10\n\t" \
    "ds_read_b128 v[224:227], v8 offset:0\n\t" \
    "s_waitcnt lgkmcnt(3)\n\t" \
    "v_smfmac_f32_16x16x64_bf16 v[0:3], v[228:231], v[236:243], v10\n\t" \
    "v_xor_b32 v9, 16384, v9\n\t" \
    "ds_read_b128 v[236:239], v9 offset:0\n\t" \
    "v_smfmac_f32_16x16x64_bf16 v[4:7], v[228:231], v[244:251], v10\n\t" \
    "ds_read_b128 v[240:243], v9 offset:1024\n\t" \
    "s_waitcnt lgkmcnt(4)\n\t" \
    "v_smfmac_f32_16x16x64_bf16 v[0:3], v[232:235], v[116:123], v10\n\t" \
    "ds_read_b128 v[228:231], v8 offset:4096\n\t" \
    "v_smfmac_f32_16x16x64_bf16 v[4:7], v[232:235], v[124:131], v10\n\t" \
    "ds_read_b128 v[244:247], v9 offset:2048\n\t" \
    "s_waitcnt lgkmcnt(5)\n\t" \
    "v_smfmac_f32_16x16x64_bf16 v[0:3], v[220:223], v[196:203], v10\n\t" \
    "ds_read_b128 v[232:235], v8 offset:8192\n\t" \
    "v_xor_b32 v8, 3072, v8\n\t" \
    "v_smfmac_f32_16x16x64_bf16 v[4:7], v[220:223], v[204:211], v10\n\t" \
    "ds_read_b128 v[248:251], v9 offset:3072\n\t" \
    "s_waitcnt lgkmcnt(6)\n\t" \
    "v_smfmac_f32_16x16x64_bf16 v[0:3], v[224:227], v[68:75], v10\n\t" \
    "v_smfmac_f32_16x16x64_bf16 v[4:7], v[224:227], v[76:83], v10\n\t" \
    "s_waitcnt lgkmcnt(3)\n\t" \
    "v_smfmac_f32_16x16x64_bf16 v[0:3], v[228:231], v[132:139], v10\n\t" \
    "v_smfmac_f32_16x16x64_bf16 v[4:7], v[228:231], v[140:147], v10\n\t" \
    "s_waitcnt lgkmcnt(0)\n\t" \
    "v_smfmac_f32_16x16x64_bf16 v[0:3], v[232:235], v[236:243], v10\n\t" \
    "v_xor_b32 v9, 49152, v9\n\t" \
    "v_smfmac_f32_16x16x64_bf16 v[4:7], v[232:235], v[244:251], v10\n\t" \
    "v_mov_b32 v253, %[stag]\n\t" \
    "s_nop 4\n\t" \
    "v_add_f32 v0, v0, v1\n\t" \
    "v_add_f32 v2, v2, v3\n\t" \
    "v_add_f32 v4, v4, v5\n\t" \
    "v_add_f32 v6, v6, v7\n\t" \
    "v_cvt_pk_bf16_f32 v0, v0, v4\n\t" \
    "v_cvt_pk_bf16_f32 v2, v2, v6\n\t" \
    "ds_write2_b32 v11, v0, v2 offset0:0 offset1:4\n\t" \
    "s_mov_b64 exec, %[onej]\n\t" \
    "ds_write_b32 v18, v253 offset:16\n\t" \
    "s_mov_b64 exec, -1\n\t" \
    GSA(1) \
    "s_setprio 0\n\t" \
    "buffer_load_dwordx4 v[236:239], v12, %[rs], %[qx] offen offset:0 sc1\n\t" \
    "buffer_load_dwordx4 v[240:243], v12, %[rs], %[qx] offen offset:1024 sc1\n\t" \
    "buffer_load_dwordx4 v[244:247], v12, %[rs], %[qx] offen offset:2048 sc1\n\t" \
    "buffer_load_dwordx4 v[248:251], v12, %[rs], %[qx] offen offset:3072 sc1\n\t" \
    "v_cmp_le_i32 s[72:73], 0, v16\n\t" \
    "v_cmp_le_i32 s[74:75], 1, v16\n\t" \
    "v_cmp_le_i32 s[76:77], 2, v16\n\t" \
    "v_cmp_le_i32 s[78:79], 3, v16\n\t" \
    "v_cmp_le_i32 s[80:81], 16, v16\n\t" \
    "v_cmp_le_i32 s[82:83], 17, v16\n\t" \
    "v_cmp_le_i32 s[84:85], 18, v16\n\t" \
    "v_cmp_le_i32 s[86:87], 19, v16\n\t" \
    "v_mov_b32 v254, v254\n\t" \
    "s_nop 2\n\t" \
    "v_mov_b32 v254, v254\n\t" \
    "s_nop 2\n\t" \
    "v_mov_b32 v254, v254\n\t" \
    "s_nop 2\n\t" \
    "v_mov_b32 v254, v254\n\t" \
    "s_nop 2\n\t" \
    "v_mov_b32 v254, v254\n\t" \
    "s_nop 2\n\t" \
    "v_mov_b32 v254, v254\n\t" \
    "s_nop 2\n\t" \
    "v_mov_b32 v254, v254\n\t" \
    "s_nop 2\n\t" \
    "v_mov_b32 v254, v254\n\t" \
    "s_nop 2\n\t" \
    "v_mov_b32 v254, v254\n\t" \
    "s_nop 2\n\t" \
    "v_mov_b32 v254, v254\n\t" \
    "s_nop 2\n\t" \
    "v_mov_b32 v254, v254\n\t" \
    "s_nop 2\n\t" \
    "v_mov_b32 v254, v254\n\t" \
    "s_nop 2\n\t" \
    "v_mov_b32 v254, v254\n\t" \
    "s_nop 2\n\t" \
    "v_mov_b32 v254, v254\n\t" \
    "s_nop 2\n\t" \
    "v_mov_b32 v254, v254\n\t" \
    "s_nop 2\n\t" \
    "v_mov_b32 v254, v254\n\t" \
    "s_nop 2\n\t" \
    "v_mov_b32 v254, v254\n\t" \
    "s_nop 2\n\t" \
    "v_mov_b32 v254, v254\n\t" \
    "s_nop 2\n\t" \
    "v_mov_b32 v254, v254\n\t" \
    "s_nop 2\n\t" \
    "v_mov_b32 v254, v254\n\t" \
    "s_nop 2\n\t" \
    "LW_%=:\n\t" \
    "ds_read_b32 v0, v18 offset:16\n\t" \
    "s_sub_u32 %[lim], %[lim], " STR(DEC1) "\n\t" \
    "s_cmp_gt_i32 %[lim], 0\n\t" \
    "v_mov_b32 v254, v254\n\t" \
    "s_nop 2\n\t" \
    "v_mov_b32 v254, v254\n\t" \
    "s_nop 2\n\t" \
    "v_mov_b32 v254, v254\n\t" \
    "s_nop 2\n\t" \
    "v_mov_b32 v254, v254\n\t" \
    "s_nop 2\n\t" \
    "v_mov_b32 v254, v254\n\t" \
    "s_nop 2\n\t" \
    "s_waitcnt lgkmcnt(0)\n\t" \
    "s_cbranch_scc0 CW_%=\n\t" \
    "v_cmp_ne_u32 vcc, v0, v253\n\t" \
    "s_cbranch_vccnz LW_%=\n\t" \
    "CW_%=:\n\t" \
    "ds_read_b128 v[212:215], v12 offset:24576\n\t" \
    "ds_read_b128 v[216:219], v12 offset:28672\n\t" \
    "ds_read_b128 v[220:223], v15 offset:0\n\t" \
    "ds_read_b128 v[224:227], v12 offset:25600\n\t" \
    "ds_read_b128 v[228:231], v12 offset:29696\n\t" \
    "v_add_u32 v253, %[se], v12\n\t" \
    "v_mov_b32 v254, v254\n\t" \
    "s_nop 2\n\t" \
    "v_mov_b32 v254, v254\n\t" \
    "s_nop 2\n\t" \
    "v_mov_b32 v254, v254\n\t" \
    "s_nop 2\n\t" \
    "v_mov_b32 v254, v254\n\t" \
    "s_nop 2\n\t" \
    "s_waitcnt vmcnt(0)\n\t" \
    "s_waitcnt lgkmcnt(4)\n\t" \
    "v_mfma_f32_16x16x32_bf16 v[0:3], v[212:215], v[236:239], 0\n\t" \
    "ds_read_b128 v[212:215], v15 offset:512\n\t" \
    "s_waitcnt lgkmcnt(4)\n\t" \
    "v_mfma_f32_16x16x32_bf16 v[4:7], v[216:219], v[236:239], 0\n\t" \
    "ds_read_b128 v[216:219], v12 offset:26624\n\t" \
    "s_waitcnt lgkmcnt(4)\n\t" \
    "v_mfma_f32_16x16x32_bf16 v[232:235], v[220:223], v[236:239], 0\n\t" \
    "ds_read_b128 v[220:223], v12 offset:30720\n\t" \
    "s_waitcnt lgkmcnt(4)\n\t" \
    "v_mfma_f32_16x16x32_bf16 v[0:3], v[224:227], v[240:243], v[0:3]\n\t" \
    "ds_read_b128 v[224:227], v15 offset:1024\n\t" \
    "s_waitcnt lgkmcnt(4)\n\t" \
    "v_mfma_f32_16x16x32_bf16 v[4:7], v[228:231], v[240:243], v[4:7]\n\t" \
    "ds_read_b128 v[228:231], v12 offset:27648\n\t" \
    "s_waitcnt lgkmcnt(4)\n\t" \
    "v_mfma_f32_16x16x32_bf16 v[232:235], v[212:215], v[240:243], v[232:235]\n\t" \
    "ds_read_b128 v[212:215], v12 offset:31744\n\t" \
    "s_waitcnt lgkmcnt(4)\n\t" \
    "v_mfma_f32_16x16x32_bf16 v[0:3], v[216:219], v[244:247], v[0:3]\n\t" \
    "ds_read_b128 v[216:219], v15 offset:1536\n\t" \
    "s_waitcnt lgkmcnt(4)\n\t" \
    "v_mfma_f32_16x16x32_bf16 v[4:7], v[220:223], v[244:247], v[4:7]\n\t" \
    "s_waitcnt lgkmcnt(3)\n\t" \
    "v_mfma_f32_16x16x32_bf16 v[232:235], v[224:227], v[244:247], v[232:235]\n\t" \
    "s_waitcnt lgkmcnt(2)\n\t" \
    "v_mfma_f32_16x16x32_bf16 v[0:3], v[228:231], v[248:251], v[0:3]\n\t" \
    "s_waitcnt lgkmcnt(1)\n\t" \
    "v_mfma_f32_16x16x32_bf16 v[4:7], v[212:215], v[248:251], v[4:7]\n\t" \
    "s_waitcnt lgkmcnt(0)\n\t" \
    "v_mfma_f32_16x16x32_bf16 v[232:235], v[216:219], v[248:251], v[232:235]\n\t" \
    "s_barrier\n\t" \
    GSA(4) \
    "ds_read_b128 v[236:239], v253 offset:8192\n\t" \
    "ds_read_b128 v[240:243], v253 offset:9216\n\t" \
    "ds_read_b128 v[244:247], v253 offset:10240\n\t" \
    "ds_read_b128 v[248:251], v253 offset:11264\n\t" \
    "ds_read2st64_b32 v[228:229], v17 offset0:0 offset1:1\n\t" \
    "ds_read2st64_b32 v[230:231], v17 offset0:2 offset1:3\n\t" \
    "s_mov_b64 exec, %[fm]\n\t" \
    "v_mov_b32 v220, %[st]\n\t" \
    "v_mov_b32 v221, %[tag]\n\t" \
    "v_mov_b32 v222, 0\n\t" \
    "buffer_store_dwordx2 v[220:221], v222, %[rs], %[po] offen sc1\n\t" \
    "s_mov_b64 exec, -1\n\t" \
    "v_mov_b32 v254, 0xff800000\n\t" \
    "v_cvt_pk_bf16_f32 v0, 0, v0\n\t" \
    "v_cvt_pk_bf16_f32 v1, 0, v1\n\t" \
    "v_cvt_pk_bf16_f32 v2, 0, v2\n\t" \
    "v_cvt_pk_bf16_f32 v3, 0, v3\n\t" \
    "v_cvt_pk_bf16_f32 v4, 0, v4\n\t" \
    "v_cvt_pk_bf16_f32 v5, 0, v5\n\t" \
    "v_cvt_pk_bf16_f32 v6, 0, v6\n\t" \
    "v_cvt_pk_bf16_f32 v7, 0, v7\n\t" \
    "v_cvt_pk_bf16_f32 v232, 0, v232\n\t" \
    "v_cvt_pk_bf16_f32 v233, 0, v233\n\t" \
    "v_mul_f32 v0, 0x3db504f3, v0\n\t" \
    "v_mul_f32 v1, 0x3db504f3, v1\n\t" \
    "v_mul_f32 v2, 0x3db504f3, v2\n\t" \
    "v_mul_f32 v3, 0x3db504f3, v3\n\t" \
    "v_mul_f32 v4, 0x3db504f3, v4\n\t" \
    "v_mul_f32 v5, 0x3db504f3, v5\n\t" \
    "v_mul_f32 v6, 0x3db504f3, v6\n\t" \
    "v_mul_f32 v7, 0x3db504f3, v7\n\t" \
    "v_mul_f32 v232, 0x3db504f3, v232\n\t" \
    "v_mul_f32 v233, 0x3db504f3, v233\n\t" \
    "v_cvt_pk_bf16_f32 v0, 0, v0\n\t" \
    "v_cvt_pk_bf16_f32 v1, 0, v1\n\t" \
    "v_cvt_pk_bf16_f32 v2, 0, v2\n\t" \
    "v_cvt_pk_bf16_f32 v3, 0, v3\n\t" \
    "v_cvt_pk_bf16_f32 v4, 0, v4\n\t" \
    "v_cvt_pk_bf16_f32 v5, 0, v5\n\t" \
    "v_cvt_pk_bf16_f32 v6, 0, v6\n\t" \
    "v_cvt_pk_bf16_f32 v7, 0, v7\n\t" \
    "v_cvt_pk_bf16_f32 v232, 0, v232\n\t" \
    "v_cvt_pk_bf16_f32 v233, 0, v233\n\t" \
    "v_cndmask_b32 v0, v254, v0, s[72:73]\n\t" \
    "v_cndmask_b32 v1, v254, v1, s[74:75]\n\t" \
    "v_cndmask_b32 v2, v254, v2, s[76:77]\n\t" \
    "v_cndmask_b32 v3, v254, v3, s[78:79]\n\t" \
    "v_cndmask_b32 v4, v254, v4, s[80:81]\n\t" \
    "v_cndmask_b32 v5, v254, v5, s[82:83]\n\t" \
    "v_cndmask_b32 v6, v254, v6, s[84:85]\n\t" \
    "v_cndmask_b32 v7, v254, v7, s[86:87]\n\t" \
    "v_max3_f32 v252, v0, v1, v2\n\t" \
    "v_max3_f32 v255, v3, v4, v5\n\t" \
    "v_max3_f32 v254, v6, v7, v232\n\t" \
    "v_max3_f32 v252, v252, v255, v254\n\t" \
    "v_max_f32 v252, v252, v233\n\t" \
    "v_mov_b32 v255, v252\n\t" \
    "v_mov_b32 v254, v252\n\t" \
    "s_nop 1\n\t" \
    "v_permlane16_swap_b32 v255, v254\n\t" \
    "v_max_f32 v252, v255, v254\n\t" \
    "v_max_f32 v255, v255, v254\n\t" \
    "s_nop 1\n\t" \
    "v_permlane32_swap_b32 v252, v255\n\t" \
    "v_max_f32 v252, v252, v255\n\t" \
    "v_mul_f32 v252, 0xbfb8aa3b, v252\n\t" \
    "v_fma_f32 v0, v0, %[l2e], v252\n\t" \
    "v_fma_f32 v1, v1, %[l2e], v252\n\t" \
    "v_fma_f32 v2, v2, %[l2e], v252\n\t" \
    "v_fma_f32 v3, v3, %[l2e], v252\n\t" \
    "v_fma_f32 v4, v4, %[l2e], v252\n\t" \
    "v_fma_f32 v5, v5, %[l2e], v252\n\t" \
    "v_fma_f32 v6, v6, %[l2e], v252\n\t" \
    "v_fma_f32 v7, v7, %[l2e], v252\n\t" \
    "v_fma_f32 v232, v232, %[l2e], v252\n\t" \
    "v_fma_f32 v233, v233, %[l2e], v252\n\t" \
    "v_exp_f32 v0, v0\n\t" \
    "v_exp_f32 v1, v1\n\t" \
    "v_exp_f32 v2, v2\n\t" \
    "v_exp_f32 v3, v3\n\t" \
    "v_exp_f32 v4, v4\n\t" \
    "v_exp_f32 v5, v5\n\t" \
    "v_exp_f32 v6, v6\n\t" \
    "v_exp_f32 v7, v7\n\t" \
    "v_exp_f32 v232, v232\n\t" \
    "v_exp_f32 v233, v233\n\t" \
    "v_add_f32 v252, v0, v1\n\t" \
    "v_add_f32 v235, v2, v3\n\t" \
    "v_add_f32 v254, v4, v5\n\t" \
    "v_add_f32 v255, v6, v7\n\t" \
    "v_add_f32 v234, v232, v233\n\t" \
    "v_add_f32 v252, v252, v235\n\t" \
    "v_add_f32 v254, v254, v255\n\t" \
    "v_add_f32 v252, v252, v254\n\t" \
    "v_add_f32 v252, v252, v234\n\t" \
    "v_mov_b32 v255, v252\n\t" \
    "v_mov_b32 v254, v252\n\t" \
    "s_nop 1\n\t" \
    "v_permlane16_swap_b32 v255, v254\n\t" \
    "v_add_f32 v252, v255, v254\n\t" \
    "v_add_f32 v255, v255, v254\n\t" \
    "s_nop 1\n\t" \
    "v_permlane32_swap_b32 v252, v255\n\t" \
    "v_add_f32 v252, v252, v255\n\t" \
    "v_rcp_f32 v252, v252\n\t" \
    "s_nop 0\n\t" \
    "v_mul_f32 v0, v0, v252\n\t" \
    "v_mul_f32 v1, v1, v252\n\t" \
    "v_mul_f32 v2, v2, v252\n\t" \
    "v_mul_f32 v3, v3, v252\n\t" \
    "v_mul_f32 v4, v4, v252\n\t" \
    "v_mul_f32 v5, v5, v252\n\t" \
    "v_mul_f32 v6, v6, v252\n\t" \
    "v_mul_f32 v7, v7, v252\n\t" \
    "v_mul_f32 v232, v232, v252\n\t" \
    "v_mul_f32 v233, v233, v252\n\t" \
    "v_cvt_pk_bf16_f32 v0, v0, v1\n\t" \
    "v_cvt_pk_bf16_f32 v1, v2, v3\n\t" \
    "v_cvt_pk_bf16_f32 v2, v4, v5\n\t" \
    "v_cvt_pk_bf16_f32 v3, v6, v7\n\t" \
    "v_cvt_pk_bf16_f32 v4, v232, v233\n\t" \
    "v_mov_b32 v5, 0\n\t" \
    "v_mov_b32 v6, 0\n\t" \
    "v_cvt_pk_bf16_f32 v7, v232, v233\n\t" \
    "s_waitcnt lgkmcnt(0)\n\t" \
    "v_mfma_f32_16x16x32_bf16 v[236:239], v[236:239], v[0:3], 0\n\t" \
    "v_mfma_f32_16x16x32_bf16 v[240:243], v[240:243], v[0:3], 0\n\t" \
    "v_mfma_f32_16x16x32_bf16 v[244:247], v[244:247], v[0:3], 0\n\t" \
    "v_mfma_f32_16x16x32_bf16 v[248:251], v[248:251], v[0:3], 0\n\t" \
    "v_mfma_f32_16x16x16_bf16 v[236:239], v[228:229], v[4:5], v[236:239]\n\t" \
    "v_mfma_f32_16x16x16_bf16 v[240:243], v[228:229], v[6:7], v[240:243]\n\t" \
    "v_mfma_f32_16x16x16_bf16 v[244:247], v[230:231], v[4:5], v[244:247]\n\t" \
    "v_mfma_f32_16x16x16_bf16 v[248:251], v[230:231], v[6:7], v[248:251]\n\t" \
    "s_nop 4\n\t" \
    "v_cvt_pk_bf16_f32 v212, v236, v237\n\t" \
    "v_cvt_pk_bf16_f32 v213, v240, v241\n\t" \
    "v_cvt_pk_bf16_f32 v214, v238, v239\n\t" \
    "v_cvt_pk_bf16_f32 v215, v242, v243\n\t" \
    "v_permlane16_swap_b32 v212, v213\n\t" \
    "s_nop 0\n\t" \
    "v_permlane16_swap_b32 v214, v215\n\t" \
    "v_cvt_pk_bf16_f32 v216, v244, v245\n\t" \
    "v_cvt_pk_bf16_f32 v217, v248, v249\n\t" \
    "v_cvt_pk_bf16_f32 v218, v246, v247\n\t" \
    "v_cvt_pk_bf16_f32 v219, v250, v251\n\t" \
    "v_permlane16_swap_b32 v216, v217\n\t" \
    "s_nop 0\n\t" \
    "v_permlane16_swap_b32 v218, v219\n\t" \
    "s_setprio 1\n\t" \
    GSA(2) \
    GSW \
    "s_nop 1\n\t" \
    : [lim] "+s"(lim), "+{v[212:219]}"(oxr) DGO GSO(0) GSO(1) GSO(2) GSO(3) GSO(4) \
    : "{v8}"(am_a), "{v9}"(t0_a), "{v10}"(idx), "{v11}"(iw_a), "{v12}"(ab_a), "{v14}"(xo_v), "{v15}"(kr_a), "{v16}"(dq), "{v17}"(vl_a), "{v18}"(fr_a), [stag] "s"(0xffffff81u + s), [onej] "s"(onej), [rs] "s"(rs), [m0] "s"(sbm + 16384u), [m1] "s"(sbm + 20480u), [st] "s"(s), [tag] "s"(P.tag), [po] "s"(FLG_OFF + F_PRG(4, h) * 128), [fm] "s"(fmask), [qx] "s"(qxo), [qd] "s"(lbase + LA_VM), [qh] "s"((j & 1) * 2048), [ox] "s"(oxo), [se] "s"((j & 1) * 4096), [l2e] "s"(0x3fb8aa3b), "{v[20:27]}"(wr[0][0]), "{v[28:35]}"(wr[0][1]), "{v[36:43]}"(wr[1][0]), "{v[44:51]}"(wr[1][1]), "{v[52:59]}"(wr[2][0]), "{v[60:67]}"(wr[2][1]), "{v[68:75]}"(wr[3][0]), "{v[76:83]}"(wr[3][1]), "{v[84:91]}"(wr[4][0]), "{v[92:99]}"(wr[4][1]), "{v[100:107]}"(wr[5][0]), "{v[108:115]}"(wr[5][1]), "{v[116:123]}"(wr[6][0]), "{v[124:131]}"(wr[6][1]), "{v[132:139]}"(wr[7][0]), "{v[140:147]}"(wr[7][1]), "{v[148:155]}"(wr[8][0]), "{v[156:163]}"(wr[8][1]), "{v[164:171]}"(wr[9][0]), "{v[172:179]}"(wr[9][1]), "{v[180:187]}"(wr[10][0]), "{v[188:195]}"(wr[10][1]), "{v[196:203]}"(wr[11][0]), "{v[204:211]}"(wr[11][1]) \
    : "memory", "scc", "vcc", "s90", "s91", "s72", "s73", "s74", "s75", "s76", "s77", "s78", "s79", "s80", "s81", "s82", "s83", "s84", "s85", "s86", "s87", "v0", "v1", "v2", "v3", "v4", "v5", "v6", "v7", "v220", "v221", "v222", "v223", "v224", "v225", "v226", "v227", "v228", "v229", "v230", "v231", "v232", "v233", "v234", "v235", "v236", "v237", "v238", "v239", "v240", "v241", "v242", "v243", "v244", "v245", "v246", "v247", "v248", "v249", "v250", "v251", "v252", "v253", "v254", "v255")

#define B_POLL() \
  asm volatile( \
    "TWB_%=:\n\t" \
    "s_memrealtime s[88:89]\n\t" \
    "s_memtime s[90:91]\n\t" \
    "s_waitcnt lgkmcnt(0)\n\t" \
    "s_lshl_b32 s88, s88, 2\n\t" \
    "s_sub_u32 s89, %[tgt], s88\n\t" \
    "s_cmp_gt_i32 s89, 16\n\t" \
    "s_cbranch_scc1 TWB_%=\n\t" \
    "s_mul_i32 s89, s89, 11\n\t" \
    "s_ashr_i32 s89, s89, 1\n\t" \
    "s_add_u32 s89, s89, s90\n\t" \
    "TCB_%=:\n\t" \
    "s_memtime s[92:93]\n\t" \
    "s_waitcnt lgkmcnt(0)\n\t" \
    "s_sub_u32 s93, s89, s92\n\t" \
    "s_cmp_gt_i32 s93, 0\n\t" \
    "s_cbranch_scc1 TCB_%=\n\t" \
    "buffer_load_dwordx4 v[220:223], %[l16], %[rs], %[m0] offen offset:0 sc1\n\t" \
    "buffer_load_dwordx4 v[224:227], %[l16], %[rs], %[m0] offen offset:1024 sc1\n\t" \
    "buffer_load_dwordx4 v[228:231], %[l16], %[rs], %[m1] offen offset:0 sc1\n\t" \
    "buffer_load_dwordx4 v[232:235], %[l16], %[rs], %[m1] offen offset:1024 sc1\n\t" \
    "v_mov_b32 v252, v252\n\t" \
    "s_nop 2\n\t" \
    "v_mov_b32 v252, v252\n\t" \
    "s_nop 2\n\t" \
    "v_mov_b32 v252, v252\n\t" \
    "s_nop 2\n\t" \
    "v_mov_b32 v252, v252\n\t" \
    "s_nop 2\n\t" \
    "v_mov_b32 v252, v252\n\t" \
    "s_nop 2\n\t" \
    "v_mov_b32 v252, v252\n\t" \
    "s_nop 2\n\t" \
    "v_mov_b32 v252, v252\n\t" \
    "s_nop 2\n\t" \
    "v_mov_b32 v252, v252\n\t" \
    "s_nop 2\n\t" \
    "v_mov_b32 v252, v252\n\t" \
    "s_nop 2\n\t" \
    "v_mov_b32 v252, v252\n\t" \
    "s_nop 2\n\t" \
    "v_mov_b32 v252, v252\n\t" \
    "s_nop 2\n\t" \
    "v_mov_b32 v252, v252\n\t" \
    "s_nop 2\n\t" \
    "v_mov_b32 v252, v252\n\t" \
    "s_nop 2\n\t" \
    "v_mov_b32 v252, v252\n\t" \
    "s_nop 2\n\t" \
    "v_mov_b32 v252, v252\n\t" \
    "s_nop 2\n\t" \
    "v_mov_b32 v252, v252\n\t" \
    "s_nop 2\n\t" \
    "v_mov_b32 v252, v252\n\t" \
    "s_nop 2\n\t" \
    "v_mov_b32 v252, v252\n\t" \
    "s_nop 2\n\t" \
    "v_mov_b32 v252, v252\n\t" \
    "s_nop 2\n\t" \
    "v_mov_b32 v252, v252\n\t" \
    "s_nop 2\n\t" \
    "v_mov_b32 v252, v252\n\t" \
    "s_nop 2\n\t" \
    "v_mov_b32 v252, v252\n\t" \
    "s_nop 2\n\t" \
    "v_mov_b32 v252, v252\n\t" \
    "s_nop 2\n\t" \
    "v_mov_b32 v252, v252\n\t" \
    "s_nop 2\n\t" \
    "PM_%=:\n\t" \
    "s_waitcnt vmcnt(3)\n\t" \
    "ds_write_b128 %[la], v[220:223] offset:0\n\t" \
    "v_max3_u32 v252, v220, v220, v221\n\t" \
    "v_max3_u32 v252, v252, v222, v223\n\t" \
    "s_waitcnt vmcnt(2)\n\t" \
    "ds_write_b128 %[la], v[224:227] offset:1024\n\t" \
    "v_max3_u32 v252, v252, v224, v225\n\t" \
    "v_max3_u32 v252, v252, v226, v227\n\t" \
    "s_waitcnt vmcnt(1)\n\t" \
    "ds_write_b128 %[la], v[228:231] offset:8192\n\t" \
    "v_max3_u32 v252, v252, v228, v229\n\t" \
    "v_max3_u32 v252, v252, v230, v231\n\t" \
    "s_waitcnt vmcnt(0)\n\t" \
    "ds_write_b128 %[la], v[232:235] offset:9216\n\t" \
    "v_max3_u32 v252, v252, v232, v233\n\t" \
    "v_max3_u32 v252, v252, v234, v235\n\t" \
    "v_cmp_eq_u32 vcc, -1, v252\n\t" \
    "s_cbranch_vccz PD_%=\n\t" \
    "s_sub_u32 %[lim], %[lim], 1\n\t" \
    "s_cmp_gt_i32 %[lim], 0\n\t" \
    "s_cbranch_scc0 PD_%=\n\t" \
    "s_sleep " STR(CADM) "\n\t" \
    "buffer_load_dwordx4 v[220:223], %[l16], %[rs], %[m0] offen offset:0 sc1\n\t" \
    "buffer_load_dwordx4 v[224:227], %[l16], %[rs], %[m0] offen offset:1024 sc1\n\t" \
    "buffer_load_dwordx4 v[228:231], %[l16], %[rs], %[m1] offen offset:0 sc1\n\t" \
    "buffer_load_dwordx4 v[232:235], %[l16], %[rs], %[m1] offen offset:1024 sc1\n\t" \
    "s_branch PM_%=\n\t" \
    "PD_%=:\n\t" \
    GSA(3) \
    "s_waitcnt lgkmcnt(0)\n\t" \
    "s_barrier\n\t" \
    : [lim] "+s"(lim) GSO(3) \
    : [l16] "v"(l16), [la] "v"(la), [rs] "s"(rs), [m0] "s"(mo), [m1] "s"(mo + 16384u), [tgt] "s"(tgt) \
    : "memory", "vcc", "scc", "v252", "s88", "s89", "s90", "s91", "s92", "s93", "v220", "v221", "v222", "v223", "v224", "v225", "v226", "v227", "v228", "v229", "v230", "v231", "v232", "v233", "v234", "v235")

#define T_WAIT() \
  asm volatile( \
    "TWC_%=:\n\t" \
    "s_memrealtime s[88:89]\n\t" \
    "s_memtime s[90:91]\n\t" \
    "s_waitcnt lgkmcnt(0)\n\t" \
    "s_lshl_b32 s88, s88, 2\n\t" \
    "s_sub_u32 s89, %[tgt], s88\n\t" \
    "s_cmp_gt_i32 s89, 16\n\t" \
    "s_cbranch_scc1 TWC_%=\n\t" \
    "s_mul_i32 s89, s89, 11\n\t" \
    "s_ashr_i32 s89, s89, 1\n\t" \
    "s_add_u32 s89, s89, s90\n\t" \
    "TCC_%=:\n\t" \
    "s_memtime s[92:93]\n\t" \
    "s_waitcnt lgkmcnt(0)\n\t" \
    "s_sub_u32 s93, s89, s92\n\t" \
    "s_cmp_gt_i32 s93, 0\n\t" \
    "s_cbranch_scc1 TCC_%=\n\t" \
    :  \
    : [tgt] "s"(tgt) \
    : "s88", "s89", "s90", "s91", "s92", "s93", "memory", "scc")

#define V_PROJ() \
  asm volatile( \
    "v_mov_b32 v0, 0\n\t" \
    "v_mov_b32 v1, 0\n\t" \
    "v_mov_b32 v2, 0\n\t" \
    "v_mov_b32 v3, 0\n\t" \
    "v_mov_b32 v4, 0\n\t" \
    "v_mov_b32 v5, 0\n\t" \
    "v_mov_b32 v6, 0\n\t" \
    "v_mov_b32 v7, 0\n\t" \
    "s_barrier\n\t" \
    "ds_read_b128 v[220:223], %[am] offset:0\n\t" \
    "ds_read_b128 v[224:227], %[am] offset:1024\n\t" \
    "ds_read_b128 v[228:231], %[am] offset:2048\n\t" \
    "ds_read_b128 v[232:235], %[am] offset:3072\n\t" \
    "ds_read_b128 v[236:239], %[am] offset:4096\n\t" \
    "ds_read_b128 v[240:243], %[am] offset:5120\n\t" \
    "ds_read_b128 v[244:247], %[am] offset:6144\n\t" \
    "ds_read_b128 v[248:251], %[am] offset:7168\n\t" \
    "s_waitcnt lgkmcnt(7)\n\t" \
    "v_smfmac_f32_16x16x64_bf16 v[0:3], v[220:223], v[28:35], %[idx]\n\t" \
    "ds_read_b128 v[220:223], %[am] offset:8192\n\t" \
    "s_waitcnt lgkmcnt(7)\n\t" \
    "v_smfmac_f32_16x16x64_bf16 v[4:7], v[224:227], v[36:43], %[idx]\n\t" \
    "ds_read_b128 v[224:227], %[am] offset:9216\n\t" \
    "s_waitcnt lgkmcnt(7)\n\t" \
    "v_smfmac_f32_16x16x64_bf16 v[0:3], v[228:231], v[44:51], %[idx]\n\t" \
    "ds_read_b128 v[228:231], %[am] offset:10240\n\t" \
    "s_waitcnt lgkmcnt(7)\n\t" \
    "v_smfmac_f32_16x16x64_bf16 v[4:7], v[232:235], v[52:59], %[idx]\n\t" \
    "ds_read_b128 v[232:235], %[am] offset:11264\n\t" \
    "s_waitcnt lgkmcnt(7)\n\t" \
    "v_smfmac_f32_16x16x64_bf16 v[0:3], v[236:239], v[60:67], %[idx]\n\t" \
    "ds_read_b128 v[236:239], %[am] offset:12288\n\t" \
    "s_waitcnt lgkmcnt(7)\n\t" \
    "v_smfmac_f32_16x16x64_bf16 v[4:7], v[240:243], v[68:75], %[idx]\n\t" \
    "ds_read_b128 v[240:243], %[am] offset:13312\n\t" \
    "s_waitcnt lgkmcnt(7)\n\t" \
    "v_smfmac_f32_16x16x64_bf16 v[0:3], v[244:247], v[76:83], %[idx]\n\t" \
    "ds_read_b128 v[244:247], %[am] offset:14336\n\t" \
    "s_waitcnt lgkmcnt(7)\n\t" \
    "v_smfmac_f32_16x16x64_bf16 v[4:7], v[248:251], v[84:91], %[idx]\n\t" \
    "ds_read_b128 v[248:251], %[am] offset:15360\n\t" \
    "s_waitcnt lgkmcnt(7)\n\t" \
    "v_smfmac_f32_16x16x64_bf16 v[0:3], v[220:223], v[92:99], %[idx]\n\t" \
    "s_waitcnt lgkmcnt(6)\n\t" \
    "v_smfmac_f32_16x16x64_bf16 v[4:7], v[224:227], v[100:107], %[idx]\n\t" \
    "s_waitcnt lgkmcnt(5)\n\t" \
    "v_smfmac_f32_16x16x64_bf16 v[0:3], v[228:231], v[108:115], %[idx]\n\t" \
    "s_waitcnt lgkmcnt(4)\n\t" \
    "v_smfmac_f32_16x16x64_bf16 v[4:7], v[232:235], v[116:123], %[idx]\n\t" \
    "s_waitcnt lgkmcnt(3)\n\t" \
    "v_smfmac_f32_16x16x64_bf16 v[0:3], v[236:239], v[124:131], %[idx]\n\t" \
    "s_waitcnt lgkmcnt(2)\n\t" \
    "v_smfmac_f32_16x16x64_bf16 v[4:7], v[240:243], v[132:139], %[idx]\n\t" \
    "s_waitcnt lgkmcnt(1)\n\t" \
    "v_smfmac_f32_16x16x64_bf16 v[0:3], v[244:247], v[140:147], %[idx]\n\t" \
    "s_waitcnt lgkmcnt(0)\n\t" \
    "v_smfmac_f32_16x16x64_bf16 v[4:7], v[248:251], v[148:155], %[idx]\n\t" \
    "s_nop 5\n\t" \
    "v_add_f32 v0, v0, v1\n\t" \
    "v_add_f32 v2, v2, v3\n\t" \
    "v_add_f32 v4, v4, v5\n\t" \
    "v_add_f32 v6, v6, v7\n\t" \
    "v_add_f32 v0, v0, v4\n\t" \
    "v_add_f32 v2, v2, v6\n\t" \
    "v_cvt_pk_bf16_f32 v0, v0, v2\n\t" \
    "v_min_u32 v0, -2, v0\n\t" \
    "s_nop 1\n\t" \
    "v_mov_b32_dpp v1, v0 quad_perm:[1,1,1,1] row_mask:0xf bank_mask:0xf\n\t" \
    "v_mov_b32_dpp v2, v0 quad_perm:[2,2,2,2] row_mask:0xf bank_mask:0xf\n\t" \
    "v_mov_b32_dpp v3, v0 quad_perm:[3,3,3,3] row_mask:0xf bank_mask:0xf\n\t" \
    "s_mov_b64 exec, %[qm]\n\t" \
    "buffer_store_dwordx4 v[0:3], %[l4], %[rs], %[vo] offen sc1\n\t" \
    "s_mov_b64 exec, -1\n\t" \
    "s_nop 1\n\t" \
    :  \
    : [am] "v"(am_a), [idx] "v"(idx), [l4] "v"(ln * 4), [rs] "s"(rs), [vo] "s"(sb + vo), [qm] "s"(0x1111111111111111ull), "{v[28:35]}"(wv[0]), "{v[36:43]}"(wv[1]), "{v[44:51]}"(wv[2]), "{v[52:59]}"(wv[3]), "{v[60:67]}"(wv[4]), "{v[68:75]}"(wv[5]), "{v[76:83]}"(wv[6]), "{v[84:91]}"(wv[7]), "{v[92:99]}"(wv[8]), "{v[100:107]}"(wv[9]), "{v[108:115]}"(wv[10]), "{v[116:123]}"(wv[11]), "{v[124:131]}"(wv[12]), "{v[132:139]}"(wv[13]), "{v[140:147]}"(wv[14]), "{v[148:155]}"(wv[15]) \
    : "memory", "v0", "v1", "v2", "v3", "v4", "v5", "v6", "v7", "v220", "v221", "v222", "v223", "v224", "v225", "v226", "v227", "v228", "v229", "v230", "v231", "v232", "v233", "v234", "v235", "v236", "v237", "v238", "v239", "v240", "v241", "v242", "v243", "v244", "v245", "v246", "v247", "v248", "v249", "v250", "v251")

#define WO_V() \
  asm volatile( \
    "v_mov_b32 v0, 0\n\t" \
    "v_mov_b32 v1, 0\n\t" \
    "v_mov_b32 v2, 0\n\t" \
    "v_mov_b32 v3, 0\n\t" \
    "v_mov_b32 v4, 0\n\t" \
    "v_mov_b32 v5, 0\n\t" \
    "v_mov_b32 v6, 0\n\t" \
    "v_mov_b32 v7, 0\n\t" \
    "v_mov_b32 v8, 0\n\t" \
    "v_mov_b32 v9, 0\n\t" \
    "v_mov_b32 v10, 0\n\t" \
    "v_mov_b32 v11, 0\n\t" \
    "v_mov_b32 v12, 0\n\t" \
    "v_mov_b32 v13, 0\n\t" \
    "v_mov_b32 v14, 0\n\t" \
    "v_mov_b32 v15, 0\n\t" \
    "v_mov_b32 v252, 0\n\t" \
    "s_barrier\n\t" \
    GSA(4) \
    DGC \
    "buffer_load_dwordx4 v[220:223], %[l16], %[rs], %[s0] offen sc1\n\t" \
    "buffer_load_dwordx4 v[224:227], %[l16], %[rs], %[s1] offen sc1\n\t" \
    "A_%=:\n\t" \
    "s_waitcnt vmcnt(1)\n\t" \
    "v_smfmac_f32_16x16x64_bf16 v[0:3], v[220:223], v[156:163], %[idx]\n\t" \
    "v_max3_u32 v252, v252, v220, v221\n\t" \
    "v_smfmac_f32_16x16x64_bf16 v[4:7], v[220:223], v[164:171], %[idx]\n\t" \
    "v_max3_u32 v252, v252, v222, v223\n\t" \
    "v_smfmac_f32_16x16x64_bf16 v[8:11], v[220:223], v[172:179], %[idx]\n\t" \
    "v_smfmac_f32_16x16x64_bf16 v[12:15], v[220:223], v[180:187], %[idx]\n\t" \
    "s_waitcnt vmcnt(0)\n\t" \
    "v_smfmac_f32_16x16x64_bf16 v[0:3], v[224:227], v[188:195], %[idx]\n\t" \
    "v_max3_u32 v252, v252, v224, v225\n\t" \
    "v_smfmac_f32_16x16x64_bf16 v[4:7], v[224:227], v[196:203], %[idx]\n\t" \
    "v_max3_u32 v252, v252, v226, v227\n\t" \
    "v_smfmac_f32_16x16x64_bf16 v[8:11], v[224:227], v[204:211], %[idx]\n\t" \
    "v_smfmac_f32_16x16x64_bf16 v[12:15], v[224:227], v[212:219], %[idx]\n\t" \
    "v_cmp_eq_u32 vcc, -1, v252\n\t" \
    "s_cbranch_vccnz SL_%=\n\t" \
    "D_%=:\n\t" \
    GSA(1) \
    "s_nop 2\n\t" \
    "v_add_f32 v0, v0, v1\n\t" \
    "v_add_f32 v2, v2, v3\n\t" \
    "v_add_f32 v4, v4, v5\n\t" \
    "v_add_f32 v6, v6, v7\n\t" \
    "v_add_f32 v8, v8, v9\n\t" \
    "v_add_f32 v10, v10, v11\n\t" \
    "v_add_f32 v12, v12, v13\n\t" \
    "v_add_f32 v14, v14, v15\n\t" \
    "v_permlane32_swap_b32 v0, v4\n\t" \
    "v_permlane32_swap_b32 v2, v6\n\t" \
    "v_permlane32_swap_b32 v8, v12\n\t" \
    "v_permlane32_swap_b32 v10, v14\n\t" \
    "v_add_f32 v0, v0, v12\n\t" \
    "v_add_f32 v2, v2, v14\n\t" \
    "ds_write2_b32 %[rw], v0, v2 offset0:0 offset1:32\n\t" \
    "s_waitcnt lgkmcnt(0)\n\t" \
    "s_barrier\n\t" \
    "s_cmp_eq_u32 %[red], 0\n\t" \
    "s_cbranch_scc0 E_%=\n\t" \
    "ds_read2st64_b64 v[0:3], %[rr] offset0:0 offset1:1\n\t" \
    "ds_read2st64_b64 v[4:7], %[rr] offset0:2 offset1:3\n\t" \
    "ds_read2st64_b64 v[8:11], %[rr] offset0:4 offset1:5\n\t" \
    "ds_read2st64_b64 v[12:15], %[rr] offset0:6 offset1:7\n\t" \
    "s_waitcnt lgkmcnt(3)\n\t" \
    "v_pk_add_f32 v[0:1], v[0:1], v[2:3]\n\t" \
    "s_waitcnt lgkmcnt(2)\n\t" \
    "v_pk_add_f32 v[4:5], v[4:5], v[6:7]\n\t" \
    "s_waitcnt lgkmcnt(1)\n\t" \
    "v_pk_add_f32 v[8:9], v[8:9], v[10:11]\n\t" \
    "v_pk_add_f32 v[0:1], v[0:1], v[4:5]\n\t" \
    "s_waitcnt lgkmcnt(0)\n\t" \
    "v_pk_add_f32 v[12:13], v[12:13], v[14:15]\n\t" \
    "s_nop 0\n\t" \
    "v_pk_add_f32 v[8:9], v[8:9], v[12:13]\n\t" \
    "s_nop 0\n\t" \
    "v_pk_add_f32 v[0:1], v[0:1], v[8:9]\n\t" \
    "s_nop 0\n\t" \
    "v_cvt_pk_bf16_f32 v0, v0, v1\n\t" \
    "v_min_u32 v0, -2, v0\n\t" \
    "buffer_store_dword v0, %[mo], %[rs], %[sn] offen sc1\n\t" \
    "s_memrealtime %[t1]\n\t" \
    DGA \
    "s_waitcnt lgkmcnt(0)\n\t" \
    GSA(2) \
    "s_branch E_%=\n\t" \
    "SL_%=:\n\t" \
    "s_sleep 1\n\t" \
    "buffer_load_dwordx4 v[220:223], %[l16], %[rs], %[s0] offen sc1\n\t" \
    "buffer_load_dwordx4 v[224:227], %[l16], %[rs], %[s1] offen sc1\n\t" \
    "s_sub_u32 %[lim], %[lim], " STR(DEC2) "\n\t" \
    "s_cmp_gt_i32 %[lim], 0\n\t" \
    "s_cbranch_scc0 G_%=\n\t" \
    "s_nop 3\n\t" \
    "v_mov_b32 v0, 0\n\t" \
    "v_mov_b32 v1, 0\n\t" \
    "v_mov_b32 v2, 0\n\t" \
    "v_mov_b32 v3, 0\n\t" \
    "v_mov_b32 v4, 0\n\t" \
    "v_mov_b32 v5, 0\n\t" \
    "v_mov_b32 v6, 0\n\t" \
    "v_mov_b32 v7, 0\n\t" \
    "v_mov_b32 v8, 0\n\t" \
    "v_mov_b32 v9, 0\n\t" \
    "v_mov_b32 v10, 0\n\t" \
    "v_mov_b32 v11, 0\n\t" \
    "v_mov_b32 v12, 0\n\t" \
    "v_mov_b32 v13, 0\n\t" \
    "v_mov_b32 v14, 0\n\t" \
    "v_mov_b32 v15, 0\n\t" \
    "v_mov_b32 v252, 0\n\t" \
    "s_branch A_%=\n\t" \
    "G_%=:\n\t" \
    "s_waitcnt vmcnt(0)\n\t" \
    "s_branch D_%=\n\t" \
    "E_%=:\n\t" \
    GSW \
    : [lim] "+s"(lim) GSO(1) GSO(4), [t1] "=&s"(t1) DGO DGQ GSO(2) \
    : [l16] "v"(l16), [rs] "s"(rs), [idx] "v"(idx), [rw] "v"(rw_a), [s0] "s"(so[0]), [s1] "s"(so[1]), [rr] "v"(rr_a), [mo] "v"(mo_v), [sn] "s"(sn), [red] "s"(wid), "{v[156:163]}"(wo[0][0][0]), "{v[164:171]}"(wo[0][0][1]), "{v[172:179]}"(wo[0][1][0]), "{v[180:187]}"(wo[0][1][1]), "{v[188:195]}"(wo[1][0][0]), "{v[196:203]}"(wo[1][0][1]), "{v[204:211]}"(wo[1][1][0]), "{v[212:219]}"(wo[1][1][1]) \
    : "memory", "vcc", "scc", "v252", "v0", "v1", "v2", "v3", "v4", "v5", "v6", "v7", "v8", "v9", "v10", "v11", "v12", "v13", "v14", "v15", "v220", "v221", "v222", "v223", "v224", "v225", "v226", "v227")

#define WO_W() \
  asm volatile( \
    "v_mov_b32 v0, 0\n\t" \
    "v_mov_b32 v1, 0\n\t" \
    "v_mov_b32 v2, 0\n\t" \
    "v_mov_b32 v3, 0\n\t" \
    "v_mov_b32 v4, 0\n\t" \
    "v_mov_b32 v5, 0\n\t" \
    "v_mov_b32 v6, 0\n\t" \
    "v_mov_b32 v7, 0\n\t" \
    "v_mov_b32 v8, 0\n\t" \
    "v_mov_b32 v9, 0\n\t" \
    "v_mov_b32 v10, 0\n\t" \
    "v_mov_b32 v11, 0\n\t" \
    "v_mov_b32 v12, 0\n\t" \
    "v_mov_b32 v13, 0\n\t" \
    "v_mov_b32 v14, 0\n\t" \
    "v_mov_b32 v15, 0\n\t" \
    "v_mov_b32 v252, 0\n\t" \
    "s_barrier\n\t" \
    GSA(4) \
    "buffer_load_dwordx4 v[220:223], %[l16], %[rs], %[s0] offen sc1\n\t" \
    PACE \
    "buffer_load_dwordx4 v[224:227], %[l16], %[rs], %[s1] offen sc1\n\t" \
    PACE \
    "buffer_load_dwordx4 v[228:231], %[l16], %[rs], %[s2] offen sc1\n\t" \
    PACE \
    "buffer_load_dwordx4 v[232:235], %[l16], %[rs], %[s3] offen sc1\n\t" \
    PACE \
    "buffer_load_dwordx4 v[236:239], %[l16], %[rs], %[s4] offen sc1\n\t" \
    PACE \
    "buffer_load_dwordx4 v[240:243], %[l16], %[rs], %[s5] offen sc1\n\t" \
    "A_%=:\n\t" \
    "s_waitcnt vmcnt(5)\n\t" \
    "v_smfmac_f32_16x16x64_bf16 v[0:3], v[220:223], v[28:35], %[idx]\n\t" \
    "v_max3_u32 v252, v252, v220, v221\n\t" \
    "v_smfmac_f32_16x16x64_bf16 v[4:7], v[220:223], v[36:43], %[idx]\n\t" \
    "v_max3_u32 v252, v252, v222, v223\n\t" \
    "v_smfmac_f32_16x16x64_bf16 v[8:11], v[220:223], v[44:51], %[idx]\n\t" \
    "v_smfmac_f32_16x16x64_bf16 v[12:15], v[220:223], v[52:59], %[idx]\n\t" \
    "s_waitcnt vmcnt(4)\n\t" \
    "v_smfmac_f32_16x16x64_bf16 v[0:3], v[224:227], v[60:67], %[idx]\n\t" \
    "v_max3_u32 v252, v252, v224, v225\n\t" \
    "v_smfmac_f32_16x16x64_bf16 v[4:7], v[224:227], v[68:75], %[idx]\n\t" \
    "v_max3_u32 v252, v252, v226, v227\n\t" \
    "v_smfmac_f32_16x16x64_bf16 v[8:11], v[224:227], v[76:83], %[idx]\n\t" \
    "v_smfmac_f32_16x16x64_bf16 v[12:15], v[224:227], v[84:91], %[idx]\n\t" \
    "s_waitcnt vmcnt(3)\n\t" \
    "v_smfmac_f32_16x16x64_bf16 v[0:3], v[228:231], v[92:99], %[idx]\n\t" \
    "v_max3_u32 v252, v252, v228, v229\n\t" \
    "v_smfmac_f32_16x16x64_bf16 v[4:7], v[228:231], v[100:107], %[idx]\n\t" \
    "v_max3_u32 v252, v252, v230, v231\n\t" \
    "v_smfmac_f32_16x16x64_bf16 v[8:11], v[228:231], v[108:115], %[idx]\n\t" \
    "v_smfmac_f32_16x16x64_bf16 v[12:15], v[228:231], v[116:123], %[idx]\n\t" \
    "s_waitcnt vmcnt(2)\n\t" \
    "v_smfmac_f32_16x16x64_bf16 v[0:3], v[232:235], v[124:131], %[idx]\n\t" \
    "v_max3_u32 v252, v252, v232, v233\n\t" \
    "v_smfmac_f32_16x16x64_bf16 v[4:7], v[232:235], v[132:139], %[idx]\n\t" \
    "v_max3_u32 v252, v252, v234, v235\n\t" \
    "v_smfmac_f32_16x16x64_bf16 v[8:11], v[232:235], v[140:147], %[idx]\n\t" \
    "v_smfmac_f32_16x16x64_bf16 v[12:15], v[232:235], v[148:155], %[idx]\n\t" \
    "s_waitcnt vmcnt(1)\n\t" \
    "v_smfmac_f32_16x16x64_bf16 v[0:3], v[236:239], v[156:163], %[idx]\n\t" \
    "v_max3_u32 v252, v252, v236, v237\n\t" \
    "v_smfmac_f32_16x16x64_bf16 v[4:7], v[236:239], v[164:171], %[idx]\n\t" \
    "v_max3_u32 v252, v252, v238, v239\n\t" \
    "v_smfmac_f32_16x16x64_bf16 v[8:11], v[236:239], v[172:179], %[idx]\n\t" \
    "v_smfmac_f32_16x16x64_bf16 v[12:15], v[236:239], v[180:187], %[idx]\n\t" \
    "s_waitcnt vmcnt(0)\n\t" \
    "v_smfmac_f32_16x16x64_bf16 v[0:3], v[240:243], v[188:195], %[idx]\n\t" \
    "v_max3_u32 v252, v252, v240, v241\n\t" \
    "v_smfmac_f32_16x16x64_bf16 v[4:7], v[240:243], v[196:203], %[idx]\n\t" \
    "v_max3_u32 v252, v252, v242, v243\n\t" \
    "v_smfmac_f32_16x16x64_bf16 v[8:11], v[240:243], v[204:211], %[idx]\n\t" \
    "v_smfmac_f32_16x16x64_bf16 v[12:15], v[240:243], v[212:219], %[idx]\n\t" \
    "v_cmp_eq_u32 vcc, -1, v252\n\t" \
    "s_cbranch_vccnz SL_%=\n\t" \
    "D_%=:\n\t" \
    GSA(1) \
    "s_nop 2\n\t" \
    "v_add_f32 v0, v0, v1\n\t" \
    "v_add_f32 v2, v2, v3\n\t" \
    "v_add_f32 v4, v4, v5\n\t" \
    "v_add_f32 v6, v6, v7\n\t" \
    "v_add_f32 v8, v8, v9\n\t" \
    "v_add_f32 v10, v10, v11\n\t" \
    "v_add_f32 v12, v12, v13\n\t" \
    "v_add_f32 v14, v14, v15\n\t" \
    "v_permlane32_swap_b32 v0, v4\n\t" \
    "v_permlane32_swap_b32 v2, v6\n\t" \
    "v_permlane32_swap_b32 v8, v12\n\t" \
    "v_permlane32_swap_b32 v10, v14\n\t" \
    "v_add_f32 v0, v0, v12\n\t" \
    "v_add_f32 v2, v2, v14\n\t" \
    "ds_write2_b32 %[rw], v0, v2 offset0:0 offset1:32\n\t" \
    "s_waitcnt lgkmcnt(0)\n\t" \
    "s_barrier\n\t" \
    "s_branch E_%=\n\t" \
    "SL_%=:\n\t" \
    "s_sleep 1\n\t" \
    "buffer_load_dwordx4 v[220:223], %[l16], %[rs], %[s0] offen sc1\n\t" \
    "buffer_load_dwordx4 v[224:227], %[l16], %[rs], %[s1] offen sc1\n\t" \
    "buffer_load_dwordx4 v[228:231], %[l16], %[rs], %[s2] offen sc1\n\t" \
    "buffer_load_dwordx4 v[232:235], %[l16], %[rs], %[s3] offen sc1\n\t" \
    "buffer_load_dwordx4 v[236:239], %[l16], %[rs], %[s4] offen sc1\n\t" \
    "buffer_load_dwordx4 v[240:243], %[l16], %[rs], %[s5] offen sc1\n\t" \
    "s_sub_u32 %[lim], %[lim], " STR(DEC2) "\n\t" \
    "s_cmp_gt_i32 %[lim], 0\n\t" \
    "s_cbranch_scc0 G_%=\n\t" \
    "s_nop 3\n\t" \
    "v_mov_b32 v0, 0\n\t" \
    "v_mov_b32 v1, 0\n\t" \
    "v_mov_b32 v2, 0\n\t" \
    "v_mov_b32 v3, 0\n\t" \
    "v_mov_b32 v4, 0\n\t" \
    "v_mov_b32 v5, 0\n\t" \
    "v_mov_b32 v6, 0\n\t" \
    "v_mov_b32 v7, 0\n\t" \
    "v_mov_b32 v8, 0\n\t" \
    "v_mov_b32 v9, 0\n\t" \
    "v_mov_b32 v10, 0\n\t" \
    "v_mov_b32 v11, 0\n\t" \
    "v_mov_b32 v12, 0\n\t" \
    "v_mov_b32 v13, 0\n\t" \
    "v_mov_b32 v14, 0\n\t" \
    "v_mov_b32 v15, 0\n\t" \
    "v_mov_b32 v252, 0\n\t" \
    "s_branch A_%=\n\t" \
    "G_%=:\n\t" \
    "s_waitcnt vmcnt(0)\n\t" \
    "s_branch D_%=\n\t" \
    "E_%=:\n\t" \
    GSW \
    : [lim] "+s"(lim) GSO(1) GSO(4) \
    : [l16] "v"(l16), [rs] "s"(rs), [idx] "v"(idx), [rw] "v"(rw_a), [s0] "s"(so[0]), [s1] "s"(so[1]), [s2] "s"(so[2]), [s3] "s"(so[3]), [s4] "s"(so[4]), [s5] "s"(so[5]), "{v[28:35]}"(wo[0][0][0]), "{v[36:43]}"(wo[0][0][1]), "{v[44:51]}"(wo[0][1][0]), "{v[52:59]}"(wo[0][1][1]), "{v[60:67]}"(wo[1][0][0]), "{v[68:75]}"(wo[1][0][1]), "{v[76:83]}"(wo[1][1][0]), "{v[84:91]}"(wo[1][1][1]), "{v[92:99]}"(wo[2][0][0]), "{v[100:107]}"(wo[2][0][1]), "{v[108:115]}"(wo[2][1][0]), "{v[116:123]}"(wo[2][1][1]), "{v[124:131]}"(wo[3][0][0]), "{v[132:139]}"(wo[3][0][1]), "{v[140:147]}"(wo[3][1][0]), "{v[148:155]}"(wo[3][1][1]), "{v[156:163]}"(wo[4][0][0]), "{v[164:171]}"(wo[4][0][1]), "{v[172:179]}"(wo[4][1][0]), "{v[180:187]}"(wo[4][1][1]), "{v[188:195]}"(wo[5][0][0]), "{v[196:203]}"(wo[5][0][1]), "{v[204:211]}"(wo[5][1][0]), "{v[212:219]}"(wo[5][1][1]) \
    : "memory", "vcc", "scc", "v252", "v0", "v1", "v2", "v3", "v4", "v5", "v6", "v7", "v8", "v9", "v10", "v11", "v12", "v13", "v14", "v15", "v220", "v221", "v222", "v223", "v224", "v225", "v226", "v227", "v228", "v229", "v230", "v231", "v232", "v233", "v234", "v235", "v236", "v237", "v238", "v239", "v240", "v241", "v242", "v243")

#define S_LOOP_K(TE_, FIN_) \
  asm volatile( \
    "s_cmp_eq_u32 %[kh], 0\n\t" \
    "s_cbranch_scc0 L1_%=\n\t" \
    "L0_%=:\n\t" \
    "s_setprio 0\n\t" \
    "s_nop 3\n\t" \
    "ds_write_b128 %[pa], v[196:199] offset:0\n\t" \
    "ds_write_b128 %[pa], v[200:203] offset:1024\n\t" \
    "ds_write_b128 %[pa], v[204:207] offset:2048\n\t" \
    "ds_write_b128 %[pa], v[208:211] offset:3072\n\t" \
    "s_sub_u32 %[a], %[t], 1\n\t" \
    "v_mov_b32 v53, %[tag]\n\t" \
    "v_mov_b32 v52, %[a]\n\t" \
    "s_sub_u32 %[b], %[t], 2\n\t" \
    "s_cmp_lt_u32 %[b], " STR(SQUICK) "\n\t" \
    "s_cselect_b64 exec, %[fm], 0\n\t" \
    "buffer_store_dwordx2 v[52:53], %[l16], %[rs], %[fo] offen sc1\n\t" \
    "s_mov_b64 exec, -1\n\t" \
    "s_add_u32 m0, %[sd], 49152\n\t" \
    "s_add_u32 %[a], %[sx], 81920\n\t" \
    "s_waitcnt lgkmcnt(0)\n\t" \
    "buffer_load_dwordx4 %[l16], %[rs], %[a] offen offset:0 sc1 lds\n\t" \
    "buffer_load_dwordx4 %[l16], %[rs], %[a] offen offset:1024 sc1 lds\n\t" \
    "buffer_load_dwordx4 %[l16], %[rs], %[a] offen offset:2048 sc1 lds\n\t" \
    "buffer_load_dwordx4 %[l16], %[rs], %[a] offen offset:3072 sc1 lds\n\t" \
    "v_mfma_f32_16x16x32_bf16 v[0:3], v[68:71], v[20:23], 0\n\t" \
    "ds_read_b128 v[44:47], %[xp] offset:6144\n\t" \
    "v_mfma_f32_16x16x32_bf16 v[4:7], v[76:79], v[20:23], 0\n\t" \
    "ds_read_b128 v[48:51], %[xp] offset:7168\n\t" \
    "v_mfma_f32_16x16x32_bf16 v[8:11], v[68:71], v[24:27], 0\n\t" \
    "v_mfma_f32_16x16x32_bf16 v[12:15], v[76:79], v[24:27], 0\n\t" \
    "v_mfma_f32_16x16x32_bf16 v[0:3], v[72:75], v[28:31], v[0:3]\n\t" \
    "ds_read_b128 v[20:23], %[xp] offset:8192\n\t" \
    "v_mfma_f32_16x16x32_bf16 v[4:7], v[80:83], v[28:31], v[4:7]\n\t" \
    "ds_read_b128 v[24:27], %[xp] offset:9216\n\t" \
    "v_mfma_f32_16x16x32_bf16 v[8:11], v[72:75], v[32:35], v[8:11]\n\t" \
    "v_mfma_f32_16x16x32_bf16 v[12:15], v[80:83], v[32:35], v[12:15]\n\t" \
    "v_mfma_f32_16x16x32_bf16 v[0:3], v[84:87], v[36:39], v[0:3]\n\t" \
    "ds_read_b128 v[28:31], %[xp] offset:10240\n\t" \
    "v_mfma_f32_16x16x32_bf16 v[4:7], v[92:95], v[36:39], v[4:7]\n\t" \
    "ds_read_b128 v[32:35], %[xp] offset:11264\n\t" \
    "v_mfma_f32_16x16x32_bf16 v[8:11], v[84:87], v[40:43], v[8:11]\n\t" \
    "s_waitcnt lgkmcnt(4)\n\t" \
    "v_mfma_f32_16x16x32_bf16 v[12:15], v[92:95], v[40:43], v[12:15]\n\t" \
    "v_mfma_f32_16x16x32_bf16 v[0:3], v[88:91], v[44:47], v[0:3]\n\t" \
    "ds_read_b128 v[36:39], %[xp] offset:12288\n\t" \
    "v_mfma_f32_16x16x32_bf16 v[4:7], v[96:99], v[44:47], v[4:7]\n\t" \
    "ds_read_b128 v[40:43], %[xp] offset:13312\n\t" \
    "v_mfma_f32_16x16x32_bf16 v[8:11], v[88:91], v[48:51], v[8:11]\n\t" \
    "s_waitcnt lgkmcnt(4)\n\t" \
    "v_mfma_f32_16x16x32_bf16 v[12:15], v[96:99], v[48:51], v[12:15]\n\t" \
    "v_mfma_f32_16x16x32_bf16 v[0:3], v[100:103], v[20:23], v[0:3]\n\t" \
    "ds_read_b128 v[44:47], %[xp] offset:14336\n\t" \
    "v_mfma_f32_16x16x32_bf16 v[4:7], v[108:111], v[20:23], v[4:7]\n\t" \
    "ds_read_b128 v[48:51], %[xp] offset:15360\n\t" \
    "v_mfma_f32_16x16x32_bf16 v[8:11], v[100:103], v[24:27], v[8:11]\n\t" \
    "s_waitcnt lgkmcnt(4)\n\t" \
    "v_mfma_f32_16x16x32_bf16 v[12:15], v[108:111], v[24:27], v[12:15]\n\t" \
    "v_mfma_f32_16x16x32_bf16 v[0:3], v[104:107], v[28:31], v[0:3]\n\t" \
    "ds_read_b128 v[20:23], %[xp] offset:16384\n\t" \
    "v_mfma_f32_16x16x32_bf16 v[4:7], v[112:115], v[28:31], v[4:7]\n\t" \
    "ds_read_b128 v[24:27], %[xp] offset:17408\n\t" \
    "v_mfma_f32_16x16x32_bf16 v[8:11], v[104:107], v[32:35], v[8:11]\n\t" \
    "s_waitcnt lgkmcnt(4)\n\t" \
    "v_mfma_f32_16x16x32_bf16 v[12:15], v[112:115], v[32:35], v[12:15]\n\t" \
    "v_mfma_f32_16x16x32_bf16 v[0:3], v[116:119], v[36:39], v[0:3]\n\t" \
    "ds_read_b128 v[28:31], %[xp] offset:18432\n\t" \
    "v_mfma_f32_16x16x32_bf16 v[4:7], v[124:127], v[36:39], v[4:7]\n\t" \
    "ds_read_b128 v[32:35], %[xp] offset:19456\n\t" \
    "v_mfma_f32_16x16x32_bf16 v[8:11], v[116:119], v[40:43], v[8:11]\n\t" \
    "s_waitcnt lgkmcnt(4)\n\t" \
    "v_mfma_f32_16x16x32_bf16 v[12:15], v[124:127], v[40:43], v[12:15]\n\t" \
    "v_mfma_f32_16x16x32_bf16 v[0:3], v[120:123], v[44:47], v[0:3]\n\t" \
    "ds_read_b128 v[36:39], %[xp] offset:20480\n\t" \
    "v_mfma_f32_16x16x32_bf16 v[4:7], v[128:131], v[44:47], v[4:7]\n\t" \
    "ds_read_b128 v[40:43], %[xp] offset:21504\n\t" \
    "v_mfma_f32_16x16x32_bf16 v[8:11], v[120:123], v[48:51], v[8:11]\n\t" \
    "s_waitcnt lgkmcnt(4)\n\t" \
    "v_mfma_f32_16x16x32_bf16 v[12:15], v[128:131], v[48:51], v[12:15]\n\t" \
    "s_waitcnt vmcnt(4)\n\t" \
    "s_barrier\n\t" \
    "s_setprio 0\n\t" \
    "s_sub_u32 %[a], %[t], 1\n\t" \
    "v_mov_b32 v53, %[tag]\n\t" \
    "v_mov_b32 v52, %[a]\n\t" \
    "s_cmp_gt_u32 %[t], 1\n\t" \
    "s_cselect_b64 exec, %[fm], 0\n\t" \
    "buffer_store_dwordx2 v[52:53], %[l16], %[rs], %[fo] offen sc1\n\t" \
    "s_mov_b64 exec, %[pm]\n\t" \
    "s_mov_b32 m0, %[pd]\n\t" \
    "s_add_u32 %[a], %[sx], %[po]\n\t" \
    "buffer_load_dwordx4 %[l16], %[rs], %[a] offen sc1 lds\n\t" \
    "s_mov_b64 exec, -1\n\t" \
    "s_add_u32 m0, %[sd], 0\n\t" \
    "s_add_u32 %[a], %[sx], 131072\n\t" \
    "buffer_load_dwordx4 %[l16], %[rs], %[a] offen offset:0 sc1 lds\n\t" \
    "buffer_load_dwordx4 %[l16], %[rs], %[a] offen offset:1024 sc1 lds\n\t" \
    "buffer_load_dwordx4 %[l16], %[rs], %[a] offen offset:2048 sc1 lds\n\t" \
    "buffer_load_dwordx4 %[l16], %[rs], %[a] offen offset:3072 sc1 lds\n\t" \
    "v_mfma_f32_16x16x32_bf16 v[0:3], v[132:135], v[20:23], v[0:3]\n\t" \
    "ds_read_b128 v[44:47], %[xp] offset:22528\n\t" \
    "v_mfma_f32_16x16x32_bf16 v[4:7], v[140:143], v[20:23], v[4:7]\n\t" \
    "ds_read_b128 v[48:51], %[xp] offset:23552\n\t" \
    "v_mfma_f32_16x16x32_bf16 v[8:11], v[132:135], v[24:27], v[8:11]\n\t" \
    "s_waitcnt lgkmcnt(4)\n\t" \
    "v_mfma_f32_16x16x32_bf16 v[12:15], v[140:143], v[24:27], v[12:15]\n\t" \
    "v_mfma_f32_16x16x32_bf16 v[0:3], v[136:139], v[28:31], v[0:3]\n\t" \
    "ds_read_b128 v[20:23], %[xp] offset:24576\n\t" \
    "v_mfma_f32_16x16x32_bf16 v[4:7], v[144:147], v[28:31], v[4:7]\n\t" \
    "ds_read_b128 v[24:27], %[xp] offset:25600\n\t" \
    "v_mfma_f32_16x16x32_bf16 v[8:11], v[136:139], v[32:35], v[8:11]\n\t" \
    "s_waitcnt lgkmcnt(4)\n\t" \
    "v_mfma_f32_16x16x32_bf16 v[12:15], v[144:147], v[32:35], v[12:15]\n\t" \
    "v_mfma_f32_16x16x32_bf16 v[0:3], v[148:151], v[36:39], v[0:3]\n\t" \
    "ds_read_b128 v[28:31], %[xp] offset:26624\n\t" \
    "v_mfma_f32_16x16x32_bf16 v[4:7], v[156:159], v[36:39], v[4:7]\n\t" \
    "ds_read_b128 v[32:35], %[xp] offset:27648\n\t" \
    "v_mfma_f32_16x16x32_bf16 v[8:11], v[148:151], v[40:43], v[8:11]\n\t" \
    "s_waitcnt lgkmcnt(4)\n\t" \
    "v_mfma_f32_16x16x32_bf16 v[12:15], v[156:159], v[40:43], v[12:15]\n\t" \
    "v_mfma_f32_16x16x32_bf16 v[0:3], v[152:155], v[44:47], v[0:3]\n\t" \
    "ds_read_b128 v[36:39], %[xp] offset:28672\n\t" \
    "v_mfma_f32_16x16x32_bf16 v[4:7], v[160:163], v[44:47], v[4:7]\n\t" \
    "ds_read_b128 v[40:43], %[xp] offset:29696\n\t" \
    "v_mfma_f32_16x16x32_bf16 v[8:11], v[152:155], v[48:51], v[8:11]\n\t" \
    "s_waitcnt lgkmcnt(4)\n\t" \
    "v_mfma_f32_16x16x32_bf16 v[12:15], v[160:163], v[48:51], v[12:15]\n\t" \
    "v_mfma_f32_16x16x32_bf16 v[0:3], v[164:167], v[20:23], v[0:3]\n\t" \
    "ds_read_b128 v[44:47], %[xp] offset:30720\n\t" \
    "v_mfma_f32_16x16x32_bf16 v[4:7], v[172:175], v[20:23], v[4:7]\n\t" \
    "ds_read_b128 v[48:51], %[xp] offset:31744\n\t" \
    "v_mfma_f32_16x16x32_bf16 v[8:11], v[164:167], v[24:27], v[8:11]\n\t" \
    "s_waitcnt lgkmcnt(4)\n\t" \
    "v_mfma_f32_16x16x32_bf16 v[12:15], v[172:175], v[24:27], v[12:15]\n\t" \
    "v_mfma_f32_16x16x32_bf16 v[0:3], v[168:171], v[28:31], v[0:3]\n\t" \
    "ds_read_b128 v[20:23], %[xp] offset:32768\n\t" \
    "v_mfma_f32_16x16x32_bf16 v[4:7], v[176:179], v[28:31], v[4:7]\n\t" \
    "ds_read_b128 v[24:27], %[xp] offset:33792\n\t" \
    "v_mfma_f32_16x16x32_bf16 v[8:11], v[168:171], v[32:35], v[8:11]\n\t" \
    "s_waitcnt lgkmcnt(4)\n\t" \
    "v_mfma_f32_16x16x32_bf16 v[12:15], v[176:179], v[32:35], v[12:15]\n\t" \
    "v_mfma_f32_16x16x32_bf16 v[0:3], v[180:183], v[36:39], v[0:3]\n\t" \
    "ds_read_b128 v[28:31], %[xp] offset:34816\n\t" \
    "v_mfma_f32_16x16x32_bf16 v[4:7], v[188:191], v[36:39], v[4:7]\n\t" \
    "ds_read_b128 v[32:35], %[xp] offset:35840\n\t" \
    "v_mfma_f32_16x16x32_bf16 v[8:11], v[180:183], v[40:43], v[8:11]\n\t" \
    "s_waitcnt lgkmcnt(4)\n\t" \
    "v_mfma_f32_16x16x32_bf16 v[12:15], v[188:191], v[40:43], v[12:15]\n\t" \
    "v_mfma_f32_16x16x32_bf16 v[0:3], v[184:187], v[44:47], v[0:3]\n\t" \
    "ds_read_b128 v[36:39], %[xp] offset:36864\n\t" \
    "v_mfma_f32_16x16x32_bf16 v[4:7], v[192:195], v[44:47], v[4:7]\n\t" \
    "ds_read_b128 v[40:43], %[xp] offset:37888\n\t" \
    "v_mfma_f32_16x16x32_bf16 v[8:11], v[184:187], v[48:51], v[8:11]\n\t" \
    "s_waitcnt lgkmcnt(4)\n\t" \
    "v_mfma_f32_16x16x32_bf16 v[12:15], v[192:195], v[48:51], v[12:15]\n\t" \
    "s_add_u32 %[sx], %[sx], 65536\n\t" \
    "s_add_u32 %[t], %[t], 1\n\t" \
    "s_waitcnt vmcnt(4)\n\t" \
    "s_barrier\n\t" \
    "s_setprio 0\n\t" \
    "s_nop 3\n\t" \
    "ds_write_b128 %[pa], v[0:3] offset:0\n\t" \
    "ds_write_b128 %[pa], v[4:7] offset:1024\n\t" \
    "ds_write_b128 %[pa], v[8:11] offset:2048\n\t" \
    "ds_write_b128 %[pa], v[12:15] offset:3072\n\t" \
    "s_sub_u32 %[a], %[t], 1\n\t" \
    "v_mov_b32 v53, %[tag]\n\t" \
    "v_mov_b32 v52, %[a]\n\t" \
    "s_sub_u32 %[b], %[t], 2\n\t" \
    "s_cmp_lt_u32 %[b], " STR(SQUICK) "\n\t" \
    "s_cselect_b64 exec, %[fm], 0\n\t" \
    "buffer_store_dwordx2 v[52:53], %[l16], %[rs], %[fo] offen sc1\n\t" \
    "s_mov_b64 exec, -1\n\t" \
    "s_add_u32 m0, %[sd], 16384\n\t" \
    "s_add_u32 %[a], %[sx], 81920\n\t" \
    "s_waitcnt lgkmcnt(0)\n\t" \
    "buffer_load_dwordx4 %[l16], %[rs], %[a] offen offset:0 sc1 lds\n\t" \
    "buffer_load_dwordx4 %[l16], %[rs], %[a] offen offset:1024 sc1 lds\n\t" \
    "buffer_load_dwordx4 %[l16], %[rs], %[a] offen offset:2048 sc1 lds\n\t" \
    "buffer_load_dwordx4 %[l16], %[rs], %[a] offen offset:3072 sc1 lds\n\t" \
    "v_mfma_f32_16x16x32_bf16 v[196:199], v[68:71], v[20:23], 0\n\t" \
    "ds_read_b128 v[44:47], %[xp] offset:38912\n\t" \
    "v_mfma_f32_16x16x32_bf16 v[200:203], v[76:79], v[20:23], 0\n\t" \
    "ds_read_b128 v[48:51], %[xp] offset:39936\n\t" \
    "v_mfma_f32_16x16x32_bf16 v[204:207], v[68:71], v[24:27], 0\n\t" \
    "v_mfma_f32_16x16x32_bf16 v[208:211], v[76:79], v[24:27], 0\n\t" \
    "v_mfma_f32_16x16x32_bf16 v[196:199], v[72:75], v[28:31], v[196:199]\n\t" \
    "ds_read_b128 v[20:23], %[xp] offset:40960\n\t" \
    "v_mfma_f32_16x16x32_bf16 v[200:203], v[80:83], v[28:31], v[200:203]\n\t" \
    "ds_read_b128 v[24:27], %[xp] offset:41984\n\t" \
    "v_mfma_f32_16x16x32_bf16 v[204:207], v[72:75], v[32:35], v[204:207]\n\t" \
    "v_mfma_f32_16x16x32_bf16 v[208:211], v[80:83], v[32:35], v[208:211]\n\t" \
    "v_mfma_f32_16x16x32_bf16 v[196:199], v[84:87], v[36:39], v[196:199]\n\t" \
    "ds_read_b128 v[28:31], %[xp] offset:43008\n\t" \
    "v_mfma_f32_16x16x32_bf16 v[200:203], v[92:95], v[36:39], v[200:203]\n\t" \
    "ds_read_b128 v[32:35], %[xp] offset:44032\n\t" \
    "v_mfma_f32_16x16x32_bf16 v[204:207], v[84:87], v[40:43], v[204:207]\n\t" \
    "s_waitcnt lgkmcnt(4)\n\t" \
    "v_mfma_f32_16x16x32_bf16 v[208:211], v[92:95], v[40:43], v[208:211]\n\t" \
    "v_mfma_f32_16x16x32_bf16 v[196:199], v[88:91], v[44:47], v[196:199]\n\t" \
    "ds_read_b128 v[36:39], %[xp] offset:45056\n\t" \
    "v_mfma_f32_16x16x32_bf16 v[200:203], v[96:99], v[44:47], v[200:203]\n\t" \
    "ds_read_b128 v[40:43], %[xp] offset:46080\n\t" \
    "v_mfma_f32_16x16x32_bf16 v[204:207], v[88:91], v[48:51], v[204:207]\n\t" \
    "s_waitcnt lgkmcnt(4)\n\t" \
    "v_mfma_f32_16x16x32_bf16 v[208:211], v[96:99], v[48:51], v[208:211]\n\t" \
    "v_mfma_f32_16x16x32_bf16 v[196:199], v[100:103], v[20:23], v[196:199]\n\t" \
    "ds_read_b128 v[44:47], %[xp] offset:47104\n\t" \
    "v_mfma_f32_16x16x32_bf16 v[200:203], v[108:111], v[20:23], v[200:203]\n\t" \
    "ds_read_b128 v[48:51], %[xp] offset:48128\n\t" \
    "v_mfma_f32_16x16x32_bf16 v[204:207], v[100:103], v[24:27], v[204:207]\n\t" \
    "s_waitcnt lgkmcnt(4)\n\t" \
    "v_mfma_f32_16x16x32_bf16 v[208:211], v[108:111], v[24:27], v[208:211]\n\t" \
    "v_mfma_f32_16x16x32_bf16 v[196:199], v[104:107], v[28:31], v[196:199]\n\t" \
    "ds_read_b128 v[20:23], %[xp] offset:49152\n\t" \
    "v_mfma_f32_16x16x32_bf16 v[200:203], v[112:115], v[28:31], v[200:203]\n\t" \
    "ds_read_b128 v[24:27], %[xp] offset:50176\n\t" \
    "v_mfma_f32_16x16x32_bf16 v[204:207], v[104:107], v[32:35], v[204:207]\n\t" \
    "s_waitcnt lgkmcnt(4)\n\t" \
    "v_mfma_f32_16x16x32_bf16 v[208:211], v[112:115], v[32:35], v[208:211]\n\t" \
    "v_mfma_f32_16x16x32_bf16 v[196:199], v[116:119], v[36:39], v[196:199]\n\t" \
    "ds_read_b128 v[28:31], %[xp] offset:51200\n\t" \
    "v_mfma_f32_16x16x32_bf16 v[200:203], v[124:127], v[36:39], v[200:203]\n\t" \
    "ds_read_b128 v[32:35], %[xp] offset:52224\n\t" \
    "v_mfma_f32_16x16x32_bf16 v[204:207], v[116:119], v[40:43], v[204:207]\n\t" \
    "s_waitcnt lgkmcnt(4)\n\t" \
    "v_mfma_f32_16x16x32_bf16 v[208:211], v[124:127], v[40:43], v[208:211]\n\t" \
    "v_mfma_f32_16x16x32_bf16 v[196:199], v[120:123], v[44:47], v[196:199]\n\t" \
    "ds_read_b128 v[36:39], %[xp] offset:53248\n\t" \
    "v_mfma_f32_16x16x32_bf16 v[200:203], v[128:131], v[44:47], v[200:203]\n\t" \
    "ds_read_b128 v[40:43], %[xp] offset:54272\n\t" \
    "v_mfma_f32_16x16x32_bf16 v[204:207], v[120:123], v[48:51], v[204:207]\n\t" \
    "s_waitcnt lgkmcnt(4)\n\t" \
    "v_mfma_f32_16x16x32_bf16 v[208:211], v[128:131], v[48:51], v[208:211]\n\t" \
    "s_waitcnt vmcnt(4)\n\t" \
    "s_barrier\n\t" \
    "s_setprio 0\n\t" \
    "s_sub_u32 %[a], %[t], 1\n\t" \
    "v_mov_b32 v53, %[tag]\n\t" \
    "v_mov_b32 v52, %[a]\n\t" \
    "s_cmp_gt_u32 %[t], 1\n\t" \
    "s_cselect_b64 exec, %[fm], 0\n\t" \
    "buffer_store_dwordx2 v[52:53], %[l16], %[rs], %[fo] offen sc1\n\t" \
    "s_mov_b64 exec, %[pm]\n\t" \
    "s_mov_b32 m0, %[pd]\n\t" \
    "s_add_u32 %[a], %[sx], %[po]\n\t" \
    "buffer_load_dwordx4 %[l16], %[rs], %[a] offen sc1 lds\n\t" \
    "s_mov_b64 exec, -1\n\t" \
    "s_add_u32 m0, %[sd], 32768\n\t" \
    "s_add_u32 %[a], %[sx], 131072\n\t" \
    "buffer_load_dwordx4 %[l16], %[rs], %[a] offen offset:0 sc1 lds\n\t" \
    "buffer_load_dwordx4 %[l16], %[rs], %[a] offen offset:1024 sc1 lds\n\t" \
    "buffer_load_dwordx4 %[l16], %[rs], %[a] offen offset:2048 sc1 lds\n\t" \
    "buffer_load_dwordx4 %[l16], %[rs], %[a] offen offset:3072 sc1 lds\n\t" \
    "v_mfma_f32_16x16x32_bf16 v[196:199], v[132:135], v[20:23], v[196:199]\n\t" \
    "ds_read_b128 v[44:47], %[xp] offset:55296\n\t" \
    "v_mfma_f32_16x16x32_bf16 v[200:203], v[140:143], v[20:23], v[200:203]\n\t" \
    "ds_read_b128 v[48:51], %[xp] offset:56320\n\t" \
    "v_mfma_f32_16x16x32_bf16 v[204:207], v[132:135], v[24:27], v[204:207]\n\t" \
    "s_waitcnt lgkmcnt(4)\n\t" \
    "v_mfma_f32_16x16x32_bf16 v[208:211], v[140:143], v[24:27], v[208:211]\n\t" \
    "v_mfma_f32_16x16x32_bf16 v[196:199], v[136:139], v[28:31], v[196:199]\n\t" \
    "ds_read_b128 v[20:23], %[xp] offset:57344\n\t" \
    "v_mfma_f32_16x16x32_bf16 v[200:203], v[144:147], v[28:31], v[200:203]\n\t" \
    "ds_read_b128 v[24:27], %[xp] offset:58368\n\t" \
    "v_mfma_f32_16x16x32_bf16 v[204:207], v[136:139], v[32:35], v[204:207]\n\t" \
    "s_waitcnt lgkmcnt(4)\n\t" \
    "v_mfma_f32_16x16x32_bf16 v[208:211], v[144:147], v[32:35], v[208:211]\n\t" \
    "v_mfma_f32_16x16x32_bf16 v[196:199], v[148:151], v[36:39], v[196:199]\n\t" \
    "ds_read_b128 v[28:31], %[xp] offset:59392\n\t" \
    "v_mfma_f32_16x16x32_bf16 v[200:203], v[156:159], v[36:39], v[200:203]\n\t" \
    "ds_read_b128 v[32:35], %[xp] offset:60416\n\t" \
    "v_mfma_f32_16x16x32_bf16 v[204:207], v[148:151], v[40:43], v[204:207]\n\t" \
    "s_waitcnt lgkmcnt(4)\n\t" \
    "v_mfma_f32_16x16x32_bf16 v[208:211], v[156:159], v[40:43], v[208:211]\n\t" \
    "v_mfma_f32_16x16x32_bf16 v[196:199], v[152:155], v[44:47], v[196:199]\n\t" \
    "ds_read_b128 v[36:39], %[xp] offset:61440\n\t" \
    "v_mfma_f32_16x16x32_bf16 v[200:203], v[160:163], v[44:47], v[200:203]\n\t" \
    "ds_read_b128 v[40:43], %[xp] offset:62464\n\t" \
    "v_mfma_f32_16x16x32_bf16 v[204:207], v[152:155], v[48:51], v[204:207]\n\t" \
    "s_waitcnt lgkmcnt(4)\n\t" \
    "v_mfma_f32_16x16x32_bf16 v[208:211], v[160:163], v[48:51], v[208:211]\n\t" \
    "v_mfma_f32_16x16x32_bf16 v[196:199], v[164:167], v[20:23], v[196:199]\n\t" \
    "ds_read_b128 v[44:47], %[xp] offset:63488\n\t" \
    "v_mfma_f32_16x16x32_bf16 v[200:203], v[172:175], v[20:23], v[200:203]\n\t" \
    "ds_read_b128 v[48:51], %[xp] offset:64512\n\t" \
    "v_mfma_f32_16x16x32_bf16 v[204:207], v[164:167], v[24:27], v[204:207]\n\t" \
    "s_waitcnt lgkmcnt(4)\n\t" \
    "v_mfma_f32_16x16x32_bf16 v[208:211], v[172:175], v[24:27], v[208:211]\n\t" \
    "v_mfma_f32_16x16x32_bf16 v[196:199], v[168:171], v[28:31], v[196:199]\n\t" \
    "ds_read_b128 v[20:23], %[xp] offset:0\n\t" \
    "v_mfma_f32_16x16x32_bf16 v[200:203], v[176:179], v[28:31], v[200:203]\n\t" \
    "ds_read_b128 v[24:27], %[xp] offset:1024\n\t" \
    "v_mfma_f32_16x16x32_bf16 v[204:207], v[168:171], v[32:35], v[204:207]\n\t" \
    "s_waitcnt lgkmcnt(4)\n\t" \
    "v_mfma_f32_16x16x32_bf16 v[208:211], v[176:179], v[32:35], v[208:211]\n\t" \
    "v_mfma_f32_16x16x32_bf16 v[196:199], v[180:183], v[36:39], v[196:199]\n\t" \
    "ds_read_b128 v[28:31], %[xp] offset:2048\n\t" \
    "v_mfma_f32_16x16x32_bf16 v[200:203], v[188:191], v[36:39], v[200:203]\n\t" \
    "ds_read_b128 v[32:35], %[xp] offset:3072\n\t" \
    "v_mfma_f32_16x16x32_bf16 v[204:207], v[180:183], v[40:43], v[204:207]\n\t" \
    "s_waitcnt lgkmcnt(4)\n\t" \
    "v_mfma_f32_16x16x32_bf16 v[208:211], v[188:191], v[40:43], v[208:211]\n\t" \
    "v_mfma_f32_16x16x32_bf16 v[196:199], v[184:187], v[44:47], v[196:199]\n\t" \
    "ds_read_b128 v[36:39], %[xp] offset:4096\n\t" \
    "v_mfma_f32_16x16x32_bf16 v[200:203], v[192:195], v[44:47], v[200:203]\n\t" \
    "ds_read_b128 v[40:43], %[xp] offset:5120\n\t" \
    "v_mfma_f32_16x16x32_bf16 v[204:207], v[184:187], v[48:51], v[204:207]\n\t" \
    "s_waitcnt lgkmcnt(4)\n\t" \
    "v_mfma_f32_16x16x32_bf16 v[208:211], v[192:195], v[48:51], v[208:211]\n\t" \
    "s_add_u32 %[sx], %[sx], 65536\n\t" \
    "s_add_u32 %[t], %[t], 1\n\t" \
    "s_waitcnt vmcnt(4)\n\t" \
    "s_barrier\n\t" \
    "s_cmp_lt_u32 %[t], %[te]\n\t" \
    "s_cbranch_scc1 L0_%=\n\t" \
    "s_cmp_lg_u32 %[fin], 0\n\t" \
    "s_cbranch_scc0 E_%=\n\t" \
    "s_nop 4\n\t" \
    "ds_write_b128 %[pa], v[196:199] offset:0\n\t" \
    "ds_write_b128 %[pa], v[200:203] offset:1024\n\t" \
    "ds_write_b128 %[pa], v[204:207] offset:2048\n\t" \
    "ds_write_b128 %[pa], v[208:211] offset:3072\n\t" \
    "s_waitcnt lgkmcnt(0)\n\t" \
    "s_barrier\n\t" \
    "s_branch E_%=\n\t" \
    "L1_%=:\n\t" \
    "s_setprio 1\n\t" \
    "s_add_u32 m0, %[sd], 49152\n\t" \
    "s_add_u32 %[a], %[sx], 81920\n\t" \
    "v_mfma_f32_16x16x32_bf16 v[0:3], v[68:71], v[20:23], 0\n\t" \
    "ds_read_b128 v[44:47], %[xp] offset:6144\n\t" \
    "v_mfma_f32_16x16x32_bf16 v[4:7], v[76:79], v[20:23], 0\n\t" \
    "ds_read_b128 v[48:51], %[xp] offset:7168\n\t" \
    "v_mfma_f32_16x16x32_bf16 v[8:11], v[68:71], v[24:27], 0\n\t" \
    "s_waitcnt lgkmcnt(4)\n\t" \
    "v_mfma_f32_16x16x32_bf16 v[12:15], v[76:79], v[24:27], 0\n\t" \
    "v_mfma_f32_16x16x32_bf16 v[0:3], v[72:75], v[28:31], v[0:3]\n\t" \
    "ds_read_b128 v[20:23], %[xp] offset:8192\n\t" \
    "v_mfma_f32_16x16x32_bf16 v[4:7], v[80:83], v[28:31], v[4:7]\n\t" \
    "ds_read_b128 v[24:27], %[xp] offset:9216\n\t" \
    "v_mfma_f32_16x16x32_bf16 v[8:11], v[72:75], v[32:35], v[8:11]\n\t" \
    "s_waitcnt lgkmcnt(4)\n\t" \
    "v_mfma_f32_16x16x32_bf16 v[12:15], v[80:83], v[32:35], v[12:15]\n\t" \
    "v_mfma_f32_16x16x32_bf16 v[0:3], v[84:87], v[36:39], v[0:3]\n\t" \
    "ds_read_b128 v[28:31], %[xp] offset:10240\n\t" \
    "v_mfma_f32_16x16x32_bf16 v[4:7], v[92:95], v[36:39], v[4:7]\n\t" \
    "ds_read_b128 v[32:35], %[xp] offset:11264\n\t" \
    "v_mfma_f32_16x16x32_bf16 v[8:11], v[84:87], v[40:43], v[8:11]\n\t" \
    "s_waitcnt lgkmcnt(4)\n\t" \
    "v_mfma_f32_16x16x32_bf16 v[12:15], v[92:95], v[40:43], v[12:15]\n\t" \
    "v_mfma_f32_16x16x32_bf16 v[0:3], v[88:91], v[44:47], v[0:3]\n\t" \
    "ds_read_b128 v[36:39], %[xp] offset:12288\n\t" \
    "v_mfma_f32_16x16x32_bf16 v[4:7], v[96:99], v[44:47], v[4:7]\n\t" \
    "ds_read_b128 v[40:43], %[xp] offset:13312\n\t" \
    "v_mfma_f32_16x16x32_bf16 v[8:11], v[88:91], v[48:51], v[8:11]\n\t" \
    "s_waitcnt lgkmcnt(4)\n\t" \
    "v_mfma_f32_16x16x32_bf16 v[12:15], v[96:99], v[48:51], v[12:15]\n\t" \
    "v_mfma_f32_16x16x32_bf16 v[0:3], v[100:103], v[20:23], v[0:3]\n\t" \
    "ds_read_b128 v[44:47], %[xp] offset:14336\n\t" \
    "v_mfma_f32_16x16x32_bf16 v[4:7], v[108:111], v[20:23], v[4:7]\n\t" \
    "ds_read_b128 v[48:51], %[xp] offset:15360\n\t" \
    "v_mfma_f32_16x16x32_bf16 v[8:11], v[100:103], v[24:27], v[8:11]\n\t" \
    "s_waitcnt lgkmcnt(4)\n\t" \
    "v_mfma_f32_16x16x32_bf16 v[12:15], v[108:111], v[24:27], v[12:15]\n\t" \
    "buffer_load_dwordx4 %[l16], %[rs], %[a] offen offset:0 sc1 lds\n\t" \
    "v_mfma_f32_16x16x32_bf16 v[0:3], v[104:107], v[28:31], v[0:3]\n\t" \
    "ds_read_b128 v[20:23], %[xp] offset:16384\n\t" \
    "v_mfma_f32_16x16x32_bf16 v[4:7], v[112:115], v[28:31], v[4:7]\n\t" \
    "ds_read_b128 v[24:27], %[xp] offset:17408\n\t" \
    "v_mfma_f32_16x16x32_bf16 v[8:11], v[104:107], v[32:35], v[8:11]\n\t" \
    "s_waitcnt lgkmcnt(4)\n\t" \
    "v_mfma_f32_16x16x32_bf16 v[12:15], v[112:115], v[32:35], v[12:15]\n\t" \
    "buffer_load_dwordx4 %[l16], %[rs], %[a] offen offset:1024 sc1 lds\n\t" \
    "v_mfma_f32_16x16x32_bf16 v[0:3], v[116:119], v[36:39], v[0:3]\n\t" \
    "ds_read_b128 v[28:31], %[xp] offset:18432\n\t" \
    "v_mfma_f32_16x16x32_bf16 v[4:7], v[124:127], v[36:39], v[4:7]\n\t" \
    "ds_read_b128 v[32:35], %[xp] offset:19456\n\t" \
    "v_mfma_f32_16x16x32_bf16 v[8:11], v[116:119], v[40:43], v[8:11]\n\t" \
    "s_waitcnt lgkmcnt(4)\n\t" \
    "v_mfma_f32_16x16x32_bf16 v[12:15], v[124:127], v[40:43], v[12:15]\n\t" \
    "buffer_load_dwordx4 %[l16], %[rs], %[a] offen offset:2048 sc1 lds\n\t" \
    "v_mfma_f32_16x16x32_bf16 v[0:3], v[120:123], v[44:47], v[0:3]\n\t" \
    "ds_read_b128 v[36:39], %[xp] offset:20480\n\t" \
    "v_mfma_f32_16x16x32_bf16 v[4:7], v[128:131], v[44:47], v[4:7]\n\t" \
    "ds_read_b128 v[40:43], %[xp] offset:21504\n\t" \
    "v_mfma_f32_16x16x32_bf16 v[8:11], v[120:123], v[48:51], v[8:11]\n\t" \
    "s_waitcnt lgkmcnt(4)\n\t" \
    "v_mfma_f32_16x16x32_bf16 v[12:15], v[128:131], v[48:51], v[12:15]\n\t" \
    "buffer_load_dwordx4 %[l16], %[rs], %[a] offen offset:3072 sc1 lds\n\t" \
    "s_waitcnt vmcnt(4)\n\t" \
    "s_barrier\n\t" \
    "s_setprio 1\n\t" \
    "s_add_u32 m0, %[sd], 0\n\t" \
    "s_add_u32 %[a], %[sx], 131072\n\t" \
    "s_add_u32 %[b], %[oo], 0x1000\n\t" \
    "s_cmp_lg_u32 %[t], 0\n\t" \
    "s_cselect_b64 %[em], -1, 0\n\t" \
    "v_mfma_f32_16x16x32_bf16 v[0:3], v[132:135], v[20:23], v[0:3]\n\t" \
    "ds_read_b128 v[44:47], %[xp] offset:22528\n\t" \
    "v_mfma_f32_16x16x32_bf16 v[4:7], v[140:143], v[20:23], v[4:7]\n\t" \
    "ds_read_b128 v[48:51], %[xp] offset:23552\n\t" \
    "v_mfma_f32_16x16x32_bf16 v[8:11], v[132:135], v[24:27], v[8:11]\n\t" \
    "ds_read_b128 v[52:55], %[pa] offset:0\n\t" \
    "s_waitcnt lgkmcnt(5)\n\t" \
    "v_mfma_f32_16x16x32_bf16 v[12:15], v[140:143], v[24:27], v[12:15]\n\t" \
    "buffer_load_dwordx4 %[l16], %[rs], %[a] offen offset:0 sc1 lds\n\t" \
    "v_mfma_f32_16x16x32_bf16 v[0:3], v[136:139], v[28:31], v[0:3]\n\t" \
    "ds_read_b128 v[20:23], %[xp] offset:24576\n\t" \
    "v_mfma_f32_16x16x32_bf16 v[4:7], v[144:147], v[28:31], v[4:7]\n\t" \
    "ds_read_b128 v[24:27], %[xp] offset:25600\n\t" \
    "v_mfma_f32_16x16x32_bf16 v[8:11], v[136:139], v[32:35], v[8:11]\n\t" \
    "ds_read_b128 v[56:59], %[pa] offset:1024\n\t" \
    "s_waitcnt lgkmcnt(6)\n\t" \
    "v_mfma_f32_16x16x32_bf16 v[12:15], v[144:147], v[32:35], v[12:15]\n\t" \
    "buffer_load_dwordx4 %[l16], %[rs], %[a] offen offset:1024 sc1 lds\n\t" \
    "v_mfma_f32_16x16x32_bf16 v[0:3], v[148:151], v[36:39], v[0:3]\n\t" \
    "ds_read_b128 v[28:31], %[xp] offset:26624\n\t" \
    "v_mfma_f32_16x16x32_bf16 v[4:7], v[156:159], v[36:39], v[4:7]\n\t" \
    "ds_read_b128 v[32:35], %[xp] offset:27648\n\t" \
    "v_mfma_f32_16x16x32_bf16 v[8:11], v[148:151], v[40:43], v[8:11]\n\t" \
    "ds_read_b128 v[60:63], %[pa] offset:2048\n\t" \
    "s_waitcnt lgkmcnt(7)\n\t" \
    "v_mfma_f32_16x16x32_bf16 v[12:15], v[156:159], v[40:43], v[12:15]\n\t" \
    "buffer_load_dwordx4 %[l16], %[rs], %[a] offen offset:2048 sc1 lds\n\t" \
    "v_mfma_f32_16x16x32_bf16 v[0:3], v[152:155], v[44:47], v[0:3]\n\t" \
    "ds_read_b128 v[36:39], %[xp] offset:28672\n\t" \
    "s_waitcnt lgkmcnt(7)\n\t" \
    "v_add_f32 v196, v196, v52\n\t" \
    "v_mfma_f32_16x16x32_bf16 v[4:7], v[160:163], v[44:47], v[4:7]\n\t" \
    "ds_read_b128 v[40:43], %[xp] offset:29696\n\t" \
    "v_add_f32 v197, v197, v53\n\t" \
    "v_mfma_f32_16x16x32_bf16 v[8:11], v[152:155], v[48:51], v[8:11]\n\t" \
    "ds_read_b128 v[64:67], %[pa] offset:3072\n\t" \
    "s_waitcnt lgkmcnt(7)\n\t" \
    "v_mfma_f32_16x16x32_bf16 v[12:15], v[160:163], v[48:51], v[12:15]\n\t" \
    "buffer_load_dwordx4 %[l16], %[rs], %[a] offen offset:3072 sc1 lds\n\t" \
    "v_add_f32 v198, v198, v54\n\t" \
    "v_add_f32 v199, v199, v55\n\t" \
    "v_mfma_f32_16x16x32_bf16 v[0:3], v[164:167], v[20:23], v[0:3]\n\t" \
    "ds_read_b128 v[44:47], %[xp] offset:30720\n\t" \
    "s_waitcnt lgkmcnt(7)\n\t" \
    "v_add_f32 v200, v200, v56\n\t" \
    "v_mfma_f32_16x16x32_bf16 v[4:7], v[172:175], v[20:23], v[4:7]\n\t" \
    "ds_read_b128 v[48:51], %[xp] offset:31744\n\t" \
    "v_add_f32 v201, v201, v57\n\t" \
    "v_mfma_f32_16x16x32_bf16 v[8:11], v[164:167], v[24:27], v[8:11]\n\t" \
    "v_add_f32 v202, v202, v58\n\t" \
    "s_waitcnt lgkmcnt(6)\n\t" \
    "v_mfma_f32_16x16x32_bf16 v[12:15], v[172:175], v[24:27], v[12:15]\n\t" \
    "v_add_f32 v203, v203, v59\n\t" \
    "s_waitcnt lgkmcnt(5)\n\t" \
    "v_add_f32 v204, v204, v60\n\t" \
    "v_add_f32 v205, v205, v61\n\t" \
    "v_mfma_f32_16x16x32_bf16 v[0:3], v[168:171], v[28:31], v[0:3]\n\t" \
    "ds_read_b128 v[20:23], %[xp] offset:32768\n\t" \
    "v_add_f32 v206, v206, v62\n\t" \
    "v_mfma_f32_16x16x32_bf16 v[4:7], v[176:179], v[28:31], v[4:7]\n\t" \
    "ds_read_b128 v[24:27], %[xp] offset:33792\n\t" \
    "v_add_f32 v207, v207, v63\n\t" \
    "v_mfma_f32_16x16x32_bf16 v[8:11], v[168:171], v[32:35], v[8:11]\n\t" \
    "s_waitcnt lgkmcnt(4)\n\t" \
    "v_add_f32 v208, v208, v64\n\t" \
    "v_mfma_f32_16x16x32_bf16 v[12:15], v[176:179], v[32:35], v[12:15]\n\t" \
    "v_add_f32 v209, v209, v65\n\t" \
    "v_add_f32 v210, v210, v66\n\t" \
    "v_add_f32 v211, v211, v67\n\t" \
    "v_mfma_f32_16x16x32_bf16 v[0:3], v[180:183], v[36:39], v[0:3]\n\t" \
    "ds_read_b128 v[28:31], %[xp] offset:34816\n\t" \
    "v_cvt_pk_bf16_f32 v52, v196, v200\n\t" \
    "v_mfma_f32_16x16x32_bf16 v[4:7], v[188:191], v[36:39], v[4:7]\n\t" \
    "ds_read_b128 v[32:35], %[xp] offset:35840\n\t" \
    "v_cvt_pk_bf16_f32 v53, v197, v201\n\t" \
    "v_mfma_f32_16x16x32_bf16 v[8:11], v[180:183], v[40:43], v[8:11]\n\t" \
    "v_cvt_pk_bf16_f32 v54, v198, v202\n\t" \
    "s_waitcnt lgkmcnt(4)\n\t" \
    "v_mfma_f32_16x16x32_bf16 v[12:15], v[188:191], v[40:43], v[12:15]\n\t" \
    "v_cvt_pk_bf16_f32 v55, v199, v203\n\t" \
    "v_cvt_pk_bf16_f32 v56, v204, v208\n\t" \
    "v_cvt_pk_bf16_f32 v57, v205, v209\n\t" \
    "v_mfma_f32_16x16x32_bf16 v[0:3], v[184:187], v[44:47], v[0:3]\n\t" \
    "ds_read_b128 v[36:39], %[xp] offset:36864\n\t" \
    "v_cvt_pk_bf16_f32 v58, v206, v210\n\t" \
    "v_mfma_f32_16x16x32_bf16 v[4:7], v[192:195], v[44:47], v[4:7]\n\t" \
    "ds_read_b128 v[40:43], %[xp] offset:37888\n\t" \
    "v_cvt_pk_bf16_f32 v59, v207, v211\n\t" \
    "v_mfma_f32_16x16x32_bf16 v[8:11], v[184:187], v[48:51], v[8:11]\n\t" \
    "s_waitcnt lgkmcnt(4)\n\t" \
    "v_mfma_f32_16x16x32_bf16 v[12:15], v[192:195], v[48:51], v[12:15]\n\t" \
    "s_mov_b64 exec, %[em]\n\t" \
    "buffer_store_dwordx4 v[52:55], %[l16], %[rs], %[oo] offen sc1\n\t" \
    "buffer_store_dwordx4 v[56:59], %[l16], %[rs], %[b] offen sc1\n\t" \
    "s_mov_b64 exec, -1\n\t" \
    "s_add_u32 %[sx], %[sx], 65536\n\t" \
    "s_add_u32 %[t], %[t], 1\n\t" \
    "s_add_u32 %[oo], %[oo], 262144\n\t" \
    "s_waitcnt vmcnt(6)\n\t" \
    "s_cmp_gt_u32 %[t], " STR(SQUICK) " + 1\n\t" \
    "s_cbranch_scc1 B0_%=\n\t" \
    "s_waitcnt vmcnt(0)\n\t" \
    "B0_%=:\n\t" \
    "s_barrier\n\t" \
    "s_setprio 1\n\t" \
    "s_add_u32 m0, %[sd], 16384\n\t" \
    "s_add_u32 %[a], %[sx], 81920\n\t" \
    "v_mfma_f32_16x16x32_bf16 v[196:199], v[68:71], v[20:23], 0\n\t" \
    "ds_read_b128 v[44:47], %[xp] offset:38912\n\t" \
    "v_mfma_f32_16x16x32_bf16 v[200:203], v[76:79], v[20:23], 0\n\t" \
    "ds_read_b128 v[48:51], %[xp] offset:39936\n\t" \
    "v_mfma_f32_16x16x32_bf16 v[204:207], v[68:71], v[24:27], 0\n\t" \
    "s_waitcnt lgkmcnt(4)\n\t" \
    "v_mfma_f32_16x16x32_bf16 v[208:211], v[76:79], v[24:27], 0\n\t" \
    "v_mfma_f32_16x16x32_bf16 v[196:199], v[72:75], v[28:31], v[196:199]\n\t" \
    "ds_read_b128 v[20:23], %[xp] offset:40960\n\t" \
    "v_mfma_f32_16x16x32_bf16 v[200:203], v[80:83], v[28:31], v[200:203]\n\t" \
    "ds_read_b128 v[24:27], %[xp] offset:41984\n\t" \
    "v_mfma_f32_16x16x32_bf16 v[204:207], v[72:75], v[32:35], v[204:207]\n\t" \
    "s_waitcnt lgkmcnt(4)\n\t" \
    "v_mfma_f32_16x16x32_bf16 v[208:211], v[80:83], v[32:35], v[208:211]\n\t" \
    "v_mfma_f32_16x16x32_bf16 v[196:199], v[84:87], v[36:39], v[196:199]\n\t" \
    "ds_read_b128 v[28:31], %[xp] offset:43008\n\t" \
    "v_mfma_f32_16x16x32_bf16 v[200:203], v[92:95], v[36:39], v[200:203]\n\t" \
    "ds_read_b128 v[32:35], %[xp] offset:44032\n\t" \
    "v_mfma_f32_16x16x32_bf16 v[204:207], v[84:87], v[40:43], v[204:207]\n\t" \
    "s_waitcnt lgkmcnt(4)\n\t" \
    "v_mfma_f32_16x16x32_bf16 v[208:211], v[92:95], v[40:43], v[208:211]\n\t" \
    "v_mfma_f32_16x16x32_bf16 v[196:199], v[88:91], v[44:47], v[196:199]\n\t" \
    "ds_read_b128 v[36:39], %[xp] offset:45056\n\t" \
    "v_mfma_f32_16x16x32_bf16 v[200:203], v[96:99], v[44:47], v[200:203]\n\t" \
    "ds_read_b128 v[40:43], %[xp] offset:46080\n\t" \
    "v_mfma_f32_16x16x32_bf16 v[204:207], v[88:91], v[48:51], v[204:207]\n\t" \
    "s_waitcnt lgkmcnt(4)\n\t" \
    "v_mfma_f32_16x16x32_bf16 v[208:211], v[96:99], v[48:51], v[208:211]\n\t" \
    "v_mfma_f32_16x16x32_bf16 v[196:199], v[100:103], v[20:23], v[196:199]\n\t" \
    "ds_read_b128 v[44:47], %[xp] offset:47104\n\t" \
    "v_mfma_f32_16x16x32_bf16 v[200:203], v[108:111], v[20:23], v[200:203]\n\t" \
    "ds_read_b128 v[48:51], %[xp] offset:48128\n\t" \
    "v_mfma_f32_16x16x32_bf16 v[204:207], v[100:103], v[24:27], v[204:207]\n\t" \
    "s_waitcnt lgkmcnt(4)\n\t" \
    "v_mfma_f32_16x16x32_bf16 v[208:211], v[108:111], v[24:27], v[208:211]\n\t" \
    "buffer_load_dwordx4 %[l16], %[rs], %[a] offen offset:0 sc1 lds\n\t" \
    "v_mfma_f32_16x16x32_bf16 v[196:199], v[104:107], v[28:31], v[196:199]\n\t" \
    "ds_read_b128 v[20:23], %[xp] offset:49152\n\t" \
    "v_mfma_f32_16x16x32_bf16 v[200:203], v[112:115], v[28:31], v[200:203]\n\t" \
    "ds_read_b128 v[24:27], %[xp] offset:50176\n\t" \
    "v_mfma_f32_16x16x32_bf16 v[204:207], v[104:107], v[32:35], v[204:207]\n\t" \
    "s_waitcnt lgkmcnt(4)\n\t" \
    "v_mfma_f32_16x16x32_bf16 v[208:211], v[112:115], v[32:35], v[208:211]\n\t" \
    "buffer_load_dwordx4 %[l16], %[rs], %[a] offen offset:1024 sc1 lds\n\t" \
    "v_mfma_f32_16x16x32_bf16 v[196:199], v[116:119], v[36:39], v[196:199]\n\t" \
    "ds_read_b128 v[28:31], %[xp] offset:51200\n\t" \
    "v_mfma_f32_16x16x32_bf16 v[200:203], v[124:127], v[36:39], v[200:203]\n\t" \
    "ds_read_b128 v[32:35], %[xp] offset:52224\n\t" \
    "v_mfma_f32_16x16x32_bf16 v[204:207], v[116:119], v[40:43], v[204:207]\n\t" \
    "s_waitcnt lgkmcnt(4)\n\t" \
    "v_mfma_f32_16x16x32_bf16 v[208:211], v[124:127], v[40:43], v[208:211]\n\t" \
    "buffer_load_dwordx4 %[l16], %[rs], %[a] offen offset:2048 sc1 lds\n\t" \
    "v_mfma_f32_16x16x32_bf16 v[196:199], v[120:123], v[44:47], v[196:199]\n\t" \
    "ds_read_b128 v[36:39], %[xp] offset:53248\n\t" \
    "v_mfma_f32_16x16x32_bf16 v[200:203], v[128:131], v[44:47], v[200:203]\n\t" \
    "ds_read_b128 v[40:43], %[xp] offset:54272\n\t" \
    "v_mfma_f32_16x16x32_bf16 v[204:207], v[120:123], v[48:51], v[204:207]\n\t" \
    "s_waitcnt lgkmcnt(4)\n\t" \
    "v_mfma_f32_16x16x32_bf16 v[208:211], v[128:131], v[48:51], v[208:211]\n\t" \
    "buffer_load_dwordx4 %[l16], %[rs], %[a] offen offset:3072 sc1 lds\n\t" \
    "s_waitcnt vmcnt(4)\n\t" \
    "s_barrier\n\t" \
    "s_setprio 1\n\t" \
    "s_add_u32 m0, %[sd], 32768\n\t" \
    "s_add_u32 %[a], %[sx], 131072\n\t" \
    "s_add_u32 %[b], %[oo], 0x1000\n\t" \
    "s_cmp_lg_u32 %[t], 0\n\t" \
    "s_cselect_b64 %[em], -1, 0\n\t" \
    "v_mfma_f32_16x16x32_bf16 v[196:199], v[132:135], v[20:23], v[196:199]\n\t" \
    "ds_read_b128 v[44:47], %[xp] offset:55296\n\t" \
    "v_mfma_f32_16x16x32_bf16 v[200:203], v[140:143], v[20:23], v[200:203]\n\t" \
    "ds_read_b128 v[48:51], %[xp] offset:56320\n\t" \
    "v_mfma_f32_16x16x32_bf16 v[204:207], v[132:135], v[24:27], v[204:207]\n\t" \
    "ds_read_b128 v[52:55], %[pa] offset:0\n\t" \
    "s_waitcnt lgkmcnt(5)\n\t" \
    "v_mfma_f32_16x16x32_bf16 v[208:211], v[140:143], v[24:27], v[208:211]\n\t" \
    "buffer_load_dwordx4 %[l16], %[rs], %[a] offen offset:0 sc1 lds\n\t" \
    "v_mfma_f32_16x16x32_bf16 v[196:199], v[136:139], v[28:31], v[196:199]\n\t" \
    "ds_read_b128 v[20:23], %[xp] offset:57344\n\t" \
    "v_mfma_f32_16x16x32_bf16 v[200:203], v[144:147], v[28:31], v[200:203]\n\t" \
    "ds_read_b128 v[24:27], %[xp] offset:58368\n\t" \
    "v_mfma_f32_16x16x32_bf16 v[204:207], v[136:139], v[32:35], v[204:207]\n\t" \
    "ds_read_b128 v[56:59], %[pa] offset:1024\n\t" \
    "s_waitcnt lgkmcnt(6)\n\t" \
    "v_mfma_f32_16x16x32_bf16 v[208:211], v[144:147], v[32:35], v[208:211]\n\t" \
    "buffer_load_dwordx4 %[l16], %[rs], %[a] offen offset:1024 sc1 lds\n\t" \
    "v_mfma_f32_16x16x32_bf16 v[196:199], v[148:151], v[36:39], v[196:199]\n\t" \
    "ds_read_b128 v[28:31], %[xp] offset:59392\n\t" \
    "v_mfma_f32_16x16x32_bf16 v[200:203], v[156:159], v[36:39], v[200:203]\n\t" \
    "ds_read_b128 v[32:35], %[xp] offset:60416\n\t" \
    "v_mfma_f32_16x16x32_bf16 v[204:207], v[148:151], v[40:43], v[204:207]\n\t" \
    "ds_read_b128 v[60:63], %[pa] offset:2048\n\t" \
    "s_waitcnt lgkmcnt(7)\n\t" \
    "v_mfma_f32_16x16x32_bf16 v[208:211], v[156:159], v[40:43], v[208:211]\n\t" \
    "buffer_load_dwordx4 %[l16], %[rs], %[a] offen offset:2048 sc1 lds\n\t" \
    "v_mfma_f32_16x16x32_bf16 v[196:199], v[152:155], v[44:47], v[196:199]\n\t" \
    "ds_read_b128 v[36:39], %[xp] offset:61440\n\t" \
    "s_waitcnt lgkmcnt(7)\n\t" \
    "v_add_f32 v0, v0, v52\n\t" \
    "v_mfma_f32_16x16x32_bf16 v[200:203], v[160:163], v[44:47], v[200:203]\n\t" \
    "ds_read_b128 v[40:43], %[xp] offset:62464\n\t" \
    "v_add_f32 v1, v1, v53\n\t" \
    "v_mfma_f32_16x16x32_bf16 v[204:207], v[152:155], v[48:51], v[204:207]\n\t" \
    "ds_read_b128 v[64:67], %[pa] offset:3072\n\t" \
    "s_waitcnt lgkmcnt(7)\n\t" \
    "v_mfma_f32_16x16x32_bf16 v[208:211], v[160:163], v[48:51], v[208:211]\n\t" \
    "buffer_load_dwordx4 %[l16], %[rs], %[a] offen offset:3072 sc1 lds\n\t" \
    "v_add_f32 v2, v2, v54\n\t" \
    "v_add_f32 v3, v3, v55\n\t" \
    "v_mfma_f32_16x16x32_bf16 v[196:199], v[164:167], v[20:23], v[196:199]\n\t" \
    "ds_read_b128 v[44:47], %[xp] offset:63488\n\t" \
    "s_waitcnt lgkmcnt(7)\n\t" \
    "v_add_f32 v4, v4, v56\n\t" \
    "v_mfma_f32_16x16x32_bf16 v[200:203], v[172:175], v[20:23], v[200:203]\n\t" \
    "ds_read_b128 v[48:51], %[xp] offset:64512\n\t" \
    "v_add_f32 v5, v5, v57\n\t" \
    "v_mfma_f32_16x16x32_bf16 v[204:207], v[164:167], v[24:27], v[204:207]\n\t" \
    "v_add_f32 v6, v6, v58\n\t" \
    "s_waitcnt lgkmcnt(6)\n\t" \
    "v_mfma_f32_16x16x32_bf16 v[208:211], v[172:175], v[24:27], v[208:211]\n\t" \
    "v_add_f32 v7, v7, v59\n\t" \
    "s_waitcnt lgkmcnt(5)\n\t" \
    "v_add_f32 v8, v8, v60\n\t" \
    "v_add_f32 v9, v9, v61\n\t" \
    "v_mfma_f32_16x16x32_bf16 v[196:199], v[168:171], v[28:31], v[196:199]\n\t" \
    "ds_read_b128 v[20:23], %[xp] offset:0\n\t" \
    "v_add_f32 v10, v10, v62\n\t" \
    "v_mfma_f32_16x16x32_bf16 v[200:203], v[176:179], v[28:31], v[200:203]\n\t" \
    "ds_read_b128 v[24:27], %[xp] offset:1024\n\t" \
    "v_add_f32 v11, v11, v63\n\t" \
    "v_mfma_f32_16x16x32_bf16 v[204:207], v[168:171], v[32:35], v[204:207]\n\t" \
    "s_waitcnt lgkmcnt(4)\n\t" \
    "v_add_f32 v12, v12, v64\n\t" \
    "v_mfma_f32_16x16x32_bf16 v[208:211], v[176:179], v[32:35], v[208:211]\n\t" \
    "v_add_f32 v13, v13, v65\n\t" \
    "v_add_f32 v14, v14, v66\n\t" \
    "v_add_f32 v15, v15, v67\n\t" \
    "v_mfma_f32_16x16x32_bf16 v[196:199], v[180:183], v[36:39], v[196:199]\n\t" \
    "ds_read_b128 v[28:31], %[xp] offset:2048\n\t" \
    "v_cvt_pk_bf16_f32 v52, v0, v4\n\t" \
    "v_mfma_f32_16x16x32_bf16 v[200:203], v[188:191], v[36:39], v[200:203]\n\t" \
    "ds_read_b128 v[32:35], %[xp] offset:3072\n\t" \
    "v_cvt_pk_bf16_f32 v53, v1, v5\n\t" \
    "v_mfma_f32_16x16x32_bf16 v[204:207], v[180:183], v[40:43], v[204:207]\n\t" \
    "v_cvt_pk_bf16_f32 v54, v2, v6\n\t" \
    "s_waitcnt lgkmcnt(4)\n\t" \
    "v_mfma_f32_16x16x32_bf16 v[208:211], v[188:191], v[40:43], v[208:211]\n\t" \
    "v_cvt_pk_bf16_f32 v55, v3, v7\n\t" \
    "v_cvt_pk_bf16_f32 v56, v8, v12\n\t" \
    "v_cvt_pk_bf16_f32 v57, v9, v13\n\t" \
    "v_mfma_f32_16x16x32_bf16 v[196:199], v[184:187], v[44:47], v[196:199]\n\t" \
    "ds_read_b128 v[36:39], %[xp] offset:4096\n\t" \
    "v_cvt_pk_bf16_f32 v58, v10, v14\n\t" \
    "v_mfma_f32_16x16x32_bf16 v[200:203], v[192:195], v[44:47], v[200:203]\n\t" \
    "ds_read_b128 v[40:43], %[xp] offset:5120\n\t" \
    "v_cvt_pk_bf16_f32 v59, v11, v15\n\t" \
    "v_mfma_f32_16x16x32_bf16 v[204:207], v[184:187], v[48:51], v[204:207]\n\t" \
    "s_waitcnt lgkmcnt(4)\n\t" \
    "v_mfma_f32_16x16x32_bf16 v[208:211], v[192:195], v[48:51], v[208:211]\n\t" \
    "s_mov_b64 exec, %[em]\n\t" \
    "buffer_store_dwordx4 v[52:55], %[l16], %[rs], %[oo] offen sc1\n\t" \
    "buffer_store_dwordx4 v[56:59], %[l16], %[rs], %[b] offen sc1\n\t" \
    "s_mov_b64 exec, -1\n\t" \
    "s_add_u32 %[sx], %[sx], 65536\n\t" \
    "s_add_u32 %[t], %[t], 1\n\t" \
    "s_add_u32 %[oo], %[oo], 262144\n\t" \
    "s_waitcnt vmcnt(6)\n\t" \
    "s_cmp_gt_u32 %[t], " STR(SQUICK) " + 1\n\t" \
    "s_cbranch_scc1 B1_%=\n\t" \
    "s_waitcnt vmcnt(0)\n\t" \
    "B1_%=:\n\t" \
    "s_barrier\n\t" \
    "s_cmp_lt_u32 %[t], %[te]\n\t" \
    "s_cbranch_scc1 L1_%=\n\t" \
    "s_cmp_lg_u32 %[fin], 0\n\t" \
    "s_cbranch_scc0 E_%=\n\t" \
    "s_nop 4\n\t" \
    "s_barrier\n\t" \
    "ds_read_b128 v[52:55], %[pa] offset:0\n\t" \
    "ds_read_b128 v[56:59], %[pa] offset:1024\n\t" \
    "ds_read_b128 v[60:63], %[pa] offset:2048\n\t" \
    "ds_read_b128 v[64:67], %[pa] offset:3072\n\t" \
    "s_add_u32 %[b], %[oo], 0x1000\n\t" \
    "s_waitcnt lgkmcnt(0)\n\t" \
    "v_add_f32 v196, v196, v52\n\t" \
    "v_add_f32 v197, v197, v53\n\t" \
    "v_add_f32 v198, v198, v54\n\t" \
    "v_add_f32 v199, v199, v55\n\t" \
    "v_add_f32 v200, v200, v56\n\t" \
    "v_add_f32 v201, v201, v57\n\t" \
    "v_add_f32 v202, v202, v58\n\t" \
    "v_add_f32 v203, v203, v59\n\t" \
    "v_add_f32 v204, v204, v60\n\t" \
    "v_add_f32 v205, v205, v61\n\t" \
    "v_add_f32 v206, v206, v62\n\t" \
    "v_add_f32 v207, v207, v63\n\t" \
    "v_add_f32 v208, v208, v64\n\t" \
    "v_add_f32 v209, v209, v65\n\t" \
    "v_add_f32 v210, v210, v66\n\t" \
    "v_add_f32 v211, v211, v67\n\t" \
    "v_cvt_pk_bf16_f32 v52, v196, v200\n\t" \
    "v_cvt_pk_bf16_f32 v53, v197, v201\n\t" \
    "v_cvt_pk_bf16_f32 v54, v198, v202\n\t" \
    "v_cvt_pk_bf16_f32 v55, v199, v203\n\t" \
    "v_cvt_pk_bf16_f32 v56, v204, v208\n\t" \
    "v_cvt_pk_bf16_f32 v57, v205, v209\n\t" \
    "v_cvt_pk_bf16_f32 v58, v206, v210\n\t" \
    "v_cvt_pk_bf16_f32 v59, v207, v211\n\t" \
    "buffer_store_dwordx4 v[52:55], %[l16], %[rs], %[oo] offen sc1\n\t" \
    "buffer_store_dwordx4 v[56:59], %[l16], %[rs], %[b] offen sc1\n\t" \
    "E_%=:\n\t" \
    "s_setprio 0\n\t" \
    "s_waitcnt lgkmcnt(0)\n\t" \
    "s_nop 7\n\t" \
    : [t] "+s"(t), [sx] "+s"(sx), [oo] "+s"(oo), [a] "=&s"(tmp), [b] "=&s"(tmp2), [em] "=&s"(em), "+{v[20:27]}"(xf[0]), "+{v[28:35]}"(xf[1]), "+{v[36:43]}"(xf[2]), "+{v[196:203]}"(ac[0]), "+{v[204:211]}"(ac[1]) \
    : [te] "s"(TE_), [fin] "s"(FIN_), [kh] "s"(kh), [sd] "s"(sd), [rs] "s"(rs), [l16] "v"(l16), [xp] "v"(xp), [pa] "v"(pa), [fm] "s"(fm), [pm] "s"(pm), [fo] "s"(fo), [po] "s"(po), [pd] "s"(pd), [tag] "s"(P.tag), "{v[68:75]}"(w[0][0]), "{v[76:83]}"(w[0][1]), "{v[84:91]}"(w[1][0]), "{v[92:99]}"(w[1][1]), "{v[100:107]}"(w[2][0]), "{v[108:115]}"(w[2][1]), "{v[116:123]}"(w[3][0]), "{v[124:131]}"(w[3][1]), "{v[132:139]}"(w[4][0]), "{v[140:147]}"(w[4][1]), "{v[148:155]}"(w[5][0]), "{v[156:163]}"(w[5][1]), "{v[164:171]}"(w[6][0]), "{v[172:179]}"(w[6][1]), "{v[180:187]}"(w[7][0]), "{v[188:195]}"(w[7][1]) \
    : "memory", "scc", "v0", "v1", "v2", "v3", "v4", "v5", "v6", "v7", "v8", "v9", "v10", "v11", "v12", "v13", "v14", "v15", "v44", "v45", "v46", "v47", "v48", "v49", "v50", "v51", "v52", "v53", "v54", "v55", "v56", "v57", "v58", "v59", "v60", "v61", "v62", "v63", "v64", "v65", "v66", "v67")

#define S_LOOP_V(TE_, FIN_) \
  asm volatile( \
    "s_cmp_eq_u32 %[kh], 0\n\t" \
    "s_cbranch_scc0 L1_%=\n\t" \
    "L0_%=:\n\t" \
    "s_setprio 0\n\t" \
    "s_nop 3\n\t" \
    "ds_write_b128 %[pa], v[196:199] offset:0\n\t" \
    "ds_write_b128 %[pa], v[200:203] offset:1024\n\t" \
    "ds_write_b128 %[pa], v[204:207] offset:2048\n\t" \
    "ds_write_b128 %[pa], v[208:211] offset:3072\n\t" \
    "s_sub_u32 %[a], %[t], 1\n\t" \
    "v_mov_b32 v53, %[tag]\n\t" \
    "v_mov_b32 v52, %[a]\n\t" \
    "s_sub_u32 %[b], %[t], 2\n\t" \
    "s_cmp_lt_u32 %[b], " STR(SQUICK) "\n\t" \
    "s_cselect_b64 exec, %[fm], 0\n\t" \
    "buffer_store_dwordx2 v[52:53], %[l16], %[rs], %[fo] offen sc1\n\t" \
    "s_mov_b64 exec, -1\n\t" \
    "s_add_u32 m0, %[sd], 49152\n\t" \
    "s_add_u32 %[a], %[sx], 81920\n\t" \
    "s_waitcnt lgkmcnt(0)\n\t" \
    "buffer_load_dwordx4 %[l16], %[rs], %[a] offen offset:0 sc1 lds\n\t" \
    "buffer_load_dwordx4 %[l16], %[rs], %[a] offen offset:1024 sc1 lds\n\t" \
    "buffer_load_dwordx4 %[l16], %[rs], %[a] offen offset:2048 sc1 lds\n\t" \
    "buffer_load_dwordx4 %[l16], %[rs], %[a] offen offset:3072 sc1 lds\n\t" \
    "v_mfma_f32_16x16x32_bf16 v[0:3], v[20:23], v[68:71], 0\n\t" \
    "ds_read_b128 v[44:47], %[xp] offset:6144\n\t" \
    "v_mfma_f32_16x16x32_bf16 v[4:7], v[20:23], v[76:79], 0\n\t" \
    "ds_read_b128 v[48:51], %[xp] offset:7168\n\t" \
    "v_mfma_f32_16x16x32_bf16 v[8:11], v[24:27], v[68:71], 0\n\t" \
    "v_mfma_f32_16x16x32_bf16 v[12:15], v[24:27], v[76:79], 0\n\t" \
    "v_mfma_f32_16x16x32_bf16 v[0:3], v[28:31], v[72:75], v[0:3]\n\t" \
    "ds_read_b128 v[20:23], %[xp] offset:8192\n\t" \
    "v_mfma_f32_16x16x32_bf16 v[4:7], v[28:31], v[80:83], v[4:7]\n\t" \
    "ds_read_b128 v[24:27], %[xp] offset:9216\n\t" \
    "v_mfma_f32_16x16x32_bf16 v[8:11], v[32:35], v[72:75], v[8:11]\n\t" \
    "v_mfma_f32_16x16x32_bf16 v[12:15], v[32:35], v[80:83], v[12:15]\n\t" \
    "v_mfma_f32_16x16x32_bf16 v[0:3], v[36:39], v[84:87], v[0:3]\n\t" \
    "ds_read_b128 v[28:31], %[xp] offset:10240\n\t" \
    "v_mfma_f32_16x16x32_bf16 v[4:7], v[36:39], v[92:95], v[4:7]\n\t" \
    "ds_read_b128 v[32:35], %[xp] offset:11264\n\t" \
    "v_mfma_f32_16x16x32_bf16 v[8:11], v[40:43], v[84:87], v[8:11]\n\t" \
    "s_waitcnt lgkmcnt(4)\n\t" \
    "v_mfma_f32_16x16x32_bf16 v[12:15], v[40:43], v[92:95], v[12:15]\n\t" \
    "v_mfma_f32_16x16x32_bf16 v[0:3], v[44:47], v[88:91], v[0:3]\n\t" \
    "ds_read_b128 v[36:39], %[xp] offset:12288\n\t" \
    "v_mfma_f32_16x16x32_bf16 v[4:7], v[44:47], v[96:99], v[4:7]\n\t" \
    "ds_read_b128 v[40:43], %[xp] offset:13312\n\t" \
    "v_mfma_f32_16x16x32_bf16 v[8:11], v[48:51], v[88:91], v[8:11]\n\t" \
    "s_waitcnt lgkmcnt(4)\n\t" \
    "v_mfma_f32_16x16x32_bf16 v[12:15], v[48:51], v[96:99], v[12:15]\n\t" \
    "v_mfma_f32_16x16x32_bf16 v[0:3], v[20:23], v[100:103], v[0:3]\n\t" \
    "ds_read_b128 v[44:47], %[xp] offset:14336\n\t" \
    "v_mfma_f32_16x16x32_bf16 v[4:7], v[20:23], v[108:111], v[4:7]\n\t" \
    "ds_read_b128 v[48:51], %[xp] offset:15360\n\t" \
    "v_mfma_f32_16x16x32_bf16 v[8:11], v[24:27], v[100:103], v[8:11]\n\t" \
    "s_waitcnt lgkmcnt(4)\n\t" \
    "v_mfma_f32_16x16x32_bf16 v[12:15], v[24:27], v[108:111], v[12:15]\n\t" \
    "v_mfma_f32_16x16x32_bf16 v[0:3], v[28:31], v[104:107], v[0:3]\n\t" \
    "ds_read_b128 v[20:23], %[xp] offset:16384\n\t" \
    "v_mfma_f32_16x16x32_bf16 v[4:7], v[28:31], v[112:115], v[4:7]\n\t" \
    "ds_read_b128 v[24:27], %[xp] offset:17408\n\t" \
    "v_mfma_f32_16x16x32_bf16 v[8:11], v[32:35], v[104:107], v[8:11]\n\t" \
    "s_waitcnt lgkmcnt(4)\n\t" \
    "v_mfma_f32_16x16x32_bf16 v[12:15], v[32:35], v[112:115], v[12:15]\n\t" \
    "v_mfma_f32_16x16x32_bf16 v[0:3], v[36:39], v[116:119], v[0:3]\n\t" \
    "ds_read_b128 v[28:31], %[xp] offset:18432\n\t" \
    "v_mfma_f32_16x16x32_bf16 v[4:7], v[36:39], v[124:127], v[4:7]\n\t" \
    "ds_read_b128 v[32:35], %[xp] offset:19456\n\t" \
    "v_mfma_f32_16x16x32_bf16 v[8:11], v[40:43], v[116:119], v[8:11]\n\t" \
    "s_waitcnt lgkmcnt(4)\n\t" \
    "v_mfma_f32_16x16x32_bf16 v[12:15], v[40:43], v[124:127], v[12:15]\n\t" \
    "v_mfma_f32_16x16x32_bf16 v[0:3], v[44:47], v[120:123], v[0:3]\n\t" \
    "ds_read_b128 v[36:39], %[xp] offset:20480\n\t" \
    "v_mfma_f32_16x16x32_bf16 v[4:7], v[44:47], v[128:131], v[4:7]\n\t" \
    "ds_read_b128 v[40:43], %[xp] offset:21504\n\t" \
    "v_mfma_f32_16x16x32_bf16 v[8:11], v[48:51], v[120:123], v[8:11]\n\t" \
    "s_waitcnt lgkmcnt(4)\n\t" \
    "v_mfma_f32_16x16x32_bf16 v[12:15], v[48:51], v[128:131], v[12:15]\n\t" \
    "s_waitcnt vmcnt(4)\n\t" \
    "s_barrier\n\t" \
    "s_setprio 0\n\t" \
    "s_sub_u32 %[a], %[t], 1\n\t" \
    "v_mov_b32 v53, %[tag]\n\t" \
    "v_mov_b32 v52, %[a]\n\t" \
    "s_cmp_gt_u32 %[t], 1\n\t" \
    "s_cselect_b64 exec, %[fm], 0\n\t" \
    "buffer_store_dwordx2 v[52:53], %[l16], %[rs], %[fo] offen sc1\n\t" \
    "s_mov_b64 exec, %[pm]\n\t" \
    "s_mov_b32 m0, %[pd]\n\t" \
    "s_add_u32 %[a], %[sx], %[po]\n\t" \
    "buffer_load_dwordx4 %[l16], %[rs], %[a] offen sc1 lds\n\t" \
    "s_mov_b64 exec, -1\n\t" \
    "s_add_u32 m0, %[sd], 0\n\t" \
    "s_add_u32 %[a], %[sx], 131072\n\t" \
    "buffer_load_dwordx4 %[l16], %[rs], %[a] offen offset:0 sc1 lds\n\t" \
    "buffer_load_dwordx4 %[l16], %[rs], %[a] offen offset:1024 sc1 lds\n\t" \
    "buffer_load_dwordx4 %[l16], %[rs], %[a] offen offset:2048 sc1 lds\n\t" \
    "buffer_load_dwordx4 %[l16], %[rs], %[a] offen offset:3072 sc1 lds\n\t" \
    "v_mfma_f32_16x16x32_bf16 v[0:3], v[20:23], v[132:135], v[0:3]\n\t" \
    "ds_read_b128 v[44:47], %[xp] offset:22528\n\t" \
    "v_mfma_f32_16x16x32_bf16 v[4:7], v[20:23], v[140:143], v[4:7]\n\t" \
    "ds_read_b128 v[48:51], %[xp] offset:23552\n\t" \
    "v_mfma_f32_16x16x32_bf16 v[8:11], v[24:27], v[132:135], v[8:11]\n\t" \
    "s_waitcnt lgkmcnt(4)\n\t" \
    "v_mfma_f32_16x16x32_bf16 v[12:15], v[24:27], v[140:143], v[12:15]\n\t" \
    "v_mfma_f32_16x16x32_bf16 v[0:3], v[28:31], v[136:139], v[0:3]\n\t" \
    "ds_read_b128 v[20:23], %[xp] offset:24576\n\t" \
    "v_mfma_f32_16x16x32_bf16 v[4:7], v[28:31], v[144:147], v[4:7]\n\t" \
    "ds_read_b128 v[24:27], %[xp] offset:25600\n\t" \
    "v_mfma_f32_16x16x32_bf16 v[8:11], v[32:35], v[136:139], v[8:11]\n\t" \
    "s_waitcnt lgkmcnt(4)\n\t" \
    "v_mfma_f32_16x16x32_bf16 v[12:15], v[32:35], v[144:147], v[12:15]\n\t" \
    "v_mfma_f32_16x16x32_bf16 v[0:3], v[36:39], v[148:151], v[0:3]\n\t" \
    "ds_read_b128 v[28:31], %[xp] offset:26624\n\t" \
    "v_mfma_f32_16x16x32_bf16 v[4:7], v[36:39], v[156:159], v[4:7]\n\t" \
    "ds_read_b128 v[32:35], %[xp] offset:27648\n\t" \
    "v_mfma_f32_16x16x32_bf16 v[8:11], v[40:43], v[148:151], v[8:11]\n\t" \
    "s_waitcnt lgkmcnt(4)\n\t" \
    "v_mfma_f32_16x16x32_bf16 v[12:15], v[40:43], v[156:159], v[12:15]\n\t" \
    "v_mfma_f32_16x16x32_bf16 v[0:3], v[44:47], v[152:155], v[0:3]\n\t" \
    "ds_read_b128 v[36:39], %[xp] offset:28672\n\t" \
    "v_mfma_f32_16x16x32_bf16 v[4:7], v[44:47], v[160:163], v[4:7]\n\t" \
    "ds_read_b128 v[40:43], %[xp] offset:29696\n\t" \
    "v_mfma_f32_16x16x32_bf16 v[8:11], v[48:51], v[152:155], v[8:11]\n\t" \
    "s_waitcnt lgkmcnt(4)\n\t" \
    "v_mfma_f32_16x16x32_bf16 v[12:15], v[48:51], v[160:163], v[12:15]\n\t" \
    "v_mfma_f32_16x16x32_bf16 v[0:3], v[20:23], v[164:167], v[0:3]\n\t" \
    "ds_read_b128 v[44:47], %[xp] offset:30720\n\t" \
    "v_mfma_f32_16x16x32_bf16 v[4:7], v[20:23], v[172:175], v[4:7]\n\t" \
    "ds_read_b128 v[48:51], %[xp] offset:31744\n\t" \
    "v_mfma_f32_16x16x32_bf16 v[8:11], v[24:27], v[164:167], v[8:11]\n\t" \
    "s_waitcnt lgkmcnt(4)\n\t" \
    "v_mfma_f32_16x16x32_bf16 v[12:15], v[24:27], v[172:175], v[12:15]\n\t" \
    "v_mfma_f32_16x16x32_bf16 v[0:3], v[28:31], v[168:171], v[0:3]\n\t" \
    "ds_read_b128 v[20:23], %[xp] offset:32768\n\t" \
    "v_mfma_f32_16x16x32_bf16 v[4:7], v[28:31], v[176:179], v[4:7]\n\t" \
    "ds_read_b128 v[24:27], %[xp] offset:33792\n\t" \
    "v_mfma_f32_16x16x32_bf16 v[8:11], v[32:35], v[168:171], v[8:11]\n\t" \
    "s_waitcnt lgkmcnt(4)\n\t" \
    "v_mfma_f32_16x16x32_bf16 v[12:15], v[32:35], v[176:179], v[12:15]\n\t" \
    "v_mfma_f32_16x16x32_bf16 v[0:3], v[36:39], v[180:183], v[0:3]\n\t" \
    "ds_read_b128 v[28:31], %[xp] offset:34816\n\t" \
    "v_mfma_f32_16x16x32_bf16 v[4:7], v[36:39], v[188:191], v[4:7]\n\t" \
    "ds_read_b128 v[32:35], %[xp] offset:35840\n\t" \
    "v_mfma_f32_16x16x32_bf16 v[8:11], v[40:43], v[180:183], v[8:11]\n\t" \
    "s_waitcnt lgkmcnt(4)\n\t" \
    "v_mfma_f32_16x16x32_bf16 v[12:15], v[40:43], v[188:191], v[12:15]\n\t" \
    "v_mfma_f32_16x16x32_bf16 v[0:3], v[44:47], v[184:187], v[0:3]\n\t" \
    "ds_read_b128 v[36:39], %[xp] offset:36864\n\t" \
    "v_mfma_f32_16x16x32_bf16 v[4:7], v[44:47], v[192:195], v[4:7]\n\t" \
    "ds_read_b128 v[40:43], %[xp] offset:37888\n\t" \
    "v_mfma_f32_16x16x32_bf16 v[8:11], v[48:51], v[184:187], v[8:11]\n\t" \
    "s_waitcnt lgkmcnt(4)\n\t" \
    "v_mfma_f32_16x16x32_bf16 v[12:15], v[48:51], v[192:195], v[12:15]\n\t" \
    "s_add_u32 %[sx], %[sx], 65536\n\t" \
    "s_add_u32 %[t], %[t], 1\n\t" \
    "s_waitcnt vmcnt(4)\n\t" \
    "s_barrier\n\t" \
    "s_setprio 0\n\t" \
    "s_nop 3\n\t" \
    "ds_write_b128 %[pa], v[0:3] offset:0\n\t" \
    "ds_write_b128 %[pa], v[4:7] offset:1024\n\t" \
    "ds_write_b128 %[pa], v[8:11] offset:2048\n\t" \
    "ds_write_b128 %[pa], v[12:15] offset:3072\n\t" \
    "s_sub_u32 %[a], %[t], 1\n\t" \
    "v_mov_b32 v53, %[tag]\n\t" \
    "v_mov_b32 v52, %[a]\n\t" \
    "s_sub_u32 %[b], %[t], 2\n\t" \
    "s_cmp_lt_u32 %[b], " STR(SQUICK) "\n\t" \
    "s_cselect_b64 exec, %[fm], 0\n\t" \
    "buffer_store_dwordx2 v[52:53], %[l16], %[rs], %[fo] offen sc1\n\t" \
    "s_mov_b64 exec, -1\n\t" \
    "s_add_u32 m0, %[sd], 16384\n\t" \
    "s_add_u32 %[a], %[sx], 81920\n\t" \
    "s_waitcnt lgkmcnt(0)\n\t" \
    "buffer_load_dwordx4 %[l16], %[rs], %[a] offen offset:0 sc1 lds\n\t" \
    "buffer_load_dwordx4 %[l16], %[rs], %[a] offen offset:1024 sc1 lds\n\t" \
    "buffer_load_dwordx4 %[l16], %[rs], %[a] offen offset:2048 sc1 lds\n\t" \
    "buffer_load_dwordx4 %[l16], %[rs], %[a] offen offset:3072 sc1 lds\n\t" \
    "v_mfma_f32_16x16x32_bf16 v[196:199], v[20:23], v[68:71], 0\n\t" \
    "ds_read_b128 v[44:47], %[xp] offset:38912\n\t" \
    "v_mfma_f32_16x16x32_bf16 v[200:203], v[20:23], v[76:79], 0\n\t" \
    "ds_read_b128 v[48:51], %[xp] offset:39936\n\t" \
    "v_mfma_f32_16x16x32_bf16 v[204:207], v[24:27], v[68:71], 0\n\t" \
    "v_mfma_f32_16x16x32_bf16 v[208:211], v[24:27], v[76:79], 0\n\t" \
    "v_mfma_f32_16x16x32_bf16 v[196:199], v[28:31], v[72:75], v[196:199]\n\t" \
    "ds_read_b128 v[20:23], %[xp] offset:40960\n\t" \
    "v_mfma_f32_16x16x32_bf16 v[200:203], v[28:31], v[80:83], v[200:203]\n\t" \
    "ds_read_b128 v[24:27], %[xp] offset:41984\n\t" \
    "v_mfma_f32_16x16x32_bf16 v[204:207], v[32:35], v[72:75], v[204:207]\n\t" \
    "v_mfma_f32_16x16x32_bf16 v[208:211], v[32:35], v[80:83], v[208:211]\n\t" \
    "v_mfma_f32_16x16x32_bf16 v[196:199], v[36:39], v[84:87], v[196:199]\n\t" \
    "ds_read_b128 v[28:31], %[xp] offset:43008\n\t" \
    "v_mfma_f32_16x16x32_bf16 v[200:203], v[36:39], v[92:95], v[200:203]\n\t" \
    "ds_read_b128 v[32:35], %[xp] offset:44032\n\t" \
    "v_mfma_f32_16x16x32_bf16 v[204:207], v[40:43], v[84:87], v[204:207]\n\t" \
    "s_waitcnt lgkmcnt(4)\n\t" \
    "v_mfma_f32_16x16x32_bf16 v[208:211], v[40:43], v[92:95], v[208:211]\n\t" \
    "v_mfma_f32_16x16x32_bf16 v[196:199], v[44:47], v[88:91], v[196:199]\n\t" \
    "ds_read_b128 v[36:39], %[xp] offset:45056\n\t" \
    "v_mfma_f32_16x16x32_bf16 v[200:203], v[44:47], v[96:99], v[200:203]\n\t" \
    "ds_read_b128 v[40:43], %[xp] offset:46080\n\t" \
    "v_mfma_f32_16x16x32_bf16 v[204:207], v[48:51], v[88:91], v[204:207]\n\t" \
    "s_waitcnt lgkmcnt(4)\n\t" \
    "v_mfma_f32_16x16x32_bf16 v[208:211], v[48:51], v[96:99], v[208:211]\n\t" \
    "v_mfma_f32_16x16x32_bf16 v[196:199], v[20:23], v[100:103], v[196:199]\n\t" \
    "ds_read_b128 v[44:47], %[xp] offset:47104\n\t" \
    "v_mfma_f32_16x16x32_bf16 v[200:203], v[20:23], v[108:111], v[200:203]\n\t" \
    "ds_read_b128 v[48:51], %[xp] offset:48128\n\t" \
    "v_mfma_f32_16x16x32_bf16 v[204:207], v[24:27], v[100:103], v[204:207]\n\t" \
    "s_waitcnt lgkmcnt(4)\n\t" \
    "v_mfma_f32_16x16x32_bf16 v[208:211], v[24:27], v[108:111], v[208:211]\n\t" \
    "v_mfma_f32_16x16x32_bf16 v[196:199], v[28:31], v[104:107], v[196:199]\n\t" \
    "ds_read_b128 v[20:23], %[xp] offset:49152\n\t" \
    "v_mfma_f32_16x16x32_bf16 v[200:203], v[28:31], v[112:115], v[200:203]\n\t" \
    "ds_read_b128 v[24:27], %[xp] offset:50176\n\t" \
    "v_mfma_f32_16x16x32_bf16 v[204:207], v[32:35], v[104:107], v[204:207]\n\t" \
    "s_waitcnt lgkmcnt(4)\n\t" \
    "v_mfma_f32_16x16x32_bf16 v[208:211], v[32:35], v[112:115], v[208:211]\n\t" \
    "v_mfma_f32_16x16x32_bf16 v[196:199], v[36:39], v[116:119], v[196:199]\n\t" \
    "ds_read_b128 v[28:31], %[xp] offset:51200\n\t" \
    "v_mfma_f32_16x16x32_bf16 v[200:203], v[36:39], v[124:127], v[200:203]\n\t" \
    "ds_read_b128 v[32:35], %[xp] offset:52224\n\t" \
    "v_mfma_f32_16x16x32_bf16 v[204:207], v[40:43], v[116:119], v[204:207]\n\t" \
    "s_waitcnt lgkmcnt(4)\n\t" \
    "v_mfma_f32_16x16x32_bf16 v[208:211], v[40:43], v[124:127], v[208:211]\n\t" \
    "v_mfma_f32_16x16x32_bf16 v[196:199], v[44:47], v[120:123], v[196:199]\n\t" \
    "ds_read_b128 v[36:39], %[xp] offset:53248\n\t" \
    "v_mfma_f32_16x16x32_bf16 v[200:203], v[44:47], v[128:131], v[200:203]\n\t" \
    "ds_read_b128 v[40:43], %[xp] offset:54272\n\t" \
    "v_mfma_f32_16x16x32_bf16 v[204:207], v[48:51], v[120:123], v[204:207]\n\t" \
    "s_waitcnt lgkmcnt(4)\n\t" \
    "v_mfma_f32_16x16x32_bf16 v[208:211], v[48:51], v[128:131], v[208:211]\n\t" \
    "s_waitcnt vmcnt(4)\n\t" \
    "s_barrier\n\t" \
    "s_setprio 0\n\t" \
    "s_sub_u32 %[a], %[t], 1\n\t" \
    "v_mov_b32 v53, %[tag]\n\t" \
    "v_mov_b32 v52, %[a]\n\t" \
    "s_cmp_gt_u32 %[t], 1\n\t" \
    "s_cselect_b64 exec, %[fm], 0\n\t" \
    "buffer_store_dwordx2 v[52:53], %[l16], %[rs], %[fo] offen sc1\n\t" \
    "s_mov_b64 exec, %[pm]\n\t" \
    "s_mov_b32 m0, %[pd]\n\t" \
    "s_add_u32 %[a], %[sx], %[po]\n\t" \
    "buffer_load_dwordx4 %[l16], %[rs], %[a] offen sc1 lds\n\t" \
    "s_mov_b64 exec, -1\n\t" \
    "s_add_u32 m0, %[sd], 32768\n\t" \
    "s_add_u32 %[a], %[sx], 131072\n\t" \
    "buffer_load_dwordx4 %[l16], %[rs], %[a] offen offset:0 sc1 lds\n\t" \
    "buffer_load_dwordx4 %[l16], %[rs], %[a] offen offset:1024 sc1 lds\n\t" \
    "buffer_load_dwordx4 %[l16], %[rs], %[a] offen offset:2048 sc1 lds\n\t" \
    "buffer_load_dwordx4 %[l16], %[rs], %[a] offen offset:3072 sc1 lds\n\t" \
    "v_mfma_f32_16x16x32_bf16 v[196:199], v[20:23], v[132:135], v[196:199]\n\t" \
    "ds_read_b128 v[44:47], %[xp] offset:55296\n\t" \
    "v_mfma_f32_16x16x32_bf16 v[200:203], v[20:23], v[140:143], v[200:203]\n\t" \
    "ds_read_b128 v[48:51], %[xp] offset:56320\n\t" \
    "v_mfma_f32_16x16x32_bf16 v[204:207], v[24:27], v[132:135], v[204:207]\n\t" \
    "s_waitcnt lgkmcnt(4)\n\t" \
    "v_mfma_f32_16x16x32_bf16 v[208:211], v[24:27], v[140:143], v[208:211]\n\t" \
    "v_mfma_f32_16x16x32_bf16 v[196:199], v[28:31], v[136:139], v[196:199]\n\t" \
    "ds_read_b128 v[20:23], %[xp] offset:57344\n\t" \
    "v_mfma_f32_16x16x32_bf16 v[200:203], v[28:31], v[144:147], v[200:203]\n\t" \
    "ds_read_b128 v[24:27], %[xp] offset:58368\n\t" \
    "v_mfma_f32_16x16x32_bf16 v[204:207], v[32:35], v[136:139], v[204:207]\n\t" \
    "s_waitcnt lgkmcnt(4)\n\t" \
    "v_mfma_f32_16x16x32_bf16 v[208:211], v[32:35], v[144:147], v[208:211]\n\t" \
    "v_mfma_f32_16x16x32_bf16 v[196:199], v[36:39], v[148:151], v[196:199]\n\t" \
    "ds_read_b128 v[28:31], %[xp] offset:59392\n\t" \
    "v_mfma_f32_16x16x32_bf16 v[200:203], v[36:39], v[156:159], v[200:203]\n\t" \
    "ds_read_b128 v[32:35], %[xp] offset:60416\n\t" \
    "v_mfma_f32_16x16x32_bf16 v[204:207], v[40:43], v[148:151], v[204:207]\n\t" \
    "s_waitcnt lgkmcnt(4)\n\t" \
    "v_mfma_f32_16x16x32_bf16 v[208:211], v[40:43], v[156:159], v[208:211]\n\t" \
    "v_mfma_f32_16x16x32_bf16 v[196:199], v[44:47], v[152:155], v[196:199]\n\t" \
    "ds_read_b128 v[36:39], %[xp] offset:61440\n\t" \
    "v_mfma_f32_16x16x32_bf16 v[200:203], v[44:47], v[160:163], v[200:203]\n\t" \
    "ds_read_b128 v[40:43], %[xp] offset:62464\n\t" \
    "v_mfma_f32_16x16x32_bf16 v[204:207], v[48:51], v[152:155], v[204:207]\n\t" \
    "s_waitcnt lgkmcnt(4)\n\t" \
    "v_mfma_f32_16x16x32_bf16 v[208:211], v[48:51], v[160:163], v[208:211]\n\t" \
    "v_mfma_f32_16x16x32_bf16 v[196:199], v[20:23], v[164:167], v[196:199]\n\t" \
    "ds_read_b128 v[44:47], %[xp] offset:63488\n\t" \
    "v_mfma_f32_16x16x32_bf16 v[200:203], v[20:23], v[172:175], v[200:203]\n\t" \
    "ds_read_b128 v[48:51], %[xp] offset:64512\n\t" \
    "v_mfma_f32_16x16x32_bf16 v[204:207], v[24:27], v[164:167], v[204:207]\n\t" \
    "s_waitcnt lgkmcnt(4)\n\t" \
    "v_mfma_f32_16x16x32_bf16 v[208:211], v[24:27], v[172:175], v[208:211]\n\t" \
    "v_mfma_f32_16x16x32_bf16 v[196:199], v[28:31], v[168:171], v[196:199]\n\t" \
    "ds_read_b128 v[20:23], %[xp] offset:0\n\t" \
    "v_mfma_f32_16x16x32_bf16 v[200:203], v[28:31], v[176:179], v[200:203]\n\t" \
    "ds_read_b128 v[24:27], %[xp] offset:1024\n\t" \
    "v_mfma_f32_16x16x32_bf16 v[204:207], v[32:35], v[168:171], v[204:207]\n\t" \
    "s_waitcnt lgkmcnt(4)\n\t" \
    "v_mfma_f32_16x16x32_bf16 v[208:211], v[32:35], v[176:179], v[208:211]\n\t" \
    "v_mfma_f32_16x16x32_bf16 v[196:199], v[36:39], v[180:183], v[196:199]\n\t" \
    "ds_read_b128 v[28:31], %[xp] offset:2048\n\t" \
    "v_mfma_f32_16x16x32_bf16 v[200:203], v[36:39], v[188:191], v[200:203]\n\t" \
    "ds_read_b128 v[32:35], %[xp] offset:3072\n\t" \
    "v_mfma_f32_16x16x32_bf16 v[204:207], v[40:43], v[180:183], v[204:207]\n\t" \
    "s_waitcnt lgkmcnt(4)\n\t" \
    "v_mfma_f32_16x16x32_bf16 v[208:211], v[40:43], v[188:191], v[208:211]\n\t" \
    "v_mfma_f32_16x16x32_bf16 v[196:199], v[44:47], v[184:187], v[196:199]\n\t" \
    "ds_read_b128 v[36:39], %[xp] offset:4096\n\t" \
    "v_mfma_f32_16x16x32_bf16 v[200:203], v[44:47], v[192:195], v[200:203]\n\t" \
    "ds_read_b128 v[40:43], %[xp] offset:5120\n\t" \
    "v_mfma_f32_16x16x32_bf16 v[204:207], v[48:51], v[184:187], v[204:207]\n\t" \
    "s_waitcnt lgkmcnt(4)\n\t" \
    "v_mfma_f32_16x16x32_bf16 v[208:211], v[48:51], v[192:195], v[208:211]\n\t" \
    "s_add_u32 %[sx], %[sx], 65536\n\t" \
    "s_add_u32 %[t], %[t], 1\n\t" \
    "s_waitcnt vmcnt(4)\n\t" \
    "s_barrier\n\t" \
    "s_cmp_lt_u32 %[t], %[te]\n\t" \
    "s_cbranch_scc1 L0_%=\n\t" \
    "s_cmp_lg_u32 %[fin], 0\n\t" \
    "s_cbranch_scc0 E_%=\n\t" \
    "s_nop 4\n\t" \
    "ds_write_b128 %[pa], v[196:199] offset:0\n\t" \
    "ds_write_b128 %[pa], v[200:203] offset:1024\n\t" \
    "ds_write_b128 %[pa], v[204:207] offset:2048\n\t" \
    "ds_write_b128 %[pa], v[208:211] offset:3072\n\t" \
    "s_waitcnt lgkmcnt(0)\n\t" \
    "s_barrier\n\t" \
    "s_branch E_%=\n\t" \
    "L1_%=:\n\t" \
    "s_setprio 1\n\t" \
    "s_add_u32 m0, %[sd], 49152\n\t" \
    "s_add_u32 %[a], %[sx], 81920\n\t" \
    "v_mfma_f32_16x16x32_bf16 v[0:3], v[20:23], v[68:71], 0\n\t" \
    "ds_read_b128 v[44:47], %[xp] offset:6144\n\t" \
    "v_mfma_f32_16x16x32_bf16 v[4:7], v[20:23], v[76:79], 0\n\t" \
    "ds_read_b128 v[48:51], %[xp] offset:7168\n\t" \
    "v_mfma_f32_16x16x32_bf16 v[8:11], v[24:27], v[68:71], 0\n\t" \
    "s_waitcnt lgkmcnt(4)\n\t" \
    "v_mfma_f32_16x16x32_bf16 v[12:15], v[24:27], v[76:79], 0\n\t" \
    "v_mfma_f32_16x16x32_bf16 v[0:3], v[28:31], v[72:75], v[0:3]\n\t" \
    "ds_read_b128 v[20:23], %[xp] offset:8192\n\t" \
    "v_mfma_f32_16x16x32_bf16 v[4:7], v[28:31], v[80:83], v[4:7]\n\t" \
    "ds_read_b128 v[24:27], %[xp] offset:9216\n\t" \
    "v_mfma_f32_16x16x32_bf16 v[8:11], v[32:35], v[72:75], v[8:11]\n\t" \
    "s_waitcnt lgkmcnt(4)\n\t" \
    "v_mfma_f32_16x16x32_bf16 v[12:15], v[32:35], v[80:83], v[12:15]\n\t" \
    "v_mfma_f32_16x16x32_bf16 v[0:3], v[36:39], v[84:87], v[0:3]\n\t" \
    "ds_read_b128 v[28:31], %[xp] offset:10240\n\t" \
    "v_mfma_f32_16x16x32_bf16 v[4:7], v[36:39], v[92:95], v[4:7]\n\t" \
    "ds_read_b128 v[32:35], %[xp] offset:11264\n\t" \
    "v_mfma_f32_16x16x32_bf16 v[8:11], v[40:43], v[84:87], v[8:11]\n\t" \
    "s_waitcnt lgkmcnt(4)\n\t" \
    "v_mfma_f32_16x16x32_bf16 v[12:15], v[40:43], v[92:95], v[12:15]\n\t" \
    "v_mfma_f32_16x16x32_bf16 v[0:3], v[44:47], v[88:91], v[0:3]\n\t" \
    "ds_read_b128 v[36:39], %[xp] offset:12288\n\t" \
    "v_mfma_f32_16x16x32_bf16 v[4:7], v[44:47], v[96:99], v[4:7]\n\t" \
    "ds_read_b128 v[40:43], %[xp] offset:13312\n\t" \
    "v_mfma_f32_16x16x32_bf16 v[8:11], v[48:51], v[88:91], v[8:11]\n\t" \
    "s_waitcnt lgkmcnt(4)\n\t" \
    "v_mfma_f32_16x16x32_bf16 v[12:15], v[48:51], v[96:99], v[12:15]\n\t" \
    "v_mfma_f32_16x16x32_bf16 v[0:3], v[20:23], v[100:103], v[0:3]\n\t" \
    "ds_read_b128 v[44:47], %[xp] offset:14336\n\t" \
    "v_mfma_f32_16x16x32_bf16 v[4:7], v[20:23], v[108:111], v[4:7]\n\t" \
    "ds_read_b128 v[48:51], %[xp] offset:15360\n\t" \
    "v_mfma_f32_16x16x32_bf16 v[8:11], v[24:27], v[100:103], v[8:11]\n\t" \
    "s_waitcnt lgkmcnt(4)\n\t" \
    "v_mfma_f32_16x16x32_bf16 v[12:15], v[24:27], v[108:111], v[12:15]\n\t" \
    "buffer_load_dwordx4 %[l16], %[rs], %[a] offen offset:0 sc1 lds\n\t" \
    "v_mfma_f32_16x16x32_bf16 v[0:3], v[28:31], v[104:107], v[0:3]\n\t" \
    "ds_read_b128 v[20:23], %[xp] offset:16384\n\t" \
    "v_mfma_f32_16x16x32_bf16 v[4:7], v[28:31], v[112:115], v[4:7]\n\t" \
    "ds_read_b128 v[24:27], %[xp] offset:17408\n\t" \
    "v_mfma_f32_16x16x32_bf16 v[8:11], v[32:35], v[104:107], v[8:11]\n\t" \
    "s_waitcnt lgkmcnt(4)\n\t" \
    "v_mfma_f32_16x16x32_bf16 v[12:15], v[32:35], v[112:115], v[12:15]\n\t" \
    "buffer_load_dwordx4 %[l16], %[rs], %[a] offen offset:1024 sc1 lds\n\t" \
    "v_mfma_f32_16x16x32_bf16 v[0:3], v[36:39], v[116:119], v[0:3]\n\t" \
    "ds_read_b128 v[28:31], %[xp] offset:18432\n\t" \
    "v_mfma_f32_16x16x32_bf16 v[4:7], v[36:39], v[124:127], v[4:7]\n\t" \
    "ds_read_b128 v[32:35], %[xp] offset:19456\n\t" \
    "v_mfma_f32_16x16x32_bf16 v[8:11], v[40:43], v[116:119], v[8:11]\n\t" \
    "s_waitcnt lgkmcnt(4)\n\t" \
    "v_mfma_f32_16x16x32_bf16 v[12:15], v[40:43], v[124:127], v[12:15]\n\t" \
    "buffer_load_dwordx4 %[l16], %[rs], %[a] offen offset:2048 sc1 lds\n\t" \
    "v_mfma_f32_16x16x32_bf16 v[0:3], v[44:47], v[120:123], v[0:3]\n\t" \
    "ds_read_b128 v[36:39], %[xp] offset:20480\n\t" \
    "v_mfma_f32_16x16x32_bf16 v[4:7], v[44:47], v[128:131], v[4:7]\n\t" \
    "ds_read_b128 v[40:43], %[xp] offset:21504\n\t" \
    "v_mfma_f32_16x16x32_bf16 v[8:11], v[48:51], v[120:123], v[8:11]\n\t" \
    "s_waitcnt lgkmcnt(4)\n\t" \
    "v_mfma_f32_16x16x32_bf16 v[12:15], v[48:51], v[128:131], v[12:15]\n\t" \
    "buffer_load_dwordx4 %[l16], %[rs], %[a] offen offset:3072 sc1 lds\n\t" \
    "s_waitcnt vmcnt(4)\n\t" \
    "s_barrier\n\t" \
    "s_setprio 1\n\t" \
    "s_add_u32 m0, %[sd], 0\n\t" \
    "s_add_u32 %[a], %[sx], 131072\n\t" \
    "s_add_u32 %[b], %[oo], 0x1000\n\t" \
    "s_cmp_lg_u32 %[t], 0\n\t" \
    "s_cselect_b64 %[em], -1, 0\n\t" \
    "v_mfma_f32_16x16x32_bf16 v[0:3], v[20:23], v[132:135], v[0:3]\n\t" \
    "ds_read_b128 v[44:47], %[xp] offset:22528\n\t" \
    "v_mfma_f32_16x16x32_bf16 v[4:7], v[20:23], v[140:143], v[4:7]\n\t" \
    "ds_read_b128 v[48:51], %[xp] offset:23552\n\t" \
    "v_mfma_f32_16x16x32_bf16 v[8:11], v[24:27], v[132:135], v[8:11]\n\t" \
    "ds_read_b128 v[52:55], %[pa] offset:0\n\t" \
    "s_waitcnt lgkmcnt(5)\n\t" \
    "v_mfma_f32_16x16x32_bf16 v[12:15], v[24:27], v[140:143], v[12:15]\n\t" \
    "buffer_load_dwordx4 %[l16], %[rs], %[a] offen offset:0 sc1 lds\n\t" \
    "v_mfma_f32_16x16x32_bf16 v[0:3], v[28:31], v[136:139], v[0:3]\n\t" \
    "ds_read_b128 v[20:23], %[xp] offset:24576\n\t" \
    "v_mfma_f32_16x16x32_bf16 v[4:7], v[28:31], v[144:147], v[4:7]\n\t" \
    "ds_read_b128 v[24:27], %[xp] offset:25600\n\t" \
    "v_mfma_f32_16x16x32_bf16 v[8:11], v[32:35], v[136:139], v[8:11]\n\t" \
    "ds_read_b128 v[56:59], %[pa] offset:1024\n\t" \
    "s_waitcnt lgkmcnt(6)\n\t" \
    "v_mfma_f32_16x16x32_bf16 v[12:15], v[32:35], v[144:147], v[12:15]\n\t" \
    "buffer_load_dwordx4 %[l16], %[rs], %[a] offen offset:1024 sc1 lds\n\t" \
    "v_mfma_f32_16x16x32_bf16 v[0:3], v[36:39], v[148:151], v[0:3]\n\t" \
    "ds_read_b128 v[28:31], %[xp] offset:26624\n\t" \
    "v_mfma_f32_16x16x32_bf16 v[4:7], v[36:39], v[156:159], v[4:7]\n\t" \
    "ds_read_b128 v[32:35], %[xp] offset:27648\n\t" \
    "v_mfma_f32_16x16x32_bf16 v[8:11], v[40:43], v[148:151], v[8:11]\n\t" \
    "ds_read_b128 v[60:63], %[pa] offset:2048\n\t" \
    "s_waitcnt lgkmcnt(7)\n\t" \
    "v_mfma_f32_16x16x32_bf16 v[12:15], v[40:43], v[156:159], v[12:15]\n\t" \
    "buffer_load_dwordx4 %[l16], %[rs], %[a] offen offset:2048 sc1 lds\n\t" \
    "v_mfma_f32_16x16x32_bf16 v[0:3], v[44:47], v[152:155], v[0:3]\n\t" \
    "ds_read_b128 v[36:39], %[xp] offset:28672\n\t" \
    "s_waitcnt lgkmcnt(7)\n\t" \
    "v_add_f32 v196, v196, v52\n\t" \
    "v_mfma_f32_16x16x32_bf16 v[4:7], v[44:47], v[160:163], v[4:7]\n\t" \
    "ds_read_b128 v[40:43], %[xp] offset:29696\n\t" \
    "v_add_f32 v197, v197, v53\n\t" \
    "v_mfma_f32_16x16x32_bf16 v[8:11], v[48:51], v[152:155], v[8:11]\n\t" \
    "ds_read_b128 v[64:67], %[pa] offset:3072\n\t" \
    "s_waitcnt lgkmcnt(7)\n\t" \
    "v_mfma_f32_16x16x32_bf16 v[12:15], v[48:51], v[160:163], v[12:15]\n\t" \
    "buffer_load_dwordx4 %[l16], %[rs], %[a] offen offset:3072 sc1 lds\n\t" \
    "v_add_f32 v198, v198, v54\n\t" \
    "v_add_f32 v199, v199, v55\n\t" \
    "v_mfma_f32_16x16x32_bf16 v[0:3], v[20:23], v[164:167], v[0:3]\n\t" \
    "ds_read_b128 v[44:47], %[xp] offset:30720\n\t" \
    "s_waitcnt lgkmcnt(7)\n\t" \
    "v_add_f32 v200, v200, v56\n\t" \
    "v_mfma_f32_16x16x32_bf16 v[4:7], v[20:23], v[172:175], v[4:7]\n\t" \
    "ds_read_b128 v[48:51], %[xp] offset:31744\n\t" \
    "v_add_f32 v201, v201, v57\n\t" \
    "v_mfma_f32_16x16x32_bf16 v[8:11], v[24:27], v[164:167], v[8:11]\n\t" \
    "v_add_f32 v202, v202, v58\n\t" \
    "s_waitcnt lgkmcnt(6)\n\t" \
    "v_mfma_f32_16x16x32_bf16 v[12:15], v[24:27], v[172:175], v[12:15]\n\t" \
    "v_add_f32 v203, v203, v59\n\t" \
    "s_waitcnt lgkmcnt(5)\n\t" \
    "v_add_f32 v204, v204, v60\n\t" \
    "v_add_f32 v205, v205, v61\n\t" \
    "v_mfma_f32_16x16x32_bf16 v[0:3], v[28:31], v[168:171], v[0:3]\n\t" \
    "ds_read_b128 v[20:23], %[xp] offset:32768\n\t" \
    "v_add_f32 v206, v206, v62\n\t" \
    "v_mfma_f32_16x16x32_bf16 v[4:7], v[28:31], v[176:179], v[4:7]\n\t" \
    "ds_read_b128 v[24:27], %[xp] offset:33792\n\t" \
    "v_add_f32 v207, v207, v63\n\t" \
    "v_mfma_f32_16x16x32_bf16 v[8:11], v[32:35], v[168:171], v[8:11]\n\t" \
    "s_waitcnt lgkmcnt(4)\n\t" \
    "v_add_f32 v208, v208, v64\n\t" \
    "v_mfma_f32_16x16x32_bf16 v[12:15], v[32:35], v[176:179], v[12:15]\n\t" \
    "v_add_f32 v209, v209, v65\n\t" \
    "v_add_f32 v210, v210, v66\n\t" \
    "v_add_f32 v211, v211, v67\n\t" \
    "v_mfma_f32_16x16x32_bf16 v[0:3], v[36:39], v[180:183], v[0:3]\n\t" \
    "ds_read_b128 v[28:31], %[xp] offset:34816\n\t" \
    "v_cvt_pk_bf16_f32 v52, v196, v197\n\t" \
    "v_mfma_f32_16x16x32_bf16 v[4:7], v[36:39], v[188:191], v[4:7]\n\t" \
    "ds_read_b128 v[32:35], %[xp] offset:35840\n\t" \
    "v_cvt_pk_bf16_f32 v53, v198, v199\n\t" \
    "v_mfma_f32_16x16x32_bf16 v[8:11], v[40:43], v[180:183], v[8:11]\n\t" \
    "v_cvt_pk_bf16_f32 v54, v204, v205\n\t" \
    "s_waitcnt lgkmcnt(4)\n\t" \
    "v_mfma_f32_16x16x32_bf16 v[12:15], v[40:43], v[188:191], v[12:15]\n\t" \
    "v_cvt_pk_bf16_f32 v55, v206, v207\n\t" \
    "v_cvt_pk_bf16_f32 v56, v200, v201\n\t" \
    "v_cvt_pk_bf16_f32 v57, v202, v203\n\t" \
    "v_mfma_f32_16x16x32_bf16 v[0:3], v[44:47], v[184:187], v[0:3]\n\t" \
    "ds_read_b128 v[36:39], %[xp] offset:36864\n\t" \
    "v_cvt_pk_bf16_f32 v58, v208, v209\n\t" \
    "v_mfma_f32_16x16x32_bf16 v[4:7], v[44:47], v[192:195], v[4:7]\n\t" \
    "ds_read_b128 v[40:43], %[xp] offset:37888\n\t" \
    "v_cvt_pk_bf16_f32 v59, v210, v211\n\t" \
    "v_mfma_f32_16x16x32_bf16 v[8:11], v[48:51], v[184:187], v[8:11]\n\t" \
    "s_waitcnt lgkmcnt(4)\n\t" \
    "v_mfma_f32_16x16x32_bf16 v[12:15], v[48:51], v[192:195], v[12:15]\n\t" \
    "s_mov_b64 exec, %[em]\n\t" \
    "buffer_store_dwordx4 v[52:55], %[l16], %[rs], %[oo] offen sc1\n\t" \
    "buffer_store_dwordx4 v[56:59], %[l16], %[rs], %[oo] offen offset:1024 sc1\n\t" \
    "s_mov_b64 exec, -1\n\t" \
    "s_add_u32 %[sx], %[sx], 65536\n\t" \
    "s_add_u32 %[t], %[t], 1\n\t" \
    "s_add_u32 %[oo], %[oo], 262144\n\t" \
    "s_waitcnt vmcnt(6)\n\t" \
    "s_cmp_gt_u32 %[t], " STR(SQUICK) " + 1\n\t" \
    "s_cbranch_scc1 B0_%=\n\t" \
    "s_waitcnt vmcnt(0)\n\t" \
    "B0_%=:\n\t" \
    "s_barrier\n\t" \
    "s_setprio 1\n\t" \
    "s_add_u32 m0, %[sd], 16384\n\t" \
    "s_add_u32 %[a], %[sx], 81920\n\t" \
    "v_mfma_f32_16x16x32_bf16 v[196:199], v[20:23], v[68:71], 0\n\t" \
    "ds_read_b128 v[44:47], %[xp] offset:38912\n\t" \
    "v_mfma_f32_16x16x32_bf16 v[200:203], v[20:23], v[76:79], 0\n\t" \
    "ds_read_b128 v[48:51], %[xp] offset:39936\n\t" \
    "v_mfma_f32_16x16x32_bf16 v[204:207], v[24:27], v[68:71], 0\n\t" \
    "s_waitcnt lgkmcnt(4)\n\t" \
    "v_mfma_f32_16x16x32_bf16 v[208:211], v[24:27], v[76:79], 0\n\t" \
    "v_mfma_f32_16x16x32_bf16 v[196:199], v[28:31], v[72:75], v[196:199]\n\t" \
    "ds_read_b128 v[20:23], %[xp] offset:40960\n\t" \
    "v_mfma_f32_16x16x32_bf16 v[200:203], v[28:31], v[80:83], v[200:203]\n\t" \
    "ds_read_b128 v[24:27], %[xp] offset:41984\n\t" \
    "v_mfma_f32_16x16x32_bf16 v[204:207], v[32:35], v[72:75], v[204:207]\n\t" \
    "s_waitcnt lgkmcnt(4)\n\t" \
    "v_mfma_f32_16x16x32_bf16 v[208:211], v[32:35], v[80:83], v[208:211]\n\t" \
    "v_mfma_f32_16x16x32_bf16 v[196:199], v[36:39], v[84:87], v[196:199]\n\t" \
    "ds_read_b128 v[28:31], %[xp] offset:43008\n\t" \
    "v_mfma_f32_16x16x32_bf16 v[200:203], v[36:39], v[92:95], v[200:203]\n\t" \
    "ds_read_b128 v[32:35], %[xp] offset:44032\n\t" \
    "v_mfma_f32_16x16x32_bf16 v[204:207], v[40:43], v[84:87], v[204:207]\n\t" \
    "s_waitcnt lgkmcnt(4)\n\t" \
    "v_mfma_f32_16x16x32_bf16 v[208:211], v[40:43], v[92:95], v[208:211]\n\t" \
    "v_mfma_f32_16x16x32_bf16 v[196:199], v[44:47], v[88:91], v[196:199]\n\t" \
    "ds_read_b128 v[36:39], %[xp] offset:45056\n\t" \
    "v_mfma_f32_16x16x32_bf16 v[200:203], v[44:47], v[96:99], v[200:203]\n\t" \
    "ds_read_b128 v[40:43], %[xp] offset:46080\n\t" \
    "v_mfma_f32_16x16x32_bf16 v[204:207], v[48:51], v[88:91], v[204:207]\n\t" \
    "s_waitcnt lgkmcnt(4)\n\t" \
    "v_mfma_f32_16x16x32_bf16 v[208:211], v[48:51], v[96:99], v[208:211]\n\t" \
    "v_mfma_f32_16x16x32_bf16 v[196:199], v[20:23], v[100:103], v[196:199]\n\t" \
    "ds_read_b128 v[44:47], %[xp] offset:47104\n\t" \
    "v_mfma_f32_16x16x32_bf16 v[200:203], v[20:23], v[108:111], v[200:203]\n\t" \
    "ds_read_b128 v[48:51], %[xp] offset:48128\n\t" \
    "v_mfma_f32_16x16x32_bf16 v[204:207], v[24:27], v[100:103], v[204:207]\n\t" \
    "s_waitcnt lgkmcnt(4)\n\t" \
    "v_mfma_f32_16x16x32_bf16 v[208:211], v[24:27], v[108:111], v[208:211]\n\t" \
    "buffer_load_dwordx4 %[l16], %[rs], %[a] offen offset:0 sc1 lds\n\t" \
    "v_mfma_f32_16x16x32_bf16 v[196:199], v[28:31], v[104:107], v[196:199]\n\t" \
    "ds_read_b128 v[20:23], %[xp] offset:49152\n\t" \
    "v_mfma_f32_16x16x32_bf16 v[200:203], v[28:31], v[112:115], v[200:203]\n\t" \
    "ds_read_b128 v[24:27], %[xp] offset:50176\n\t" \
    "v_mfma_f32_16x16x32_bf16 v[204:207], v[32:35], v[104:107], v[204:207]\n\t" \
    "s_waitcnt lgkmcnt(4)\n\t" \
    "v_mfma_f32_16x16x32_bf16 v[208:211], v[32:35], v[112:115], v[208:211]\n\t" \
    "buffer_load_dwordx4 %[l16], %[rs], %[a] offen offset:1024 sc1 lds\n\t" \
    "v_mfma_f32_16x16x32_bf16 v[196:199], v[36:39], v[116:119], v[196:199]\n\t" \
    "ds_read_b128 v[28:31], %[xp] offset:51200\n\t" \
    "v_mfma_f32_16x16x32_bf16 v[200:203], v[36:39], v[124:127], v[200:203]\n\t" \
    "ds_read_b128 v[32:35], %[xp] offset:52224\n\t" \
    "v_mfma_f32_16x16x32_bf16 v[204:207], v[40:43], v[116:119], v[204:207]\n\t" \
    "s_waitcnt lgkmcnt(4)\n\t" \
    "v_mfma_f32_16x16x32_bf16 v[208:211], v[40:43], v[124:127], v[208:211]\n\t" \
    "buffer_load_dwordx4 %[l16], %[rs], %[a] offen offset:2048 sc1 lds\n\t" \
    "v_mfma_f32_16x16x32_bf16 v[196:199], v[44:47], v[120:123], v[196:199]\n\t" \
    "ds_read_b128 v[36:39], %[xp] offset:53248\n\t" \
    "v_mfma_f32_16x16x32_bf16 v[200:203], v[44:47], v[128:131], v[200:203]\n\t" \
    "ds_read_b128 v[40:43], %[xp] offset:54272\n\t" \
    "v_mfma_f32_16x16x32_bf16 v[204:207], v[48:51], v[120:123], v[204:207]\n\t" \
    "s_waitcnt lgkmcnt(4)\n\t" \
    "v_mfma_f32_16x16x32_bf16 v[208:211], v[48:51], v[128:131], v[208:211]\n\t" \
    "buffer_load_dwordx4 %[l16], %[rs], %[a] offen offset:3072 sc1 lds\n\t" \
    "s_waitcnt vmcnt(4)\n\t" \
    "s_barrier\n\t" \
    "s_setprio 1\n\t" \
    "s_add_u32 m0, %[sd], 32768\n\t" \
    "s_add_u32 %[a], %[sx], 131072\n\t" \
    "s_add_u32 %[b], %[oo], 0x1000\n\t" \
    "s_cmp_lg_u32 %[t], 0\n\t" \
    "s_cselect_b64 %[em], -1, 0\n\t" \
    "v_mfma_f32_16x16x32_bf16 v[196:199], v[20:23], v[132:135], v[196:199]\n\t" \
    "ds_read_b128 v[44:47], %[xp] offset:55296\n\t" \
    "v_mfma_f32_16x16x32_bf16 v[200:203], v[20:23], v[140:143], v[200:203]\n\t" \
    "ds_read_b128 v[48:51], %[xp] offset:56320\n\t" \
    "v_mfma_f32_16x16x32_bf16 v[204:207], v[24:27], v[132:135], v[204:207]\n\t" \
    "ds_read_b128 v[52:55], %[pa] offset:0\n\t" \
    "s_waitcnt lgkmcnt(5)\n\t" \
    "v_mfma_f32_16x16x32_bf16 v[208:211], v[24:27], v[140:143], v[208:211]\n\t" \
    "buffer_load_dwordx4 %[l16], %[rs], %[a] offen offset:0 sc1 lds\n\t" \
    "v_mfma_f32_16x16x32_bf16 v[196:199], v[28:31], v[136:139], v[196:199]\n\t" \
    "ds_read_b128 v[20:23], %[xp] offset:57344\n\t" \
    "v_mfma_f32_16x16x32_bf16 v[200:203], v[28:31], v[144:147], v[200:203]\n\t" \
    "ds_read_b128 v[24:27], %[xp] offset:58368\n\t" \
    "v_mfma_f32_16x16x32_bf16 v[204:207], v[32:35], v[136:139], v[204:207]\n\t" \
    "ds_read_b128 v[56:59], %[pa] offset:1024\n\t" \
    "s_waitcnt lgkmcnt(6)\n\t" \
    "v_mfma_f32_16x16x32_bf16 v[208:211], v[32:35], v[144:147], v[208:211]\n\t" \
    "buffer_load_dwordx4 %[l16], %[rs], %[a] offen offset:1024 sc1 lds\n\t" \
    "v_mfma_f32_16x16x32_bf16 v[196:199], v[36:39], v[148:151], v[196:199]\n\t" \
    "ds_read_b128 v[28:31], %[xp] offset:59392\n\t" \
    "v_mfma_f32_16x16x32_bf16 v[200:203], v[36:39], v[156:159], v[200:203]\n\t" \
    "ds_read_b128 v[32:35], %[xp] offset:60416\n\t" \
    "v_mfma_f32_16x16x32_bf16 v[204:207], v[40:43], v[148:151], v[204:207]\n\t" \
    "ds_read_b128 v[60:63], %[pa] offset:2048\n\t" \
    "s_waitcnt lgkmcnt(7)\n\t" \
    "v_mfma_f32_16x16x32_bf16 v[208:211], v[40:43], v[156:159], v[208:211]\n\t" \
    "buffer_load_dwordx4 %[l16], %[rs], %[a] offen offset:2048 sc1 lds\n\t" \
    "v_mfma_f32_16x16x32_bf16 v[196:199], v[44:47], v[152:155], v[196:199]\n\t" \
    "ds_read_b128 v[36:39], %[xp] offset:61440\n\t" \
    "s_waitcnt lgkmcnt(7)\n\t" \
    "v_add_f32 v0, v0, v52\n\t" \
    "v_mfma_f32_16x16x32_bf16 v[200:203], v[44:47], v[160:163], v[200:203]\n\t" \
    "ds_read_b128 v[40:43], %[xp] offset:62464\n\t" \
    "v_add_f32 v1, v1, v53\n\t" \
    "v_mfma_f32_16x16x32_bf16 v[204:207], v[48:51], v[152:155], v[204:207]\n\t" \
    "ds_read_b128 v[64:67], %[pa] offset:3072\n\t" \
    "s_waitcnt lgkmcnt(7)\n\t" \
    "v_mfma_f32_16x16x32_bf16 v[208:211], v[48:51], v[160:163], v[208:211]\n\t" \
    "buffer_load_dwordx4 %[l16], %[rs], %[a] offen offset:3072 sc1 lds\n\t" \
    "v_add_f32 v2, v2, v54\n\t" \
    "v_add_f32 v3, v3, v55\n\t" \
    "v_mfma_f32_16x16x32_bf16 v[196:199], v[20:23], v[164:167], v[196:199]\n\t" \
    "ds_read_b128 v[44:47], %[xp] offset:63488\n\t" \
    "s_waitcnt lgkmcnt(7)\n\t" \
    "v_add_f32 v4, v4, v56\n\t" \
    "v_mfma_f32_16x16x32_bf16 v[200:203], v[20:23], v[172:175], v[200:203]\n\t" \
    "ds_read_b128 v[48:51], %[xp] offset:64512\n\t" \
    "v_add_f32 v5, v5, v57\n\t" \
    "v_mfma_f32_16x16x32_bf16 v[204:207], v[24:27], v[164:167], v[204:207]\n\t" \
    "v_add_f32 v6, v6, v58\n\t" \
    "s_waitcnt lgkmcnt(6)\n\t" \
    "v_mfma_f32_16x16x32_bf16 v[208:211], v[24:27], v[172:175], v[208:211]\n\t" \
    "v_add_f32 v7, v7, v59\n\t" \
    "s_waitcnt lgkmcnt(5)\n\t" \
    "v_add_f32 v8, v8, v60\n\t" \
    "v_add_f32 v9, v9, v61\n\t" \
    "v_mfma_f32_16x16x32_bf16 v[196:199], v[28:31], v[168:171], v[196:199]\n\t" \
    "ds_read_b128 v[20:23], %[xp] offset:0\n\t" \
    "v_add_f32 v10, v10, v62\n\t" \
    "v_mfma_f32_16x16x32_bf16 v[200:203], v[28:31], v[176:179], v[200:203]\n\t" \
    "ds_read_b128 v[24:27], %[xp] offset:1024\n\t" \
    "v_add_f32 v11, v11, v63\n\t" \
    "v_mfma_f32_16x16x32_bf16 v[204:207], v[32:35], v[168:171], v[204:207]\n\t" \
    "s_waitcnt lgkmcnt(4)\n\t" \
    "v_add_f32 v12, v12, v64\n\t" \
    "v_mfma_f32_16x16x32_bf16 v[208:211], v[32:35], v[176:179], v[208:211]\n\t" \
    "v_add_f32 v13, v13, v65\n\t" \
    "v_add_f32 v14, v14, v66\n\t" \
    "v_add_f32 v15, v15, v67\n\t" \
    "v_mfma_f32_16x16x32_bf16 v[196:199], v[36:39], v[180:183], v[196:199]\n\t" \
    "ds_read_b128 v[28:31], %[xp] offset:2048\n\t" \
    "v_cvt_pk_bf16_f32 v52, v0, v1\n\t" \
    "v_mfma_f32_16x16x32_bf16 v[200:203], v[36:39], v[188:191], v[200:203]\n\t" \
    "ds_read_b128 v[32:35], %[xp] offset:3072\n\t" \
    "v_cvt_pk_bf16_f32 v53, v2, v3\n\t" \
    "v_mfma_f32_16x16x32_bf16 v[204:207], v[40:43], v[180:183], v[204:207]\n\t" \
    "v_cvt_pk_bf16_f32 v54, v8, v9\n\t" \
    "s_waitcnt lgkmcnt(4)\n\t" \
    "v_mfma_f32_16x16x32_bf16 v[208:211], v[40:43], v[188:191], v[208:211]\n\t" \
    "v_cvt_pk_bf16_f32 v55, v10, v11\n\t" \
    "v_cvt_pk_bf16_f32 v56, v4, v5\n\t" \
    "v_cvt_pk_bf16_f32 v57, v6, v7\n\t" \
    "v_mfma_f32_16x16x32_bf16 v[196:199], v[44:47], v[184:187], v[196:199]\n\t" \
    "ds_read_b128 v[36:39], %[xp] offset:4096\n\t" \
    "v_cvt_pk_bf16_f32 v58, v12, v13\n\t" \
    "v_mfma_f32_16x16x32_bf16 v[200:203], v[44:47], v[192:195], v[200:203]\n\t" \
    "ds_read_b128 v[40:43], %[xp] offset:5120\n\t" \
    "v_cvt_pk_bf16_f32 v59, v14, v15\n\t" \
    "v_mfma_f32_16x16x32_bf16 v[204:207], v[48:51], v[184:187], v[204:207]\n\t" \
    "s_waitcnt lgkmcnt(4)\n\t" \
    "v_mfma_f32_16x16x32_bf16 v[208:211], v[48:51], v[192:195], v[208:211]\n\t" \
    "s_mov_b64 exec, %[em]\n\t" \
    "buffer_store_dwordx4 v[52:55], %[l16], %[rs], %[oo] offen sc1\n\t" \
    "buffer_store_dwordx4 v[56:59], %[l16], %[rs], %[oo] offen offset:1024 sc1\n\t" \
    "s_mov_b64 exec, -1\n\t" \
    "s_add_u32 %[sx], %[sx], 65536\n\t" \
    "s_add_u32 %[t], %[t], 1\n\t" \
    "s_add_u32 %[oo], %[oo], 262144\n\t" \
    "s_waitcnt vmcnt(6)\n\t" \
    "s_cmp_gt_u32 %[t], " STR(SQUICK) " + 1\n\t" \
    "s_cbranch_scc1 B1_%=\n\t" \
    "s_waitcnt vmcnt(0)\n\t" \
    "B1_%=:\n\t" \
    "s_barrier\n\t" \
    "s_cmp_lt_u32 %[t], %[te]\n\t" \
    "s_cbranch_scc1 L1_%=\n\t" \
    "s_cmp_lg_u32 %[fin], 0\n\t" \
    "s_cbranch_scc0 E_%=\n\t" \
    "s_nop 4\n\t" \
    "s_barrier\n\t" \
    "ds_read_b128 v[52:55], %[pa] offset:0\n\t" \
    "ds_read_b128 v[56:59], %[pa] offset:1024\n\t" \
    "ds_read_b128 v[60:63], %[pa] offset:2048\n\t" \
    "ds_read_b128 v[64:67], %[pa] offset:3072\n\t" \
    "s_add_u32 %[b], %[oo], 0x1000\n\t" \
    "s_waitcnt lgkmcnt(0)\n\t" \
    "v_add_f32 v196, v196, v52\n\t" \
    "v_add_f32 v197, v197, v53\n\t" \
    "v_add_f32 v198, v198, v54\n\t" \
    "v_add_f32 v199, v199, v55\n\t" \
    "v_add_f32 v200, v200, v56\n\t" \
    "v_add_f32 v201, v201, v57\n\t" \
    "v_add_f32 v202, v202, v58\n\t" \
    "v_add_f32 v203, v203, v59\n\t" \
    "v_add_f32 v204, v204, v60\n\t" \
    "v_add_f32 v205, v205, v61\n\t" \
    "v_add_f32 v206, v206, v62\n\t" \
    "v_add_f32 v207, v207, v63\n\t" \
    "v_add_f32 v208, v208, v64\n\t" \
    "v_add_f32 v209, v209, v65\n\t" \
    "v_add_f32 v210, v210, v66\n\t" \
    "v_add_f32 v211, v211, v67\n\t" \
    "v_cvt_pk_bf16_f32 v52, v196, v197\n\t" \
    "v_cvt_pk_bf16_f32 v53, v198, v199\n\t" \
    "v_cvt_pk_bf16_f32 v54, v204, v205\n\t" \
    "v_cvt_pk_bf16_f32 v55, v206, v207\n\t" \
    "v_cvt_pk_bf16_f32 v56, v200, v201\n\t" \
    "v_cvt_pk_bf16_f32 v57, v202, v203\n\t" \
    "v_cvt_pk_bf16_f32 v58, v208, v209\n\t" \
    "v_cvt_pk_bf16_f32 v59, v210, v211\n\t" \
    "buffer_store_dwordx4 v[52:55], %[l16], %[rs], %[oo] offen sc1\n\t" \
    "buffer_store_dwordx4 v[56:59], %[l16], %[rs], %[oo] offen offset:1024 sc1\n\t" \
    "E_%=:\n\t" \
    "s_setprio 0\n\t" \
    "s_waitcnt lgkmcnt(0)\n\t" \
    "s_nop 7\n\t" \
    : [t] "+s"(t), [sx] "+s"(sx), [oo] "+s"(oo), [a] "=&s"(tmp), [b] "=&s"(tmp2), [em] "=&s"(em), "+{v[20:27]}"(xf[0]), "+{v[28:35]}"(xf[1]), "+{v[36:43]}"(xf[2]), "+{v[196:203]}"(ac[0]), "+{v[204:211]}"(ac[1]) \
    : [te] "s"(TE_), [fin] "s"(FIN_), [kh] "s"(kh), [sd] "s"(sd), [rs] "s"(rs), [l16] "v"(l16), [xp] "v"(xp), [pa] "v"(pa), [fm] "s"(fm), [pm] "s"(pm), [fo] "s"(fo), [po] "s"(po), [pd] "s"(pd), [tag] "s"(P.tag), "{v[68:75]}"(w[0][0]), "{v[76:83]}"(w[0][1]), "{v[84:91]}"(w[1][0]), "{v[92:99]}"(w[1][1]), "{v[100:107]}"(w[2][0]), "{v[108:115]}"(w[2][1]), "{v[116:123]}"(w[3][0]), "{v[124:131]}"(w[3][1]), "{v[132:139]}"(w[4][0]), "{v[140:147]}"(w[4][1]), "{v[148:155]}"(w[5][0]), "{v[156:163]}"(w[5][1]), "{v[164:171]}"(w[6][0]), "{v[172:179]}"(w[6][1]), "{v[180:187]}"(w[7][0]), "{v[188:195]}"(w[7][1]) \
    : "memory", "scc", "v0", "v1", "v2", "v3", "v4", "v5", "v6", "v7", "v8", "v9", "v10", "v11", "v12", "v13", "v14", "v15", "v44", "v45", "v46", "v47", "v48", "v49", "v50", "v51", "v52", "v53", "v54", "v55", "v56", "v57", "v58", "v59", "v60", "v61", "v62", "v63", "v64", "v65", "v66", "v67")

#define S_LOOP_X(TE_, FIN_) \
  asm volatile( \
    "s_cmp_eq_u32 %[kh], 0\n\t" \
    "s_cbranch_scc0 L1_%=\n\t" \
    "L0_%=:\n\t" \
    "s_setprio 0\n\t" \
    "s_nop 3\n\t" \
    "ds_write_b128 %[pa], v[196:199] offset:0\n\t" \
    "ds_write_b128 %[pa], v[200:203] offset:1024\n\t" \
    "ds_write_b128 %[pa], v[204:207] offset:2048\n\t" \
    "ds_write_b128 %[pa], v[208:211] offset:3072\n\t" \
    "s_sub_u32 %[a], %[t], 1\n\t" \
    "v_mov_b32 v53, %[tag]\n\t" \
    "v_mov_b32 v52, %[a]\n\t" \
    "s_sub_u32 %[b], %[t], 2\n\t" \
    "s_cmp_lt_u32 %[b], " STR(SQUICK) "\n\t" \
    "s_cselect_b64 exec, %[fm], 0\n\t" \
    "buffer_store_dwordx2 v[52:53], %[l16], %[rs], %[fo] offen sc1\n\t" \
    "s_mov_b64 exec, -1\n\t" \
    "s_add_u32 m0, %[sd], 49152\n\t" \
    "s_add_u32 %[a], %[sx], 278528\n\t" \
    "s_waitcnt lgkmcnt(0)\n\t" \
    "buffer_load_dwordx4 %[l16], %[rs], %[a] offen offset:0 sc1 lds\n\t" \
    "buffer_load_dwordx4 %[l16], %[rs], %[a] offen offset:1024 sc1 lds\n\t" \
    "buffer_load_dwordx4 %[l16], %[rs], %[a] offen offset:2048 sc1 lds\n\t" \
    "buffer_load_dwordx4 %[l16], %[rs], %[a] offen offset:3072 sc1 lds\n\t" \
    "v_mfma_f32_16x16x32_bf16 v[0:3], v[68:71], v[20:23], 0\n\t" \
    "ds_read_b128 v[44:47], %[xp] offset:6144\n\t" \
    "v_mfma_f32_16x16x32_bf16 v[4:7], v[76:79], v[20:23], 0\n\t" \
    "ds_read_b128 v[48:51], %[xp] offset:7168\n\t" \
    "v_mfma_f32_16x16x32_bf16 v[8:11], v[68:71], v[24:27], 0\n\t" \
    "v_mfma_f32_16x16x32_bf16 v[12:15], v[76:79], v[24:27], 0\n\t" \
    "v_mfma_f32_16x16x32_bf16 v[0:3], v[72:75], v[28:31], v[0:3]\n\t" \
    "ds_read_b128 v[20:23], %[xp] offset:8192\n\t" \
    "v_mfma_f32_16x16x32_bf16 v[4:7], v[80:83], v[28:31], v[4:7]\n\t" \
    "ds_read_b128 v[24:27], %[xp] offset:9216\n\t" \
    "v_mfma_f32_16x16x32_bf16 v[8:11], v[72:75], v[32:35], v[8:11]\n\t" \
    "v_mfma_f32_16x16x32_bf16 v[12:15], v[80:83], v[32:35], v[12:15]\n\t" \
    "v_mfma_f32_16x16x32_bf16 v[0:3], v[84:87], v[36:39], v[0:3]\n\t" \
    "ds_read_b128 v[28:31], %[xp] offset:10240\n\t" \
    "v_mfma_f32_16x16x32_bf16 v[4:7], v[92:95], v[36:39], v[4:7]\n\t" \
    "ds_read_b128 v[32:35], %[xp] offset:11264\n\t" \
    "v_mfma_f32_16x16x32_bf16 v[8:11], v[84:87], v[40:43], v[8:11]\n\t" \
    "s_waitcnt lgkmcnt(4)\n\t" \
    "v_mfma_f32_16x16x32_bf16 v[12:15], v[92:95], v[40:43], v[12:15]\n\t" \
    "v_mfma_f32_16x16x32_bf16 v[0:3], v[88:91], v[44:47], v[0:3]\n\t" \
    "ds_read_b128 v[36:39], %[xp] offset:12288\n\t" \
    "v_mfma_f32_16x16x32_bf16 v[4:7], v[96:99], v[44:47], v[4:7]\n\t" \
    "ds_read_b128 v[40:43], %[xp] offset:13312\n\t" \
    "v_mfma_f32_16x16x32_bf16 v[8:11], v[88:91], v[48:51], v[8:11]\n\t" \
    "s_waitcnt lgkmcnt(4)\n\t" \
    "v_mfma_f32_16x16x32_bf16 v[12:15], v[96:99], v[48:51], v[12:15]\n\t" \
    "v_mfma_f32_16x16x32_bf16 v[0:3], v[100:103], v[20:23], v[0:3]\n\t" \
    "ds_read_b128 v[44:47], %[xp] offset:14336\n\t" \
    "v_mfma_f32_16x16x32_bf16 v[4:7], v[108:111], v[20:23], v[4:7]\n\t" \
    "ds_read_b128 v[48:51], %[xp] offset:15360\n\t" \
    "v_mfma_f32_16x16x32_bf16 v[8:11], v[100:103], v[24:27], v[8:11]\n\t" \
    "s_waitcnt lgkmcnt(4)\n\t" \
    "v_mfma_f32_16x16x32_bf16 v[12:15], v[108:111], v[24:27], v[12:15]\n\t" \
    "v_mfma_f32_16x16x32_bf16 v[0:3], v[104:107], v[28:31], v[0:3]\n\t" \
    "ds_read_b128 v[20:23], %[xp] offset:16384\n\t" \
    "v_mfma_f32_16x16x32_bf16 v[4:7], v[112:115], v[28:31], v[4:7]\n\t" \
    "ds_read_b128 v[24:27], %[xp] offset:17408\n\t" \
    "v_mfma_f32_16x16x32_bf16 v[8:11], v[104:107], v[32:35], v[8:11]\n\t" \
    "s_waitcnt lgkmcnt(4)\n\t" \
    "v_mfma_f32_16x16x32_bf16 v[12:15], v[112:115], v[32:35], v[12:15]\n\t" \
    "v_mfma_f32_16x16x32_bf16 v[0:3], v[116:119], v[36:39], v[0:3]\n\t" \
    "ds_read_b128 v[28:31], %[xp] offset:18432\n\t" \
    "v_mfma_f32_16x16x32_bf16 v[4:7], v[124:127], v[36:39], v[4:7]\n\t" \
    "ds_read_b128 v[32:35], %[xp] offset:19456\n\t" \
    "v_mfma_f32_16x16x32_bf16 v[8:11], v[116:119], v[40:43], v[8:11]\n\t" \
    "s_waitcnt lgkmcnt(4)\n\t" \
    "v_mfma_f32_16x16x32_bf16 v[12:15], v[124:127], v[40:43], v[12:15]\n\t" \
    "v_mfma_f32_16x16x32_bf16 v[0:3], v[120:123], v[44:47], v[0:3]\n\t" \
    "ds_read_b128 v[36:39], %[xp] offset:20480\n\t" \
    "v_mfma_f32_16x16x32_bf16 v[4:7], v[128:131], v[44:47], v[4:7]\n\t" \
    "ds_read_b128 v[40:43], %[xp] offset:21504\n\t" \
    "v_mfma_f32_16x16x32_bf16 v[8:11], v[120:123], v[48:51], v[8:11]\n\t" \
    "s_waitcnt lgkmcnt(4)\n\t" \
    "v_mfma_f32_16x16x32_bf16 v[12:15], v[128:131], v[48:51], v[12:15]\n\t" \
    "s_waitcnt vmcnt(4)\n\t" \
    "s_barrier\n\t" \
    "s_setprio 0\n\t" \
    "s_sub_u32 %[a], %[t], 1\n\t" \
    "v_mov_b32 v53, %[tag]\n\t" \
    "v_mov_b32 v52, %[a]\n\t" \
    "s_cmp_gt_u32 %[t], 1\n\t" \
    "s_cselect_b64 exec, %[fm], 0\n\t" \
    "buffer_store_dwordx2 v[52:53], %[l16], %[rs], %[fo] offen sc1\n\t" \
    "s_mov_b64 exec, %[pm]\n\t" \
    "s_mov_b32 m0, %[pd]\n\t" \
    "s_add_u32 %[a], %[sx], %[po]\n\t" \
    "buffer_load_dwordx4 %[l16], %[rs], %[a] offen sc1 lds\n\t" \
    "s_mov_b64 exec, -1\n\t" \
    "s_add_u32 m0, %[sd], 0\n\t" \
    "s_add_u32 %[a], %[sx], 524288\n\t" \
    "buffer_load_dwordx4 %[l16], %[rs], %[a] offen offset:0 sc1 lds\n\t" \
    "buffer_load_dwordx4 %[l16], %[rs], %[a] offen offset:1024 sc1 lds\n\t" \
    "buffer_load_dwordx4 %[l16], %[rs], %[a] offen offset:2048 sc1 lds\n\t" \
    "buffer_load_dwordx4 %[l16], %[rs], %[a] offen offset:3072 sc1 lds\n\t" \
    "v_mfma_f32_16x16x32_bf16 v[0:3], v[132:135], v[20:23], v[0:3]\n\t" \
    "ds_read_b128 v[44:47], %[xp] offset:22528\n\t" \
    "v_mfma_f32_16x16x32_bf16 v[4:7], v[140:143], v[20:23], v[4:7]\n\t" \
    "ds_read_b128 v[48:51], %[xp] offset:23552\n\t" \
    "v_mfma_f32_16x16x32_bf16 v[8:11], v[132:135], v[24:27], v[8:11]\n\t" \
    "s_waitcnt lgkmcnt(4)\n\t" \
    "v_mfma_f32_16x16x32_bf16 v[12:15], v[140:143], v[24:27], v[12:15]\n\t" \
    "v_mfma_f32_16x16x32_bf16 v[0:3], v[136:139], v[28:31], v[0:3]\n\t" \
    "ds_read_b128 v[20:23], %[xp] offset:24576\n\t" \
    "v_mfma_f32_16x16x32_bf16 v[4:7], v[144:147], v[28:31], v[4:7]\n\t" \
    "ds_read_b128 v[24:27], %[xp] offset:25600\n\t" \
    "v_mfma_f32_16x16x32_bf16 v[8:11], v[136:139], v[32:35], v[8:11]\n\t" \
    "s_waitcnt lgkmcnt(4)\n\t" \
    "v_mfma_f32_16x16x32_bf16 v[12:15], v[144:147], v[32:35], v[12:15]\n\t" \
    "v_mfma_f32_16x16x32_bf16 v[0:3], v[148:151], v[36:39], v[0:3]\n\t" \
    "ds_read_b128 v[28:31], %[xp] offset:26624\n\t" \
    "v_mfma_f32_16x16x32_bf16 v[4:7], v[156:159], v[36:39], v[4:7]\n\t" \
    "ds_read_b128 v[32:35], %[xp] offset:27648\n\t" \
    "v_mfma_f32_16x16x32_bf16 v[8:11], v[148:151], v[40:43], v[8:11]\n\t" \
    "s_waitcnt lgkmcnt(4)\n\t" \
    "v_mfma_f32_16x16x32_bf16 v[12:15], v[156:159], v[40:43], v[12:15]\n\t" \
    "v_mfma_f32_16x16x32_bf16 v[0:3], v[152:155], v[44:47], v[0:3]\n\t" \
    "ds_read_b128 v[36:39], %[xp] offset:28672\n\t" \
    "v_mfma_f32_16x16x32_bf16 v[4:7], v[160:163], v[44:47], v[4:7]\n\t" \
    "ds_read_b128 v[40:43], %[xp] offset:29696\n\t" \
    "v_mfma_f32_16x16x32_bf16 v[8:11], v[152:155], v[48:51], v[8:11]\n\t" \
    "s_waitcnt lgkmcnt(4)\n\t" \
    "v_mfma_f32_16x16x32_bf16 v[12:15], v[160:163], v[48:51], v[12:15]\n\t" \
    "v_mfma_f32_16x16x32_bf16 v[0:3], v[164:167], v[20:23], v[0:3]\n\t" \
    "ds_read_b128 v[44:47], %[xp] offset:30720\n\t" \
    "v_mfma_f32_16x16x32_bf16 v[4:7], v[172:175], v[20:23], v[4:7]\n\t" \
    "ds_read_b128 v[48:51], %[xp] offset:31744\n\t" \
    "v_mfma_f32_16x16x32_bf16 v[8:11], v[164:167], v[24:27], v[8:11]\n\t" \
    "s_waitcnt lgkmcnt(4)\n\t" \
    "v_mfma_f32_16x16x32_bf16 v[12:15], v[172:175], v[24:27], v[12:15]\n\t" \
    "v_mfma_f32_16x16x32_bf16 v[0:3], v[168:171], v[28:31], v[0:3]\n\t" \
    "ds_read_b128 v[20:23], %[xp] offset:32768\n\t" \
    "v_mfma_f32_16x16x32_bf16 v[4:7], v[176:179], v[28:31], v[4:7]\n\t" \
    "ds_read_b128 v[24:27], %[xp] offset:33792\n\t" \
    "v_mfma_f32_16x16x32_bf16 v[8:11], v[168:171], v[32:35], v[8:11]\n\t" \
    "s_waitcnt lgkmcnt(4)\n\t" \
    "v_mfma_f32_16x16x32_bf16 v[12:15], v[176:179], v[32:35], v[12:15]\n\t" \
    "v_mfma_f32_16x16x32_bf16 v[0:3], v[180:183], v[36:39], v[0:3]\n\t" \
    "ds_read_b128 v[28:31], %[xp] offset:34816\n\t" \
    "v_mfma_f32_16x16x32_bf16 v[4:7], v[188:191], v[36:39], v[4:7]\n\t" \
    "ds_read_b128 v[32:35], %[xp] offset:35840\n\t" \
    "v_mfma_f32_16x16x32_bf16 v[8:11], v[180:183], v[40:43], v[8:11]\n\t" \
    "s_waitcnt lgkmcnt(4)\n\t" \
    "v_mfma_f32_16x16x32_bf16 v[12:15], v[188:191], v[40:43], v[12:15]\n\t" \
    "v_mfma_f32_16x16x32_bf16 v[0:3], v[184:187], v[44:47], v[0:3]\n\t" \
    "ds_read_b128 v[36:39], %[xp] offset:36864\n\t" \
    "v_mfma_f32_16x16x32_bf16 v[4:7], v[192:195], v[44:47], v[4:7]\n\t" \
    "ds_read_b128 v[40:43], %[xp] offset:37888\n\t" \
    "v_mfma_f32_16x16x32_bf16 v[8:11], v[184:187], v[48:51], v[8:11]\n\t" \
    "s_waitcnt lgkmcnt(4)\n\t" \
    "v_mfma_f32_16x16x32_bf16 v[12:15], v[192:195], v[48:51], v[12:15]\n\t" \
    "s_add_u32 %[sx], %[sx], 262144\n\t" \
    "s_add_u32 %[t], %[t], 1\n\t" \
    "s_waitcnt vmcnt(4)\n\t" \
    "s_barrier\n\t" \
    "s_setprio 0\n\t" \
    "s_nop 3\n\t" \
    "ds_write_b128 %[pa], v[0:3] offset:0\n\t" \
    "ds_write_b128 %[pa], v[4:7] offset:1024\n\t" \
    "ds_write_b128 %[pa], v[8:11] offset:2048\n\t" \
    "ds_write_b128 %[pa], v[12:15] offset:3072\n\t" \
    "s_sub_u32 %[a], %[t], 1\n\t" \
    "v_mov_b32 v53, %[tag]\n\t" \
    "v_mov_b32 v52, %[a]\n\t" \
    "s_sub_u32 %[b], %[t], 2\n\t" \
    "s_cmp_lt_u32 %[b], " STR(SQUICK) "\n\t" \
    "s_cselect_b64 exec, %[fm], 0\n\t" \
    "buffer_store_dwordx2 v[52:53], %[l16], %[rs], %[fo] offen sc1\n\t" \
    "s_mov_b64 exec, -1\n\t" \
    "s_add_u32 m0, %[sd], 16384\n\t" \
    "s_add_u32 %[a], %[sx], 278528\n\t" \
    "s_waitcnt lgkmcnt(0)\n\t" \
    "buffer_load_dwordx4 %[l16], %[rs], %[a] offen offset:0 sc1 lds\n\t" \
    "buffer_load_dwordx4 %[l16], %[rs], %[a] offen offset:1024 sc1 lds\n\t" \
    "buffer_load_dwordx4 %[l16], %[rs], %[a] offen offset:2048 sc1 lds\n\t" \
    "buffer_load_dwordx4 %[l16], %[rs], %[a] offen offset:3072 sc1 lds\n\t" \
    "v_mfma_f32_16x16x32_bf16 v[196:199], v[68:71], v[20:23], 0\n\t" \
    "ds_read_b128 v[44:47], %[xp] offset:38912\n\t" \
    "v_mfma_f32_16x16x32_bf16 v[200:203], v[76:79], v[20:23], 0\n\t" \
    "ds_read_b128 v[48:51], %[xp] offset:39936\n\t" \
    "v_mfma_f32_16x16x32_bf16 v[204:207], v[68:71], v[24:27], 0\n\t" \
    "v_mfma_f32_16x16x32_bf16 v[208:211], v[76:79], v[24:27], 0\n\t" \
    "v_mfma_f32_16x16x32_bf16 v[196:199], v[72:75], v[28:31], v[196:199]\n\t" \
    "ds_read_b128 v[20:23], %[xp] offset:40960\n\t" \
    "v_mfma_f32_16x16x32_bf16 v[200:203], v[80:83], v[28:31], v[200:203]\n\t" \
    "ds_read_b128 v[24:27], %[xp] offset:41984\n\t" \
    "v_mfma_f32_16x16x32_bf16 v[204:207], v[72:75], v[32:35], v[204:207]\n\t" \
    "v_mfma_f32_16x16x32_bf16 v[208:211], v[80:83], v[32:35], v[208:211]\n\t" \
    "v_mfma_f32_16x16x32_bf16 v[196:199], v[84:87], v[36:39], v[196:199]\n\t" \
    "ds_read_b128 v[28:31], %[xp] offset:43008\n\t" \
    "v_mfma_f32_16x16x32_bf16 v[200:203], v[92:95], v[36:39], v[200:203]\n\t" \
    "ds_read_b128 v[32:35], %[xp] offset:44032\n\t" \
    "v_mfma_f32_16x16x32_bf16 v[204:207], v[84:87], v[40:43], v[204:207]\n\t" \
    "s_waitcnt lgkmcnt(4)\n\t" \
    "v_mfma_f32_16x16x32_bf16 v[208:211], v[92:95], v[40:43], v[208:211]\n\t" \
    "v_mfma_f32_16x16x32_bf16 v[196:199], v[88:91], v[44:47], v[196:199]\n\t" \
    "ds_read_b128 v[36:39], %[xp] offset:45056\n\t" \
    "v_mfma_f32_16x16x32_bf16 v[200:203], v[96:99], v[44:47], v[200:203]\n\t" \
    "ds_read_b128 v[40:43], %[xp] offset:46080\n\t" \
    "v_mfma_f32_16x16x32_bf16 v[204:207], v[88:91], v[48:51], v[204:207]\n\t" \
    "s_waitcnt lgkmcnt(4)\n\t" \
    "v_mfma_f32_16x16x32_bf16 v[208:211], v[96:99], v[48:51], v[208:211]\n\t" \
    "v_mfma_f32_16x16x32_bf16 v[196:199], v[100:103], v[20:23], v[196:199]\n\t" \
    "ds_read_b128 v[44:47], %[xp] offset:47104\n\t" \
    "v_mfma_f32_16x16x32_bf16 v[200:203], v[108:111], v[20:23], v[200:203]\n\t" \
    "ds_read_b128 v[48:51], %[xp] offset:48128\n\t" \
    "v_mfma_f32_16x16x32_bf16 v[204:207], v[100:103], v[24:27], v[204:207]\n\t" \
    "s_waitcnt lgkmcnt(4)\n\t" \
    "v_mfma_f32_16x16x32_bf16 v[208:211], v[108:111], v[24:27], v[208:211]\n\t" \
    "v_mfma_f32_16x16x32_bf16 v[196:199], v[104:107], v[28:31], v[196:199]\n\t" \
    "ds_read_b128 v[20:23], %[xp] offset:49152\n\t" \
    "v_mfma_f32_16x16x32_bf16 v[200:203], v[112:115], v[28:31], v[200:203]\n\t" \
    "ds_read_b128 v[24:27], %[xp] offset:50176\n\t" \
    "v_mfma_f32_16x16x32_bf16 v[204:207], v[104:107], v[32:35], v[204:207]\n\t" \
    "s_waitcnt lgkmcnt(4)\n\t" \
    "v_mfma_f32_16x16x32_bf16 v[208:211], v[112:115], v[32:35], v[208:211]\n\t" \
    "v_mfma_f32_16x16x32_bf16 v[196:199], v[116:119], v[36:39], v[196:199]\n\t" \
    "ds_read_b128 v[28:31], %[xp] offset:51200\n\t" \
    "v_mfma_f32_16x16x32_bf16 v[200:203], v[124:127], v[36:39], v[200:203]\n\t" \
    "ds_read_b128 v[32:35], %[xp] offset:52224\n\t" \
    "v_mfma_f32_16x16x32_bf16 v[204:207], v[116:119], v[40:43], v[204:207]\n\t" \
    "s_waitcnt lgkmcnt(4)\n\t" \
    "v_mfma_f32_16x16x32_bf16 v[208:211], v[124:127], v[40:43], v[208:211]\n\t" \
    "v_mfma_f32_16x16x32_bf16 v[196:199], v[120:123], v[44:47], v[196:199]\n\t" \
    "ds_read_b128 v[36:39], %[xp] offset:53248\n\t" \
    "v_mfma_f32_16x16x32_bf16 v[200:203], v[128:131], v[44:47], v[200:203]\n\t" \
    "ds_read_b128 v[40:43], %[xp] offset:54272\n\t" \
    "v_mfma_f32_16x16x32_bf16 v[204:207], v[120:123], v[48:51], v[204:207]\n\t" \
    "s_waitcnt lgkmcnt(4)\n\t" \
    "v_mfma_f32_16x16x32_bf16 v[208:211], v[128:131], v[48:51], v[208:211]\n\t" \
    "s_waitcnt vmcnt(4)\n\t" \
    "s_barrier\n\t" \
    "s_setprio 0\n\t" \
    "s_sub_u32 %[a], %[t], 1\n\t" \
    "v_mov_b32 v53, %[tag]\n\t" \
    "v_mov_b32 v52, %[a]\n\t" \
    "s_cmp_gt_u32 %[t], 1\n\t" \
    "s_cselect_b64 exec, %[fm], 0\n\t" \
    "buffer_store_dwordx2 v[52:53], %[l16], %[rs], %[fo] offen sc1\n\t" \
    "s_mov_b64 exec, %[pm]\n\t" \
    "s_mov_b32 m0, %[pd]\n\t" \
    "s_add_u32 %[a], %[sx], %[po]\n\t" \
    "buffer_load_dwordx4 %[l16], %[rs], %[a] offen sc1 lds\n\t" \
    "s_mov_b64 exec, -1\n\t" \
    "s_add_u32 m0, %[sd], 32768\n\t" \
    "s_add_u32 %[a], %[sx], 524288\n\t" \
    "buffer_load_dwordx4 %[l16], %[rs], %[a] offen offset:0 sc1 lds\n\t" \
    "buffer_load_dwordx4 %[l16], %[rs], %[a] offen offset:1024 sc1 lds\n\t" \
    "buffer_load_dwordx4 %[l16], %[rs], %[a] offen offset:2048 sc1 lds\n\t" \
    "buffer_load_dwordx4 %[l16], %[rs], %[a] offen offset:3072 sc1 lds\n\t" \
    "v_mfma_f32_16x16x32_bf16 v[196:199], v[132:135], v[20:23], v[196:199]\n\t" \
    "ds_read_b128 v[44:47], %[xp] offset:55296\n\t" \
    "v_mfma_f32_16x16x32_bf16 v[200:203], v[140:143], v[20:23], v[200:203]\n\t" \
    "ds_read_b128 v[48:51], %[xp] offset:56320\n\t" \
    "v_mfma_f32_16x16x32_bf16 v[204:207], v[132:135], v[24:27], v[204:207]\n\t" \
    "s_waitcnt lgkmcnt(4)\n\t" \
    "v_mfma_f32_16x16x32_bf16 v[208:211], v[140:143], v[24:27], v[208:211]\n\t" \
    "v_mfma_f32_16x16x32_bf16 v[196:199], v[136:139], v[28:31], v[196:199]\n\t" \
    "ds_read_b128 v[20:23], %[xp] offset:57344\n\t" \
    "v_mfma_f32_16x16x32_bf16 v[200:203], v[144:147], v[28:31], v[200:203]\n\t" \
    "ds_read_b128 v[24:27], %[xp] offset:58368\n\t" \
    "v_mfma_f32_16x16x32_bf16 v[204:207], v[136:139], v[32:35], v[204:207]\n\t" \
    "s_waitcnt lgkmcnt(4)\n\t" \
    "v_mfma_f32_16x16x32_bf16 v[208:211], v[144:147], v[32:35], v[208:211]\n\t" \
    "v_mfma_f32_16x16x32_bf16 v[196:199], v[148:151], v[36:39], v[196:199]\n\t" \
    "ds_read_b128 v[28:31], %[xp] offset:59392\n\t" \
    "v_mfma_f32_16x16x32_bf16 v[200:203], v[156:159], v[36:39], v[200:203]\n\t" \
    "ds_read_b128 v[32:35], %[xp] offset:60416\n\t" \
    "v_mfma_f32_16x16x32_bf16 v[204:207], v[148:151], v[40:43], v[204:207]\n\t" \
    "s_waitcnt lgkmcnt(4)\n\t" \
    "v_mfma_f32_16x16x32_bf16 v[208:211], v[156:159], v[40:43], v[208:211]\n\t" \
    "v_mfma_f32_16x16x32_bf16 v[196:199], v[152:155], v[44:47], v[196:199]\n\t" \
    "ds_read_b128 v[36:39], %[xp] offset:61440\n\t" \
    "v_mfma_f32_16x16x32_bf16 v[200:203], v[160:163], v[44:47], v[200:203]\n\t" \
    "ds_read_b128 v[40:43], %[xp] offset:62464\n\t" \
    "v_mfma_f32_16x16x32_bf16 v[204:207], v[152:155], v[48:51], v[204:207]\n\t" \
    "s_waitcnt lgkmcnt(4)\n\t" \
    "v_mfma_f32_16x16x32_bf16 v[208:211], v[160:163], v[48:51], v[208:211]\n\t" \
    "v_mfma_f32_16x16x32_bf16 v[196:199], v[164:167], v[20:23], v[196:199]\n\t" \
    "ds_read_b128 v[44:47], %[xp] offset:63488\n\t" \
    "v_mfma_f32_16x16x32_bf16 v[200:203], v[172:175], v[20:23], v[200:203]\n\t" \
    "ds_read_b128 v[48:51], %[xp] offset:64512\n\t" \
    "v_mfma_f32_16x16x32_bf16 v[204:207], v[164:167], v[24:27], v[204:207]\n\t" \
    "s_waitcnt lgkmcnt(4)\n\t" \
    "v_mfma_f32_16x16x32_bf16 v[208:211], v[172:175], v[24:27], v[208:211]\n\t" \
    "v_mfma_f32_16x16x32_bf16 v[196:199], v[168:171], v[28:31], v[196:199]\n\t" \
    "ds_read_b128 v[20:23], %[xp] offset:0\n\t" \
    "v_mfma_f32_16x16x32_bf16 v[200:203], v[176:179], v[28:31], v[200:203]\n\t" \
    "ds_read_b128 v[24:27], %[xp] offset:1024\n\t" \
    "v_mfma_f32_16x16x32_bf16 v[204:207], v[168:171], v[32:35], v[204:207]\n\t" \
    "s_waitcnt lgkmcnt(4)\n\t" \
    "v_mfma_f32_16x16x32_bf16 v[208:211], v[176:179], v[32:35], v[208:211]\n\t" \
    "v_mfma_f32_16x16x32_bf16 v[196:199], v[180:183], v[36:39], v[196:199]\n\t" \
    "ds_read_b128 v[28:31], %[xp] offset:2048\n\t" \
    "v_mfma_f32_16x16x32_bf16 v[200:203], v[188:191], v[36:39], v[200:203]\n\t" \
    "ds_read_b128 v[32:35], %[xp] offset:3072\n\t" \
    "v_mfma_f32_16x16x32_bf16 v[204:207], v[180:183], v[40:43], v[204:207]\n\t" \
    "s_waitcnt lgkmcnt(4)\n\t" \
    "v_mfma_f32_16x16x32_bf16 v[208:211], v[188:191], v[40:43], v[208:211]\n\t" \
    "v_mfma_f32_16x16x32_bf16 v[196:199], v[184:187], v[44:47], v[196:199]\n\t" \
    "ds_read_b128 v[36:39], %[xp] offset:4096\n\t" \
    "v_mfma_f32_16x16x32_bf16 v[200:203], v[192:195], v[44:47], v[200:203]\n\t" \
    "ds_read_b128 v[40:43], %[xp] offset:5120\n\t" \
    "v_mfma_f32_16x16x32_bf16 v[204:207], v[184:187], v[48:51], v[204:207]\n\t" \
    "s_waitcnt lgkmcnt(4)\n\t" \
    "v_mfma_f32_16x16x32_bf16 v[208:211], v[192:195], v[48:51], v[208:211]\n\t" \
    "s_add_u32 %[sx], %[sx], 262144\n\t" \
    "s_add_u32 %[t], %[t], 1\n\t" \
    "s_waitcnt vmcnt(4)\n\t" \
    "s_barrier\n\t" \
    "s_cmp_lt_u32 %[t], %[te]\n\t" \
    "s_cbranch_scc1 L0_%=\n\t" \
    "s_cmp_lg_u32 %[fin], 0\n\t" \
    "s_cbranch_scc0 E_%=\n\t" \
    "s_nop 4\n\t" \
    "ds_write_b128 %[pa], v[196:199] offset:0\n\t" \
    "ds_write_b128 %[pa], v[200:203] offset:1024\n\t" \
    "ds_write_b128 %[pa], v[204:207] offset:2048\n\t" \
    "ds_write_b128 %[pa], v[208:211] offset:3072\n\t" \
    "s_waitcnt lgkmcnt(0)\n\t" \
    "s_barrier\n\t" \
    "s_branch E_%=\n\t" \
    "L1_%=:\n\t" \
    "s_setprio 1\n\t" \
    "s_add_u32 m0, %[sd], 49152\n\t" \
    "s_add_u32 %[a], %[sx], 278528\n\t" \
    "v_mfma_f32_16x16x32_bf16 v[0:3], v[68:71], v[20:23], 0\n\t" \
    "ds_read_b128 v[44:47], %[xp] offset:6144\n\t" \
    "v_mfma_f32_16x16x32_bf16 v[4:7], v[76:79], v[20:23], 0\n\t" \
    "ds_read_b128 v[48:51], %[xp] offset:7168\n\t" \
    "v_mfma_f32_16x16x32_bf16 v[8:11], v[68:71], v[24:27], 0\n\t" \
    "s_waitcnt lgkmcnt(4)\n\t" \
    "v_mfma_f32_16x16x32_bf16 v[12:15], v[76:79], v[24:27], 0\n\t" \
    "v_mfma_f32_16x16x32_bf16 v[0:3], v[72:75], v[28:31], v[0:3]\n\t" \
    "ds_read_b128 v[20:23], %[xp] offset:8192\n\t" \
    "v_mfma_f32_16x16x32_bf16 v[4:7], v[80:83], v[28:31], v[4:7]\n\t" \
    "ds_read_b128 v[24:27], %[xp] offset:9216\n\t" \
    "v_mfma_f32_16x16x32_bf16 v[8:11], v[72:75], v[32:35], v[8:11]\n\t" \
    "s_waitcnt lgkmcnt(4)\n\t" \
    "v_mfma_f32_16x16x32_bf16 v[12:15], v[80:83], v[32:35], v[12:15]\n\t" \
    "v_mfma_f32_16x16x32_bf16 v[0:3], v[84:87], v[36:39], v[0:3]\n\t" \
    "ds_read_b128 v[28:31], %[xp] offset:10240\n\t" \
    "v_mfma_f32_16x16x32_bf16 v[4:7], v[92:95], v[36:39], v[4:7]\n\t" \
    "ds_read_b128 v[32:35], %[xp] offset:11264\n\t" \
    "v_mfma_f32_16x16x32_bf16 v[8:11], v[84:87], v[40:43], v[8:11]\n\t" \
    "s_waitcnt lgkmcnt(4)\n\t" \
    "v_mfma_f32_16x16x32_bf16 v[12:15], v[92:95], v[40:43], v[12:15]\n\t" \
    "v_mfma_f32_16x16x32_bf16 v[0:3], v[88:91], v[44:47], v[0:3]\n\t" \
    "ds_read_b128 v[36:39], %[xp] offset:12288\n\t" \
    "v_mfma_f32_16x16x32_bf16 v[4:7], v[96:99], v[44:47], v[4:7]\n\t" \
    "ds_read_b128 v[40:43], %[xp] offset:13312\n\t" \
    "v_mfma_f32_16x16x32_bf16 v[8:11], v[88:91], v[48:51], v[8:11]\n\t" \
    "s_waitcnt lgkmcnt(4)\n\t" \
    "v_mfma_f32_16x16x32_bf16 v[12:15], v[96:99], v[48:51], v[12:15]\n\t" \
    "v_mfma_f32_16x16x32_bf16 v[0:3], v[100:103], v[20:23], v[0:3]\n\t" \
    "ds_read_b128 v[44:47], %[xp] offset:14336\n\t" \
    "v_mfma_f32_16x16x32_bf16 v[4:7], v[108:111], v[20:23], v[4:7]\n\t" \
    "ds_read_b128 v[48:51], %[xp] offset:15360\n\t" \
    "v_mfma_f32_16x16x32_bf16 v[8:11], v[100:103], v[24:27], v[8:11]\n\t" \
    "s_waitcnt lgkmcnt(4)\n\t" \
    "v_mfma_f32_16x16x32_bf16 v[12:15], v[108:111], v[24:27], v[12:15]\n\t" \
    "buffer_load_dwordx4 %[l16], %[rs], %[a] offen offset:0 sc1 lds\n\t" \
    "v_mfma_f32_16x16x32_bf16 v[0:3], v[104:107], v[28:31], v[0:3]\n\t" \
    "ds_read_b128 v[20:23], %[xp] offset:16384\n\t" \
    "v_mfma_f32_16x16x32_bf16 v[4:7], v[112:115], v[28:31], v[4:7]\n\t" \
    "ds_read_b128 v[24:27], %[xp] offset:17408\n\t" \
    "v_mfma_f32_16x16x32_bf16 v[8:11], v[104:107], v[32:35], v[8:11]\n\t" \
    "s_waitcnt lgkmcnt(4)\n\t" \
    "v_mfma_f32_16x16x32_bf16 v[12:15], v[112:115], v[32:35], v[12:15]\n\t" \
    "buffer_load_dwordx4 %[l16], %[rs], %[a] offen offset:1024 sc1 lds\n\t" \
    "v_mfma_f32_16x16x32_bf16 v[0:3], v[116:119], v[36:39], v[0:3]\n\t" \
    "ds_read_b128 v[28:31], %[xp] offset:18432\n\t" \
    "v_mfma_f32_16x16x32_bf16 v[4:7], v[124:127], v[36:39], v[4:7]\n\t" \
    "ds_read_b128 v[32:35], %[xp] offset:19456\n\t" \
    "v_mfma_f32_16x16x32_bf16 v[8:11], v[116:119], v[40:43], v[8:11]\n\t" \
    "s_waitcnt lgkmcnt(4)\n\t" \
    "v_mfma_f32_16x16x32_bf16 v[12:15], v[124:127], v[40:43], v[12:15]\n\t" \
    "buffer_load_dwordx4 %[l16], %[rs], %[a] offen offset:2048 sc1 lds\n\t" \
    "v_mfma_f32_16x16x32_bf16 v[0:3], v[120:123], v[44:47], v[0:3]\n\t" \
    "ds_read_b128 v[36:39], %[xp] offset:20480\n\t" \
    "v_mfma_f32_16x16x32_bf16 v[4:7], v[128:131], v[44:47], v[4:7]\n\t" \
    "ds_read_b128 v[40:43], %[xp] offset:21504\n\t" \
    "v_mfma_f32_16x16x32_bf16 v[8:11], v[120:123], v[48:51], v[8:11]\n\t" \
    "s_waitcnt lgkmcnt(4)\n\t" \
    "v_mfma_f32_16x16x32_bf16 v[12:15], v[128:131], v[48:51], v[12:15]\n\t" \
    "buffer_load_dwordx4 %[l16], %[rs], %[a] offen offset:3072 sc1 lds\n\t" \
    "s_waitcnt vmcnt(4)\n\t" \
    "s_barrier\n\t" \
    "s_setprio 1\n\t" \
    "s_add_u32 m0, %[sd], 0\n\t" \
    "s_add_u32 %[a], %[sx], 524288\n\t" \
    "s_add_u32 %[b], %[oo], 0x1000\n\t" \
    "s_cmp_lg_u32 %[t], 0\n\t" \
    "s_cselect_b64 %[em], -1, 0\n\t" \
    "v_mfma_f32_16x16x32_bf16 v[0:3], v[132:135], v[20:23], v[0:3]\n\t" \
    "ds_read_b128 v[44:47], %[xp] offset:22528\n\t" \
    "v_mfma_f32_16x16x32_bf16 v[4:7], v[140:143], v[20:23], v[4:7]\n\t" \
    "ds_read_b128 v[48:51], %[xp] offset:23552\n\t" \
    "v_mfma_f32_16x16x32_bf16 v[8:11], v[132:135], v[24:27], v[8:11]\n\t" \
    "ds_read_b128 v[52:55], %[pa] offset:0\n\t" \
    "s_waitcnt lgkmcnt(5)\n\t" \
    "v_mfma_f32_16x16x32_bf16 v[12:15], v[140:143], v[24:27], v[12:15]\n\t" \
    "buffer_load_dwordx4 %[l16], %[rs], %[a] offen offset:0 sc1 lds\n\t" \
    "v_mfma_f32_16x16x32_bf16 v[0:3], v[136:139], v[28:31], v[0:3]\n\t" \
    "ds_read_b128 v[20:23], %[xp] offset:24576\n\t" \
    "v_mfma_f32_16x16x32_bf16 v[4:7], v[144:147], v[28:31], v[4:7]\n\t" \
    "ds_read_b128 v[24:27], %[xp] offset:25600\n\t" \
    "v_mfma_f32_16x16x32_bf16 v[8:11], v[136:139], v[32:35], v[8:11]\n\t" \
    "ds_read_b128 v[56:59], %[pa] offset:1024\n\t" \
    "s_waitcnt lgkmcnt(6)\n\t" \
    "v_mfma_f32_16x16x32_bf16 v[12:15], v[144:147], v[32:35], v[12:15]\n\t" \
    "buffer_load_dwordx4 %[l16], %[rs], %[a] offen offset:1024 sc1 lds\n\t" \
    "v_mfma_f32_16x16x32_bf16 v[0:3], v[148:151], v[36:39], v[0:3]\n\t" \
    "ds_read_b128 v[28:31], %[xp] offset:26624\n\t" \
    "v_mfma_f32_16x16x32_bf16 v[4:7], v[156:159], v[36:39], v[4:7]\n\t" \
    "ds_read_b128 v[32:35], %[xp] offset:27648\n\t" \
    "v_mfma_f32_16x16x32_bf16 v[8:11], v[148:151], v[40:43], v[8:11]\n\t" \
    "ds_read_b128 v[60:63], %[pa] offset:2048\n\t" \
    "s_waitcnt lgkmcnt(7)\n\t" \
    "v_mfma_f32_16x16x32_bf16 v[12:15], v[156:159], v[40:43], v[12:15]\n\t" \
    "buffer_load_dwordx4 %[l16], %[rs], %[a] offen offset:2048 sc1 lds\n\t" \
    "v_mfma_f32_16x16x32_bf16 v[0:3], v[152:155], v[44:47], v[0:3]\n\t" \
    "ds_read_b128 v[36:39], %[xp] offset:28672\n\t" \
    "s_waitcnt lgkmcnt(7)\n\t" \
    "v_add_f32 v196, v196, v52\n\t" \
    "v_mfma_f32_16x16x32_bf16 v[4:7], v[160:163], v[44:47], v[4:7]\n\t" \
    "ds_read_b128 v[40:43], %[xp] offset:29696\n\t" \
    "v_add_f32 v197, v197, v53\n\t" \
    "v_mfma_f32_16x16x32_bf16 v[8:11], v[152:155], v[48:51], v[8:11]\n\t" \
    "ds_read_b128 v[64:67], %[pa] offset:3072\n\t" \
    "s_waitcnt lgkmcnt(7)\n\t" \
    "v_mfma_f32_16x16x32_bf16 v[12:15], v[160:163], v[48:51], v[12:15]\n\t" \
    "buffer_load_dwordx4 %[l16], %[rs], %[a] offen offset:3072 sc1 lds\n\t" \
    "v_add_f32 v198, v198, v54\n\t" \
    "v_add_f32 v199, v199, v55\n\t" \
    "v_mfma_f32_16x16x32_bf16 v[0:3], v[164:167], v[20:23], v[0:3]\n\t" \
    "ds_read_b128 v[44:47], %[xp] offset:30720\n\t" \
    "s_waitcnt lgkmcnt(7)\n\t" \
    "v_add_f32 v200, v200, v56\n\t" \
    "v_mfma_f32_16x16x32_bf16 v[4:7], v[172:175], v[20:23], v[4:7]\n\t" \
    "ds_read_b128 v[48:51], %[xp] offset:31744\n\t" \
    "v_add_f32 v201, v201, v57\n\t" \
    "v_mfma_f32_16x16x32_bf16 v[8:11], v[164:167], v[24:27], v[8:11]\n\t" \
    "v_add_f32 v202, v202, v58\n\t" \
    "s_waitcnt lgkmcnt(6)\n\t" \
    "v_mfma_f32_16x16x32_bf16 v[12:15], v[172:175], v[24:27], v[12:15]\n\t" \
    "v_add_f32 v203, v203, v59\n\t" \
    "s_waitcnt lgkmcnt(5)\n\t" \
    "v_add_f32 v204, v204, v60\n\t" \
    "v_add_f32 v205, v205, v61\n\t" \
    "v_mfma_f32_16x16x32_bf16 v[0:3], v[168:171], v[28:31], v[0:3]\n\t" \
    "ds_read_b128 v[20:23], %[xp] offset:32768\n\t" \
    "v_add_f32 v206, v206, v62\n\t" \
    "v_mfma_f32_16x16x32_bf16 v[4:7], v[176:179], v[28:31], v[4:7]\n\t" \
    "ds_read_b128 v[24:27], %[xp] offset:33792\n\t" \
    "v_add_f32 v207, v207, v63\n\t" \
    "v_mfma_f32_16x16x32_bf16 v[8:11], v[168:171], v[32:35], v[8:11]\n\t" \
    "s_waitcnt lgkmcnt(4)\n\t" \
    "v_add_f32 v208, v208, v64\n\t" \
    "v_mfma_f32_16x16x32_bf16 v[12:15], v[176:179], v[32:35], v[12:15]\n\t" \
    "v_add_f32 v209, v209, v65\n\t" \
    "v_add_f32 v210, v210, v66\n\t" \
    "v_add_f32 v211, v211, v67\n\t" \
    "v_mfma_f32_16x16x32_bf16 v[0:3], v[180:183], v[36:39], v[0:3]\n\t" \
    "ds_read_b128 v[28:31], %[xp] offset:34816\n\t" \
    "v_mfma_f32_16x16x32_bf16 v[4:7], v[188:191], v[36:39], v[4:7]\n\t" \
    "ds_read_b128 v[32:35], %[xp] offset:35840\n\t" \
    "s_mov_b64 exec, %[em]\n\t" \
    "buffer_store_dwordx4 v[196:199], %[l16], %[rs], %[oo] offen offset:0 sc1\n\t" \
    "s_mov_b64 exec, -1\n\t" \
    "v_mfma_f32_16x16x32_bf16 v[8:11], v[180:183], v[40:43], v[8:11]\n\t" \
    "s_waitcnt lgkmcnt(4)\n\t" \
    "v_mfma_f32_16x16x32_bf16 v[12:15], v[188:191], v[40:43], v[12:15]\n\t" \
    "s_mov_b64 exec, %[em]\n\t" \
    "buffer_store_dwordx4 v[200:203], %[l16], %[rs], %[oo] offen offset:1024 sc1\n\t" \
    "s_mov_b64 exec, -1\n\t" \
    "v_mfma_f32_16x16x32_bf16 v[0:3], v[184:187], v[44:47], v[0:3]\n\t" \
    "ds_read_b128 v[36:39], %[xp] offset:36864\n\t" \
    "v_mfma_f32_16x16x32_bf16 v[4:7], v[192:195], v[44:47], v[4:7]\n\t" \
    "ds_read_b128 v[40:43], %[xp] offset:37888\n\t" \
    "s_mov_b64 exec, %[em]\n\t" \
    "buffer_store_dwordx4 v[204:207], %[l16], %[rs], %[oo] offen offset:2048 sc1\n\t" \
    "s_mov_b64 exec, -1\n\t" \
    "v_mfma_f32_16x16x32_bf16 v[8:11], v[184:187], v[48:51], v[8:11]\n\t" \
    "s_waitcnt lgkmcnt(4)\n\t" \
    "v_mfma_f32_16x16x32_bf16 v[12:15], v[192:195], v[48:51], v[12:15]\n\t" \
    "s_mov_b64 exec, %[em]\n\t" \
    "buffer_store_dwordx4 v[208:211], %[l16], %[rs], %[oo] offen offset:3072 sc1\n\t" \
    "s_mov_b64 exec, -1\n\t" \
    "s_add_u32 %[sx], %[sx], 262144\n\t" \
    "s_add_u32 %[t], %[t], 1\n\t" \
    "s_add_u32 %[oo], %[oo], 524288\n\t" \
    "s_waitcnt vmcnt(8)\n\t" \
    "s_cmp_gt_u32 %[t], " STR(SQUICK) " + 1\n\t" \
    "s_cbranch_scc1 B0_%=\n\t" \
    "s_waitcnt vmcnt(0)\n\t" \
    "B0_%=:\n\t" \
    "s_barrier\n\t" \
    "s_setprio 1\n\t" \
    "s_add_u32 m0, %[sd], 16384\n\t" \
    "s_add_u32 %[a], %[sx], 278528\n\t" \
    "v_mfma_f32_16x16x32_bf16 v[196:199], v[68:71], v[20:23], 0\n\t" \
    "ds_read_b128 v[44:47], %[xp] offset:38912\n\t" \
    "v_mfma_f32_16x16x32_bf16 v[200:203], v[76:79], v[20:23], 0\n\t" \
    "ds_read_b128 v[48:51], %[xp] offset:39936\n\t" \
    "v_mfma_f32_16x16x32_bf16 v[204:207], v[68:71], v[24:27], 0\n\t" \
    "s_waitcnt lgkmcnt(4)\n\t" \
    "v_mfma_f32_16x16x32_bf16 v[208:211], v[76:79], v[24:27], 0\n\t" \
    "v_mfma_f32_16x16x32_bf16 v[196:199], v[72:75], v[28:31], v[196:199]\n\t" \
    "ds_read_b128 v[20:23], %[xp] offset:40960\n\t" \
    "v_mfma_f32_16x16x32_bf16 v[200:203], v[80:83], v[28:31], v[200:203]\n\t" \
    "ds_read_b128 v[24:27], %[xp] offset:41984\n\t" \
    "v_mfma_f32_16x16x32_bf16 v[204:207], v[72:75], v[32:35], v[204:207]\n\t" \
    "s_waitcnt lgkmcnt(4)\n\t" \
    "v_mfma_f32_16x16x32_bf16 v[208:211], v[80:83], v[32:35], v[208:211]\n\t" \
    "v_mfma_f32_16x16x32_bf16 v[196:199], v[84:87], v[36:39], v[196:199]\n\t" \
    "ds_read_b128 v[28:31], %[xp] offset:43008\n\t" \
    "v_mfma_f32_16x16x32_bf16 v[200:203], v[92:95], v[36:39], v[200:203]\n\t" \
    "ds_read_b128 v[32:35], %[xp] offset:44032\n\t" \
    "v_mfma_f32_16x16x32_bf16 v[204:207], v[84:87], v[40:43], v[204:207]\n\t" \
    "s_waitcnt lgkmcnt(4)\n\t" \
    "v_mfma_f32_16x16x32_bf16 v[208:211], v[92:95], v[40:43], v[208:211]\n\t" \
    "v_mfma_f32_16x16x32_bf16 v[196:199], v[88:91], v[44:47], v[196:199]\n\t" \
    "ds_read_b128 v[36:39], %[xp] offset:45056\n\t" \
    "v_mfma_f32_16x16x32_bf16 v[200:203], v[96:99], v[44:47], v[200:203]\n\t" \
    "ds_read_b128 v[40:43], %[xp] offset:46080\n\t" \
    "v_mfma_f32_16x16x32_bf16 v[204:207], v[88:91], v[48:51], v[204:207]\n\t" \
    "s_waitcnt lgkmcnt(4)\n\t" \
    "v_mfma_f32_16x16x32_bf16 v[208:211], v[96:99], v[48:51], v[208:211]\n\t" \
    "v_mfma_f32_16x16x32_bf16 v[196:199], v[100:103], v[20:23], v[196:199]\n\t" \
    "ds_read_b128 v[44:47], %[xp] offset:47104\n\t" \
    "v_mfma_f32_16x16x32_bf16 v[200:203], v[108:111], v[20:23], v[200:203]\n\t" \
    "ds_read_b128 v[48:51], %[xp] offset:48128\n\t" \
    "v_mfma_f32_16x16x32_bf16 v[204:207], v[100:103], v[24:27], v[204:207]\n\t" \
    "s_waitcnt lgkmcnt(4)\n\t" \
    "v_mfma_f32_16x16x32_bf16 v[208:211], v[108:111], v[24:27], v[208:211]\n\t" \
    "buffer_load_dwordx4 %[l16], %[rs], %[a] offen offset:0 sc1 lds\n\t" \
    "v_mfma_f32_16x16x32_bf16 v[196:199], v[104:107], v[28:31], v[196:199]\n\t" \
    "ds_read_b128 v[20:23], %[xp] offset:49152\n\t" \
    "v_mfma_f32_16x16x32_bf16 v[200:203], v[112:115], v[28:31], v[200:203]\n\t" \
    "ds_read_b128 v[24:27], %[xp] offset:50176\n\t" \
    "v_mfma_f32_16x16x32_bf16 v[204:207], v[104:107], v[32:35], v[204:207]\n\t" \
    "s_waitcnt lgkmcnt(4)\n\t" \
    "v_mfma_f32_16x16x32_bf16 v[208:211], v[112:115], v[32:35], v[208:211]\n\t" \
    "buffer_load_dwordx4 %[l16], %[rs], %[a] offen offset:1024 sc1 lds\n\t" \
    "v_mfma_f32_16x16x32_bf16 v[196:199], v[116:119], v[36:39], v[196:199]\n\t" \
    "ds_read_b128 v[28:31], %[xp] offset:51200\n\t" \
    "v_mfma_f32_16x16x32_bf16 v[200:203], v[124:127], v[36:39], v[200:203]\n\t" \
    "ds_read_b128 v[32:35], %[xp] offset:52224\n\t" \
    "v_mfma_f32_16x16x32_bf16 v[204:207], v[116:119], v[40:43], v[204:207]\n\t" \
    "s_waitcnt lgkmcnt(4)\n\t" \
    "v_mfma_f32_16x16x32_bf16 v[208:211], v[124:127], v[40:43], v[208:211]\n\t" \
    "buffer_load_dwordx4 %[l16], %[rs], %[a] offen offset:2048 sc1 lds\n\t" \
    "v_mfma_f32_16x16x32_bf16 v[196:199], v[120:123], v[44:47], v[196:199]\n\t" \
    "ds_read_b128 v[36:39], %[xp] offset:53248\n\t" \
    "v_mfma_f32_16x16x32_bf16 v[200:203], v[128:131], v[44:47], v[200:203]\n\t" \
    "ds_read_b128 v[40:43], %[xp] offset:54272\n\t" \
    "v_mfma_f32_16x16x32_bf16 v[204:207], v[120:123], v[48:51], v[204:207]\n\t" \
    "s_waitcnt lgkmcnt(4)\n\t" \
    "v_mfma_f32_16x16x32_bf16 v[208:211], v[128:131], v[48:51], v[208:211]\n\t" \
    "buffer_load_dwordx4 %[l16], %[rs], %[a] offen offset:3072 sc1 lds\n\t" \
    "s_waitcnt vmcnt(4)\n\t" \
    "s_barrier\n\t" \
    "s_setprio 1\n\t" \
    "s_add_u32 m0, %[sd], 32768\n\t" \
    "s_add_u32 %[a], %[sx], 524288\n\t" \
    "s_add_u32 %[b], %[oo], 0x1000\n\t" \
    "s_cmp_lg_u32 %[t], 0\n\t" \
    "s_cselect_b64 %[em], -1, 0\n\t" \
    "v_mfma_f32_16x16x32_bf16 v[196:199], v[132:135], v[20:23], v[196:199]\n\t" \
    "ds_read_b128 v[44:47], %[xp] offset:55296\n\t" \
    "v_mfma_f32_16x16x32_bf16 v[200:203], v[140:143], v[20:23], v[200:203]\n\t" \
    "ds_read_b128 v[48:51], %[xp] offset:56320\n\t" \
    "v_mfma_f32_16x16x32_bf16 v[204:207], v[132:135], v[24:27], v[204:207]\n\t" \
    "ds_read_b128 v[52:55], %[pa] offset:0\n\t" \
    "s_waitcnt lgkmcnt(5)\n\t" \
    "v_mfma_f32_16x16x32_bf16 v[208:211], v[140:143], v[24:27], v[208:211]\n\t" \
    "buffer_load_dwordx4 %[l16], %[rs], %[a] offen offset:0 sc1 lds\n\t" \
    "v_mfma_f32_16x16x32_bf16 v[196:199], v[136:139], v[28:31], v[196:199]\n\t" \
    "ds_read_b128 v[20:23], %[xp] offset:57344\n\t" \
    "v_mfma_f32_16x16x32_bf16 v[200:203], v[144:147], v[28:31], v[200:203]\n\t" \
    "ds_read_b128 v[24:27], %[xp] offset:58368\n\t" \
    "v_mfma_f32_16x16x32_bf16 v[204:207], v[136:139], v[32:35], v[204:207]\n\t" \
    "ds_read_b128 v[56:59], %[pa] offset:1024\n\t" \
    "s_waitcnt lgkmcnt(6)\n\t" \
    "v_mfma_f32_16x16x32_bf16 v[208:211], v[144:147], v[32:35], v[208:211]\n\t" \
    "buffer_load_dwordx4 %[l16], %[rs], %[a] offen offset:1024 sc1 lds\n\t" \
    "v_mfma_f32_16x16x32_bf16 v[196:199], v[148:151], v[36:39], v[196:199]\n\t" \
    "ds_read_b128 v[28:31], %[xp] offset:59392\n\t" \
    "v_mfma_f32_16x16x32_bf16 v[200:203], v[156:159], v[36:39], v[200:203]\n\t" \
    "ds_read_b128 v[32:35], %[xp] offset:60416\n\t" \
    "v_mfma_f32_16x16x32_bf16 v[204:207], v[148:151], v[40:43], v[204:207]\n\t" \
    "ds_read_b128 v[60:63], %[pa] offset:2048\n\t" \
    "s_waitcnt lgkmcnt(7)\n\t" \
    "v_mfma_f32_16x16x32_bf16 v[208:211], v[156:159], v[40:43], v[208:211]\n\t" \
    "buffer_load_dwordx4 %[l16], %[rs], %[a] offen offset:2048 sc1 lds\n\t" \
    "v_mfma_f32_16x16x32_bf16 v[196:199], v[152:155], v[44:47], v[196:199]\n\t" \
    "ds_read_b128 v[36:39], %[xp] offset:61440\n\t" \
    "s_waitcnt lgkmcnt(7)\n\t" \
    "v_add_f32 v0, v0, v52\n\t" \
    "v_mfma_f32_16x16x32_bf16 v[200:203], v[160:163], v[44:47], v[200:203]\n\t" \
    "ds_read_b128 v[40:43], %[xp] offset:62464\n\t" \
    "v_add_f32 v1, v1, v53\n\t" \
    "v_mfma_f32_16x16x32_bf16 v[204:207], v[152:155], v[48:51], v[204:207]\n\t" \
    "ds_read_b128 v[64:67], %[pa] offset:3072\n\t" \
    "s_waitcnt lgkmcnt(7)\n\t" \
    "v_mfma_f32_16x16x32_bf16 v[208:211], v[160:163], v[48:51], v[208:211]\n\t" \
    "buffer_load_dwordx4 %[l16], %[rs], %[a] offen offset:3072 sc1 lds\n\t" \
    "v_add_f32 v2, v2, v54\n\t" \
    "v_add_f32 v3, v3, v55\n\t" \
    "v_mfma_f32_16x16x32_bf16 v[196:199], v[164:167], v[20:23], v[196:199]\n\t" \
    "ds_read_b128 v[44:47], %[xp] offset:63488\n\t" \
    "s_waitcnt lgkmcnt(7)\n\t" \
    "v_add_f32 v4, v4, v56\n\t" \
    "v_mfma_f32_16x16x32_bf16 v[200:203], v[172:175], v[20:23], v[200:203]\n\t" \
    "ds_read_b128 v[48:51], %[xp] offset:64512\n\t" \
    "v_add_f32 v5, v5, v57\n\t" \
    "v_mfma_f32_16x16x32_bf16 v[204:207], v[164:167], v[24:27], v[204:207]\n\t" \
    "v_add_f32 v6, v6, v58\n\t" \
    "s_waitcnt lgkmcnt(6)\n\t" \
    "v_mfma_f32_16x16x32_bf16 v[208:211], v[172:175], v[24:27], v[208:211]\n\t" \
    "v_add_f32 v7, v7, v59\n\t" \
    "s_waitcnt lgkmcnt(5)\n\t" \
    "v_add_f32 v8, v8, v60\n\t" \
    "v_add_f32 v9, v9, v61\n\t" \
    "v_mfma_f32_16x16x32_bf16 v[196:199], v[168:171], v[28:31], v[196:199]\n\t" \
    "ds_read_b128 v[20:23], %[xp] offset:0\n\t" \
    "v_add_f32 v10, v10, v62\n\t" \
    "v_mfma_f32_16x16x32_bf16 v[200:203], v[176:179], v[28:31], v[200:203]\n\t" \
    "ds_read_b128 v[24:27], %[xp] offset:1024\n\t" \
    "v_add_f32 v11, v11, v63\n\t" \
    "v_mfma_f32_16x16x32_bf16 v[204:207], v[168:171], v[32:35], v[204:207]\n\t" \
    "s_waitcnt lgkmcnt(4)\n\t" \
    "v_add_f32 v12, v12, v64\n\t" \
    "v_mfma_f32_16x16x32_bf16 v[208:211], v[176:179], v[32:35], v[208:211]\n\t" \
    "v_add_f32 v13, v13, v65\n\t" \
    "v_add_f32 v14, v14, v66\n\t" \
    "v_add_f32 v15, v15, v67\n\t" \
    "v_mfma_f32_16x16x32_bf16 v[196:199], v[180:183], v[36:39], v[196:199]\n\t" \
    "ds_read_b128 v[28:31], %[xp] offset:2048\n\t" \
    "v_mfma_f32_16x16x32_bf16 v[200:203], v[188:191], v[36:39], v[200:203]\n\t" \
    "ds_read_b128 v[32:35], %[xp] offset:3072\n\t" \
    "s_mov_b64 exec, %[em]\n\t" \
    "buffer_store_dwordx4 v[0:3], %[l16], %[rs], %[oo] offen offset:0 sc1\n\t" \
    "s_mov_b64 exec, -1\n\t" \
    "v_mfma_f32_16x16x32_bf16 v[204:207], v[180:183], v[40:43], v[204:207]\n\t" \
    "s_waitcnt lgkmcnt(4)\n\t" \
    "v_mfma_f32_16x16x32_bf16 v[208:211], v[188:191], v[40:43], v[208:211]\n\t" \
    "s_mov_b64 exec, %[em]\n\t" \
    "buffer_store_dwordx4 v[4:7], %[l16], %[rs], %[oo] offen offset:1024 sc1\n\t" \
    "s_mov_b64 exec, -1\n\t" \
    "v_mfma_f32_16x16x32_bf16 v[196:199], v[184:187], v[44:47], v[196:199]\n\t" \
    "ds_read_b128 v[36:39], %[xp] offset:4096\n\t" \
    "v_mfma_f32_16x16x32_bf16 v[200:203], v[192:195], v[44:47], v[200:203]\n\t" \
    "ds_read_b128 v[40:43], %[xp] offset:5120\n\t" \
    "s_mov_b64 exec, %[em]\n\t" \
    "buffer_store_dwordx4 v[8:11], %[l16], %[rs], %[oo] offen offset:2048 sc1\n\t" \
    "s_mov_b64 exec, -1\n\t" \
    "v_mfma_f32_16x16x32_bf16 v[204:207], v[184:187], v[48:51], v[204:207]\n\t" \
    "s_waitcnt lgkmcnt(4)\n\t" \
    "v_mfma_f32_16x16x32_bf16 v[208:211], v[192:195], v[48:51], v[208:211]\n\t" \
    "s_mov_b64 exec, %[em]\n\t" \
    "buffer_store_dwordx4 v[12:15], %[l16], %[rs], %[oo] offen offset:3072 sc1\n\t" \
    "s_mov_b64 exec, -1\n\t" \
    "s_add_u32 %[sx], %[sx], 262144\n\t" \
    "s_add_u32 %[t], %[t], 1\n\t" \
    "s_add_u32 %[oo], %[oo], 524288\n\t" \
    "s_waitcnt vmcnt(8)\n\t" \
    "s_cmp_gt_u32 %[t], " STR(SQUICK) " + 1\n\t" \
    "s_cbranch_scc1 B1_%=\n\t" \
    "s_waitcnt vmcnt(0)\n\t" \
    "B1_%=:\n\t" \
    "s_barrier\n\t" \
    "s_cmp_lt_u32 %[t], %[te]\n\t" \
    "s_cbranch_scc1 L1_%=\n\t" \
    "s_cmp_lg_u32 %[fin], 0\n\t" \
    "s_cbranch_scc0 E_%=\n\t" \
    "s_nop 4\n\t" \
    "s_barrier\n\t" \
    "ds_read_b128 v[52:55], %[pa] offset:0\n\t" \
    "ds_read_b128 v[56:59], %[pa] offset:1024\n\t" \
    "ds_read_b128 v[60:63], %[pa] offset:2048\n\t" \
    "ds_read_b128 v[64:67], %[pa] offset:3072\n\t" \
    "s_add_u32 %[b], %[oo], 0x1000\n\t" \
    "s_waitcnt lgkmcnt(0)\n\t" \
    "v_add_f32 v196, v196, v52\n\t" \
    "v_add_f32 v197, v197, v53\n\t" \
    "v_add_f32 v198, v198, v54\n\t" \
    "v_add_f32 v199, v199, v55\n\t" \
    "v_add_f32 v200, v200, v56\n\t" \
    "v_add_f32 v201, v201, v57\n\t" \
    "v_add_f32 v202, v202, v58\n\t" \
    "v_add_f32 v203, v203, v59\n\t" \
    "v_add_f32 v204, v204, v60\n\t" \
    "v_add_f32 v205, v205, v61\n\t" \
    "v_add_f32 v206, v206, v62\n\t" \
    "v_add_f32 v207, v207, v63\n\t" \
    "v_add_f32 v208, v208, v64\n\t" \
    "v_add_f32 v209, v209, v65\n\t" \
    "v_add_f32 v210, v210, v66\n\t" \
    "v_add_f32 v211, v211, v67\n\t" \
    "buffer_store_dwordx4 v[196:199], %[l16], %[rs], %[oo] offen offset:0 sc1\n\t" \
    "buffer_store_dwordx4 v[200:203], %[l16], %[rs], %[oo] offen offset:1024 sc1\n\t" \
    "buffer_store_dwordx4 v[204:207], %[l16], %[rs], %[oo] offen offset:2048 sc1\n\t" \
    "buffer_store_dwordx4 v[208:211], %[l16], %[rs], %[oo] offen offset:3072 sc1\n\t" \
    "E_%=:\n\t" \
    "s_setprio 0\n\t" \
    "s_waitcnt lgkmcnt(0)\n\t" \
    "s_nop 7\n\t" \
    : [t] "+s"(t), [sx] "+s"(sx), [oo] "+s"(oo), [a] "=&s"(tmp), [b] "=&s"(tmp2), [em] "=&s"(em), "+{v[20:27]}"(xf[0]), "+{v[28:35]}"(xf[1]), "+{v[36:43]}"(xf[2]), "+{v[196:203]}"(ac[0]), "+{v[204:211]}"(ac[1]) \
    : [te] "s"(TE_), [fin] "s"(FIN_), [kh] "s"(kh), [sd] "s"(sd), [rs] "s"(rs), [l16] "v"(l16), [xp] "v"(xp), [pa] "v"(pa), [fm] "s"(fm), [pm] "s"(pm), [fo] "s"(fo), [po] "s"(po), [pd] "s"(pd), [tag] "s"(P.tag), "{v[68:75]}"(w[0][0]), "{v[76:83]}"(w[0][1]), "{v[84:91]}"(w[1][0]), "{v[92:99]}"(w[1][1]), "{v[100:107]}"(w[2][0]), "{v[108:115]}"(w[2][1]), "{v[116:123]}"(w[3][0]), "{v[124:131]}"(w[3][1]), "{v[132:139]}"(w[4][0]), "{v[140:147]}"(w[4][1]), "{v[148:155]}"(w[5][0]), "{v[156:163]}"(w[5][1]), "{v[164:171]}"(w[6][0]), "{v[172:179]}"(w[6][1]), "{v[180:187]}"(w[7][0]), "{v[188:195]}"(w[7][1]) \
    : "memory", "scc", "v0", "v1", "v2", "v3", "v4", "v5", "v6", "v7", "v8", "v9", "v10", "v11", "v12", "v13", "v14", "v15", "v44", "v45", "v46", "v47", "v48", "v49", "v50", "v51", "v52", "v53", "v54", "v55", "v56", "v57", "v58", "v59", "v60", "v61", "v62", "v63", "v64", "v65", "v66", "v67")
#define AQ_PIN0() asm volatile("" : "+{v[28:35]}"(wr[0][0]), "+{v[36:43]}"(wr[0][1]))
#define AQ_PIN1() asm volatile("" : "+{v[44:51]}"(wr[1][0]), "+{v[52:59]}"(wr[1][1]))
#define AQ_PIN2() asm volatile("" : "+{v[60:67]}"(wr[2][0]), "+{v[68:75]}"(wr[2][1]))
#define AQ_PIN3() asm volatile("" : "+{v[76:83]}"(wr[3][0]), "+{v[84:91]}"(wr[3][1]))
#define AQ_PIN4() asm volatile("" : "+{v[92:99]}"(wr[4][0]), "+{v[100:107]}"(wr[4][1]))
#define AQ_PIN5() asm volatile("" : "+{v[108:115]}"(wr[5][0]), "+{v[116:123]}"(wr[5][1]))
#define AQ_PIN6() asm volatile("" : "+{v[124:131]}"(wr[6][0]), "+{v[132:139]}"(wr[6][1]))
#define AQ_PIN7() asm volatile("" : "+{v[140:147]}"(wr[7][0]), "+{v[148:155]}"(wr[7][1]))
#define AQ_PIN8() asm volatile("" : "+{v[156:163]}"(wr[8][0]), "+{v[164:171]}"(wr[8][1]))
#define AQ_PIN9() asm volatile("" : "+{v[172:179]}"(wr[9][0]), "+{v[180:187]}"(wr[9][1]))
#define AQ_PIN10() asm volatile("" : "+{v[188:195]}"(wr[10][0]), "+{v[196:203]}"(wr[10][1]))
#define AQ_PIN11() asm volatile("" : "+{v[204:211]}"(wr[11][0]), "+{v[212:219]}"(wr[11][1]))
#define AK_PIN0() asm volatile("" : "+{v[20:27]}"(wr[0][0]), "+{v[28:35]}"(wr[0][1]))
#define AK_PIN1() asm volatile("" : "+{v[36:43]}"(wr[1][0]), "+{v[44:51]}"(wr[1][1]))
#define AK_PIN2() asm volatile("" : "+{v[52:59]}"(wr[2][0]), "+{v[60:67]}"(wr[2][1]))
#define AK_PIN3() asm volatile("" : "+{v[68:75]}"(wr[3][0]), "+{v[76:83]}"(wr[3][1]))
#define AK_PIN4() asm volatile("" : "+{v[84:91]}"(wr[4][0]), "+{v[92:99]}"(wr[4][1]))
#define AK_PIN5() asm volatile("" : "+{v[100:107]}"(wr[5][0]), "+{v[108:115]}"(wr[5][1]))
#define AK_PIN6() asm volatile("" : "+{v[116:123]}"(wr[6][0]), "+{v[124:131]}"(wr[6][1]))
#define AK_PIN7() asm volatile("" : "+{v[132:139]}"(wr[7][0]), "+{v[140:147]}"(wr[7][1]))
#define AK_PIN8() asm volatile("" : "+{v[148:155]}"(wr[8][0]), "+{v[156:163]}"(wr[8][1]))
#define AK_PIN9() asm volatile("" : "+{v[164:171]}"(wr[9][0]), "+{v[172:179]}"(wr[9][1]))
#define AK_PIN10() asm volatile("" : "+{v[180:187]}"(wr[10][0]), "+{v[188:195]}"(wr[10][1]))
#define AK_PIN11() asm volatile("" : "+{v[196:203]}"(wr[11][0]), "+{v[204:211]}"(wr[11][1]))
#define V_PIN() asm volatile("" : "+{v[28:35]}"(wv[0]), "+{v[36:43]}"(wv[1]), "+{v[44:51]}"(wv[2]), "+{v[52:59]}"(wv[3]), "+{v[60:67]}"(wv[4]), "+{v[68:75]}"(wv[5]), "+{v[76:83]}"(wv[6]), "+{v[84:91]}"(wv[7]), "+{v[92:99]}"(wv[8]), "+{v[100:107]}"(wv[9]), "+{v[108:115]}"(wv[10]), "+{v[116:123]}"(wv[11]), "+{v[124:131]}"(wv[12]), "+{v[132:139]}"(wv[13]), "+{v[140:147]}"(wv[14]), "+{v[148:155]}"(wv[15]))
#define WOV_PIN0() asm volatile("" : "+{v[156:163]}"(wo[0][0][0]), "+{v[164:171]}"(wo[0][0][1]), "+{v[172:179]}"(wo[0][1][0]), "+{v[180:187]}"(wo[0][1][1]))
#define WOV_PIN1() asm volatile("" : "+{v[188:195]}"(wo[1][0][0]), "+{v[196:203]}"(wo[1][0][1]), "+{v[204:211]}"(wo[1][1][0]), "+{v[212:219]}"(wo[1][1][1]))
#define WOW_PIN0() asm volatile("" : "+{v[28:35]}"(wo[0][0][0]), "+{v[36:43]}"(wo[0][0][1]), "+{v[44:51]}"(wo[0][1][0]), "+{v[52:59]}"(wo[0][1][1]))
#define WOW_PIN1() asm volatile("" : "+{v[60:67]}"(wo[1][0][0]), "+{v[68:75]}"(wo[1][0][1]), "+{v[76:83]}"(wo[1][1][0]), "+{v[84:91]}"(wo[1][1][1]))
#define WOW_PIN2() asm volatile("" : "+{v[92:99]}"(wo[2][0][0]), "+{v[100:107]}"(wo[2][0][1]), "+{v[108:115]}"(wo[2][1][0]), "+{v[116:123]}"(wo[2][1][1]))
#define WOW_PIN3() asm volatile("" : "+{v[124:131]}"(wo[3][0][0]), "+{v[132:139]}"(wo[3][0][1]), "+{v[140:147]}"(wo[3][1][0]), "+{v[148:155]}"(wo[3][1][1]))
#define WOW_PIN4() asm volatile("" : "+{v[156:163]}"(wo[4][0][0]), "+{v[164:171]}"(wo[4][0][1]), "+{v[172:179]}"(wo[4][1][0]), "+{v[180:187]}"(wo[4][1][1]))
#define WOW_PIN5() asm volatile("" : "+{v[188:195]}"(wo[5][0][0]), "+{v[196:203]}"(wo[5][0][1]), "+{v[204:211]}"(wo[5][1][0]), "+{v[212:219]}"(wo[5][1][1]))
#define A_PIN(QW, i) do { switch (i) { case 0: if (QW) AQ_PIN0(); else AK_PIN0(); break; case 1: if (QW) AQ_PIN1(); else AK_PIN1(); break; case 2: if (QW) AQ_PIN2(); else AK_PIN2(); break; case 3: if (QW) AQ_PIN3(); else AK_PIN3(); break; case 4: if (QW) AQ_PIN4(); else AK_PIN4(); break; case 5: if (QW) AQ_PIN5(); else AK_PIN5(); break; case 6: if (QW) AQ_PIN6(); else AK_PIN6(); break; case 7: if (QW) AQ_PIN7(); else AK_PIN7(); break; case 8: if (QW) AQ_PIN8(); else AK_PIN8(); break; case 9: if (QW) AQ_PIN9(); else AK_PIN9(); break; case 10: if (QW) AQ_PIN10(); else AK_PIN10(); break; case 11: if (QW) AQ_PIN11(); else AK_PIN11(); break; } } while (0)
#define WO_PIN(VW, i) do { switch (i) { case 0: if (VW) WOV_PIN0(); else WOW_PIN0(); break; case 1: if (VW) WOV_PIN1(); else WOW_PIN1(); break; case 2: if (!VW) WOW_PIN2(); break; case 3: if (!VW) WOW_PIN3(); break; case 4: if (!VW) WOW_PIN4(); break; case 5: if (!VW) WOW_PIN5(); break; } } while (0)

__device__ __forceinline__ u32x4 ldsv(LAS unsigned char* p) { return *(volatile LAS u32x4*)p; }
#define DMA_A dman
#define DMA_V dman
#define DMA_O dmaw
#define TOUCH 0
#define TAUX 0
#define POISON4 ((u32x4){~0u, ~0u, ~0u, ~0u})
#define DLYM 0
#define DLYA 8
#define DLYK (DLYA + 8)
#define DLYO 8
#define FLA 0
#define FLB 0
#define CADM 1
#define CADO 1

#define RTC(id)
#define DCA 3
#define DCB 2
#define DL1 28
#define DL2 28
#define DLB 8
#define ADL 0
#define SN0 150
#define HELP 1
#define BORD 0
#define QT 0
__device__ __forceinline__ void track(int& est, int d, int up, int dn) { est += max(min(d - est, up), min(-dn, (d - est) >> 1)); }
__device__ __forceinline__ unsigned rfl(unsigned v) { unsigned r; asm volatile("s_nop 0\n\tv_readfirstlane_b32 %0, %1\n\ts_nop 4" : "=s"(r) : "v"(v)); return r; }
__device__ __forceinline__ void plan_wait(rsrc_t rs, unsigned fo, int s, int& lim, unsigned& tm, unsigned& d) {
  while ((d & 255u) != (unsigned)s && (lim -= DEC1) > 0) {
    __builtin_amdgcn_s_sleep(1);
    const u32x2 v = __builtin_amdgcn_raw_buffer_load_b64(rs, 0, fo, AUX_SC1);
    tm = rfl(v.x); d = rfl(v.y);
  }
  const unsigned now = (unsigned)__builtin_amdgcn_s_memrealtime() * 4u;
  tm = now + min((int)(tm - now), 2000);
  d = min(d >> 8, 1000u);
}
#define SPIN_BUDGET (1 << 21)

__device__ __forceinline__ unsigned rec_go(LAS unsigned char* w, const Ptrs& P, u32x2 f, int wid, int lane) {
  if (wid >= 6) {
    if (__any(f.y != P.tag)) f.x = flag_wait(P, wid == 6 ? F_LN0(lane) : F_CAL(lane), 0, lane);
    if (wid == 7) { const unsigned m = near_half(f.x); if (lane == 0) *(volatile LAS unsigned*)w = m; }
  }
  LBARRIER();
  return __builtin_amdgcn_readfirstlane(*(volatile LAS unsigned*)w);
}

#define SLOT(i) ((i) * 16384)
#define LA_MEM SLOT(8)
#define LA_Q SLOT(9)
#define LA_K (SLOT(9) + 2048)
#define LA_P (SLOT(9) + 4096)
#define LA_VM (SLOT(9) + 6144)
#define LA_KX (SLOT(9) + 8192)

template <bool QW>
__device__ __forceinline__ void a_wave(LAS unsigned char* lds, Ptrs& P, int h, int wid) {
  char* ws = P.ws;
  const int lane = opq(0), tid = wid * 64 + lane;
  const int j = wid & 3, g = lane >> 4, q4 = (lane & 15) >> 2, p4 = lane & 3;
  const rsrc_t rs = WS_RSRC(P);
  const rsrc_t rq = __builtin_amdgcn_make_buffer_rsrc((void*)P.Wq, 0, HD * DM * 2, RSRC_W3);
  const rsrc_t rk = __builtin_amdgcn_make_buffer_rsrc((void*)P.Wk, 0, HD * DM * 2, RSRC_W3);
  const unsigned lbase = (unsigned)(size_t)lds;
  int lim = SPIN_BUDGET;
  u32x8 wr[12][2];
  const int trow = 16 * (g >> 1) + 4 * (g & 1) + 8 * (q4 >> 1) + (q4 & 1);
  const int tsw = ((q4 & 1) | ((g & 1) << 1) | ((q4 >> 1) << 2)) << 1;
  const int toff = trow * 256 + (((4 * j + (p4 >> 1)) ^ tsw) << 4) + 8 * (p4 & 1);
  {
    unsigned dv[2];
    #pragma unroll
    for (int x = 0; x < 2; ++x) {
      const int row = 8 * wid + 4 * x + (lane >> 4);
      const int sw = ((row & 1) | (((row >> 2) & 1) << 1) | (((row >> 3) & 1) << 2)) << 1;
      dv[x] = (unsigned)(row * (HD * 2) + h * 256 + (((lane & 15) ^ sw) << 4));
    }
#define A_GRP(n) do { _Pragma("unroll") for (int i = 0; i < 8; ++i) \
      DMA_A(((n) & 1) ? rk : rq, dv[i & 1], (4 * (((n) & 1) ? ((n) >> 1) ^ ((n) >> 2) : (((n) >> 1) + 1) & 3) + (i >> 1)) * 64 * HD * 2, lbase + SLOT(4 * ((n) & 1) + (i >> 1)) + wid * 2048 + (i & 1) * 1024); } while (0)
    A_GRP(0); A_GRP(1);
    #pragma unroll
    for (int n = 0; n < 6; ++n) {
      VMW(8);
      BARRIER();
      if ((n & 1) == (QW ? 0 : 1)) {
        #pragma unroll
        for (int b = 0; b < 4; ++b) {
          LAS unsigned char* t = lds + SLOT(QW ? 0 : 4) + ((b ^ j) << 14);
          wr[4 * (n >> 1) + b][0] = rd_frag<8192, 512>(t + toff);
          wr[4 * (n >> 1) + b][1] = rd_frag<8192, 512>(t + (toff ^ 32));
        }
        #pragma unroll
        for (int b = 0; b < 4; ++b) A_PIN(QW, 4 * (n >> 1) + b);
      }
      LBARRIER();
      A_GRP(n + 2);
    }
  }
  const unsigned cb = comm_base(ws);
  u32x2 f0 = {0u, 0u};
  if (wid >= 6) f0 = ld8(rs, FLG_OFF + (wid == 6 ? F_LN0(lane) : F_CAL(lane)) * 128);
  get_tag(P);
  VMWAIT();
  BARRIER();
  #pragma unroll
  for (int rd = 0; rd < 2; ++rd) {
    u32x8 f[2][2];
    #pragma unroll
    for (int i = 0; i < 2; ++i) {
      f[i][0] = rd_frag<8192, 512>(lds + SLOT((QW ? 0 : 4) + 2 * rd + i) + toff);
      f[i][1] = rd_frag<8192, 512>(lds + SLOT((QW ? 0 : 4) + 2 * rd + i) + (toff ^ 32));
    }
    asm volatile("" : "+v"(f[0][0]), "+v"(f[0][1]), "+v"(f[1][0]), "+v"(f[1][1]));
    LBARRIER();
    #pragma unroll
    for (int i = 0; i < 2; ++i)
      #pragma unroll
      for (int t = 0; t < 2; ++t) {
        LAS unsigned char* p = lds + SLOT((QW ? 0 : 4) + 2 * rd + i) + j * 4096 + t * 2048 + lane * 16;
        *(LAS u32x4*)p = (u32x4){f[i][t][0], f[i][t][1], f[i][t][2], f[i][t][3]};
        *(LAS u32x4*)(p + 1024) = (u32x4){f[i][t][4], f[i][t][5], f[i][t][6], f[i][t][7]};
      }
  }
  const unsigned mask = rec_go(lds + LA_P + 128, P, f0, wid, lane);
  const int idx = (lane & 1) ? 0xEEEE : 0x4444;
  const unsigned ab_a = lbase + LA_MEM + lane * 16, am_a = ab_a + j * 1024;
  const unsigned t0_a = lbase + SLOT((QW ? 0 : 4) + j) + j * 4096 + lane * 16;
  const unsigned iw_a = lbase + (QW ? LA_Q : LA_K) + j * 512 + q4 * 128 + g * 32 + p4 * 4;
  const unsigned qr_a = lbase + LA_Q + g * 128 + (lane & 7) * 16;
  const unsigned kr_a = lbase + LA_K + g * 128 + (2 * q4 + (lane & 1)) * 16;
  const unsigned fr_a = lbase + LA_P + p4 * 4;
  if (tid < 8) *(LAS unsigned*)(lds + LA_P + tid * 4) = 0u;
  const unsigned vo_v = VB_AT(h) + j * 512 + lane * 4, md = lbase + LA_VM - VB_AT(h);
  const unsigned oo_v = OB_AT((lane >> 2) & 1, h) + (j * 16 + (g >> 1) * 8 + p4 * 2 + (g & 1)) * 16;
  const unsigned long long onej = 1ull << j;
  const unsigned lkx = lbase + LA_KX + j * 2048;
  u32x8 oxr = {};
  const unsigned xo_v = (lane >> 5) * 2048 + (lane & 31) * 16, vl_a = lbase + LA_VM + (j & 1) * 1024 + lane * 4;
  const int dq = 16 * (j >> 1) + (lane & 15) - 4 * g;
  const unsigned fb = step_base(cb, mask, 0);
  unsigned tgt = (unsigned)__builtin_amdgcn_s_memrealtime() * 4u;
  unsigned long long tp = 0;
  unsigned pt = 0, pd = 0;
  const unsigned long long fmask = __builtin_amdgcn_readfirstlane((16u >> wid) & 1u);
  int known = 0;
  __builtin_amdgcn_s_setprio(QW ? 2 : 1);
  LBARRIER();
  report(P, wid);
  TL(72);
  for (int s = 0; s < NSEG; ++s) {
    const unsigned sb = step_base(cb, mask, s);
    const unsigned sbm = sb - (lbase + LA_MEM);
    const unsigned fo = (unsigned)(s * NH + h) * 8192u + j * 2048 - (lbase + LA_MEM);
    GSDECL;
    RTC(h);
    lim = __builtin_amdgcn_readfirstlane(lim);
    if (QW) {
      if (wid == 3 && s > 0) {
        if (opq(0) == 0) __builtin_amdgcn_raw_buffer_store_b32((unsigned)tp * 4u - tgt, rs, 0, fb + FL_O(h), AUX_SC1);
        plan_wait(rs, fb + FL_T(s), s, lim, pt, pd);
        tgt = pt;
      }
      if (s >= known && s >= 1) {
        const u32x2 v = *(volatile LAS u32x2*)(lds + LA_P + 640 + (opq(0) & 3) * 8);
        if (!__any(v.y != P.tag)) known = rfl(min(min(__builtin_amdgcn_readlane(v.x, 0), __builtin_amdgcn_readlane(v.x, 1)), __builtin_amdgcn_readlane(v.x, 2)));
      }
      kv_wait(P, s, h, known, opq(0));
      const unsigned kxo = WS_KF + fo, vxo = WS_VF + fo;
      if (wid < 3) for (int i = 0; i < ADL; ++i) __builtin_amdgcn_s_sleep(1);
      dmas(rs, ab_a, kxo, lkx);
      dmas(rs, ab_a, kxo + 1024, lkx + 1024);
      dmas(rs, ab_a, WS_QF + fo, lbase + LA_P + 1024);
      dmas(rs, ab_a, WS_QF + fo + 1024, lbase + LA_P + 1024);
      if (wid == 2 && s + 1 >= known) {
        const int ln = opq(0);
        const unsigned fv = FLG_OFF + F_PRG(min((ln >> 1) & 3, 2), h) * 128 + (ln & 1) * 4;
        asm volatile("s_mov_b32 m0, %2\n\ts_nop 0\n\tbuffer_load_dword %0, %1, 0 offen sc1 lds" :: "v"(fv), "s"(rs), "s"(lbase + LA_P + 640) : "memory");
      }
      unsigned rg = 0x3c000000u;
      A_MAIN_Q();
      TL(s);
      GSFLUSH(0, 24 + wid); GSFLUSH(1, 4 + wid); GSFLUSH(2, 12 + j); GSFLUSH(3, 16 + j); GSFLUSH(4, 20 + j);
      if (s + PD < NSEG && !(opq(0) & 8)) __builtin_amdgcn_raw_buffer_store_b128(poison4(), rs, oo_v, step_base(cb, mask, s + PD), AUX_SC1);
    } else {
      const unsigned qxo = WS_QF + fo - (j & 1) * 2048, oxo = WS_OX + (unsigned)(max(s - 1, 0) * NH + h) * 8192u + (j & 1) * 4096 + (j >> 1) * 1024;
      A_MAIN_K();
      if (wid == 7) GSFLUSH(0, 28);
      GSFLUSH(1, 4 + wid);
    }
  }
  if (!QW) {
    const unsigned oxo = WS_OX + (unsigned)((NSEG - 1) * NH + h) * 8192u + (j & 1) * 4096 + (j >> 1) * 1024 + xo_v;
    st16(rs, oxo, (u32x4){oxr[0], oxr[1], oxr[2], oxr[3]});
    st16(rs, oxo + 512, (u32x4){oxr[4], oxr[5], oxr[6], oxr[7]});
  }
  VMWAIT();
  BARRIER();
  if (wid == 4) flag_set(P, F_PRG(4, h), NSEG, opq(0));
}

#define LB_MEM 0
#define LB_RED 16384
template <bool VW>
__device__ __forceinline__ void b_wave(LAS unsigned char* lds, Ptrs& P, int r, int c, int wid, int bs) {
  constexpr int NHD = VW ? 2 : 6;
  char* ws = P.ws;
  const int lane = opq(0), tid = wid * 64 + lane;
  const int j = wid & 3, g = lane >> 4, q4 = (lane & 15) >> 2, p4 = lane & 3;
  const int b = r * 32 + c;
  const rsrc_t rs = WS_RSRC(P);
  const rsrc_t rv = __builtin_amdgcn_make_buffer_rsrc((void*)P.Wv, 0, HD * DM * 2, RSRC_W3);
  const rsrc_t ro = __builtin_amdgcn_make_buffer_rsrc((void*)P.Wo, 0, HD * DM * 2, RSRC_W3);
  const unsigned lbase = (unsigned)(size_t)lds;
  int lim = SPIN_BUDGET;
  const int h0 = ((VW ? 2 * j : 8 + 6 * j) + 5 * c) & 31;
  u32x8 wv[16], wo[6][2][2];
  {
    const int nl = c >= 20 ? 6 : 5;
    #pragma unroll
    for (int i = 0; i < 2; ++i) {
      const unsigned L = bs * 1024 + wid * 128 + i * 64 + lane, row = L / nl;
      touch(P.Wo, HD * DM * 2, row * (DM * 2) + (c - (bs >> 1)) * 64 + (L - row * nl) * 128, lds);
    }
    if ((wid & 3) < 2 && lane < 40) {
      const unsigned nh = 16 - 2 * nl, M = (bs * 2 + (wid & 1)) * 40 + lane, row = M / nh, k = M - row * nh;
      if (row < 256) touch(VW ? P.Wq : P.Wk, HD * DM * 2, (row + (VW ? 0 : 512)) * (HD * 2) + (c - (bs >> 1)) / 10 * 3072 + (k >> 1) * 1024 + 256 + (k & 1) * 128, lds);
    }
    VMWAIT();
    BARRIER();
    const int pos = lane >> 2;
    const int prow = (pos & 9) | ((pos & 2) << 1) | ((pos & 4) >> 1);
    const unsigned dv0 = (unsigned)(prow * (DM * 2) + c * 64 + ((lane & 3) << 4));
    const unsigned dv1 = (unsigned)(prow * (DM * 2) + c * 64 + (((lane & 3) ^ 2) << 4));
    const int toff = wid * 16384 + (g * 16 + q4) * 64 + (((p4 >> 1) ^ ((g & 1) << 1)) << 4) + 8 * (p4 & 1);
    #pragma unroll
    for (int rd = 0; rd < NHD / 2; ++rd) {
      #pragma unroll
      for (int hh = 0; hh < 2; ++hh) {
        const int h = (h0 + 2 * rd + hh) & 31;
        #pragma unroll
        for (int i = 0; i < 8; ++i)
          DMA_O(ro, (i & 1) ? dv1 : dv0, (unsigned)(h * 128 + i * 16) * (DM * 2), lbase + wid * 16384 + hh * 8192 + i * 1024);
      }
      VMWAIT();
      #pragma unroll
      for (int hh = 0; hh < 2; ++hh)
        #pragma unroll
        for (int ks = 0; ks < 2; ++ks)
          #pragma unroll
          for (int t = 0; t < 2; ++t)
            wo[2 * rd + hh][ks][t] = rd_frag<4096, 256>(lds + (toff ^ (t * 32)) + hh * 8192 + ks * 512);
      #pragma unroll
      for (int hh = 0; hh < 2; ++hh)
        WO_PIN(VW, 2 * rd + hh);
      asm volatile("s_waitcnt lgkmcnt(0)" ::: "memory");
    }
    const int lr = lane >> 3;
    #pragma unroll
    for (int i = 0; i < 16; ++i) {
      const int sw = (((lr >> 2) & 1) | ((i & 1) << 1)) << 1;
      const unsigned dv = (unsigned)(lr * (HD * 2) + b * 128 + (((lane & 7) ^ sw) << 4));
      DMA_V(rv, dv, (unsigned)(wid * 128 + i * 8) * (HD * 2), lbase + wid * 16384 + i * 1024);
    }
    VMWAIT();
    BARRIER();
    if (VW) {
      const int trow = 16 * (g >> 1) + 4 * (g & 1) + 8 * (q4 >> 1) + (q4 & 1);
      const int tsw = ((g & 1) | ((q4 >> 1) << 1)) << 1;
      LAS unsigned char* tv = lds + trow * 128 + (((2 * j + (p4 >> 1)) ^ tsw) << 4) + 8 * (p4 & 1);
      #pragma unroll
      for (int ks = 0; ks < 16; ++ks) wv[ks] = rd_frag<4096, 256>(tv + ks * 8192);
      V_PIN();
    }
    LBARRIER();
  }
  const unsigned cb = comm_base(ws);
  get_tag(P);
  const unsigned mask = rec_go(lds + LB_RED + 4096 + 64, P, (u32x2){0u, P.tag - 1u}, wid, lane);
  const int idx = (lane & 1) ? 0xEEEE : 0x4444;
  const unsigned am_a = lbase + LB_MEM + lane * 16;
  const unsigned fb = step_base(cb, mask, 0);
  volatile LAS unsigned* tsl = (volatile LAS unsigned*)(lds + LB_RED + 4096);
  if (tid < 8) tsl[tid] = tid ? 0u : (unsigned)__builtin_amdgcn_s_memrealtime() * 4u;
  unsigned dn = 600;
  int da = 570, gb = 360;
  if (VW) __builtin_amdgcn_s_setprio(2);
  LBARRIER();
  report(P, wid);
  TL(72);
  for (int s = 0; s < NSEG; ++s) {
    const unsigned sb = step_base(cb, mask, s);
    const int ln = opq(lane), gg = ln >> 4;
    GSDECL;
    RTC(32 + b);
    const int l16 = ln * 16;
    const int xc = 32 * (c & 1) + 2 * (ln & 15);
    const unsigned mo_v = MF_AT(c >> 1) + ((xc >> 4) * 16 + (4 * r + gg) * 2 + ((xc >> 3) & 1)) * 16 + (xc & 7) * 2;
    if (!VW) {
      const unsigned tm = __builtin_amdgcn_readfirstlane(*tsl), tgt = tm + DLB, la = lbase + LB_MEM + j * 2048 + l16, mo = sb + j * 2048;
      B_POLL();
      if (wid == 7) {
        const int late = (int)((unsigned)__builtin_amdgcn_s_memrealtime() * 4u - tgt) - SN0;
        const unsigned to = tm + dn + (late > 80 ? late : 0);
        if (ln == 0) tsl[1] = to;
        if (b == 0) {
          dn = da + DL1;
          if (ln == 0) { *tsl = to + gb + DL2; st8(rs, fb + FL_T(s + 1), (u32x2){to + gb + DL2, (dn << 8) | (s + 1)}); }
          const unsigned va = ld4(rs, fb + FL_O(ln & 31)), vb = ld4(rs, fb + FL_B(ln));
          if (s > 2) {
            track(da, rfl(wmax(va)), DCA, DCB);
            track(gb, rfl(wmax(vb)), DCA, DCB);
          }
        }
      }
      if (wid == 7 && s > 0 && s + 2 < NSEG) __builtin_amdgcn_raw_buffer_store_b32(~0u, rs, mo_v, step_base(cb, mask, s + 2), AUX_SC1);
    } else {
      GS(3);
      const unsigned vo = VB_AT(b >> 1) + ((b & 1) * 4 + j) * 256;
      V_PROJ();
      GS(0); GSFLUSH(0, 4 + j);
      if (s + PD < NSEG && ln < 16) st16(rs, step_base(cb, mask, s + PD) + vo + l16, poison4());
    }
    GSFLUSH(3, 24 + wid);
    if (s + 1 == NSEG) break;
    unsigned so[6];
    #pragma unroll
    for (int i = 0; i < NHD; ++i) { const int h = (h0 + i) & 31; so[i] = __builtin_amdgcn_readfirstlane(sb + OB_AT(r, h)); }
    const unsigned sn = __builtin_amdgcn_readfirstlane(step_base(cb, mask, s + 1));
    const unsigned rw_a = lbase + LB_RED + (wid * 128 + (gg & 1) * 64 + (gg >> 1) * 16 + (ln & 15)) * 4;
    const unsigned rr_a = lbase + LB_RED + ln * 8;
    if (wid == 7) {
      if (b != 0) {
        __builtin_amdgcn_s_sleep(4);
        unsigned tm = 0;
        dn = 0;
        plan_wait(rs, fb + FL_T(s + 1), s + 1, lim, tm, dn);
        if (ln == 0) *tsl = tm;
      }
      const unsigned tgt = rfl(tsl[1]);
      T_WAIT();
    }
    lim = __builtin_amdgcn_readfirstlane(lim);
    unsigned long long t1;
    if (VW) WO_V(); else WO_W();
    if (wid == 0) {
      if (ln == 0) st4(rs, fb + FL_B(b), (unsigned)t1 * 4u - tsl[1]);
    }
    if (wid == 0) {
      GSFLUSH(2, 16);
      TL(s);
    }
    GSFLUSH(1, 8 + wid);
    if (wid == 0 || wid == 4) { GSFLUSH(4, 17 + (wid >> 2)); GSFLUSH(0, 19 + (wid >> 2)); }
  }
  __builtin_amdgcn_s_setprio(0);
  const int ln = opq(0);
  TL(64);
  volatile LAS int* rdy = (volatile LAS int*)(lds + 98304 + 3 * 16384);
  if (tid == 0) *rdy = XS3 + 1;
  LBARRIER();
#define T_SRC(sg, i) (WS_OX + (unsigned)((sg) * NH + ((h0 + (i)) & 31)) * 8192u + r * 1024)
#define T_MMA(i, k, f) do { const u32x8 w0 = wo[i][(k) & 1][0], w1 = wo[i][(k) & 1][1]; const int e = ((k) >> 1) * 4; \
    a0 = MFMA32(((u32x4){w0[e], w0[e + 1], w0[e + 2], w0[e + 3]}), f, a0); a1 = MFMA32(((u32x4){w1[e], w1[e + 1], w1[e + 2], w1[e + 3]}), f, a1); } while (0)
#define T_DMA(sg, i) do { _Pragma("unroll") for (int k = 0; k < 4; ++k) dmas(rs, ln * 16, T_SRC(sg, i) + k * 2048, lbase + j * 24576 + (i) * 4096 + k * 1024); } while (0)
  bool ahead = false;
  f32x4 k0, k1;
  #pragma unroll 1
  for (int sg = XS3; sg < NSEG; ++sg) {
    f32x4 a0 = zero4(), a1 = zero4();
    if (VW) {
      if (wid == 0) {
        const int w = -wmax(-(int)flag_wait(P, F_PRG(4, ln & 31), min(sg + 2, NSEG), ln));
        if (ln == 0) *rdy = max(w, min(sg + 2, NSEG));
      } else while (__builtin_amdgcn_readfirstlane(*rdy) <= sg) __builtin_amdgcn_s_sleep(1);
      u32x4 of[8];
      #pragma unroll
      for (int i = 0; i < 8; ++i) of[i] = __builtin_amdgcn_raw_buffer_load_b128(rs, ln * 16 + (i & 3) * 2048, T_SRC(sg, i >> 2), AUX_SC1);
      #pragma unroll
      for (int i = 0; i < 8; ++i) T_MMA(i >> 2, i & 3, of[i]);
    } else {
      if (!ahead) {
        while (__builtin_amdgcn_readfirstlane(*rdy) <= sg) __builtin_amdgcn_s_sleep(1);
        #pragma unroll
        for (int i = 0; i < NHD; ++i) T_DMA(sg, i);
      }
      VMW(20);
      ahead = __builtin_amdgcn_readfirstlane(*rdy) > sg + 1;
      #pragma unroll
      for (int i = 0; i < NHD; ++i) {
        if (ahead) VMW(20); else G_WAIT_V(20 - 4 * i);
        u32x4 of[4];
        #pragma unroll
        for (int k = 0; k < 4; ++k) of[k] = *(const LAS u32x4*)(lds + j * 24576 + i * 4096 + k * 1024 + ln * 16);
        asm volatile("s_waitcnt lgkmcnt(0)" : "+v"(of[0]), "+v"(of[1]), "+v"(of[2]), "+v"(of[3]) :: "memory");
        if (ahead) T_DMA(sg + 1, i);
        #pragma unroll
        for (int k = 0; k < 4; ++k) T_MMA(i, k, of[k]);
      }
    }
    LAS f32x4* pw = (LAS f32x4*)(lds + 98304 + ((sg & 3) * 8 + wid) * 2048) + ln;
    if (wid == 0 && sg == NSEG - 1) { k0 = a0; k1 = a1; } else { pw[0] = a0; pw[64] = a1; }
  }
  if (VW) {
    const int fq = ln >> 4, cb2 = 16 * (fq & 1) + 8 * (fq >> 1);
    const unsigned eo = (unsigned)((NSEG - 1 - j) * 32 + r * 16 + (ln & 15)) * 2048u + (unsigned)(c * 32 + cb2) * 2u;
    cnt_wait(P, C_W4(7), 8, ln);
    const u32x4 gv = ld16(rs, WS_G + (unsigned)((NSEG - 1 - j) * 2 + r) * 32768u + c * 1024 + blk_in(ln & 15, cb2));
    const u32x4 xv = *(const u32x4*)((const char*)P.x + eo);
    LBARRIER();
    const LAS f32x4* pr = (const LAS f32x4*)(lds + 98304 + (3 - j) * 16384) + ln;
    f32x4 t[2];
    #pragma unroll
    for (int k = 0; k < 2; ++k)
      t[k] = (((wid ? pr[k * 64] : k ? k1 : k0) + pr[128 + k * 64]) + (pr[256 + k * 64] + pr[384 + k * 64])) + ((pr[512 + k * 64] + pr[640 + k * 64]) + (pr[768 + k * 64] + pr[896 + k * 64]));
    u32x4 v = epi_pack(t[0], t[1]);
    #pragma unroll
    for (int k = 0; k < 4; ++k) {
      v[k] = pack2(lo16(v[k]) * lo16(gv[k]), hi16(v[k]) * hi16(gv[k]));
      v[k] = pack2(lo16(xv[k]) + lo16(v[k]), hi16(xv[k]) + hi16(v[k]));
    }
    *(u32x4*)((char*)P.out + eo) = v;
  } else LBARRIER();
}

#define DMA_S dman
#define LS_P 131072
template <int MODE>
__device__ __forceinline__ void s_role(LAS unsigned char* lds, Ptrs& P, const u16* W, int h, unsigned obase, int nseg, int fl, int wid, int s0) {
  constexpr bool XM = MODE == 2;
  constexpr unsigned RS = XM ? DM * 2 : HD * 2, SS = XM ? 262144u : 65536u;
  const int lane = opq(0);
  const int kh = wid >> 2, tp = wid & 3;
  const rsrc_t rs = WS_RSRC(P);
  const rsrc_t rw = __builtin_amdgcn_make_buffer_rsrc((void*)W, 0, HD * DM * 2, RSRC_W3);
  const unsigned lbase = (unsigned)(size_t)lds;
  u32x8 w[8][2];
  {
    const int g = lane >> 4, q4 = (lane & 15) >> 2, p4 = lane & 3;
    const int trow = XM ? 16 * g + 4 * (q4 >> 1) + (q4 & 1) : 8 * g + q4;
#define S_SW(row) ((((row) & 1) | ((((row) >> (XM ? 2 : 1)) & 1) << 1) | ((((row) >> (XM ? 4 : 3)) & 1) << 2)) << 1)
    const int toff = trow * 256 + (((4 * tp + (p4 >> 1)) ^ S_SW(trow)) << 4) + 8 * (p4 & 1);
    unsigned dv[2];
    #pragma unroll
    for (int x = 0; x < 2; ++x) {
      const int row = 8 * wid + 4 * x + (lane >> 4);
      dv[x] = (unsigned)row * RS + (XM ? (h >> 3) * 1024 * RS + (h & 7) * 256 : h * 256) + (((lane & 15) ^ S_SW(row)) << 4);
    }
#define S_IMG(i) do { DMA_S(rw, dv[0], (i) * 64 * RS, lbase + ((i) & 7) * 16384 + wid * 2048); \
                      DMA_S(rw, dv[1], (i) * 64 * RS, lbase + ((i) & 7) * 16384 + wid * 2048 + 1024); } while (0)
    #pragma unroll
    for (int i = 0; i < 8; ++i) S_IMG(i);
    #pragma unroll
    for (int b = 0; b < 4; ++b) {
      if (b == 3 && !XM) get_tag(P);
      if (b < 3) VMW(8); else VMWAIT();
      BARRIER();
      if (kh == (b >> 1)) {
        #pragma unroll
        for (int i = 4 * (b & 1); i < 4 * (b & 1) + 4; ++i) {
          w[i][0] = rd_frag<XM ? 2048 : 8192, XM ? 512 : 1024>(lds + toff + i * 16384);
          w[i][1] = rd_frag<XM ? 2048 : 8192, XM ? 512 : 1024>(lds + (toff ^ 32) + i * 16384);
        }
      }
      LBARRIER();
      if (b < 2) {
        #pragma unroll
        for (int i = 4 * b + 8; i < 4 * b + 12; ++i) S_IMG(i);
      }
    }
  }
  TL(XM ? 74 : 72);
  const unsigned l16 = lane * 16;
  volatile LAS int* rd = (volatile LAS int*)(lds + LS_P + 20480);
#define X_READY(want) do { \
    if (wid == 0) { \
      const int n_ = -wmax(-(int)flag_wait(P, F_PRG(4, (h & 24) + (lane & 7)), s0 + (want), lane)) - s0; \
      if (lane == 0) *rd = max(n_, want); \
    } \
    LBARRIER(); } while (0)
  if (XM) X_READY(2);
  else {
    if (wid == 0) setup_wait(P, lane);
    BARRIER();
  }
  unsigned sx = (XM ? WS_OX + s0 * SS + (h >> 3) * 65536 : WS_XN) + kh * 32768 + tp * 4096;
  const unsigned sd = lbase + kh * 65536 + tp * 4096;
  #pragma unroll
  for (int i = 0; i < 12; ++i) dmas(rs, l16, sx + (i >> 3) * SS + ((i >> 2) & 1) * 16384 + (i & 3) * 1024, sd + (i >> 2) * 16384 + (i & 3) * 1024);
  VMWAIT();
  BARRIER();
  const unsigned xl = kh * 65536 + (XM ? l16 : blk_in(lane & 15, (lane >> 4) * 8));
  const unsigned xp = lbase + xl, pa = lbase + LS_P + tp * 4096 + l16;
  u32x8 xf[3], ac[2] = {};
  #pragma unroll
  for (int i = 0; i < 3; ++i) {
    const u32x4 a = *(const LAS u32x4*)(lds + xl + i * 2048), b = *(const LAS u32x4*)(lds + xl + i * 2048 + 1024);
    xf[i] = (u32x8){a[0], a[1], a[2], a[3], b[0], b[1], b[2], b[3]};
  }
  unsigned long long fm, pm;
  asm volatile("s_cmp_eq_u32 %2, 1\n\ts_cselect_b64 %0, 1, 0\n\ts_cmp_eq_u32 %3, 1\n\ts_cselect_b64 %1, -1, 0" : "=&s"(fm), "=&s"(pm) : "s"(wid), "s"(XM ? 0 : wid >> 1) : "scc");
  const unsigned fo = FLG_OFF + fl * 128, pd = lbase + LS_P + 16384;
  const unsigned po = 4 * 65536 + h * 2048 + (wid & 1) * 1024 - tp * 4096 - kh * 32768;
  unsigned oo = XM ? obase + (unsigned)h * 16384u + tp * 4096 + (s0 - 1) * 0x80000u : obase + (unsigned)h * 8192u + tp * (MODE ? 2048 : 1024) - 0x40000u, tmp, tmp2;
  unsigned long long em;
  int t = 0;
  if (MODE == 0) S_LOOP_K(nseg, 1);
  else if (MODE == 1) S_LOOP_V(nseg, 1);
  else {
    #pragma unroll 1
    while (t < nseg) {
      X_READY(min(t + 4, nseg));
      const int n = __builtin_amdgcn_readfirstlane(*rd), te = n >= nseg ? nseg : (n - 2) & ~1;
      TL(t);
      S_LOOP_X(te, (nseg - 1 - te) >> 31);
    }
  }
  TL(XM ? 65 : 64);
  VMWAIT();
  BARRIER();
  if (wid == 1) flag_set(P, fl, nseg, lane);
}

#define CALN 6
__device__ __forceinline__ void setup_role(LAS unsigned char* lds, Ptrs& P, int cu, int wid) {
  const int lane = opq(0), tid = wid * 64 + lane;
  const rsrc_t rs = WS_RSRC(P);
  u32x4 v[8];
  const u32x4* px = (const u32x4*)(P.x + (size_t)(cu * 32 + wid * 4 + (lane >> 4)) * DM) + (lane & 15);
  #pragma unroll
  for (int i = 0; i < 8; ++i) v[i] = __builtin_nontemporal_load(px + i * 16);
  const unsigned epo = __hip_atomic_load((gu32*)(P.ws + EPO_OFF), RLX_AGENT);
  const unsigned cb = comm_base(P.ws);
  const u32x4 z = {0u, 0u, 0u, 0u};
  if (wid == 0 && cu < 16) st16(rs, CNT_OFF + cu * 1024 + lane * 16, z);
  if (cu < 4) st16(rs, cb + FL_B(0) + cu * 8192 + tid * 16, z);
  if (cu == 4 && tid < 256) {
    const int g4 = tid >> 6, k = (tid >> 5) & 1 & (g4 >> 1);
    st16(rs, WS_H1 + ((g4 & 1) ? 127u * 32768u : 0u) + (g4 * 8 + ((tid >> 2) & 7)) * 1024 + blk_in((g4 & 1) ? 15 - k : k, (tid & 3) * 8), z);
  }
  #pragma unroll
  for (int i = 0; i < 2; ++i) {
    const int f = i * 64 + cu;
    if (f < PD * NCHUNK * 2) {
      u32x4 m = {~0u, ~0u, ~0u, ~0u};
      if (f < 4) {
        const int ks = (f >> 1) * 8 + wid;
        m = *(const u32x4*)(P.mem0 + ((lane >> 1) & 7) * DM + 64 * ks + 16 * (lane >> 4) + 8 * (lane & 1));
        m.x = canon1(m.x); m.y = canon1(m.y); m.z = canon1(m.z); m.w = canon1(m.w);
      }
      st16(rs, cb + (unsigned)(f / (NCHUNK * 2)) * CB_STEP + (f % (NCHUNK * 2)) * 8192u + tid * 16, m);
    }
  }
  const unsigned bi = blk_in((wid & 3) * 4 + (lane >> 4), (lane & 3) * 8);
  const int c0 = (lane >> 2) & 3;
  ln_rows(v);
  TL(69);
  #pragma unroll
  for (int i = 0; i < 8; ++i) st16(rs, WS_XN + xn_blk(cu * 2 + (wid >> 2), 4 * i + c0) + bi, v[i]);
  P.tag = __builtin_amdgcn_readfirstlane(epo) + 1u;
  ln_rows(v);
  asm volatile("" ::: "memory");
  #pragma unroll
  for (int i = 0; i < 8; ++i) st16(rs, WS_XN2 + (unsigned)(cu * 2 + (wid >> 2)) * 32768u + (4 * i + c0) * 1024 + bi, v[i]);
  touch(P.w1, DM * DM * 2, ((cu & 31) * 512 + tid) * 128, lds);
  VMW(9);
  BARRIER();
  if (wid == 0) flag_set(P, F_LN0(cu), 1, lane);
  TL(70);
  if (wid == 0) VMW(2); else VMW(1);
  BARRIER();
  if (wid == 0) flag_set(P, F_LN1(cu), 1, lane);
  TL(71);
  if (cu < 32 && (wid >> 1) == 1) {
    const int id = cu * 2 + (wid & 1);
    unsigned best[2] = {65535u, 65535u};
    #pragma unroll
    for (int it = 0; it < CALN; ++it) {
      const unsigned long long c0 = __builtin_amdgcn_s_memtime();
      asm volatile("s_waitcnt lgkmcnt(0)" ::: "memory");
      if (lane == 0) st4(rs, cb + (id & 3) * CB_STEP + (30 + ((id >> 2) & 1)) * 16384u + (it & 1) * 8192u + (id >> 3) * 1024u, 0u);
      VMWAIT();
      const unsigned d = (unsigned)(__builtin_amdgcn_s_memtime() - c0);
      if (it >= 2) best[it & 1] = min(best[it & 1], d);
    }
    flag_set(P, F_CAL(id), (best[0] << 16) | best[1], lane);
  }
}

__global__ __launch_bounds__(512, 2) void mega_kernel(const u16* x, const u16* mem0, const u16* Wq, const u16* Wk, const u16* Wv, const u16* Wo,
                                                     const u16* w1, const u16* w2, const u16* w3, const u16* w4, u16* out, char* ws) {
  __shared__ __attribute__((aligned(16))) unsigned char smem[163840];
  LAS unsigned char* lds = (LAS unsigned char*)smem;
  Ptrs P;
  P.x = x; P.mem0 = mem0; P.Wq = Wq; P.Wk = Wk; P.Wv = Wv; P.Wo = Wo; P.w1 = w1; P.w2 = w2; P.w3 = w3; P.w4 = w4; P.out = out; P.ws = ws;
  const int xcd = blockIdx.x & 7, slot = blockIdx.x >> 3;
  const int wid = __builtin_amdgcn_readfirstlane(threadIdx.x >> 6);
  TL(79);
  P.tag = 0;
  const int na = xcd == 2 ? 8 : 12;
  const bool isa = xcd < 3 && slot < na;
  const int ah = xcd * 12 + slot, bi = xcd * 20 + slot - na, br = bi & 1, bc = bi >> 1, bs = slot - na;
  if (isa) {
    if (wid < 4) a_wave<true>(lds, P, ah, wid); else a_wave<false>(lds, P, ah, wid);
  } else if (xcd < 3) {
    if (wid < 4) b_wave<true>(lds, P, br, bc, wid, bs); else b_wave<false>(lds, P, br, bc, wid, bs);
  } else {
    if ((xcd & 3) == 3) {
      setup_role(lds, P, (xcd >> 2) * 32 + slot, wid);
    } else if (xcd == 6) s_role<1>(lds, P, Wv, slot, WS_VF, NSEG, F_PRG(2, slot), wid, 0);
    else s_role<0>(lds, P, xcd == 4 ? Wk : Wq, slot, xcd == 4 ? WS_KF : WS_QF, NSEG, F_PRG(xcd - 4, slot), wid, 0);
    if ((xcd & 3) == 3) mlp_role(lds, P, xcd >> 2, slot, wid);
    else s_role<2>(lds, P, Wo, slot, WS_PT, xcd == 4 ? XS1 : xcd == 5 ? XS2 - XS1 : XS3 - XS2, F_PRG(xcd + 1, slot), wid, xcd == 4 ? 0 : xcd == 5 ? XS1 : XS2);
    report(P, wid);
    fin_role(lds, P, wid);
    if (blockIdx.x == 3 && wid == 0) {
      cnt_wait(P, C_ARR, 256, threadIdx.x);
      if (threadIdx.x == 0) __hip_atomic_store((gu32*)(ws + EPO_OFF), P.tag, RLX_AGENT);
    }
  }
  TL(78);
}

extern "C" void kernel_launch(void* const* d_in, const int* in_sizes, int n_in, void* d_out, int out_size,
                              void* d_ws, size_t ws_size, hipStream_t stream) {
  mega_kernel<<<256, 512, 0, stream>>>((const u16*)d_in[0], (const u16*)d_in[1], (const u16*)d_in[2], (const u16*)d_in[3], (const u16*)d_in[4],
                                       (const u16*)d_in[5], (const u16*)d_in[8], (const u16*)d_in[9], (const u16*)d_in[10], (const u16*)d_in[11],
                                       (u16*)d_out, (char*)d_ws);
}
```

```cpp
#include <hip/hip_runtime.h>
#include <hip/hip_bf16.h>
#include <stdint.h>

#define SEQ 2048
#define DM 1024
#define NH 32
#define DH 128
#define HD (NH * DH)
#define MM 8
#define SEG 32
#define NSEG (SEQ / SEG)
#define FF 3584

typedef unsigned short u16;
typedef __attribute__((ext_vector_type(8))) short bf16x8;
typedef __attribute__((ext_vector_type(4))) short bf16x4;
typedef __attribute__((ext_vector_type(4))) float f32x4;
typedef __attribute__((ext_vector_type(4))) unsigned u32x4;
typedef __attribute__((ext_vector_type(2))) unsigned u32x2;
typedef __attribute__((ext_vector_type(2))) float f32x2;
typedef __attribute__((ext_vector_type(2))) __bf16 bf16v2;
typedef __attribute__((ext_vector_type(8))) unsigned u32x8;
#define LAS __attribute__((address_space(3)))
#define GAS __attribute__((address_space(1)))

__device__ __forceinline__ unsigned pack2(float lo, float hi) {
  return __builtin_bit_cast(unsigned, __builtin_convertvector((f32x2){lo, hi}, bf16v2));
}
__device__ __forceinline__ float rb1(float x) { return __uint_as_float(pack2(0.f, x)); }

typedef GAS unsigned gu32;
#define RLX_AGENT __ATOMIC_RELAXED, __HIP_MEMORY_SCOPE_AGENT
#define SPIN_CAP (1u << 20)
#define AUX_SC1 16
#define RSRC_W3 0x00020000
typedef __amdgpu_buffer_rsrc_t rsrc_t;

__device__ __forceinline__ u32x4 ld16(rsrc_t rs, unsigned off) { return __builtin_amdgcn_raw_buffer_load_b128(rs, off, 0, AUX_SC1); }
__device__ __forceinline__ u32x2 ld8(rsrc_t rs, unsigned off) { return __builtin_amdgcn_raw_buffer_load_b64(rs, off, 0, AUX_SC1); }
__device__ __forceinline__ unsigned ld4(rsrc_t rs, unsigned off) { return __builtin_amdgcn_raw_buffer_load_b32(rs, off, 0, AUX_SC1); }
__device__ __forceinline__ void st16(rsrc_t rs, unsigned off, u32x4 v) { __builtin_amdgcn_raw_buffer_store_b128(v, rs, off, 0, AUX_SC1); }
__device__ __forceinline__ void st8(rsrc_t rs, unsigned off, u32x2 v) { __builtin_amdgcn_raw_buffer_store_b64(v, rs, off, 0, AUX_SC1); }
__device__ __forceinline__ void st4(rsrc_t rs, unsigned off, unsigned v) { __builtin_amdgcn_raw_buffer_store_b32(v, rs, off, 0, AUX_SC1); }
__device__ __forceinline__ float lo16(unsigned w) { return __uint_as_float(w << 16); }
__device__ __forceinline__ float hi16(unsigned w) { return __uint_as_float(w & 0xffff0000u); }

__device__ __forceinline__ float xsum16_32(float v) {
  unsigned u = __float_as_uint(v);
  auto a = __builtin_amdgcn_permlane16_swap(u, u, false, false);
  v = __uint_as_float(a[0]) + __uint_as_float(a[1]);
  u = __float_as_uint(v);
  auto b = __builtin_amdgcn_permlane32_swap(u, u, false, false);
  return __uint_as_float(b[0]) + __uint_as_float(b[1]);
}

__device__ __forceinline__ unsigned canon1(unsigned w) { return min(w, 0xfffffffeu); }
__device__ __forceinline__ unsigned max4(u32x4 v) { return max(max(v.x, v.y), max(v.z, v.w)); }
__device__ __forceinline__ int opq(int) { int v; asm volatile("v_mbcnt_lo_u32_b32 %0, -1, 0\n\tv_mbcnt_hi_u32_b32 %0, -1, %0" : "=v"(v)); return v; }
__device__ __forceinline__ u32x4 poison4() { unsigned m = ~0u; asm volatile("" : "+v"(m)); return (u32x4){m, m, m, m}; }
__device__ __forceinline__ f32x4 zero4() { float m = 0.f; asm volatile("" : "+v"(m)); return (f32x4){m, m, m, m}; }
#define MFMA32(a, b, c) __builtin_amdgcn_mfma_f32_16x16x32_bf16(__builtin_bit_cast(bf16x8, a), __builtin_bit_cast(bf16x8, b), c, 0, 0, 0)
#define MFMA16(a, b, c) __builtin_amdgcn_mfma_f32_16x16x16bf16_1k(__builtin_bit_cast(bf16x4, a), __builtin_bit_cast(bf16x4, b), c, 0, 0, 0)
#define LBARRIER() do { asm volatile("s_waitcnt lgkmcnt(0)" ::: "memory"); __builtin_amdgcn_s_barrier(); asm volatile("" ::: "memory"); } while (0)
#define BARRIER() do { asm volatile("" ::: "memory"); __builtin_amdgcn_s_barrier(); asm volatile("" ::: "memory"); } while (0)
#define VMWAIT() asm volatile("s_waitcnt vmcnt(0)" ::: "memory")
#define VMW(n) asm volatile("s_waitcnt vmcnt(" #n ")" ::: "memory")
#define DELAY(n) do { if ((n) > 0) __builtin_amdgcn_s_sleep(n); } while (0)

#define EPO_OFF 0
#define TMO_OFF 128
#define FLG_OFF 256
#define F_LN0(i) (i)
#define F_LN1(i) (64 + (i))
#define F_CAL(i) (128 + (i))
#define F_PRG(t, h) (192 + (t) * 32 + (h))
#define CNT_OFF 65536
#define C_W1(p2) (p2)
#define C_W2(p2) (16 + (p2))
#define C_LN(pm) (32 + (pm))
#define C_W3(pm, hf) (40 + (pm) * 2 + (hf))
#define C_W4(pm) (56 + (pm))
#define C_HEAD 64
#define C_ARR 72
#define C_P4(pm, pc) (96 + (pm) * 4 + (pc))
#define NCNT 128
#define WS_XN (1u << 20)
#define WS_XN2 (WS_XN + (4u << 20))
#define WS_QF (WS_XN2 + (4u << 20))
#define WS_KF (WS_QF + (16u << 20))
#define WS_VF (WS_KF + (16u << 20))
#define WS_OX (WS_VF + (16u << 20))
#define WS_H1 (WS_OX + (16u << 20) + (1u << 20))
#define WS_X1 (WS_H1 + (4u << 20) + 65536u)
#define WS_X1N (WS_X1 + (4u << 20))
#define WS_H3 (WS_X1N + (4u << 20))
#define WS_P4 (WS_H3 + (14u << 20))
#define WS_G (WS_P4 + (8u << 20))
#define WS_PT (WS_G + (4u << 20))
#define WS_COMM (WS_PT + (32u << 20))
#define WS_END (WS_COMM + (4u << 20))
#define CB_STEP 524288u
#define PD 3
__device__ __forceinline__ unsigned comm_base(const char* ws) { return WS_COMM + ((0u - ((unsigned)(size_t)ws + WS_COMM)) & 0x1fffffu); }
__device__ __forceinline__ unsigned step_base(unsigned cb, unsigned mask, int s) { return cb + (unsigned)(s & 3) * CB_STEP + mask; }
#define MF_AT(ks) (((ks) >> 3) * 16384u + ((ks) & 7) * 1024u)
#define OB_AT(r, h) ((2u + (r) * 4u + ((h) >> 3)) * 16384u + ((h) & 7) * 1024u)
#define VB_AT(h) ((10u + ((h) >> 2)) * 16384u + ((h) & 3) * 2048u)
#define XS1 22
#define XS2 44
#define XS3 60
#define CSL 8
#define FSL 6
#define SQUICK 3
#define NCHUNK 18
#define FL_O(h) (29u * 16384u + (h) * 128u)
#define FL_B(b) (28u * 16384u + (b) * 128u)
#define FL_T(s) FL_O(32 + ((s) & 3))

struct Ptrs { const u16 *x, *mem0, *Wq, *Wk, *Wv, *Wo, *w1, *w2, *w3, *w4; u16* out; char* ws; unsigned tag; };
#define WS_RSRC(P) __builtin_amdgcn_make_buffer_rsrc((void*)(P).ws, 0, WS_END, RSRC_W3)

__device__ __forceinline__ bool spin_fail(unsigned& spins, const Ptrs& P, int lane) {
  asm volatile("" ::: "memory");
  if (++spins > SPIN_CAP) {
    unsigned tv;
    asm volatile("v_mov_b32 %0, %1" : "=v"(tv) : "s"(P.tag));
    if (lane == 0) __builtin_amdgcn_raw_buffer_store_b32(tv, WS_RSRC(P), 0, TMO_OFF, AUX_SC1);
    return true;
  }
  return (spins & 255u) == 0u && (unsigned)__builtin_amdgcn_readfirstlane(__builtin_amdgcn_raw_buffer_load_b32(WS_RSRC(P), 0, TMO_OFF, AUX_SC1)) == P.tag;
}
__device__ __forceinline__ unsigned cnt_load(char* ws, int idx) {
  return __builtin_amdgcn_readfirstlane(__hip_atomic_load((gu32*)(ws + CNT_OFF + idx * 128), RLX_AGENT));
}
__device__ __forceinline__ void cnt_add(char* ws, int idx, int lane) {
  asm volatile("s_waitcnt vmcnt(0)" ::: "memory");
  if (lane == 0) __hip_atomic_fetch_add((gu32*)(ws + CNT_OFF + idx * 128), 1u, RLX_AGENT);
}
__device__ __forceinline__ void cnt_wait(const Ptrs& P, int idx, unsigned want, int lane) {
  for (unsigned spins = 0;;) {
    if (cnt_load(P.ws, idx) >= want) break;
    __builtin_amdgcn_s_sleep(CSL);
    if (spin_fail(spins, P, lane)) break;
  }
}
__device__ __forceinline__ unsigned flag_wait(const Ptrs& P, int f, unsigned want, int lane) {
  const rsrc_t rs = WS_RSRC(P);
  u32x2 v;
  for (unsigned spins = 0;;) {
    v = ld8(rs, FLG_OFF + f * 128);
    if (!__any(v.y != P.tag || v.x < want)) break;
    __builtin_amdgcn_s_sleep(FSL);
    if (spin_fail(spins, P, lane)) break;
  }
  asm volatile("" ::: "memory");
  return v.x;
}
__device__ __forceinline__ void flag_set(const Ptrs& P, int f, unsigned val, int lane) {
  if (lane == 0) st8(WS_RSRC(P), FLG_OFF + f * 128, (u32x2){val, P.tag});
}
__device__ __forceinline__ void get_tag(Ptrs& P) { P.tag = __builtin_amdgcn_readfirstlane(__hip_atomic_load((gu32*)(P.ws + EPO_OFF), RLX_AGENT)) + 1u; }
__device__ __forceinline__ void setup_wait(const Ptrs& P, int lane) { flag_wait(P, F_LN0(lane), 0, lane); }
__device__ __forceinline__ void clock_wait(unsigned tgt) {
  while ((int)(tgt - (unsigned)__builtin_amdgcn_s_memrealtime()) > 0) {}
}
__device__ __forceinline__ void report(const Ptrs& P, int wid) {
  if (wid == 0 && opq(0) == 0) __hip_atomic_fetch_add((gu32*)(P.ws + CNT_OFF + C_ARR * 128), 1u, RLX_AGENT);
}
__device__ __forceinline__ void kv_wait(const Ptrs& P, int s, int h, int& known, int lane) {
  if (s < known) return;
  const unsigned v = flag_wait(P, F_PRG(min(lane, 2), h), s + 1, lane);
  known = __builtin_amdgcn_readfirstlane(min(min(__builtin_amdgcn_readlane(v, 0), __builtin_amdgcn_readlane(v, 1)), __builtin_amdgcn_readlane(v, 2)));
}
__device__ __forceinline__ int wmax(int v) {
  v = max(v, __builtin_amdgcn_update_dpp(0, v, 0x128, 0xf, 0xf, false));
  v = max(v, __builtin_amdgcn_update_dpp(0, v, 0x124, 0xf, 0xf, false));
  v = max(v, __builtin_amdgcn_update_dpp(0, v, 0x122, 0xf, 0xf, false));
  v = max(v, __builtin_amdgcn_update_dpp(0, v, 0x121, 0xf, 0xf, false));
  return max(max(__builtin_amdgcn_readlane(v, 0), __builtin_amdgcn_readlane(v, 16)), max(__builtin_amdgcn_readlane(v, 32), __builtin_amdgcn_readlane(v, 48)));
}
__device__ __forceinline__ unsigned near_half(unsigned v) { return wmax(-(int)(v & 65535u)) > wmax(-(int)(v >> 16)) ? 8192u : 0u; }
__device__ __forceinline__ void dmaw(rsrc_t rs, unsigned voff, unsigned soff, unsigned ldsaddr) {
  asm volatile("s_mov_b32 m0, %3\n\ts_nop 0\n\tbuffer_load_dwordx4 %0, %1, %2 offen lds" :: "v"(voff), "s"(rs), "s"(soff), "s"(ldsaddr) : "memory");
}
__device__ __forceinline__ void dman(rsrc_t rs, unsigned voff, unsigned soff, unsigned ldsaddr) {
  asm volatile("s_mov_b32 m0, %3\n\ts_nop 0\n\tbuffer_load_dwordx4 %0, %1, %2 offen nt lds" :: "v"(voff), "s"(rs), "s"(soff), "s"(ldsaddr) : "memory");
}
__device__ __forceinline__ void dmas(rsrc_t rs, unsigned voff, unsigned soff, unsigned ldsaddr) {
  asm volatile("s_mov_b32 m0, %3\n\ts_nop 0\n\tbuffer_load_dwordx4 %0, %1, %2 offen sc1 lds" :: "v"(voff), "s"(rs), "s"(soff), "s"(ldsaddr) : "memory");
}
__device__ __forceinline__ void touch(const void* w, unsigned bytes, unsigned voff, LAS unsigned char* lds) {
  asm volatile("s_mov_b32 m0, %2\n\ts_nop 0\n\tbuffer_load_dword %0, %1, 0 offen lds"
               :: "v"(voff), "s"(__builtin_amdgcn_make_buffer_rsrc((void*)w, 0, bytes, RSRC_W3)), "s"((unsigned)(size_t)lds + 148480) : "memory");
}
__device__ __forceinline__ u32x2 trd(LAS unsigned char* p) {
  return __builtin_bit_cast(u32x2, __builtin_amdgcn_ds_read_tr16_b64_v4i16((LAS bf16x4*)p));
}
template <int BO, int UO>
__device__ __forceinline__ u32x8 rd_frag(LAS unsigned char* p) {
  const u32x2 a = trd(p), b = trd(p + UO), c = trd(p + BO), d = trd(p + BO + UO);
  return (u32x8){a.x, a.y, b.x, b.y, c.x, c.y, d.x, d.y};
}

#define TL(i) do {} while (0)
#define TLW(i) do {} while (0)
#define GSDECL
#define GS(i) do {} while (0)
#define GSFLUSH(k, i) do {} while (0)

__device__ __forceinline__ int lds_byte(int r, int c) {
  const int st = (r >> 4) * 2 + (c >> 5), rr = r & 15, cc = c & 31, ob = rr * 64 + cc * 2;
  return st * 1024 + (ob ^ (((ob >> 9) & 1) << 5));
}
#define PFNT ""
#define G_DvoffB dmaw
#define HSP 3
#define PFD 3
#define PFH 4
struct Pf { unsigned v, step; bool on, a; };
#define G_PF(t) do { if (pf.on && (t) < nt) asm volatile("s_mov_b32 m0, %3\n\ts_nop 0\n\tbuffer_load_dword %0, %1, %2 offen sc1 " PFNT "lds" \
    :: "v"(pf.v), "s"(pf.a ? ra : rb), "s"((t) * pf.step), "s"((unsigned)(size_t)lds + 148480) : "memory"); } while (0)

__device__ __forceinline__ void gemm_loop(LAS unsigned char* lds, rsrc_t ra, rsrc_t rb, unsigned cA, unsigned cB, int lda, int ldb, int nt,
                                          const Pf& pf, f32x4 (&acc)[2][2][4][2]) {
  const int wid = __builtin_amdgcn_readfirstlane(threadIdx.x >> 6), lane = opq(0), tid = wid * 64 + lane;
  const int wr = wid >> 2, wc = wid & 3, fr = lane & 15, fq = lane >> 4;
  unsigned voffA[2], voffB[2];
  #pragma unroll
  for (int i = 0; i < 2; ++i) {
    voffA[i] = (unsigned)(((wid >> 1) + 4 * i) * 16 * lda + (wid & 1) * 1024 + lane * 16);
    const int b = tid * 16 + i * 8192, r = b >> 8, pos = (b >> 4) & 15;
    const int c = pos ^ (((r & 3) << 1) | (((r >> 3) & 1) << 3));
    voffB[i] = (unsigned)(r * ldb + c * 16);
  }
  const unsigned hA = 128 * lda;
  const unsigned kB = 64 * ldb;
  const unsigned ldsw = (unsigned)(size_t)lds + (unsigned)wid * 1024u;
  const int aoff = 65536 + lds_byte(wr * 64 + fr, fq * 8);
  const int q = (lane & 15) >> 2, p = lane & 3;
  const unsigned boff0 = (unsigned)(size_t)lds + (8 * fq + q) * 256 + (((wc * 4 + (p >> 1)) ^ ((q << 1) | ((fq & 1) << 3))) << 4) + 8 * (p & 1);
  const unsigned boff1 = boff0 ^ 32;
#define G_SB(b, h) (((b) * 2 + (h)) * 16384)
#define G_SA(b, h) (65536 + ((b) * 2 + (h)) * 16384)
#define G_ST1(bufoff, r, so, voff, i) G_D##voff(r, (voff)[i], so, ldsw + (bufoff) + (i) * 8192)
#define G_DvoffA dmas
#define G_STAGE(bufoff, so, voff) do { G_ST1(bufoff, G_R##voff, so, voff, 0); G_ST1(bufoff, G_R##voff, so, voff, 1); } while (0)
#define G_RvoffA ra
#define G_RvoffB rb
#define G_LDA(dst, b, h) do { _Pragma("unroll") for (int m = 0; m < 4; ++m) _Pragma("unroll") for (int k = 0; k < 2; ++k) \
    dst[m][k] = *(const LAS bf16x8*)(lds + (G_SA(b, h) - 65536) + aoff + m * 2048 + k * 1024); } while (0)
#define G_TR(dst, base, off) asm volatile("ds_read_b64_tr_b16 %0, %1 offset:%2" : "=v"(dst) : "v"(base), "i"(off))
#define G_LDB(dst, b, h) do { _Pragma("unroll") for (int n = 0; n < 2; ++n) _Pragma("unroll") for (int k = 0; k < 2; ++k) { \
    bf16x4 _v0, _v1; \
    G_TR(_v0, (n ? boff1 : boff0), G_SB(b, h) + k * 8192); \
    G_TR(_v1, (n ? boff1 : boff0), G_SB(b, h) + k * 8192 + 1024); \
    dst[n][k] = __builtin_shufflevector(_v0, _v1, 0, 1, 2, 3, 4, 5, 6, 7); } } while (0)
#define G_WAIT_B(Bt) asm volatile("s_waitcnt lgkmcnt(0)" : "+v"(Bt[0][0]), "+v"(Bt[0][1]), "+v"(Bt[1][0]), "+v"(Bt[1][1]) :: "memory")
#define G_MMA(ai, bj, At, Bt) do { __builtin_amdgcn_s_setprio(1); _Pragma("unroll") for (int m = 0; m < 4; ++m) _Pragma("unroll") for (int n = 0; n < 2; ++n) _Pragma("unroll") for (int k = 0; k < 2; ++k) \
    acc[ai][bj][m][n] = __builtin_amdgcn_mfma_f32_16x16x32_bf16(Bt[n][k], At[m][k], acc[ai][bj][m][n], 0, 0, 0); \
    __builtin_amdgcn_s_setprio(0); } while (0)
#define G_WAIT_V(n) asm volatile("s_waitcnt vmcnt(%0)" :: "n"(n) : "memory")
#define G_WAIT_L(n) asm volatile("s_waitcnt lgkmcnt(" #n ")" ::: "memory")
#define G_BAR __builtin_amdgcn_s_barrier()
#define G_SCHED __builtin_amdgcn_sched_barrier(0)
#define G_APTR(t) (cA + (t) * 2048)
  #pragma unroll
  for (int a = 0; a < 2; ++a)
    #pragma unroll
    for (int b = 0; b < 2; ++b)
      #pragma unroll
      for (int m = 0; m < 4; ++m)
        #pragma unroll
        for (int n = 0; n < 2; ++n) acc[a][b][m][n] = (f32x4){0.f, 0.f, 0.f, 0.f};
  bf16x8 At[4][2], B0[2][2], B1[2][2];
  {
    const unsigned a0 = G_APTR(0);
    #pragma unroll
    for (int t = 2; t < PFD; ++t) G_PF(t);
    G_STAGE(G_SB(0, 0), cB, voffB); G_STAGE(G_SA(0, 0), a0, voffA); G_STAGE(G_SB(0, 1), cB + 256, voffB); G_STAGE(G_SA(0, 1), a0 + hA, voffA);
    if (wr == 1) G_BAR;
    G_WAIT_V(4); G_BAR;
    G_STAGE(G_SB(1, 0), cB + kB, voffB); G_STAGE(G_SA(1, 0), a0 + 2048, voffA); G_STAGE(G_SB(1, 1), cB + kB + 256, voffB);
    G_WAIT_V(6); G_BAR;
  }
  for (int t = 0; t < nt; t += 2) {
    const int t2 = (t == nt - 2) ? 0 : t + 2;
    const unsigned a1 = G_APTR(t + 1);
    const unsigned a2 = G_APTR(t2);
    const unsigned b2 = cB + t2 * kB;
    const unsigned a3 = a2 + 2048;
    const unsigned b3 = b2 + kB;
    G_LDB(B0, 0, 0); G_SCHED; G_LDA(At, 0, 0); G_STAGE(G_SA(1, 1), a1 + hA, voffA);
    G_WAIT_L(8); G_BAR; G_WAIT_B(B0); G_MMA(0, 0, At, B0); G_BAR; G_SCHED;
    G_LDB(B1, 0, 1); G_STAGE(G_SB(0, 0), b2, voffB);
    G_BAR; G_WAIT_B(B1); G_MMA(0, 1, At, B1); G_BAR;
    G_LDA(At, 0, 1); G_STAGE(G_SA(0, 0), a2, voffA);
    G_BAR; G_WAIT_L(0); G_MMA(1, 0, At, B0); G_BAR; G_SCHED;
    G_STAGE(G_SB(0, 1), b2 + 256, voffB);
    G_WAIT_V(6); G_PF(t + PFD); G_BAR; G_MMA(1, 1, At, B1); G_BAR;
    G_LDB(B0, 1, 0); G_SCHED; G_LDA(At, 1, 0); G_STAGE(G_SA(0, 1), a2 + hA, voffA);
    G_WAIT_L(8); G_BAR; G_WAIT_B(B0); G_MMA(0, 0, At, B0); G_BAR; G_SCHED;
    G_LDB(B1, 1, 1); G_STAGE(G_SB(1, 0), b3, voffB);
    G_BAR; G_WAIT_B(B1); G_MMA(0, 1, At, B1); G_BAR;
    G_LDA(At, 1, 1); G_STAGE(G_SA(1, 0), a3, voffA);
    G_BAR; G_WAIT_L(0); G_MMA(1, 0, At, B0); G_BAR; G_SCHED;
    G_STAGE(G_SB(1, 1), b3 + 256, voffB);
    G_WAIT_V(6); G_PF(t + PFD + 1); G_BAR; G_MMA(1, 1, At, B1); G_BAR;
  }
  if (wr == 0) G_BAR;
}

__device__ __forceinline__ void gemm_h(LAS unsigned char* lds, rsrc_t ra, rsrc_t rb, unsigned cA, unsigned cB, int lda, int ldb, int nt,
                                       const Pf& pf, f32x4 (&acc)[1][2][4][2]) {
  const int wid = __builtin_amdgcn_readfirstlane(threadIdx.x >> 6), lane = opq(0), tid = wid * 64 + lane;
  const int wr = wid >> 2, wc = wid & 3, fr = lane & 15, fq = lane >> 4;
  unsigned voffA[2], voffB[2];
  #pragma unroll
  for (int i = 0; i < 2; ++i) {
    voffA[i] = (unsigned)(((wid >> 1) + 4 * i) * 16 * lda + (wid & 1) * 1024 + lane * 16);
    const int b = tid * 16 + i * 8192, r = b >> 8, pos = (b >> 4) & 15;
    const int c = pos ^ (((r & 3) << 1) | (((r >> 3) & 1) << 3));
    voffB[i] = (unsigned)(r * ldb + c * 16);
  }
  const unsigned kB = 64 * ldb;
  const unsigned ldsw = (unsigned)(size_t)lds + (unsigned)wid * 1024u;
  const int q = (lane & 15) >> 2, p = lane & 3;
  const int aoffs = 32768 + lds_byte(wr * 64 + fr, fq * 8);
  const unsigned boffs = (unsigned)(size_t)lds + (8 * fq + q) * 256 + (((wc * 4 + (p >> 1)) ^ ((q << 1) | ((fq & 1) << 3))) << 4) + 8 * (p & 1);
  #pragma unroll
  for (int i = 0; i < 16; ++i) acc[0][i >> 3][(i >> 1) & 3][i & 1] = (f32x4){0.f, 0.f, 0.f, 0.f};
  bf16x8 At[4][2], B0[2][2], B1[2][2];
#define H_STAGE(so, t) do { G_STAGE(so, cB + (t) * kB, voffB); G_STAGE((so) + 32768, G_APTR(t), voffA); G_STAGE((so) + 16384, cB + (t) * kB + 256, voffB); } while (0)
  #pragma unroll
  for (int t = 2; t < PFH; ++t) G_PF(t);
  H_STAGE(0, 0); H_STAGE(49152, 1);
  G_WAIT_V(6); G_BAR;
  if (wr == 1) G_BAR;
  int cur = 0, nxt = 98304;
#define H_M4(bj, m, Bt) do { _Pragma("unroll") for (int n = 0; n < 2; ++n) _Pragma("unroll") for (int k = 0; k < 2; ++k) \
    acc[0][bj][m][n] = __builtin_amdgcn_mfma_f32_16x16x32_bf16(Bt[n][k], At[m][k], acc[0][bj][m][n], 0, 0, 0); G_SCHED; } while (0)
  for (int t = 0; t < nt; ++t) {
    const int aoff = aoffs + cur;
    const unsigned boff0 = boffs + cur, boff1 = boff0 ^ 32;
    const int t2 = t + 2 < nt ? t + 2 : 0;
    const unsigned a2 = G_APTR(t2), b2 = cB + t2 * kB;
    G_LDB(B0, 0, 0); G_ST1(nxt, rb, b2, voffB, 0); G_LDA(At, 0, 0); G_SCHED; G_ST1(nxt, rb, b2, voffB, 1); G_LDB(B1, 0, 1); G_SCHED;
    if (HSP > 2) G_ST1(nxt + 32768, ra, a2, voffA, 0);
    G_WAIT_V(HSP); G_WAIT_B(B0); G_WAIT_B(B1); G_BAR; G_SCHED;
    __builtin_amdgcn_s_setprio(1);
    H_M4(0, 0, B0); if (HSP < 3) G_ST1(nxt + 32768, ra, a2, voffA, 0);
    H_M4(0, 1, B0); G_ST1(nxt + 32768, ra, a2, voffA, 1);
    H_M4(0, 2, B0); G_ST1(nxt + 16384, rb, b2 + 256, voffB, 0);
    H_M4(0, 3, B0); G_ST1(nxt + 16384, rb, b2 + 256, voffB, 1);
    H_M4(1, 0, B1); G_PF(t + PFH);
    H_M4(1, 1, B1); H_M4(1, 2, B1); H_M4(1, 3, B1);
    __builtin_amdgcn_s_setprio(0);
    G_BAR; G_SCHED;
    nxt = cur;
    cur = cur == 98304 ? 0 : cur + 49152;
  }
  if (wr == 0) G_BAR;
}

__device__ __forceinline__ u32x4 epi_pack(const f32x4 a0, const f32x4 a1) {
  const unsigned x0 = pack2(a0[0], a0[1]), x1 = pack2(a0[2], a0[3]);
  const unsigned y0 = pack2(a1[0], a1[1]), y1 = pack2(a1[2], a1[3]);
  auto s0 = __builtin_amdgcn_permlane16_swap(x0, y0, false, false);
  auto s1 = __builtin_amdgcn_permlane16_swap(x1, y1, false, false);
  return (u32x4){s0[0], s1[0], s0[1], s1[1]};
}
__device__ __forceinline__ unsigned gelu2(unsigned w) {
  const f32x2 x = {lo16(w), hi16(w)};
  const float c0 = -2.f * 1.4426950408889634f * 0.7978845608028654f, c1 = c0 * 0.044715f;
  const f32x2 a = x * (x * x * c1 + c0);
  const f32x2 d = (f32x2){__builtin_amdgcn_exp2f(a[0]), __builtin_amdgcn_exp2f(a[1])} + 1.f;
  const f32x2 y = x * (f32x2){__builtin_amdgcn_rcpf(d[0]), __builtin_amdgcn_rcpf(d[1])};
  return pack2(y[0], y[1]);
}
__device__ __forceinline__ float silu_f(float b) { return b * __builtin_amdgcn_rcpf(1.f + __expf(-b)); }

__device__ __forceinline__ unsigned blk_in(int r, int c) { const int ob = r * 64 + c * 2; return ob ^ (((ob >> 9) & 1) << 5); }
__device__ __forceinline__ unsigned xn_blk(int rb, int cb) { return (unsigned)(((rb >> 1) * 32 + cb) * 2 + (rb & 1)) * 1024u; }

template <int EPI, int NA>
__device__ __forceinline__ void epi_rows(const f32x4 (&acc)[NA][2][4][2], rsrc_t rs, unsigned obase, int ldc, int pm2, int pn2, int sh, int wr, int wc, int ln) {
  const int fq = ln >> 4, cb = 16 * (fq & 1) + 8 * (fq >> 1), fr = (ln & 15) + sh;
  const unsigned lo = (unsigned)((fr >> 4) * 16 * ldc) + blk_in(fr & 15, cb);
  u32x4 r[8];
  if (EPI == 2) {
    #pragma unroll
    for (int i = 0; i < 8; ++i) r[i] = ld16(rs, WS_XN + xn_blk(pm2 * 8 + 4 * wr + (i >> 1), pn2 * 4 + 4 * (i & 1) + wc) + lo);
  }
  #pragma unroll
  for (int ai = 0; ai < NA; ++ai)
    #pragma unroll
    for (int m = 0; m < 4; ++m)
      #pragma unroll
      for (int bj = 0; bj < 2; ++bj) {
        u32x4 w = epi_pack(acc[ai][bj][m][0], acc[ai][bj][m][1]);
        #pragma unroll
        for (int i = 0; i < 4; ++i) w[i] = EPI < 2 ? gelu2(w[i]) : pack2(lo16(r[2 * m + bj][i]) + lo16(w[i]), hi16(r[2 * m + bj][i]) + hi16(w[i]));
        st16(rs, obase + (unsigned)(pm2 * 128 + 128 * ai + 64 * wr + 16 * m) * (unsigned)ldc + (unsigned)(pn2 * 4 + 4 * bj + wc) * 1024u + lo, w);
      }
}

__device__ __forceinline__ float row_sum16(float v) {
  v += __uint_as_float(__builtin_amdgcn_update_dpp(0, (int)__float_as_uint(v), 0x128, 0xf, 0xf, false));
  v += __uint_as_float(__builtin_amdgcn_update_dpp(0, (int)__float_as_uint(v), 0x124, 0xf, 0xf, false));
  v += __uint_as_float(__builtin_amdgcn_update_dpp(0, (int)__float_as_uint(v), 0x122, 0xf, 0xf, false));
  v += __uint_as_float(__builtin_amdgcn_update_dpp(0, (int)__float_as_uint(v), 0x121, 0xf, 0xf, false));
  return v;
}
__device__ __forceinline__ void ln_rows(u32x4 (&a)[8]) {
  f32x2 v[32];
  #pragma unroll
  for (int i = 0; i < 32; ++i) v[i] = (f32x2){lo16(a[i >> 2][i & 3]), hi16(a[i >> 2][i & 3])};
  f32x2 s[4] = {v[0], v[1], v[2], v[3]};
  #pragma unroll
  for (int i = 4; i < 32; ++i) s[i & 3] += v[i];
  s[0] = (s[0] + s[1]) + (s[2] + s[3]);
  const float mean = row_sum16(s[0][0] + s[0][1]) * (1.f / DM);
  #pragma unroll
  for (int i = 0; i < 32; ++i) { const f32x2 d = v[i] - mean; s[i & 3] = i < 4 ? d * d : s[i & 3] + d * d; }
  s[0] = (s[0] + s[1]) + (s[2] + s[3]);
  const float var = row_sum16(s[0][0] + s[0][1]) * (1.f / DM);
  const float mb = rb1(mean), vb = rb1(var);
  const float rden = __builtin_amdgcn_rcpf(rb1(__builtin_amdgcn_sqrtf(rb1(vb + 1e-5f))));
  #pragma unroll
  for (int i = 0; i < 32; ++i) {
    const f32x2 d = v[i] - mb;
    const unsigned w = pack2(d[0], d[1]);
    const f32x2 y = (f32x2){lo16(w), hi16(w)} * rden;
    a[i >> 2][i & 3] = pack2(y[0], y[1]);
  }
}

template <int N>
__device__ __forceinline__ void fin_out(rsrc_t rs, const Ptrs& P, unsigned po, unsigned go, unsigned eo) {
  u32x4 pv[N][8], gv[N], xv[N];
  #pragma unroll
  for (int c = 0; c < N; ++c) {
    #pragma unroll
    for (int i = 0; i < 8; ++i) pv[c][i] = ld16(rs, po + c * 16384 + (i >> 1) * 131072 + (i & 1) * 1024);
    gv[c] = ld16(rs, go + c * 4096);
    xv[c] = __builtin_nontemporal_load((const u32x4*)((const char*)P.x + eo + c * 256));
  }
  #pragma unroll
  for (int c = 0; c < N; ++c) {
#define PF4(i) __builtin_bit_cast(f32x4, pv[c][i])
    u32x4 v = epi_pack((PF4(0) + PF4(2)) + (PF4(4) + PF4(6)), (PF4(1) + PF4(3)) + (PF4(5) + PF4(7)));
    #pragma unroll
    for (int k = 0; k < 4; ++k) {
      v[k] = pack2(lo16(v[k]) * lo16(gv[c][k]), hi16(v[k]) * hi16(gv[c][k]));
      v[k] = pack2(lo16(xv[c][k]) + lo16(v[k]), hi16(xv[c][k]) + hi16(v[k]));
    }
    *(u32x4*)((char*)P.out + eo + c * 256) = v;
  }
}

template <int KH>
__device__ __forceinline__ void epi_w4(const f32x4 (&acc)[2][2][4][2], rsrc_t rs, const Ptrs& P, int pm, int pc, int wid, int ln) {
  char* ws = P.ws;
  const int wr = wid >> 2, wc = wid & 3, fr = ln & 15, fq = ln >> 4;
  const unsigned pb = WS_P4 + (unsigned)(pm * 4 + pc) * 262144u + wid * 16384 + ln * 16;
  #pragma unroll
  for (int i = 0; i < 16; ++i) st16(rs, pb + KH * 131072 + i * 1024, __builtin_bit_cast(u32x4, acc[KH ^ 1][(i >> 1) & 1][i >> 2][i & 1]));
  const unsigned eb = (unsigned)(pm * 256 + 128 * KH + 64 * wr) * 2048u + (unsigned)(pc * 8 + wc) * 1024u + blk_in(fr, 16 * (fq & 1) + 8 * (fq >> 1));
  u32x4 pp[16], r1[8];
  #pragma unroll
  for (int i = 0; i < 8; ++i) r1[i] = ld16(rs, WS_X1 + eb + (i >> 1) * 32768 + (i & 1) * 4096);
  VMWAIT();
  BARRIER();
  if (wid == 0) {
    if (ln == 0) __hip_atomic_fetch_add((gu32*)(ws + CNT_OFF + C_P4(pm, pc) * 128), 1u, RLX_AGENT);
    cnt_wait(P, C_P4(pm, pc), 2, ln);
  }
  BARRIER();
  #pragma unroll
  for (int i = 0; i < 16; ++i) pp[i] = ld16(rs, pb + (KH ^ 1) * 131072 + i * 1024);
  asm volatile("" ::: "memory");
  #pragma unroll
  for (int i = 0; i < 8; ++i) {
    u32x4 v = epi_pack(acc[KH][i & 1][i >> 1][0] + __builtin_bit_cast(f32x4, pp[2 * i]), acc[KH][i & 1][i >> 1][1] + __builtin_bit_cast(f32x4, pp[2 * i + 1]));
    #pragma unroll
    for (int k = 0; k < 4; ++k) {
      v[k] = pack2(lo16(r1[i][k]) + lo16(v[k]), hi16(r1[i][k]) + hi16(v[k]));
      v[k] = pack2(silu_f(lo16(v[k])), silu_f(hi16(v[k])));
    }
    st16(rs, WS_G + eb + (i >> 1) * 32768 + (i & 1) * 4096, v);
  }
}

#define W4S 1
__device__ __forceinline__ void mlp_role(LAS unsigned char* lds, const Ptrs& P, int hf, int slot, int wid) {
  const int wr = wid >> 2, wc = wid & 3;
  char* ws = P.ws;
  const rsrc_t rs = WS_RSRC(P);
#define M_DONE(c) do { VMWAIT(); BARRIER(); if (wid == 0) cnt_add(ws, c, opq(0)); } while (0)
  #pragma unroll 1
  for (int code = 0; code < 2; ++code) {
    const int p2 = hf * 8 + (slot >> 2), pn = slot & 3;
    int ln = opq(0);
    if (wid == 0) {
      if (code == 0) flag_wait(P, F_LN1(p2 * 4 + (ln & 3)), 0, ln);
      else {
        const unsigned co = CNT_OFF + C_W1(min(max(p2 - 1 + min(ln, 2), 0), 15)) * 128;
        for (unsigned spins = 0; __any(ld4(rs, co) < 4u);) {
          __builtin_amdgcn_s_sleep(4);
          if (spin_fail(spins, P, ln)) break;
        }
      }
    }
    BARRIER();
    TLW(3 * code);
    const rsrc_t rb = __builtin_amdgcn_make_buffer_rsrc((void*)(code ? P.w2 : P.w1), 0, DM * DM * 2, RSRC_W3);
    const unsigned cA = (code ? WS_H1 : WS_XN2) + p2 * 128 * 2048, cB = pn * 512;
    Pf pf;
    pf.on = wc == 0; pf.a = wr;
    ln &= 31;
    pf.v = wr ? cA + (pn * 2 + (ln >> 4)) * 32768 + (ln & 15) * 128 : cB + ((slot >> 2) * 8 + (ln >> 2)) * 2048 + (ln & 3) * 128;
    pf.step = wr ? 2048 : 64 * 2048;
    f32x4 acq[1][2][4][2];
    gemm_h(lds, rs, rb, cA, cB, 2048, 2048, 16, pf, acq);
    TLW(3 * code + 1);
    ln = opq(0);
    if (code == 0) {
      if (wid < 4) touch(P.w2, DM * DM * 2, ((hf * 32 + slot) * 256 + wid * 64 + ln) * 128, lds);
      epi_rows<0, 1>(acq, rs, WS_H1, 2048, p2, 2 * pn, (pn & 1) ? -1 - (pn >> 1) : 1 + (pn >> 1), wr, wc, ln);
      if (wid == 0) setup_wait(P, ln);
      M_DONE(C_W1(p2));
    } else {
      epi_rows<2, 1>(acq, rs, WS_X1, 2048, p2, 2 * pn, 0, wr, wc, ln);
      M_DONE(C_W2(p2));
      if (wid == 0) cnt_wait(P, C_W2(p2), 4, ln);
      BARRIER();
      const unsigned ro = (unsigned)(p2 * 8 + pn * 2 + wr) * 32768u + ((ln >> 2) & 3) * 1024 + blk_in(wc * 4 + (ln >> 4), (ln & 3) * 8);
      u32x4 v[8];
      #pragma unroll
      for (int i = 0; i < 8; ++i) v[i] = ld16(rs, WS_X1 + ro + i * 4096);
      ln_rows(v);
      #pragma unroll
      for (int i = 0; i < 8; ++i) st16(rs, WS_X1N + ro + i * 4096, v[i]);
      M_DONE(C_LN(p2 >> 1));
    }
    TLW(3 * code + 2);
  }
  #pragma unroll 1
  for (int code = 2; code < 5; ++code) {
    const int pm = hf * 4 + (slot & 3), pn = (slot >> 2) + (code == 3 ? 8 : 0);
    int ln = opq(0);
    if (pn >= 14) {
      #pragma unroll 1
      for (int i = 0; i < 7 * W4S; ++i) {
        if ((wid & (W4S - 1)) == (i & (W4S - 1))) touch(P.w4, DM * FF * 2, (((i / W4S) * 16 + hf * 8 + (slot & 7)) * 512 + wid * 64 + ln) * 128, lds);
        __builtin_amdgcn_s_sleep(96 / W4S);
      }
      continue;
    }
    if (code != 3) {
      if (wid == 0) cnt_wait(P, code == 2 ? C_LN(pm) : C_W3(pm, pn >> 2), code == 2 ? 8 : 7, ln);
      BARRIER();
    }
    TLW(3 * code);
    const bool w4 = code == 4;
    const int lda = w4 ? 7168 : 2048, ldb = w4 ? 2048 : 7168, nt = w4 ? 28 : 16, na = w4 ? 4 : 8;
    const rsrc_t rb = __builtin_amdgcn_make_buffer_rsrc((void*)(w4 ? P.w4 : P.w3), 0, DM * FF * 2, RSRC_W3);
    const unsigned cA = (w4 ? WS_H3 + (pn >> 2) * 28 * 2048 : WS_X1N) + pm * 256 * lda, cB = w4 ? (pn >> 2) * 28 * 64 * 2048 + (pn & 3) * 512 : pn * 512;
    Pf pf;
    pf.on = wc == 0 && (!wr || code != 3); pf.a = wr;
    pf.v = wr ? cA + ((pn & (na - 1)) * (16 / na) + ((ln >> 4) & (16 / na - 1))) * 16 * lda + (ln & 15) * 128 : cB + ((slot & 3) * 16 + (ln >> 2)) * ldb + (ln & 3) * 128;
    pf.step = wr ? 2048 : 64 * ldb;
    f32x4 acc[2][2][4][2];
    gemm_loop(lds, rs, rb, cA, cB, lda, ldb, nt, pf, acc);
    TLW(3 * code + 1);
    ln = opq(0);
    if (!w4) epi_rows<1, 2>(acc, rs, WS_H3, 7168, 2 * pm, 2 * pn, 0, wr, wc, ln);
    else if (pn >> 2) epi_w4<1>(acc, rs, P, pm, pn & 3, wid, ln);
    else epi_w4<0>(acc, rs, P, pm, pn & 3, wid, ln);
    M_DONE(w4 ? C_W4(pm) : C_W3(pm, pn >= 7));
    TLW(3 * code + 2);
  }
}

#define FTH 0
#define FINE 52
__device__ __forceinline__ void fin_role(LAS unsigned char* lds, const Ptrs& P, int wid) {
  const int wr = wid >> 2, wc = wid & 3;
  const rsrc_t rs = WS_RSRC(P);
  volatile LAS int* tk = (volatile LAS int*)(lds + 147456);
  unsigned tks = 0;
#define POP() do { if (wid == 0 && opq(0) == 0) tks = __hip_atomic_fetch_add((gu32*)(P.ws + CNT_OFF + C_HEAD * 128), 1u, RLX_AGENT); } while (0)
  POP();
  for (int round = 0;; ++round) {
    if (wid == 0 && opq(0) == 0) tk[round & 1] = tks;
    LBARRIER();
    const int u = __builtin_amdgcn_readfirstlane(tk[round & 1]);
    if (u >= 2 * FINE + 8 * (XS3 - FINE)) break;
    const bool big = u < 2 * FINE;
    const int sg = big ? u >> 1 : FINE + ((u - 2 * FINE) >> 3), cc0 = big ? (u & 1) * 4 : (u - 2 * FINE) & 7;
    const int ln = opq(0), fq = ln >> 4, cb = 16 * (fq & 1) + 8 * (fq >> 1);
    const unsigned eo = (unsigned)(sg * 32 + wr * 16 + (ln & 15)) * 2048u + (unsigned)(cc0 * 128 + wc * 32 + cb) * 2u;
    const unsigned go = WS_G + (unsigned)(sg * 2 + wr) * 32768u + (cc0 * 4 + wc) * 1024 + blk_in(ln & 15, cb);
    const unsigned po = WS_PT + (unsigned)sg * 524288u + cc0 * 16384 + wc * 4096 + wr * 2048 + ln * 16;
    POP();
    if (wid == 1) cnt_wait(P, C_W4(sg >> 3), 8, ln);
    if (wid == 2) { const int xg = (sg >= XS1) + (sg >= XS2); flag_wait(P, F_PRG(5 + xg, ln & 31), sg + 1 - (xg == 2 ? XS2 : xg * XS1), ln); }
    BARRIER();
    if (!big) fin_out<1>(rs, P, po, go, eo);
    else {
      fin_out<2>(rs, P, po, go, eo); fin_out<2>(rs, P, po + 32768, go + 8192, eo + 512);
    }
  }
}

#define QPRIO 0
#define VPOS 3
#define VLDI "buffer_load_dword v254, v17, %[rs], %[sb] offen sc1\n\tbuffer_load_dword v255, v17, %[rs], %[sb] offen offset:256 sc1\n\t"
#define VLD(p) VLD##p
#define VLD0
#define VLD1
#define VLD2
#define VLD3 VLDI
#define VLD4
#define DEC1 1
#define DEC2 1
#define DGA
#define DGB
#define DGC
#define DGO
#define DGP
#define DGQ
#define PACE "s_sleep 1\n\t"
#define STR_(x) #x
#define STR(x) STR_(x)
#define GSW
#define GSA(k)
#define GSO(k)
#define A_MAIN_Q() \
  asm volatile( \
    "v_mov_b32 v253, %[tag]\n\t" \
    "buffer_load_dwordx4 v[20:23], v12, %[rs], %[vx] offen sc1\n\t" \
    "buffer_load_dwordx4 v[24:27], v12, %[rs], %[vx] offen offset:1024 sc1\n\t" \
    "ds_read_b128 v[236:239], v9 offset:0\n\t" \
    "ds_read_b128 v[240:243], v9 offset:1024\n\t" \
    "ds_read_b128 v[244:247], v9 offset:2048\n\t" \
    "ds_read_b128 v[248:251], v9 offset:3072\n\t" \
    "v_mov_b32 v0, 0\n\t" \
    "v_mov_b32 v1, 0\n\t" \
    "v_mov_b32 v2, 0\n\t" \
    "v_mov_b32 v3, 0\n\t" \
    "v_mov_b32 v4, 0\n\t" \
    "v_mov_b32 v5, 0\n\t" \
    "v_mov_b32 v6, 0\n\t" \
    "v_mov_b32 v7, 0\n\t" \
    "s_cmp_lg_u32 %[sc], 3\n\t" \
    "s_cbranch_scc1 PB_%=\n\t" \
    "TWA_%=:\n\t" \
    "s_memrealtime s[88:89]\n\t" \
    "s_memtime s[90:91]\n\t" \
    "s_waitcnt lgkmcnt(0)\n\t" \
    "s_lshl_b32 s88, s88, 2\n\t" \
    "s_sub_u32 s89, %[tgt], s88\n\t" \
    "s_cmp_gt_i32 s89, 16\n\t" \
    "s_cbranch_scc1 TWA_%=\n\t" \
    "s_mul_i32 s89, s89, 11\n\t" \
    "s_ashr_i32 s89, s89, 1\n\t" \
    "s_add_u32 s89, s89, s90\n\t" \
    DGB \
    "TCA_%=:\n\t" \
    "s_memtime s[92:93]\n\t" \
    "s_waitcnt lgkmcnt(0)\n\t" \
    "s_sub_u32 s93, s89, s92\n\t" \
    "s_cmp_gt_i32 s93, 0\n\t" \
    "s_cbranch_scc1 TCA_%=\n\t" \
    "PB_%=:\n\t" \
    "s_waitcnt lgkmcnt(0)\n\t" \
    "s_barrier\n\t" \
    "buffer_load_dwordx4 v[220:223], v8, %[rs], %[m0] offen sc1\n\t" \
    "buffer_load_dwordx4 v[224:227], v8, %[rs], %[m1] offen sc1\n\t" \
    "PM_%=:\n\t" \
    "s_waitcnt vmcnt(1)\n\t" \
    "ds_write_b128 v8, v[220:223] offset:0\n\t" \
    "v_max3_u32 v252, v220, v221, v222\n\t" \
    "s_waitcnt vmcnt(0)\n\t" \
    "ds_write_b128 v8, v[224:227] offset:4096\n\t" \
    "v_max3_u32 v252, v252, v223, v224\n\t" \
    "v_max3_u32 v252, v252, v225, v226\n\t" \
    "v_max_u32 v252, v252, v227\n\t" \
    "v_cmp_eq_u32 vcc, -1, v252\n\t" \
    "s_cbranch_vccz PD_%=\n\t" \
    "s_sub_u32 %[lim], %[lim], 1\n\t" \
    "s_cmp_gt_i32 %[lim], 0\n\t" \
    "s_cbranch_scc0 PD_%=\n\t" \
    "s_sleep " STR(CADM) "\n\t" \
    "buffer_load_dwordx4 v[220:223], v8, %[rs], %[m0] offen sc1\n\t" \
    "buffer_load_dwordx4 v[224:227], v8, %[rs], %[m1] offen sc1\n\t" \
    "s_branch PM_%=\n\t" \
    "PD_%=:\n\t" \
    GSA(0) \
    "v_smfmac_f32_16x16x64_bf16 v[0:3], v[220:223], v[236:243], v10\n\t" \
    "v_xor_b32 v9, 16384, v9\n\t" \
    "ds_read_b128 v[236:239], v9 offset:0\n\t" \
    "ds_read_b128 v[240:243], v9 offset:1024\n\t" \
    "v_smfmac_f32_16x16x64_bf16 v[4:7], v[220:223], v[244:251], v10\n\t" \
    "ds_read_b128 v[244:247], v9 offset:2048\n\t" \
    "ds_read_b128 v[248:251], v9 offset:3072\n\t" \
    "s_waitcnt lgkmcnt(4)\n\t" \
    "s_barrier\n\t" \
    "ds_read_b128 v[228:231], v8 offset:8192\n\t" \
    "ds_read_b128 v[232:235], v8 offset:12288\n\t" \
    "v_xor_b32 v8, 1024, v8\n\t" \
    "ds_read_b128 v[220:223], v8 offset:0\n\t" \
    "v_smfmac_f32_16x16x64_bf16 v[0:3], v[224:227], v[28:35], v10\n\t" \
    "v_smfmac_f32_16x16x64_bf16 v[4:7], v[224:227], v[36:43], v10\n\t" \
    "ds_read_b128 v[224:227], v8 offset:4096\n\t" \
    "s_waitcnt lgkmcnt(3)\n\t" \
    "v_smfmac_f32_16x16x64_bf16 v[0:3], v[228:231], v[92:99], v10\n\t" \
    "v_smfmac_f32_16x16x64_bf16 v[4:7], v[228:231], v[100:107], v10\n\t" \
    "ds_read_b128 v[228:231], v8 offset:8192\n\t" \
    "s_waitcnt lgkmcnt(3)\n\t" \
    "v_smfmac_f32_16x16x64_bf16 v[0:3], v[232:235], v[156:163], v10\n\t" \
    "v_smfmac_f32_16x16x64_bf16 v[4:7], v[232:235], v[164:171], v10\n\t" \
    "ds_read_b128 v[232:235], v8 offset:12288\n\t" \
    "s_waitcnt lgkmcnt(3)\n\t" \
    "v_smfmac_f32_16x16x64_bf16 v[0:3], v[220:223], v[236:243], v10\n\t" \
    "v_xor_b32 v9, 49152, v9\n\t" \
    "ds_read_b128 v[236:239], v9 offset:0\n\t" \
    "v_smfmac_f32_16x16x64_bf16 v[4:7], v[220:223], v[244:251], v10\n\t" \
    "ds_read_b128 v[240:243], v9 offset:1024\n\t" \
    "s_waitcnt lgkmcnt(4)\n\t" \
    "v_smfmac_f32_16x16x64_bf16 v[0:3], v[224:227], v[44:51], v10\n\t" \
    "v_xor_b32 v8, 3072, v8\n\t" \
    "ds_read_b128 v[220:223], v8 offset:4096\n\t" \
    "v_smfmac_f32_16x16x64_bf16 v[4:7], v[224:227], v[52:59], v10\n\t" \
    "ds_read_b128 v[244:247], v9 offset:2048\n\t" \
    "s_waitcnt lgkmcnt(5)\n\t" \
    "v_smfmac_f32_16x16x64_bf16 v[0:3], v[228:231], v[108:115], v10\n\t" \
    "ds_read_b128 v[224:227], v8 offset:8192\n\t" \
    "v_smfmac_f32_16x16x64_bf16 v[4:7], v[228:231], v[116:123], v10\n\t" \
    "ds_read_b128 v[248:251], v9 offset:3072\n\t" \
    "s_waitcnt lgkmcnt(6)\n\t" \
    "v_smfmac_f32_16x16x64_bf16 v[0:3], v[232:235], v[172:179], v10\n\t" \
    "ds_read_b128 v[228:231], v8 offset:0\n\t" \
    "v_smfmac_f32_16x16x64_bf16 v[4:7], v[232:235], v[180:187], v10\n\t" \
    "ds_read_b128 v[232:235], v8 offset:12288\n\t" \
    "s_waitcnt lgkmcnt(5)\n\t" \
    "v_smfmac_f32_16x16x64_bf16 v[0:3], v[220:223], v[60:67], v10\n\t" \
    "v_smfmac_f32_16x16x64_bf16 v[4:7], v[220:223], v[68:75], v10\n\t" \
    "v_xor_b32 v8, 1024, v8\n\t" \
    "ds_read_b128 v[220:223], v8 offset:4096\n\t" \
    "s_waitcnt lgkmcnt(4)\n\t" \
    "v_smfmac_f32_16x16x64_bf16 v[0:3], v[224:227], v[124:131], v10\n\t" \
    "v_smfmac_f32_16x16x64_bf16 v[4:7], v[224:227], v[132:139], v10\n\t" \
    "ds_read_b128 v[224:227], v8 offset:8192\n\t" \
    "s_waitcnt lgkmcnt(3)\n\t" \
    "v_smfmac_f32_16x16x64_bf16 v[0:3], v[228:231], v[236:243], v10\n\t" \
    "v_xor_b32 v9, 16384, v9\n\t" \
    "ds_read_b128 v[236:239], v9 offset:0\n\t" \
    "v_smfmac_f32_16x16x64_bf16 v[4:7], v[228:231], v[244:251], v10\n\t" \
    "ds_read_b128 v[240:243], v9 offset:1024\n\t" \
    "s_waitcnt lgkmcnt(4)\n\t" \
    "v_smfmac_f32_16x16x64_bf16 v[0:3], v[232:235], v[188:195], v10\n\t" \
    "ds_read_b128 v[228:231], v8 offset:12288\n\t" \
    "v_smfmac_f32_16x16x64_bf16 v[4:7], v[232:235], v[196:203], v10\n\t" \
    "ds_read_b128 v[244:247], v9 offset:2048\n\t" \
    "s_waitcnt lgkmcnt(5)\n\t" \
    "v_smfmac_f32_16x16x64_bf16 v[0:3], v[220:223], v[76:83], v10\n\t" \
    "ds_read_b128 v[232:235], v8 offset:0\n\t" \
    "v_xor_b32 v8, 3072, v8\n\t" \
    "v_smfmac_f32_16x16x64_bf16 v[4:7], v[220:223], v[84:91], v10\n\t" \
    "ds_read_b128 v[248:251], v9 offset:3072\n\t" \
    "s_waitcnt lgkmcnt(6)\n\t" \
    "v_smfmac_f32_16x16x64_bf16 v[0:3], v[224:227], v[140:147], v10\n\t" \
    "ds_read_b128 v[220:223], v12 offset:24576\n\t" \
    "v_smfmac_f32_16x16x64_bf16 v[4:7], v[224:227], v[148:155], v10\n\t" \
    "ds_read_b128 v[224:227], v12 offset:28672\n\t" \
    "s_waitcnt lgkmcnt(5)\n\t" \
    "v_smfmac_f32_16x16x64_bf16 v[0:3], v[228:231], v[204:211], v10\n\t" \
    "v_smfmac_f32_16x16x64_bf16 v[4:7], v[228:231], v[212:219], v10\n\t" \
    "ds_read_b128 v[228:231], v12 offset:25600\n\t" \
    "s_waitcnt lgkmcnt(3)\n\t" \
    "v_smfmac_f32_16x16x64_bf16 v[0:3], v[232:235], v[236:243], v10\n\t" \
    "v_xor_b32 v9, 49152, v9\n\t" \
    "v_smfmac_f32_16x16x64_bf16 v[4:7], v[232:235], v[244:251], v10\n\t" \
    VLD(4) \
    "s_nop 5\n\t" \
    "v_add_f32 v0, v0, v1\n\t" \
    "v_add_f32 v2, v2, v3\n\t" \
    "v_add_f32 v4, v4, v5\n\t" \
    "v_add_f32 v6, v6, v7\n\t" \
    "v_mul_f32 v0, %[sl], v0\n\t" \
    "v_mul_f32 v2, %[sl], v2\n\t" \
    "v_mul_f32 v4, %[sl], v4\n\t" \
    "v_mul_f32 v6, %[sl], v6\n\t" \
    "v_cvt_pk_bf16_f32 v0, v0, v4\n\t" \
    "v_cvt_pk_bf16_f32 v2, v2, v6\n\t" \
    "ds_write2_b32 v11, v0, v2 offset0:0 offset1:4\n\t" \
    "s_mov_b64 exec, %[onej]\n\t" \
    "ds_write_b32 v16, v253\n\t" \
    "s_mov_b64 exec, -1\n\t" \
    GSA(1) \
    "s_setprio " STR(QPRIO) "\n\t" \
    "ds_read_b128 v[232:235], v12 offset:29696\n\t" \
    "s_nop 5\n\t" \
    "ds_read_b128 v[0:3], v12 offset:26624\n\t" \
    "s_nop 5\n\t" \
    "ds_read_b128 v[4:7], v12 offset:27648\n\t" \
    "s_nop 5\n\t" \
    "ds_read_b32 v252, v16\n\t" \
    "ds_read_b128 v[236:239], v14 offset:0\n\t" \
    "s_nop 5\n\t" \
    "ds_read_b128 v[240:243], v14 offset:512\n\t" \
    "s_nop 5\n\t" \
    "ds_read_b128 v[244:247], v14 offset:1024\n\t" \
    "s_nop 5\n\t" \
    VLD(3) \
    "ds_read_b128 v[248:251], v14 offset:1536\n\t" \
    "s_nop 5\n\t" \
    "s_setprio 0\n\t" \
    "s_waitcnt lgkmcnt(4)\n\t" \
    "v_cmp_ne_u32 vcc, v253, v252\n\t" \
    "s_cbranch_vccnz RP_%=\n\t" \
    "CP_%=:\n\t" \
    VLD(0) \
    "s_waitcnt lgkmcnt(2)\n\t" \
    "v_mfma_f32_16x16x32_bf16 v[220:223], v[220:223], v[236:239], 0\n\t" \
    "v_mfma_f32_16x16x32_bf16 v[224:227], v[224:227], v[236:239], 0\n\t" \
    "v_mfma_f32_16x16x32_bf16 v[220:223], v[228:231], v[240:243], v[220:223]\n\t" \
    "ds_read_b128 v[228:231], v12 offset:30720\n\t" \
    "v_mfma_f32_16x16x32_bf16 v[224:227], v[232:235], v[240:243], v[224:227]\n\t" \
    "ds_read_b128 v[232:235], v12 offset:31744\n\t" \
    "s_setprio " STR(QPRIO) "\n\t" \
    VLD(1) \
    "s_waitcnt lgkmcnt(2)\n\t" \
    "v_mfma_f32_16x16x32_bf16 v[220:223], v[0:3], v[244:247], v[220:223]\n\t" \
    "v_mfma_f32_16x16x32_bf16 v[220:223], v[4:7], v[248:251], v[220:223]\n\t" \
    "s_waitcnt lgkmcnt(1)\n\t" \
    "v_mfma_f32_16x16x32_bf16 v[224:227], v[228:231], v[244:247], v[224:227]\n\t" \
    "s_waitcnt lgkmcnt(0)\n\t" \
    "v_mfma_f32_16x16x32_bf16 v[224:227], v[232:235], v[248:251], v[224:227]\n\t" \
    GSA(2) \
    "s_setprio 2\n\t" \
    "ds_read_b32 v252, v16 offset:16\n\t" \
    "ds_read_b128 v[0:3], v15 offset:0\n\t" \
    "ds_read_b128 v[4:7], v15 offset:512\n\t" \
    "ds_read_b128 v[228:231], v15 offset:1024\n\t" \
    "ds_read_b128 v[232:235], v15 offset:1536\n\t" \
    VLD(2) \
    "v_exp_f32 v220, v220\n\t" \
    "v_exp_f32 v221, v221\n\t" \
    "v_exp_f32 v222, v222\n\t" \
    "v_exp_f32 v223, v223\n\t" \
    "v_exp_f32 v224, v224\n\t" \
    "v_exp_f32 v225, v225\n\t" \
    "v_exp_f32 v226, v226\n\t" \
    "v_exp_f32 v227, v227\n\t" \
    "v_cvt_pk_bf16_f32 v220, v220, v221\n\t" \
    "v_cvt_pk_bf16_f32 v221, v222, v223\n\t" \
    "s_waitcnt lgkmcnt(4)\n\t" \
    "v_cmp_ne_u32 vcc, %[tag], v252\n\t" \
    "s_cbranch_vccnz RK_%=\n\t" \
    "CK_%=:\n\t" \
    "s_waitcnt lgkmcnt(3)\n\t" \
    "v_mfma_f32_16x16x32_bf16 v[0:3], v[0:3], v[236:239], 0\n\t" \
    "v_cvt_pk_bf16_f32 v222, v224, v225\n\t" \
    "v_cvt_pk_bf16_f32 v223, v226, v227\n\t" \
    "s_waitcnt lgkmcnt(2)\n\t" \
    "v_mfma_f32_16x16x32_bf16 v[0:3], v[4:7], v[240:243], v[0:3]\n\t" \
    "v_mov_b32 v224, %[one]\n\t" \
    "v_mov_b32 v225, %[one]\n\t" \
    "s_waitcnt lgkmcnt(1)\n\t" \
    "v_mfma_f32_16x16x32_bf16 v[0:3], v[228:231], v[244:247], v[0:3]\n\t" \
    "v_mov_b32 v226, %[one]\n\t" \
    "v_mov_b32 v227, %[one]\n\t" \
    "s_waitcnt lgkmcnt(0)\n\t" \
    "v_mfma_f32_16x16x32_bf16 v[0:3], v[232:235], v[248:251], v[0:3]\n\t" \
    "v_mfma_f32_16x16x32_bf16 v[244:247], v[224:227], v[220:223], 0\n\t" \
    "v_mov_b32 v229, 0\n\t" \
    "v_mfma_f32_16x16x32_bf16 v[236:239], v[20:23], v[220:223], 0\n\t" \
    "v_mov_b32 v230, 0\n\t" \
    "v_mfma_f32_16x16x32_bf16 v[240:243], v[24:27], v[220:223], 0\n\t" \
    "s_nop 2\n\t" \
    "v_exp_f32 v0, v0\n\t" \
    "v_exp_f32 v1, v1\n\t" \
    "s_nop 0\n\t" \
    "v_cvt_pk_bf16_f32 v228, v0, v1\n\t" \
    "v_cvt_pk_bf16_f32 v231, v0, v1\n\t" \
    GSA(3) \
    "CM_%=:\n\t" \
    "s_nop 0\n\t" \
    "v_mfma_f32_16x16x16_bf16 v[244:247], v[224:225], v[228:229], v[244:247]\n\t" \
    "s_waitcnt vmcnt(0)\n\t" \
    "v_mfma_f32_16x16x16_bf16 v[236:239], v[254:255], v[228:229], v[236:239]\n\t" \
    "v_max_u32 v7, v254, v255\n\t" \
    "v_mfma_f32_16x16x16_bf16 v[240:243], v[254:255], v[230:231], v[240:243]\n\t" \
    "v_cmp_eq_u32 s[88:89], -1, v7\n\t" \
    "s_not_b64 exec, %[hi]\n\t" \
    "s_nop 1\n\t" \
    "v_rcp_f32 v2, v244\n\t" \
    "v_subrev_u32 v253, 0x21800000, v244\n\t" \
    "v_cmp_gt_u32 vcc, v253, %[rg]\n\t" \
    "v_pk_mul_f32 v[236:237], v[236:237], v[2:3] op_sel_hi:[1,0]\n\t" \
    "v_pk_mul_f32 v[238:239], v[238:239], v[2:3] op_sel_hi:[1,0]\n\t" \
    "v_pk_mul_f32 v[240:241], v[240:241], v[2:3] op_sel_hi:[1,0]\n\t" \
    "v_pk_mul_f32 v[242:243], v[242:243], v[2:3] op_sel_hi:[1,0]\n\t" \
    "s_or_b64 vcc, vcc, s[88:89]\n\t" \
    "v_cvt_pk_bf16_f32 v4, v236, v237\n\t" \
    "v_cvt_pk_bf16_f32 v5, v238, v239\n\t" \
    "v_cvt_pk_bf16_f32 v6, v240, v241\n\t" \
    "v_cvt_pk_bf16_f32 v7, v242, v243\n\t" \
    "s_cbranch_scc1 FX_%=\n\t" \
    "buffer_store_dwordx4 v[4:7], v18, %[rs], %[sb] offen sc1\n\t" \
    "s_memrealtime %[tp]\n\t" \
    DGA \
    GSA(4) \
    "v_max3_u32 v252, v4, v5, v6\n\t" \
    "v_max_u32 v252, v252, v7\n\t" \
    "v_cmp_eq_u32 vcc, -1, v252\n\t" \
    "s_cbranch_vccnz RC_%=\n\t" \
    "CC_%=:\n\t" \
    "s_mov_b64 exec, -1\n\t" \
    "v_add_u32 v252, %[vd], v12\n\t" \
    "v_add_u32 v253, %[md], v17\n\t" \
    "ds_write_b128 v252, v[20:23]\n\t" \
    "ds_write_b128 v252, v[24:27] offset:1024\n\t" \
    "ds_write2st64_b32 v253, v254, v255 offset0:0 offset1:1\n\t" \
    "s_waitcnt lgkmcnt(0)\n\t" \
    "s_cmp_lg_u32 %[sc], 3\n\t" \
    "s_cbranch_scc1 NH_%=\n\t" \
    "buffer_load_dwordx2 v[0:1], off, %[rs], %[fl] sc1\n\t" \
    "s_barrier\n\t" \
    "s_waitcnt vmcnt(0)\n\t" \
    "v_readfirstlane_b32 %[pt], v0\n\t" \
    "v_readfirstlane_b32 %[pd], v1\n\t" \
    "s_nop 3\n\t" \
    "s_branch E_%=\n\t" \
    "NH_%=:\n\t" \
    "s_barrier\n\t" \
    "s_branch E_%=\n\t" \
    "RP_%=:\n\t" \
    "s_waitcnt lgkmcnt(0)\n\t" \
    "ds_read_b32 v252, v16\n\t" \
    "ds_read_b128 v[236:239], v14 offset:0\n\t" \
    "ds_read_b128 v[240:243], v14 offset:512\n\t" \
    "ds_read_b128 v[244:247], v14 offset:1024\n\t" \
    "ds_read_b128 v[248:251], v14 offset:1536\n\t" \
    "s_sub_u32 %[lim], %[lim], " STR(DEC1) "\n\t" \
    "s_cmp_gt_i32 %[lim], 0\n\t" \
    "s_waitcnt lgkmcnt(4)\n\t" \
    "s_cbranch_scc0 CP_%=\n\t" \
    "v_cmp_ne_u32 vcc, %[tag], v252\n\t" \
    "s_cbranch_vccnz RP_%=\n\t" \
    "s_branch CP_%=\n\t" \
    "RK_%=:\n\t" \
    "s_waitcnt lgkmcnt(0)\n\t" \
    "ds_read_b32 v252, v16 offset:16\n\t" \
    "ds_read_b128 v[0:3], v15 offset:0\n\t" \
    "ds_read_b128 v[4:7], v15 offset:512\n\t" \
    "ds_read_b128 v[228:231], v15 offset:1024\n\t" \
    "ds_read_b128 v[232:235], v15 offset:1536\n\t" \
    "s_sub_u32 %[lim], %[lim], " STR(DEC1) "\n\t" \
    "s_cmp_gt_i32 %[lim], 0\n\t" \
    "s_waitcnt lgkmcnt(4)\n\t" \
    "s_cbranch_scc0 CK_%=\n\t" \
    "v_cmp_ne_u32 vcc, %[tag], v252\n\t" \
    "s_cbranch_vccnz RK_%=\n\t" \
    "s_branch CK_%=\n\t" \
    "RC_%=:\n\t" \
    "v_min_u32 v4, -2, v4\n\t" \
    "v_min_u32 v5, -2, v5\n\t" \
    "v_min_u32 v6, -2, v6\n\t" \
    "v_min_u32 v7, -2, v7\n\t" \
    "buffer_store_dwordx4 v[4:7], v18, %[rs], %[sb] offen sc1\n\t" \
    "s_branch CC_%=\n\t" \
    "FX_%=:\n\t" \
    "s_mov_b64 exec, -1\n\t" \
    "s_cmp_eq_u64 s[88:89], 0\n\t" \
    "s_cbranch_scc1 RM_%=\n\t" \
    "buffer_load_dword v254, v17, %[rs], %[sb] offen sc1\n\t" \
    "buffer_load_dword v255, v17, %[rs], %[sb] offen offset:256 sc1\n\t" \
    "v_mfma_f32_16x16x32_bf16 v[244:247], v[224:227], v[220:223], 0\n\t" \
    "v_mfma_f32_16x16x32_bf16 v[236:239], v[20:23], v[220:223], 0\n\t" \
    "v_mfma_f32_16x16x32_bf16 v[240:243], v[24:27], v[220:223], 0\n\t" \
    "s_sub_u32 %[lim], %[lim], " STR(DEC2) "\n\t" \
    "s_cmp_gt_i32 %[lim], 0\n\t" \
    "s_cbranch_scc1 CM_%=\n\t" \
    "s_waitcnt vmcnt(0)\n\t" \
    "v_mov_b32 v254, 0\n\t" \
    "v_mov_b32 v255, 0\n\t" \
    "s_branch CM_%=\n\t" \
    "RM_%=:\n\t" \
    "s_mov_b32 %[rg], -1\n\t" \
    "s_nop 7\n\t" \
    "ds_read_b128 v[236:239], v14 offset:0\n\t" \
    "ds_read_b128 v[240:243], v14 offset:512\n\t" \
    "ds_read_b128 v[244:247], v14 offset:1024\n\t" \
    "ds_read_b128 v[248:251], v14 offset:1536\n\t" \
    "ds_read_b128 v[232:235], v12 offset:24576\n\t" \
    "s_waitcnt lgkmcnt(0)\n\t" \
    "v_mfma_f32_16x16x32_bf16 v[220:223], v[232:235], v[236:239], 0\n\t" \
    "ds_read_b128 v[232:235], v12 offset:25600\n\t" \
    "s_waitcnt lgkmcnt(0)\n\t" \
    "v_mfma_f32_16x16x32_bf16 v[220:223], v[232:235], v[240:243], v[220:223]\n\t" \
    "ds_read_b128 v[232:235], v12 offset:26624\n\t" \
    "s_waitcnt lgkmcnt(0)\n\t" \
    "v_mfma_f32_16x16x32_bf16 v[220:223], v[232:235], v[244:247], v[220:223]\n\t" \
    "ds_read_b128 v[232:235], v12 offset:27648\n\t" \
    "s_waitcnt lgkmcnt(0)\n\t" \
    "v_mfma_f32_16x16x32_bf16 v[220:223], v[232:235], v[248:251], v[220:223]\n\t" \
    "ds_read_b128 v[232:235], v12 offset:28672\n\t" \
    "s_waitcnt lgkmcnt(0)\n\t" \
    "v_mfma_f32_16x16x32_bf16 v[224:227], v[232:235], v[236:239], 0\n\t" \
    "ds_read_b128 v[232:235], v12 offset:29696\n\t" \
    "s_waitcnt lgkmcnt(0)\n\t" \
    "v_mfma_f32_16x16x32_bf16 v[224:227], v[232:235], v[240:243], v[224:227]\n\t" \
    "ds_read_b128 v[232:235], v12 offset:30720\n\t" \
    "s_waitcnt lgkmcnt(0)\n\t" \
    "v_mfma_f32_16x16x32_bf16 v[224:227], v[232:235], v[244:247], v[224:227]\n\t" \
    "ds_read_b128 v[232:235], v12 offset:31744\n\t" \
    "s_waitcnt lgkmcnt(0)\n\t" \
    "v_mfma_f32_16x16x32_bf16 v[224:227], v[232:235], v[248:251], v[224:227]\n\t" \
    "ds_read_b128 v[232:235], v15 offset:0\n\t" \
    "s_waitcnt lgkmcnt(0)\n\t" \
    "v_mfma_f32_16x16x32_bf16 v[0:3], v[232:235], v[236:239], 0\n\t" \
    "ds_read_b128 v[232:235], v15 offset:512\n\t" \
    "s_waitcnt lgkmcnt(0)\n\t" \
    "v_mfma_f32_16x16x32_bf16 v[0:3], v[232:235], v[240:243], v[0:3]\n\t" \
    "ds_read_b128 v[232:235], v15 offset:1024\n\t" \
    "s_waitcnt lgkmcnt(0)\n\t" \
    "v_mfma_f32_16x16x32_bf16 v[0:3], v[232:235], v[244:247], v[0:3]\n\t" \
    "ds_read_b128 v[232:235], v15 offset:1536\n\t" \
    "s_waitcnt lgkmcnt(0)\n\t" \
    "v_mfma_f32_16x16x32_bf16 v[0:3], v[232:235], v[248:251], v[0:3]\n\t" \
    "v_max3_f32 v252, v220, v221, v222\n\t" \
    "v_max3_f32 v253, v223, v224, v225\n\t" \
    "v_max3_f32 v252, v252, v226, v227\n\t" \
    "s_nop 4\n\t" \
    "v_max3_f32 v253, v253, v0, v1\n\t" \
    "v_max_f32 v252, v252, v253\n\t" \
    "v_mov_b32 v253, v252\n\t" \
    "v_mov_b32 v2, v252\n\t" \
    "s_nop 1\n\t" \
    "v_permlane16_swap_b32 v253, v2\n\t" \
    "v_max_f32 v252, v253, v2\n\t" \
    "v_max_f32 v253, v253, v2\n\t" \
    "s_nop 1\n\t" \
    "v_permlane32_swap_b32 v252, v253\n\t" \
    "v_max_f32 v252, v252, v253\n\t" \
    "v_sub_f32 v220, v220, v252\n\t" \
    "v_sub_f32 v221, v221, v252\n\t" \
    "v_sub_f32 v222, v222, v252\n\t" \
    "v_sub_f32 v223, v223, v252\n\t" \
    "v_sub_f32 v224, v224, v252\n\t" \
    "v_sub_f32 v225, v225, v252\n\t" \
    "v_sub_f32 v226, v226, v252\n\t" \
    "v_sub_f32 v227, v227, v252\n\t" \
    "v_sub_f32 v0, v0, v252\n\t" \
    "v_sub_f32 v1, v1, v252\n\t" \
    "v_exp_f32 v220, v220\n\t" \
    "v_exp_f32 v221, v221\n\t" \
    "v_exp_f32 v222, v222\n\t" \
    "v_exp_f32 v223, v223\n\t" \
    "v_exp_f32 v224, v224\n\t" \
    "v_exp_f32 v225, v225\n\t" \
    "v_exp_f32 v226, v226\n\t" \
    "v_exp_f32 v227, v227\n\t" \
    "v_cvt_pk_bf16_f32 v220, v220, v221\n\t" \
    "v_cvt_pk_bf16_f32 v221, v222, v223\n\t" \
    "v_cvt_pk_bf16_f32 v222, v224, v225\n\t" \
    "v_cvt_pk_bf16_f32 v223, v226, v227\n\t" \
    "v_mov_b32 v224, %[one]\n\t" \
    "v_mov_b32 v225, %[one]\n\t" \
    "v_mov_b32 v226, %[one]\n\t" \
    "v_mov_b32 v227, %[one]\n\t" \
    "s_nop 1\n\t" \
    "v_mfma_f32_16x16x32_bf16 v[244:247], v[224:227], v[220:223], 0\n\t" \
    "v_mfma_f32_16x16x32_bf16 v[236:239], v[20:23], v[220:223], 0\n\t" \
    "v_mfma_f32_16x16x32_bf16 v[240:243], v[24:27], v[220:223], 0\n\t" \
    "v_exp_f32 v0, v0\n\t" \
    "v_exp_f32 v1, v1\n\t" \
    "s_nop 0\n\t" \
    "v_cvt_pk_bf16_f32 v228, v0, v1\n\t" \
    "v_cvt_pk_bf16_f32 v231, v0, v1\n\t" \
    "s_nop 1\n\t" \
    "s_branch CM_%=\n\t" \
    "E_%=:\n\t" \
    GSW \
    "s_nop 1\n\t" \
    : [lim] "+s"(lim), [rg] "+s"(rg), [tp] "=&s"(tp), [pt] "=&s"(pt), [pd] "=&s"(pd) DGO DGP GSO(0) GSO(1) GSO(2) GSO(3) GSO(4) \
    : "{v8}"(am_a), "{v9}"(t0_a), "{v10}"(idx), "{v11}"(iw_a), "{v12}"(ab_a), "{v14}"(qr_a), "{v15}"(kr_a), "{v16}"(fr_a), "{v17}"(vo_v), "{v18}"(oo_v), [rs] "s"(rs), [sb] "s"(sb), [tag] "s"(0xffffff81u + s), [hi] "s"(0xff00ff00ff00ff00ull), [tgt] "s"(tgt), [sc] "s"(wid), [onej] "s"(onej), [sl] "s"(0x3e0293ee), [one] "s"(0x3f803f80), [m0] "s"(sbm), [m1] "s"(sbm + 4096u), [vx] "s"(vxo), [fl] "s"(fb + FL_T(s + 1)), [vd] "s"(8192 + j * 2048), [md] "s"(md), "{v[28:35]}"(wr[0][0]), "{v[36:43]}"(wr[0][1]), "{v[44:51]}"(wr[1][0]), "{v[52:59]}"(wr[1][1]), "{v[60:67]}"(wr[2][0]), "{v[68:75]}"(wr[2][1]), "{v[76:83]}"(wr[3][0]), "{v[84:91]}"(wr[3][1]), "{v[92:99]}"(wr[4][0]), "{v[100:107]}"(wr[4][1]), "{v[108:115]}"(wr[5][0]), "{v[116:123]}"(wr[5][1]), "{v[124:131]}"(wr[6][0]), "{v[132:139]}"(wr[6][1]), "{v[140:147]}"(wr[7][0]), "{v[148:155]}"(wr[7][1]), "{v[156:163]}"(wr[8][0]), "{v[164:171]}"(wr[8][1]), "{v[172:179]}"(wr[9][0]), "{v[180:187]}"(wr[9][1]), "{v[188:195]}"(wr[10][0]), "{v[196:203]}"(wr[10][1]), "{v[204:211]}"(wr[11][0]), "{v[212:219]}"(wr[11][1]) \
    : "memory", "vcc", "scc", "s88", "s89", "s90", "s91", "s92", "s93", "v0", "v1", "v2", "v3", "v4", "v5", "v6", "v7", "v20", "v21", "v22", "v23", "v24", "v25", "v26", "v27", "v220", "v221", "v222", "v223", "v224", "v225", "v226", "v227", "v228", "v229", "v230", "v231", "v232", "v233", "v234", "v235", "v236", "v237", "v238", "v239", "v240", "v241", "v242", "v243", "v244", "v245", "v246", "v247", "v248", "v249", "v250", "v251", "v252", "v253", "v254", "v255")

#define A_MAIN_K() \
  asm volatile( \
    "ds_read_b128 v[236:239], v9 offset:0\n\t" \
    "ds_read_b128 v[240:243], v9 offset:1024\n\t" \
    "ds_read_b128 v[244:247], v9 offset:2048\n\t" \
    "ds_read_b128 v[248:251], v9 offset:3072\n\t" \
    "v_mov_b32 v0, 0\n\t" \
    "v_mov_b32 v1, 0\n\t" \
    "v_mov_b32 v2, 0\n\t" \
    "v_mov_b32 v3, 0\n\t" \
    "v_mov_b32 v4, 0\n\t" \
    "v_mov_b32 v5, 0\n\t" \
    "v_mov_b32 v6, 0\n\t" \
    "v_mov_b32 v7, 0\n\t" \
    "s_waitcnt lgkmcnt(0)\n\t" \
    "s_barrier\n\t" \
    "buffer_load_dwordx4 v[220:223], v8, %[rs], %[m0] offen sc1\n\t" \
    "buffer_load_dwordx4 v[224:227], v8, %[rs], %[m1] offen sc1\n\t" \
    GSA(3) \
    "v_mov_b32 v252, v252\n\t" \
    "s_nop 2\n\t" \
    "v_mov_b32 v252, v252\n\t" \
    "s_nop 2\n\t" \
    "v_mov_b32 v252, v252\n\t" \
    "s_nop 2\n\t" \
    "v_mov_b32 v252, v252\n\t" \
    "s_nop 2\n\t" \
    "v_mov_b32 v252, v252\n\t" \
    "s_nop 2\n\t" \
    "v_mov_b32 v252, v252\n\t" \
    "s_nop 2\n\t" \
    "v_mov_b32 v252, v252\n\t" \
    "s_nop 2\n\t" \
    "v_mov_b32 v252, v252\n\t" \
    "s_nop 2\n\t" \
    "v_mov_b32 v252, v252\n\t" \
    "s_nop 2\n\t" \
    "v_mov_b32 v252, v252\n\t" \
    "s_nop 2\n\t" \
    "v_mov_b32 v252, v252\n\t" \
    "s_nop 2\n\t" \
    "v_mov_b32 v252, v252\n\t" \
    "s_nop 2\n\t" \
    "v_mov_b32 v252, v252\n\t" \
    "s_nop 2\n\t" \
    "v_mov_b32 v252, v252\n\t" \
    "s_nop 2\n\t" \
    "v_mov_b32 v252, v252\n\t" \
    "s_nop 2\n\t" \
    "v_mov_b32 v252, v252\n\t" \
    "s_nop 2\n\t" \
    "v_mov_b32 v252, v252\n\t" \
    "s_nop 2\n\t" \
    "v_mov_b32 v252, v252\n\t" \
    "s_nop 2\n\t" \
    "v_mov_b32 v252, v252\n\t" \
    "s_nop 2\n\t" \
    "v_mov_b32 v252, v252\n\t" \
    "s_nop 2\n\t" \
    "v_mov_b32 v252, v252\n\t" \
    "s_nop 2\n\t" \
    "v_mov_b32 v252, v252\n\t" \
    "s_nop 2\n\t" \
    "v_mov_b32 v252, v252\n\t" \
    "s_nop 2\n\t" \
    "v_mov_b32 v252, v252\n\t" \
    "s_nop 2\n\t" \
    "v_mov_b32 v252, v252\n\t" \
    "s_nop 2\n\t" \
    "v_mov_b32 v252, v252\n\t" \
    "s_nop 2\n\t" \
    "v_mov_b32 v252, v252\n\t" \
    "s_nop 2\n\t" \
    "v_mov_b32 v252, v252\n\t" \
    "s_nop 2\n\t" \
    "v_mov_b32 v252, v252\n\t" \
    "s_nop 2\n\t" \
    "v_mov_b32 v252, v252\n\t" \
    "s_nop 2\n\t" \
    "PM_%=:\n\t" \
    "s_waitcnt vmcnt(1)\n\t" \
    "ds_write_b128 v8, v[220:223] offset:8192\n\t" \
    "v_max3_u32 v252, v220, v221, v222\n\t" \
    "s_waitcnt vmcnt(0)\n\t" \
    "ds_write_b128 v8, v[224:227] offset:12288\n\t" \
    "v_max3_u32 v252, v252, v223, v224\n\t" \
    "v_max3_u32 v252, v252, v225, v226\n\t" \
    "v_max_u32 v252, v252, v227\n\t" \
    "v_cmp_eq_u32 vcc, -1, v252\n\t" \
    "s_cbranch_vccz PD_%=\n\t" \
    "s_sub_u32 %[lim], %[lim], 1\n\t" \
    "s_cmp_gt_i32 %[lim], 0\n\t" \
    "s_cbranch_scc0 PD_%=\n\t" \
    "s_sleep " STR(CADM) "\n\t" \
    "buffer_load_dwordx4 v[220:223], v8, %[rs], %[m0] offen sc1\n\t" \
    "buffer_load_dwordx4 v[224:227], v8, %[rs], %[m1] offen sc1\n\t" \
    "s_branch PM_%=\n\t" \
    "PD_%=:\n\t" \
    GSA(0) \
    "v_smfmac_f32_16x16x64_bf16 v[0:3], v[220:223], v[236:243], v10\n\t" \
    "v_xor_b32 v9, 16384, v9\n\t" \
    "ds_read_b128 v[236:239], v9 offset:0\n\t" \
    "ds_read_b128 v[240:243], v9 offset:1024\n\t" \
    "v_smfmac_f32_16x16x64_bf16 v[4:7], v[220:223], v[244:251], v10\n\t" \
    "ds_read_b128 v[244:247], v9 offset:2048\n\t" \
    "ds_read_b128 v[248:251], v9 offset:3072\n\t" \
    "s_waitcnt lgkmcnt(4)\n\t" \
    "s_barrier\n\t" \
    DGA \
    "ds_read_b128 v[228:231], v8 offset:0\n\t" \
    "ds_read_b128 v[232:235], v8 offset:4096\n\t" \
    "v_smfmac_f32_16x16x64_bf16 v[0:3], v[224:227], v[148:155], v10\n\t" \
    "v_xor_b32 v8, 1024, v8\n\t" \
    "ds_read_b128 v[220:223], v8 offset:8192\n\t" \
    "v_smfmac_f32_16x16x64_bf16 v[4:7], v[224:227], v[156:163], v10\n\t" \
    "ds_read_b128 v[224:227], v8 offset:12288\n\t" \
    "buffer_store_dwordx4 v[212:215], v14, %[rs], %[ox] offen offset:0 sc1\n\t" \
    "buffer_store_dwordx4 v[216:219], v14, %[rs], %[ox] offen offset:512 sc1\n\t" \
    "s_waitcnt lgkmcnt(3)\n\t" \
    "v_smfmac_f32_16x16x64_bf16 v[0:3], v[228:231], v[20:27], v10\n\t" \
    "v_smfmac_f32_16x16x64_bf16 v[4:7], v[228:231], v[28:35], v10\n\t" \
    "ds_read_b128 v[228:231], v8 offset:0\n\t" \
    "s_waitcnt lgkmcnt(3)\n\t" \
    "v_smfmac_f32_16x16x64_bf16 v[0:3], v[232:235], v[84:91], v10\n\t" \
    "v_smfmac_f32_16x16x64_bf16 v[4:7], v[232:235], v[92:99], v10\n\t" \
    "ds_read_b128 v[232:235], v8 offset:4096\n\t" \
    "s_waitcnt lgkmcnt(3)\n\t" \
    "v_smfmac_f32_16x16x64_bf16 v[0:3], v[220:223], v[236:243], v10\n\t" \
    "v_xor_b32 v9, 49152, v9\n\t" \
    "ds_read_b128 v[236:239], v9 offset:0\n\t" \
    "v_smfmac_f32_16x16x64_bf16 v[4:7], v[220:223], v[244:251], v10\n\t" \
    "ds_read_b128 v[240:243], v9 offset:1024\n\t" \
    "s_waitcnt lgkmcnt(4)\n\t" \
    "v_smfmac_f32_16x16x64_bf16 v[0:3], v[224:227], v[164:171], v10\n\t" \
    "v_xor_b32 v8, 3072, v8\n\t" \
    "ds_read_b128 v[220:223], v8 offset:12288\n\t" \
    "v_smfmac_f32_16x16x64_bf16 v[4:7], v[224:227], v[172:179], v10\n\t" \
    "ds_read_b128 v[244:247], v9 offset:2048\n\t" \
    "s_waitcnt lgkmcnt(5)\n\t" \
    "v_smfmac_f32_16x16x64_bf16 v[0:3], v[228:231], v[36:43], v10\n\t" \
    "ds_read_b128 v[224:227], v8 offset:0\n\t" \
    "v_smfmac_f32_16x16x64_bf16 v[4:7], v[228:231], v[44:51], v10\n\t" \
    "ds_read_b128 v[248:251], v9 offset:3072\n\t" \
    "s_waitcnt lgkmcnt(6)\n\t" \
    "v_smfmac_f32_16x16x64_bf16 v[0:3], v[232:235], v[100:107], v10\n\t" \
    "ds_read_b128 v[228:231], v8 offset:8192\n\t" \
    "v_smfmac_f32_16x16x64_bf16 v[4:7], v[232:235], v[108:115], v10\n\t" \
    "ds_read_b128 v[232:235], v8 offset:4096\n\t" \
    "s_waitcnt lgkmcnt(5)\n\t" \
    "v_smfmac_f32_16x16x64_bf16 v[0:3], v[220:223], v[180:187], v10\n\t" \
    "v_smfmac_f32_16x16x64_bf16 v[4:7], v[220:223], v[188:195], v10\n\t" \
    "v_xor_b32 v8, 1024, v8\n\t" \
    "ds_read_b128 v[220:223], v8 offset:12288\n\t" \
    "s_waitcnt lgkmcnt(4)\n\t" \
    "v_smfmac_f32_16x16x64_bf16 v[0:3], v[224:227], v[52:59], v10\n\t" \
    "v_smfmac_f32_16x16x64_bf16 v[4:7], v[224:227], v[60:67], v10\n\t" \
    "ds_read_b128 v[224:227], v8 offset:0\n\t" \
    "s_waitcnt lgkmcnt(3)\n\t" \
    "v_smfmac_f32_16x16x64_bf16 v[0:3], v[228:231], v[236:243], v10\n\t" \
    "v_xor_b32 v9, 16384, v9\n\t" \
    "ds_read_b128 v[236:239], v9 offset:0\n\t" \
    "v_smfmac_f32_16x16x64_bf16 v[4:7], v[228:231], v[244:251], v10\n\t" \
    "ds_read_b128 v[240:243], v9 offset:1024\n\t" \
    "s_waitcnt lgkmcnt(4)\n\t" \
    "v_smfmac_f32_16x16x64_bf16 v[0:3], v[232:235], v[116:123], v10\n\t" \
    "ds_read_b128 v[228:231], v8 offset:4096\n\t" \
    "v_smfmac_f32_16x16x64_bf16 v[4:7], v[232:235], v[124:131], v10\n\t" \
    "ds_read_b128 v[244:247], v9 offset:2048\n\t" \
    "s_waitcnt lgkmcnt(5)\n\t" \
    "v_smfmac_f32_16x16x64_bf16 v[0:3], v[220:223], v[196:203], v10\n\t" \
    "ds_read_b128 v[232:235], v8 offset:8192\n\t" \
    "v_xor_b32 v8, 3072, v8\n\t" \
    "v_smfmac_f32_16x16x64_bf16 v[4:7], v[220:223], v[204:211], v10\n\t" \
    "ds_read_b128 v[248:251], v9 offset:3072\n\t" \
    "s_waitcnt lgkmcnt(6)\n\t" \
    "v_smfmac_f32_16x16x64_bf16 v[0:3], v[224:227], v[68:75], v10\n\t" \
    "v_smfmac_f32_16x16x64_bf16 v[4:7], v[224:227], v[76:83], v10\n\t" \
    "s_waitcnt lgkmcnt(3)\n\t" \
    "v_smfmac_f32_16x16x64_bf16 v[0:3], v[228:231], v[132:139], v10\n\t" \
    "v_smfmac_f32_16x16x64_bf16 v[4:7], v[228:231], v[140:147], v10\n\t" \
    "s_waitcnt lgkmcnt(0)\n\t" \
    "v_smfmac_f32_16x16x64_bf16 v[0:3], v[232:235], v[236:243], v10\n\t" \
    "v_xor_b32 v9, 49152, v9\n\t" \
    "v_smfmac_f32_16x16x64_bf16 v[4:7], v[232:235], v[244:251], v10\n\t" \
    "v_mov_b32 v253, %[stag]\n\t" \
    "s_nop 4\n\t" \
    "v_add_f32 v0, v0, v1\n\t" \
    "v_add_f32 v2, v2, v3\n\t" \
    "v_add_f32 v4, v4, v5\n\t" \
    "v_add_f32 v6, v6, v7\n\t" \
    "v_cvt_pk_bf16_f32 v0, v0, v4\n\t" \
    "v_cvt_pk_bf16_f32 v2, v2, v6\n\t" \
    "ds_write2_b32 v11, v0, v2 offset0:0 offset1:4\n\t" \
    "s_mov_b64 exec, %[onej]\n\t" \
    "ds_write_b32 v18, v253 offset:16\n\t" \
    "s_mov_b64 exec, -1\n\t" \
    GSA(1) \
    "s_setprio 0\n\t" \
    "buffer_load_dwordx4 v[236:239], v12, %[rs], %[qx] offen offset:0 sc1\n\t" \
    "buffer_load_dwordx4 v[240:243], v12, %[rs], %[qx] offen offset:1024 sc1\n\t" \
    "buffer_load_dwordx4 v[244:247], v12, %[rs], %[qx] offen offset:2048 sc1\n\t" \
    "buffer_load_dwordx4 v[248:251], v12, %[rs], %[qx] offen offset:3072 sc1\n\t" \
    "v_cmp_le_i32 s[72:73], 0, v16\n\t" \
    "v_cmp_le_i32 s[74:75], 1, v16\n\t" \
    "v_cmp_le_i32 s[76:77], 2, v16\n\t" \
    "v_cmp_le_i32 s[78:79], 3, v16\n\t" \
    "v_cmp_le_i32 s[80:81], 16, v16\n\t" \
    "v_cmp_le_i32 s[82:83], 17, v16\n\t" \
    "v_cmp_le_i32 s[84:85], 18, v16\n\t" \
    "v_cmp_le_i32 s[86:87], 19, v16\n\t" \
    "v_mov_b32 v254, v254\n\t" \
    "s_nop 2\n\t" \
    "v_mov_b32 v254, v254\n\t" \
    "s_nop 2\n\t" \
    "v_mov_b32 v254, v254\n\t" \
    "s_nop 2\n\t" \
    "v_mov_b32 v254, v254\n\t" \
    "s_nop 2\n\t" \
    "v_mov_b32 v254, v254\n\t" \
    "s_nop 2\n\t" \
    "v_mov_b32 v254, v254\n\t" \
    "s_nop 2\n\t" \
    "v_mov_b32 v254, v254\n\t" \
    "s_nop 2\n\t" \
    "v_mov_b32 v254, v254\n\t" \
    "s_nop 2\n\t" \
    "v_mov_b32 v254, v254\n\t" \
    "s_nop 2\n\t" \
    "v_mov_b32 v254, v254\n\t" \
    "s_nop 2\n\t" \
    "v_mov_b32 v254, v254\n\t" \
    "s_nop 2\n\t" \
    "v_mov_b32 v254, v254\n\t" \
    "s_nop 2\n\t" \
    "v_mov_b32 v254, v254\n\t" \
    "s_nop 2\n\t" \
    "v_mov_b32 v254, v254\n\t" \
    "s_nop 2\n\t" \
    "v_mov_b32 v254, v254\n\t" \
    "s_nop 2\n\t" \
    "v_mov_b32 v254, v254\n\t" \
    "s_nop 2\n\t" \
    "v_mov_b32 v254, v254\n\t" \
    "s_nop 2\n\t" \
    "v_mov_b32 v254, v254\n\t" \
    "s_nop 2\n\t" \
    "v_mov_b32 v254, v254\n\t" \
    "s_nop 2\n\t" \
    "v_mov_b32 v254, v254\n\t" \
    "s_nop 2\n\t" \
    "LW_%=:\n\t" \
    "ds_read_b32 v0, v18 offset:16\n\t" \
    "s_sub_u32 %[lim], %[lim], " STR(DEC1) "\n\t" \
    "s_cmp_gt_i32 %[lim], 0\n\t" \
    "v_mov_b32 v254, v254\n\t" \
    "s_nop 2\n\t" \
    "v_mov_b32 v254, v254\n\t" \
    "s_nop 2\n\t" \
    "v_mov_b32 v254, v254\n\t" \
    "s_nop 2\n\t" \
    "v_mov_b32 v254, v254\n\t" \
    "s_nop 2\n\t" \
    "v_mov_b32 v254, v254\n\t" \
    "s_nop 2\n\t" \
    "s_waitcnt lgkmcnt(0)\n\t" \
    "s_cbranch_scc0 CW_%=\n\t" \
    "v_cmp_ne_u32 vcc, v0, v253\n\t" \
    "s_cbranch_vccnz LW_%=\n\t" \
    "CW_%=:\n\t" \
    "ds_read_b128 v[212:215], v12 offset:24576\n\t" \
    "ds_read_b128 v[216:219], v12 offset:28672\n\t" \
    "ds_read_b128 v[220:223], v15 offset:0\n\t" \
    "ds_read_b128 v[224:227], v12 offset:25600\n\t" \
    "ds_read_b128 v[228:231], v12 offset:29696\n\t" \
    "v_add_u32 v253, %[se], v12\n\t" \
    "v_mov_b32 v254, v254\n\t" \
    "s_nop 2\n\t" \
    "v_mov_b32 v254, v254\n\t" \
    "s_nop 2\n\t" \
    "v_mov_b32 v254, v254\n\t" \
    "s_nop 2\n\t" \
    "v_mov_b32 v254, v254\n\t" \
    "s_nop 2\n\t" \
    "s_waitcnt vmcnt(0)\n\t" \
    "s_waitcnt lgkmcnt(4)\n\t" \
    "v_mfma_f32_16x16x32_bf16 v[0:3], v[212:215], v[236:239], 0\n\t" \
    "ds_read_b128 v[212:215], v15 offset:512\n\t" \
    "s_waitcnt lgkmcnt(4)\n\t" \
    "v_mfma_f32_16x16x32_bf16 v[4:7], v[216:219], v[236:239], 0\n\t" \
    "ds_read_b128 v[216:219], v12 offset:26624\n\t" \
    "s_waitcnt lgkmcnt(4)\n\t" \
    "v_mfma_f32_16x16x32_bf16 v[232:235], v[220:223], v[236:239], 0\n\t" \
    "ds_read_b128 v[220:223], v12 offset:30720\n\t" \
    "s_waitcnt lgkmcnt(4)\n\t" \
    "v_mfma_f32_16x16x32_bf16 v[0:3], v[224:227], v[240:243], v[0:3]\n\t" \
    "ds_read_b128 v[224:227], v15 offset:1024\n\t" \
    "s_waitcnt lgkmcnt(4)\n\t" \
    "v_mfma_f32_16x16x32_bf16 v[4:7], v[228:231], v[240:243], v[4:7]\n\t" \
    "ds_read_b128 v[228:231], v12 offset:27648\n\t" \
    "s_waitcnt lgkmcnt(4)\n\t" \
    "v_mfma_f32_16x16x32_bf16 v[232:235], v[212:215], v[240:243], v[232:235]\n\t" \
    "ds_read_b128 v[212:215], v12 offset:31744\n\t" \
    "s_waitcnt lgkmcnt(4)\n\t" \
    "v_mfma_f32_16x16x32_bf16 v[0:3], v[216:219], v[244:247], v[0:3]\n\t" \
    "ds_read_b128 v[216:219], v15 offset:1536\n\t" \
    "s_waitcnt lgkmcnt(4)\n\t" \
    "v_mfma_f32_16x16x32_bf16 v[4:7], v[220:223], v[244:247], v[4:7]\n\t" \
    "s_waitcnt lgkmcnt(3)\n\t" \
    "v_mfma_f32_16x16x32_bf16 v[232:235], v[224:227], v[244:247], v[232:235]\n\t" \
    "s_waitcnt lgkmcnt(2)\n\t" \
    "v_mfma_f32_16x16x32_bf16 v[0:3], v[228:231], v[248:251], v[0:3]\n\t" \
    "s_waitcnt lgkmcnt(1)\n\t" \
    "v_mfma_f32_16x16x32_bf16 v[4:7], v[212:215], v[248:251], v[4:7]\n\t" \
    "s_waitcnt lgkmcnt(0)\n\t" \
    "v_mfma_f32_16x16x32_bf16 v[232:235], v[216:219], v[248:251], v[232:235]\n\t" \
    "s_barrier\n\t" \
    GSA(4) \
    "ds_read_b128 v[236:239], v253 offset:8192\n\t" \
    "ds_read_b128 v[240:243], v253 offset:9216\n\t" \
    "ds_read_b128 v[244:247], v253 offset:10240\n\t" \
    "ds_read_b128 v[248:251], v253 offset:11264\n\t" \
    "ds_read2st64_b32 v[228:229], v17 offset0:0 offset1:1\n\t" \
    "ds_read2st64_b32 v[230:231], v17 offset0:2 offset1:3\n\t" \
    "s_mov_b64 exec, %[fm]\n\t" \
    "v_mov_b32 v220, %[st]\n\t" \
    "v_mov_b32 v221, %[tag]\n\t" \
    "v_mov_b32 v222, 0\n\t" \
    "buffer_store_dwordx2 v[220:221], v222, %[rs], %[po] offen sc1\n\t" \
    "s_mov_b64 exec, -1\n\t" \
    "v_mov_b32 v254, 0xff800000\n\t" \
    "v_cvt_pk_bf16_f32 v0, 0, v0\n\t" \
    "v_cvt_pk_bf16_f32 v1, 0, v1\n\t" \
    "v_cvt_pk_bf16_f32 v2, 0, v2\n\t" \
    "v_cvt_pk_bf16_f32 v3, 0, v3\n\t" \
    "v_cvt_pk_bf16_f32 v4, 0, v4\n\t" \
    "v_cvt_pk_bf16_f32 v5, 0, v5\n\t" \
    "v_cvt_pk_bf16_f32 v6, 0, v6\n\t" \
    "v_cvt_pk_bf16_f32 v7, 0, v7\n\t" \
    "v_cvt_pk_bf16_f32 v232, 0, v232\n\t" \
    "v_cvt_pk_bf16_f32 v233, 0, v233\n\t" \
    "v_mul_f32 v0, 0x3db504f3, v0\n\t" \
    "v_mul_f32 v1, 0x3db504f3, v1\n\t" \
    "v_mul_f32 v2, 0x3db504f3, v2\n\t" \
    "v_mul_f32 v3, 0x3db504f3, v3\n\t" \
    "v_mul_f32 v4, 0x3db504f3, v4\n\t" \
    "v_mul_f32 v5, 0x3db504f3, v5\n\t" \
    "v_mul_f32 v6, 0x3db504f3, v6\n\t" \
    "v_mul_f32 v7, 0x3db504f3, v7\n\t" \
    "v_mul_f32 v232, 0x3db504f3, v232\n\t" \
    "v_mul_f32 v233, 0x3db504f3, v233\n\t" \
    "v_cvt_pk_bf16_f32 v0, 0, v0\n\t" \
    "v_cvt_pk_bf16_f32 v1, 0, v1\n\t" \
    "v_cvt_pk_bf16_f32 v2, 0, v2\n\t" \
    "v_cvt_pk_bf16_f32 v3, 0, v3\n\t" \
    "v_cvt_pk_bf16_f32 v4, 0, v4\n\t" \
    "v_cvt_pk_bf16_f32 v5, 0, v5\n\t" \
    "v_cvt_pk_bf16_f32 v6, 0, v6\n\t" \
    "v_cvt_pk_bf16_f32 v7, 0, v7\n\t" \
    "v_cvt_pk_bf16_f32 v232, 0, v232\n\t" \
    "v_cvt_pk_bf16_f32 v233, 0, v233\n\t" \
    "v_cndmask_b32 v0, v254, v0, s[72:73]\n\t" \
    "v_cndmask_b32 v1, v254, v1, s[74:75]\n\t" \
    "v_cndmask_b32 v2, v254, v2, s[76:77]\n\t" \
    "v_cndmask_b32 v3, v254, v3, s[78:79]\n\t" \
    "v_cndmask_b32 v4, v254, v4, s[80:81]\n\t" \
    "v_cndmask_b32 v5, v254, v5, s[82:83]\n\t" \
    "v_cndmask_b32 v6, v254, v6, s[84:85]\n\t" \
    "v_cndmask_b32 v7, v254, v7, s[86:87]\n\t" \
    "v_max3_f32 v252, v0, v1, v2\n\t" \
    "v_max3_f32 v255, v3, v4, v5\n\t" \
    "v_max3_f32 v254, v6, v7, v232\n\t" \
    "v_max3_f32 v252, v252, v255, v254\n\t" \
    "v_max_f32 v252, v252, v233\n\t" \
    "v_mov_b32 v255, v252\n\t" \
    "v_mov_b32 v254, v252\n\t" \
    "s_nop 1\n\t" \
    "v_permlane16_swap_b32 v255, v254\n\t" \
    "v_max_f32 v252, v255, v254\n\t" \
    "v_max_f32 v255, v255, v254\n\t" \
    "s_nop 1\n\t" \
    "v_permlane32_swap_b32 v252, v255\n\t" \
    "v_max_f32 v252, v252, v255\n\t" \
    "v_mul_f32 v252, 0xbfb8aa3b, v252\n\t" \
    "v_fma_f32 v0, v0, %[l2e], v252\n\t" \
    "v_fma_f32 v1, v1, %[l2e], v252\n\t" \
    "v_fma_f32 v2, v2, %[l2e], v252\n\t" \
    "v_fma_f32 v3, v3, %[l2e], v252\n\t" \
    "v_fma_f32 v4, v4, %[l2e], v252\n\t" \
    "v_fma_f32 v5, v5, %[l2e], v252\n\t" \
    "v_fma_f32 v6, v6, %[l2e], v252\n\t" \
    "v_fma_f32 v7, v7, %[l2e], v252\n\t" \
    "v_fma_f32 v232, v232, %[l2e], v252\n\t" \
    "v_fma_f32 v233, v233, %[l2e], v252\n\t" \
    "v_exp_f32 v0, v0\n\t" \
    "v_exp_f32 v1, v1\n\t" \
    "v_exp_f32 v2, v2\n\t" \
    "v_exp_f32 v3, v3\n\t" \
    "v_exp_f32 v4, v4\n\t" \
    "v_exp_f32 v5, v5\n\t" \
    "v_exp_f32 v6, v6\n\t" \
    "v_exp_f32 v7, v7\n\t" \
    "v_exp_f32 v232, v232\n\t" \
    "v_exp_f32 v233, v233\n\t" \
    "v_add_f32 v252, v0, v1\n\t" \
    "v_add_f32 v235, v2, v3\n\t" \
    "v_add_f32 v254, v4, v5\n\t" \
    "v_add_f32 v255, v6, v7\n\t" \
    "v_add_f32 v234, v232, v233\n\t" \
    "v_add_f32 v252, v252, v235\n\t" \
    "v_add_f32 v254, v254, v255\n\t" \
    "v_add_f32 v252, v252, v254\n\t" \
    "v_add_f32 v252, v252, v234\n\t" \
    "v_mov_b32 v255, v252\n\t" \
    "v_mov_b32 v254, v252\n\t" \
    "s_nop 1\n\t" \
    "v_permlane16_swap_b32 v255, v254\n\t" \
    "v_add_f32 v252, v255, v254\n\t" \
    "v_add_f32 v255, v255, v254\n\t" \
    "s_nop 1\n\t" \
    "v_permlane32_swap_b32 v252, v255\n\t" \
    "v_add_f32 v252, v252, v255\n\t" \
    "v_rcp_f32 v252, v252\n\t" \
    "s_nop 0\n\t" \
    "v_mul_f32 v0, v0, v252\n\t" \
    "v_mul_f32 v1, v1, v252\n\t" \
    "v_mul_f32 v2, v2, v252\n\t" \
    "v_mul_f32 v3, v3, v252\n\t" \
    "v_mul_f32 v4, v4, v252\n\t" \
    "v_mul_f32 v5, v5, v252\n\t" \
    "v_mul_f32 v6, v6, v252\n\t" \
    "v_mul_f32 v7, v7, v252\n\t" \
    "v_mul_f32 v232, v232, v252\n\t" \
    "v_mul_f32 v233, v233, v252\n\t" \
    "v_cvt_pk_bf16_f32 v0, v0, v1\n\t" \
    "v_cvt_pk_bf16_f32 v1, v2, v3\n\t" \
    "v_cvt_pk_bf16_f32 v2, v4, v5\n\t" \
    "v_cvt_pk_bf16_f32 v3, v6, v7\n\t" \
    "v_cvt_pk_bf16_f32 v4, v232, v233\n\t" \
    "v_mov_b32 v5, 0\n\t" \
    "v_mov_b32 v6, 0\n\t" \
    "v_cvt_pk_bf16_f32 v7, v232, v233\n\t" \
    "s_waitcnt lgkmcnt(0)\n\t" \
    "v_mfma_f32_16x16x32_bf16 v[236:239], v[236:239], v[0:3], 0\n\t" \
    "v_mfma_f32_16x16x32_bf16 v[240:243], v[240:243], v[0:3], 0\n\t" \
    "v_mfma_f32_16x16x32_bf16 v[244:247], v[244:247], v[0:3], 0\n\t" \
    "v_mfma_f32_16x16x32_bf16 v[248:251], v[248:251], v[0:3], 0\n\t" \
    "v_mfma_f32_16x16x16_bf16 v[236:239], v[228:229], v[4:5], v[236:239]\n\t" \
    "v_mfma_f32_16x16x16_bf16 v[240:243], v[228:229], v[6:7], v[240:243]\n\t" \
    "v_mfma_f32_16x16x16_bf16 v[244:247], v[230:231], v[4:5], v[244:247]\n\t" \
    "v_mfma_f32_16x16x16_bf16 v[248:251], v[230:231], v[6:7], v[248:251]\n\t" \
    "s_nop 4\n\t" \
    "v_cvt_pk_bf16_f32 v212, v236, v237\n\t" \
    "v_cvt_pk_bf16_f32 v213, v240, v241\n\t" \
    "v_cvt_pk_bf16_f32 v214, v238, v239\n\t" \
    "v_cvt_pk_bf16_f32 v215, v242, v243\n\t" \
    "v_permlane16_swap_b32 v212, v213\n\t" \
    "s_nop 0\n\t" \
    "v_permlane16_swap_b32 v214, v215\n\t" \
    "v_cvt_pk_bf16_f32 v216, v244, v245\n\t" \
    "v_cvt_pk_bf16_f32 v217, v248, v249\n\t" \
    "v_cvt_pk_bf16_f32 v218, v246, v247\n\t" \
    "v_cvt_pk_bf16_f32 v219, v250, v251\n\t" \
    "v_permlane16_swap_b32 v216, v217\n\t" \
    "s_nop 0\n\t" \
    "v_permlane16_swap_b32 v218, v219\n\t" \
    "s_setprio 1\n\t" \
    GSA(2) \
    GSW \
    "s_nop 1\n\t" \
    : [lim] "+s"(lim), "+{v[212:219]}"(oxr) DGO GSO(0) GSO(1) GSO(2) GSO(3) GSO(4) \
    : "{v8}"(am_a), "{v9}"(t0_a), "{v10}"(idx), "{v11}"(iw_a), "{v12}"(ab_a), "{v14}"(xo_v), "{v15}"(kr_a), "{v16}"(dq), "{v17}"(vl_a), "{v18}"(fr_a), [stag] "s"(0xffffff81u + s), [onej] "s"(onej), [rs] "s"(rs), [m0] "s"(sbm + 16384u), [m1] "s"(sbm + 20480u), [st] "s"(s), [tag] "s"(P.tag), [po] "s"(FLG_OFF + F_PRG(4, h) * 128), [fm] "s"(fmask), [qx] "s"(qxo), [qd] "s"(lbase + LA_VM), [qh] "s"((j & 1) * 2048), [ox] "s"(oxo), [se] "s"((j & 1) * 4096), [l2e] "s"(0x3fb8aa3b), "{v[20:27]}"(wr[0][0]), "{v[28:35]}"(wr[0][1]), "{v[36:43]}"(wr[1][0]), "{v[44:51]}"(wr[1][1]), "{v[52:59]}"(wr[2][0]), "{v[60:67]}"(wr[2][1]), "{v[68:75]}"(wr[3][0]), "{v[76:83]}"(wr[3][1]), "{v[84:91]}"(wr[4][0]), "{v[92:99]}"(wr[4][1]), "{v[100:107]}"(wr[5][0]), "{v[108:115]}"(wr[5][1]), "{v[116:123]}"(wr[6][0]), "{v[124:131]}"(wr[6][1]), "{v[132:139]}"(wr[7][0]), "{v[140:147]}"(wr[7][1]), "{v[148:155]}"(wr[8][0]), "{v[156:163]}"(wr[8][1]), "{v[164:171]}"(wr[9][0]), "{v[172:179]}"(wr[9][1]), "{v[180:187]}"(wr[10][0]), "{v[188:195]}"(wr[10][1]), "{v[196:203]}"(wr[11][0]), "{v[204:211]}"(wr[11][1]) \
    : "memory", "scc", "vcc", "s90", "s91", "s72", "s73", "s74", "s75", "s76", "s77", "s78", "s79", "s80", "s81", "s82", "s83", "s84", "s85", "s86", "s87", "v0", "v1", "v2", "v3", "v4", "v5", "v6", "v7", "v220", "v221", "v222", "v223", "v224", "v225", "v226", "v227", "v228", "v229", "v230", "v231", "v232", "v233", "v234", "v235", "v236", "v237", "v238", "v239", "v240", "v241", "v242", "v243", "v244", "v245", "v246", "v247", "v248", "v249", "v250", "v251", "v252", "v253", "v254", "v255")

#define B_POLL() \
  asm volatile( \
    "TWB_%=:\n\t" \
    "s_memrealtime s[88:89]\n\t" \
    "s_memtime s[90:91]\n\t" \
    "s_waitcnt lgkmcnt(0)\n\t" \
    "s_lshl_b32 s88, s88, 2\n\t" \
    "s_sub_u32 s89, %[tgt], s88\n\t" \
    "s_cmp_gt_i32 s89, 16\n\t" \
    "s_cbranch_scc1 TWB_%=\n\t" \
    "s_mul_i32 s89, s89, 11\n\t" \
    "s_ashr_i32 s89, s89, 1\n\t" \
    "s_add_u32 s89, s89, s90\n\t" \
    "TCB_%=:\n\t" \
    "s_memtime s[92:93]\n\t" \
    "s_waitcnt lgkmcnt(0)\n\t" \
    "s_sub_u32 s93, s89, s92\n\t" \
    "s_cmp_gt_i32 s93, 0\n\t" \
    "s_cbranch_scc1 TCB_%=\n\t" \
    "buffer_load_dwordx4 v[220:223], %[l16], %[rs], %[m0] offen offset:0 sc1\n\t" \
    "buffer_load_dwordx4 v[224:227], %[l16], %[rs], %[m0] offen offset:1024 sc1\n\t" \
    "buffer_load_dwordx4 v[228:231], %[l16], %[rs], %[m1] offen offset:0 sc1\n\t" \
    "buffer_load_dwordx4 v[232:235], %[l16], %[rs], %[m1] offen offset:1024 sc1\n\t" \
    "v_mov_b32 v252, v252\n\t" \
    "s_nop 2\n\t" \
    "v_mov_b32 v252, v252\n\t" \
    "s_nop 2\n\t" \
    "v_mov_b32 v252, v252\n\t" \
    "s_nop 2\n\t" \
    "v_mov_b32 v252, v252\n\t" \
    "s_nop 2\n\t" \
    "v_mov_b32 v252, v252\n\t" \
    "s_nop 2\n\t" \
    "v_mov_b32 v252, v252\n\t" \
    "s_nop 2\n\t" \
    "v_mov_b32 v252, v252\n\t" \
    "s_nop 2\n\t" \
    "v_mov_b32 v252, v252\n\t" \
    "s_nop 2\n\t" \
    "v_mov_b32 v252, v252\n\t" \
    "s_nop 2\n\t" \
    "v_mov_b32 v252, v252\n\t" \
    "s_nop 2\n\t" \
    "v_mov_b32 v252, v252\n\t" \
    "s_nop 2\n\t" \
    "v_mov_b32 v252, v252\n\t" \
    "s_nop 2\n\t" \
    "v_mov_b32 v252, v252\n\t" \
    "s_nop 2\n\t" \
    "v_mov_b32 v252, v252\n\t" \
    "s_nop 2\n\t" \
    "v_mov_b32 v252, v252\n\t" \
    "s_nop 2\n\t" \
    "v_mov_b32 v252, v252\n\t" \
    "s_nop 2\n\t" \
    "v_mov_b32 v252, v252\n\t" \
    "s_nop 2\n\t" \
    "v_mov_b32 v252, v252\n\t" \
    "s_nop 2\n\t" \
    "v_mov_b32 v252, v252\n\t" \
    "s_nop 2\n\t" \
    "v_mov_b32 v252, v252\n\t" \
    "s_nop 2\n\t" \
    "v_mov_b32 v252, v252\n\t" \
    "s_nop 2\n\t" \
    "v_mov_b32 v252, v252\n\t" \
    "s_nop 2\n\t" \
    "v_mov_b32 v252, v252\n\t" \
    "s_nop 2\n\t" \
    "v_mov_b32 v252, v252\n\t" \
    "s_nop 2\n\t" \
    "PM_%=:\n\t" \
    "s_waitcnt vmcnt(3)\n\t" \
    "ds_write_b128 %[la], v[220:223] offset:0\n\t" \
    "v_max3_u32 v252, v220, v220, v221\n\t" \
    "v_max3_u32 v252, v252, v222, v223\n\t" \
    "s_waitcnt vmcnt(2)\n\t" \
    "ds_write_b128 %[la], v[224:227] offset:1024\n\t" \
    "v_max3_u32 v252, v252, v224, v225\n\t" \
    "v_max3_u32 v252, v252, v226, v227\n\t" \
    "s_waitcnt vmcnt(1)\n\t" \
    "ds_write_b128 %[la], v[228:231] offset:8192\n\t" \
    "v_max3_u32 v252, v252, v228, v229\n\t" \
    "v_max3_u32 v252, v252, v230, v231\n\t" \
    "s_waitcnt vmcnt(0)\n\t" \
    "ds_write_b128 %[la], v[232:235] offset:9216\n\t" \
    "v_max3_u32 v252, v252, v232, v233\n\t" \
    "v_max3_u32 v252, v252, v234, v235\n\t" \
    "v_cmp_eq_u32 vcc, -1, v252\n\t" \
    "s_cbranch_vccz PD_%=\n\t" \
    "s_sub_u32 %[lim], %[lim], 1\n\t" \
    "s_cmp_gt_i32 %[lim], 0\n\t" \
    "s_cbranch_scc0 PD_%=\n\t" \
    "s_sleep " STR(CADM) "\n\t" \
    "buffer_load_dwordx4 v[220:223], %[l16], %[rs], %[m0] offen offset:0 sc1\n\t" \
    "buffer_load_dwordx4 v[224:227], %[l16], %[rs], %[m0] offen offset:1024 sc1\n\t" \
    "buffer_load_dwordx4 v[228:231], %[l16], %[rs], %[m1] offen offset:0 sc1\n\t" \
    "buffer_load_dwordx4 v[232:235], %[l16], %[rs], %[m1] offen offset:1024 sc1\n\t" \
    "s_branch PM_%=\n\t" \
    "PD_%=:\n\t" \
    GSA(3) \
    "s_waitcnt lgkmcnt(0)\n\t" \
    "s_barrier\n\t" \
    : [lim] "+s"(lim) GSO(3) \
    : [l16] "v"(l16), [la] "v"(la), [rs] "s"(rs), [m0] "s"(mo), [m1] "s"(mo + 16384u), [tgt] "s"(tgt) \
    : "memory", "vcc", "scc", "v252", "s88", "s89", "s90", "s91", "s92", "s93", "v220", "v221", "v222", "v223", "v224", "v225", "v226", "v227", "v228", "v229", "v230", "v231", "v232", "v233", "v234", "v235")

#define T_WAIT() \
  asm volatile( \
    "TWC_%=:\n\t" \
    "s_memrealtime s[88:89]\n\t" \
    "s_memtime s[90:91]\n\t" \
    "s_waitcnt lgkmcnt(0)\n\t" \
    "s_lshl_b32 s88, s88, 2\n\t" \
    "s_sub_u32 s89, %[tgt], s88\n\t" \
    "s_cmp_gt_i32 s89, 16\n\t" \
    "s_cbranch_scc1 TWC_%=\n\t" \
    "s_mul_i32 s89, s89, 11\n\t" \
    "s_ashr_i32 s89, s89, 1\n\t" \
    "s_add_u32 s89, s89, s90\n\t" \
    "TCC_%=:\n\t" \
    "s_memtime s[92:93]\n\t" \
    "s_waitcnt lgkmcnt(0)\n\t" \
    "s_sub_u32 s93, s89, s92\n\t" \
    "s_cmp_gt_i32 s93, 0\n\t" \
    "s_cbranch_scc1 TCC_%=\n\t" \
    :  \
    : [tgt] "s"(tgt) \
    : "s88", "s89", "s90", "s91", "s92", "s93", "memory", "scc")

#define V_PROJ() \
  asm volatile( \
    "v_mov_b32 v0, 0\n\t" \
    "v_mov_b32 v1, 0\n\t" \
    "v_mov_b32 v2, 0\n\t" \
    "v_mov_b32 v3, 0\n\t" \
    "v_mov_b32 v4, 0\n\t" \
    "v_mov_b32 v5, 0\n\t" \
    "v_mov_b32 v6, 0\n\t" \
    "v_mov_b32 v7, 0\n\t" \
    "s_barrier\n\t" \
    "ds_read_b128 v[220:223], %[am] offset:0\n\t" \
    "ds_read_b128 v[224:227], %[am] offset:1024\n\t" \
    "ds_read_b128 v[228:231], %[am] offset:2048\n\t" \
    "ds_read_b128 v[232:235], %[am] offset:3072\n\t" \
    "ds_read_b128 v[236:239], %[am] offset:4096\n\t" \
    "ds_read_b128 v[240:243], %[am] offset:5120\n\t" \
    "ds_read_b128 v[244:247], %[am] offset:6144\n\t" \
    "ds_read_b128 v[248:251], %[am] offset:7168\n\t" \
    "s_waitcnt lgkmcnt(7)\n\t" \
    "v_smfmac_f32_16x16x64_bf16 v[0:3], v[220:223], v[28:35], %[idx]\n\t" \
    "ds_read_b128 v[220:223], %[am] offset:8192\n\t" \
    "s_waitcnt lgkmcnt(7)\n\t" \
    "v_smfmac_f32_16x16x64_bf16 v[4:7], v[224:227], v[36:43], %[idx]\n\t" \
    "ds_read_b128 v[224:227], %[am] offset:9216\n\t" \
    "s_waitcnt lgkmcnt(7)\n\t" \
    "v_smfmac_f32_16x16x64_bf16 v[0:3], v[228:231], v[44:51], %[idx]\n\t" \
    "ds_read_b128 v[228:231], %[am] offset:10240\n\t" \
    "s_waitcnt lgkmcnt(7)\n\t" \
    "v_smfmac_f32_16x16x64_bf16 v[4:7], v[232:235], v[52:59], %[idx]\n\t" \
    "ds_read_b128 v[232:235], %[am] offset:11264\n\t" \
    "s_waitcnt lgkmcnt(7)\n\t" \
    "v_smfmac_f32_16x16x64_bf16 v[0:3], v[236:239], v[60:67], %[idx]\n\t" \
    "ds_read_b128 v[236:239], %[am] offset:12288\n\t" \
    "s_waitcnt lgkmcnt(7)\n\t" \
    "v_smfmac_f32_16x16x64_bf16 v[4:7], v[240:243], v[68:75], %[idx]\n\t" \
    "ds_read_b128 v[240:243], %[am] offset:13312\n\t" \
    "s_waitcnt lgkmcnt(7)\n\t" \
    "v_smfmac_f32_16x16x64_bf16 v[0:3], v[244:247], v[76:83], %[idx]\n\t" \
    "ds_read_b128 v[244:247], %[am] offset:14336\n\t" \
    "s_waitcnt lgkmcnt(7)\n\t" \
    "v_smfmac_f32_16x16x64_bf16 v[4:7], v[248:251], v[84:91], %[idx]\n\t" \
    "ds_read_b128 v[248:251], %[am] offset:15360\n\t" \
    "s_waitcnt lgkmcnt(7)\n\t" \
    "v_smfmac_f32_16x16x64_bf16 v[0:3], v[220:223], v[92:99], %[idx]\n\t" \
    "s_waitcnt lgkmcnt(6)\n\t" \
    "v_smfmac_f32_16x16x64_bf16 v[4:7], v[224:227], v[100:107], %[idx]\n\t" \
    "s_waitcnt lgkmcnt(5)\n\t" \
    "v_smfmac_f32_16x16x64_bf16 v[0:3], v[228:231], v[108:115], %[idx]\n\t" \
    "s_waitcnt lgkmcnt(4)\n\t" \
    "v_smfmac_f32_16x16x64_bf16 v[4:7], v[232:235], v[116:123], %[idx]\n\t" \
    "s_waitcnt lgkmcnt(3)\n\t" \
    "v_smfmac_f32_16x16x64_bf16 v[0:3], v[236:239], v[124:131], %[idx]\n\t" \
    "s_waitcnt lgkmcnt(2)\n\t" \
    "v_smfmac_f32_16x16x64_bf16 v[4:7], v[240:243], v[132:139], %[idx]\n\t" \
    "s_waitcnt lgkmcnt(1)\n\t" \
    "v_smfmac_f32_16x16x64_bf16 v[0:3], v[244:247], v[140:147], %[idx]\n\t" \
    "s_waitcnt lgkmcnt(0)\n\t" \
    "v_smfmac_f32_16x16x64_bf16 v[4:7], v[248:251], v[148:155], %[idx]\n\t" \
    "s_nop 5\n\t" \
    "v_add_f32 v0, v0, v1\n\t" \
    "v_add_f32 v2, v2, v3\n\t" \
    "v_add_f32 v4, v4, v5\n\t" \
    "v_add_f32 v6, v6, v7\n\t" \
    "v_add_f32 v0, v0, v4\n\t" \
    "v_add_f32 v2, v2, v6\n\t" \
    "v_cvt_pk_bf16_f32 v0, v0, v2\n\t" \
    "v_min_u32 v0, -2, v0\n\t" \
    "s_nop 1\n\t" \
    "v_mov_b32_dpp v1, v0 quad_perm:[1,1,1,1] row_mask:0xf bank_mask:0xf\n\t" \
    "v_mov_b32_dpp v2, v0 quad_perm:[2,2,2,2] row_mask:0xf bank_mask:0xf\n\t" \
    "v_mov_b32_dpp v3, v0 quad_perm:[3,3,3,3] row_mask:0xf bank_mask:0xf\n\t" \
    "s_mov_b64 exec, %[qm]\n\t" \
    "buffer_store_dwordx4 v[0:3], %[l4], %[rs], %[vo] offen sc1\n\t" \
    "s_mov_b64 exec, -1\n\t" \
    "s_nop 1\n\t" \
    :  \
    : [am] "v"(am_a), [idx] "v"(idx), [l4] "v"(ln * 4), [rs] "s"(rs), [vo] "s"(sb + vo), [qm] "s"(0x1111111111111111ull), "{v[28:35]}"(wv[0]), "{v[36:43]}"(wv[1]), "{v[44:51]}"(wv[2]), "{v[52:59]}"(wv[3]), "{v[60:67]}"(wv[4]), "{v[68:75]}"(wv[5]), "{v[76:83]}"(wv[6]), "{v[84:91]}"(wv[7]), "{v[92:99]}"(wv[8]), "{v[100:107]}"(wv[9]), "{v[108:115]}"(wv[10]), "{v[116:123]}"(wv[11]), "{v[124:131]}"(wv[12]), "{v[132:139]}"(wv[13]), "{v[140:147]}"(wv[14]), "{v[148:155]}"(wv[15]) \
    : "memory", "v0", "v1", "v2", "v3", "v4", "v5", "v6", "v7", "v220", "v221", "v222", "v223", "v224", "v225", "v226", "v227", "v228", "v229", "v230", "v231", "v232", "v233", "v234", "v235", "v236", "v237", "v238", "v239", "v240", "v241", "v242", "v243", "v244", "v245", "v246", "v247", "v248", "v249", "v250", "v251")

#define WO_V() \
  asm volatile( \
    "v_mov_b32 v0, 0\n\t" \
    "v_mov_b32 v1, 0\n\t" \
    "v_mov_b32 v2, 0\n\t" \
    "v_mov_b32 v3, 0\n\t" \
    "v_mov_b32 v4, 0\n\t" \
    "v_mov_b32 v5, 0\n\t" \
    "v_mov_b32 v6, 0\n\t" \
    "v_mov_b32 v7, 0\n\t" \
    "v_mov_b32 v8, 0\n\t" \
    "v_mov_b32 v9, 0\n\t" \
    "v_mov_b32 v10, 0\n\t" \
    "v_mov_b32 v11, 0\n\t" \
    "v_mov_b32 v12, 0\n\t" \
    "v_mov_b32 v13, 0\n\t" \
    "v_mov_b32 v14, 0\n\t" \
    "v_mov_b32 v15, 0\n\t" \
    "v_mov_b32 v252, 0\n\t" \
    "s_barrier\n\t" \
    GSA(4) \
    DGC \
    "buffer_load_dwordx4 v[220:223], %[l16], %[rs], %[s0] offen sc1\n\t" \
    "buffer_load_dwordx4 v[224:227], %[l16], %[rs], %[s1] offen sc1\n\t" \
    "A_%=:\n\t" \
    "s_waitcnt vmcnt(1)\n\t" \
    "v_smfmac_f32_16x16x64_bf16 v[0:3], v[220:223], v[156:163], %[idx]\n\t" \
    "v_max3_u32 v252, v252, v220, v221\n\t" \
    "v_smfmac_f32_16x16x64_bf16 v[4:7], v[220:223], v[164:171], %[idx]\n\t" \
    "v_max3_u32 v252, v252, v222, v223\n\t" \
    "v_smfmac_f32_16x16x64_bf16 v[8:11], v[220:223], v[172:179], %[idx]\n\t" \
    "v_smfmac_f32_16x16x64_bf16 v[12:15], v[220:223], v[180:187], %[idx]\n\t" \
    "s_waitcnt vmcnt(0)\n\t" \
    "v_smfmac_f32_16x16x64_bf16 v[0:3], v[224:227], v[188:195], %[idx]\n\t" \
    "v_max3_u32 v252, v252, v224, v225\n\t" \
    "v_smfmac_f32_16x16x64_bf16 v[4:7], v[224:227], v[196:203], %[idx]\n\t" \
    "v_max3_u32 v252, v252, v226, v227\n\t" \
    "v_smfmac_f32_16x16x64_bf16 v[8:11], v[224:227], v[204:211], %[idx]\n\t" \
    "v_smfmac_f32_16x16x64_bf16 v[12:15], v[224:227], v[212:219], %[idx]\n\t" \
    "v_cmp_eq_u32 vcc, -1, v252\n\t" \
    "s_cbranch_vccnz SL_%=\n\t" \
    "D_%=:\n\t" \
    GSA(1) \
    "s_nop 2\n\t" \
    "v_add_f32 v0, v0, v1\n\t" \
    "v_add_f32 v2, v2, v3\n\t" \
    "v_add_f32 v4, v4, v5\n\t" \
    "v_add_f32 v6, v6, v7\n\t" \
    "v_add_f32 v8, v8, v9\n\t" \
    "v_add_f32 v10, v10, v11\n\t" \
    "v_add_f32 v12, v12, v13\n\t" \
    "v_add_f32 v14, v14, v15\n\t" \
    "v_permlane32_swap_b32 v0, v4\n\t" \
    "v_permlane32_swap_b32 v2, v6\n\t" \
    "v_permlane32_swap_b32 v8, v12\n\t" \
    "v_permlane32_swap_b32 v10, v14\n\t" \
    "v_add_f32 v0, v0, v12\n\t" \
    "v_add_f32 v2, v2, v14\n\t" \
    "ds_write2_b32 %[rw], v0, v2 offset0:0 offset1:32\n\t" \
    "s_waitcnt lgkmcnt(0)\n\t" \
    "s_barrier\n\t" \
    "s_cmp_eq_u32 %[red], 0\n\t" \
    "s_cbranch_scc0 E_%=\n\t" \
    "ds_read2st64_b64 v[0:3], %[rr] offset0:0 offset1:1\n\t" \
    "ds_read2st64_b64 v[4:7], %[rr] offset0:2 offset1:3\n\t" \
    "ds_read2st64_b64 v[8:11], %[rr] offset0:4 offset1:5\n\t" \
    "ds_read2st64_b64 v[12:15], %[rr] offset0:6 offset1:7\n\t" \
    "s_waitcnt lgkmcnt(3)\n\t" \
    "v_pk_add_f32 v[0:1], v[0:1], v[2:3]\n\t" \
    "s_waitcnt lgkmcnt(2)\n\t" \
    "v_pk_add_f32 v[4:5], v[4:5], v[6:7]\n\t" \
    "s_waitcnt lgkmcnt(1)\n\t" \
    "v_pk_add_f32 v[8:9], v[8:9], v[10:11]\n\t" \
    "v_pk_add_f32 v[0:1], v[0:1], v[4:5]\n\t" \
    "s_waitcnt lgkmcnt(0)\n\t" \
    "v_pk_add_f32 v[12:13], v[12:13], v[14:15]\n\t" \
    "s_nop 0\n\t" \
    "v_pk_add_f32 v[8:9], v[8:9], v[12:13]\n\t" \
    "s_nop 0\n\t" \
    "v_pk_add_f32 v[0:1], v[0:1], v[8:9]\n\t" \
    "s_nop 0\n\t" \
    "v_cvt_pk_bf16_f32 v0, v0, v1\n\t" \
    "v_min_u32 v0, -2, v0\n\t" \
    "buffer_store_dword v0, %[mo], %[rs], %[sn] offen sc1\n\t" \
    "s_memrealtime %[t1]\n\t" \
    DGA \
    "s_waitcnt lgkmcnt(0)\n\t" \
    GSA(2) \
    "s_branch E_%=\n\t" \
    "SL_%=:\n\t" \
    "s_sleep 1\n\t" \
    "buffer_load_dwordx4 v[220:223], %[l16], %[rs], %[s0] offen sc1\n\t" \
    "buffer_load_dwordx4 v[224:227], %[l16], %[rs], %[s1] offen sc1\n\t" \
    "s_sub_u32 %[lim], %[lim], " STR(DEC2) "\n\t" \
    "s_cmp_gt_i32 %[lim], 0\n\t" \
    "s_cbranch_scc0 G_%=\n\t" \
    "s_nop 3\n\t" \
    "v_mov_b32 v0, 0\n\t" \
    "v_mov_b32 v1, 0\n\t" \
    "v_mov_b32 v2, 0\n\t" \
    "v_mov_b32 v3, 0\n\t" \
    "v_mov_b32 v4, 0\n\t" \
    "v_mov_b32 v5, 0\n\t" \
    "v_mov_b32 v6, 0\n\t" \
    "v_mov_b32 v7, 0\n\t" \
    "v_mov_b32 v8, 0\n\t" \
    "v_mov_b32 v9, 0\n\t" \
    "v_mov_b32 v10, 0\n\t" \
    "v_mov_b32 v11, 0\n\t" \
    "v_mov_b32 v12, 0\n\t" \
    "v_mov_b32 v13, 0\n\t" \
    "v_mov_b32 v14, 0\n\t" \
    "v_mov_b32 v15, 0\n\t" \
    "v_mov_b32 v252, 0\n\t" \
    "s_branch A_%=\n\t" \
    "G_%=:\n\t" \
    "s_waitcnt vmcnt(0)\n\t" \
    "s_branch D_%=\n\t" \
    "E_%=:\n\t" \
    GSW \
    : [lim] "+s"(lim) GSO(1) GSO(4), [t1] "=&s"(t1) DGO DGQ GSO(2) \
    : [l16] "v"(l16), [rs] "s"(rs), [idx] "v"(idx), [rw] "v"(rw_a), [s0] "s"(so[0]), [s1] "s"(so[1]), [rr] "v"(rr_a), [mo] "v"(mo_v), [sn] "s"(sn), [red] "s"(wid), "{v[156:163]}"(wo[0][0][0]), "{v[164:171]}"(wo[0][0][1]), "{v[172:179]}"(wo[0][1][0]), "{v[180:187]}"(wo[0][1][1]), "{v[188:195]}"(wo[1][0][0]), "{v[196:203]}"(wo[1][0][1]), "{v[204:211]}"(wo[1][1][0]), "{v[212:219]}"(wo[1][1][1]) \
    : "memory", "vcc", "scc", "v252", "v0", "v1", "v2", "v3", "v4", "v5", "v6", "v7", "v8", "v9", "v10", "v11", "v12", "v13", "v14", "v15", "v220", "v221", "v222", "v223", "v224", "v225", "v226", "v227")

#define WO_W() \
  asm volatile( \
    "v_mov_b32 v0, 0\n\t" \
    "v_mov_b32 v1, 0\n\t" \
    "v_mov_b32 v2, 0\n\t" \
    "v_mov_b32 v3, 0\n\t" \
    "v_mov_b32 v4, 0\n\t" \
    "v_mov_b32 v5, 0\n\t" \
    "v_mov_b32 v6, 0\n\t" \
    "v_mov_b32 v7, 0\n\t" \
    "v_mov_b32 v8, 0\n\t" \
    "v_mov_b32 v9, 0\n\t" \
    "v_mov_b32 v10, 0\n\t" \
    "v_mov_b32 v11, 0\n\t" \
    "v_mov_b32 v12, 0\n\t" \
    "v_mov_b32 v13, 0\n\t" \
    "v_mov_b32 v14, 0\n\t" \
    "v_mov_b32 v15, 0\n\t" \
    "v_mov_b32 v252, 0\n\t" \
    "s_barrier\n\t" \
    GSA(4) \
    "buffer_load_dwordx4 v[220:223], %[l16], %[rs], %[s0] offen sc1\n\t" \
    "s_nop 15\n\t" \
    "buffer_load_dwordx4 v[224:227], %[l16], %[rs], %[s1] offen sc1\n\t" \
    "s_nop 15\n\t" \
    "buffer_load_dwordx4 v[228:231], %[l16], %[rs], %[s2] offen sc1\n\t" \
    "s_nop 15\n\t" \
    "buffer_load_dwordx4 v[232:235], %[l16], %[rs], %[s3] offen sc1\n\t" \
    "s_nop 15\n\t" \
    "buffer_load_dwordx4 v[236:239], %[l16], %[rs], %[s4] offen sc1\n\t" \
    "s_nop 15\n\t" \
    "buffer_load_dwordx4 v[240:243], %[l16], %[rs], %[s5] offen sc1\n\t" \
    "A_%=:\n\t" \
    "s_waitcnt vmcnt(5)\n\t" \
    "v_smfmac_f32_16x16x64_bf16 v[0:3], v[220:223], v[28:35], %[idx]\n\t" \
    "v_max3_u32 v252, v252, v220, v221\n\t" \
    "v_smfmac_f32_16x16x64_bf16 v[4:7], v[220:223], v[36:43], %[idx]\n\t" \
    "v_max3_u32 v252, v252, v222, v223\n\t" \
    "v_smfmac_f32_16x16x64_bf16 v[8:11], v[220:223], v[44:51], %[idx]\n\t" \
    "v_smfmac_f32_16x16x64_bf16 v[12:15], v[220:223], v[52:59], %[idx]\n\t" \
    "s_waitcnt vmcnt(4)\n\t" \
    "v_smfmac_f32_16x16x64_bf16 v[0:3], v[224:227], v[60:67], %[idx]\n\t" \
    "v_max3_u32 v252, v252, v224, v225\n\t" \
    "v_smfmac_f32_16x16x64_bf16 v[4:7], v[224:227], v[68:75], %[idx]\n\t" \
    "v_max3_u32 v252, v252, v226, v227\n\t" \
    "v_smfmac_f32_16x16x64_bf16 v[8:11], v[224:227], v[76:83], %[idx]\n\t" \
    "v_smfmac_f32_16x16x64_bf16 v[12:15], v[224:227], v[84:91], %[idx]\n\t" \
    "s_waitcnt vmcnt(3)\n\t" \
    "v_smfmac_f32_16x16x64_bf16 v[0:3], v[228:231], v[92:99], %[idx]\n\t" \
    "v_max3_u32 v252, v252, v228, v229\n\t" \
    "v_smfmac_f32_16x16x64_bf16 v[4:7], v[228:231], v[100:107], %[idx]\n\t" \
    "v_max3_u32 v252, v252, v230, v231\n\t" \
    "v_smfmac_f32_16x16x64_bf16 v[8:11], v[228:231], v[108:115], %[idx]\n\t" \
    "v_smfmac_f32_16x16x64_bf16 v[12:15], v[228:231], v[116:123], %[idx]\n\t" \
    "s_waitcnt vmcnt(2)\n\t" \
    "v_smfmac_f32_16x16x64_bf16 v[0:3], v[232:235], v[124:131], %[idx]\n\t" \
    "v_max3_u32 v252, v252, v232, v233\n\t" \
    "v_smfmac_f32_16x16x64_bf16 v[4:7], v[232:235], v[132:139], %[idx]\n\t" \
    "v_max3_u32 v252, v252, v234, v235\n\t" \
    "v_smfmac_f32_16x16x64_bf16 v[8:11], v[232:235], v[140:147], %[idx]\n\t" \
    "v_smfmac_f32_16x16x64_bf16 v[12:15], v[232:235], v[148:155], %[idx]\n\t" \
    "s_waitcnt vmcnt(1)\n\t" \
    "v_smfmac_f32_16x16x64_bf16 v[0:3], v[236:239], v[156:163], %[idx]\n\t" \
    "v_max3_u32 v252, v252, v236, v237\n\t" \
    "v_smfmac_f32_16x16x64_bf16 v[4:7], v[236:239], v[164:171], %[idx]\n\t" \
    "v_max3_u32 v252, v252, v238, v239\n\t" \
    "v_smfmac_f32_16x16x64_bf16 v[8:11], v[236:239], v[172:179], %[idx]\n\t" \
    "v_smfmac_f32_16x16x64_bf16 v[12:15], v[236:239], v[180:187], %[idx]\n\t" \
    "s_waitcnt vmcnt(0)\n\t" \
    "v_smfmac_f32_16x16x64_bf16 v[0:3], v[240:243], v[188:195], %[idx]\n\t" \
    "v_max3_u32 v252, v252, v240, v241\n\t" \
    "v_smfmac_f32_16x16x64_bf16 v[4:7], v[240:243], v[196:203], %[idx]\n\t" \
    "v_max3_u32 v252, v252, v242, v243\n\t" \
    "v_smfmac_f32_16x16x64_bf16 v[8:11], v[240:243], v[204:211], %[idx]\n\t" \
    "v_smfmac_f32_16x16x64_bf16 v[12:15], v[240:243], v[212:219], %[idx]\n\t" \
    "v_cmp_eq_u32 vcc, -1, v252\n\t" \
    "s_cbranch_vccnz SL_%=\n\t" \
    "D_%=:\n\t" \
    GSA(1) \
    "s_nop 2\n\t" \
    "v_add_f32 v0, v0, v1\n\t" \
    "v_add_f32 v2, v2, v3\n\t" \
    "v_add_f32 v4, v4, v5\n\t" \
    "v_add_f32 v6, v6, v7\n\t" \
    "v_add_f32 v8, v8, v9\n\t" \
    "v_add_f32 v10, v10, v11\n\t" \
    "v_add_f32 v12, v12, v13\n\t" \
    "v_add_f32 v14, v14, v15\n\t" \
    "v_permlane32_swap_b32 v0, v4\n\t" \
    "v_permlane32_swap_b32 v2, v6\n\t" \
    "v_permlane32_swap_b32 v8, v12\n\t" \
    "v_permlane32_swap_b32 v10, v14\n\t" \
    "v_add_f32 v0, v0, v12\n\t" \
    "v_add_f32 v2, v2, v14\n\t" \
    "ds_write2_b32 %[rw], v0, v2 offset0:0 offset1:32\n\t" \
    "s_waitcnt lgkmcnt(0)\n\t" \
    "s_barrier\n\t" \
    "s_branch E_%=\n\t" \
    "SL_%=:\n\t" \
    "s_sleep 1\n\t" \
    "buffer_load_dwordx4 v[220:223], %[l16], %[rs], %[s0] offen sc1\n\t" \
    "buffer_load_dwordx4 v[224:227], %[l16], %[rs], %[s1] offen sc1\n\t" \
    "buffer_load_dwordx4 v[228:231], %[l16], %[rs], %[s2] offen sc1\n\t" \
    "buffer_load_dwordx4 v[232:235], %[l16], %[rs], %[s3] offen sc1\n\t" \
    "buffer_load_dwordx4 v[236:239], %[l16], %[rs], %[s4] offen sc1\n\t" \
    "buffer_load_dwordx4 v[240:243], %[l16], %[rs], %[s5] offen sc1\n\t" \
    "s_sub_u32 %[lim], %[lim], " STR(DEC2) "\n\t" \
    "s_cmp_gt_i32 %[lim], 0\n\t" \
    "s_cbranch_scc0 G_%=\n\t" \
    "s_nop 3\n\t" \
    "v_mov_b32 v0, 0\n\t" \
    "v_mov_b32 v1, 0\n\t" \
    "v_mov_b32 v2, 0\n\t" \
    "v_mov_b32 v3, 0\n\t" \
    "v_mov_b32 v4, 0\n\t" \
    "v_mov_b32 v5, 0\n\t" \
    "v_mov_b32 v6, 0\n\t" \
    "v_mov_b32 v7, 0\n\t" \
    "v_mov_b32 v8, 0\n\t" \
    "v_mov_b32 v9, 0\n\t" \
    "v_mov_b32 v10, 0\n\t" \
    "v_mov_b32 v11, 0\n\t" \
    "v_mov_b32 v12, 0\n\t" \
    "v_mov_b32 v13, 0\n\t" \
    "v_mov_b32 v14, 0\n\t" \
    "v_mov_b32 v15, 0\n\t" \
    "v_mov_b32 v252, 0\n\t" \
    "s_branch A_%=\n\t" \
    "G_%=:\n\t" \
    "s_waitcnt vmcnt(0)\n\t" \
    "s_branch D_%=\n\t" \
    "E_%=:\n\t" \
    GSW \
    : [lim] "+s"(lim) GSO(1) GSO(4) \
    : [l16] "v"(l16), [rs] "s"(rs), [idx] "v"(idx), [rw] "v"(rw_a), [s0] "s"(so[0]), [s1] "s"(so[1]), [s2] "s"(so[2]), [s3] "s"(so[3]), [s4] "s"(so[4]), [s5] "s"(so[5]), "{v[28:35]}"(wo[0][0][0]), "{v[36:43]}"(wo[0][0][1]), "{v[44:51]}"(wo[0][1][0]), "{v[52:59]}"(wo[0][1][1]), "{v[60:67]}"(wo[1][0][0]), "{v[68:75]}"(wo[1][0][1]), "{v[76:83]}"(wo[1][1][0]), "{v[84:91]}"(wo[1][1][1]), "{v[92:99]}"(wo[2][0][0]), "{v[100:107]}"(wo[2][0][1]), "{v[108:115]}"(wo[2][1][0]), "{v[116:123]}"(wo[2][1][1]), "{v[124:131]}"(wo[3][0][0]), "{v[132:139]}"(wo[3][0][1]), "{v[140:147]}"(wo[3][1][0]), "{v[148:155]}"(wo[3][1][1]), "{v[156:163]}"(wo[4][0][0]), "{v[164:171]}"(wo[4][0][1]), "{v[172:179]}"(wo[4][1][0]), "{v[180:187]}"(wo[4][1][1]), "{v[188:195]}"(wo[5][0][0]), "{v[196:203]}"(wo[5][0][1]), "{v[204:211]}"(wo[5][1][0]), "{v[212:219]}"(wo[5][1][1]) \
    : "memory", "vcc", "scc", "v252", "v0", "v1", "v2", "v3", "v4", "v5", "v6", "v7", "v8", "v9", "v10", "v11", "v12", "v13", "v14", "v15", "v220", "v221", "v222", "v223", "v224", "v225", "v226", "v227", "v228", "v229", "v230", "v231", "v232", "v233", "v234", "v235", "v236", "v237", "v238", "v239", "v240", "v241", "v242", "v243")

#define S_LOOP_K(TE_, FIN_) \
  asm volatile( \
    "s_cmp_eq_u32 %[kh], 0\n\t" \
    "s_cbranch_scc0 L1_%=\n\t" \
    "L0_%=:\n\t" \
    "s_setprio 0\n\t" \
    "s_nop 3\n\t" \
    "ds_write_b128 %[pa], v[196:199] offset:0\n\t" \
    "ds_write_b128 %[pa], v[200:203] offset:1024\n\t" \
    "ds_write_b128 %[pa], v[204:207] offset:2048\n\t" \
    "ds_write_b128 %[pa], v[208:211] offset:3072\n\t" \
    "s_sub_u32 %[a], %[t], 1\n\t" \
    "v_mov_b32 v53, %[tag]\n\t" \
    "v_mov_b32 v52, %[a]\n\t" \
    "s_sub_u32 %[b], %[t], 2\n\t" \
    "s_cmp_lt_u32 %[b], " STR(SQUICK) "\n\t" \
    "s_cselect_b64 exec, %[fm], 0\n\t" \
    "buffer_store_dwordx2 v[52:53], %[l16], %[rs], %[fo] offen sc1\n\t" \
    "s_mov_b64 exec, -1\n\t" \
    "s_add_u32 m0, %[sd], 49152\n\t" \
    "s_add_u32 %[a], %[sx], 81920\n\t" \
    "s_waitcnt lgkmcnt(0)\n\t" \
    "buffer_load_dwordx4 %[l16], %[rs], %[a] offen offset:0 sc1 lds\n\t" \
    "buffer_load_dwordx4 %[l16], %[rs], %[a] offen offset:1024 sc1 lds\n\t" \
    "buffer_load_dwordx4 %[l16], %[rs], %[a] offen offset:2048 sc1 lds\n\t" \
    "buffer_load_dwordx4 %[l16], %[rs], %[a] offen offset:3072 sc1 lds\n\t" \
    "v_mfma_f32_16x16x32_bf16 v[0:3], v[68:71], v[20:23], 0\n\t" \
    "ds_read_b128 v[44:47], %[xp] offset:6144\n\t" \
    "v_mfma_f32_16x16x32_bf16 v[4:7], v[76:79], v[20:23], 0\n\t" \
    "ds_read_b128 v[48:51], %[xp] offset:7168\n\t" \
    "v_mfma_f32_16x16x32_bf16 v[8:11], v[68:71], v[24:27], 0\n\t" \
    "v_mfma_f32_16x16x32_bf16 v[12:15], v[76:79], v[24:27], 0\n\t" \
    "v_mfma_f32_16x16x32_bf16 v[0:3], v[72:75], v[28:31], v[0:3]\n\t" \
    "ds_read_b128 v[20:23], %[xp] offset:8192\n\t" \
    "v_mfma_f32_16x16x32_bf16 v[4:7], v[80:83], v[28:31], v[4:7]\n\t" \
    "ds_read_b128 v[24:27], %[xp] offset:9216\n\t" \
    "v_mfma_f32_16x16x32_bf16 v[8:11], v[72:75], v[32:35], v[8:11]\n\t" \
    "v_mfma_f32_16x16x32_bf16 v[12:15], v[80:83], v[32:35], v[12:15]\n\t" \
    "v_mfma_f32_16x16x32_bf16 v[0:3], v[84:87], v[36:39], v[0:3]\n\t" \
    "ds_read_b128 v[28:31], %[xp] offset:10240\n\t" \
    "v_mfma_f32_16x16x32_bf16 v[4:7], v[92:95], v[36:39], v[4:7]\n\t" \
    "ds_read_b128 v[32:35], %[xp] offset:11264\n\t" \
    "v_mfma_f32_16x16x32_bf16 v[8:11], v[84:87], v[40:43], v[8:11]\n\t" \
    "s_waitcnt lgkmcnt(4)\n\t" \
    "v_mfma_f32_16x16x32_bf16 v[12:15], v[92:95], v[40:43], v[12:15]\n\t" \
    "v_mfma_f32_16x16x32_bf16 v[0:3], v[88:91], v[44:47], v[0:3]\n\t" \
    "ds_read_b128 v[36:39], %[xp] offset:12288\n\t" \
    "v_mfma_f32_16x16x32_bf16 v[4:7], v[96:99], v[44:47], v[4:7]\n\t" \
    "ds_read_b128 v[40:43], %[xp] offset:13312\n\t" \
    "v_mfma_f32_16x16x32_bf16 v[8:11], v[88:91], v[48:51], v[8:11]\n\t" \
    "s_waitcnt lgkmcnt(4)\n\t" \
    "v_mfma_f32_16x16x32_bf16 v[12:15], v[96:99], v[48:51], v[12:15]\n\t" \
    "v_mfma_f32_16x16x32_bf16 v[0:3], v[100:103], v[20:23], v[0:3]\n\t" \
    "ds_read_b128 v[44:47], %[xp] offset:14336\n\t" \
    "v_mfma_f32_16x16x32_bf16 v[4:7], v[108:111], v[20:23], v[4:7]\n\t" \
    "ds_read_b128 v[48:51], %[xp] offset:15360\n\t" \
    "v_mfma_f32_16x16x32_bf16 v[8:11], v[100:103], v[24:27], v[8:11]\n\t" \
    "s_waitcnt lgkmcnt(4)\n\t" \
    "v_mfma_f32_16x16x32_bf16 v[12:15], v[108:111], v[24:27], v[12:15]\n\t" \
    "v_mfma_f32_16x16x32_bf16 v[0:3], v[104:107], v[28:31], v[0:3]\n\t" \
    "ds_read_b128 v[20:23], %[xp] offset:16384\n\t" \
    "v_mfma_f32_16x16x32_bf16 v[4:7], v[112:115], v[28:31], v[4:7]\n\t" \
    "ds_read_b128 v[24:27], %[xp] offset:17408\n\t" \
    "v_mfma_f32_16x16x32_bf16 v[8:11], v[104:107], v[32:35], v[8:11]\n\t" \
    "s_waitcnt lgkmcnt(4)\n\t" \
    "v_mfma_f32_16x16x32_bf16 v[12:15], v[112:115], v[32:35], v[12:15]\n\t" \
    "v_mfma_f32_16x16x32_bf16 v[0:3], v[116:119], v[36:39], v[0:3]\n\t" \
    "ds_read_b128 v[28:31], %[xp] offset:18432\n\t" \
    "v_mfma_f32_16x16x32_bf16 v[4:7], v[124:127], v[36:39], v[4:7]\n\t" \
    "ds_read_b128 v[32:35], %[xp] offset:19456\n\t" \
    "v_mfma_f32_16x16x32_bf16 v[8:11], v[116:119], v[40:43], v[8:11]\n\t" \
    "s_waitcnt lgkmcnt(4)\n\t" \
    "v_mfma_f32_16x16x32_bf16 v[12:15], v[124:127], v[40:43], v[12:15]\n\t" \
    "v_mfma_f32_16x16x32_bf16 v[0:3], v[120:123], v[44:47], v[0:3]\n\t" \
    "ds_read_b128 v[36:39], %[xp] offset:20480\n\t" \
    "v_mfma_f32_16x16x32_bf16 v[4:7], v[128:131], v[44:47], v[4:7]\n\t" \
    "ds_read_b128 v[40:43], %[xp] offset:21504\n\t" \
    "v_mfma_f32_16x16x32_bf16 v[8:11], v[120:123], v[48:51], v[8:11]\n\t" \
    "s_waitcnt lgkmcnt(4)\n\t" \
    "v_mfma_f32_16x16x32_bf16 v[12:15], v[128:131], v[48:51], v[12:15]\n\t" \
    "s_waitcnt vmcnt(4)\n\t" \
    "s_barrier\n\t" \
    "s_setprio 0\n\t" \
    "s_sub_u32 %[a], %[t], 1\n\t" \
    "v_mov_b32 v53, %[tag]\n\t" \
    "v_mov_b32 v52, %[a]\n\t" \
    "s_cmp_gt_u32 %[t], 1\n\t" \
    "s_cselect_b64 exec, %[fm], 0\n\t" \
    "buffer_store_dwordx2 v[52:53], %[l16], %[rs], %[fo] offen sc1\n\t" \
    "s_mov_b64 exec, %[pm]\n\t" \
    "s_mov_b32 m0, %[pd]\n\t" \
    "s_add_u32 %[a], %[sx], %[po]\n\t" \
    "buffer_load_dwordx4 %[l16], %[rs], %[a] offen sc1 lds\n\t" \
    "s_mov_b64 exec, -1\n\t" \
    "s_add_u32 m0, %[sd], 0\n\t" \
    "s_add_u32 %[a], %[sx], 131072\n\t" \
    "buffer_load_dwordx4 %[l16], %[rs], %[a] offen offset:0 sc1 lds\n\t" \
    "buffer_load_dwordx4 %[l16], %[rs], %[a] offen offset:1024 sc1 lds\n\t" \
    "buffer_load_dwordx4 %[l16], %[rs], %[a] offen offset:2048 sc1 lds\n\t" \
    "buffer_load_dwordx4 %[l16], %[rs], %[a] offen offset:3072 sc1 lds\n\t" \
    "v_mfma_f32_16x16x32_bf16 v[0:3], v[132:135], v[20:23], v[0:3]\n\t" \
    "ds_read_b128 v[44:47], %[xp] offset:22528\n\t" \
    "v_mfma_f32_16x16x32_bf16 v[4:7], v[140:143], v[20:23], v[4:7]\n\t" \
    "ds_read_b128 v[48:51], %[xp] offset:23552\n\t" \
    "v_mfma_f32_16x16x32_bf16 v[8:11], v[132:135], v[24:27], v[8:11]\n\t" \
    "s_waitcnt lgkmcnt(4)\n\t" \
    "v_mfma_f32_16x16x32_bf16 v[12:15], v[140:143], v[24:27], v[12:15]\n\t" \
    "v_mfma_f32_16x16x32_bf16 v[0:3], v[136:139], v[28:31], v[0:3]\n\t" \
    "ds_read_b128 v[20:23], %[xp] offset:24576\n\t" \
    "v_mfma_f32_16x16x32_bf16 v[4:7], v[144:147], v[28:31], v[4:7]\n\t" \
    "ds_read_b128 v[24:27], %[xp] offset:25600\n\t" \
    "v_mfma_f32_16x16x32_bf16 v[8:11], v[136:139], v[32:35], v[8:11]\n\t" \
    "s_waitcnt lgkmcnt(4)\n\t" \
    "v_mfma_f32_16x16x32_bf16 v[12:15], v[144:147], v[32:35], v[12:15]\n\t" \
    "v_mfma_f32_16x16x32_bf16 v[0:3], v[148:151], v[36:39], v[0:3]\n\t" \
    "ds_read_b128 v[28:31], %[xp] offset:26624\n\t" \
    "v_mfma_f32_16x16x32_bf16 v[4:7], v[156:159], v[36:39], v[4:7]\n\t" \
    "ds_read_b128 v[32:35], %[xp] offset:27648\n\t" \
    "v_mfma_f32_16x16x32_bf16 v[8:11], v[148:151], v[40:43], v[8:11]\n\t" \
    "s_waitcnt lgkmcnt(4)\n\t" \
    "v_mfma_f32_16x16x32_bf16 v[12:15], v[156:159], v[40:43], v[12:15]\n\t" \
    "v_mfma_f32_16x16x32_bf16 v[0:3], v[152:155], v[44:47], v[0:3]\n\t" \
    "ds_read_b128 v[36:39], %[xp] offset:28672\n\t" \
    "v_mfma_f32_16x16x32_bf16 v[4:7], v[160:163], v[44:47], v[4:7]\n\t" \
    "ds_read_b128 v[40:43], %[xp] offset:29696\n\t" \
    "v_mfma_f32_16x16x32_bf16 v[8:11], v[152:155], v[48:51], v[8:11]\n\t" \
    "s_waitcnt lgkmcnt(4)\n\t" \
    "v_mfma_f32_16x16x32_bf16 v[12:15], v[160:163], v[48:51], v[12:15]\n\t" \
    "v_mfma_f32_16x16x32_bf16 v[0:3], v[164:167], v[20:23], v[0:3]\n\t" \
    "ds_read_b128 v[44:47], %[xp] offset:30720\n\t" \
    "v_mfma_f32_16x16x32_bf16 v[4:7], v[172:175], v[20:23], v[4:7]\n\t" \
    "ds_read_b128 v[48:51], %[xp] offset:31744\n\t" \
    "v_mfma_f32_16x16x32_bf16 v[8:11], v[164:167], v[24:27], v[8:11]\n\t" \
    "s_waitcnt lgkmcnt(4)\n\t" \
    "v_mfma_f32_16x16x32_bf16 v[12:15], v[172:175], v[24:27], v[12:15]\n\t" \
    "v_mfma_f32_16x16x32_bf16 v[0:3], v[168:171], v[28:31], v[0:3]\n\t" \
    "ds_read_b128 v[20:23], %[xp] offset:32768\n\t" \
    "v_mfma_f32_16x16x32_bf16 v[4:7], v[176:179], v[28:31], v[4:7]\n\t" \
    "ds_read_b128 v[24:27], %[xp] offset:33792\n\t" \
    "v_mfma_f32_16x16x32_bf16 v[8:11], v[168:171], v[32:35], v[8:11]\n\t" \
    "s_waitcnt lgkmcnt(4)\n\t" \
    "v_mfma_f32_16x16x32_bf16 v[12:15], v[176:179], v[32:35], v[12:15]\n\t" \
    "v_mfma_f32_16x16x32_bf16 v[0:3], v[180:183], v[36:39], v[0:3]\n\t" \
    "ds_read_b128 v[28:31], %[xp] offset:34816\n\t" \
    "v_mfma_f32_16x16x32_bf16 v[4:7], v[188:191], v[36:39], v[4:7]\n\t" \
    "ds_read_b128 v[32:35], %[xp] offset:35840\n\t" \
    "v_mfma_f32_16x16x32_bf16 v[8:11], v[180:183], v[40:43], v[8:11]\n\t" \
    "s_waitcnt lgkmcnt(4)\n\t" \
    "v_mfma_f32_16x16x32_bf16 v[12:15], v[188:191], v[40:43], v[12:15]\n\t" \
    "v_mfma_f32_16x16x32_bf16 v[0:3], v[184:187], v[44:47], v[0:3]\n\t" \
    "ds_read_b128 v[36:39], %[xp] offset:36864\n\t" \
    "v_mfma_f32_16x16x32_bf16 v[4:7], v[192:195], v[44:47], v[4:7]\n\t" \
    "ds_read_b128 v[40:43], %[xp] offset:37888\n\t" \
    "v_mfma_f32_16x16x32_bf16 v[8:11], v[184:187], v[48:51], v[8:11]\n\t" \
    "s_waitcnt lgkmcnt(4)\n\t" \
    "v_mfma_f32_16x16x32_bf16 v[12:15], v[192:195], v[48:51], v[12:15]\n\t" \
    "s_add_u32 %[sx], %[sx], 65536\n\t" \
    "s_add_u32 %[t], %[t], 1\n\t" \
    "s_waitcnt vmcnt(4)\n\t" \
    "s_barrier\n\t" \
    "s_setprio 0\n\t" \
    "s_nop 3\n\t" \
    "ds_write_b128 %[pa], v[0:3] offset:0\n\t" \
    "ds_write_b128 %[pa], v[4:7] offset:1024\n\t" \
    "ds_write_b128 %[pa], v[8:11] offset:2048\n\t" \
    "ds_write_b128 %[pa], v[12:15] offset:3072\n\t" \
    "s_sub_u32 %[a], %[t], 1\n\t" \
    "v_mov_b32 v53, %[tag]\n\t" \
    "v_mov_b32 v52, %[a]\n\t" \
    "s_sub_u32 %[b], %[t], 2\n\t" \
    "s_cmp_lt_u32 %[b], " STR(SQUICK) "\n\t" \
    "s_cselect_b64 exec, %[fm], 0\n\t" \
    "buffer_store_dwordx2 v[52:53], %[l16], %[rs], %[fo] offen sc1\n\t" \
    "s_mov_b64 exec, -1\n\t" \
    "s_add_u32 m0, %[sd], 16384\n\t" \
    "s_add_u32 %[a], %[sx], 81920\n\t" \
    "s_waitcnt lgkmcnt(0)\n\t" \
    "buffer_load_dwordx4 %[l16], %[rs], %[a] offen offset:0 sc1 lds\n\t" \
    "buffer_load_dwordx4 %[l16], %[rs], %[a] offen offset:1024 sc1 lds\n\t" \
    "buffer_load_dwordx4 %[l16], %[rs], %[a] offen offset:2048 sc1 lds\n\t" \
    "buffer_load_dwordx4 %[l16], %[rs], %[a] offen offset:3072 sc1 lds\n\t" \
    "v_mfma_f32_16x16x32_bf16 v[196:199], v[68:71], v[20:23], 0\n\t" \
    "ds_read_b128 v[44:47], %[xp] offset:38912\n\t" \
    "v_mfma_f32_16x16x32_bf16 v[200:203], v[76:79], v[20:23], 0\n\t" \
    "ds_read_b128 v[48:51], %[xp] offset:39936\n\t" \
    "v_mfma_f32_16x16x32_bf16 v[204:207], v[68:71], v[24:27], 0\n\t" \
    "v_mfma_f32_16x16x32_bf16 v[208:211], v[76:79], v[24:27], 0\n\t" \
    "v_mfma_f32_16x16x32_bf16 v[196:199], v[72:75], v[28:31], v[196:199]\n\t" \
    "ds_read_b128 v[20:23], %[xp] offset:40960\n\t" \
    "v_mfma_f32_16x16x32_bf16 v[200:203], v[80:83], v[28:31], v[200:203]\n\t" \
    "ds_read_b128 v[24:27], %[xp] offset:41984\n\t" \
    "v_mfma_f32_16x16x32_bf16 v[204:207], v[72:75], v[32:35], v[204:207]\n\t" \
    "v_mfma_f32_16x16x32_bf16 v[208:211], v[80:83], v[32:35], v[208:211]\n\t" \
    "v_mfma_f32_16x16x32_bf16 v[196:199], v[84:87], v[36:39], v[196:199]\n\t" \
    "ds_read_b128 v[28:31], %[xp] offset:43008\n\t" \
    "v_mfma_f32_16x16x32_bf16 v[200:203], v[92:95], v[36:39], v[200:203]\n\t" \
    "ds_read_b128 v[32:35], %[xp] offset:44032\n\t" \
    "v_mfma_f32_16x16x32_bf16 v[204:207], v[84:87], v[40:43], v[204:207]\n\t" \
    "s_waitcnt lgkmcnt(4)\n\t" \
    "v_mfma_f32_16x16x32_bf16 v[208:211], v[92:95], v[40:43], v[208:211]\n\t" \
    "v_mfma_f32_16x16x32_bf16 v[196:199], v[88:91], v[44:47], v[196:199]\n\t" \
    "ds_read_b128 v[36:39], %[xp] offset:45056\n\t" \
    "v_mfma_f32_16x16x32_bf16 v[200:203], v[96:99], v[44:47], v[200:203]\n\t" \
    "ds_read_b128 v[40:43], %[xp] offset:46080\n\t" \
    "v_mfma_f32_16x16x32_bf16 v[204:207], v[88:91], v[48:51], v[204:207]\n\t" \
    "s_waitcnt lgkmcnt(4)\n\t" \
    "v_mfma_f32_16x16x32_bf16 v[208:211], v[96:99], v[48:51], v[208:211]\n\t" \
    "v_mfma_f32_16x16x32_bf16 v[196:199], v[100:103], v[20:23], v[196:199]\n\t" \
    "ds_read_b128 v[44:47], %[xp] offset:47104\n\t" \
    "v_mfma_f32_16x16x32_bf16 v[200:203], v[108:111], v[20:23], v[200:203]\n\t" \
    "ds_read_b128 v[48:51], %[xp] offset:48128\n\t" \
    "v_mfma_f32_16x16x32_bf16 v[204:207], v[100:103], v[24:27], v[204:207]\n\t" \
    "s_waitcnt lgkmcnt(4)\n\t" \
    "v_mfma_f32_16x16x32_bf16 v[208:211], v[108:111], v[24:27], v[208:211]\n\t" \
    "v_mfma_f32_16x16x32_bf16 v[196:199], v[104:107], v[28:31], v[196:199]\n\t" \
    "ds_read_b128 v[20:23], %[xp] offset:49152\n\t" \
    "v_mfma_f32_16x16x32_bf16 v[200:203], v[112:115], v[28:31], v[200:203]\n\t" \
    "ds_read_b128 v[24:27], %[xp] offset:50176\n\t" \
    "v_mfma_f32_16x16x32_bf16 v[204:207], v[104:107], v[32:35], v[204:207]\n\t" \
    "s_waitcnt lgkmcnt(4)\n\t" \
    "v_mfma_f32_16x16x32_bf16 v[208:211], v[112:115], v[32:35], v[208:211]\n\t" \
    "v_mfma_f32_16x16x32_bf16 v[196:199], v[116:119], v[36:39], v[196:199]\n\t" \
    "ds_read_b128 v[28:31], %[xp] offset:51200\n\t" \
    "v_mfma_f32_16x16x32_bf16 v[200:203], v[124:127], v[36:39], v[200:203]\n\t" \
    "ds_read_b128 v[32:35], %[xp] offset:52224\n\t" \
    "v_mfma_f32_16x16x32_bf16 v[204:207], v[116:119], v[40:43], v[204:207]\n\t" \
    "s_waitcnt lgkmcnt(4)\n\t" \
    "v_mfma_f32_16x16x32_bf16 v[208:211], v[124:127], v[40:43], v[208:211]\n\t" \
    "v_mfma_f32_16x16x32_bf16 v[196:199], v[120:123], v[44:47], v[196:199]\n\t" \
    "ds_read_b128 v[36:39], %[xp] offset:53248\n\t" \
    "v_mfma_f32_16x16x32_bf16 v[200:203], v[128:131], v[44:47], v[200:203]\n\t" \
    "ds_read_b128 v[40:43], %[xp] offset:54272\n\t" \
    "v_mfma_f32_16x16x32_bf16 v[204:207], v[120:123], v[48:51], v[204:207]\n\t" \
    "s_waitcnt lgkmcnt(4)\n\t" \
    "v_mfma_f32_16x16x32_bf16 v[208:211], v[128:131], v[48:51], v[208:211]\n\t" \
    "s_waitcnt vmcnt(4)\n\t" \
    "s_barrier\n\t" \
    "s_setprio 0\n\t" \
    "s_sub_u32 %[a], %[t], 1\n\t" \
    "v_mov_b32 v53, %[tag]\n\t" \
    "v_mov_b32 v52, %[a]\n\t" \
    "s_cmp_gt_u32 %[t], 1\n\t" \
    "s_cselect_b64 exec, %[fm], 0\n\t" \
    "buffer_store_dwordx2 v[52:53], %[l16], %[rs], %[fo] offen sc1\n\t" \
    "s_mov_b64 exec, %[pm]\n\t" \
    "s_mov_b32 m0, %[pd]\n\t" \
    "s_add_u32 %[a], %[sx], %[po]\n\t" \
    "buffer_load_dwordx4 %[l16], %[rs], %[a] offen sc1 lds\n\t" \
    "s_mov_b64 exec, -1\n\t" \
    "s_add_u32 m0, %[sd], 32768\n\t" \
    "s_add_u32 %[a], %[sx], 131072\n\t" \
    "buffer_load_dwordx4 %[l16], %[rs], %[a] offen offset:0 sc1 lds\n\t" \
    "buffer_load_dwordx4 %[l16], %[rs], %[a] offen offset:1024 sc1 lds\n\t" \
    "buffer_load_dwordx4 %[l16], %[rs], %[a] offen offset:2048 sc1 lds\n\t" \
    "buffer_load_dwordx4 %[l16], %[rs], %[a] offen offset:3072 sc1 lds\n\t" \
    "v_mfma_f32_16x16x32_bf16 v[196:199], v[132:135], v[20:23], v[196:199]\n\t" \
    "ds_read_b128 v[44:47], %[xp] offset:55296\n\t" \
    "v_mfma_f32_16x16x32_bf16 v[200:203], v[140:143], v[20:23], v[200:203]\n\t" \
    "ds_read_b128 v[48:51], %[xp] offset:56320\n\t" \
    "v_mfma_f32_16x16x32_bf16 v[204:207], v[132:135], v[24:27], v[204:207]\n\t" \
    "s_waitcnt lgkmcnt(4)\n\t" \
    "v_mfma_f32_16x16x32_bf16 v[208:211], v[140:143], v[24:27], v[208:211]\n\t" \
    "v_mfma_f32_16x16x32_bf16 v[196:199], v[136:139], v[28:31], v[196:199]\n\t" \
    "ds_read_b128 v[20:23], %[xp] offset:57344\n\t" \
    "v_mfma_f32_16x16x32_bf16 v[200:203], v[144:147], v[28:31], v[200:203]\n\t" \
    "ds_read_b128 v[24:27], %[xp] offset:58368\n\t" \
    "v_mfma_f32_16x16x32_bf16 v[204:207], v[136:139], v[32:35], v[204:207]\n\t" \
    "s_waitcnt lgkmcnt(4)\n\t" \
    "v_mfma_f32_16x16x32_bf16 v[208:211], v[144:147], v[32:35], v[208:211]\n\t" \
    "v_mfma_f32_16x16x32_bf16 v[196:199], v[148:151], v[36:39], v[196:199]\n\t" \
    "ds_read_b128 v[28:31], %[xp] offset:59392\n\t" \
    "v_mfma_f32_16x16x32_bf16 v[200:203], v[156:159], v[36:39], v[200:203]\n\t" \
    "ds_read_b128 v[32:35], %[xp] offset:60416\n\t" \
    "v_mfma_f32_16x16x32_bf16 v[204:207], v[148:151], v[40:43], v[204:207]\n\t" \
    "s_waitcnt lgkmcnt(4)\n\t" \
    "v_mfma_f32_16x16x32_bf16 v[208:211], v[156:159], v[40:43], v[208:211]\n\t" \
    "v_mfma_f32_16x16x32_bf16 v[196:199], v[152:155], v[44:47], v[196:199]\n\t" \
    "ds_read_b128 v[36:39], %[xp] offset:61440\n\t" \
    "v_mfma_f32_16x16x32_bf16 v[200:203], v[160:163], v[44:47], v[200:203]\n\t" \
    "ds_read_b128 v[40:43], %[xp] offset:62464\n\t" \
    "v_mfma_f32_16x16x32_bf16 v[204:207], v[152:155], v[48:51], v[204:207]\n\t" \
    "s_waitcnt lgkmcnt(4)\n\t" \
    "v_mfma_f32_16x16x32_bf16 v[208:211], v[160:163], v[48:51], v[208:211]\n\t" \
    "v_mfma_f32_16x16x32_bf16 v[196:199], v[164:167], v[20:23], v[196:199]\n\t" \
    "ds_read_b128 v[44:47], %[xp] offset:63488\n\t" \
    "v_mfma_f32_16x16x32_bf16 v[200:203], v[172:175], v[20:23], v[200:203]\n\t" \
    "ds_read_b128 v[48:51], %[xp] offset:64512\n\t" \
    "v_mfma_f32_16x16x32_bf16 v[204:207], v[164:167], v[24:27], v[204:207]\n\t" \
    "s_waitcnt lgkmcnt(4)\n\t" \
    "v_mfma_f32_16x16x32_bf16 v[208:211], v[172:175], v[24:27], v[208:211]\n\t" \
    "v_mfma_f32_16x16x32_bf16 v[196:199], v[168:171], v[28:31], v[196:199]\n\t" \
    "ds_read_b128 v[20:23], %[xp] offset:0\n\t" \
    "v_mfma_f32_16x16x32_bf16 v[200:203], v[176:179], v[28:31], v[200:203]\n\t" \
    "ds_read_b128 v[24:27], %[xp] offset:1024\n\t" \
    "v_mfma_f32_16x16x32_bf16 v[204:207], v[168:171], v[32:35], v[204:207]\n\t" \
    "s_waitcnt lgkmcnt(4)\n\t" \
    "v_mfma_f32_16x16x32_bf16 v[208:211], v[176:179], v[32:35], v[208:211]\n\t" \
    "v_mfma_f32_16x16x32_bf16 v[196:199], v[180:183], v[36:39], v[196:199]\n\t" \
    "ds_read_b128 v[28:31], %[xp] offset:2048\n\t" \
    "v_mfma_f32_16x16x32_bf16 v[200:203], v[188:191], v[36:39], v[200:203]\n\t" \
    "ds_read_b128 v[32:35], %[xp] offset:3072\n\t" \
    "v_mfma_f32_16x16x32_bf16 v[204:207], v[180:183], v[40:43], v[204:207]\n\t" \
    "s_waitcnt lgkmcnt(4)\n\t" \
    "v_mfma_f32_16x16x32_bf16 v[208:211], v[188:191], v[40:43], v[208:211]\n\t" \
    "v_mfma_f32_16x16x32_bf16 v[196:199], v[184:187], v[44:47], v[196:199]\n\t" \
    "ds_read_b128 v[36:39], %[xp] offset:4096\n\t" \
    "v_mfma_f32_16x16x32_bf16 v[200:203], v[192:195], v[44:47], v[200:203]\n\t" \
    "ds_read_b128 v[40:43], %[xp] offset:5120\n\t" \
    "v_mfma_f32_16x16x32_bf16 v[204:207], v[184:187], v[48:51], v[204:207]\n\t" \
    "s_waitcnt lgkmcnt(4)\n\t" \
    "v_mfma_f32_16x16x32_bf16 v[208:211], v[192:195], v[48:51], v[208:211]\n\t" \
    "s_add_u32 %[sx], %[sx], 65536\n\t" \
    "s_add_u32 %[t], %[t], 1\n\t" \
    "s_waitcnt vmcnt(4)\n\t" \
    "s_barrier\n\t" \
    "s_cmp_lt_u32 %[t], %[te]\n\t" \
    "s_cbranch_scc1 L0_%=\n\t" \
    "s_cmp_lg_u32 %[fin], 0\n\t" \
    "s_cbranch_scc0 E_%=\n\t" \
    "s_nop 4\n\t" \
    "ds_write_b128 %[pa], v[196:199] offset:0\n\t" \
    "ds_write_b128 %[pa], v[200:203] offset:1024\n\t" \
    "ds_write_b128 %[pa], v[204:207] offset:2048\n\t" \
    "ds_write_b128 %[pa], v[208:211] offset:3072\n\t" \
    "s_waitcnt lgkmcnt(0)\n\t" \
    "s_barrier\n\t" \
    "s_branch E_%=\n\t" \
    "L1_%=:\n\t" \
    "s_setprio 1\n\t" \
    "s_add_u32 m0, %[sd], 49152\n\t" \
    "s_add_u32 %[a], %[sx], 81920\n\t" \
    "v_mfma_f32_16x16x32_bf16 v[0:3], v[68:71], v[20:23], 0\n\t" \
    "ds_read_b128 v[44:47], %[xp] offset:6144\n\t" \
    "v_mfma_f32_16x16x32_bf16 v[4:7], v[76:79], v[20:23], 0\n\t" \
    "ds_read_b128 v[48:51], %[xp] offset:7168\n\t" \
    "v_mfma_f32_16x16x32_bf16 v[8:11], v[68:71], v[24:27], 0\n\t" \
    "s_waitcnt lgkmcnt(4)\n\t" \
    "v_mfma_f32_16x16x32_bf16 v[12:15], v[76:79], v[24:27], 0\n\t" \
    "v_mfma_f32_16x16x32_bf16 v[0:3], v[72:75], v[28:31], v[0:3]\n\t" \
    "ds_read_b128 v[20:23], %[xp] offset:8192\n\t" \
    "v_mfma_f32_16x16x32_bf16 v[4:7], v[80:83], v[28:31], v[4:7]\n\t" \
    "ds_read_b128 v[24:27], %[xp] offset:9216\n\t" \
    "v_mfma_f32_16x16x32_bf16 v[8:11], v[72:75], v[32:35], v[8:11]\n\t" \
    "s_waitcnt lgkmcnt(4)\n\t" \
    "v_mfma_f32_16x16x32_bf16 v[12:15], v[80:83], v[32:35], v[12:15]\n\t" \
    "v_mfma_f32_16x16x32_bf16 v[0:3], v[84:87], v[36:39], v[0:3]\n\t" \
    "ds_read_b128 v[28:31], %[xp] offset:10240\n\t" \
    "v_mfma_f32_16x16x32_bf16 v[4:7], v[92:95], v[36:39], v[4:7]\n\t" \
    "ds_read_b128 v[32:35], %[xp] offset:11264\n\t" \
    "v_mfma_f32_16x16x32_bf16 v[8:11], v[84:87], v[40:43], v[8:11]\n\t" \
    "s_waitcnt lgkmcnt(4)\n\t" \
    "v_mfma_f32_16x16x32_bf16 v[12:15], v[92:95], v[40:43], v[12:15]\n\t" \
    "v_mfma_f32_16x16x32_bf16 v[0:3], v[88:91], v[44:47], v[0:3]\n\t" \
    "ds_read_b128 v[36:39], %[xp] offset:12288\n\t" \
    "v_mfma_f32_16x16x32_bf16 v[4:7], v[96:99], v[44:47], v[4:7]\n\t" \
    "ds_read_b128 v[40:43], %[xp] offset:13312\n\t" \
    "v_mfma_f32_16x16x32_bf16 v[8:11], v[88:91], v[48:51], v[8:11]\n\t" \
    "s_waitcnt lgkmcnt(4)\n\t" \
    "v_mfma_f32_16x16x32_bf16 v[12:15], v[96:99], v[48:51], v[12:15]\n\t" \
    "v_mfma_f32_16x16x32_bf16 v[0:3], v[100:103], v[20:23], v[0:3]\n\t" \
    "ds_read_b128 v[44:47], %[xp] offset:14336\n\t" \
    "v_mfma_f32_16x16x32_bf16 v[4:7], v[108:111], v[20:23], v[4:7]\n\t" \
    "ds_read_b128 v[48:51], %[xp] offset:15360\n\t" \
    "v_mfma_f32_16x16x32_bf16 v[8:11], v[100:103], v[24:27], v[8:11]\n\t" \
    "s_waitcnt lgkmcnt(4)\n\t" \
    "v_mfma_f32_16x16x32_bf16 v[12:15], v[108:111], v[24:27], v[12:15]\n\t" \
    "buffer_load_dwordx4 %[l16], %[rs], %[a] offen offset:0 sc1 lds\n\t" \
    "v_mfma_f32_16x16x32_bf16 v[0:3], v[104:107], v[28:31], v[0:3]\n\t" \
    "ds_read_b128 v[20:23], %[xp] offset:16384\n\t" \
    "v_mfma_f32_16x16x32_bf16 v[4:7], v[112:115], v[28:31], v[4:7]\n\t" \
    "ds_read_b128 v[24:27], %[xp] offset:17408\n\t" \
    "v_mfma_f32_16x16x32_bf16 v[8:11], v[104:107], v[32:35], v[8:11]\n\t" \
    "s_waitcnt lgkmcnt(4)\n\t" \
    "v_mfma_f32_16x16x32_bf16 v[12:15], v[112:115], v[32:35], v[12:15]\n\t" \
    "buffer_load_dwordx4 %[l16], %[rs], %[a] offen offset:1024 sc1 lds\n\t" \
    "v_mfma_f32_16x16x32_bf16 v[0:3], v[116:119], v[36:39], v[0:3]\n\t" \
    "ds_read_b128 v[28:31], %[xp] offset:18432\n\t" \
    "v_mfma_f32_16x16x32_bf16 v[4:7], v[124:127], v[36:39], v[4:7]\n\t" \
    "ds_read_b128 v[32:35], %[xp] offset:19456\n\t" \
    "v_mfma_f32_16x16x32_bf16 v[8:11], v[116:119], v[40:43], v[8:11]\n\t" \
    "s_waitcnt lgkmcnt(4)\n\t" \
    "v_mfma_f32_16x16x32_bf16 v[12:15], v[124:127], v[40:43], v[12:15]\n\t" \
    "buffer_load_dwordx4 %[l16], %[rs], %[a] offen offset:2048 sc1 lds\n\t" \
    "v_mfma_f32_16x16x32_bf16 v[0:3], v[120:123], v[44:47], v[0:3]\n\t" \
    "ds_read_b128 v[36:39], %[xp] offset:20480\n\t" \
    "v_mfma_f32_16x16x32_bf16 v[4:7], v[128:131], v[44:47], v[4:7]\n\t" \
    "ds_read_b128 v[40:43], %[xp] offset:21504\n\t" \
    "v_mfma_f32_16x16x32_bf16 v[8:11], v[120:123], v[48:51], v[8:11]\n\t" \
    "s_waitcnt lgkmcnt(4)\n\t" \
    "v_mfma_f32_16x16x32_bf16 v[12:15], v[128:131], v[48:51], v[12:15]\n\t" \
    "buffer_load_dwordx4 %[l16], %[rs], %[a] offen offset:3072 sc1 lds\n\t" \
    "s_waitcnt vmcnt(4)\n\t" \
    "s_barrier\n\t" \
    "s_setprio 1\n\t" \
    "s_add_u32 m0, %[sd], 0\n\t" \
    "s_add_u32 %[a], %[sx], 131072\n\t" \
    "s_add_u32 %[b], %[oo], 0x1000\n\t" \
    "s_cmp_lg_u32 %[t], 1\n\t" \
    "s_cselect_b64 %[em], -1, 0\n\t" \
    "v_mfma_f32_16x16x32_bf16 v[0:3], v[132:135], v[20:23], v[0:3]\n\t" \
    "ds_read_b128 v[44:47], %[xp] offset:22528\n\t" \
    "v_mfma_f32_16x16x32_bf16 v[4:7], v[140:143], v[20:23], v[4:7]\n\t" \
    "ds_read_b128 v[48:51], %[xp] offset:23552\n\t" \
    "v_mfma_f32_16x16x32_bf16 v[8:11], v[132:135], v[24:27], v[8:11]\n\t" \
    "ds_read_b128 v[52:55], %[pa] offset:0\n\t" \
    "s_waitcnt lgkmcnt(5)\n\t" \
    "v_mfma_f32_16x16x32_bf16 v[12:15], v[140:143], v[24:27], v[12:15]\n\t" \
    "buffer_load_dwordx4 %[l16], %[rs], %[a] offen offset:0 sc1 lds\n\t" \
    "v_mfma_f32_16x16x32_bf16 v[0:3], v[136:139], v[28:31], v[0:3]\n\t" \
    "ds_read_b128 v[20:23], %[xp] offset:24576\n\t" \
    "v_mfma_f32_16x16x32_bf16 v[4:7], v[144:147], v[28:31], v[4:7]\n\t" \
    "ds_read_b128 v[24:27], %[xp] offset:25600\n\t" \
    "v_mfma_f32_16x16x32_bf16 v[8:11], v[136:139], v[32:35], v[8:11]\n\t" \
    "ds_read_b128 v[56:59], %[pa] offset:1024\n\t" \
    "s_waitcnt lgkmcnt(6)\n\t" \
    "v_mfma_f32_16x16x32_bf16 v[12:15], v[144:147], v[32:35], v[12:15]\n\t" \
    "buffer_load_dwordx4 %[l16], %[rs], %[a] offen offset:1024 sc1 lds\n\t" \
    "v_mfma_f32_16x16x32_bf16 v[0:3], v[148:151], v[36:39], v[0:3]\n\t" \
    "ds_read_b128 v[28:31], %[xp] offset:26624\n\t" \
    "v_mfma_f32_16x16x32_bf16 v[4:7], v[156:159], v[36:39], v[4:7]\n\t" \
    "ds_read_b128 v[32:35], %[xp] offset:27648\n\t" \
    "v_mfma_f32_16x16x32_bf16 v[8:11], v[148:151], v[40:43], v[8:11]\n\t" \
    "ds_read_b128 v[60:63], %[pa] offset:2048\n\t" \
    "s_waitcnt lgkmcnt(7)\n\t" \
    "v_mfma_f32_16x16x32_bf16 v[12:15], v[156:159], v[40:43], v[12:15]\n\t" \
    "buffer_load_dwordx4 %[l16], %[rs], %[a] offen offset:2048 sc1 lds\n\t" \
    "v_mfma_f32_16x16x32_bf16 v[0:3], v[152:155], v[44:47], v[0:3]\n\t" \
    "ds_read_b128 v[36:39], %[xp] offset:28672\n\t" \
    "s_waitcnt lgkmcnt(7)\n\t" \
    "v_add_f32 v196, v196, v52\n\t" \
    "v_mfma_f32_16x16x32_bf16 v[4:7], v[160:163], v[44:47], v[4:7]\n\t" \
    "ds_read_b128 v[40:43], %[xp] offset:29696\n\t" \
    "v_add_f32 v197, v197, v53\n\t" \
    "v_mfma_f32_16x16x32_bf16 v[8:11], v[152:155], v[48:51], v[8:11]\n\t" \
    "ds_read_b128 v[64:67], %[pa] offset:3072\n\t" \
    "s_waitcnt lgkmcnt(7)\n\t" \
    "v_mfma_f32_16x16x32_bf16 v[12:15], v[160:163], v[48:51], v[12:15]\n\t" \
    "buffer_load_dwordx4 %[l16], %[rs], %[a] offen offset:3072 sc1 lds\n\t" \
    "v_add_f32 v198, v198, v54\n\t" \
    "v_add_f32 v199, v199, v55\n\t" \
    "v_mfma_f32_16x16x32_bf16 v[0:3], v[164:167], v[20:23], v[0:3]\n\t" \
    "ds_read_b128 v[44:47], %[xp] offset:30720\n\t" \
    "s_waitcnt lgkmcnt(7)\n\t" \
    "v_add_f32 v200, v200, v56\n\t" \
    "v_mfma_f32_16x16x32_bf16 v[4:7], v[172:175], v[20:23], v[4:7]\n\t" \
    "ds_read_b128 v[48:51], %[xp] offset:31744\n\t" \
    "v_add_f32 v201, v201, v57\n\t" \
    "v_mfma_f32_16x16x32_bf16 v[8:11], v[164:167], v[24:27], v[8:11]\n\t" \
    "v_add_f32 v202, v202, v58\n\t" \
    "s_waitcnt lgkmcnt(6)\n\t" \
    "v_mfma_f32_16x16x32_bf16 v[12:15], v[172:175], v[24:27], v[12:15]\n\t" \
    "v_add_f32 v203, v203, v59\n\t" \
    "s_waitcnt lgkmcnt(5)\n\t" \
    "v_add_f32 v204, v204, v60\n\t" \
    "v_add_f32 v205, v205, v61\n\t" \
    "v_mfma_f32_16x16x32_bf16 v[0:3], v[168:171], v[28:31], v[0:3]\n\t" \
    "ds_read_b128 v[20:23], %[xp] offset:32768\n\t" \
    "v_add_f32 v206, v206, v62\n\t" \
    "v_mfma_f32_16x16x32_bf16 v[4:7], v[176:179], v[28:31], v[4:7]\n\t" \
    "ds_read_b128 v[24:27], %[xp] offset:33792\n\t" \
    "v_add_f32 v207, v207, v63\n\t" \
    "v_mfma_f32_16x16x32_bf16 v[8:11], v[168:171], v[32:35], v[8:11]\n\t" \
    "s_waitcnt lgkmcnt(4)\n\t" \
    "v_add_f32 v208, v208, v64\n\t" \
    "v_mfma_f32_16x16x32_bf16 v[12:15], v[176:179], v[32:35], v[12:15]\n\t" \
    "v_add_f32 v209, v209, v65\n\t" \
    "v_add_f32 v210, v210, v66\n\t" \
    "v_add_f32 v211, v211, v67\n\t" \
    "v_mfma_f32_16x16x32_bf16 v[0:3], v[180:183], v[36:39], v[0:3]\n\t" \
    "ds_read_b128 v[28:31], %[xp] offset:34816\n\t" \
    "v_cvt_pk_bf16_f32 v52, v196, v200\n\t" \
    "v_mfma_f32_16x16x32_bf16 v[4:7], v[188:191], v[36:39], v[4:7]\n\t" \
    "ds_read_b128 v[32:35], %[xp] offset:35840\n\t" \
    "v_cvt_pk_bf16_f32 v53, v197, v201\n\t" \
    "v_mfma_f32_16x16x32_bf16 v[8:11], v[180:183], v[40:43], v[8:11]\n\t" \
    "v_cvt_pk_bf16_f32 v54, v198, v202\n\t" \
    "s_waitcnt lgkmcnt(4)\n\t" \
    "v_mfma_f32_16x16x32_bf16 v[12:15], v[188:191], v[40:43], v[12:15]\n\t" \
    "v_cvt_pk_bf16_f32 v55, v199, v203\n\t" \
    "v_cvt_pk_bf16_f32 v56, v204, v208\n\t" \
    "v_cvt_pk_bf16_f32 v57, v205, v209\n\t" \
    "v_mfma_f32_16x16x32_bf16 v[0:3], v[184:187], v[44:47], v[0:3]\n\t" \
    "ds_read_b128 v[36:39], %[xp] offset:36864\n\t" \
    "v_cvt_pk_bf16_f32 v58, v206, v210\n\t" \
    "v_mfma_f32_16x16x32_bf16 v[4:7], v[192:195], v[44:47], v[4:7]\n\t" \
    "ds_read_b128 v[40:43], %[xp] offset:37888\n\t" \
    "v_cvt_pk_bf16_f32 v59, v207, v211\n\t" \
    "v_mfma_f32_16x16x32_bf16 v[8:11], v[184:187], v[48:51], v[8:11]\n\t" \
    "s_waitcnt lgkmcnt(4)\n\t" \
    "v_mfma_f32_16x16x32_bf16 v[12:15], v[192:195], v[48:51], v[12:15]\n\t" \
    "s_mov_b64 exec, %[em]\n\t" \
    "buffer_store_dwordx4 v[52:55], %[l16], %[rs], %[oo] offen sc1\n\t" \
    "buffer_store_dwordx4 v[56:59], %[l16], %[rs], %[b] offen sc1\n\t" \
    "s_mov_b64 exec, -1\n\t" \
    "s_add_u32 %[sx], %[sx], 65536\n\t" \
    "s_add_u32 %[t], %[t], 1\n\t" \
    "s_add_u32 %[oo], %[oo], 262144\n\t" \
    "s_waitcnt vmcnt(6)\n\t" \
    "s_cmp_gt_u32 %[t], " STR(SQUICK) " + 1\n\t" \
    "s_cbranch_scc1 B0_%=\n\t" \
    "s_waitcnt vmcnt(0)\n\t" \
    "B0_%=:\n\t" \
    "s_barrier\n\t" \
    "s_setprio 1\n\t" \
    "s_add_u32 m0, %[sd], 16384\n\t" \
    "s_add_u32 %[a], %[sx], 81920\n\t" \
    "v_mfma_f32_16x16x32_bf16 v[196:199], v[68:71], v[20:23], 0\n\t" \
    "ds_read_b128 v[44:47], %[xp] offset:38912\n\t" \
    "v_mfma_f32_16x16x32_bf16 v[200:203], v[76:79], v[20:23], 0\n\t" \
    "ds_read_b128 v[48:51], %[xp] offset:39936\n\t" \
    "v_mfma_f32_16x16x32_bf16 v[204:207], v[68:71], v[24:27], 0\n\t" \
    "s_waitcnt lgkmcnt(4)\n\t" \
    "v_mfma_f32_16x16x32_bf16 v[208:211], v[76:79], v[24:27], 0\n\t" \
    "v_mfma_f32_16x16x32_bf16 v[196:199], v[72:75], v[28:31], v[196:199]\n\t" \
    "ds_read_b128 v[20:23], %[xp] offset:40960\n\t" \
    "v_mfma_f32_16x16x32_bf16 v[200:203], v[80:83], v[28:31], v[200:203]\n\t" \
    "ds_read_b128 v[24:27], %[xp] offset:41984\n\t" \
    "v_mfma_f32_16x16x32_bf16 v[204:207], v[72:75], v[32:35], v[204:207]\n\t" \
    "s_waitcnt lgkmcnt(4)\n\t" \
    "v_mfma_f32_16x16x32_bf16 v[208:211], v[80:83], v[32:35], v[208:211]\n\t" \
    "v_mfma_f32_16x16x32_bf16 v[196:199], v[84:87], v[36:39], v[196:199]\n\t" \
    "ds_read_b128 v[28:31], %[xp] offset:43008\n\t" \
    "v_mfma_f32_16x16x32_bf16 v[200:203], v[92:95], v[36:39], v[200:203]\n\t" \
    "ds_read_b128 v[32:35], %[xp] offset:44032\n\t" \
    "v_mfma_f32_16x16x32_bf16 v[204:207], v[84:87], v[40:43], v[204:207]\n\t" \
    "s_waitcnt lgkmcnt(4)\n\t" \
    "v_mfma_f32_16x16x32_bf16 v[208:211], v[92:95], v[40:43], v[208:211]\n\t" \
    "v_mfma_f32_16x16x32_bf16 v[196:199], v[88:91], v[44:47], v[196:199]\n\t" \
    "ds_read_b128 v[36:39], %[xp] offset:45056\n\t" \
    "v_mfma_f32_16x16x32_bf16 v[200:203], v[96:99], v[44:47], v[200:203]\n\t" \
    "ds_read_b128 v[40:43], %[xp] offset:46080\n\t" \
    "v_mfma_f32_16x16x32_bf16 v[204:207], v[88:91], v[48:51], v[204:207]\n\t" \
    "s_waitcnt lgkmcnt(4)\n\t" \
    "v_mfma_f32_16x16x32_bf16 v[208:211], v[96:99], v[48:51], v[208:211]\n\t" \
    "v_mfma_f32_16x16x32_bf16 v[196:199], v[100:103], v[20:23], v[196:199]\n\t" \
    "ds_read_b128 v[44:47], %[xp] offset:47104\n\t" \
    "v_mfma_f32_16x16x32_bf16 v[200:203], v[108:111], v[20:23], v[200:203]\n\t" \
    "ds_read_b128 v[48:51], %[xp] offset:48128\n\t" \
    "v_mfma_f32_16x16x32_bf16 v[204:207], v[100:103], v[24:27], v[204:207]\n\t" \
    "s_waitcnt lgkmcnt(4)\n\t" \
    "v_mfma_f32_16x16x32_bf16 v[208:211], v[108:111], v[24:27], v[208:211]\n\t" \
    "buffer_load_dwordx4 %[l16], %[rs], %[a] offen offset:0 sc1 lds\n\t" \
    "v_mfma_f32_16x16x32_bf16 v[196:199], v[104:107], v[28:31], v[196:199]\n\t" \
    "ds_read_b128 v[20:23], %[xp] offset:49152\n\t" \
    "v_mfma_f32_16x16x32_bf16 v[200:203], v[112:115], v[28:31], v[200:203]\n\t" \
    "ds_read_b128 v[24:27], %[xp] offset:50176\n\t" \
    "v_mfma_f32_16x16x32_bf16 v[204:207], v[104:107], v[32:35], v[204:207]\n\t" \
    "s_waitcnt lgkmcnt(4)\n\t" \
    "v_mfma_f32_16x16x32_bf16 v[208:211], v[112:115], v[32:35], v[208:211]\n\t" \
    "buffer_load_dwordx4 %[l16], %[rs], %[a] offen offset:1024 sc1 lds\n\t" \
    "v_mfma_f32_16x16x32_bf16 v[196:199], v[116:119], v[36:39], v[196:199]\n\t" \
    "ds_read_b128 v[28:31], %[xp] offset:51200\n\t" \
    "v_mfma_f32_16x16x32_bf16 v[200:203], v[124:127], v[36:39], v[200:203]\n\t" \
    "ds_read_b128 v[32:35], %[xp] offset:52224\n\t" \
    "v_mfma_f32_16x16x32_bf16 v[204:207], v[116:119], v[40:43], v[204:207]\n\t" \
    "s_waitcnt lgkmcnt(4)\n\t" \
    "v_mfma_f32_16x16x32_bf16 v[208:211], v[124:127], v[40:43], v[208:211]\n\t" \
    "buffer_load_dwordx4 %[l16], %[rs], %[a] offen offset:2048 sc1 lds\n\t" \
    "v_mfma_f32_16x16x32_bf16 v[196:199], v[120:123], v[44:47], v[196:199]\n\t" \
    "ds_read_b128 v[36:39], %[xp] offset:53248\n\t" \
    "v_mfma_f32_16x16x32_bf16 v[200:203], v[128:131], v[44:47], v[200:203]\n\t" \
    "ds_read_b128 v[40:43], %[xp] offset:54272\n\t" \
    "v_mfma_f32_16x16x32_bf16 v[204:207], v[120:123], v[48:51], v[204:207]\n\t" \
    "s_waitcnt lgkmcnt(4)\n\t" \
    "v_mfma_f32_16x16x32_bf16 v[208:211], v[128:131], v[48:51], v[208:211]\n\t" \
    "buffer_load_dwordx4 %[l16], %[rs], %[a] offen offset:3072 sc1 lds\n\t" \
    "s_waitcnt vmcnt(4)\n\t" \
    "s_barrier\n\t" \
    "s_setprio 1\n\t" \
    "s_add_u32 m0, %[sd], 32768\n\t" \
    "s_add_u32 %[a], %[sx], 131072\n\t" \
    "s_add_u32 %[b], %[oo], 0x1000\n\t" \
    "s_cmp_lg_u32 %[t], 1\n\t" \
    "s_cselect_b64 %[em], -1, 0\n\t" \
    "v_mfma_f32_16x16x32_bf16 v[196:199], v[132:135], v[20:23], v[196:199]\n\t" \
    "ds_read_b128 v[44:47], %[xp] offset:55296\n\t" \
    "v_mfma_f32_16x16x32_bf16 v[200:203], v[140:143], v[20:23], v[200:203]\n\t" \
    "ds_read_b128 v[48:51], %[xp] offset:56320\n\t" \
    "v_mfma_f32_16x16x32_bf16 v[204:207], v[132:135], v[24:27], v[204:207]\n\t" \
    "ds_read_b128 v[52:55], %[pa] offset:0\n\t" \
    "s_waitcnt lgkmcnt(5)\n\t" \
    "v_mfma_f32_16x16x32_bf16 v[208:211], v[140:143], v[24:27], v[208:211]\n\t" \
    "buffer_load_dwordx4 %[l16], %[rs], %[a] offen offset:0 sc1 lds\n\t" \
    "v_mfma_f32_16x16x32_bf16 v[196:199], v[136:139], v[28:31], v[196:199]\n\t" \
    "ds_read_b128 v[20:23], %[xp] offset:57344\n\t" \
    "v_mfma_f32_16x16x32_bf16 v[200:203], v[144:147], v[28:31], v[200:203]\n\t" \
    "ds_read_b128 v[24:27], %[xp] offset:58368\n\t" \
    "v_mfma_f32_16x16x32_bf16 v[204:207], v[136:139], v[32:35], v[204:207]\n\t" \
    "ds_read_b128 v[56:59], %[pa] offset:1024\n\t" \
    "s_waitcnt lgkmcnt(6)\n\t" \
    "v_mfma_f32_16x16x32_bf16 v[208:211], v[144:147], v[32:35], v[208:211]\n\t" \
    "buffer_load_dwordx4 %[l16], %[rs], %[a] offen offset:1024 sc1 lds\n\t" \
    "v_mfma_f32_16x16x32_bf16 v[196:199], v[148:151], v[36:39], v[196:199]\n\t" \
    "ds_read_b128 v[28:31], %[xp] offset:59392\n\t" \
    "v_mfma_f32_16x16x32_bf16 v[200:203], v[156:159], v[36:39], v[200:203]\n\t" \
    "ds_read_b128 v[32:35], %[xp] offset:60416\n\t" \
    "v_mfma_f32_16x16x32_bf16 v[204:207], v[148:151], v[40:43], v[204:207]\n\t" \
    "ds_read_b128 v[60:63], %[pa] offset:2048\n\t" \
    "s_waitcnt lgkmcnt(7)\n\t" \
    "v_mfma_f32_16x16x32_bf16 v[208:211], v[156:159], v[40:43], v[208:211]\n\t" \
    "buffer_load_dwordx4 %[l16], %[rs], %[a] offen offset:2048 sc1 lds\n\t" \
    "v_mfma_f32_16x16x32_bf16 v[196:199], v[152:155], v[44:47], v[196:199]\n\t" \
    "ds_read_b128 v[36:39], %[xp] offset:61440\n\t" \
    "s_waitcnt lgkmcnt(7)\n\t" \
    "v_add_f32 v0, v0, v52\n\t" \
    "v_mfma_f32_16x16x32_bf16 v[200:203], v[160:163], v[44:47], v[200:203]\n\t" \
    "ds_read_b128 v[40:43], %[xp] offset:62464\n\t" \
    "v_add_f32 v1, v1, v53\n\t" \
    "v_mfma_f32_16x16x32_bf16 v[204:207], v[152:155], v[48:51], v[204:207]\n\t" \
    "ds_read_b128 v[64:67], %[pa] offset:3072\n\t" \
    "s_waitcnt lgkmcnt(7)\n\t" \
    "v_mfma_f32_16x16x32_bf16 v[208:211], v[160:163], v[48:51], v[208:211]\n\t" \
    "buffer_load_dwordx4 %[l16], %[rs], %[a] offen offset:3072 sc1 lds\n\t" \
    "v_add_f32 v2, v2, v54\n\t" \
    "v_add_f32 v3, v3, v55\n\t" \
    "v_mfma_f32_16x16x32_bf16 v[196:199], v[164:167], v[20:23], v[196:199]\n\t" \
    "ds_read_b128 v[44:47], %[xp] offset:63488\n\t" \
    "s_waitcnt lgkmcnt(7)\n\t" \
    "v_add_f32 v4, v4, v56\n\t" \
    "v_mfma_f32_16x16x32_bf16 v[200:203], v[172:175], v[20:23], v[200:203]\n\t" \
    "ds_read_b128 v[48:51], %[xp] offset:64512\n\t" \
    "v_add_f32 v5, v5, v57\n\t" \
    "v_mfma_f32_16x16x32_bf16 v[204:207], v[164:167], v[24:27], v[204:207]\n\t" \
    "v_add_f32 v6, v6, v58\n\t" \
    "s_waitcnt lgkmcnt(6)\n\t" \
    "v_mfma_f32_16x16x32_bf16 v[208:211], v[172:175], v[24:27], v[208:211]\n\t" \
    "v_add_f32 v7, v7, v59\n\t" \
    "s_waitcnt lgkmcnt(5)\n\t" \
    "v_add_f32 v8, v8, v60\n\t" \
    "v_add_f32 v9, v9, v61\n\t" \
    "v_mfma_f32_16x16x32_bf16 v[196:199], v[168:171], v[28:31], v[196:199]\n\t" \
    "ds_read_b128 v[20:23], %[xp] offset:0\n\t" \
    "v_add_f32 v10, v10, v62\n\t" \
    "v_mfma_f32_16x16x32_bf16 v[200:203], v[176:179], v[28:31], v[200:203]\n\t" \
    "ds_read_b128 v[24:27], %[xp] offset:1024\n\t" \
    "v_add_f32 v11, v11, v63\n\t" \
    "v_mfma_f32_16x16x32_bf16 v[204:207], v[168:171], v[32:35], v[204:207]\n\t" \
    "s_waitcnt lgkmcnt(4)\n\t" \
    "v_add_f32 v12, v12, v64\n\t" \
    "v_mfma_f32_16x16x32_bf16 v[208:211], v[176:179], v[32:35], v[208:211]\n\t" \
    "v_add_f32 v13, v13, v65\n\t" \
    "v_add_f32 v14, v14, v66\n\t" \
    "v_add_f32 v15, v15, v67\n\t" \
    "v_mfma_f32_16x16x32_bf16 v[196:199], v[180:183], v[36:39], v[196:199]\n\t" \
    "ds_read_b128 v[28:31], %[xp] offset:2048\n\t" \
    "v_cvt_pk_bf16_f32 v52, v0, v4\n\t" \
    "v_mfma_f32_16x16x32_bf16 v[200:203], v[188:191], v[36:39], v[200:203]\n\t" \
    "ds_read_b128 v[32:35], %[xp] offset:3072\n\t" \
    "v_cvt_pk_bf16_f32 v53, v1, v5\n\t" \
    "v_mfma_f32_16x16x32_bf16 v[204:207], v[180:183], v[40:43], v[204:207]\n\t" \
    "v_cvt_pk_bf16_f32 v54, v2, v6\n\t" \
    "s_waitcnt lgkmcnt(4)\n\t" \
    "v_mfma_f32_16x16x32_bf16 v[208:211], v[188:191], v[40:43], v[208:211]\n\t" \
    "v_cvt_pk_bf16_f32 v55, v3, v7\n\t" \
    "v_cvt_pk_bf16_f32 v56, v8, v12\n\t" \
    "v_cvt_pk_bf16_f32 v57, v9, v13\n\t" \
    "v_mfma_f32_16x16x32_bf16 v[196:199], v[184:187], v[44:47], v[196:199]\n\t" \
    "ds_read_b128 v[36:39], %[xp] offset:4096\n\t" \
    "v_cvt_pk_bf16_f32 v58, v10, v14\n\t" \
    "v_mfma_f32_16x16x32_bf16 v[200:203], v[192:195], v[44:47], v[200:203]\n\t" \
    "ds_read_b128 v[40:43], %[xp] offset:5120\n\t" \
    "v_cvt_pk_bf16_f32 v59, v11, v15\n\t" \
    "v_mfma_f32_16x16x32_bf16 v[204:207], v[184:187], v[48:51], v[204:207]\n\t" \
    "s_waitcnt lgkmcnt(4)\n\t" \
    "v_mfma_f32_16x16x32_bf16 v[208:211], v[192:195], v[48:51], v[208:211]\n\t" \
    "s_mov_b64 exec, %[em]\n\t" \
    "buffer_store_dwordx4 v[52:55], %[l16], %[rs], %[oo] offen sc1\n\t" \
    "buffer_store_dwordx4 v[56:59], %[l16], %[rs], %[b] offen sc1\n\t" \
    "s_mov_b64 exec, -1\n\t" \
    "s_add_u32 %[sx], %[sx], 65536\n\t" \
    "s_add_u32 %[t], %[t], 1\n\t" \
    "s_add_u32 %[oo], %[oo], 262144\n\t" \
    "s_waitcnt vmcnt(6)\n\t" \
    "s_cmp_gt_u32 %[t], " STR(SQUICK) " + 1\n\t" \
    "s_cbranch_scc1 B1_%=\n\t" \
    "s_waitcnt vmcnt(0)\n\t" \
    "B1_%=:\n\t" \
    "s_barrier\n\t" \
    "s_cmp_lt_u32 %[t], %[te]\n\t" \
    "s_cbranch_scc1 L1_%=\n\t" \
    "s_cmp_lg_u32 %[fin], 0\n\t" \
    "s_cbranch_scc0 E_%=\n\t" \
    "s_nop 4\n\t" \
    "s_barrier\n\t" \
    "ds_read_b128 v[52:55], %[pa] offset:0\n\t" \
    "ds_read_b128 v[56:59], %[pa] offset:1024\n\t" \
    "ds_read_b128 v[60:63], %[pa] offset:2048\n\t" \
    "ds_read_b128 v[64:67], %[pa] offset:3072\n\t" \
    "s_add_u32 %[b], %[oo], 0x1000\n\t" \
    "s_waitcnt lgkmcnt(0)\n\t" \
    "v_add_f32 v196, v196, v52\n\t" \
    "v_add_f32 v197, v197, v53\n\t" \
    "v_add_f32 v198, v198, v54\n\t" \
    "v_add_f32 v199, v199, v55\n\t" \
    "v_add_f32 v200, v200, v56\n\t" \
    "v_add_f32 v201, v201, v57\n\t" \
    "v_add_f32 v202, v202, v58\n\t" \
    "v_add_f32 v203, v203, v59\n\t" \
    "v_add_f32 v204, v204, v60\n\t" \
    "v_add_f32 v205, v205, v61\n\t" \
    "v_add_f32 v206, v206, v62\n\t" \
    "v_add_f32 v207, v207, v63\n\t" \
    "v_add_f32 v208, v208, v64\n\t" \
    "v_add_f32 v209, v209, v65\n\t" \
    "v_add_f32 v210, v210, v66\n\t" \
    "v_add_f32 v211, v211, v67\n\t" \
    "v_cvt_pk_bf16_f32 v52, v196, v200\n\t" \
    "v_cvt_pk_bf16_f32 v53, v197, v201\n\t" \
    "v_cvt_pk_bf16_f32 v54, v198, v202\n\t" \
    "v_cvt_pk_bf16_f32 v55, v199, v203\n\t" \
    "v_cvt_pk_bf16_f32 v56, v204, v208\n\t" \
    "v_cvt_pk_bf16_f32 v57, v205, v209\n\t" \
    "v_cvt_pk_bf16_f32 v58, v206, v210\n\t" \
    "v_cvt_pk_bf16_f32 v59, v207, v211\n\t" \
    "buffer_store_dwordx4 v[52:55], %[l16], %[rs], %[oo] offen sc1\n\t" \
    "buffer_store_dwordx4 v[56:59], %[l16], %[rs], %[b] offen sc1\n\t" \
    "E_%=:\n\t" \
    "s_setprio 0\n\t" \
    "s_waitcnt lgkmcnt(0)\n\t" \
    "s_nop 7\n\t" \
    : [t] "+s"(t), [sx] "+s"(sx), [oo] "+s"(oo), [a] "=&s"(tmp), [b] "=&s"(tmp2), [em] "=&s"(em), "+{v[20:27]}"(xf[0]), "+{v[28:35]}"(xf[1]), "+{v[36:43]}"(xf[2]), "+{v[196:203]}"(ac[0]), "+{v[204:211]}"(ac[1]) \
    : [te] "s"(TE_), [fin] "s"(FIN_), [kh] "s"(kh), [sd] "s"(sd), [rs] "s"(rs), [l16] "v"(l16), [xp] "v"(xp), [pa] "v"(pa), [fm] "s"(fm), [pm] "s"(pm), [fo] "s"(fo), [po] "s"(po), [pd] "s"(pd), [tag] "s"(P.tag), "{v[68:75]}"(w[0][0]), "{v[76:83]}"(w[0][1]), "{v[84:91]}"(w[1][0]), "{v[92:99]}"(w[1][1]), "{v[100:107]}"(w[2][0]), "{v[108:115]}"(w[2][1]), "{v[116:123]}"(w[3][0]), "{v[124:131]}"(w[3][1]), "{v[132:139]}"(w[4][0]), "{v[140:147]}"(w[4][1]), "{v[148:155]}"(w[5][0]), "{v[156:163]}"(w[5][1]), "{v[164:171]}"(w[6][0]), "{v[172:179]}"(w[6][1]), "{v[180:187]}"(w[7][0]), "{v[188:195]}"(w[7][1]) \
    : "memory", "scc", "v0", "v1", "v2", "v3", "v4", "v5", "v6", "v7", "v8", "v9", "v10", "v11", "v12", "v13", "v14", "v15", "v44", "v45", "v46", "v47", "v48", "v49", "v50", "v51", "v52", "v53", "v54", "v55", "v56", "v57", "v58", "v59", "v60", "v61", "v62", "v63", "v64", "v65", "v66", "v67")

#define S_LOOP_V(TE_, FIN_) \
  asm volatile( \
    "s_cmp_eq_u32 %[kh], 0\n\t" \
    "s_cbranch_scc0 L1_%=\n\t" \
    "L0_%=:\n\t" \
    "s_setprio 0\n\t" \
    "s_nop 3\n\t" \
    "ds_write_b128 %[pa], v[196:199] offset:0\n\t" \
    "ds_write_b128 %[pa], v[200:203] offset:1024\n\t" \
    "ds_write_b128 %[pa], v[204:207] offset:2048\n\t" \
    "ds_write_b128 %[pa], v[208:211] offset:3072\n\t" \
    "s_sub_u32 %[a], %[t], 1\n\t" \
    "v_mov_b32 v53, %[tag]\n\t" \
    "v_mov_b32 v52, %[a]\n\t" \
    "s_sub_u32 %[b], %[t], 2\n\t" \
    "s_cmp_lt_u32 %[b], " STR(SQUICK) "\n\t" \
    "s_cselect_b64 exec, %[fm], 0\n\t" \
    "buffer_store_dwordx2 v[52:53], %[l16], %[rs], %[fo] offen sc1\n\t" \
    "s_mov_b64 exec, -1\n\t" \
    "s_add_u32 m0, %[sd], 49152\n\t" \
    "s_add_u32 %[a], %[sx], 81920\n\t" \
    "s_waitcnt lgkmcnt(0)\n\t" \
    "buffer_load_dwordx4 %[l16], %[rs], %[a] offen offset:0 sc1 lds\n\t" \
    "buffer_load_dwordx4 %[l16], %[rs], %[a] offen offset:1024 sc1 lds\n\t" \
    "buffer_load_dwordx4 %[l16], %[rs], %[a] offen offset:2048 sc1 lds\n\t" \
    "buffer_load_dwordx4 %[l16], %[rs], %[a] offen offset:3072 sc1 lds\n\t" \
    "v_mfma_f32_16x16x32_bf16 v[0:3], v[20:23], v[68:71], 0\n\t" \
    "ds_read_b128 v[44:47], %[xp] offset:6144\n\t" \
    "v_mfma_f32_16x16x32_bf16 v[4:7], v[20:23], v[76:79], 0\n\t" \
    "ds_read_b128 v[48:51], %[xp] offset:7168\n\t" \
    "v_mfma_f32_16x16x32_bf16 v[8:11], v[24:27], v[68:71], 0\n\t" \
    "v_mfma_f32_16x16x32_bf16 v[12:15], v[24:27], v[76:79], 0\n\t" \
    "v_mfma_f32_16x16x32_bf16 v[0:3], v[28:31], v[72:75], v[0:3]\n\t" \
    "ds_read_b128 v[20:23], %[xp] offset:8192\n\t" \
    "v_mfma_f32_16x16x32_bf16 v[4:7], v[28:31], v[80:83], v[4:7]\n\t" \
    "ds_read_b128 v[24:27], %[xp] offset:9216\n\t" \
    "v_mfma_f32_16x16x32_bf16 v[8:11], v[32:35], v[72:75], v[8:11]\n\t" \
    "v_mfma_f32_16x16x32_bf16 v[12:15], v[32:35], v[80:83], v[12:15]\n\t" \
    "v_mfma_f32_16x16x32_bf16 v[0:3], v[36:39], v[84:87], v[0:3]\n\t" \
    "ds_read_b128 v[28:31], %[xp] offset:10240\n\t" \
    "v_mfma_f32_16x16x32_bf16 v[4:7], v[36:39], v[92:95], v[4:7]\n\t" \
    "ds_read_b128 v[32:35], %[xp] offset:11264\n\t" \
    "v_mfma_f32_16x16x32_bf16 v[8:11], v[40:43], v[84:87], v[8:11]\n\t" \
    "s_waitcnt lgkmcnt(4)\n\t" \
    "v_mfma_f32_16x16x32_bf16 v[12:15], v[40:43], v[92:95], v[12:15]\n\t" \
    "v_mfma_f32_16x16x32_bf16 v[0:3], v[44:47], v[88:91], v[0:3]\n\t" \
    "ds_read_b128 v[36:39], %[xp] offset:12288\n\t" \
    "v_mfma_f32_16x16x32_bf16 v[4:7], v[44:47], v[96:99], v[4:7]\n\t" \
    "ds_read_b128 v[40:43], %[xp] offset:13312\n\t" \
    "v_mfma_f32_16x16x32_bf16 v[8:11], v[48:51], v[88:91], v[8:11]\n\t" \
    "s_waitcnt lgkmcnt(4)\n\t" \
    "v_mfma_f32_16x16x32_bf16 v[12:15], v[48:51], v[96:99], v[12:15]\n\t" \
    "v_mfma_f32_16x16x32_bf16 v[0:3], v[20:23], v[100:103], v[0:3]\n\t" \
    "ds_read_b128 v[44:47], %[xp] offset:14336\n\t" \
    "v_mfma_f32_16x16x32_bf16 v[4:7], v[20:23], v[108:111], v[4:7]\n\t" \
    "ds_read_b128 v[48:51], %[xp] offset:15360\n\t" \
    "v_mfma_f32_16x16x32_bf16 v[8:11], v[24:27], v[100:103], v[8:11]\n\t" \
    "s_waitcnt lgkmcnt(4)\n\t" \
    "v_mfma_f32_16x16x32_bf16 v[12:15], v[24:27], v[108:111], v[12:15]\n\t" \
    "v_mfma_f32_16x16x32_bf16 v[0:3], v[28:31], v[104:107], v[0:3]\n\t" \
    "ds_read_b128 v[20:23], %[xp] offset:16384\n\t" \
    "v_mfma_f32_16x16x32_bf16 v[4:7], v[28:31], v[112:115], v[4:7]\n\t" \
    "ds_read_b128 v[24:27], %[xp] offset:17408\n\t" \
    "v_mfma_f32_16x16x32_bf16 v[8:11], v[32:35], v[104:107], v[8:11]\n\t" \
    "s_waitcnt lgkmcnt(4)\n\t" \
    "v_mfma_f32_16x16x32_bf16 v[12:15], v[32:35], v[112:115], v[12:15]\n\t" \
    "v_mfma_f32_16x16x32_bf16 v[0:3], v[36:39], v[116:119], v[0:3]\n\t" \
    "ds_read_b128 v[28:31], %[xp] offset:18432\n\t" \
    "v_mfma_f32_16x16x32_bf16 v[4:7], v[36:39], v[124:127], v[4:7]\n\t" \
    "ds_read_b128 v[32:35], %[xp] offset:19456\n\t" \
    "v_mfma_f32_16x16x32_bf16 v[8:11], v[40:43], v[116:119], v[8:11]\n\t" \
    "s_waitcnt lgkmcnt(4)\n\t" \
    "v_mfma_f32_16x16x32_bf16 v[12:15], v[40:43], v[124:127], v[12:15]\n\t" \
    "v_mfma_f32_16x16x32_bf16 v[0:3], v[44:47], v[120:123], v[0:3]\n\t" \
    "ds_read_b128 v[36:39], %[xp] offset:20480\n\t" \
    "v_mfma_f32_16x16x32_bf16 v[4:7], v[44:47], v[128:131], v[4:7]\n\t" \
    "ds_read_b128 v[40:43], %[xp] offset:21504\n\t" \
    "v_mfma_f32_16x16x32_bf16 v[8:11], v[48:51], v[120:123], v[8:11]\n\t" \
    "s_waitcnt lgkmcnt(4)\n\t" \
    "v_mfma_f32_16x16x32_bf16 v[12:15], v[48:51], v[128:131], v[12:15]\n\t" \
    "s_waitcnt vmcnt(4)\n\t" \
    "s_barrier\n\t" \
    "s_setprio 0\n\t" \
    "s_sub_u32 %[a], %[t], 1\n\t" \
    "v_mov_b32 v53, %[tag]\n\t" \
    "v_mov_b32 v52, %[a]\n\t" \
    "s_cmp_gt_u32 %[t], 1\n\t" \
    "s_cselect_b64 exec, %[fm], 0\n\t" \
    "buffer_store_dwordx2 v[52:53], %[l16], %[rs], %[fo] offen sc1\n\t" \
    "s_mov_b64 exec, %[pm]\n\t" \
    "s_mov_b32 m0, %[pd]\n\t" \
    "s_add_u32 %[a], %[sx], %[po]\n\t" \
    "buffer_load_dwordx4 %[l16], %[rs], %[a] offen sc1 lds\n\t" \
    "s_mov_b64 exec, -1\n\t" \
    "s_add_u32 m0, %[sd], 0\n\t" \
    "s_add_u32 %[a], %[sx], 131072\n\t" \
    "buffer_load_dwordx4 %[l16], %[rs], %[a] offen offset:0 sc1 lds\n\t" \
    "buffer_load_dwordx4 %[l16], %[rs], %[a] offen offset:1024 sc1 lds\n\t" \
    "buffer_load_dwordx4 %[l16], %[rs], %[a] offen offset:2048 sc1 lds\n\t" \
    "buffer_load_dwordx4 %[l16], %[rs], %[a] offen offset:3072 sc1 lds\n\t" \
    "v_mfma_f32_16x16x32_bf16 v[0:3], v[20:23], v[132:135], v[0:3]\n\t" \
    "ds_read_b128 v[44:47], %[xp] offset:22528\n\t" \
    "v_mfma_f32_16x16x32_bf16 v[4:7], v[20:23], v[140:143], v[4:7]\n\t" \
    "ds_read_b128 v[48:51], %[xp] offset:23552\n\t" \
    "v_mfma_f32_16x16x32_bf16 v[8:11], v[24:27], v[132:135], v[8:11]\n\t" \
    "s_waitcnt lgkmcnt(4)\n\t" \
    "v_mfma_f32_16x16x32_bf16 v[12:15], v[24:27], v[140:143], v[12:15]\n\t" \
    "v_mfma_f32_16x16x32_bf16 v[0:3], v[28:31], v[136:139], v[0:3]\n\t" \
    "ds_read_b128 v[20:23], %[xp] offset:24576\n\t" \
    "v_mfma_f32_16x16x32_bf16 v[4:7], v[28:31], v[144:147], v[4:7]\n\t" \
    "ds_read_b128 v[24:27], %[xp] offset:25600\n\t" \
    "v_mfma_f32_16x16x32_bf16 v[8:11], v[32:35], v[136:139], v[8:11]\n\t" \
    "s_waitcnt lgkmcnt(4)\n\t" \
    "v_mfma_f32_16x16x32_bf16 v[12:15], v[32:35], v[144:147], v[12:15]\n\t" \
    "v_mfma_f32_16x16x32_bf16 v[0:3], v[36:39], v[148:151], v[0:3]\n\t" \
    "ds_read_b128 v[28:31], %[xp] offset:26624\n\t" \
    "v_mfma_f32_16x16x32_bf16 v[4:7], v[36:39], v[156:159], v[4:7]\n\t" \
    "ds_read_b128 v[32:35], %[xp] offset:27648\n\t" \
    "v_mfma_f32_16x16x32_bf16 v[8:11], v[40:43], v[148:151], v[8:11]\n\t" \
    "s_waitcnt lgkmcnt(4)\n\t" \
    "v_mfma_f32_16x16x32_bf16 v[12:15], v[40:43], v[156:159], v[12:15]\n\t" \
    "v_mfma_f32_16x16x32_bf16 v[0:3], v[44:47], v[152:155], v[0:3]\n\t" \
    "ds_read_b128 v[36:39], %[xp] offset:28672\n\t" \
    "v_mfma_f32_16x16x32_bf16 v[4:7], v[44:47], v[160:163], v[4:7]\n\t" \
    "ds_read_b128 v[40:43], %[xp] offset:29696\n\t" \
    "v_mfma_f32_16x16x32_bf16 v[8:11], v[48:51], v[152:155], v[8:11]\n\t" \
    "s_waitcnt lgkmcnt(4)\n\t" \
    "v_mfma_f32_16x16x32_bf16 v[12:15], v[48:51], v[160:163], v[12:15]\n\t" \
    "v_mfma_f32_16x16x32_bf16 v[0:3], v[20:23], v[164:167], v[0:3]\n\t" \
    "ds_read_b128 v[44:47], %[xp] offset:30720\n\t" \
    "v_mfma_f32_16x16x32_bf16 v[4:7], v[20:23], v[172:175], v[4:7]\n\t" \
    "ds_read_b128 v[48:51], %[xp] offset:31744\n\t" \
    "v_mfma_f32_16x16x32_bf16 v[8:11], v[24:27], v[164:167], v[8:11]\n\t" \
    "s_waitcnt lgkmcnt(4)\n\t" \
    "v_mfma_f32_16x16x32_bf16 v[12:15], v[24:27], v[172:175], v[12:15]\n\t" \
    "v_mfma_f32_16x16x32_bf16 v[0:3], v[28:31], v[168:171], v[0:3]\n\t" \
    "ds_read_b128 v[20:23], %[xp] offset:32768\n\t" \
    "v_mfma_f32_16x16x32_bf16 v[4:7], v[28:31], v[176:179], v[4:7]\n\t" \
    "ds_read_b128 v[24:27], %[xp] offset:33792\n\t" \
    "v_mfma_f32_16x16x32_bf16 v[8:11], v[32:35], v[168:171], v[8:11]\n\t" \
    "s_waitcnt lgkmcnt(4)\n\t" \
    "v_mfma_f32_16x16x32_bf16 v[12:15], v[32:35], v[176:179], v[12:15]\n\t" \
    "v_mfma_f32_16x16x32_bf16 v[0:3], v[36:39], v[180:183], v[0:3]\n\t" \
    "ds_read_b128 v[28:31], %[xp] offset:34816\n\t" \
    "v_mfma_f32_16x16x32_bf16 v[4:7], v[36:39], v[188:191], v[4:7]\n\t" \
    "ds_read_b128 v[32:35], %[xp] offset:35840\n\t" \
    "v_mfma_f32_16x16x32_bf16 v[8:11], v[40:43], v[180:183], v[8:11]\n\t" \
    "s_waitcnt lgkmcnt(4)\n\t" \
    "v_mfma_f32_16x16x32_bf16 v[12:15], v[40:43], v[188:191], v[12:15]\n\t" \
    "v_mfma_f32_16x16x32_bf16 v[0:3], v[44:47], v[184:187], v[0:3]\n\t" \
    "ds_read_b128 v[36:39], %[xp] offset:36864\n\t" \
    "v_mfma_f32_16x16x32_bf16 v[4:7], v[44:47], v[192:195], v[4:7]\n\t" \
    "ds_read_b128 v[40:43], %[xp] offset:37888\n\t" \
    "v_mfma_f32_16x16x32_bf16 v[8:11], v[48:51], v[184:187], v[8:11]\n\t" \
    "s_waitcnt lgkmcnt(4)\n\t" \
    "v_mfma_f32_16x16x32_bf16 v[12:15], v[48:51], v[192:195], v[12:15]\n\t" \
    "s_add_u32 %[sx], %[sx], 65536\n\t" \
    "s_add_u32 %[t], %[t], 1\n\t" \
    "s_waitcnt vmcnt(4)\n\t" \
    "s_barrier\n\t" \
    "s_setprio 0\n\t" \
    "s_nop 3\n\t" \
    "ds_write_b128 %[pa], v[0:3] offset:0\n\t" \
    "ds_write_b128 %[pa], v[4:7] offset:1024\n\t" \
    "ds_write_b128 %[pa], v[8:11] offset:2048\n\t" \
    "ds_write_b128 %[pa], v[12:15] offset:3072\n\t" \
    "s_sub_u32 %[a], %[t], 1\n\t" \
    "v_mov_b32 v53, %[tag]\n\t" \
    "v_mov_b32 v52, %[a]\n\t" \
    "s_sub_u32 %[b], %[t], 2\n\t" \
    "s_cmp_lt_u32 %[b], " STR(SQUICK) "\n\t" \
    "s_cselect_b64 exec, %[fm], 0\n\t" \
    "buffer_store_dwordx2 v[52:53], %[l16], %[rs], %[fo] offen sc1\n\t" \
    "s_mov_b64 exec, -1\n\t" \
    "s_add_u32 m0, %[sd], 16384\n\t" \
    "s_add_u32 %[a], %[sx], 81920\n\t" \
    "s_waitcnt lgkmcnt(0)\n\t" \
    "buffer_load_dwordx4 %[l16], %[rs], %[a] offen offset:0 sc1 lds\n\t" \
    "buffer_load_dwordx4 %[l16], %[rs], %[a] offen offset:1024 sc1 lds\n\t" \
    "buffer_load_dwordx4 %[l16], %[rs], %[a] offen offset:2048 sc1 lds\n\t" \
    "buffer_load_dwordx4 %[l16], %[rs], %[a] offen offset:3072 sc1 lds\n\t" \
    "v_mfma_f32_16x16x32_bf16 v[196:199], v[20:23], v[68:71], 0\n\t" \
    "ds_read_b128 v[44:47], %[xp] offset:38912\n\t" \
    "v_mfma_f32_16x16x32_bf16 v[200:203], v[20:23], v[76:79], 0\n\t" \
    "ds_read_b128 v[48:51], %[xp] offset:39936\n\t" \
    "v_mfma_f32_16x16x32_bf16 v[204:207], v[24:27], v[68:71], 0\n\t" \
    "v_mfma_f32_16x16x32_bf16 v[208:211], v[24:27], v[76:79], 0\n\t" \
    "v_mfma_f32_16x16x32_bf16 v[196:199], v[28:31], v[72:75], v[196:199]\n\t" \
    "ds_read_b128 v[20:23], %[xp] offset:40960\n\t" \
    "v_mfma_f32_16x16x32_bf16 v[200:203], v[28:31], v[80:83], v[200:203]\n\t" \
    "ds_read_b128 v[24:27], %[xp] offset:41984\n\t" \
    "v_mfma_f32_16x16x32_bf16 v[204:207], v[32:35], v[72:75], v[204:207]\n\t" \
    "v_mfma_f32_16x16x32_bf16 v[208:211], v[32:35], v[80:83], v[208:211]\n\t" \
    "v_mfma_f32_16x16x32_bf16 v[196:199], v[36:39], v[84:87], v[196:199]\n\t" \
    "ds_read_b128 v[28:31], %[xp] offset:43008\n\t" \
    "v_mfma_f32_16x16x32_bf16 v[200:203], v[36:39], v[92:95], v[200:203]\n\t" \
    "ds_read_b128 v[32:35], %[xp] offset:44032\n\t" \
    "v_mfma_f32_16x16x32_bf16 v[204:207], v[40:43], v[84:87], v[204:207]\n\t" \
    "s_waitcnt lgkmcnt(4)\n\t" \
    "v_mfma_f32_16x16x32_bf16 v[208:211], v[40:43], v[92:95], v[208:211]\n\t" \
    "v_mfma_f32_16x16x32_bf16 v[196:199], v[44:47], v[88:91], v[196:199]\n\t" \
    "ds_read_b128 v[36:39], %[xp] offset:45056\n\t" \
    "v_mfma_f32_16x16x32_bf16 v[200:203], v[44:47], v[96:99], v[200:203]\n\t" \
    "ds_read_b128 v[40:43], %[xp] offset:46080\n\t" \
    "v_mfma_f32_16x16x32_bf16 v[204:207], v[48:51], v[88:91], v[204:207]\n\t" \
    "s_waitcnt lgkmcnt(4)\n\t" \
    "v_mfma_f32_16x16x32_bf16 v[208:211], v[48:51], v[96:99], v[208:211]\n\t" \
    "v_mfma_f32_16x16x32_bf16 v[196:199], v[20:23], v[100:103], v[196:199]\n\t" \
    "ds_read_b128 v[44:47], %[xp] offset:47104\n\t" \
    "v_mfma_f32_16x16x32_bf16 v[200:203], v[20:23], v[108:111], v[200:203]\n\t" \
    "ds_read_b128 v[48:51], %[xp] offset:48128\n\t" \
    "v_mfma_f32_16x16x32_bf16 v[204:207], v[24:27], v[100:103], v[204:207]\n\t" \
    "s_waitcnt lgkmcnt(4)\n\t" \
    "v_mfma_f32_16x16x32_bf16 v[208:211], v[24:27], v[108:111], v[208:211]\n\t" \
    "v_mfma_f32_16x16x32_bf16 v[196:199], v[28:31], v[104:107], v[196:199]\n\t" \
    "ds_read_b128 v[20:23], %[xp] offset:49152\n\t" \
    "v_mfma_f32_16x16x32_bf16 v[200:203], v[28:31], v[112:115], v[200:203]\n\t" \
    "ds_read_b128 v[24:27], %[xp] offset:50176\n\t" \
    "v_mfma_f32_16x16x32_bf16 v[204:207], v[32:35], v[104:107], v[204:207]\n\t" \
    "s_waitcnt lgkmcnt(4)\n\t" \
    "v_mfma_f32_16x16x32_bf16 v[208:211], v[32:35], v[112:115], v[208:211]\n\t" \
    "v_mfma_f32_16x16x32_bf16 v[196:199], v[36:39], v[116:119], v[196:199]\n\t" \
    "ds_read_b128 v[28:31], %[xp] offset:51200\n\t" \
    "v_mfma_f32_16x16x32_bf16 v[200:203], v[36:39], v[124:127], v[200:203]\n\t" \
    "ds_read_b128 v[32:35], %[xp] offset:52224\n\t" \
    "v_mfma_f32_16x16x32_bf16 v[204:207], v[40:43], v[116:119], v[204:207]\n\t" \
    "s_waitcnt lgkmcnt(4)\n\t" \
    "v_mfma_f32_16x16x32_bf16 v[208:211], v[40:43], v[124:127], v[208:211]\n\t" \
    "v_mfma_f32_16x16x32_bf16 v[196:199], v[44:47], v[120:123], v[196:199]\n\t" \
    "ds_read_b128 v[36:39], %[xp] offset:53248\n\t" \
    "v_mfma_f32_16x16x32_bf16 v[200:203], v[44:47], v[128:131], v[200:203]\n\t" \
    "ds_read_b128 v[40:43], %[xp] offset:54272\n\t" \
    "v_mfma_f32_16x16x32_bf16 v[204:207], v[48:51], v[120:123], v[204:207]\n\t" \
    "s_waitcnt lgkmcnt(4)\n\t" \
    "v_mfma_f32_16x16x32_bf16 v[208:211], v[48:51], v[128:131], v[208:211]\n\t" \
    "s_waitcnt vmcnt(4)\n\t" \
    "s_barrier\n\t" \
    "s_setprio 0\n\t" \
    "s_sub_u32 %[a], %[t], 1\n\t" \
    "v_mov_b32 v53, %[tag]\n\t" \
    "v_mov_b32 v52, %[a]\n\t" \
    "s_cmp_gt_u32 %[t], 1\n\t" \
    "s_cselect_b64 exec, %[fm], 0\n\t" \
    "buffer_store_dwordx2 v[52:53], %[l16], %[rs], %[fo] offen sc1\n\t" \
    "s_mov_b64 exec, %[pm]\n\t" \
    "s_mov_b32 m0, %[pd]\n\t" \
    "s_add_u32 %[a], %[sx], %[po]\n\t" \
    "buffer_load_dwordx4 %[l16], %[rs], %[a] offen sc1 lds\n\t" \
    "s_mov_b64 exec, -1\n\t" \
    "s_add_u32 m0, %[sd], 32768\n\t" \
    "s_add_u32 %[a], %[sx], 131072\n\t" \
    "buffer_load_dwordx4 %[l16], %[rs], %[a] offen offset:0 sc1 lds\n\t" \
    "buffer_load_dwordx4 %[l16], %[rs], %[a] offen offset:1024 sc1 lds\n\t" \
    "buffer_load_dwordx4 %[l16], %[rs], %[a] offen offset:2048 sc1 lds\n\t" \
    "buffer_load_dwordx4 %[l16], %[rs], %[a] offen offset:3072 sc1 lds\n\t" \
    "v_mfma_f32_16x16x32_bf16 v[196:199], v[20:23], v[132:135], v[196:199]\n\t" \
    "ds_read_b128 v[44:47], %[xp] offset:55296\n\t" \
    "v_mfma_f32_16x16x32_bf16 v[200:203], v[20:23], v[140:143], v[200:203]\n\t" \
    "ds_read_b128 v[48:51], %[xp] offset:56320\n\t" \
    "v_mfma_f32_16x16x32_bf16 v[204:207], v[24:27], v[132:135], v[204:207]\n\t" \
    "s_waitcnt lgkmcnt(4)\n\t" \
    "v_mfma_f32_16x16x32_bf16 v[208:211], v[24:27], v[140:143], v[208:211]\n\t" \
    "v_mfma_f32_16x16x32_bf16 v[196:199], v[28:31], v[136:139], v[196:199]\n\t" \
    "ds_read_b128 v[20:23], %[xp] offset:57344\n\t" \
    "v_mfma_f32_16x16x32_bf16 v[200:203], v[28:31], v[144:147], v[200:203]\n\t" \
    "ds_read_b128 v[24:27], %[xp] offset:58368\n\t" \
    "v_mfma_f32_16x16x32_bf16 v[204:207], v[32:35], v[136:139], v[204:207]\n\t" \
    "s_waitcnt lgkmcnt(4)\n\t" \
    "v_mfma_f32_16x16x32_bf16 v[208:211], v[32:35], v[144:147], v[208:211]\n\t" \
    "v_mfma_f32_16x16x32_bf16 v[196:199], v[36:39], v[148:151], v[196:199]\n\t" \
    "ds_read_b128 v[28:31], %[xp] offset:59392\n\t" \
    "v_mfma_f32_16x16x32_bf16 v[200:203], v[36:39], v[156:159], v[200:203]\n\t" \
    "ds_read_b128 v[32:35], %[xp] offset:60416\n\t" \
    "v_mfma_f32_16x16x32_bf16 v[204:207], v[40:43], v[148:151], v[204:207]\n\t" \
    "s_waitcnt lgkmcnt(4)\n\t" \
    "v_mfma_f32_16x16x32_bf16 v[208:211], v[40:43], v[156:159], v[208:211]\n\t" \
    "v_mfma_f32_16x16x32_bf16 v[196:199], v[44:47], v[152:155], v[196:199]\n\t" \
    "ds_read_b128 v[36:39], %[xp] offset:61440\n\t" \
    "v_mfma_f32_16x16x32_bf16 v[200:203], v[44:47], v[160:163], v[200:203]\n\t" \
    "ds_read_b128 v[40:43], %[xp] offset:62464\n\t" \
    "v_mfma_f32_16x16x32_bf16 v[204:207], v[48:51], v[152:155], v[204:207]\n\t" \
    "s_waitcnt lgkmcnt(4)\n\t" \
    "v_mfma_f32_16x16x32_bf16 v[208:211], v[48:51], v[160:163], v[208:211]\n\t" \
    "v_mfma_f32_16x16x32_bf16 v[196:199], v[20:23], v[164:167], v[196:199]\n\t" \
    "ds_read_b128 v[44:47], %[xp] offset:63488\n\t" \
    "v_mfma_f32_16x16x32_bf16 v[200:203], v[20:23], v[172:175], v[200:203]\n\t" \
    "ds_read_b128 v[48:51], %[xp] offset:64512\n\t" \
    "v_mfma_f32_16x16x32_bf16 v[204:207], v[24:27], v[164:167], v[204:207]\n\t" \
    "s_waitcnt lgkmcnt(4)\n\t" \
    "v_mfma_f32_16x16x32_bf16 v[208:211], v[24:27], v[172:175], v[208:211]\n\t" \
    "v_mfma_f32_16x16x32_bf16 v[196:199], v[28:31], v[168:171], v[196:199]\n\t" \
    "ds_read_b128 v[20:23], %[xp] offset:0\n\t" \
    "v_mfma_f32_16x16x32_bf16 v[200:203], v[28:31], v[176:179], v[200:203]\n\t" \
    "ds_read_b128 v[24:27], %[xp] offset:1024\n\t" \
    "v_mfma_f32_16x16x32_bf16 v[204:207], v[32:35], v[168:171], v[204:207]\n\t" \
    "s_waitcnt lgkmcnt(4)\n\t" \
    "v_mfma_f32_16x16x32_bf16 v[208:211], v[32:35], v[176:179], v[208:211]\n\t" \
    "v_mfma_f32_16x16x32_bf16 v[196:199], v[36:39], v[180:183], v[196:199]\n\t" \
    "ds_read_b128 v[28:31], %[xp] offset:2048\n\t" \
    "v_mfma_f32_16x16x32_bf16 v[200:203], v[36:39], v[188:191], v[200:203]\n\t" \
    "ds_read_b128 v[32:35], %[xp] offset:3072\n\t" \
    "v_mfma_f32_16x16x32_bf16 v[204:207], v[40:43], v[180:183], v[204:207]\n\t" \
    "s_waitcnt lgkmcnt(4)\n\t" \
    "v_mfma_f32_16x16x32_bf16 v[208:211], v[40:43], v[188:191], v[208:211]\n\t" \
    "v_mfma_f32_16x16x32_bf16 v[196:199], v[44:47], v[184:187], v[196:199]\n\t" \
    "ds_read_b128 v[36:39], %[xp] offset:4096\n\t" \
    "v_mfma_f32_16x16x32_bf16 v[200:203], v[44:47], v[192:195], v[200:203]\n\t" \
    "ds_read_b128 v[40:43], %[xp] offset:5120\n\t" \
    "v_mfma_f32_16x16x32_bf16 v[204:207], v[48:51], v[184:187], v[204:207]\n\t" \
    "s_waitcnt lgkmcnt(4)\n\t" \
    "v_mfma_f32_16x16x32_bf16 v[208:211], v[48:51], v[192:195], v[208:211]\n\t" \
    "s_add_u32 %[sx], %[sx], 65536\n\t" \
    "s_add_u32 %[t], %[t], 1\n\t" \
    "s_waitcnt vmcnt(4)\n\t" \
    "s_barrier\n\t" \
    "s_cmp_lt_u32 %[t], %[te]\n\t" \
    "s_cbranch_scc1 L0_%=\n\t" \
    "s_cmp_lg_u32 %[fin], 0\n\t" \
    "s_cbranch_scc0 E_%=\n\t" \
    "s_nop 4\n\t" \
    "ds_write_b128 %[pa], v[196:199] offset:0\n\t" \
    "ds_write_b128 %[pa], v[200:203] offset:1024\n\t" \
    "ds_write_b128 %[pa], v[204:207] offset:2048\n\t" \
    "ds_write_b128 %[pa], v[208:211] offset:3072\n\t" \
    "s_waitcnt lgkmcnt(0)\n\t" \
    "s_barrier\n\t" \
    "s_branch E_%=\n\t" \
    "L1_%=:\n\t" \
    "s_setprio 1\n\t" \
    "s_add_u32 m0, %[sd], 49152\n\t" \
    "s_add_u32 %[a], %[sx], 81920\n\t" \
    "v_mfma_f32_16x16x32_bf16 v[0:3], v[20:23], v[68:71], 0\n\t" \
    "ds_read_b128 v[44:47], %[xp] offset:6144\n\t" \
    "v_mfma_f32_16x16x32_bf16 v[4:7], v[20:23], v[76:79], 0\n\t" \
    "ds_read_b128 v[48:51], %[xp] offset:7168\n\t" \
    "v_mfma_f32_16x16x32_bf16 v[8:11], v[24:27], v[68:71], 0\n\t" \
    "s_waitcnt lgkmcnt(4)\n\t" \
    "v_mfma_f32_16x16x32_bf16 v[12:15], v[24:27], v[76:79], 0\n\t" \
    "v_mfma_f32_16x16x32_bf16 v[0:3], v[28:31], v[72:75], v[0:3]\n\t" \
    "ds_read_b128 v[20:23], %[xp] offset:8192\n\t" \
    "v_mfma_f32_16x16x32_bf16 v[4:7], v[28:31], v[80:83], v[4:7]\n\t" \
    "ds_read_b128 v[24:27], %[xp] offset:9216\n\t" \
    "v_mfma_f32_16x16x32_bf16 v[8:11], v[32:35], v[72:75], v[8:11]\n\t" \
    "s_waitcnt lgkmcnt(4)\n\t" \
    "v_mfma_f32_16x16x32_bf16 v[12:15], v[32:35], v[80:83], v[12:15]\n\t" \
    "v_mfma_f32_16x16x32_bf16 v[0:3], v[36:39], v[84:87], v[0:3]\n\t" \
    "ds_read_b128 v[28:31], %[xp] offset:10240\n\t" \
    "v_mfma_f32_16x16x32_bf16 v[4:7], v[36:39], v[92:95], v[4:7]\n\t" \
    "ds_read_b128 v[32:35], %[xp] offset:11264\n\t" \
    "v_mfma_f32_16x16x32_bf16 v[8:11], v[40:43], v[84:87], v[8:11]\n\t" \
    "s_waitcnt lgkmcnt(4)\n\t" \
    "v_mfma_f32_16x16x32_bf16 v[12:15], v[40:43], v[92:95], v[12:15]\n\t" \
    "v_mfma_f32_16x16x32_bf16 v[0:3], v[44:47], v[88:91], v[0:3]\n\t" \
    "ds_read_b128 v[36:39], %[xp] offset:12288\n\t" \
    "v_mfma_f32_16x16x32_bf16 v[4:7], v[44:47], v[96:99], v[4:7]\n\t" \
    "ds_read_b128 v[40:43], %[xp] offset:13312\n\t" \
    "v_mfma_f32_16x16x32_bf16 v[8:11], v[48:51], v[88:91], v[8:11]\n\t" \
    "s_waitcnt lgkmcnt(4)\n\t" \
    "v_mfma_f32_16x16x32_bf16 v[12:15], v[48:51], v[96:99], v[12:15]\n\t" \
    "v_mfma_f32_16x16x32_bf16 v[0:3], v[20:23], v[100:103], v[0:3]\n\t" \
    "ds_read_b128 v[44:47], %[xp] offset:14336\n\t" \
    "v_mfma_f32_16x16x32_bf16 v[4:7], v[20:23], v[108:111], v[4:7]\n\t" \
    "ds_read_b128 v[48:51], %[xp] offset:15360\n\t" \
    "v_mfma_f32_16x16x32_bf16 v[8:11], v[24:27], v[100:103], v[8:11]\n\t" \
    "s_waitcnt lgkmcnt(4)\n\t" \
    "v_mfma_f32_16x16x32_bf16 v[12:15], v[24:27], v[108:111], v[12:15]\n\t" \
    "buffer_load_dwordx4 %[l16], %[rs], %[a] offen offset:0 sc1 lds\n\t" \
    "v_mfma_f32_16x16x32_bf16 v[0:3], v[28:31], v[104:107], v[0:3]\n\t" \
    "ds_read_b128 v[20:23], %[xp] offset:16384\n\t" \
    "v_mfma_f32_16x16x32_bf16 v[4:7], v[28:31], v[112:115], v[4:7]\n\t" \
    "ds_read_b128 v[24:27], %[xp] offset:17408\n\t" \
    "v_mfma_f32_16x16x32_bf16 v[8:11], v[32:35], v[104:107], v[8:11]\n\t" \
    "s_waitcnt lgkmcnt(4)\n\t" \
    "v_mfma_f32_16x16x32_bf16 v[12:15], v[32:35], v[112:115], v[12:15]\n\t" \
    "buffer_load_dwordx4 %[l16], %[rs], %[a] offen offset:1024 sc1 lds\n\t" \
    "v_mfma_f32_16x16x32_bf16 v[0:3], v[36:39], v[116:119], v[0:3]\n\t" \
    "ds_read_b128 v[28:31], %[xp] offset:18432\n\t" \
    "v_mfma_f32_16x16x32_bf16 v[4:7], v[36:39], v[124:127], v[4:7]\n\t" \
    "ds_read_b128 v[32:35], %[xp] offset:19456\n\t" \
    "v_mfma_f32_16x16x32_bf16 v[8:11], v[40:43], v[116:119], v[8:11]\n\t" \
    "s_waitcnt lgkmcnt(4)\n\t" \
    "v_mfma_f32_16x16x32_bf16 v[12:15], v[40:43], v[124:127], v[12:15]\n\t" \
    "buffer_load_dwordx4 %[l16], %[rs], %[a] offen offset:2048 sc1 lds\n\t" \
    "v_mfma_f32_16x16x32_bf16 v[0:3], v[44:47], v[120:123], v[0:3]\n\t" \
    "ds_read_b128 v[36:39], %[xp] offset:20480\n\t" \
    "v_mfma_f32_16x16x32_bf16 v[4:7], v[44:47], v[128:131], v[4:7]\n\t" \
    "ds_read_b128 v[40:43], %[xp] offset:21504\n\t" \
    "v_mfma_f32_16x16x32_bf16 v[8:11], v[48:51], v[120:123], v[8:11]\n\t" \
    "s_waitcnt lgkmcnt(4)\n\t" \
    "v_mfma_f32_16x16x32_bf16 v[12:15], v[48:51], v[128:131], v[12:15]\n\t" \
    "buffer_load_dwordx4 %[l16], %[rs], %[a] offen offset:3072 sc1 lds\n\t" \
    "s_waitcnt vmcnt(4)\n\t" \
    "s_barrier\n\t" \
    "s_setprio 1\n\t" \
    "s_add_u32 m0, %[sd], 0\n\t" \
    "s_add_u32 %[a], %[sx], 131072\n\t" \
    "s_add_u32 %[b], %[oo], 0x1000\n\t" \
    "s_cmp_lg_u32 %[t], 1\n\t" \
    "s_cselect_b64 %[em], -1, 0\n\t" \
    "v_mfma_f32_16x16x32_bf16 v[0:3], v[20:23], v[132:135], v[0:3]\n\t" \
    "ds_read_b128 v[44:47], %[xp] offset:22528\n\t" \
    "v_mfma_f32_16x16x32_bf16 v[4:7], v[20:23], v[140:143], v[4:7]\n\t" \
    "ds_read_b128 v[48:51], %[xp] offset:23552\n\t" \
    "v_mfma_f32_16x16x32_bf16 v[8:11], v[24:27], v[132:135], v[8:11]\n\t" \
    "ds_read_b128 v[52:55], %[pa] offset:0\n\t" \
    "s_waitcnt lgkmcnt(5)\n\t" \
    "v_mfma_f32_16x16x32_bf16 v[12:15], v[24:27], v[140:143], v[12:15]\n\t" \
    "buffer_load_dwordx4 %[l16], %[rs], %[a] offen offset:0 sc1 lds\n\t" \
    "v_mfma_f32_16x16x32_bf16 v[0:3], v[28:31], v[136:139], v[0:3]\n\t" \
    "ds_read_b128 v[20:23], %[xp] offset:24576\n\t" \
    "v_mfma_f32_16x16x32_bf16 v[4:7], v[28:31], v[144:147], v[4:7]\n\t" \
    "ds_read_b128 v[24:27], %[xp] offset:25600\n\t" \
    "v_mfma_f32_16x16x32_bf16 v[8:11], v[32:35], v[136:139], v[8:11]\n\t" \
    "ds_read_b128 v[56:59], %[pa] offset:1024\n\t" \
    "s_waitcnt lgkmcnt(6)\n\t" \
    "v_mfma_f32_16x16x32_bf16 v[12:15], v[32:35], v[144:147], v[12:15]\n\t" \
    "buffer_load_dwordx4 %[l16], %[rs], %[a] offen offset:1024 sc1 lds\n\t" \
    "v_mfma_f32_16x16x32_bf16 v[0:3], v[36:39], v[148:151], v[0:3]\n\t" \
    "ds_read_b128 v[28:31], %[xp] offset:26624\n\t" \
    "v_mfma_f32_16x16x32_bf16 v[4:7], v[36:39], v[156:159], v[4:7]\n\t" \
    "ds_read_b128 v[32:35], %[xp] offset:27648\n\t" \
    "v_mfma_f32_16x16x32_bf16 v[8:11], v[40:43], v[148:151], v[8:11]\n\t" \
    "ds_read_b128 v[60:63], %[pa] offset:2048\n\t" \
    "s_waitcnt lgkmcnt(7)\n\t" \
    "v_mfma_f32_16x16x32_bf16 v[12:15], v[40:43], v[156:159], v[12:15]\n\t" \
    "buffer_load_dwordx4 %[l16], %[rs], %[a] offen offset:2048 sc1 lds\n\t" \
    "v_mfma_f32_16x16x32_bf16 v[0:3], v[44:47], v[152:155], v[0:3]\n\t" \
    "ds_read_b128 v[36:39], %[xp] offset:28672\n\t" \
    "s_waitcnt lgkmcnt(7)\n\t" \
    "v_add_f32 v196, v196, v52\n\t" \
    "v_mfma_f32_16x16x32_bf16 v[4:7], v[44:47], v[160:163], v[4:7]\n\t" \
    "ds_read_b128 v[40:43], %[xp] offset:29696\n\t" \
    "v_add_f32 v197, v197, v53\n\t" \
    "v_mfma_f32_16x16x32_bf16 v[8:11], v[48:51], v[152:155], v[8:11]\n\t" \
    "ds_read_b128 v[64:67], %[pa] offset:3072\n\t" \
    "s_waitcnt lgkmcnt(7)\n\t" \
    "v_mfma_f32_16x16x32_bf16 v[12:15], v[48:51], v[160:163], v[12:15]\n\t" \
    "buffer_load_dwordx4 %[l16], %[rs], %[a] offen offset:3072 sc1 lds\n\t" \
    "v_add_f32 v198, v198, v54\n\t" \
    "v_add_f32 v199, v199, v55\n\t" \
    "v_mfma_f32_16x16x32_bf16 v[0:3], v[20:23], v[164:167], v[0:3]\n\t" \
    "ds_read_b128 v[44:47], %[xp] offset:30720\n\t" \
    "s_waitcnt lgkmcnt(7)\n\t" \
    "v_add_f32 v200, v200, v56\n\t" \
    "v_mfma_f32_16x16x32_bf16 v[4:7], v[20:23], v[172:175], v[4:7]\n\t" \
    "ds_read_b128 v[48:51], %[xp] offset:31744\n\t" \
    "v_add_f32 v201, v201, v57\n\t" \
    "v_mfma_f32_16x16x32_bf16 v[8:11], v[24:27], v[164:167], v[8:11]\n\t" \
    "v_add_f32 v202, v202, v58\n\t" \
    "s_waitcnt lgkmcnt(6)\n\t" \
    "v_mfma_f32_16x16x32_bf16 v[12:15], v[24:27], v[172:175], v[12:15]\n\t" \
    "v_add_f32 v203, v203, v59\n\t" \
    "s_waitcnt lgkmcnt(5)\n\t" \
    "v_add_f32 v204, v204, v60\n\t" \
    "v_add_f32 v205, v205, v61\n\t" \
    "v_mfma_f32_16x16x32_bf16 v[0:3], v[28:31], v[168:171], v[0:3]\n\t" \
    "ds_read_b128 v[20:23], %[xp] offset:32768\n\t" \
    "v_add_f32 v206, v206, v62\n\t" \
    "v_mfma_f32_16x16x32_bf16 v[4:7], v[28:31], v[176:179], v[4:7]\n\t" \
    "ds_read_b128 v[24:27], %[xp] offset:33792\n\t" \
    "v_add_f32 v207, v207, v63\n\t" \
    "v_mfma_f32_16x16x32_bf16 v[8:11], v[32:35], v[168:171], v[8:11]\n\t" \
    "s_waitcnt lgkmcnt(4)\n\t" \
    "v_add_f32 v208, v208, v64\n\t" \
    "v_mfma_f32_16x16x32_bf16 v[12:15], v[32:35], v[176:179], v[12:15]\n\t" \
    "v_add_f32 v209, v209, v65\n\t" \
    "v_add_f32 v210, v210, v66\n\t" \
    "v_add_f32 v211, v211, v67\n\t" \
    "v_mfma_f32_16x16x32_bf16 v[0:3], v[36:39], v[180:183], v[0:3]\n\t" \
    "ds_read_b128 v[28:31], %[xp] offset:34816\n\t" \
    "v_cvt_pk_bf16_f32 v52, v196, v197\n\t" \
    "v_mfma_f32_16x16x32_bf16 v[4:7], v[36:39], v[188:191], v[4:7]\n\t" \
    "ds_read_b128 v[32:35], %[xp] offset:35840\n\t" \
    "v_cvt_pk_bf16_f32 v53, v198, v199\n\t" \
    "v_mfma_f32_16x16x32_bf16 v[8:11], v[40:43], v[180:183], v[8:11]\n\t" \
    "v_cvt_pk_bf16_f32 v54, v204, v205\n\t" \
    "s_waitcnt lgkmcnt(4)\n\t" \
    "v_mfma_f32_16x16x32_bf16 v[12:15], v[40:43], v[188:191], v[12:15]\n\t" \
    "v_cvt_pk_bf16_f32 v55, v206, v207\n\t" \
    "v_cvt_pk_bf16_f32 v56, v200, v201\n\t" \
    "v_cvt_pk_bf16_f32 v57, v202, v203\n\t" \
    "v_mfma_f32_16x16x32_bf16 v[0:3], v[44:47], v[184:187], v[0:3]\n\t" \
    "ds_read_b128 v[36:39], %[xp] offset:36864\n\t" \
    "v_cvt_pk_bf16_f32 v58, v208, v209\n\t" \
    "v_mfma_f32_16x16x32_bf16 v[4:7], v[44:47], v[192:195], v[4:7]\n\t" \
    "ds_read_b128 v[40:43], %[xp] offset:37888\n\t" \
    "v_cvt_pk_bf16_f32 v59, v210, v211\n\t" \
    "v_mfma_f32_16x16x32_bf16 v[8:11], v[48:51], v[184:187], v[8:11]\n\t" \
    "s_waitcnt lgkmcnt(4)\n\t" \
    "v_mfma_f32_16x16x32_bf16 v[12:15], v[48:51], v[192:195], v[12:15]\n\t" \
    "s_mov_b64 exec, %[em]\n\t" \
    "buffer_store_dwordx4 v[52:55], %[l16], %[rs], %[oo] offen sc1\n\t" \
    "buffer_store_dwordx4 v[56:59], %[l16], %[rs], %[oo] offen offset:1024 sc1\n\t" \
    "s_mov_b64 exec, -1\n\t" \
    "s_add_u32 %[sx], %[sx], 65536\n\t" \
    "s_add_u32 %[t], %[t], 1\n\t" \
    "s_add_u32 %[oo], %[oo], 262144\n\t" \
    "s_waitcnt vmcnt(6)\n\t" \
    "s_cmp_gt_u32 %[t], " STR(SQUICK) " + 1\n\t" \
    "s_cbranch_scc1 B0_%=\n\t" \
    "s_waitcnt vmcnt(0)\n\t" \
    "B0_%=:\n\t" \
    "s_barrier\n\t" \
    "s_setprio 1\n\t" \
    "s_add_u32 m0, %[sd], 16384\n\t" \
    "s_add_u32 %[a], %[sx], 81920\n\t" \
    "v_mfma_f32_16x16x32_bf16 v[196:199], v[20:23], v[68:71], 0\n\t" \
    "ds_read_b128 v[44:47], %[xp] offset:38912\n\t" \
    "v_mfma_f32_16x16x32_bf16 v[200:203], v[20:23], v[76:79], 0\n\t" \
    "ds_read_b128 v[48:51], %[xp] offset:39936\n\t" \
    "v_mfma_f32_16x16x32_bf16 v[204:207], v[24:27], v[68:71], 0\n\t" \
    "s_waitcnt lgkmcnt(4)\n\t" \
    "v_mfma_f32_16x16x32_bf16 v[208:211], v[24:27], v[76:79], 0\n\t" \
    "v_mfma_f32_16x16x32_bf16 v[196:199], v[28:31], v[72:75], v[196:199]\n\t" \
    "ds_read_b128 v[20:23], %[xp] offset:40960\n\t" \
    "v_mfma_f32_16x16x32_bf16 v[200:203], v[28:31], v[80:83], v[200:203]\n\t" \
    "ds_read_b128 v[24:27], %[xp] offset:41984\n\t" \
    "v_mfma_f32_16x16x32_bf16 v[204:207], v[32:35], v[72:75], v[204:207]\n\t" \
    "s_waitcnt lgkmcnt(4)\n\t" \
    "v_mfma_f32_16x16x32_bf16 v[208:211], v[32:35], v[80:83], v[208:211]\n\t" \
    "v_mfma_f32_16x16x32_bf16 v[196:199], v[36:39], v[84:87], v[196:199]\n\t" \
    "ds_read_b128 v[28:31], %[xp] offset:43008\n\t" \
    "v_mfma_f32_16x16x32_bf16 v[200:203], v[36:39], v[92:95], v[200:203]\n\t" \
    "ds_read_b128 v[32:35], %[xp] offset:44032\n\t" \
    "v_mfma_f32_16x16x32_bf16 v[204:207], v[40:43], v[84:87], v[204:207]\n\t" \
    "s_waitcnt lgkmcnt(4)\n\t" \
    "v_mfma_f32_16x16x32_bf16 v[208:211], v[40:43], v[92:95], v[208:211]\n\t" \
    "v_mfma_f32_16x16x32_bf16 v[196:199], v[44:47], v[88:91], v[196:199]\n\t" \
    "ds_read_b128 v[36:39], %[xp] offset:45056\n\t" \
    "v_mfma_f32_16x16x32_bf16 v[200:203], v[44:47], v[96:99], v[200:203]\n\t" \
    "ds_read_b128 v[40:43], %[xp] offset:46080\n\t" \
    "v_mfma_f32_16x16x32_bf16 v[204:207], v[48:51], v[88:91], v[204:207]\n\t" \
    "s_waitcnt lgkmcnt(4)\n\t" \
    "v_mfma_f32_16x16x32_bf16 v[208:211], v[48:51], v[96:99], v[208:211]\n\t" \
    "v_mfma_f32_16x16x32_bf16 v[196:199], v[20:23], v[100:103], v[196:199]\n\t" \
    "ds_read_b128 v[44:47], %[xp] offset:47104\n\t" \
    "v_mfma_f32_16x16x32_bf16 v[200:203], v[20:23], v[108:111], v[200:203]\n\t" \
    "ds_read_b128 v[48:51], %[xp] offset:48128\n\t" \
    "v_mfma_f32_16x16x32_bf16 v[204:207], v[24:27], v[100:103], v[204:207]\n\t" \
    "s_waitcnt lgkmcnt(4)\n\t" \
    "v_mfma_f32_16x16x32_bf16 v[208:211], v[24:27], v[108:111], v[208:211]\n\t" \
    "buffer_load_dwordx4 %[l16], %[rs], %[a] offen offset:0 sc1 lds\n\t" \
    "v_mfma_f32_16x16x32_bf16 v[196:199], v[28:31], v[104:107], v[196:199]\n\t" \
    "ds_read_b128 v[20:23], %[xp] offset:49152\n\t" \
    "v_mfma_f32_16x16x32_bf16 v[200:203], v[28:31], v[112:115], v[200:203]\n\t" \
    "ds_read_b128 v[24:27], %[xp] offset:50176\n\t" \
    "v_mfma_f32_16x16x32_bf16 v[204:207], v[32:35], v[104:107], v[204:207]\n\t" \
    "s_waitcnt lgkmcnt(4)\n\t" \
    "v_mfma_f32_16x16x32_bf16 v[208:211], v[32:35], v[112:115], v[208:211]\n\t" \
    "buffer_load_dwordx4 %[l16], %[rs], %[a] offen offset:1024 sc1 lds\n\t" \
    "v_mfma_f32_16x16x32_bf16 v[196:199], v[36:39], v[116:119], v[196:199]\n\t" \
    "ds_read_b128 v[28:31], %[xp] offset:51200\n\t" \
    "v_mfma_f32_16x16x32_bf16 v[200:203], v[36:39], v[124:127], v[200:203]\n\t" \
    "ds_read_b128 v[32:35], %[xp] offset:52224\n\t" \
    "v_mfma_f32_16x16x32_bf16 v[204:207], v[40:43], v[116:119], v[204:207]\n\t" \
    "s_waitcnt lgkmcnt(4)\n\t" \
    "v_mfma_f32_16x16x32_bf16 v[208:211], v[40:43], v[124:127], v[208:211]\n\t" \
    "buffer_load_dwordx4 %[l16], %[rs], %[a] offen offset:2048 sc1 lds\n\t" \
    "v_mfma_f32_16x16x32_bf16 v[196:199], v[44:47], v[120:123], v[196:199]\n\t" \
    "ds_read_b128 v[36:39], %[xp] offset:53248\n\t" \
    "v_mfma_f32_16x16x32_bf16 v[200:203], v[44:47], v[128:131], v[200:203]\n\t" \
    "ds_read_b128 v[40:43], %[xp] offset:54272\n\t" \
    "v_mfma_f32_16x16x32_bf16 v[204:207], v[48:51], v[120:123], v[204:207]\n\t" \
    "s_waitcnt lgkmcnt(4)\n\t" \
    "v_mfma_f32_16x16x32_bf16 v[208:211], v[48:51], v[128:131], v[208:211]\n\t" \
    "buffer_load_dwordx4 %[l16], %[rs], %[a] offen offset:3072 sc1 lds\n\t" \
    "s_waitcnt vmcnt(4)\n\t" \
    "s_barrier\n\t" \
    "s_setprio 1\n\t" \
    "s_add_u32 m0, %[sd], 32768\n\t" \
    "s_add_u32 %[a], %[sx], 131072\n\t" \
    "s_add_u32 %[b], %[oo], 0x1000\n\t" \
    "s_cmp_lg_u32 %[t], 1\n\t" \
    "s_cselect_b64 %[em], -1, 0\n\t" \
    "v_mfma_f32_16x16x32_bf16 v[196:199], v[20:23], v[132:135], v[196:199]\n\t" \
    "ds_read_b128 v[44:47], %[xp] offset:55296\n\t" \
    "v_mfma_f32_16x16x32_bf16 v[200:203], v[20:23], v[140:143], v[200:203]\n\t" \
    "ds_read_b128 v[48:51], %[xp] offset:56320\n\t" \
    "v_mfma_f32_16x16x32_bf16 v[204:207], v[24:27], v[132:135], v[204:207]\n\t" \
    "ds_read_b128 v[52:55], %[pa] offset:0\n\t" \
    "s_waitcnt lgkmcnt(5)\n\t" \
    "v_mfma_f32_16x16x32_bf16 v[208:211], v[24:27], v[140:143], v[208:211]\n\t" \
    "buffer_load_dwordx4 %[l16], %[rs], %[a] offen offset:0 sc1 lds\n\t" \
    "v_mfma_f32_16x16x32_bf16 v[196:199], v[28:31], v[136:139], v[196:199]\n\t" \
    "ds_read_b128 v[20:23], %[xp] offset:57344\n\t" \
    "v_mfma_f32_16x16x32_bf16 v[200:203], v[28:31], v[144:147], v[200:203]\n\t" \
    "ds_read_b128 v[24:27], %[xp] offset:58368\n\t" \
    "v_mfma_f32_16x16x32_bf16 v[204:207], v[32:35], v[136:139], v[204:207]\n\t" \
    "ds_read_b128 v[56:59], %[pa] offset:1024\n\t" \
    "s_waitcnt lgkmcnt(6)\n\t" \
    "v_mfma_f32_16x16x32_bf16 v[208:211], v[32:35], v[144:147], v[208:211]\n\t" \
    "buffer_load_dwordx4 %[l16], %[rs], %[a] offen offset:1024 sc1 lds\n\t" \
    "v_mfma_f32_16x16x32_bf16 v[196:199], v[36:39], v[148:151], v[196:199]\n\t" \
    "ds_read_b128 v[28:31], %[xp] offset:59392\n\t" \
    "v_mfma_f32_16x16x32_bf16 v[200:203], v[36:39], v[156:159], v[200:203]\n\t" \
    "ds_read_b128 v[32:35], %[xp] offset:60416\n\t" \
    "v_mfma_f32_16x16x32_bf16 v[204:207], v[40:43], v[148:151], v[204:207]\n\t" \
    "ds_read_b128 v[60:63], %[pa] offset:2048\n\t" \
    "s_waitcnt lgkmcnt(7)\n\t" \
    "v_mfma_f32_16x16x32_bf16 v[208:211], v[40:43], v[156:159], v[208:211]\n\t" \
    "buffer_load_dwordx4 %[l16], %[rs], %[a] offen offset:2048 sc1 lds\n\t" \
    "v_mfma_f32_16x16x32_bf16 v[196:199], v[44:47], v[152:155], v[196:199]\n\t" \
    "ds_read_b128 v[36:39], %[xp] offset:61440\n\t" \
    "s_waitcnt lgkmcnt(7)\n\t" \
    "v_add_f32 v0, v0, v52\n\t" \
    "v_mfma_f32_16x16x32_bf16 v[200:203], v[44:47], v[160:163], v[200:203]\n\t" \
    "ds_read_b128 v[40:43], %[xp] offset:62464\n\t" \
    "v_add_f32 v1, v1, v53\n\t" \
    "v_mfma_f32_16x16x32_bf16 v[204:207], v[48:51], v[152:155], v[204:207]\n\t" \
    "ds_read_b128 v[64:67], %[pa] offset:3072\n\t" \
    "s_waitcnt lgkmcnt(7)\n\t" \
    "v_mfma_f32_16x16x32_bf16 v[208:211], v[48:51], v[160:163], v[208:211]\n\t" \
    "buffer_load_dwordx4 %[l16], %[rs], %[a] offen offset:3072 sc1 lds\n\t" \
    "v_add_f32 v2, v2, v54\n\t" \
    "v_add_f32 v3, v3, v55\n\t" \
    "v_mfma_f32_16x16x32_bf16 v[196:199], v[20:23], v[164:167], v[196:199]\n\t" \
    "ds_read_b128 v[44:47], %[xp] offset:63488\n\t" \
    "s_waitcnt lgkmcnt(7)\n\t" \
    "v_add_f32 v4, v4, v56\n\t" \
    "v_mfma_f32_16x16x32_bf16 v[200:203], v[20:23], v[172:175], v[200:203]\n\t" \
    "ds_read_b128 v[48:51], %[xp] offset:64512\n\t" \
    "v_add_f32 v5, v5, v57\n\t" \
    "v_mfma_f32_16x16x32_bf16 v[204:207], v[24:27], v[164:167], v[204:207]\n\t" \
    "v_add_f32 v6, v6, v58\n\t" \
    "s_waitcnt lgkmcnt(6)\n\t" \
    "v_mfma_f32_16x16x32_bf16 v[208:211], v[24:27], v[172:175], v[208:211]\n\t" \
    "v_add_f32 v7, v7, v59\n\t" \
    "s_waitcnt lgkmcnt(5)\n\t" \
    "v_add_f32 v8, v8, v60\n\t" \
    "v_add_f32 v9, v9, v61\n\t" \
    "v_mfma_f32_16x16x32_bf16 v[196:199], v[28:31], v[168:171], v[196:199]\n\t" \
    "ds_read_b128 v[20:23], %[xp] offset:0\n\t" \
    "v_add_f32 v10, v10, v62\n\t" \
    "v_mfma_f32_16x16x32_bf16 v[200:203], v[28:31], v[176:179], v[200:203]\n\t" \
    "ds_read_b128 v[24:27], %[xp] offset:1024\n\t" \
    "v_add_f32 v11, v11, v63\n\t" \
    "v_mfma_f32_16x16x32_bf16 v[204:207], v[32:35], v[168:171], v[204:207]\n\t" \
    "s_waitcnt lgkmcnt(4)\n\t" \
    "v_add_f32 v12, v12, v64\n\t" \
    "v_mfma_f32_16x16x32_bf16 v[208:211], v[32:35], v[176:179], v[208:211]\n\t" \
    "v_add_f32 v13, v13, v65\n\t" \
    "v_add_f32 v14, v14, v66\n\t" \
    "v_add_f32 v15, v15, v67\n\t" \
    "v_mfma_f32_16x16x32_bf16 v[196:199], v[36:39], v[180:183], v[196:199]\n\t" \
    "ds_read_b128 v[28:31], %[xp] offset:2048\n\t" \
    "v_cvt_pk_bf16_f32 v52, v0, v1\n\t" \
    "v_mfma_f32_16x16x32_bf16 v[200:203], v[36:39], v[188:191], v[200:203]\n\t" \
    "ds_read_b128 v[32:35], %[xp] offset:3072\n\t" \
    "v_cvt_pk_bf16_f32 v53, v2, v3\n\t" \
    "v_mfma_f32_16x16x32_bf16 v[204:207], v[40:43], v[180:183], v[204:207]\n\t" \
    "v_cvt_pk_bf16_f32 v54, v8, v9\n\t" \
    "s_waitcnt lgkmcnt(4)\n\t" \
    "v_mfma_f32_16x16x32_bf16 v[208:211], v[40:43], v[188:191], v[208:211]\n\t" \
    "v_cvt_pk_bf16_f32 v55, v10, v11\n\t" \
    "v_cvt_pk_bf16_f32 v56, v4, v5\n\t" \
    "v_cvt_pk_bf16_f32 v57, v6, v7\n\t" \
    "v_mfma_f32_16x16x32_bf16 v[196:199], v[44:47], v[184:187], v[196:199]\n\t" \
    "ds_read_b128 v[36:39], %[xp] offset:4096\n\t" \
    "v_cvt_pk_bf16_f32 v58, v12, v13\n\t" \
    "v_mfma_f32_16x16x32_bf16 v[200:203], v[44:47], v[192:195], v[200:203]\n\t" \
    "ds_read_b128 v[40:43], %[xp] offset:5120\n\t" \
    "v_cvt_pk_bf16_f32 v59, v14, v15\n\t" \
    "v_mfma_f32_16x16x32_bf16 v[204:207], v[48:51], v[184:187], v[204:207]\n\t" \
    "s_waitcnt lgkmcnt(4)\n\t" \
    "v_mfma_f32_16x16x32_bf16 v[208:211], v[48:51], v[192:195], v[208:211]\n\t" \
    "s_mov_b64 exec, %[em]\n\t" \
    "buffer_store_dwordx4 v[52:55], %[l16], %[rs], %[oo] offen sc1\n\t" \
    "buffer_store_dwordx4 v[56:59], %[l16], %[rs], %[oo] offen offset:1024 sc1\n\t" \
    "s_mov_b64 exec, -1\n\t" \
    "s_add_u32 %[sx], %[sx], 65536\n\t" \
    "s_add_u32 %[t], %[t], 1\n\t" \
    "s_add_u32 %[oo], %[oo], 262144\n\t" \
    "s_waitcnt vmcnt(6)\n\t" \
    "s_cmp_gt_u32 %[t], " STR(SQUICK) " + 1\n\t" \
    "s_cbranch_scc1 B1_%=\n\t" \
    "s_waitcnt vmcnt(0)\n\t" \
    "B1_%=:\n\t" \
    "s_barrier\n\t" \
    "s_cmp_lt_u32 %[t], %[te]\n\t" \
    "s_cbranch_scc1 L1_%=\n\t" \
    "s_cmp_lg_u32 %[fin], 0\n\t" \
    "s_cbranch_scc0 E_%=\n\t" \
    "s_nop 4\n\t" \
    "s_barrier\n\t" \
    "ds_read_b128 v[52:55], %[pa] offset:0\n\t" \
    "ds_read_b128 v[56:59], %[pa] offset:1024\n\t" \
    "ds_read_b128 v[60:63], %[pa] offset:2048\n\t" \
    "ds_read_b128 v[64:67], %[pa] offset:3072\n\t" \
    "s_add_u32 %[b], %[oo], 0x1000\n\t" \
    "s_waitcnt lgkmcnt(0)\n\t" \
    "v_add_f32 v196, v196, v52\n\t" \
    "v_add_f32 v197, v197, v53\n\t" \
    "v_add_f32 v198, v198, v54\n\t" \
    "v_add_f32 v199, v199, v55\n\t" \
    "v_add_f32 v200, v200, v56\n\t" \
    "v_add_f32 v201, v201, v57\n\t" \
    "v_add_f32 v202, v202, v58\n\t" \
    "v_add_f32 v203, v203, v59\n\t" \
    "v_add_f32 v204, v204, v60\n\t" \
    "v_add_f32 v205, v205, v61\n\t" \
    "v_add_f32 v206, v206, v62\n\t" \
    "v_add_f32 v207, v207, v63\n\t" \
    "v_add_f32 v208, v208, v64\n\t" \
    "v_add_f32 v209, v209, v65\n\t" \
    "v_add_f32 v210, v210, v66\n\t" \
    "v_add_f32 v211, v211, v67\n\t" \
    "v_cvt_pk_bf16_f32 v52, v196, v197\n\t" \
    "v_cvt_pk_bf16_f32 v53, v198, v199\n\t" \
    "v_cvt_pk_bf16_f32 v54, v204, v205\n\t" \
    "v_cvt_pk_bf16_f32 v55, v206, v207\n\t" \
    "v_cvt_pk_bf16_f32 v56, v200, v201\n\t" \
    "v_cvt_pk_bf16_f32 v57, v202, v203\n\t" \
    "v_cvt_pk_bf16_f32 v58, v208, v209\n\t" \
    "v_cvt_pk_bf16_f32 v59, v210, v211\n\t" \
    "buffer_store_dwordx4 v[52:55], %[l16], %[rs], %[oo] offen sc1\n\t" \
    "buffer_store_dwordx4 v[56:59], %[l16], %[rs], %[oo] offen offset:1024 sc1\n\t" \
    "E_%=:\n\t" \
    "s_setprio 0\n\t" \
    "s_waitcnt lgkmcnt(0)\n\t" \
    "s_nop 7\n\t" \
    : [t] "+s"(t), [sx] "+s"(sx), [oo] "+s"(oo), [a] "=&s"(tmp), [b] "=&s"(tmp2), [em] "=&s"(em), "+{v[20:27]}"(xf[0]), "+{v[28:35]}"(xf[1]), "+{v[36:43]}"(xf[2]), "+{v[196:203]}"(ac[0]), "+{v[204:211]}"(ac[1]) \
    : [te] "s"(TE_), [fin] "s"(FIN_), [kh] "s"(kh), [sd] "s"(sd), [rs] "s"(rs), [l16] "v"(l16), [xp] "v"(xp), [pa] "v"(pa), [fm] "s"(fm), [pm] "s"(pm), [fo] "s"(fo), [po] "s"(po), [pd] "s"(pd), [tag] "s"(P.tag), "{v[68:75]}"(w[0][0]), "{v[76:83]}"(w[0][1]), "{v[84:91]}"(w[1][0]), "{v[92:99]}"(w[1][1]), "{v[100:107]}"(w[2][0]), "{v[108:115]}"(w[2][1]), "{v[116:123]}"(w[3][0]), "{v[124:131]}"(w[3][1]), "{v[132:139]}"(w[4][0]), "{v[140:147]}"(w[4][1]), "{v[148:155]}"(w[5][0]), "{v[156:163]}"(w[5][1]), "{v[164:171]}"(w[6][0]), "{v[172:179]}"(w[6][1]), "{v[180:187]}"(w[7][0]), "{v[188:195]}"(w[7][1]) \
    : "memory", "scc", "v0", "v1", "v2", "v3", "v4", "v5", "v6", "v7", "v8", "v9", "v10", "v11", "v12", "v13", "v14", "v15", "v44", "v45", "v46", "v47", "v48", "v49", "v50", "v51", "v52", "v53", "v54", "v55", "v56", "v57", "v58", "v59", "v60", "v61", "v62", "v63", "v64", "v65", "v66", "v67")

#define S_LOOP_X(TE_, FIN_) \
  asm volatile( \
    "s_cmp_eq_u32 %[kh], 0\n\t" \
    "s_cbranch_scc0 L1_%=\n\t" \
    "L0_%=:\n\t" \
    "s_setprio 0\n\t" \
    "s_nop 3\n\t" \
    "ds_write_b128 %[pa], v[196:199] offset:0\n\t" \
    "ds_write_b128 %[pa], v[200:203] offset:1024\n\t" \
    "ds_write_b128 %[pa], v[204:207] offset:2048\n\t" \
    "ds_write_b128 %[pa], v[208:211] offset:3072\n\t" \
    "s_sub_u32 %[a], %[t], 1\n\t" \
    "v_mov_b32 v53, %[tag]\n\t" \
    "v_mov_b32 v52, %[a]\n\t" \
    "s_sub_u32 %[b], %[t], 2\n\t" \
    "s_cmp_lt_u32 %[b], " STR(SQUICK) "\n\t" \
    "s_cselect_b64 exec, %[fm], 0\n\t" \
    "buffer_store_dwordx2 v[52:53], %[l16], %[rs], %[fo] offen sc1\n\t" \
    "s_mov_b64 exec, -1\n\t" \
    "s_add_u32 m0, %[sd], 49152\n\t" \
    "s_add_u32 %[a], %[sx], 278528\n\t" \
    "s_waitcnt lgkmcnt(0)\n\t" \
    "buffer_load_dwordx4 %[l16], %[rs], %[a] offen offset:0 sc1 lds\n\t" \
    "buffer_load_dwordx4 %[l16], %[rs], %[a] offen offset:1024 sc1 lds\n\t" \
    "buffer_load_dwordx4 %[l16], %[rs], %[a] offen offset:2048 sc1 lds\n\t" \
    "buffer_load_dwordx4 %[l16], %[rs], %[a] offen offset:3072 sc1 lds\n\t" \
    "v_mfma_f32_16x16x32_bf16 v[0:3], v[68:71], v[20:23], 0\n\t" \
    "ds_read_b128 v[44:47], %[xp] offset:6144\n\t" \
    "v_mfma_f32_16x16x32_bf16 v[4:7], v[76:79], v[20:23], 0\n\t" \
    "ds_read_b128 v[48:51], %[xp] offset:7168\n\t" \
    "v_mfma_f32_16x16x32_bf16 v[8:11], v[68:71], v[24:27], 0\n\t" \
    "v_mfma_f32_16x16x32_bf16 v[12:15], v[76:79], v[24:27], 0\n\t" \
    "v_mfma_f32_16x16x32_bf16 v[0:3], v[72:75], v[28:31], v[0:3]\n\t" \
    "ds_read_b128 v[20:23], %[xp] offset:8192\n\t" \
    "v_mfma_f32_16x16x32_bf16 v[4:7], v[80:83], v[28:31], v[4:7]\n\t" \
    "ds_read_b128 v[24:27], %[xp] offset:9216\n\t" \
    "v_mfma_f32_16x16x32_bf16 v[8:11], v[72:75], v[32:35], v[8:11]\n\t" \
    "v_mfma_f32_16x16x32_bf16 v[12:15], v[80:83], v[32:35], v[12:15]\n\t" \
    "v_mfma_f32_16x16x32_bf16 v[0:3], v[84:87], v[36:39], v[0:3]\n\t" \
    "ds_read_b128 v[28:31], %[xp] offset:10240\n\t" \
    "v_mfma_f32_16x16x32_bf16 v[4:7], v[92:95], v[36:39], v[4:7]\n\t" \
    "ds_read_b128 v[32:35], %[xp] offset:11264\n\t" \
    "v_mfma_f32_16x16x32_bf16 v[8:11], v[84:87], v[40:43], v[8:11]\n\t" \
    "s_waitcnt lgkmcnt(4)\n\t" \
    "v_mfma_f32_16x16x32_bf16 v[12:15], v[92:95], v[40:43], v[12:15]\n\t" \
    "v_mfma_f32_16x16x32_bf16 v[0:3], v[88:91], v[44:47], v[0:3]\n\t" \
    "ds_read_b128 v[36:39], %[xp] offset:12288\n\t" \
    "v_mfma_f32_16x16x32_bf16 v[4:7], v[96:99], v[44:47], v[4:7]\n\t" \
    "ds_read_b128 v[40:43], %[xp] offset:13312\n\t" \
    "v_mfma_f32_16x16x32_bf16 v[8:11], v[88:91], v[48:51], v[8:11]\n\t" \
    "s_waitcnt lgkmcnt(4)\n\t" \
    "v_mfma_f32_16x16x32_bf16 v[12:15], v[96:99], v[48:51], v[12:15]\n\t" \
    "v_mfma_f32_16x16x32_bf16 v[0:3], v[100:103], v[20:23], v[0:3]\n\t" \
    "ds_read_b128 v[44:47], %[xp] offset:14336\n\t" \
    "v_mfma_f32_16x16x32_bf16 v[4:7], v[108:111], v[20:23], v[4:7]\n\t" \
    "ds_read_b128 v[48:51], %[xp] offset:15360\n\t" \
    "v_mfma_f32_16x16x32_bf16 v[8:11], v[100:103], v[24:27], v[8:11]\n\t" \
    "s_waitcnt lgkmcnt(4)\n\t" \
    "v_mfma_f32_16x16x32_bf16 v[12:15], v[108:111], v[24:27], v[12:15]\n\t" \
    "v_mfma_f32_16x16x32_bf16 v[0:3], v[104:107], v[28:31], v[0:3]\n\t" \
    "ds_read_b128 v[20:23], %[xp] offset:16384\n\t" \
    "v_mfma_f32_16x16x32_bf16 v[4:7], v[112:115], v[28:31], v[4:7]\n\t" \
    "ds_read_b128 v[24:27], %[xp] offset:17408\n\t" \
    "v_mfma_f32_16x16x32_bf16 v[8:11], v[104:107], v[32:35], v[8:11]\n\t" \
    "s_waitcnt lgkmcnt(4)\n\t" \
    "v_mfma_f32_16x16x32_bf16 v[12:15], v[112:115], v[32:35], v[12:15]\n\t" \
    "v_mfma_f32_16x16x32_bf16 v[0:3], v[116:119], v[36:39], v[0:3]\n\t" \
    "ds_read_b128 v[28:31], %[xp] offset:18432\n\t" \
    "v_mfma_f32_16x16x32_bf16 v[4:7], v[124:127], v[36:39], v[4:7]\n\t" \
    "ds_read_b128 v[32:35], %[xp] offset:19456\n\t" \
    "v_mfma_f32_16x16x32_bf16 v[8:11], v[116:119], v[40:43], v[8:11]\n\t" \
    "s_waitcnt lgkmcnt(4)\n\t" \
    "v_mfma_f32_16x16x32_bf16 v[12:15], v[124:127], v[40:43], v[12:15]\n\t" \
    "v_mfma_f32_16x16x32_bf16 v[0:3], v[120:123], v[44:47], v[0:3]\n\t" \
    "ds_read_b128 v[36:39], %[xp] offset:20480\n\t" \
    "v_mfma_f32_16x16x32_bf16 v[4:7], v[128:131], v[44:47], v[4:7]\n\t" \
    "ds_read_b128 v[40:43], %[xp] offset:21504\n\t" \
    "v_mfma_f32_16x16x32_bf16 v[8:11], v[120:123], v[48:51], v[8:11]\n\t" \
    "s_waitcnt lgkmcnt(4)\n\t" \
    "v_mfma_f32_16x16x32_bf16 v[12:15], v[128:131], v[48:51], v[12:15]\n\t" \
    "s_waitcnt vmcnt(4)\n\t" \
    "s_barrier\n\t" \
    "s_setprio 0\n\t" \
    "s_sub_u32 %[a], %[t], 1\n\t" \
    "v_mov_b32 v53, %[tag]\n\t" \
    "v_mov_b32 v52, %[a]\n\t" \
    "s_cmp_gt_u32 %[t], 1\n\t" \
    "s_cselect_b64 exec, %[fm], 0\n\t" \
    "buffer_store_dwordx2 v[52:53], %[l16], %[rs], %[fo] offen sc1\n\t" \
    "s_mov_b64 exec, %[pm]\n\t" \
    "s_mov_b32 m0, %[pd]\n\t" \
    "s_add_u32 %[a], %[sx], %[po]\n\t" \
    "buffer_load_dwordx4 %[l16], %[rs], %[a] offen sc1 lds\n\t" \
    "s_mov_b64 exec, -1\n\t" \
    "s_add_u32 m0, %[sd], 0\n\t" \
    "s_add_u32 %[a], %[sx], 524288\n\t" \
    "buffer_load_dwordx4 %[l16], %[rs], %[a] offen offset:0 sc1 lds\n\t" \
    "buffer_load_dwordx4 %[l16], %[rs], %[a] offen offset:1024 sc1 lds\n\t" \
    "buffer_load_dwordx4 %[l16], %[rs], %[a] offen offset:2048 sc1 lds\n\t" \
    "buffer_load_dwordx4 %[l16], %[rs], %[a] offen offset:3072 sc1 lds\n\t" \
    "v_mfma_f32_16x16x32_bf16 v[0:3], v[132:135], v[20:23], v[0:3]\n\t" \
    "ds_read_b128 v[44:47], %[xp] offset:22528\n\t" \
    "v_mfma_f32_16x16x32_bf16 v[4:7], v[140:143], v[20:23], v[4:7]\n\t" \
    "ds_read_b128 v[48:51], %[xp] offset:23552\n\t" \
    "v_mfma_f32_16x16x32_bf16 v[8:11], v[132:135], v[24:27], v[8:11]\n\t" \
    "s_waitcnt lgkmcnt(4)\n\t" \
    "v_mfma_f32_16x16x32_bf16 v[12:15], v[140:143], v[24:27], v[12:15]\n\t" \
    "v_mfma_f32_16x16x32_bf16 v[0:3], v[136:139], v[28:31], v[0:3]\n\t" \
    "ds_read_b128 v[20:23], %[xp] offset:24576\n\t" \
    "v_mfma_f32_16x16x32_bf16 v[4:7], v[144:147], v[28:31], v[4:7]\n\t" \
    "ds_read_b128 v[24:27], %[xp] offset:25600\n\t" \
    "v_mfma_f32_16x16x32_bf16 v[8:11], v[136:139], v[32:35], v[8:11]\n\t" \
    "s_waitcnt lgkmcnt(4)\n\t" \
    "v_mfma_f32_16x16x32_bf16 v[12:15], v[144:147], v[32:35], v[12:15]\n\t" \
    "v_mfma_f32_16x16x32_bf16 v[0:3], v[148:151], v[36:39], v[0:3]\n\t" \
    "ds_read_b128 v[28:31], %[xp] offset:26624\n\t" \
    "v_mfma_f32_16x16x32_bf16 v[4:7], v[156:159], v[36:39], v[4:7]\n\t" \
    "ds_read_b128 v[32:35], %[xp] offset:27648\n\t" \
    "v_mfma_f32_16x16x32_bf16 v[8:11], v[148:151], v[40:43], v[8:11]\n\t" \
    "s_waitcnt lgkmcnt(4)\n\t" \
    "v_mfma_f32_16x16x32_bf16 v[12:15], v[156:159], v[40:43], v[12:15]\n\t" \
    "v_mfma_f32_16x16x32_bf16 v[0:3], v[152:155], v[44:47], v[0:3]\n\t" \
    "ds_read_b128 v[36:39], %[xp] offset:28672\n\t" \
    "v_mfma_f32_16x16x32_bf16 v[4:7], v[160:163], v[44:47], v[4:7]\n\t" \
    "ds_read_b128 v[40:43], %[xp] offset:29696\n\t" \
    "v_mfma_f32_16x16x32_bf16 v[8:11], v[152:155], v[48:51], v[8:11]\n\t" \
    "s_waitcnt lgkmcnt(4)\n\t" \
    "v_mfma_f32_16x16x32_bf16 v[12:15], v[160:163], v[48:51], v[12:15]\n\t" \
    "v_mfma_f32_16x16x32_bf16 v[0:3], v[164:167], v[20:23], v[0:3]\n\t" \
    "ds_read_b128 v[44:47], %[xp] offset:30720\n\t" \
    "v_mfma_f32_16x16x32_bf16 v[4:7], v[172:175], v[20:23], v[4:7]\n\t" \
    "ds_read_b128 v[48:51], %[xp] offset:31744\n\t" \
    "v_mfma_f32_16x16x32_bf16 v[8:11], v[164:167], v[24:27], v[8:11]\n\t" \
    "s_waitcnt lgkmcnt(4)\n\t" \
    "v_mfma_f32_16x16x32_bf16 v[12:15], v[172:175], v[24:27], v[12:15]\n\t" \
    "v_mfma_f32_16x16x32_bf16 v[0:3], v[168:171], v[28:31], v[0:3]\n\t" \
    "ds_read_b128 v[20:23], %[xp] offset:32768\n\t" \
    "v_mfma_f32_16x16x32_bf16 v[4:7], v[176:179], v[28:31], v[4:7]\n\t" \
    "ds_read_b128 v[24:27], %[xp] offset:33792\n\t" \
    "v_mfma_f32_16x16x32_bf16 v[8:11], v[168:171], v[32:35], v[8:11]\n\t" \
    "s_waitcnt lgkmcnt(4)\n\t" \
    "v_mfma_f32_16x16x32_bf16 v[12:15], v[176:179], v[32:35], v[12:15]\n\t" \
    "v_mfma_f32_16x16x32_bf16 v[0:3], v[180:183], v[36:39], v[0:3]\n\t" \
    "ds_read_b128 v[28:31], %[xp] offset:34816\n\t" \
    "v_mfma_f32_16x16x32_bf16 v[4:7], v[188:191], v[36:39], v[4:7]\n\t" \
    "ds_read_b128 v[32:35], %[xp] offset:35840\n\t" \
    "v_mfma_f32_16x16x32_bf16 v[8:11], v[180:183], v[40:43], v[8:11]\n\t" \
    "s_waitcnt lgkmcnt(4)\n\t" \
    "v_mfma_f32_16x16x32_bf16 v[12:15], v[188:191], v[40:43], v[12:15]\n\t" \
    "v_mfma_f32_16x16x32_bf16 v[0:3], v[184:187], v[44:47], v[0:3]\n\t" \
    "ds_read_b128 v[36:39], %[xp] offset:36864\n\t" \
    "v_mfma_f32_16x16x32_bf16 v[4:7], v[192:195], v[44:47], v[4:7]\n\t" \
    "ds_read_b128 v[40:43], %[xp] offset:37888\n\t" \
    "v_mfma_f32_16x16x32_bf16 v[8:11], v[184:187], v[48:51], v[8:11]\n\t" \
    "s_waitcnt lgkmcnt(4)\n\t" \
    "v_mfma_f32_16x16x32_bf16 v[12:15], v[192:195], v[48:51], v[12:15]\n\t" \
    "s_add_u32 %[sx], %[sx], 262144\n\t" \
    "s_add_u32 %[t], %[t], 1\n\t" \
    "s_waitcnt vmcnt(4)\n\t" \
    "s_barrier\n\t" \
    "s_setprio 0\n\t" \
    "s_nop 3\n\t" \
    "ds_write_b128 %[pa], v[0:3] offset:0\n\t" \
    "ds_write_b128 %[pa], v[4:7] offset:1024\n\t" \
    "ds_write_b128 %[pa], v[8:11] offset:2048\n\t" \
    "ds_write_b128 %[pa], v[12:15] offset:3072\n\t" \
    "s_sub_u32 %[a], %[t], 1\n\t" \
    "v_mov_b32 v53, %[tag]\n\t" \
    "v_mov_b32 v52, %[a]\n\t" \
    "s_sub_u32 %[b], %[t], 2\n\t" \
    "s_cmp_lt_u32 %[b], " STR(SQUICK) "\n\t" \
    "s_cselect_b64 exec, %[fm], 0\n\t" \
    "buffer_store_dwordx2 v[52:53], %[l16], %[rs], %[fo] offen sc1\n\t" \
    "s_mov_b64 exec, -1\n\t" \
    "s_add_u32 m0, %[sd], 16384\n\t" \
    "s_add_u32 %[a], %[sx], 278528\n\t" \
    "s_waitcnt lgkmcnt(0)\n\t" \
    "buffer_load_dwordx4 %[l16], %[rs], %[a] offen offset:0 sc1 lds\n\t" \
    "buffer_load_dwordx4 %[l16], %[rs], %[a] offen offset:1024 sc1 lds\n\t" \
    "buffer_load_dwordx4 %[l16], %[rs], %[a] offen offset:2048 sc1 lds\n\t" \
    "buffer_load_dwordx4 %[l16], %[rs], %[a] offen offset:3072 sc1 lds\n\t" \
    "v_mfma_f32_16x16x32_bf16 v[196:199], v[68:71], v[20:23], 0\n\t" \
    "ds_read_b128 v[44:47], %[xp] offset:38912\n\t" \
    "v_mfma_f32_16x16x32_bf16 v[200:203], v[76:79], v[20:23], 0\n\t" \
    "ds_read_b128 v[48:51], %[xp] offset:39936\n\t" \
    "v_mfma_f32_16x16x32_bf16 v[204:207], v[68:71], v[24:27], 0\n\t" \
    "v_mfma_f32_16x16x32_bf16 v[208:211], v[76:79], v[24:27], 0\n\t" \
    "v_mfma_f32_16x16x32_bf16 v[196:199], v[72:75], v[28:31], v[196:199]\n\t" \
    "ds_read_b128 v[20:23], %[xp] offset:40960\n\t" \
    "v_mfma_f32_16x16x32_bf16 v[200:203], v[80:83], v[28:31], v[200:203]\n\t" \
    "ds_read_b128 v[24:27], %[xp] offset:41984\n\t" \
    "v_mfma_f32_16x16x32_bf16 v[204:207], v[72:75], v[32:35], v[204:207]\n\t" \
    "v_mfma_f32_16x16x32_bf16 v[208:211], v[80:83], v[32:35], v[208:211]\n\t" \
    "v_mfma_f32_16x16x32_bf16 v[196:199], v[84:87], v[36:39], v[196:199]\n\t" \
    "ds_read_b128 v[28:31], %[xp] offset:43008\n\t" \
    "v_mfma_f32_16x16x32_bf16 v[200:203], v[92:95], v[36:39], v[200:203]\n\t" \
    "ds_read_b128 v[32:35], %[xp] offset:44032\n\t" \
    "v_mfma_f32_16x16x32_bf16 v[204:207], v[84:87], v[40:43], v[204:207]\n\t" \
    "s_waitcnt lgkmcnt(4)\n\t" \
    "v_mfma_f32_16x16x32_bf16 v[208:211], v[92:95], v[40:43], v[208:211]\n\t" \
    "v_mfma_f32_16x16x32_bf16 v[196:199], v[88:91], v[44:47], v[196:199]\n\t" \
    "ds_read_b128 v[36:39], %[xp] offset:45056\n\t" \
    "v_mfma_f32_16x16x32_bf16 v[200:203], v[96:99], v[44:47], v[200:203]\n\t" \
    "ds_read_b128 v[40:43], %[xp] offset:46080\n\t" \
    "v_mfma_f32_16x16x32_bf16 v[204:207], v[88:91], v[48:51], v[204:207]\n\t" \
    "s_waitcnt lgkmcnt(4)\n\t" \
    "v_mfma_f32_16x16x32_bf16 v[208:211], v[96:99], v[48:51], v[208:211]\n\t" \
    "v_mfma_f32_16x16x32_bf16 v[196:199], v[100:103], v[20:23], v[196:199]\n\t" \
    "ds_read_b128 v[44:47], %[xp] offset:47104\n\t" \
    "v_mfma_f32_16x16x32_bf16 v[200:203], v[108:111], v[20:23], v[200:203]\n\t" \
    "ds_read_b128 v[48:51], %[xp] offset:48128\n\t" \
    "v_mfma_f32_16x16x32_bf16 v[204:207], v[100:103], v[24:27], v[204:207]\n\t" \
    "s_waitcnt lgkmcnt(4)\n\t" \
    "v_mfma_f32_16x16x32_bf16 v[208:211], v[108:111], v[24:27], v[208:211]\n\t" \
    "v_mfma_f32_16x16x32_bf16 v[196:199], v[104:107], v[28:31], v[196:199]\n\t" \
    "ds_read_b128 v[20:23], %[xp] offset:49152\n\t" \
    "v_mfma_f32_16x16x32_bf16 v[200:203], v[112:115], v[28:31], v[200:203]\n\t" \
    "ds_read_b128 v[24:27], %[xp] offset:50176\n\t" \
    "v_mfma_f32_16x16x32_bf16 v[204:207], v[104:107], v[32:35], v[204:207]\n\t" \
    "s_waitcnt lgkmcnt(4)\n\t" \
    "v_mfma_f32_16x16x32_bf16 v[208:211], v[112:115], v[32:35], v[208:211]\n\t" \
    "v_mfma_f32_16x16x32_bf16 v[196:199], v[116:119], v[36:39], v[196:199]\n\t" \
    "ds_read_b128 v[28:31], %[xp] offset:51200\n\t" \
    "v_mfma_f32_16x16x32_bf16 v[200:203], v[124:127], v[36:39], v[200:203]\n\t" \
    "ds_read_b128 v[32:35], %[xp] offset:52224\n\t" \
    "v_mfma_f32_16x16x32_bf16 v[204:207], v[116:119], v[40:43], v[204:207]\n\t" \
    "s_waitcnt lgkmcnt(4)\n\t" \
    "v_mfma_f32_16x16x32_bf16 v[208:211], v[124:127], v[40:43], v[208:211]\n\t" \
    "v_mfma_f32_16x16x32_bf16 v[196:199], v[120:123], v[44:47], v[196:199]\n\t" \
    "ds_read_b128 v[36:39], %[xp] offset:53248\n\t" \
    "v_mfma_f32_16x16x32_bf16 v[200:203], v[128:131], v[44:47], v[200:203]\n\t" \
    "ds_read_b128 v[40:43], %[xp] offset:54272\n\t" \
    "v_mfma_f32_16x16x32_bf16 v[204:207], v[120:123], v[48:51], v[204:207]\n\t" \
    "s_waitcnt lgkmcnt(4)\n\t" \
    "v_mfma_f32_16x16x32_bf16 v[208:211], v[128:131], v[48:51], v[208:211]\n\t" \
    "s_waitcnt vmcnt(4)\n\t" \
    "s_barrier\n\t" \
    "s_setprio 0\n\t" \
    "s_sub_u32 %[a], %[t], 1\n\t" \
    "v_mov_b32 v53, %[tag]\n\t" \
    "v_mov_b32 v52, %[a]\n\t" \
    "s_cmp_gt_u32 %[t], 1\n\t" \
    "s_cselect_b64 exec, %[fm], 0\n\t" \
    "buffer_store_dwordx2 v[52:53], %[l16], %[rs], %[fo] offen sc1\n\t" \
    "s_mov_b64 exec, %[pm]\n\t" \
    "s_mov_b32 m0, %[pd]\n\t" \
    "s_add_u32 %[a], %[sx], %[po]\n\t" \
    "buffer_load_dwordx4 %[l16], %[rs], %[a] offen sc1 lds\n\t" \
    "s_mov_b64 exec, -1\n\t" \
    "s_add_u32 m0, %[sd], 32768\n\t" \
    "s_add_u32 %[a], %[sx], 524288\n\t" \
    "buffer_load_dwordx4 %[l16], %[rs], %[a] offen offset:0 sc1 lds\n\t" \
    "buffer_load_dwordx4 %[l16], %[rs], %[a] offen offset:1024 sc1 lds\n\t" \
    "buffer_load_dwordx4 %[l16], %[rs], %[a] offen offset:2048 sc1 lds\n\t" \
    "buffer_load_dwordx4 %[l16], %[rs], %[a] offen offset:3072 sc1 lds\n\t" \
    "v_mfma_f32_16x16x32_bf16 v[196:199], v[132:135], v[20:23], v[196:199]\n\t" \
    "ds_read_b128 v[44:47], %[xp] offset:55296\n\t" \
    "v_mfma_f32_16x16x32_bf16 v[200:203], v[140:143], v[20:23], v[200:203]\n\t" \
    "ds_read_b128 v[48:51], %[xp] offset:56320\n\t" \
    "v_mfma_f32_16x16x32_bf16 v[204:207], v[132:135], v[24:27], v[204:207]\n\t" \
    "s_waitcnt lgkmcnt(4)\n\t" \
    "v_mfma_f32_16x16x32_bf16 v[208:211], v[140:143], v[24:27], v[208:211]\n\t" \
    "v_mfma_f32_16x16x32_bf16 v[196:199], v[136:139], v[28:31], v[196:199]\n\t" \
    "ds_read_b128 v[20:23], %[xp] offset:57344\n\t" \
    "v_mfma_f32_16x16x32_bf16 v[200:203], v[144:147], v[28:31], v[200:203]\n\t" \
    "ds_read_b128 v[24:27], %[xp] offset:58368\n\t" \
    "v_mfma_f32_16x16x32_bf16 v[204:207], v[136:139], v[32:35], v[204:207]\n\t" \
    "s_waitcnt lgkmcnt(4)\n\t" \
    "v_mfma_f32_16x16x32_bf16 v[208:211], v[144:147], v[32:35], v[208:211]\n\t" \
    "v_mfma_f32_16x16x32_bf16 v[196:199], v[148:151], v[36:39], v[196:199]\n\t" \
    "ds_read_b128 v[28:31], %[xp] offset:59392\n\t" \
    "v_mfma_f32_16x16x32_bf16 v[200:203], v[156:159], v[36:39], v[200:203]\n\t" \
    "ds_read_b128 v[32:35], %[xp] offset:60416\n\t" \
    "v_mfma_f32_16x16x32_bf16 v[204:207], v[148:151], v[40:43], v[204:207]\n\t" \
    "s_waitcnt lgkmcnt(4)\n\t" \
    "v_mfma_f32_16x16x32_bf16 v[208:211], v[156:159], v[40:43], v[208:211]\n\t" \
    "v_mfma_f32_16x16x32_bf16 v[196:199], v[152:155], v[44:47], v[196:199]\n\t" \
    "ds_read_b128 v[36:39], %[xp] offset:61440\n\t" \
    "v_mfma_f32_16x16x32_bf16 v[200:203], v[160:163], v[44:47], v[200:203]\n\t" \
    "ds_read_b128 v[40:43], %[xp] offset:62464\n\t" \
    "v_mfma_f32_16x16x32_bf16 v[204:207], v[152:155], v[48:51], v[204:207]\n\t" \
    "s_waitcnt lgkmcnt(4)\n\t" \
    "v_mfma_f32_16x16x32_bf16 v[208:211], v[160:163], v[48:51], v[208:211]\n\t" \
    "v_mfma_f32_16x16x32_bf16 v[196:199], v[164:167], v[20:23], v[196:199]\n\t" \
    "ds_read_b128 v[44:47], %[xp] offset:63488\n\t" \
    "v_mfma_f32_16x16x32_bf16 v[200:203], v[172:175], v[20:23], v[200:203]\n\t" \
    "ds_read_b128 v[48:51], %[xp] offset:64512\n\t" \
    "v_mfma_f32_16x16x32_bf16 v[204:207], v[164:167], v[24:27], v[204:207]\n\t" \
    "s_waitcnt lgkmcnt(4)\n\t" \
    "v_mfma_f32_16x16x32_bf16 v[208:211], v[172:175], v[24:27], v[208:211]\n\t" \
    "v_mfma_f32_16x16x32_bf16 v[196:199], v[168:171], v[28:31], v[196:199]\n\t" \
    "ds_read_b128 v[20:23], %[xp] offset:0\n\t" \
    "v_mfma_f32_16x16x32_bf16 v[200:203], v[176:179], v[28:31], v[200:203]\n\t" \
    "ds_read_b128 v[24:27], %[xp] offset:1024\n\t" \
    "v_mfma_f32_16x16x32_bf16 v[204:207], v[168:171], v[32:35], v[204:207]\n\t" \
    "s_waitcnt lgkmcnt(4)\n\t" \
    "v_mfma_f32_16x16x32_bf16 v[208:211], v[176:179], v[32:35], v[208:211]\n\t" \
    "v_mfma_f32_16x16x32_bf16 v[196:199], v[180:183], v[36:39], v[196:199]\n\t" \
    "ds_read_b128 v[28:31], %[xp] offset:2048\n\t" \
    "v_mfma_f32_16x16x32_bf16 v[200:203], v[188:191], v[36:39], v[200:203]\n\t" \
    "ds_read_b128 v[32:35], %[xp] offset:3072\n\t" \
    "v_mfma_f32_16x16x32_bf16 v[204:207], v[180:183], v[40:43], v[204:207]\n\t" \
    "s_waitcnt lgkmcnt(4)\n\t" \
    "v_mfma_f32_16x16x32_bf16 v[208:211], v[188:191], v[40:43], v[208:211]\n\t" \
    "v_mfma_f32_16x16x32_bf16 v[196:199], v[184:187], v[44:47], v[196:199]\n\t" \
    "ds_read_b128 v[36:39], %[xp] offset:4096\n\t" \
    "v_mfma_f32_16x16x32_bf16 v[200:203], v[192:195], v[44:47], v[200:203]\n\t" \
    "ds_read_b128 v[40:43], %[xp] offset:5120\n\t" \
    "v_mfma_f32_16x16x32_bf16 v[204:207], v[184:187], v[48:51], v[204:207]\n\t" \
    "s_waitcnt lgkmcnt(4)\n\t" \
    "v_mfma_f32_16x16x32_bf16 v[208:211], v[192:195], v[48:51], v[208:211]\n\t" \
    "s_add_u32 %[sx], %[sx], 262144\n\t" \
    "s_add_u32 %[t], %[t], 1\n\t" \
    "s_waitcnt vmcnt(4)\n\t" \
    "s_barrier\n\t" \
    "s_cmp_lt_u32 %[t], %[te]\n\t" \
    "s_cbranch_scc1 L0_%=\n\t" \
    "s_cmp_lg_u32 %[fin], 0\n\t" \
    "s_cbranch_scc0 E_%=\n\t" \
    "s_nop 4\n\t" \
    "ds_write_b128 %[pa], v[196:199] offset:0\n\t" \
    "ds_write_b128 %[pa], v[200:203] offset:1024\n\t" \
    "ds_write_b128 %[pa], v[204:207] offset:2048\n\t" \
    "ds_write_b128 %[pa], v[208:211] offset:3072\n\t" \
    "s_waitcnt lgkmcnt(0)\n\t" \
    "s_barrier\n\t" \
    "s_branch E_%=\n\t" \
    "L1_%=:\n\t" \
    "s_setprio 1\n\t" \
    "s_add_u32 m0, %[sd], 49152\n\t" \
    "s_add_u32 %[a], %[sx], 278528\n\t" \
    "v_mfma_f32_16x16x32_bf16 v[0:3], v[68:71], v[20:23], 0\n\t" \
    "ds_read_b128 v[44:47], %[xp] offset:6144\n\t" \
    "v_mfma_f32_16x16x32_bf16 v[4:7], v[76:79], v[20:23], 0\n\t" \
    "ds_read_b128 v[48:51], %[xp] offset:7168\n\t" \
    "v_mfma_f32_16x16x32_bf16 v[8:11], v[68:71], v[24:27], 0\n\t" \
    "s_waitcnt lgkmcnt(4)\n\t" \
    "v_mfma_f32_16x16x32_bf16 v[12:15], v[76:79], v[24:27], 0\n\t" \
    "v_mfma_f32_16x16x32_bf16 v[0:3], v[72:75], v[28:31], v[0:3]\n\t" \
    "ds_read_b128 v[20:23], %[xp] offset:8192\n\t" \
    "v_mfma_f32_16x16x32_bf16 v[4:7], v[80:83], v[28:31], v[4:7]\n\t" \
    "ds_read_b128 v[24:27], %[xp] offset:9216\n\t" \
    "v_mfma_f32_16x16x32_bf16 v[8:11], v[72:75], v[32:35], v[8:11]\n\t" \
    "s_waitcnt lgkmcnt(4)\n\t" \
    "v_mfma_f32_16x16x32_bf16 v[12:15], v[80:83], v[32:35], v[12:15]\n\t" \
    "v_mfma_f32_16x16x32_bf16 v[0:3], v[84:87], v[36:39], v[0:3]\n\t" \
    "ds_read_b128 v[28:31], %[xp] offset:10240\n\t" \
    "v_mfma_f32_16x16x32_bf16 v[4:7], v[92:95], v[36:39], v[4:7]\n\t" \
    "ds_read_b128 v[32:35], %[xp] offset:11264\n\t" \
    "v_mfma_f32_16x16x32_bf16 v[8:11], v[84:87], v[40:43], v[8:11]\n\t" \
    "s_waitcnt lgkmcnt(4)\n\t" \
    "v_mfma_f32_16x16x32_bf16 v[12:15], v[92:95], v[40:43], v[12:15]\n\t" \
    "v_mfma_f32_16x16x32_bf16 v[0:3], v[88:91], v[44:47], v[0:3]\n\t" \
    "ds_read_b128 v[36:39], %[xp] offset:12288\n\t" \
    "v_mfma_f32_16x16x32_bf16 v[4:7], v[96:99], v[44:47], v[4:7]\n\t" \
    "ds_read_b128 v[40:43], %[xp] offset:13312\n\t" \
    "v_mfma_f32_16x16x32_bf16 v[8:11], v[88:91], v[48:51], v[8:11]\n\t" \
    "s_waitcnt lgkmcnt(4)\n\t" \
    "v_mfma_f32_16x16x32_bf16 v[12:15], v[96:99], v[48:51], v[12:15]\n\t" \
    "v_mfma_f32_16x16x32_bf16 v[0:3], v[100:103], v[20:23], v[0:3]\n\t" \
    "ds_read_b128 v[44:47], %[xp] offset:14336\n\t" \
    "v_mfma_f32_16x16x32_bf16 v[4:7], v[108:111], v[20:23], v[4:7]\n\t" \
    "ds_read_b128 v[48:51], %[xp] offset:15360\n\t" \
    "v_mfma_f32_16x16x32_bf16 v[8:11], v[100:103], v[24:27], v[8:11]\n\t" \
    "s_waitcnt lgkmcnt(4)\n\t" \
    "v_mfma_f32_16x16x32_bf16 v[12:15], v[108:111], v[24:27], v[12:15]\n\t" \
    "buffer_load_dwordx4 %[l16], %[rs], %[a] offen offset:0 sc1 lds\n\t" \
    "v_mfma_f32_16x16x32_bf16 v[0:3], v[104:107], v[28:31], v[0:3]\n\t" \
    "ds_read_b128 v[20:23], %[xp] offset:16384\n\t" \
    "v_mfma_f32_16x16x32_bf16 v[4:7], v[112:115], v[28:31], v[4:7]\n\t" \
    "ds_read_b128 v[24:27], %[xp] offset:17408\n\t" \
    "v_mfma_f32_16x16x32_bf16 v[8:11], v[104:107], v[32:35], v[8:11]\n\t" \
    "s_waitcnt lgkmcnt(4)\n\t" \
    "v_mfma_f32_16x16x32_bf16 v[12:15], v[112:115], v[32:35], v[12:15]\n\t" \
    "buffer_load_dwordx4 %[l16], %[rs], %[a] offen offset:1024 sc1 lds\n\t" \
    "v_mfma_f32_16x16x32_bf16 v[0:3], v[116:119], v[36:39], v[0:3]\n\t" \
    "ds_read_b128 v[28:31], %[xp] offset:18432\n\t" \
    "v_mfma_f32_16x16x32_bf16 v[4:7], v[124:127], v[36:39], v[4:7]\n\t" \
    "ds_read_b128 v[32:35], %[xp] offset:19456\n\t" \
    "v_mfma_f32_16x16x32_bf16 v[8:11], v[116:119], v[40:43], v[8:11]\n\t" \
    "s_waitcnt lgkmcnt(4)\n\t" \
    "v_mfma_f32_16x16x32_bf16 v[12:15], v[124:127], v[40:43], v[12:15]\n\t" \
    "buffer_load_dwordx4 %[l16], %[rs], %[a] offen offset:2048 sc1 lds\n\t" \
    "v_mfma_f32_16x16x32_bf16 v[0:3], v[120:123], v[44:47], v[0:3]\n\t" \
    "ds_read_b128 v[36:39], %[xp] offset:20480\n\t" \
    "v_mfma_f32_16x16x32_bf16 v[4:7], v[128:131], v[44:47], v[4:7]\n\t" \
    "ds_read_b128 v[40:43], %[xp] offset:21504\n\t" \
    "v_mfma_f32_16x16x32_bf16 v[8:11], v[120:123], v[48:51], v[8:11]\n\t" \
    "s_waitcnt lgkmcnt(4)\n\t" \
    "v_mfma_f32_16x16x32_bf16 v[12:15], v[128:131], v[48:51], v[12:15]\n\t" \
    "buffer_load_dwordx4 %[l16], %[rs], %[a] offen offset:3072 sc1 lds\n\t" \
    "s_waitcnt vmcnt(4)\n\t" \
    "s_barrier\n\t" \
    "s_setprio 1\n\t" \
    "s_add_u32 m0, %[sd], 0\n\t" \
    "s_add_u32 %[a], %[sx], 524288\n\t" \
    "s_add_u32 %[b], %[oo], 0x1000\n\t" \
    "s_cmp_lg_u32 %[t], 0\n\t" \
    "s_cselect_b64 %[em], -1, 0\n\t" \
    "v_mfma_f32_16x16x32_bf16 v[0:3], v[132:135], v[20:23], v[0:3]\n\t" \
    "ds_read_b128 v[44:47], %[xp] offset:22528\n\t" \
    "v_mfma_f32_16x16x32_bf16 v[4:7], v[140:143], v[20:23], v[4:7]\n\t" \
    "ds_read_b128 v[48:51], %[xp] offset:23552\n\t" \
    "v_mfma_f32_16x16x32_bf16 v[8:11], v[132:135], v[24:27], v[8:11]\n\t" \
    "ds_read_b128 v[52:55], %[pa] offset:0\n\t" \
    "s_waitcnt lgkmcnt(5)\n\t" \
    "v_mfma_f32_16x16x32_bf16 v[12:15], v[140:143], v[24:27], v[12:15]\n\t" \
    "buffer_load_dwordx4 %[l16], %[rs], %[a] offen offset:0 sc1 lds\n\t" \
    "v_mfma_f32_16x16x32_bf16 v[0:3], v[136:139], v[28:31], v[0:3]\n\t" \
    "ds_read_b128 v[20:23], %[xp] offset:24576\n\t" \
    "v_mfma_f32_16x16x32_bf16 v[4:7], v[144:147], v[28:31], v[4:7]\n\t" \
    "ds_read_b128 v[24:27], %[xp] offset:25600\n\t" \
    "v_mfma_f32_16x16x32_bf16 v[8:11], v[136:139], v[32:35], v[8:11]\n\t" \
    "ds_read_b128 v[56:59], %[pa] offset:1024\n\t" \
    "s_waitcnt lgkmcnt(6)\n\t" \
    "v_mfma_f32_16x16x32_bf16 v[12:15], v[144:147], v[32:35], v[12:15]\n\t" \
    "buffer_load_dwordx4 %[l16], %[rs], %[a] offen offset:1024 sc1 lds\n\t" \
    "v_mfma_f32_16x16x32_bf16 v[0:3], v[148:151], v[36:39], v[0:3]\n\t" \
    "ds_read_b128 v[28:31], %[xp] offset:26624\n\t" \
    "v_mfma_f32_16x16x32_bf16 v[4:7], v[156:159], v[36:39], v[4:7]\n\t" \
    "ds_read_b128 v[32:35], %[xp] offset:27648\n\t" \
    "v_mfma_f32_16x16x32_bf16 v[8:11], v[148:151], v[40:43], v[8:11]\n\t" \
    "ds_read_b128 v[60:63], %[pa] offset:2048\n\t" \
    "s_waitcnt lgkmcnt(7)\n\t" \
    "v_mfma_f32_16x16x32_bf16 v[12:15], v[156:159], v[40:43], v[12:15]\n\t" \
    "buffer_load_dwordx4 %[l16], %[rs], %[a] offen offset:2048 sc1 lds\n\t" \
    "v_mfma_f32_16x16x32_bf16 v[0:3], v[152:155], v[44:47], v[0:3]\n\t" \
    "ds_read_b128 v[36:39], %[xp] offset:28672\n\t" \
    "s_waitcnt lgkmcnt(7)\n\t" \
    "v_add_f32 v196, v196, v52\n\t" \
    "v_mfma_f32_16x16x32_bf16 v[4:7], v[160:163], v[44:47], v[4:7]\n\t" \
    "ds_read_b128 v[40:43], %[xp] offset:29696\n\t" \
    "v_add_f32 v197, v197, v53\n\t" \
    "v_mfma_f32_16x16x32_bf16 v[8:11], v[152:155], v[48:51], v[8:11]\n\t" \
    "ds_read_b128 v[64:67], %[pa] offset:3072\n\t" \
    "s_waitcnt lgkmcnt(7)\n\t" \
    "v_mfma_f32_16x16x32_bf16 v[12:15], v[160:163], v[48:51], v[12:15]\n\t" \
    "buffer_load_dwordx4 %[l16], %[rs], %[a] offen offset:3072 sc1 lds\n\t" \
    "v_add_f32 v198, v198, v54\n\t" \
    "v_add_f32 v199, v199, v55\n\t" \
    "v_mfma_f32_16x16x32_bf16 v[0:3], v[164:167], v[20:23], v[0:3]\n\t" \
    "ds_read_b128 v[44:47], %[xp] offset:30720\n\t" \
    "s_waitcnt lgkmcnt(7)\n\t" \
    "v_add_f32 v200, v200, v56\n\t" \
    "v_mfma_f32_16x16x32_bf16 v[4:7], v[172:175], v[20:23], v[4:7]\n\t" \
    "ds_read_b128 v[48:51], %[xp] offset:31744\n\t" \
    "v_add_f32 v201, v201, v57\n\t" \
    "v_mfma_f32_16x16x32_bf16 v[8:11], v[164:167], v[24:27], v[8:11]\n\t" \
    "v_add_f32 v202, v202, v58\n\t" \
    "s_waitcnt lgkmcnt(6)\n\t" \
    "v_mfma_f32_16x16x32_bf16 v[12:15], v[172:175], v[24:27], v[12:15]\n\t" \
    "v_add_f32 v203, v203, v59\n\t" \
    "s_waitcnt lgkmcnt(5)\n\t" \
    "v_add_f32 v204, v204, v60\n\t" \
    "v_add_f32 v205, v205, v61\n\t" \
    "v_mfma_f32_16x16x32_bf16 v[0:3], v[168:171], v[28:31], v[0:3]\n\t" \
    "ds_read_b128 v[20:23], %[xp] offset:32768\n\t" \
    "v_add_f32 v206, v206, v62\n\t" \
    "v_mfma_f32_16x16x32_bf16 v[4:7], v[176:179], v[28:31], v[4:7]\n\t" \
    "ds_read_b128 v[24:27], %[xp] offset:33792\n\t" \
    "v_add_f32 v207, v207, v63\n\t" \
    "v_mfma_f32_16x16x32_bf16 v[8:11], v[168:171], v[32:35], v[8:11]\n\t" \
    "s_waitcnt lgkmcnt(4)\n\t" \
    "v_add_f32 v208, v208, v64\n\t" \
    "v_mfma_f32_16x16x32_bf16 v[12:15], v[176:179], v[32:35], v[12:15]\n\t" \
    "v_add_f32 v209, v209, v65\n\t" \
    "v_add_f32 v210, v210, v66\n\t" \
    "v_add_f32 v211, v211, v67\n\t" \
    "v_mfma_f32_16x16x32_bf16 v[0:3], v[180:183], v[36:39], v[0:3]\n\t" \
    "ds_read_b128 v[28:31], %[xp] offset:34816\n\t" \
    "v_mfma_f32_16x16x32_bf16 v[4:7], v[188:191], v[36:39], v[4:7]\n\t" \
    "ds_read_b128 v[32:35], %[xp] offset:35840\n\t" \
    "s_mov_b64 exec, %[em]\n\t" \
    "buffer_store_dwordx4 v[196:199], %[l16], %[rs], %[oo] offen offset:0 sc1\n\t" \
    "s_mov_b64 exec, -1\n\t" \
    "v_mfma_f32_16x16x32_bf16 v[8:11], v[180:183], v[40:43], v[8:11]\n\t" \
    "s_waitcnt lgkmcnt(4)\n\t" \
    "v_mfma_f32_16x16x32_bf16 v[12:15], v[188:191], v[40:43], v[12:15]\n\t" \
    "s_mov_b64 exec, %[em]\n\t" \
    "buffer_store_dwordx4 v[200:203], %[l16], %[rs], %[oo] offen offset:1024 sc1\n\t" \
    "s_mov_b64 exec, -1\n\t" \
    "v_mfma_f32_16x16x32_bf16 v[0:3], v[184:187], v[44:47], v[0:3]\n\t" \
    "ds_read_b128 v[36:39], %[xp] offset:36864\n\t" \
    "v_mfma_f32_16x16x32_bf16 v[4:7], v[192:195], v[44:47], v[4:7]\n\t" \
    "ds_read_b128 v[40:43], %[xp] offset:37888\n\t" \
    "s_mov_b64 exec, %[em]\n\t" \
    "buffer_store_dwordx4 v[204:207], %[l16], %[rs], %[oo] offen offset:2048 sc1\n\t" \
    "s_mov_b64 exec, -1\n\t" \
    "v_mfma_f32_16x16x32_bf16 v[8:11], v[184:187], v[48:51], v[8:11]\n\t" \
    "s_waitcnt lgkmcnt(4)\n\t" \
    "v_mfma_f32_16x16x32_bf16 v[12:15], v[192:195], v[48:51], v[12:15]\n\t" \
    "s_mov_b64 exec, %[em]\n\t" \
    "buffer_store_dwordx4 v[208:211], %[l16], %[rs], %[oo] offen offset:3072 sc1\n\t" \
    "s_mov_b64 exec, -1\n\t" \
    "s_add_u32 %[sx], %[sx], 262144\n\t" \
    "s_add_u32 %[t], %[t], 1\n\t" \
    "s_add_u32 %[oo], %[oo], 524288\n\t" \
    "s_waitcnt vmcnt(8)\n\t" \
    "s_cmp_gt_u32 %[t], " STR(SQUICK) " + 1\n\t" \
    "s_cbranch_scc1 B0_%=\n\t" \
    "s_waitcnt vmcnt(0)\n\t" \
    "B0_%=:\n\t" \
    "s_barrier\n\t" \
    "s_setprio 1\n\t" \
    "s_add_u32 m0, %[sd], 16384\n\t" \
    "s_add_u32 %[a], %[sx], 278528\n\t" \
    "v_mfma_f32_16x16x32_bf16 v[196:199], v[68:71], v[20:23], 0\n\t" \
    "ds_read_b128 v[44:47], %[xp] offset:38912\n\t" \
    "v_mfma_f32_16x16x32_bf16 v[200:203], v[76:79], v[20:23], 0\n\t" \
    "ds_read_b128 v[48:51], %[xp] offset:39936\n\t" \
    "v_mfma_f32_16x16x32_bf16 v[204:207], v[68:71], v[24:27], 0\n\t" \
    "s_waitcnt lgkmcnt(4)\n\t" \
    "v_mfma_f32_16x16x32_bf16 v[208:211], v[76:79], v[24:27], 0\n\t" \
    "v_mfma_f32_16x16x32_bf16 v[196:199], v[72:75], v[28:31], v[196:199]\n\t" \
    "ds_read_b128 v[20:23], %[xp] offset:40960\n\t" \
    "v_mfma_f32_16x16x32_bf16 v[200:203], v[80:83], v[28:31], v[200:203]\n\t" \
    "ds_read_b128 v[24:27], %[xp] offset:41984\n\t" \
    "v_mfma_f32_16x16x32_bf16 v[204:207], v[72:75], v[32:35], v[204:207]\n\t" \
    "s_waitcnt lgkmcnt(4)\n\t" \
    "v_mfma_f32_16x16x32_bf16 v[208:211], v[80:83], v[32:35], v[208:211]\n\t" \
    "v_mfma_f32_16x16x32_bf16 v[196:199], v[84:87], v[36:39], v[196:199]\n\t" \
    "ds_read_b128 v[28:31], %[xp] offset:43008\n\t" \
    "v_mfma_f32_16x16x32_bf16 v[200:203], v[92:95], v[36:39], v[200:203]\n\t" \
    "ds_read_b128 v[32:35], %[xp] offset:44032\n\t" \
    "v_mfma_f32_16x16x32_bf16 v[204:207], v[84:87], v[40:43], v[204:207]\n\t" \
    "s_waitcnt lgkmcnt(4)\n\t" \
    "v_mfma_f32_16x16x32_bf16 v[208:211], v[92:95], v[40:43], v[208:211]\n\t" \
    "v_mfma_f32_16x16x32_bf16 v[196:199], v[88:91], v[44:47], v[196:199]\n\t" \
    "ds_read_b128 v[36:39], %[xp] offset:45056\n\t" \
    "v_mfma_f32_16x16x32_bf16 v[200:203], v[96:99], v[44:47], v[200:203]\n\t" \
    "ds_read_b128 v[40:43], %[xp] offset:46080\n\t" \
    "v_mfma_f32_16x16x32_bf16 v[204:207], v[88:91], v[48:51], v[204:207]\n\t" \
    "s_waitcnt lgkmcnt(4)\n\t" \
    "v_mfma_f32_16x16x32_bf16 v[208:211], v[96:99], v[48:51], v[208:211]\n\t" \
    "v_mfma_f32_16x16x32_bf16 v[196:199], v[100:103], v[20:23], v[196:199]\n\t" \
    "ds_read_b128 v[44:47], %[xp] offset:47104\n\t" \
    "v_mfma_f32_16x16x32_bf16 v[200:203], v[108:111], v[20:23], v[200:203]\n\t" \
    "ds_read_b128 v[48:51], %[xp] offset:48128\n\t" \
    "v_mfma_f32_16x16x32_bf16 v[204:207], v[100:103], v[24:27], v[204:207]\n\t" \
    "s_waitcnt lgkmcnt(4)\n\t" \
    "v_mfma_f32_16x16x32_bf16 v[208:211], v[108:111], v[24:27], v[208:211]\n\t" \
    "buffer_load_dwordx4 %[l16], %[rs], %[a] offen offset:0 sc1 lds\n\t" \
    "v_mfma_f32_16x16x32_bf16 v[196:199], v[104:107], v[28:31], v[196:199]\n\t" \
    "ds_read_b128 v[20:23], %[xp] offset:49152\n\t" \
    "v_mfma_f32_16x16x32_bf16 v[200:203], v[112:115], v[28:31], v[200:203]\n\t" \
    "ds_read_b128 v[24:27], %[xp] offset:50176\n\t" \
    "v_mfma_f32_16x16x32_bf16 v[204:207], v[104:107], v[32:35], v[204:207]\n\t" \
    "s_waitcnt lgkmcnt(4)\n\t" \
    "v_mfma_f32_16x16x32_bf16 v[208:211], v[112:115], v[32:35], v[208:211]\n\t" \
    "buffer_load_dwordx4 %[l16], %[rs], %[a] offen offset:1024 sc1 lds\n\t" \
    "v_mfma_f32_16x16x32_bf16 v[196:199], v[116:119], v[36:39], v[196:199]\n\t" \
    "ds_read_b128 v[28:31], %[xp] offset:51200\n\t" \
    "v_mfma_f32_16x16x32_bf16 v[200:203], v[124:127], v[36:39], v[200:203]\n\t" \
    "ds_read_b128 v[32:35], %[xp] offset:52224\n\t" \
    "v_mfma_f32_16x16x32_bf16 v[204:207], v[116:119], v[40:43], v[204:207]\n\t" \
    "s_waitcnt lgkmcnt(4)\n\t" \
    "v_mfma_f32_16x16x32_bf16 v[208:211], v[124:127], v[40:43], v[208:211]\n\t" \
    "buffer_load_dwordx4 %[l16], %[rs], %[a] offen offset:2048 sc1 lds\n\t" \
    "v_mfma_f32_16x16x32_bf16 v[196:199], v[120:123], v[44:47], v[196:199]\n\t" \
    "ds_read_b128 v[36:39], %[xp] offset:53248\n\t" \
    "v_mfma_f32_16x16x32_bf16 v[200:203], v[128:131], v[44:47], v[200:203]\n\t" \
    "ds_read_b128 v[40:43], %[xp] offset:54272\n\t" \
    "v_mfma_f32_16x16x32_bf16 v[204:207], v[120:123], v[48:51], v[204:207]\n\t" \
    "s_waitcnt lgkmcnt(4)\n\t" \
    "v_mfma_f32_16x16x32_bf16 v[208:211], v[128:131], v[48:51], v[208:211]\n\t" \
    "buffer_load_dwordx4 %[l16], %[rs], %[a] offen offset:3072 sc1 lds\n\t" \
    "s_waitcnt vmcnt(4)\n\t" \
    "s_barrier\n\t" \
    "s_setprio 1\n\t" \
    "s_add_u32 m0, %[sd], 32768\n\t" \
    "s_add_u32 %[a], %[sx], 524288\n\t" \
    "s_add_u32 %[b], %[oo], 0x1000\n\t" \
    "s_cmp_lg_u32 %[t], 0\n\t" \
    "s_cselect_b64 %[em], -1, 0\n\t" \
    "v_mfma_f32_16x16x32_bf16 v[196:199], v[132:135], v[20:23], v[196:199]\n\t" \
    "ds_read_b128 v[44:47], %[xp] offset:55296\n\t" \
    "v_mfma_f32_16x16x32_bf16 v[200:203], v[140:143], v[20:23], v[200:203]\n\t" \
    "ds_read_b128 v[48:51], %[xp] offset:56320\n\t" \
    "v_mfma_f32_16x16x32_bf16 v[204:207], v[132:135], v[24:27], v[204:207]\n\t" \
    "ds_read_b128 v[52:55], %[pa] offset:0\n\t" \
    "s_waitcnt lgkmcnt(5)\n\t" \
    "v_mfma_f32_16x16x32_bf16 v[208:211], v[140:143], v[24:27], v[208:211]\n\t" \
    "buffer_load_dwordx4 %[l16], %[rs], %[a] offen offset:0 sc1 lds\n\t" \
    "v_mfma_f32_16x16x32_bf16 v[196:199], v[136:139], v[28:31], v[196:199]\n\t" \
    "ds_read_b128 v[20:23], %[xp] offset:57344\n\t" \
    "v_mfma_f32_16x16x32_bf16 v[200:203], v[144:147], v[28:31], v[200:203]\n\t" \
    "ds_read_b128 v[24:27], %[xp] offset:58368\n\t" \
    "v_mfma_f32_16x16x32_bf16 v[204:207], v[136:139], v[32:35], v[204:207]\n\t" \
    "ds_read_b128 v[56:59], %[pa] offset:1024\n\t" \
    "s_waitcnt lgkmcnt(6)\n\t" \
    "v_mfma_f32_16x16x32_bf16 v[208:211], v[144:147], v[32:35], v[208:211]\n\t" \
    "buffer_load_dwordx4 %[l16], %[rs], %[a] offen offset:1024 sc1 lds\n\t" \
    "v_mfma_f32_16x16x32_bf16 v[196:199], v[148:151], v[36:39], v[196:199]\n\t" \
    "ds_read_b128 v[28:31], %[xp] offset:59392\n\t" \
    "v_mfma_f32_16x16x32_bf16 v[200:203], v[156:159], v[36:39], v[200:203]\n\t" \
    "ds_read_b128 v[32:35], %[xp] offset:60416\n\t" \
    "v_mfma_f32_16x16x32_bf16 v[204:207], v[148:151], v[40:43], v[204:207]\n\t" \
    "ds_read_b128 v[60:63], %[pa] offset:2048\n\t" \
    "s_waitcnt lgkmcnt(7)\n\t" \
    "v_mfma_f32_16x16x32_bf16 v[208:211], v[156:159], v[40:43], v[208:211]\n\t" \
    "buffer_load_dwordx4 %[l16], %[rs], %[a] offen offset:2048 sc1 lds\n\t" \
    "v_mfma_f32_16x16x32_bf16 v[196:199], v[152:155], v[44:47], v[196:199]\n\t" \
    "ds_read_b128 v[36:39], %[xp] offset:61440\n\t" \
    "s_waitcnt lgkmcnt(7)\n\t" \
    "v_add_f32 v0, v0, v52\n\t" \
    "v_mfma_f32_16x16x32_bf16 v[200:203], v[160:163], v[44:47], v[200:203]\n\t" \
    "ds_read_b128 v[40:43], %[xp] offset:62464\n\t" \
    "v_add_f32 v1, v1, v53\n\t" \
    "v_mfma_f32_16x16x32_bf16 v[204:207], v[152:155], v[48:51], v[204:207]\n\t" \
    "ds_read_b128 v[64:67], %[pa] offset:3072\n\t" \
    "s_waitcnt lgkmcnt(7)\n\t" \
    "v_mfma_f32_16x16x32_bf16 v[208:211], v[160:163], v[48:51], v[208:211]\n\t" \
    "buffer_load_dwordx4 %[l16], %[rs], %[a] offen offset:3072 sc1 lds\n\t" \
    "v_add_f32 v2, v2, v54\n\t" \
    "v_add_f32 v3, v3, v55\n\t" \
    "v_mfma_f32_16x16x32_bf16 v[196:199], v[164:167], v[20:23], v[196:199]\n\t" \
    "ds_read_b128 v[44:47], %[xp] offset:63488\n\t" \
    "s_waitcnt lgkmcnt(7)\n\t" \
    "v_add_f32 v4, v4, v56\n\t" \
    "v_mfma_f32_16x16x32_bf16 v[200:203], v[172:175], v[20:23], v[200:203]\n\t" \
    "ds_read_b128 v[48:51], %[xp] offset:64512\n\t" \
    "v_add_f32 v5, v5, v57\n\t" \
    "v_mfma_f32_16x16x32_bf16 v[204:207], v[164:167], v[24:27], v[204:207]\n\t" \
    "v_add_f32 v6, v6, v58\n\t" \
    "s_waitcnt lgkmcnt(6)\n\t" \
    "v_mfma_f32_16x16x32_bf16 v[208:211], v[172:175], v[24:27], v[208:211]\n\t" \
    "v_add_f32 v7, v7, v59\n\t" \
    "s_waitcnt lgkmcnt(5)\n\t" \
    "v_add_f32 v8, v8, v60\n\t" \
    "v_add_f32 v9, v9, v61\n\t" \
    "v_mfma_f32_16x16x32_bf16 v[196:199], v[168:171], v[28:31], v[196:199]\n\t" \
    "ds_read_b128 v[20:23], %[xp] offset:0\n\t" \
    "v_add_f32 v10, v10, v62\n\t" \
    "v_mfma_f32_16x16x32_bf16 v[200:203], v[176:179], v[28:31], v[200:203]\n\t" \
    "ds_read_b128 v[24:27], %[xp] offset:1024\n\t" \
    "v_add_f32 v11, v11, v63\n\t" \
    "v_mfma_f32_16x16x32_bf16 v[204:207], v[168:171], v[32:35], v[204:207]\n\t" \
    "s_waitcnt lgkmcnt(4)\n\t" \
    "v_add_f32 v12, v12, v64\n\t" \
    "v_mfma_f32_16x16x32_bf16 v[208:211], v[176:179], v[32:35], v[208:211]\n\t" \
    "v_add_f32 v13, v13, v65\n\t" \
    "v_add_f32 v14, v14, v66\n\t" \
    "v_add_f32 v15, v15, v67\n\t" \
    "v_mfma_f32_16x16x32_bf16 v[196:199], v[180:183], v[36:39], v[196:199]\n\t" \
    "ds_read_b128 v[28:31], %[xp] offset:2048\n\t" \
    "v_mfma_f32_16x16x32_bf16 v[200:203], v[188:191], v[36:39], v[200:203]\n\t" \
    "ds_read_b128 v[32:35], %[xp] offset:3072\n\t" \
    "s_mov_b64 exec, %[em]\n\t" \
    "buffer_store_dwordx4 v[0:3], %[l16], %[rs], %[oo] offen offset:0 sc1\n\t" \
    "s_mov_b64 exec, -1\n\t" \
    "v_mfma_f32_16x16x32_bf16 v[204:207], v[180:183], v[40:43], v[204:207]\n\t" \
    "s_waitcnt lgkmcnt(4)\n\t" \
    "v_mfma_f32_16x16x32_bf16 v[208:211], v[188:191], v[40:43], v[208:211]\n\t" \
    "s_mov_b64 exec, %[em]\n\t" \
    "buffer_store_dwordx4 v[4:7], %[l16], %[rs], %[oo] offen offset:1024 sc1\n\t" \
    "s_mov_b64 exec, -1\n\t" \
    "v_mfma_f32_16x16x32_bf16 v[196:199], v[184:187], v[44:47], v[196:199]\n\t" \
    "ds_read_b128 v[36:39], %[xp] offset:4096\n\t" \
    "v_mfma_f32_16x16x32_bf16 v[200:203], v[192:195], v[44:47], v[200:203]\n\t" \
    "ds_read_b128 v[40:43], %[xp] offset:5120\n\t" \
    "s_mov_b64 exec, %[em]\n\t" \
    "buffer_store_dwordx4 v[8:11], %[l16], %[rs], %[oo] offen offset:2048 sc1\n\t" \
    "s_mov_b64 exec, -1\n\t" \
    "v_mfma_f32_16x16x32_bf16 v[204:207], v[184:187], v[48:51], v[204:207]\n\t" \
    "s_waitcnt lgkmcnt(4)\n\t" \
    "v_mfma_f32_16x16x32_bf16 v[208:211], v[192:195], v[48:51], v[208:211]\n\t" \
    "s_mov_b64 exec, %[em]\n\t" \
    "buffer_store_dwordx4 v[12:15], %[l16], %[rs], %[oo] offen offset:3072 sc1\n\t" \
    "s_mov_b64 exec, -1\n\t" \
    "s_add_u32 %[sx], %[sx], 262144\n\t" \
    "s_add_u32 %[t], %[t], 1\n\t" \
    "s_add_u32 %[oo], %[oo], 524288\n\t" \
    "s_waitcnt vmcnt(8)\n\t" \
    "s_cmp_gt_u32 %[t], " STR(SQUICK) " + 1\n\t" \
    "s_cbranch_scc1 B1_%=\n\t" \
    "s_waitcnt vmcnt(0)\n\t" \
    "B1_%=:\n\t" \
    "s_barrier\n\t" \
    "s_cmp_lt_u32 %[t], %[te]\n\t" \
    "s_cbranch_scc1 L1_%=\n\t" \
    "s_cmp_lg_u32 %[fin], 0\n\t" \
    "s_cbranch_scc0 E_%=\n\t" \
    "s_nop 4\n\t" \
    "s_barrier\n\t" \
    "ds_read_b128 v[52:55], %[pa] offset:0\n\t" \
    "ds_read_b128 v[56:59], %[pa] offset:1024\n\t" \
    "ds_read_b128 v[60:63], %[pa] offset:2048\n\t" \
    "ds_read_b128 v[64:67], %[pa] offset:3072\n\t" \
    "s_add_u32 %[b], %[oo], 0x1000\n\t" \
    "s_waitcnt lgkmcnt(0)\n\t" \
    "v_add_f32 v196, v196, v52\n\t" \
    "v_add_f32 v197, v197, v53\n\t" \
    "v_add_f32 v198, v198, v54\n\t" \
    "v_add_f32 v199, v199, v55\n\t" \
    "v_add_f32 v200, v200, v56\n\t" \
    "v_add_f32 v201, v201, v57\n\t" \
    "v_add_f32 v202, v202, v58\n\t" \
    "v_add_f32 v203, v203, v59\n\t" \
    "v_add_f32 v204, v204, v60\n\t" \
    "v_add_f32 v205, v205, v61\n\t" \
    "v_add_f32 v206, v206, v62\n\t" \
    "v_add_f32 v207, v207, v63\n\t" \
    "v_add_f32 v208, v208, v64\n\t" \
    "v_add_f32 v209, v209, v65\n\t" \
    "v_add_f32 v210, v210, v66\n\t" \
    "v_add_f32 v211, v211, v67\n\t" \
    "buffer_store_dwordx4 v[196:199], %[l16], %[rs], %[oo] offen offset:0 sc1\n\t" \
    "buffer_store_dwordx4 v[200:203], %[l16], %[rs], %[oo] offen offset:1024 sc1\n\t" \
    "buffer_store_dwordx4 v[204:207], %[l16], %[rs], %[oo] offen offset:2048 sc1\n\t" \
    "buffer_store_dwordx4 v[208:211], %[l16], %[rs], %[oo] offen offset:3072 sc1\n\t" \
    "E_%=:\n\t" \
    "s_setprio 0\n\t" \
    "s_waitcnt lgkmcnt(0)\n\t" \
    "s_nop 7\n\t" \
    : [t] "+s"(t), [sx] "+s"(sx), [oo] "+s"(oo), [a] "=&s"(tmp), [b] "=&s"(tmp2), [em] "=&s"(em), "+{v[20:27]}"(xf[0]), "+{v[28:35]}"(xf[1]), "+{v[36:43]}"(xf[2]), "+{v[196:203]}"(ac[0]), "+{v[204:211]}"(ac[1]) \
    : [te] "s"(TE_), [fin] "s"(FIN_), [kh] "s"(kh), [sd] "s"(sd), [rs] "s"(rs), [l16] "v"(l16), [xp] "v"(xp), [pa] "v"(pa), [fm] "s"(fm), [pm] "s"(pm), [fo] "s"(fo), [po] "s"(po), [pd] "s"(pd), [tag] "s"(P.tag), "{v[68:75]}"(w[0][0]), "{v[76:83]}"(w[0][1]), "{v[84:91]}"(w[1][0]), "{v[92:99]}"(w[1][1]), "{v[100:107]}"(w[2][0]), "{v[108:115]}"(w[2][1]), "{v[116:123]}"(w[3][0]), "{v[124:131]}"(w[3][1]), "{v[132:139]}"(w[4][0]), "{v[140:147]}"(w[4][1]), "{v[148:155]}"(w[5][0]), "{v[156:163]}"(w[5][1]), "{v[164:171]}"(w[6][0]), "{v[172:179]}"(w[6][1]), "{v[180:187]}"(w[7][0]), "{v[188:195]}"(w[7][1]) \
    : "memory", "scc", "v0", "v1", "v2", "v3", "v4", "v5", "v6", "v7", "v8", "v9", "v10", "v11", "v12", "v13", "v14", "v15", "v44", "v45", "v46", "v47", "v48", "v49", "v50", "v51", "v52", "v53", "v54", "v55", "v56", "v57", "v58", "v59", "v60", "v61", "v62", "v63", "v64", "v65", "v66", "v67")
#define AQ_PIN0() asm volatile("" : "+{v[28:35]}"(wr[0][0]), "+{v[36:43]}"(wr[0][1]))
#define AQ_PIN1() asm volatile("" : "+{v[44:51]}"(wr[1][0]), "+{v[52:59]}"(wr[1][1]))
#define AQ_PIN2() asm volatile("" : "+{v[60:67]}"(wr[2][0]), "+{v[68:75]}"(wr[2][1]))
#define AQ_PIN3() asm volatile("" : "+{v[76:83]}"(wr[3][0]), "+{v[84:91]}"(wr[3][1]))
#define AQ_PIN4() asm volatile("" : "+{v[92:99]}"(wr[4][0]), "+{v[100:107]}"(wr[4][1]))
#define AQ_PIN5() asm volatile("" : "+{v[108:115]}"(wr[5][0]), "+{v[116:123]}"(wr[5][1]))
#define AQ_PIN6() asm volatile("" : "+{v[124:131]}"(wr[6][0]), "+{v[132:139]}"(wr[6][1]))
#define AQ_PIN7() asm volatile("" : "+{v[140:147]}"(wr[7][0]), "+{v[148:155]}"(wr[7][1]))
#define AQ_PIN8() asm volatile("" : "+{v[156:163]}"(wr[8][0]), "+{v[164:171]}"(wr[8][1]))
#define AQ_PIN9() asm volatile("" : "+{v[172:179]}"(wr[9][0]), "+{v[180:187]}"(wr[9][1]))
#define AQ_PIN10() asm volatile("" : "+{v[188:195]}"(wr[10][0]), "+{v[196:203]}"(wr[10][1]))
#define AQ_PIN11() asm volatile("" : "+{v[204:211]}"(wr[11][0]), "+{v[212:219]}"(wr[11][1]))
#define AK_PIN0() asm volatile("" : "+{v[20:27]}"(wr[0][0]), "+{v[28:35]}"(wr[0][1]))
#define AK_PIN1() asm volatile("" : "+{v[36:43]}"(wr[1][0]), "+{v[44:51]}"(wr[1][1]))
#define AK_PIN2() asm volatile("" : "+{v[52:59]}"(wr[2][0]), "+{v[60:67]}"(wr[2][1]))
#define AK_PIN3() asm volatile("" : "+{v[68:75]}"(wr[3][0]), "+{v[76:83]}"(wr[3][1]))
#define AK_PIN4() asm volatile("" : "+{v[84:91]}"(wr[4][0]), "+{v[92:99]}"(wr[4][1]))
#define AK_PIN5() asm volatile("" : "+{v[100:107]}"(wr[5][0]), "+{v[108:115]}"(wr[5][1]))
#define AK_PIN6() asm volatile("" : "+{v[116:123]}"(wr[6][0]), "+{v[124:131]}"(wr[6][1]))
#define AK_PIN7() asm volatile("" : "+{v[132:139]}"(wr[7][0]), "+{v[140:147]}"(wr[7][1]))
#define AK_PIN8() asm volatile("" : "+{v[148:155]}"(wr[8][0]), "+{v[156:163]}"(wr[8][1]))
#define AK_PIN9() asm volatile("" : "+{v[164:171]}"(wr[9][0]), "+{v[172:179]}"(wr[9][1]))
#define AK_PIN10() asm volatile("" : "+{v[180:187]}"(wr[10][0]), "+{v[188:195]}"(wr[10][1]))
#define AK_PIN11() asm volatile("" : "+{v[196:203]}"(wr[11][0]), "+{v[204:211]}"(wr[11][1]))
#define V_PIN() asm volatile("" : "+{v[28:35]}"(wv[0]), "+{v[36:43]}"(wv[1]), "+{v[44:51]}"(wv[2]), "+{v[52:59]}"(wv[3]), "+{v[60:67]}"(wv[4]), "+{v[68:75]}"(wv[5]), "+{v[76:83]}"(wv[6]), "+{v[84:91]}"(wv[7]), "+{v[92:99]}"(wv[8]), "+{v[100:107]}"(wv[9]), "+{v[108:115]}"(wv[10]), "+{v[116:123]}"(wv[11]), "+{v[124:131]}"(wv[12]), "+{v[132:139]}"(wv[13]), "+{v[140:147]}"(wv[14]), "+{v[148:155]}"(wv[15]))
#define WOV_PIN0() asm volatile("" : "+{v[156:163]}"(wo[0][0][0]), "+{v[164:171]}"(wo[0][0][1]), "+{v[172:179]}"(wo[0][1][0]), "+{v[180:187]}"(wo[0][1][1]))
#define WOV_PIN1() asm volatile("" : "+{v[188:195]}"(wo[1][0][0]), "+{v[196:203]}"(wo[1][0][1]), "+{v[204:211]}"(wo[1][1][0]), "+{v[212:219]}"(wo[1][1][1]))
#define WOW_PIN0() asm volatile("" : "+{v[28:35]}"(wo[0][0][0]), "+{v[36:43]}"(wo[0][0][1]), "+{v[44:51]}"(wo[0][1][0]), "+{v[52:59]}"(wo[0][1][1]))
#define WOW_PIN1() asm volatile("" : "+{v[60:67]}"(wo[1][0][0]), "+{v[68:75]}"(wo[1][0][1]), "+{v[76:83]}"(wo[1][1][0]), "+{v[84:91]}"(wo[1][1][1]))
#define WOW_PIN2() asm volatile("" : "+{v[92:99]}"(wo[2][0][0]), "+{v[100:107]}"(wo[2][0][1]), "+{v[108:115]}"(wo[2][1][0]), "+{v[116:123]}"(wo[2][1][1]))
#define WOW_PIN3() asm volatile("" : "+{v[124:131]}"(wo[3][0][0]), "+{v[132:139]}"(wo[3][0][1]), "+{v[140:147]}"(wo[3][1][0]), "+{v[148:155]}"(wo[3][1][1]))
#define WOW_PIN4() asm volatile("" : "+{v[156:163]}"(wo[4][0][0]), "+{v[164:171]}"(wo[4][0][1]), "+{v[172:179]}"(wo[4][1][0]), "+{v[180:187]}"(wo[4][1][1]))
#define WOW_PIN5() asm volatile("" : "+{v[188:195]}"(wo[5][0][0]), "+{v[196:203]}"(wo[5][0][1]), "+{v[204:211]}"(wo[5][1][0]), "+{v[212:219]}"(wo[5][1][1]))
#define A_PIN(QW, i) do { switch (i) { case 0: if (QW) AQ_PIN0(); else AK_PIN0(); break; case 1: if (QW) AQ_PIN1(); else AK_PIN1(); break; case 2: if (QW) AQ_PIN2(); else AK_PIN2(); break; case 3: if (QW) AQ_PIN3(); else AK_PIN3(); break; case 4: if (QW) AQ_PIN4(); else AK_PIN4(); break; case 5: if (QW) AQ_PIN5(); else AK_PIN5(); break; case 6: if (QW) AQ_PIN6(); else AK_PIN6(); break; case 7: if (QW) AQ_PIN7(); else AK_PIN7(); break; case 8: if (QW) AQ_PIN8(); else AK_PIN8(); break; case 9: if (QW) AQ_PIN9(); else AK_PIN9(); break; case 10: if (QW) AQ_PIN10(); else AK_PIN10(); break; case 11: if (QW) AQ_PIN11(); else AK_PIN11(); break; } } while (0)
#define WO_PIN(VW, i) do { switch (i) { case 0: if (VW) WOV_PIN0(); else WOW_PIN0(); break; case 1: if (VW) WOV_PIN1(); else WOW_PIN1(); break; case 2: if (!VW) WOW_PIN2(); break; case 3: if (!VW) WOW_PIN3(); break; case 4: if (!VW) WOW_PIN4(); break; case 5: if (!VW) WOW_PIN5(); break; } } while (0)

__device__ __forceinline__ u32x4 ldsv(LAS unsigned char* p) { return *(volatile LAS u32x4*)p; }
#define DMA_A dman
#define DMA_V dman
#define DMA_O dmaw
#define TOUCH 0
#define TAUX 0
#define POISON4 ((u32x4){~0u, ~0u, ~0u, ~0u})
#define DLYM 0
#define DLYA 8
#define DLYK (DLYA + 8)
#define DLYO 8
#define FLA 0
#define FLB 0
#define CADM 1
#define CADO 1

#define RTC(id)
#define DCA 3
#define DCB 2
#define DL1 28
#define DL2 28
#define DLB 8
#define ADL 0
#define SN0 150
#define HELP 256
#define BORD 0
#define QT 0
__device__ __forceinline__ void track(int& est, int d, int up, int dn) { est += max(min(d - est, up), min(-dn, (d - est) >> 1)); }
__device__ __forceinline__ unsigned rfl(unsigned v) { unsigned r; asm volatile("s_nop 0\n\tv_readfirstlane_b32 %0, %1\n\ts_nop 4" : "=s"(r) : "v"(v)); return r; }
__device__ __forceinline__ void plan_wait(rsrc_t rs, unsigned fo, int s, int& lim, unsigned& tm, unsigned& d) {
  while ((d & 255u) != (unsigned)s && (lim -= DEC1) > 0) {
    __builtin_amdgcn_s_sleep(1);
    const u32x2 v = __builtin_amdgcn_raw_buffer_load_b64(rs, 0, fo, AUX_SC1);
    tm = rfl(v.x); d = rfl(v.y);
  }
  const unsigned now = (unsigned)__builtin_amdgcn_s_memrealtime() * 4u;
  tm = now + min((int)(tm - now), 2000);
  d = min(d >> 8, 1000u);
}
#define SPIN_BUDGET (1 << 21)

__device__ __forceinline__ unsigned rec_go(LAS unsigned char* w, const Ptrs& P, u32x2 f, int wid, int lane) {
  if (wid >= 6) {
    if (__any(f.y != P.tag)) f.x = flag_wait(P, wid == 6 ? F_LN0(lane) : F_CAL(lane), 0, lane);
    if (wid == 7) { const unsigned m = near_half(f.x); if (lane == 0) *(volatile LAS unsigned*)w = m; }
  }
  LBARRIER();
  return __builtin_amdgcn_readfirstlane(*(volatile LAS unsigned*)w);
}

#define SLOT(i) ((i) * 16384)
#define LA_MEM SLOT(8)
#define LA_Q SLOT(9)
#define LA_K (SLOT(9) + 2048)
#define LA_P (SLOT(9) + 4096)
#define LA_VM (SLOT(9) + 6144)
#define LA_KX (SLOT(9) + 8192)

template <bool QW>
__device__ __forceinline__ void a_wave(LAS unsigned char* lds, Ptrs& P, int h, int wid) {
  char* ws = P.ws;
  const int lane = opq(0), tid = wid * 64 + lane;
  const int j = wid & 3, g = lane >> 4, q4 = (lane & 15) >> 2, p4 = lane & 3;
  const rsrc_t rs = WS_RSRC(P);
  const rsrc_t rq = __builtin_amdgcn_make_buffer_rsrc((void*)P.Wq, 0, HD * DM * 2, RSRC_W3);
  const rsrc_t rk = __builtin_amdgcn_make_buffer_rsrc((void*)P.Wk, 0, HD * DM * 2, RSRC_W3);
  const unsigned lbase = (unsigned)(size_t)lds;
  int lim = SPIN_BUDGET;
  u32x8 wr[12][2];
  const int trow = 16 * (g >> 1) + 4 * (g & 1) + 8 * (q4 >> 1) + (q4 & 1);
  const int tsw = ((q4 & 1) | ((g & 1) << 1) | ((q4 >> 1) << 2)) << 1;
  const int toff = trow * 256 + (((4 * j + (p4 >> 1)) ^ tsw) << 4) + 8 * (p4 & 1);
  {
    unsigned dv[2];
    #pragma unroll
    for (int x = 0; x < 2; ++x) {
      const int row = 8 * wid + 4 * x + (lane >> 4);
      const int sw = ((row & 1) | (((row >> 2) & 1) << 1) | (((row >> 3) & 1) << 2)) << 1;
      dv[x] = (unsigned)(row * (HD * 2) + h * 256 + (((lane & 15) ^ sw) << 4));
    }
#define A_GRP(n) do { _Pragma("unroll") for (int i = 0; i < 8; ++i) \
      DMA_A(((n) & 1) ? rk : rq, dv[i & 1], (4 * (((n) & 1) ? ((n) >> 1) ^ ((n) >> 2) : (((n) >> 1) + 1) & 3) + (i >> 1)) * 64 * HD * 2, lbase + SLOT(4 * ((n) & 1) + (i >> 1)) + wid * 2048 + (i & 1) * 1024); } while (0)
    A_GRP(0); A_GRP(1);
    #pragma unroll
    for (int n = 0; n < 6; ++n) {
      VMW(8);
      BARRIER();
      if ((n & 1) == (QW ? 0 : 1)) {
        #pragma unroll
        for (int b = 0; b < 4; ++b) {
          LAS unsigned char* t = lds + SLOT(QW ? 0 : 4) + ((b ^ j) << 14);
          wr[4 * (n >> 1) + b][0] = rd_frag<8192, 512>(t + toff);
          wr[4 * (n >> 1) + b][1] = rd_frag<8192, 512>(t + (toff ^ 32));
        }
        #pragma unroll
        for (int b = 0; b < 4; ++b) A_PIN(QW, 4 * (n >> 1) + b);
      }
      LBARRIER();
      A_GRP(n + 2);
    }
  }
  const unsigned cb = comm_base(ws);
  u32x2 f0 = {0u, 0u};
  #pragma unroll
  for (int rd = 0; rd < 2; ++rd) {
    if (rd == 0) VMW(8);
    else {
      if (wid >= 6) f0 = ld8(rs, FLG_OFF + (wid == 6 ? F_LN0(lane) : F_CAL(lane)) * 128);
      const unsigned fv = wid == 3 ? FLG_OFF + F_PRG(min((lane >> 1) & 3, 2), h) * 128 + (lane & 1) * 4 : WS_QF + j * (16u << 20) + h * 8192 + lane * 128;
      if (QW) asm volatile("s_mov_b32 m0, %2\n\ts_nop 0\n\tbuffer_load_dword %0, %1, 0 offen sc1 lds" :: "v"(fv), "s"(rs), "s"(lbase + LA_P + (wid == 3 ? 640 : 1024)) : "memory");
      get_tag(P);
      VMWAIT();
    }
    BARRIER();
    u32x8 f[2][2];
    #pragma unroll
    for (int i = 0; i < 2; ++i) {
      f[i][0] = rd_frag<8192, 512>(lds + SLOT((QW ? 0 : 2) + 4 * rd + i) + toff);
      f[i][1] = rd_frag<8192, 512>(lds + SLOT((QW ? 0 : 2) + 4 * rd + i) + (toff ^ 32));
    }
    asm volatile("" : "+v"(f[0][0]), "+v"(f[0][1]), "+v"(f[1][0]), "+v"(f[1][1]));
    LBARRIER();
    #pragma unroll
    for (int i = 0; i < 2; ++i)
      #pragma unroll
      for (int t = 0; t < 2; ++t) {
        LAS unsigned char* p = lds + SLOT((QW ? 0 : 2) + 4 * rd + i) + j * 4096 + t * 2048 + lane * 16;
        *(LAS u32x4*)p = (u32x4){f[i][t][0], f[i][t][1], f[i][t][2], f[i][t][3]};
        *(LAS u32x4*)(p + 1024) = (u32x4){f[i][t][4], f[i][t][5], f[i][t][6], f[i][t][7]};
      }
  }
  const unsigned mask = rec_go(lds + LA_P + 128, P, f0, wid, lane);
  const int idx = (lane & 1) ? 0xEEEE : 0x4444;
  const unsigned ab_a = lbase + LA_MEM + lane * 16, am_a = ab_a + j * 1024;
  const unsigned t0_a = lbase + SLOT((QW ? 0 : 4) + j) + j * 4096 + lane * 16;
  const unsigned iw_a = lbase + (QW ? LA_Q : LA_K) + j * 512 + q4 * 128 + g * 32 + p4 * 4;
  const unsigned qr_a = lbase + LA_Q + g * 128 + (lane & 7) * 16;
  const unsigned kr_a = lbase + LA_K + g * 128 + (2 * q4 + (lane & 1)) * 16;
  const unsigned fr_a = lbase + LA_P + p4 * 4;
  if (tid < 8) *(LAS unsigned*)(lds + LA_P + tid * 4) = 0u;
  const unsigned vo_v = VB_AT(h) + j * 512 + lane * 4, md = lbase + LA_VM - VB_AT(h);
  const unsigned oo_v = OB_AT((lane >> 2) & 1, h) + (j * 16 + (g >> 1) * 8 + p4 * 2 + (g & 1)) * 16;
  const unsigned long long onej = 1ull << j;
  const unsigned lkx = lbase + LA_KX + j * 2048;
  u32x8 oxr = {};
  const unsigned xo_v = (lane >> 5) * 2048 + (lane & 31) * 16, vl_a = lbase + LA_VM + (j & 1) * 1024 + lane * 4;
  const int dq = 16 * (j >> 1) + (lane & 15) - 4 * g;
  const unsigned fb = step_base(cb, mask, 0);
  unsigned tgt = (unsigned)__builtin_amdgcn_s_memrealtime() * 4u;
  unsigned long long tp = 0;
  unsigned pt = 0, pd = 0;
  const unsigned long long fmask = __builtin_amdgcn_readfirstlane((16u >> wid) & 1u);
  int known = 0;
  __builtin_amdgcn_s_setprio(QW ? 2 : 1);
  LBARRIER();
  report(P, wid);
  TL(72);
  for (int s = 0; s < NSEG; ++s) {
    const unsigned sb = step_base(cb, mask, s);
    const unsigned sbm = sb - (lbase + LA_MEM);
    const unsigned fo = (unsigned)(s * NH + h) * 8192u + j * 2048 - (lbase + LA_MEM);
    GSDECL;
    RTC(h);
    lim = __builtin_amdgcn_readfirstlane(lim);
    if (QW) {
      if (wid == 3 && s > 0) {
        if (opq(0) == 0) __builtin_amdgcn_raw_buffer_store_b32((unsigned)tp * 4u - tgt, rs, 0, fb + FL_O(h), AUX_SC1);
        plan_wait(rs, fb + FL_T(s), s, lim, pt, pd);
        tgt = pt;
      }
      if (s >= known) {
        const u32x2 v = *(volatile LAS u32x2*)(lds + LA_P + 640 + (opq(0) & 3) * 8);
        if (!__any(v.y != P.tag)) known = rfl(min(min(__builtin_amdgcn_readlane(v.x, 0), __builtin_amdgcn_readlane(v.x, 1)), __builtin_amdgcn_readlane(v.x, 2)));
      }
      kv_wait(P, s, h, known, opq(0));
      const unsigned kxo = WS_KF + fo, vxo = WS_VF + fo;
      if (wid < 3) for (int i = 0; i < ADL; ++i) __builtin_amdgcn_s_sleep(1);
      dmas(rs, ab_a, kxo, lkx);
      dmas(rs, ab_a, kxo + 1024, lkx + 1024);
      dmas(rs, ab_a, WS_QF + fo, lbase + LA_P + 1024);
      dmas(rs, ab_a, WS_QF + fo + 1024, lbase + LA_P + 1024);
      if (wid == 2 && s + 1 >= known) {
        const int ln = opq(0);
        const unsigned fv = FLG_OFF + F_PRG(min((ln >> 1) & 3, 2), h) * 128 + (ln & 1) * 4;
        asm volatile("s_mov_b32 m0, %2\n\ts_nop 0\n\tbuffer_load_dword %0, %1, 0 offen sc1 lds" :: "v"(fv), "s"(rs), "s"(lbase + LA_P + 640) : "memory");
      }
      unsigned rg = 0x3c000000u;
      A_MAIN_Q();
      TL(s);
      GSFLUSH(0, 24 + wid); GSFLUSH(1, 4 + wid); GSFLUSH(2, 12 + j); GSFLUSH(3, 16 + j); GSFLUSH(4, 20 + j);
      if (s + PD < NSEG && !(opq(0) & 8)) __builtin_amdgcn_raw_buffer_store_b128(poison4(), rs, oo_v, step_base(cb, mask, s + PD), AUX_SC1);
    } else {
      const unsigned qxo = WS_QF + fo - (j & 1) * 2048, oxo = WS_OX + (unsigned)(max(s - 1, 0) * NH + h) * 8192u + (j & 1) * 4096 + (j >> 1) * 1024;
      A_MAIN_K();
      if (wid == 7) GSFLUSH(0, 28);
      GSFLUSH(1, 4 + wid);
    }
  }
  if (!QW) {
    const unsigned oxo = WS_OX + (unsigned)((NSEG - 1) * NH + h) * 8192u + (j & 1) * 4096 + (j >> 1) * 1024 + xo_v;
    st16(rs, oxo, (u32x4){oxr[0], oxr[1], oxr[2], oxr[3]});
    st16(rs, oxo + 512, (u32x4){oxr[4], oxr[5], oxr[6], oxr[7]});
  }
  VMWAIT();
  BARRIER();
  if (wid == 4) flag_set(P, F_PRG(4, h), NSEG, opq(0));
}

#define LB_MEM 0
#define LB_RED 16384
template <bool VW>
__device__ __forceinline__ void b_wave(LAS unsigned char* lds, Ptrs& P, int r, int c, int wid, int bs, int xg) {
  constexpr int NHD = VW ? 2 : 6;
  char* ws = P.ws;
  const int lane = opq(0), tid = wid * 64 + lane;
  const int j = wid & 3, g = lane >> 4, q4 = (lane & 15) >> 2, p4 = lane & 3;
  const int b = r * 32 + c;
  const rsrc_t rs = WS_RSRC(P);
  const rsrc_t rv = __builtin_amdgcn_make_buffer_rsrc((void*)P.Wv, 0, HD * DM * 2, RSRC_W3);
  const rsrc_t ro = __builtin_amdgcn_make_buffer_rsrc((void*)P.Wo, 0, HD * DM * 2, RSRC_W3);
  const unsigned lbase = (unsigned)(size_t)lds;
  int lim = SPIN_BUDGET;
  const int h0 = ((VW ? 2 * j : 8 + 6 * j) + 5 * c) & 31;
  u32x8 wv[16], wo[6][2][2];
  {
    if ((wid & 3) < 2 && xg < 2) {
      const unsigned nk = 8 + 2 * xg, M = (bs * 2 + (wid & 1)) * 64 + lane, row = M / nk, k = M - row * nk;
      if (row < HELP) touch(VW ? P.Wq : P.Wk, HD * DM * 2, (row < 256 ? row + (VW ? 512 : 256) : row + 512) * (HD * 2) + min(xg * 4096 + (k >> 1) * 1024 + 256, 7936u) + (k & 1) * 128, lds);
    }
    const int nl = xg == 2 ? 6 : 5;
    #pragma unroll
    for (int i = 0; i < 2; ++i) {
      const unsigned L = bs * 1024 + wid * 128 + i * 64 + lane, row = L / nl;
      touch(P.Wo, HD * DM * 2, row * (DM * 2) + (c - (bs >> 1)) * 64 + (L - row * nl) * 128, lds);
    }
    VMWAIT();
    BARRIER();
    const int pos = lane >> 2;
    const int prow = (pos & 9) | ((pos & 2) << 1) | ((pos & 4) >> 1);
    const unsigned dv0 = (unsigned)(prow * (DM * 2) + c * 64 + ((lane & 3) << 4));
    const unsigned dv1 = (unsigned)(prow * (DM * 2) + c * 64 + (((lane & 3) ^ 2) << 4));
    const int toff = wid * 16384 + (g * 16 + q4) * 64 + (((p4 >> 1) ^ ((g & 1) << 1)) << 4) + 8 * (p4 & 1);
    #pragma unroll
    for (int rd = 0; rd < NHD / 2; ++rd) {
      #pragma unroll
      for (int hh = 0; hh < 2; ++hh) {
        const int h = (h0 + 2 * rd + hh) & 31;
        #pragma unroll
        for (int i = 0; i < 8; ++i)
          DMA_O(ro, (i & 1) ? dv1 : dv0, (unsigned)(h * 128 + i * 16) * (DM * 2), lbase + wid * 16384 + hh * 8192 + i * 1024);
      }
      VMWAIT();
      #pragma unroll
      for (int hh = 0; hh < 2; ++hh)
        #pragma unroll
        for (int ks = 0; ks < 2; ++ks)
          #pragma unroll
          for (int t = 0; t < 2; ++t)
            wo[2 * rd + hh][ks][t] = rd_frag<4096, 256>(lds + (toff ^ (t * 32)) + hh * 8192 + ks * 512);
      #pragma unroll
      for (int hh = 0; hh < 2; ++hh)
        WO_PIN(VW, 2 * rd + hh);
      asm volatile("s_waitcnt lgkmcnt(0)" ::: "memory");
    }
    const int lr = lane >> 3;
    #pragma unroll
    for (int i = 0; i < 16; ++i) {
      const int sw = (((lr >> 2) & 1) | ((i & 1) << 1)) << 1;
      const unsigned dv = (unsigned)(lr * (HD * 2) + b * 128 + (((lane & 7) ^ sw) << 4));
      DMA_V(rv, dv, (unsigned)(wid * 128 + i * 8) * (HD * 2), lbase + wid * 16384 + i * 1024);
    }
    VMWAIT();
    BARRIER();
    if (VW) {
      const int trow = 16 * (g >> 1) + 4 * (g & 1) + 8 * (q4 >> 1) + (q4 & 1);
      const int tsw = ((g & 1) | ((q4 >> 1) << 1)) << 1;
      LAS unsigned char* tv = lds + trow * 128 + (((2 * j + (p4 >> 1)) ^ tsw) << 4) + 8 * (p4 & 1);
      #pragma unroll
      for (int ks = 0; ks < 16; ++ks) wv[ks] = rd_frag<4096, 256>(tv + ks * 8192);
      V_PIN();
    }
    LBARRIER();
  }
  const unsigned cb = comm_base(ws);
  get_tag(P);
  const unsigned mask = rec_go(lds + LB_RED + 4096 + 64, P, (u32x2){0u, P.tag - 1u}, wid, lane);
  const int idx = (lane & 1) ? 0xEEEE : 0x4444;
  const unsigned am_a = lbase + LB_MEM + lane * 16;
  const unsigned fb = step_base(cb, mask, 0);
  volatile LAS unsigned* tsl = (volatile LAS unsigned*)(lds + LB_RED + 4096);
  if (tid < 8) tsl[tid] = tid ? 0u : (unsigned)__builtin_amdgcn_s_memrealtime() * 4u;
  unsigned dn = 600;
  int da = 570, gb = 360;
  if (VW) __builtin_amdgcn_s_setprio(2);
  LBARRIER();
  report(P, wid);
  TL(72);
  for (int s = 0; s < NSEG; ++s) {
    const unsigned sb = step_base(cb, mask, s);
    const int ln = opq(lane), gg = ln >> 4;
    GSDECL;
    RTC(32 + b);
    const int l16 = ln * 16;
    const int xc = 32 * (c & 1) + 2 * (ln & 15);
    const unsigned mo_v = MF_AT(c >> 1) + ((xc >> 4) * 16 + (4 * r + gg) * 2 + ((xc >> 3) & 1)) * 16 + (xc & 7) * 2;
    if (!VW) {
      const unsigned tm = __builtin_amdgcn_readfirstlane(*tsl), tgt = tm + DLB, la = lbase + LB_MEM + j * 2048 + l16, mo = sb + j * 2048;
      B_POLL();
      if (wid == 7) {
        const int late = (int)((unsigned)__builtin_amdgcn_s_memrealtime() * 4u - tgt) - SN0;
        const unsigned to = tm + dn + (late > 80 ? late : 0);
        if (ln == 0) tsl[1] = to;
        if (b == 0) {
          dn = da + DL1;
          if (ln == 0) { *tsl = to + gb + DL2; st8(rs, fb + FL_T(s + 1), (u32x2){to + gb + DL2, (dn << 8) | (s + 1)}); }
          const unsigned va = ld4(rs, fb + FL_O(ln & 31)), vb = ld4(rs, fb + FL_B(ln));
          if (s > 2) {
            track(da, rfl(wmax(va)), DCA, DCB);
            track(gb, rfl(wmax(vb)), DCA, DCB);
          }
        }
      }
      if (wid == 7 && s > 0 && s + 2 < NSEG) __builtin_amdgcn_raw_buffer_store_b32(~0u, rs, mo_v, step_base(cb, mask, s + 2), AUX_SC1);
    } else {
      GS(3);
      const unsigned vo = VB_AT(b >> 1) + ((b & 1) * 4 + j) * 256;
      V_PROJ();
      GS(0); GSFLUSH(0, 4 + j);
      if (s + PD < NSEG && ln < 16) st16(rs, step_base(cb, mask, s + PD) + vo + l16, poison4());
    }
    GSFLUSH(3, 24 + wid);
    if (s + 1 == NSEG) break;
    unsigned so[6];
    #pragma unroll
    for (int i = 0; i < NHD; ++i) { const int h = (h0 + i) & 31; so[i] = __builtin_amdgcn_readfirstlane(sb + OB_AT(r, h)); }
    const unsigned sn = __builtin_amdgcn_readfirstlane(step_base(cb, mask, s + 1));
    const unsigned rw_a = lbase + LB_RED + (wid * 128 + (gg & 1) * 64 + (gg >> 1) * 16 + (ln & 15)) * 4;
    const unsigned rr_a = lbase + LB_RED + ln * 8;
    if (wid == 7) {
      if (b != 0) {
        __builtin_amdgcn_s_sleep(4);
        unsigned tm = 0;
        dn = 0;
        plan_wait(rs, fb + FL_T(s + 1), s + 1, lim, tm, dn);
        if (ln == 0) *tsl = tm;
      }
      const unsigned tgt = rfl(tsl[1]);
      T_WAIT();
    }
    lim = __builtin_amdgcn_readfirstlane(lim);
    unsigned long long t1;
    if (VW) WO_V(); else WO_W();
    if (wid == 0) {
      if (ln == 0) st4(rs, fb + FL_B(b), (unsigned)t1 * 4u - tsl[1]);
    }
    if (wid == 0) {
      GSFLUSH(2, 16);
      TL(s);
    }
    GSFLUSH(1, 8 + wid);
    if (wid == 0 || wid == 4) { GSFLUSH(4, 17 + (wid >> 2)); GSFLUSH(0, 19 + (wid >> 2)); }
  }
  __builtin_amdgcn_s_setprio(0);
  const int ln = opq(0);
  TL(64);
  volatile LAS int* rdy = (volatile LAS int*)(lds + 98304 + 3 * 16384);
  if (tid == 0) *rdy = XS3 + 1;
  LBARRIER();
#define T_SRC(sg, i) (WS_OX + (unsigned)((sg) * NH + ((h0 + (i)) & 31)) * 8192u + r * 1024)
#define T_MMA(i, k, f) do { const u32x8 w0 = wo[i][(k) & 1][0], w1 = wo[i][(k) & 1][1]; const int e = ((k) >> 1) * 4; \
    a0 = MFMA32(((u32x4){w0[e], w0[e + 1], w0[e + 2], w0[e + 3]}), f, a0); a1 = MFMA32(((u32x4){w1[e], w1[e + 1], w1[e + 2], w1[e + 3]}), f, a1); } while (0)
#define T_DMA(sg, i) do { _Pragma("unroll") for (int k = 0; k < 4; ++k) dmas(rs, ln * 16, T_SRC(sg, i) + k * 2048, lbase + j * 24576 + (i) * 4096 + k * 1024); } while (0)
  bool ahead = false;
  f32x4 k0, k1;
  #pragma unroll 1
  for (int sg = XS3; sg < NSEG; ++sg) {
    f32x4 a0 = zero4(), a1 = zero4();
    if (VW) {
      if (wid == 0) {
        const int w = -wmax(-(int)flag_wait(P, F_PRG(4, ln & 31), min(sg + 2, NSEG), ln));
        if (ln == 0) *rdy = max(w, min(sg + 2, NSEG));
      } else while (__builtin_amdgcn_readfirstlane(*rdy) <= sg) __builtin_amdgcn_s_sleep(1);
      u32x4 of[8];
      #pragma unroll
      for (int i = 0; i < 8; ++i) of[i] = __builtin_amdgcn_raw_buffer_load_b128(rs, ln * 16 + (i & 3) * 2048, T_SRC(sg, i >> 2), AUX_SC1);
      #pragma unroll
      for (int i = 0; i < 8; ++i) T_MMA(i >> 2, i & 3, of[i]);
    } else {
      if (!ahead) {
        while (__builtin_amdgcn_readfirstlane(*rdy) <= sg) __builtin_amdgcn_s_sleep(1);
        #pragma unroll
        for (int i = 0; i < NHD; ++i) T_DMA(sg, i);
      }
      VMW(20);
      ahead = __builtin_amdgcn_readfirstlane(*rdy) > sg + 1;
      #pragma unroll
      for (int i = 0; i < NHD; ++i) {
        if (ahead) VMW(20); else G_WAIT_V(20 - 4 * i);
        u32x4 of[4];
        #pragma unroll
        for (int k = 0; k < 4; ++k) of[k] = *(const LAS u32x4*)(lds + j * 24576 + i * 4096 + k * 1024 + ln * 16);
        asm volatile("s_waitcnt lgkmcnt(0)" : "+v"(of[0]), "+v"(of[1]), "+v"(of[2]), "+v"(of[3]) :: "memory");
        if (ahead) T_DMA(sg + 1, i);
        #pragma unroll
        for (int k = 0; k < 4; ++k) T_MMA(i, k, of[k]);
      }
    }
    LAS f32x4* pw = (LAS f32x4*)(lds + 98304 + ((sg & 3) * 8 + wid) * 2048) + ln;
    if (wid == 0 && sg == NSEG - 1) { k0 = a0; k1 = a1; } else { pw[0] = a0; pw[64] = a1; }
  }
  if (VW) {
    const int fq = ln >> 4, cb2 = 16 * (fq & 1) + 8 * (fq >> 1);
    const unsigned eo = (unsigned)((NSEG - 1 - j) * 32 + r * 16 + (ln & 15)) * 2048u + (unsigned)(c * 32 + cb2) * 2u;
    cnt_wait(P, C_W4(7), 8, ln);
    const u32x4 gv = ld16(rs, WS_G + (unsigned)((NSEG - 1 - j) * 2 + r) * 32768u + c * 1024 + blk_in(ln & 15, cb2));
    const u32x4 xv = *(const u32x4*)((const char*)P.x + eo);
    LBARRIER();
    const LAS f32x4* pr = (const LAS f32x4*)(lds + 98304 + (3 - j) * 16384) + ln;
    f32x4 t[2];
    #pragma unroll
    for (int k = 0; k < 2; ++k)
      t[k] = (((wid ? pr[k * 64] : k ? k1 : k0) + pr[128 + k * 64]) + (pr[256 + k * 64] + pr[384 + k * 64])) + ((pr[512 + k * 64] + pr[640 + k * 64]) + (pr[768 + k * 64] + pr[896 + k * 64]));
    u32x4 v = epi_pack(t[0], t[1]);
    #pragma unroll
    for (int k = 0; k < 4; ++k) {
      v[k] = pack2(lo16(v[k]) * lo16(gv[k]), hi16(v[k]) * hi16(gv[k]));
      v[k] = pack2(lo16(xv[k]) + lo16(v[k]), hi16(xv[k]) + hi16(v[k]));
    }
    *(u32x4*)((char*)P.out + eo) = v;
  } else LBARRIER();
}

#define DMA_S dman
#define XDL 2
#define XPC 0
#define LS_P 131072
template <int MODE>
__device__ __forceinline__ void s_role(LAS unsigned char* lds, Ptrs& P, const u16* W, int h, unsigned obase, int nseg, int fl, int wid, int s0) {
  constexpr bool XM = MODE == 2;
  constexpr unsigned RS = XM ? DM * 2 : HD * 2, SS = XM ? 262144u : 65536u;
  const int lane = opq(0);
  const int kh = wid >> 2, tp = wid & 3;
  const rsrc_t rs = WS_RSRC(P);
  const rsrc_t rw = __builtin_amdgcn_make_buffer_rsrc((void*)W, 0, HD * DM * 2, RSRC_W3);
  const unsigned lbase = (unsigned)(size_t)lds;
  u32x8 w[8][2];
  if (XM && XDL) {
    if (wid == 0) flag_wait(P, F_PRG(4, h), max(s0 - XDL, 0), lane);
    BARRIER();
  }
  {
    const int g = lane >> 4, q4 = (lane & 15) >> 2, p4 = lane & 3;
    const int trow = XM ? 16 * g + 4 * (q4 >> 1) + (q4 & 1) : 8 * g + q4;
#define S_SW(row) ((((row) & 1) | ((((row) >> (XM ? 2 : 1)) & 1) << 1) | ((((row) >> (XM ? 4 : 3)) & 1) << 2)) << 1)
    const int toff = trow * 256 + (((4 * tp + (p4 >> 1)) ^ S_SW(trow)) << 4) + 8 * (p4 & 1);
    unsigned dv[2];
    #pragma unroll
    for (int x = 0; x < 2; ++x) {
      const int row = 8 * wid + 4 * x + (lane >> 4);
      dv[x] = (unsigned)row * RS + (XM ? (h >> 3) * 1024 * RS + (h & 7) * 256 : h * 256) + (((lane & 15) ^ S_SW(row)) << 4);
    }
#define S_IMG(i) do { DMA_S(rw, dv[0], (i) * 64 * RS, lbase + ((i) & 7) * 16384 + wid * 2048); \
                      DMA_S(rw, dv[1], (i) * 64 * RS, lbase + ((i) & 7) * 16384 + wid * 2048 + 1024); if (XM && XPC) __builtin_amdgcn_s_sleep(XPC); } while (0)
    #pragma unroll
    for (int i = 0; i < 8; ++i) S_IMG(i);
    #pragma unroll
    for (int b = 0; b < 4; ++b) {
      if (b == 3 && !XM) get_tag(P);
      if (b < 3) VMW(8); else VMWAIT();
      BARRIER();
      if (kh == (b >> 1)) {
        #pragma unroll
        for (int i = 4 * (b & 1); i < 4 * (b & 1) + 4; ++i) {
          w[i][0] = rd_frag<XM ? 2048 : 8192, XM ? 512 : 1024>(lds + toff + i * 16384);
          w[i][1] = rd_frag<XM ? 2048 : 8192, XM ? 512 : 1024>(lds + (toff ^ 32) + i * 16384);
        }
      }
      LBARRIER();
      if (b < 2) {
        #pragma unroll
        for (int i = 4 * b + 8; i < 4 * b + 12; ++i) S_IMG(i);
      }
    }
  }
  TL(XM ? 74 : 72);
  const unsigned l16 = lane * 16;
  volatile LAS int* rd = (volatile LAS int*)(lds + LS_P + 20480);
#define X_READY(want) do { \
    if (wid == 0) { \
      const int n_ = -wmax(-(int)flag_wait(P, F_PRG(4, (h & 24) + (lane & 7)), s0 + (want), lane)) - s0; \
      if (lane == 0) *rd = max(n_, want); \
    } \
    LBARRIER(); } while (0)
  if (XM) X_READY(2);
  else {
    if (wid == 0) flag_wait(P, F_LN0(lane & 7), 0, lane);
    BARRIER();
  }
  unsigned sx = (XM ? WS_OX + s0 * SS + (h >> 3) * 65536 : WS_XN) + kh * 32768 + tp * 4096;
  const unsigned sd = lbase + kh * 65536 + tp * 4096;
  const unsigned xl = kh * 65536 + (XM ? l16 : blk_in(lane & 15, (lane >> 4) * 8));
  const unsigned xp = lbase + xl, pa = lbase + LS_P + tp * 4096 + l16;
  if (XM) {
    #pragma unroll
    for (int i = 0; i < 12; ++i) dmas(rs, l16, sx + (i >> 3) * SS + ((i >> 2) & 1) * 16384 + (i & 3) * 1024, sd + (i >> 2) * 16384 + (i & 3) * 1024);
    VMWAIT();
    BARRIER();
  } else {
    #pragma unroll
    for (int i = 0; i < 16; ++i) dmas(rs, l16, sx + (i >> 3) * SS + ((i >> 2) & 1) * 16384 + (i & 3) * 1024, sd + ((i >> 2) ^ 2) * 16384 + (i & 3) * 1024);
    VMW(8);
    BARRIER();
    f32x4 a[4] = {};
    #pragma unroll
    for (int ks = 0; ks < 16; ++ks)
      #pragma unroll
      for (int i = 0; i < 4; ++i) {
        const u32x4 x = *(const LAS u32x4*)(lds + xl + 32768 + ks * 2048 + (i >> 1) * 1024);
        const u32x8 wf = w[ks >> 1][i & 1];
        const u32x4 wh = {wf[(ks & 1) * 4], wf[(ks & 1) * 4 + 1], wf[(ks & 1) * 4 + 2], wf[(ks & 1) * 4 + 3]};
        a[i] = MODE ? MFMA32(x, wh, a[i]) : MFMA32(wh, x, a[i]);
      }
    LAS f32x4* pq = (LAS f32x4*)(lds + LS_P + tp * 4096 + l16);
    if (!kh) {
      #pragma unroll
      for (int i = 0; i < 4; ++i) pq[i * 64] = a[i];
    }
    LBARRIER();
    if (kh) {
      #pragma unroll
      for (int i = 0; i < 4; ++i) a[i] += pq[i * 64];
      #pragma unroll
      for (int i = 0; i < 2; ++i) {
        const f32x4 a0 = a[MODE ? i : 2 * i], a1 = a[MODE ? 2 + i : 2 * i + 1];
        if (MODE) st16(rs, obase + h * 8192 + (2 * tp + i) * 1024 + l16, (u32x4){pack2(a0[0], a0[1]), pack2(a0[2], a0[3]), pack2(a1[0], a1[1]), pack2(a1[2], a1[3])});
        else st16(rs, obase + h * 8192 + i * 4096 + tp * 1024 + l16, (u32x4){pack2(a0[0], a1[0]), pack2(a0[1], a1[1]), pack2(a0[2], a1[2]), pack2(a0[3], a1[3])});
      }
    }
    sx += SS;
    #pragma unroll
    for (int i = 0; i < 4; ++i) dmas(rs, l16, sx + SS + i * 1024, sd + 32768 + i * 1024);
    VMW(4);
    BARRIER();
    if (wid == 1) flag_set(P, fl, 1, lane);
    TL(62);
  }
  u32x8 xf[3], ac[2] = {};
  #pragma unroll
  for (int i = 0; i < 3; ++i) {
    const u32x4 a = *(const LAS u32x4*)(lds + xl + i * 2048), b = *(const LAS u32x4*)(lds + xl + i * 2048 + 1024);
    xf[i] = (u32x8){a[0], a[1], a[2], a[3], b[0], b[1], b[2], b[3]};
  }
  unsigned long long fm, pm;
  asm volatile("s_cmp_eq_u32 %2, 1\n\ts_cselect_b64 %0, 1, 0\n\ts_cmp_eq_u32 %3, 1\n\ts_cselect_b64 %1, -1, 0" : "=&s"(fm), "=&s"(pm) : "s"(wid), "s"(XM ? 0 : wid >> 1) : "scc");
  const unsigned fo = FLG_OFF + fl * 128, pd = lbase + LS_P + 16384;
  const unsigned po = 4 * 65536 + h * 2048 + (wid & 1) * 1024 - tp * 4096 - kh * 32768;
  unsigned oo = XM ? obase + (unsigned)h * 16384u + tp * 4096 + (s0 - 1) * 0x80000u : obase + (unsigned)h * 8192u + tp * (MODE ? 2048 : 1024), tmp, tmp2;
  unsigned long long em;
  int t = XM ? 0 : 1;
  if (MODE < 2) {
    #pragma unroll 1
    for (int part = 0; part < 2; ++part) {
      if (part == 1) { TL(63); if (wid == 0) setup_wait(P, lane); }
      if (MODE) S_LOOP_V(part ? nseg + 1 : 3, 0); else S_LOOP_K(part ? nseg + 1 : 3, 0);
    }
  } else {
    #pragma unroll 1
    while (t < nseg) {
      X_READY(min(t + 4, nseg));
      const int n = __builtin_amdgcn_readfirstlane(*rd), te = n >= nseg ? nseg : (n - 2) & ~1;
      TL(t);
      S_LOOP_X(te, (nseg - 1 - te) >> 31);
    }
  }
  TL(XM ? 65 : 64);
  VMWAIT();
  BARRIER();
  if (wid == 1) flag_set(P, fl, nseg, lane);
}

#define LND 32
#define CALN 6
__device__ __forceinline__ void setup_role(LAS unsigned char* lds, Ptrs& P, int cu, int wid) {
  const int lane = opq(0), tid = wid * 64 + lane;
  const rsrc_t rs = WS_RSRC(P);
  u32x4 v[8];
  const u32x4* px = (const u32x4*)(P.x + (size_t)(cu * 32 + wid * 4 + (lane >> 4)) * DM) + (lane & 15);
  #pragma unroll
  for (int i = 0; i < 8; ++i) v[i] = __builtin_nontemporal_load(px + i * 16);
  const unsigned epo = __hip_atomic_load((gu32*)(P.ws + EPO_OFF), RLX_AGENT);
  const unsigned cb = comm_base(P.ws);
  const u32x4 z = {0u, 0u, 0u, 0u};
  const int cr = 63 - cu;
  if (wid == 0 && cr < 16) st16(rs, CNT_OFF + cr * 1024 + lane * 16, z);
  if (cr < 4) st16(rs, cb + FL_B(0) + cr * 8192 + tid * 16, z);
  if (cr == 4 && tid < 256) {
    const int g4 = tid >> 6, k = (tid >> 5) & 1 & (g4 >> 1);
    st16(rs, WS_H1 + ((g4 & 1) ? 127u * 32768u : 0u) + (g4 * 8 + ((tid >> 2) & 7)) * 1024 + blk_in((g4 & 1) ? 15 - k : k, (tid & 3) * 8), z);
  }
  #pragma unroll
  for (int i = 0; i < 2; ++i) {
    const int f = i * 54 + cr;
    if (cr < PD * NCHUNK) {
      u32x4 m = {~0u, ~0u, ~0u, ~0u};
      if (f < 4) {
        const int ks = (f >> 1) * 8 + wid;
        m = *(const u32x4*)(P.mem0 + ((lane >> 1) & 7) * DM + 64 * ks + 16 * (lane >> 4) + 8 * (lane & 1));
        m.x = canon1(m.x); m.y = canon1(m.y); m.z = canon1(m.z); m.w = canon1(m.w);
      }
      st16(rs, cb + (unsigned)(f / (NCHUNK * 2)) * CB_STEP + (f % (NCHUNK * 2)) * 8192u + tid * 16, m);
    }
  }
  const unsigned bi = blk_in((wid & 3) * 4 + (lane >> 4), (lane & 3) * 8);
  const int c0 = (lane >> 2) & 3;
  ln_rows(v);
  TL(69);
  if (cu >= 8) __builtin_amdgcn_s_sleep(LND);
  #pragma unroll
  for (int i = 0; i < 8; ++i) st16(rs, WS_XN + xn_blk(cu * 2 + (wid >> 2), 4 * i + c0) + bi, v[i]);
  P.tag = __builtin_amdgcn_readfirstlane(epo) + 1u;
  ln_rows(v);
  if (cu < 8) {
    VMWAIT();
    BARRIER();
    if (wid == 0) flag_set(P, F_LN0(cu), 1, lane);
    TL(66);
  }
  asm volatile("" ::: "memory");
  #pragma unroll
  for (int i = 0; i < 8; ++i) st16(rs, WS_XN2 + (unsigned)(cu * 2 + (wid >> 2)) * 32768u + (4 * i + c0) * 1024 + bi, v[i]);
  touch(P.w1, DM * DM * 2, ((cu & 31) * 512 + tid) * 128, lds);
  VMW(9);
  BARRIER();
  if (wid == 0) flag_set(P, F_LN0(cu), 1, lane);
  TL(70);
  if (wid == 0) VMW(2); else VMW(1);
  BARRIER();
  if (wid == 0) flag_set(P, F_LN1(cu), 1, lane);
  TL(71);
  if (cu < 32 && (wid >> 1) == 1) {
    const int id = cu * 2 + (wid & 1);
    unsigned best[2] = {65535u, 65535u};
    #pragma unroll
    for (int it = 0; it < CALN; ++it) {
      const unsigned long long c0 = __builtin_amdgcn_s_memtime();
      asm volatile("s_waitcnt lgkmcnt(0)" ::: "memory");
      if (lane == 0) st4(rs, cb + (id & 3) * CB_STEP + (30 + ((id >> 2) & 1)) * 16384u + (it & 1) * 8192u + (id >> 3) * 1024u, 0u);
      VMWAIT();
      const unsigned d = (unsigned)(__builtin_amdgcn_s_memtime() - c0);
      if (it >= 2) best[it & 1] = min(best[it & 1], d);
    }
    flag_set(P, F_CAL(id), (best[0] << 16) | best[1], lane);
  }
}

#define CPF 1
__global__ __launch_bounds__(512, 2) void mega_kernel(const u16* x, const u16* mem0, const u16* Wq, const u16* Wk, const u16* Wv, const u16* Wo,
                                                     const u16* w1, const u16* w2, const u16* w3, const u16* w4, u16* out, char* ws) {
  __shared__ __attribute__((aligned(16))) unsigned char smem[163840];
  LAS unsigned char* lds = (LAS unsigned char*)smem;
  Ptrs P;
  P.x = x; P.mem0 = mem0; P.Wq = Wq; P.Wk = Wk; P.Wv = Wv; P.Wo = Wo; P.w1 = w1; P.w2 = w2; P.w3 = w3; P.w4 = w4; P.out = out; P.ws = ws;
  const int xcd = blockIdx.x & 7, slot = blockIdx.x >> 3;
  const int wid = __builtin_amdgcn_readfirstlane(threadIdx.x >> 6);
  TL(79);
  P.tag = 0;
  if (wid == 7) {
    unsigned long long pc;
    unsigned len;
    asm volatile("s_getpc_b64 %0\nMK_BEG:\n\ts_mov_b32 %1, MK_END-MK_BEG" : "=s"(pc), "=s"(len));
    touch((const void*)pc, len, (slot * 64 + opq(0)) * 128, lds);
  }
  const int na = xcd == 2 ? 8 : 12;
  const bool isa = xcd < 3 && slot < na;
  const int ah = xcd == 2 ? slot * 4 + 2 : xcd * 16 + (slot & 3) * 4 + (slot >> 2) + (slot >> 3), bs = slot - na, br = bs & 1, bc = (xcd == 1 ? 22 : xcd * 5) + (bs >> 1);
  if (isa) {
    if (wid < 4) a_wave<true>(lds, P, ah, wid); else a_wave<false>(lds, P, ah, wid);
  } else if (xcd < 3) {
    if (wid < 4) b_wave<true>(lds, P, br, bc, wid, bs, xcd); else b_wave<false>(lds, P, br, bc, wid, bs, xcd);
  } else {
    if ((xcd & 3) == 3) {
      setup_role(lds, P, (xcd >> 2) * 32 + slot, wid);
    } else if (xcd == 6) s_role<1>(lds, P, Wv, slot, WS_VF, NSEG, F_PRG(2, slot), wid, 0);
    else s_role<0>(lds, P, xcd == 4 ? Wk : Wq, slot, xcd == 4 ? WS_KF : WS_QF, NSEG, F_PRG(xcd - 4, slot), wid, 0);
    if ((xcd & 3) == 3) mlp_role(lds, P, xcd >> 2, slot, wid);
    else s_role<2>(lds, P, Wo, slot, WS_PT, xcd == 4 ? XS1 : xcd == 5 ? XS2 - XS1 : XS3 - XS2, F_PRG(xcd + 1, slot), wid, xcd == 4 ? 0 : xcd == 5 ? XS1 : XS2);
    report(P, wid);
    fin_role(lds, P, wid);
    if (blockIdx.x == 3 && wid == 0) {
      cnt_wait(P, C_ARR, 256, threadIdx.x);
      if (threadIdx.x == 0) __hip_atomic_store((gu32*)(ws + EPO_OFF), P.tag, RLX_AGENT);
    }
  }
  asm volatile("MK_END:");
  TL(78);
}

extern "C" void kernel_launch(void* const* d_in, const int* in_sizes, int n_in, void* d_out, int out_size,
                              void* d_ws, size_t ws_size, hipStream_t stream) {
  mega_kernel<<<256, 512, 0, stream>>>((const u16*)d_in[0], (const u16*)d_in[1], (const u16*)d_in[2], (const u16*)d_in[3], (const u16*)d_in[4],
                                       (const u16*)d_in[5], (const u16*)d_in[8], (const u16*)d_in[9], (const u16*)d_in[10], (const u16*)d_in[11],
                                       (u16*)d_out, (char*)d_ws);
}
```

```cpp
#include <hip/hip_runtime.h>
#include <hip/hip_bf16.h>
#include <stdint.h>

#define SEQ 2048
#define DM 1024
#define NH 32
#define DH 128
#define HD (NH * DH)
#define MM 8
#define SEG 32
#define NSEG (SEQ / SEG)
#define FF 3584

typedef unsigned short u16;
typedef __attribute__((ext_vector_type(8))) short bf16x8;
typedef __attribute__((ext_vector_type(4))) short bf16x4;
typedef __attribute__((ext_vector_type(4))) float f32x4;
typedef __attribute__((ext_vector_type(4))) unsigned u32x4;
typedef __attribute__((ext_vector_type(2))) unsigned u32x2;
typedef __attribute__((ext_vector_type(2))) float f32x2;
typedef __attribute__((ext_vector_type(2))) __bf16 bf16v2;
typedef __attribute__((ext_vector_type(8))) unsigned u32x8;
#define LAS __attribute__((address_space(3)))
#define GAS __attribute__((address_space(1)))

__device__ __forceinline__ unsigned pack2(float lo, float hi) {
  return __builtin_bit_cast(unsigned, __builtin_convertvector((f32x2){lo, hi}, bf16v2));
}
__device__ __forceinline__ float rb1(float x) { return __uint_as_float(pack2(0.f, x)); }

typedef GAS unsigned gu32;
#define RLX_AGENT __ATOMIC_RELAXED, __HIP_MEMORY_SCOPE_AGENT
#define SPIN_CAP (1u << 20)
#define AUX_SC1 16
#define RSRC_W3 0x00020000
typedef __amdgpu_buffer_rsrc_t rsrc_t;

__device__ __forceinline__ u32x4 ld16(rsrc_t rs, unsigned off) { return __builtin_amdgcn_raw_buffer_load_b128(rs, off, 0, AUX_SC1); }
__device__ __forceinline__ u32x2 ld8(rsrc_t rs, unsigned off) { return __builtin_amdgcn_raw_buffer_load_b64(rs, off, 0, AUX_SC1); }
__device__ __forceinline__ unsigned ld4(rsrc_t rs, unsigned off) { return __builtin_amdgcn_raw_buffer_load_b32(rs, off, 0, AUX_SC1); }
__device__ __forceinline__ void st16(rsrc_t rs, unsigned off, u32x4 v) { __builtin_amdgcn_raw_buffer_store_b128(v, rs, off, 0, AUX_SC1); }
__device__ __forceinline__ void st8(rsrc_t rs, unsigned off, u32x2 v) { __builtin_amdgcn_raw_buffer_store_b64(v, rs, off, 0, AUX_SC1); }
__device__ __forceinline__ void st4(rsrc_t rs, unsigned off, unsigned v) { __builtin_amdgcn_raw_buffer_store_b32(v, rs, off, 0, AUX_SC1); }
__device__ __forceinline__ float lo16(unsigned w) { return __uint_as_float(w << 16); }
__device__ __forceinline__ float hi16(unsigned w) { return __uint_as_float(w & 0xffff0000u); }

__device__ __forceinline__ float xsum16_32(float v) {
  unsigned u = __float_as_uint(v);
  auto a = __builtin_amdgcn_permlane16_swap(u, u, false, false);
  v = __uint_as_float(a[0]) + __uint_as_float(a[1]);
  u = __float_as_uint(v);
  auto b = __builtin_amdgcn_permlane32_swap(u, u, false, false);
  return __uint_as_float(b[0]) + __uint_as_float(b[1]);
}

__device__ __forceinline__ unsigned canon1(unsigned w) { return min(w, 0xfffffffeu); }
__device__ __forceinline__ unsigned max4(u32x4 v) { return max(max(v.x, v.y), max(v.z, v.w)); }
__device__ __forceinline__ int opq(int) { int v; asm volatile("v_mbcnt_lo_u32_b32 %0, -1, 0\n\tv_mbcnt_hi_u32_b32 %0, -1, %0" : "=v"(v)); return v; }
__device__ __forceinline__ u32x4 poison4() { unsigned m = ~0u; asm volatile("" : "+v"(m)); return (u32x4){m, m, m, m}; }
__device__ __forceinline__ f32x4 zero4() { float m = 0.f; asm volatile("" : "+v"(m)); return (f32x4){m, m, m, m}; }
#define MFMA32(a, b, c) __builtin_amdgcn_mfma_f32_16x16x32_bf16(__builtin_bit_cast(bf16x8, a), __builtin_bit_cast(bf16x8, b), c, 0, 0, 0)
#define MFMA16(a, b, c) __builtin_amdgcn_mfma_f32_16x16x16bf16_1k(__builtin_bit_cast(bf16x4, a), __builtin_bit_cast(bf16x4, b), c, 0, 0, 0)
#define LBARRIER() do { asm volatile("s_waitcnt lgkmcnt(0)" ::: "memory"); __builtin_amdgcn_s_barrier(); asm volatile("" ::: "memory"); } while (0)
#define BARRIER() do { asm volatile("" ::: "memory"); __builtin_amdgcn_s_barrier(); asm volatile("" ::: "memory"); } while (0)
#define VMWAIT() asm volatile("s_waitcnt vmcnt(0)" ::: "memory")
#define VMW(n) asm volatile("s_waitcnt vmcnt(" #n ")" ::: "memory")
#define DELAY(n) do { if ((n) > 0) __builtin_amdgcn_s_sleep(n); } while (0)

#define EPO_OFF 0
#define TMO_OFF 128
#define FLG_OFF 256
#define F_LN0(i) (i)
#define F_LN1(i) (64 + (i))
#define F_CAL(i) (128 + (i))
#define F_PRG(t, h) (192 + (t) * 32 + (h))
#define CNT_OFF 65536
#define C_W1(p2) (p2)
#define C_W2(p2) (16 + (p2))
#define C_LN(pm) (32 + (pm))
#define C_W3(pm, hf) (40 + (pm) * 2 + (hf))
#define C_W4(pm) (56 + (pm))
#define C_HEAD 64
#define C_ARR 72
#define C_P4(pm, pc) (96 + (pm) * 4 + (pc))
#define NCNT 128
#define WS_XN (1u << 20)
#define WS_XN2 (WS_XN + (4u << 20))
#define WS_QF (WS_XN2 + (4u << 20))
#define WS_KF (WS_QF + (16u << 20))
#define WS_VF (WS_KF + (16u << 20))
#define WS_OX (WS_VF + (16u << 20))
#define WS_H1 (WS_OX + (16u << 20) + (1u << 20))
#define WS_X1 (WS_H1 + (4u << 20) + 65536u)
#define WS_X1N (WS_X1 + (4u << 20))
#define WS_H3 (WS_X1N + (4u << 20))
#define WS_P4 (WS_H3 + (14u << 20))
#define WS_G (WS_P4 + (8u << 20))
#define WS_PT (WS_G + (4u << 20))
#define WS_COMM (WS_PT + (32u << 20))
#define WS_END (WS_COMM + (4u << 20))
#define CB_STEP 524288u
#define PD 3
__device__ __forceinline__ unsigned comm_base(const char* ws) { return WS_COMM + ((0u - ((unsigned)(size_t)ws + WS_COMM)) & 0x1fffffu); }
__device__ __forceinline__ unsigned step_base(unsigned cb, unsigned mask, int s) { return cb + (unsigned)(s & 3) * CB_STEP + mask; }
#define MF_AT(ks) (((ks) >> 3) * 16384u + ((ks) & 7) * 1024u)
#define OB_AT(r, h) ((2u + (r) * 4u + ((h) >> 3)) * 16384u + ((h) & 7) * 1024u)
#define VB_AT(h) ((10u + ((h) >> 2)) * 16384u + ((h) & 3) * 2048u)
#define XS1 22
#define XS2 44
#define XS3 60
#define CSL 8
#define FSL 6
#define SQUICK 3
#define NCHUNK 18
#define FL_O(h) (29u * 16384u + (h) * 128u)
#define FL_B(b) (28u * 16384u + (b) * 128u)
#define FL_T(s) FL_O(32 + ((s) & 3))

struct Ptrs { const u16 *x, *mem0, *Wq, *Wk, *Wv, *Wo, *w1, *w2, *w3, *w4; u16* out; char* ws; unsigned tag; };
#define WS_RSRC(P) __builtin_amdgcn_make_buffer_rsrc((void*)(P).ws, 0, WS_END, RSRC_W3)

__device__ __forceinline__ bool spin_fail(unsigned& spins, const Ptrs& P, int lane) {
  asm volatile("" ::: "memory");
  if (++spins > SPIN_CAP) {
    unsigned tv;
    asm volatile("v_mov_b32 %0, %1" : "=v"(tv) : "s"(P.tag));
    if (lane == 0) __builtin_amdgcn_raw_buffer_store_b32(tv, WS_RSRC(P), 0, TMO_OFF, AUX_SC1);
    return true;
  }
  return (spins & 255u) == 0u && (unsigned)__builtin_amdgcn_readfirstlane(__builtin_amdgcn_raw_buffer_load_b32(WS_RSRC(P), 0, TMO_OFF, AUX_SC1)) == P.tag;
}
__device__ __forceinline__ unsigned cnt_load(char* ws, int idx) {
  return __builtin_amdgcn_readfirstlane(__hip_atomic_load((gu32*)(ws + CNT_OFF + idx * 128), RLX_AGENT));
}
__device__ __forceinline__ void cnt_add(char* ws, int idx, int lane) {
  asm volatile("s_waitcnt vmcnt(0)" ::: "memory");
  if (lane == 0) __hip_atomic_fetch_add((gu32*)(ws + CNT_OFF + idx * 128), 1u, RLX_AGENT);
}
__device__ __forceinline__ void cnt_wait(const Ptrs& P, int idx, unsigned want, int lane) {
  for (unsigned spins = 0;;) {
    if (cnt_load(P.ws, idx) >= want) break;
    __builtin_amdgcn_s_sleep(CSL);
    if (spin_fail(spins, P, lane)) break;
  }
}
template <int SL = FSL>
__device__ __forceinline__ unsigned flag_wait(const Ptrs& P, int f, unsigned want, int lane) {
  const rsrc_t rs = WS_RSRC(P);
  u32x2 v;
  for (unsigned spins = 0;;) {
    v = ld8(rs, FLG_OFF + f * 128);
    if (!__any(v.y != P.tag || v.x < want)) break;
    __builtin_amdgcn_s_sleep(SL);
    if (spin_fail(spins, P, lane)) break;
  }
  asm volatile("" ::: "memory");
  return v.x;
}
__device__ __forceinline__ void flag_set(const Ptrs& P, int f, unsigned val, int lane) {
  if (lane == 0) st8(WS_RSRC(P), FLG_OFF + f * 128, (u32x2){val, P.tag});
}
__device__ __forceinline__ void get_tag(Ptrs& P) { P.tag = __builtin_amdgcn_readfirstlane(__hip_atomic_load((gu32*)(P.ws + EPO_OFF), RLX_AGENT)) + 1u; }
__device__ __forceinline__ void setup_wait(const Ptrs& P, int lane) { flag_wait(P, F_LN0(lane), 0, lane); }
__device__ __forceinline__ void clock_wait(unsigned tgt) {
  while ((int)(tgt - (unsigned)__builtin_amdgcn_s_memrealtime()) > 0) {}
}
__device__ __forceinline__ void report(const Ptrs& P, int wid) {
  if (wid == 0 && opq(0) == 0) __hip_atomic_fetch_add((gu32*)(P.ws + CNT_OFF + C_ARR * 128), 1u, RLX_AGENT);
}
__device__ __forceinline__ void kv_wait(const Ptrs& P, int s, int h, int& known, int lane) {
  if (s < known) return;
  const unsigned v = flag_wait(P, F_PRG(min(lane, 2), h), s + 1, lane);
  known = __builtin_amdgcn_readfirstlane(min(min(__builtin_amdgcn_readlane(v, 0), __builtin_amdgcn_readlane(v, 1)), __builtin_amdgcn_readlane(v, 2)));
}
__device__ __forceinline__ int wmax(int v) {
  v = max(v, __builtin_amdgcn_update_dpp(0, v, 0x128, 0xf, 0xf, false));
  v = max(v, __builtin_amdgcn_update_dpp(0, v, 0x124, 0xf, 0xf, false));
  v = max(v, __builtin_amdgcn_update_dpp(0, v, 0x122, 0xf, 0xf, false));
  v = max(v, __builtin_amdgcn_update_dpp(0, v, 0x121, 0xf, 0xf, false));
  return max(max(__builtin_amdgcn_readlane(v, 0), __builtin_amdgcn_readlane(v, 16)), max(__builtin_amdgcn_readlane(v, 32), __builtin_amdgcn_readlane(v, 48)));
}
__device__ __forceinline__ unsigned near_half(unsigned v) { return wmax(-(int)(v & 65535u)) > wmax(-(int)(v >> 16)) ? 8192u : 0u; }
__device__ __forceinline__ void dmaw(rsrc_t rs, unsigned voff, unsigned soff, unsigned ldsaddr) {
  asm volatile("s_mov_b32 m0, %3\n\ts_nop 0\n\tbuffer_load_dwordx4 %0, %1, %2 offen lds" :: "v"(voff), "s"(rs), "s"(soff), "s"(ldsaddr) : "memory");
}
__device__ __forceinline__ void dman(rsrc_t rs, unsigned voff, unsigned soff, unsigned ldsaddr) {
  asm volatile("s_mov_b32 m0, %3\n\ts_nop 0\n\tbuffer_load_dwordx4 %0, %1, %2 offen nt lds" :: "v"(voff), "s"(rs), "s"(soff), "s"(ldsaddr) : "memory");
}
__device__ __forceinline__ void dmas(rsrc_t rs, unsigned voff, unsigned soff, unsigned ldsaddr) {
  asm volatile("s_mov_b32 m0, %3\n\ts_nop 0\n\tbuffer_load_dwordx4 %0, %1, %2 offen sc1 lds" :: "v"(voff), "s"(rs), "s"(soff), "s"(ldsaddr) : "memory");
}
__device__ __forceinline__ void touch(const void* w, unsigned bytes, unsigned voff, LAS unsigned char* lds) {
  asm volatile("s_mov_b32 m0, %2\n\ts_nop 0\n\tbuffer_load_dword %0, %1, 0 offen lds"
               :: "v"(voff), "s"(__builtin_amdgcn_make_buffer_rsrc((void*)w, 0, bytes, RSRC_W3)), "s"((unsigned)(size_t)lds + 148480) : "memory");
}
__device__ __forceinline__ u32x2 trd(LAS unsigned char* p) {
  return __builtin_bit_cast(u32x2, __builtin_amdgcn_ds_read_tr16_b64_v4i16((LAS bf16x4*)p));
}
template <int BO, int UO>
__device__ __forceinline__ u32x8 rd_frag(LAS unsigned char* p) {
  const u32x2 a = trd(p), b = trd(p + UO), c = trd(p + BO), d = trd(p + BO + UO);
  return (u32x8){a.x, a.y, b.x, b.y, c.x, c.y, d.x, d.y};
}

#define TL(i) do {} while (0)
#define TLW(i) do {} while (0)
#define GSDECL
#define GS(i) do {} while (0)
#define GSFLUSH(k, i) do {} while (0)

__device__ __forceinline__ int lds_byte(int r, int c) {
  const int st = (r >> 4) * 2 + (c >> 5), rr = r & 15, cc = c & 31, ob = rr * 64 + cc * 2;
  return st * 1024 + (ob ^ (((ob >> 9) & 1) << 5));
}
#define PFNT ""
#define G_DvoffB dmaw
#define HSP 3
#define PFD 3
#define PFH 4
struct Pf { unsigned v, step; bool on, a; };
#define G_PF(t) do { if (pf.on && (t) < nt) asm volatile("s_mov_b32 m0, %3\n\ts_nop 0\n\tbuffer_load_dword %0, %1, %2 offen sc1 " PFNT "lds" \
    :: "v"(pf.v), "s"(pf.a ? ra : rb), "s"((t) * pf.step), "s"((unsigned)(size_t)lds + 148480) : "memory"); } while (0)

__device__ __forceinline__ void gemm_loop(LAS unsigned char* lds, rsrc_t ra, rsrc_t rb, unsigned cA, unsigned cB, int lda, int ldb, int nt,
                                          const Pf& pf, f32x4 (&acc)[2][2][4][2]) {
  const int wid = __builtin_amdgcn_readfirstlane(threadIdx.x >> 6), lane = opq(0), tid = wid * 64 + lane;
  const int wr = wid >> 2, wc = wid & 3, fr = lane & 15, fq = lane >> 4;
  unsigned voffA[2], voffB[2];
  #pragma unroll
  for (int i = 0; i < 2; ++i) {
    voffA[i] = (unsigned)(((wid >> 1) + 4 * i) * 16 * lda + (wid & 1) * 1024 + lane * 16);
    const int b = tid * 16 + i * 8192, r = b >> 8, pos = (b >> 4) & 15;
    const int c = pos ^ (((r & 3) << 1) | (((r >> 3) & 1) << 3));
    voffB[i] = (unsigned)(r * ldb + c * 16);
  }
  const unsigned hA = 128 * lda;
  const unsigned kB = 64 * ldb;
  const unsigned ldsw = (unsigned)(size_t)lds + (unsigned)wid * 1024u;
  const int aoff = 65536 + lds_byte(wr * 64 + fr, fq * 8);
  const int q = (lane & 15) >> 2, p = lane & 3;
  const unsigned boff0 = (unsigned)(size_t)lds + (8 * fq + q) * 256 + (((wc * 4 + (p >> 1)) ^ ((q << 1) | ((fq & 1) << 3))) << 4) + 8 * (p & 1);
  const unsigned boff1 = boff0 ^ 32;
#define G_SB(b, h) (((b) * 2 + (h)) * 16384)
#define G_SA(b, h) (65536 + ((b) * 2 + (h)) * 16384)
#define G_ST1(bufoff, r, so, voff, i) G_D##voff(r, (voff)[i], so, ldsw + (bufoff) + (i) * 8192)
#define G_DvoffA dmas
#define G_STAGE(bufoff, so, voff) do { G_ST1(bufoff, G_R##voff, so, voff, 0); G_ST1(bufoff, G_R##voff, so, voff, 1); } while (0)
#define G_RvoffA ra
#define G_RvoffB rb
#define G_LDA(dst, b, h) do { _Pragma("unroll") for (int m = 0; m < 4; ++m) _Pragma("unroll") for (int k = 0; k < 2; ++k) \
    dst[m][k] = *(const LAS bf16x8*)(lds + (G_SA(b, h) - 65536) + aoff + m * 2048 + k * 1024); } while (0)
#define G_TR(dst, base, off) asm volatile("ds_read_b64_tr_b16 %0, %1 offset:%2" : "=v"(dst) : "v"(base), "i"(off))
#define G_LDB(dst, b, h) do { _Pragma("unroll") for (int n = 0; n < 2; ++n) _Pragma("unroll") for (int k = 0; k < 2; ++k) { \
    bf16x4 _v0, _v1; \
    G_TR(_v0, (n ? boff1 : boff0), G_SB(b, h) + k * 8192); \
    G_TR(_v1, (n ? boff1 : boff0), G_SB(b, h) + k * 8192 + 1024); \
    dst[n][k] = __builtin_shufflevector(_v0, _v1, 0, 1, 2, 3, 4, 5, 6, 7); } } while (0)
#define G_WAIT_B(Bt) asm volatile("s_waitcnt lgkmcnt(0)" : "+v"(Bt[0][0]), "+v"(Bt[0][1]), "+v"(Bt[1][0]), "+v"(Bt[1][1]) :: "memory")
#define G_MMA(ai, bj, At, Bt) do { __builtin_amdgcn_s_setprio(1); _Pragma("unroll") for (int m = 0; m < 4; ++m) _Pragma("unroll") for (int n = 0; n < 2; ++n) _Pragma("unroll") for (int k = 0; k < 2; ++k) \
    acc[ai][bj][m][n] = __builtin_amdgcn_mfma_f32_16x16x32_bf16(Bt[n][k], At[m][k], acc[ai][bj][m][n], 0, 0, 0); \
    __builtin_amdgcn_s_setprio(0); } while (0)
#define G_WAIT_V(n) asm volatile("s_waitcnt vmcnt(%0)" :: "n"(n) : "memory")
#define G_WAIT_L(n) asm volatile("s_waitcnt lgkmcnt(" #n ")" ::: "memory")
#define G_BAR __builtin_amdgcn_s_barrier()
#define G_SCHED __builtin_amdgcn_sched_barrier(0)
#define G_APTR(t) (cA + (t) * 2048)
  #pragma unroll
  for (int a = 0; a < 2; ++a)
    #pragma unroll
    for (int b = 0; b < 2; ++b)
      #pragma unroll
      for (int m = 0; m < 4; ++m)
        #pragma unroll
        for (int n = 0; n < 2; ++n) acc[a][b][m][n] = (f32x4){0.f, 0.f, 0.f, 0.f};
  bf16x8 At[4][2], B0[2][2], B1[2][2];
  {
    const unsigned a0 = G_APTR(0);
    #pragma unroll
    for (int t = 2; t < PFD; ++t) G_PF(t);
    G_STAGE(G_SB(0, 0), cB, voffB); G_STAGE(G_SA(0, 0), a0, voffA); G_STAGE(G_SB(0, 1), cB + 256, voffB); G_STAGE(G_SA(0, 1), a0 + hA, voffA);
    if (wr == 1) G_BAR;
    G_WAIT_V(4); G_BAR;
    G_STAGE(G_SB(1, 0), cB + kB, voffB); G_STAGE(G_SA(1, 0), a0 + 2048, voffA); G_STAGE(G_SB(1, 1), cB + kB + 256, voffB);
    G_WAIT_V(6); G_BAR;
  }
  for (int t = 0; t < nt; t += 2) {
    const int t2 = (t == nt - 2) ? 0 : t + 2;
    const unsigned a1 = G_APTR(t + 1);
    const unsigned a2 = G_APTR(t2);
    const unsigned b2 = cB + t2 * kB;
    const unsigned a3 = a2 + 2048;
    const unsigned b3 = b2 + kB;
    G_LDB(B0, 0, 0); G_SCHED; G_LDA(At, 0, 0); G_STAGE(G_SA(1, 1), a1 + hA, voffA);
    G_WAIT_L(8); G_BAR; G_WAIT_B(B0); G_MMA(0, 0, At, B0); G_BAR; G_SCHED;
    G_LDB(B1, 0, 1); G_STAGE(G_SB(0, 0), b2, voffB);
    G_BAR; G_WAIT_B(B1); G_MMA(0, 1, At, B1); G_BAR;
    G_LDA(At, 0, 1); G_STAGE(G_SA(0, 0), a2, voffA);
    G_BAR; G_WAIT_L(0); G_MMA(1, 0, At, B0); G_BAR; G_SCHED;
    G_STAGE(G_SB(0, 1), b2 + 256, voffB);
    G_WAIT_V(6); G_PF(t + PFD); G_BAR; G_MMA(1, 1, At, B1); G_BAR;
    G_LDB(B0, 1, 0); G_SCHED; G_LDA(At, 1, 0); G_STAGE(G_SA(0, 1), a2 + hA, voffA);
    G_WAIT_L(8); G_BAR; G_WAIT_B(B0); G_MMA(0, 0, At, B0); G_BAR; G_SCHED;
    G_LDB(B1, 1, 1); G_STAGE(G_SB(1, 0), b3, voffB);
    G_BAR; G_WAIT_B(B1); G_MMA(0, 1, At, B1); G_BAR;
    G_LDA(At, 1, 1); G_STAGE(G_SA(1, 0), a3, voffA);
    G_BAR; G_WAIT_L(0); G_MMA(1, 0, At, B0); G_BAR; G_SCHED;
    G_STAGE(G_SB(1, 1), b3 + 256, voffB);
    G_WAIT_V(6); G_PF(t + PFD + 1); G_BAR; G_MMA(1, 1, At, B1); G_BAR;
  }
  if (wr == 0) G_BAR;
}

__device__ __forceinline__ void gemm_h(LAS unsigned char* lds, rsrc_t ra, rsrc_t rb, unsigned cA, unsigned cB, int lda, int ldb, int nt,
                                       const Pf& pf, f32x4 (&acc)[1][2][4][2]) {
  const int wid = __builtin_amdgcn_readfirstlane(threadIdx.x >> 6), lane = opq(0), tid = wid * 64 + lane;
  const int wr = wid >> 2, wc = wid & 3, fr = lane & 15, fq = lane >> 4;
  unsigned voffA[2], voffB[2];
  #pragma unroll
  for (int i = 0; i < 2; ++i) {
    voffA[i] = (unsigned)(((wid >> 1) + 4 * i) * 16 * lda + (wid & 1) * 1024 + lane * 16);
    const int b = tid * 16 + i * 8192, r = b >> 8, pos = (b >> 4) & 15;
    const int c = pos ^ (((r & 3) << 1) | (((r >> 3) & 1) << 3));
    voffB[i] = (unsigned)(r * ldb + c * 16);
  }
  const unsigned kB = 64 * ldb;
  const unsigned ldsw = (unsigned)(size_t)lds + (unsigned)wid * 1024u;
  const int q = (lane & 15) >> 2, p = lane & 3;
  const int aoffs = 32768 + lds_byte(wr * 64 + fr, fq * 8);
  const unsigned boffs = (unsigned)(size_t)lds + (8 * fq + q) * 256 + (((wc * 4 + (p >> 1)) ^ ((q << 1) | ((fq & 1) << 3))) << 4) + 8 * (p & 1);
  #pragma unroll
  for (int i = 0; i < 16; ++i) acc[0][i >> 3][(i >> 1) & 3][i & 1] = (f32x4){0.f, 0.f, 0.f, 0.f};
  bf16x8 At[4][2], B0[2][2], B1[2][2];
#define H_STAGE(so, t) do { G_STAGE(so, cB + (t) * kB, voffB); G_STAGE((so) + 32768, G_APTR(t), voffA); G_STAGE((so) + 16384, cB + (t) * kB + 256, voffB); } while (0)
  #pragma unroll
  for (int t = 2; t < PFH; ++t) G_PF(t);
  H_STAGE(0, 0); H_STAGE(49152, 1);
  G_WAIT_V(6); G_BAR;
  if (wr == 1) G_BAR;
  int cur = 0, nxt = 98304;
#define H_M4(bj, m, Bt) do { _Pragma("unroll") for (int n = 0; n < 2; ++n) _Pragma("unroll") for (int k = 0; k < 2; ++k) \
    acc[0][bj][m][n] = __builtin_amdgcn_mfma_f32_16x16x32_bf16(Bt[n][k], At[m][k], acc[0][bj][m][n], 0, 0, 0); G_SCHED; } while (0)
  for (int t = 0; t < nt; ++t) {
    const int aoff = aoffs + cur;
    const unsigned boff0 = boffs + cur, boff1 = boff0 ^ 32;
    const int t2 = t + 2 < nt ? t + 2 : 0;
    const unsigned a2 = G_APTR(t2), b2 = cB + t2 * kB;
    G_LDB(B0, 0, 0); G_ST1(nxt, rb, b2, voffB, 0); G_LDA(At, 0, 0); G_SCHED; G_ST1(nxt, rb, b2, voffB, 1); G_LDB(B1, 0, 1); G_SCHED;
    if (HSP > 2) G_ST1(nxt + 32768, ra, a2, voffA, 0);
    G_WAIT_V(HSP); G_WAIT_B(B0); G_WAIT_B(B1); G_BAR; G_SCHED;
    __builtin_amdgcn_s_setprio(1);
    H_M4(0, 0, B0); if (HSP < 3) G_ST1(nxt + 32768, ra, a2, voffA, 0);
    H_M4(0, 1, B0); G_ST1(nxt + 32768, ra, a2, voffA, 1);
    H_M4(0, 2, B0); G_ST1(nxt + 16384, rb, b2 + 256, voffB, 0);
    H_M4(0, 3, B0); G_ST1(nxt + 16384, rb, b2 + 256, voffB, 1);
    H_M4(1, 0, B1); G_PF(t + PFH);
    H_M4(1, 1, B1); H_M4(1, 2, B1); H_M4(1, 3, B1);
    __builtin_amdgcn_s_setprio(0);
    G_BAR; G_SCHED;
    nxt = cur;
    cur = cur == 98304 ? 0 : cur + 49152;
  }
  if (wr == 0) G_BAR;
}

__device__ __forceinline__ u32x4 epi_pack(const f32x4 a0, const f32x4 a1) {
  const unsigned x0 = pack2(a0[0], a0[1]), x1 = pack2(a0[2], a0[3]);
  const unsigned y0 = pack2(a1[0], a1[1]), y1 = pack2(a1[2], a1[3]);
  auto s0 = __builtin_amdgcn_permlane16_swap(x0, y0, false, false);
  auto s1 = __builtin_amdgcn_permlane16_swap(x1, y1, false, false);
  return (u32x4){s0[0], s1[0], s0[1], s1[1]};
}
__device__ __forceinline__ unsigned gelu2(unsigned w) {
  const f32x2 x = {lo16(w), hi16(w)};
  const float c0 = -2.f * 1.4426950408889634f * 0.7978845608028654f, c1 = c0 * 0.044715f;
  const f32x2 a = x * (x * x * c1 + c0);
  const f32x2 d = (f32x2){__builtin_amdgcn_exp2f(a[0]), __builtin_amdgcn_exp2f(a[1])} + 1.f;
  const f32x2 y = x * (f32x2){__builtin_amdgcn_rcpf(d[0]), __builtin_amdgcn_rcpf(d[1])};
  return pack2(y[0], y[1]);
}
__device__ __forceinline__ float silu_f(float b) { return b * __builtin_amdgcn_rcpf(1.f + __expf(-b)); }

__device__ __forceinline__ unsigned blk_in(int r, int c) { const int ob = r * 64 + c * 2; return ob ^ (((ob >> 9) & 1) << 5); }
__device__ __forceinline__ unsigned xn_blk(int rb, int cb) { return (unsigned)(((rb >> 1) * 32 + cb) * 2 + (rb & 1)) * 1024u; }

template <int EPI, int NA>
__device__ __forceinline__ void epi_rows(const f32x4 (&acc)[NA][2][4][2], rsrc_t rs, unsigned obase, int ldc, int pm2, int pn2, int sh, int wr, int wc, int ln) {
  const int fq = ln >> 4, cb = 16 * (fq & 1) + 8 * (fq >> 1), fr = (ln & 15) + sh;
  const unsigned lo = (unsigned)((fr >> 4) * 16 * ldc) + blk_in(fr & 15, cb);
  u32x4 r[8];
  if (EPI == 2) {
    #pragma unroll
    for (int i = 0; i < 8; ++i) r[i] = ld16(rs, WS_XN + xn_blk(pm2 * 8 + 4 * wr + (i >> 1), pn2 * 4 + 4 * (i & 1) + wc) + lo);
  }
  #pragma unroll
  for (int ai = 0; ai < NA; ++ai)
    #pragma unroll
    for (int m = 0; m < 4; ++m)
      #pragma unroll
      for (int bj = 0; bj < 2; ++bj) {
        u32x4 w = epi_pack(acc[ai][bj][m][0], acc[ai][bj][m][1]);
        #pragma unroll
        for (int i = 0; i < 4; ++i) w[i] = EPI < 2 ? gelu2(w[i]) : pack2(lo16(r[2 * m + bj][i]) + lo16(w[i]), hi16(r[2 * m + bj][i]) + hi16(w[i]));
        st16(rs, obase + (unsigned)(pm2 * 128 + 128 * ai + 64 * wr + 16 * m) * (unsigned)ldc + (unsigned)(pn2 * 4 + 4 * bj + wc) * 1024u + lo, w);
      }
}

__device__ __forceinline__ float row_sum16(float v) {
  v += __uint_as_float(__builtin_amdgcn_update_dpp(0, (int)__float_as_uint(v), 0x128, 0xf, 0xf, false));
  v += __uint_as_float(__builtin_amdgcn_update_dpp(0, (int)__float_as_uint(v), 0x124, 0xf, 0xf, false));
  v += __uint_as_float(__builtin_amdgcn_update_dpp(0, (int)__float_as_uint(v), 0x122, 0xf, 0xf, false));
  v += __uint_as_float(__builtin_amdgcn_update_dpp(0, (int)__float_as_uint(v), 0x121, 0xf, 0xf, false));
  return v;
}
__device__ __forceinline__ void ln_rows(u32x4 (&a)[8]) {
  f32x2 v[32];
  #pragma unroll
  for (int i = 0; i < 32; ++i) v[i] = (f32x2){lo16(a[i >> 2][i & 3]), hi16(a[i >> 2][i & 3])};
  f32x2 s[4] = {v[0], v[1], v[2], v[3]};
  #pragma unroll
  for (int i = 4; i < 32; ++i) s[i & 3] += v[i];
  s[0] = (s[0] + s[1]) + (s[2] + s[3]);
  const float mean = row_sum16(s[0][0] + s[0][1]) * (1.f / DM);
  #pragma unroll
  for (int i = 0; i < 32; ++i) { const f32x2 d = v[i] - mean; s[i & 3] = i < 4 ? d * d : s[i & 3] + d * d; }
  s[0] = (s[0] + s[1]) + (s[2] + s[3]);
  const float var = row_sum16(s[0][0] + s[0][1]) * (1.f / DM);
  const float mb = rb1(mean), vb = rb1(var);
  const float rden = __builtin_amdgcn_rcpf(rb1(__builtin_amdgcn_sqrtf(rb1(vb + 1e-5f))));
  #pragma unroll
  for (int i = 0; i < 32; ++i) {
    const f32x2 d = v[i] - mb;
    const unsigned w = pack2(d[0], d[1]);
    const f32x2 y = (f32x2){lo16(w), hi16(w)} * rden;
    a[i >> 2][i & 3] = pack2(y[0], y[1]);
  }
}

template <int N>
__device__ __forceinline__ void fin_out(rsrc_t rs, const Ptrs& P, unsigned po, unsigned go, unsigned eo) {
  u32x4 pv[N][8], gv[N], xv[N];
  #pragma unroll
  for (int c = 0; c < N; ++c) {
    #pragma unroll
    for (int i = 0; i < 8; ++i) pv[c][i] = ld16(rs, po + c * 16384 + (i >> 1) * 131072 + (i & 1) * 1024);
    gv[c] = ld16(rs, go + c * 4096);
    xv[c] = __builtin_nontemporal_load((const u32x4*)((const char*)P.x + eo + c * 256));
  }
  #pragma unroll
  for (int c = 0; c < N; ++c) {
#define PF4(i) __builtin_bit_cast(f32x4, pv[c][i])
    u32x4 v = epi_pack((PF4(0) + PF4(2)) + (PF4(4) + PF4(6)), (PF4(1) + PF4(3)) + (PF4(5) + PF4(7)));
    #pragma unroll
    for (int k = 0; k < 4; ++k) {
      v[k] = pack2(lo16(v[k]) * lo16(gv[c][k]), hi16(v[k]) * hi16(gv[c][k]));
      v[k] = pack2(lo16(xv[c][k]) + lo16(v[k]), hi16(xv[c][k]) + hi16(v[k]));
    }
    *(u32x4*)((char*)P.out + eo + c * 256) = v;
  }
}

template <int KH>
__device__ __forceinline__ void epi_w4(const f32x4 (&acc)[2][2][4][2], rsrc_t rs, const Ptrs& P, int pm, int pc, int wid, int ln) {
  char* ws = P.ws;
  const int wr = wid >> 2, wc = wid & 3, fr = ln & 15, fq = ln >> 4;
  const unsigned pb = WS_P4 + (unsigned)(pm * 4 + pc) * 262144u + wid * 16384 + ln * 16;
  #pragma unroll
  for (int i = 0; i < 16; ++i) st16(rs, pb + KH * 131072 + i * 1024, __builtin_bit_cast(u32x4, acc[KH ^ 1][(i >> 1) & 1][i >> 2][i & 1]));
  const unsigned eb = (unsigned)(pm * 256 + 128 * KH + 64 * wr) * 2048u + (unsigned)(pc * 8 + wc) * 1024u + blk_in(fr, 16 * (fq & 1) + 8 * (fq >> 1));
  u32x4 pp[16], r1[8];
  #pragma unroll
  for (int i = 0; i < 8; ++i) r1[i] = ld16(rs, WS_X1 + eb + (i >> 1) * 32768 + (i & 1) * 4096);
  VMWAIT();
  BARRIER();
  if (wid == 0) {
    if (ln == 0) __hip_atomic_fetch_add((gu32*)(ws + CNT_OFF + C_P4(pm, pc) * 128), 1u, RLX_AGENT);
    cnt_wait(P, C_P4(pm, pc), 2, ln);
  }
  BARRIER();
  #pragma unroll
  for (int i = 0; i < 16; ++i) pp[i] = ld16(rs, pb + (KH ^ 1) * 131072 + i * 1024);
  asm volatile("" ::: "memory");
  #pragma unroll
  for (int i = 0; i < 8; ++i) {
    u32x4 v = epi_pack(acc[KH][i & 1][i >> 1][0] + __builtin_bit_cast(f32x4, pp[2 * i]), acc[KH][i & 1][i >> 1][1] + __builtin_bit_cast(f32x4, pp[2 * i + 1]));
    #pragma unroll
    for (int k = 0; k < 4; ++k) {
      v[k] = pack2(lo16(r1[i][k]) + lo16(v[k]), hi16(r1[i][k]) + hi16(v[k]));
      v[k] = pack2(silu_f(lo16(v[k])), silu_f(hi16(v[k])));
    }
    st16(rs, WS_G + eb + (i >> 1) * 32768 + (i & 1) * 4096, v);
  }
}

#define W4S 1
__device__ __forceinline__ void mlp_role(LAS unsigned char* lds, const Ptrs& P, int hf, int slot, int wid) {
  const int wr = wid >> 2, wc = wid & 3;
  char* ws = P.ws;
  const rsrc_t rs = WS_RSRC(P);
#define M_DONE(c) do { VMWAIT(); BARRIER(); if (wid == 0) cnt_add(ws, c, opq(0)); } while (0)
  #pragma unroll 1
  for (int code = 0; code < 2; ++code) {
    const int p2 = hf * 8 + (slot >> 2), pn = slot & 3;
    int ln = opq(0);
    if (wid == 0) {
      if (code == 0) flag_wait(P, F_LN1(p2 * 4 + (ln & 3)), 0, ln);
      else {
        const unsigned co = CNT_OFF + C_W1(min(max(p2 - 1 + min(ln, 2), 0), 15)) * 128;
        for (unsigned spins = 0; __any(ld4(rs, co) < 4u);) {
          __builtin_amdgcn_s_sleep(4);
          if (spin_fail(spins, P, ln)) break;
        }
      }
    }
    BARRIER();
    TLW(3 * code);
    const rsrc_t rb = __builtin_amdgcn_make_buffer_rsrc((void*)(code ? P.w2 : P.w1), 0, DM * DM * 2, RSRC_W3);
    const unsigned cA = (code ? WS_H1 : WS_XN2) + p2 * 128 * 2048, cB = pn * 512;
    Pf pf;
    pf.on = wc == 0; pf.a = wr;
    ln &= 31;
    pf.v = wr ? cA + (pn * 2 + (ln >> 4)) * 32768 + (ln & 15) * 128 : cB + ((slot >> 2) * 8 + (ln >> 2)) * 2048 + (ln & 3) * 128;
    pf.step = wr ? 2048 : 64 * 2048;
    f32x4 acq[1][2][4][2];
    gemm_h(lds, rs, rb, cA, cB, 2048, 2048, 16, pf, acq);
    TLW(3 * code + 1);
    ln = opq(0);
    if (code == 0) {
      if (wid < 4) touch(P.w2, DM * DM * 2, ((hf * 32 + slot) * 256 + wid * 64 + ln) * 128, lds);
      epi_rows<0, 1>(acq, rs, WS_H1, 2048, p2, 2 * pn, (pn & 1) ? -1 - (pn >> 1) : 1 + (pn >> 1), wr, wc, ln);
      if (wid == 0) setup_wait(P, ln);
      M_DONE(C_W1(p2));
    } else {
      epi_rows<2, 1>(acq, rs, WS_X1, 2048, p2, 2 * pn, 0, wr, wc, ln);
      M_DONE(C_W2(p2));
      if (wid == 0) cnt_wait(P, C_W2(p2), 4, ln);
      BARRIER();
      const unsigned ro = (unsigned)(p2 * 8 + pn * 2 + wr) * 32768u + ((ln >> 2) & 3) * 1024 + blk_in(wc * 4 + (ln >> 4), (ln & 3) * 8);
      u32x4 v[8];
      #pragma unroll
      for (int i = 0; i < 8; ++i) v[i] = ld16(rs, WS_X1 + ro + i * 4096);
      ln_rows(v);
      #pragma unroll
      for (int i = 0; i < 8; ++i) st16(rs, WS_X1N + ro + i * 4096, v[i]);
      M_DONE(C_LN(p2 >> 1));
    }
    TLW(3 * code + 2);
  }
  #pragma unroll 1
  for (int code = 2; code < 5; ++code) {
    const int pm = hf * 4 + (slot & 3), pn = (slot >> 2) + (code == 3 ? 8 : 0);
    int ln = opq(0);
    if (pn >= 14) {
      #pragma unroll 1
      for (int i = 0; i < 7 * W4S; ++i) {
        if ((wid & (W4S - 1)) == (i & (W4S - 1))) touch(P.w4, DM * FF * 2, (((i / W4S) * 16 + hf * 8 + (slot & 7)) * 512 + wid * 64 + ln) * 128, lds);
        __builtin_amdgcn_s_sleep(96 / W4S);
      }
      continue;
    }
    if (code != 3) {
      if (wid == 0) cnt_wait(P, code == 2 ? C_LN(pm) : C_W3(pm, pn >> 2), code == 2 ? 8 : 7, ln);
      BARRIER();
    }
    TLW(3 * code);
    const bool w4 = code == 4;
    const int lda = w4 ? 7168 : 2048, ldb = w4 ? 2048 : 7168, nt = w4 ? 28 : 16, na = w4 ? 4 : 8;
    const rsrc_t rb = __builtin_amdgcn_make_buffer_rsrc((void*)(w4 ? P.w4 : P.w3), 0, DM * FF * 2, RSRC_W3);
    const unsigned cA = (w4 ? WS_H3 + (pn >> 2) * 28 * 2048 : WS_X1N) + pm * 256 * lda, cB = w4 ? (pn >> 2) * 28 * 64 * 2048 + (pn & 3) * 512 : pn * 512;
    Pf pf;
    pf.on = wc == 0 && (!wr || code != 3); pf.a = wr;
    pf.v = wr ? cA + ((pn & (na - 1)) * (16 / na) + ((ln >> 4) & (16 / na - 1))) * 16 * lda + (ln & 15) * 128 : cB + ((slot & 3) * 16 + (ln >> 2)) * ldb + (ln & 3) * 128;
    pf.step = wr ? 2048 : 64 * ldb;
    f32x4 acc[2][2][4][2];
    gemm_loop(lds, rs, rb, cA, cB, lda, ldb, nt, pf, acc);
    TLW(3 * code + 1);
    ln = opq(0);
    if (!w4) epi_rows<1, 2>(acc, rs, WS_H3, 7168, 2 * pm, 2 * pn, 0, wr, wc, ln);
    else if (pn >> 2) epi_w4<1>(acc, rs, P, pm, pn & 3, wid, ln);
    else epi_w4<0>(acc, rs, P, pm, pn & 3, wid, ln);
    M_DONE(w4 ? C_W4(pm) : C_W3(pm, pn >= 7));
    TLW(3 * code + 2);
  }
}

#define FTH 0
#define FINE 52
__device__ __forceinline__ void fin_role(LAS unsigned char* lds, const Ptrs& P, int wid) {
  const int wr = wid >> 2, wc = wid & 3;
  const rsrc_t rs = WS_RSRC(P);
  volatile LAS int* tk = (volatile LAS int*)(lds + 147456);
  unsigned tks = 0;
#define POP() do { if (wid == 0 && opq(0) == 0) tks = __hip_atomic_fetch_add((gu32*)(P.ws + CNT_OFF + C_HEAD * 128), 1u, RLX_AGENT); } while (0)
  POP();
  for (int round = 0;; ++round) {
    if (wid == 0 && opq(0) == 0) tk[round & 1] = tks;
    LBARRIER();
    const int u = __builtin_amdgcn_readfirstlane(tk[round & 1]);
    if (u >= 2 * FINE + 8 * (XS3 - FINE)) break;
    const bool big = u < 2 * FINE;
    const int sg = big ? u >> 1 : FINE + ((u - 2 * FINE) >> 3), cc0 = big ? (u & 1) * 4 : (u - 2 * FINE) & 7;
    const int ln = opq(0), fq = ln >> 4, cb = 16 * (fq & 1) + 8 * (fq >> 1);
    const unsigned eo = (unsigned)(sg * 32 + wr * 16 + (ln & 15)) * 2048u + (unsigned)(cc0 * 128 + wc * 32 + cb) * 2u;
    const unsigned go = WS_G + (unsigned)(sg * 2 + wr) * 32768u + (cc0 * 4 + wc) * 1024 + blk_in(ln & 15, cb);
    const unsigned po = WS_PT + (unsigned)sg * 524288u + cc0 * 16384 + wc * 4096 + wr * 2048 + ln * 16;
    POP();
    if (wid == 1) cnt_wait(P, C_W4(sg >> 3), 8, ln);
    if (wid == 2) { const int xg = (sg >= XS1) + (sg >= XS2); flag_wait(P, F_PRG(5 + xg, ln & 31), sg + 1 - (xg == 2 ? XS2 : xg * XS1), ln); }
    BARRIER();
    if (!big) fin_out<1>(rs, P, po, go, eo);
    else {
      fin_out<2>(rs, P, po, go, eo); fin_out<2>(rs, P, po + 32768, go + 8192, eo + 512);
    }
  }
}

#define QPRIO 0
#define VPOS 3
#define VLDI "buffer_load_dword v254, v17, %[rs], %[sb] offen sc1\n\tbuffer_load_dword v255, v17, %[rs], %[sb] offen offset:256 sc1\n\t"
#define VLD(p) VLD##p
#define VLD0
#define VLD1
#define VLD2
#define VLD3 VLDI
#define VLD4
#define DEC1 1
#define DEC2 1
#define DGA
#define DGB
#define DGC
#define DGO
#define DGP
#define DGQ
#define PACE "s_sleep 1\n\t"
#define STR_(x) #x
#define STR(x) STR_(x)
#define GSW
#define GSA(k)
#define GSO(k)
#define A_MAIN_Q() \
  asm volatile( \
    "v_mov_b32 v253, %[tag]\n\t" \
    "buffer_load_dwordx4 v[20:23], v12, %[rs], %[vx] offen sc1\n\t" \
    "buffer_load_dwordx4 v[24:27], v12, %[rs], %[vx] offen offset:1024 sc1\n\t" \
    "ds_read_b128 v[236:239], v9 offset:0\n\t" \
    "ds_read_b128 v[240:243], v9 offset:1024\n\t" \
    "ds_read_b128 v[244:247], v9 offset:2048\n\t" \
    "ds_read_b128 v[248:251], v9 offset:3072\n\t" \
    "v_mov_b32 v0, 0\n\t" \
    "v_mov_b32 v1, 0\n\t" \
    "v_mov_b32 v2, 0\n\t" \
    "v_mov_b32 v3, 0\n\t" \
    "v_mov_b32 v4, 0\n\t" \
    "v_mov_b32 v5, 0\n\t" \
    "v_mov_b32 v6, 0\n\t" \
    "v_mov_b32 v7, 0\n\t" \
    "s_cmp_lg_u32 %[sc], 3\n\t" \
    "s_cbranch_scc1 PB_%=\n\t" \
    "TWA_%=:\n\t" \
    "s_memrealtime s[88:89]\n\t" \
    "s_memtime s[90:91]\n\t" \
    "s_waitcnt lgkmcnt(0)\n\t" \
    "s_lshl_b32 s88, s88, 2\n\t" \
    "s_sub_u32 s89, %[tgt], s88\n\t" \
    "s_cmp_gt_i32 s89, 16\n\t" \
    "s_cbranch_scc1 TWA_%=\n\t" \
    "s_mul_i32 s89, s89, 11\n\t" \
    "s_ashr_i32 s89, s89, 1\n\t" \
    "s_add_u32 s89, s89, s90\n\t" \
    DGB \
    "TCA_%=:\n\t" \
    "s_memtime s[92:93]\n\t" \
    "s_waitcnt lgkmcnt(0)\n\t" \
    "s_sub_u32 s93, s89, s92\n\t" \
    "s_cmp_gt_i32 s93, 0\n\t" \
    "s_cbranch_scc1 TCA_%=\n\t" \
    "PB_%=:\n\t" \
    "s_waitcnt lgkmcnt(0)\n\t" \
    "s_barrier\n\t" \
    "buffer_load_dwordx4 v[220:223], v8, %[rs], %[m0] offen sc1\n\t" \
    "buffer_load_dwordx4 v[224:227], v8, %[rs], %[m1] offen sc1\n\t" \
    "PM_%=:\n\t" \
    "s_waitcnt vmcnt(1)\n\t" \
    "ds_write_b128 v8, v[220:223] offset:0\n\t" \
    "v_max3_u32 v252, v220, v221, v222\n\t" \
    "s_waitcnt vmcnt(0)\n\t" \
    "ds_write_b128 v8, v[224:227] offset:4096\n\t" \
    "v_max3_u32 v252, v252, v223, v224\n\t" \
    "v_max3_u32 v252, v252, v225, v226\n\t" \
    "v_max_u32 v252, v252, v227\n\t" \
    "v_cmp_eq_u32 vcc, -1, v252\n\t" \
    "s_cbranch_vccz PD_%=\n\t" \
    "s_sub_u32 %[lim], %[lim], 1\n\t" \
    "s_cmp_gt_i32 %[lim], 0\n\t" \
    "s_cbranch_scc0 PD_%=\n\t" \
    "s_sleep " STR(CADM) "\n\t" \
    "buffer_load_dwordx4 v[220:223], v8, %[rs], %[m0] offen sc1\n\t" \
    "buffer_load_dwordx4 v[224:227], v8, %[rs], %[m1] offen sc1\n\t" \
    "s_branch PM_%=\n\t" \
    "PD_%=:\n\t" \
    GSA(0) \
    "v_smfmac_f32_16x16x64_bf16 v[0:3], v[220:223], v[236:243], v10\n\t" \
    "v_xor_b32 v9, 16384, v9\n\t" \
    "ds_read_b128 v[236:239], v9 offset:0\n\t" \
    "ds_read_b128 v[240:243], v9 offset:1024\n\t" \
    "v_smfmac_f32_16x16x64_bf16 v[4:7], v[220:223], v[244:251], v10\n\t" \
    "ds_read_b128 v[244:247], v9 offset:2048\n\t" \
    "ds_read_b128 v[248:251], v9 offset:3072\n\t" \
    "s_waitcnt lgkmcnt(4)\n\t" \
    "s_barrier\n\t" \
    "ds_read_b128 v[228:231], v8 offset:8192\n\t" \
    "ds_read_b128 v[232:235], v8 offset:12288\n\t" \
    "v_xor_b32 v8, 1024, v8\n\t" \
    "ds_read_b128 v[220:223], v8 offset:0\n\t" \
    "v_smfmac_f32_16x16x64_bf16 v[0:3], v[224:227], v[28:35], v10\n\t" \
    "v_smfmac_f32_16x16x64_bf16 v[4:7], v[224:227], v[36:43], v10\n\t" \
    "ds_read_b128 v[224:227], v8 offset:4096\n\t" \
    "s_waitcnt lgkmcnt(3)\n\t" \
    "v_smfmac_f32_16x16x64_bf16 v[0:3], v[228:231], v[92:99], v10\n\t" \
    "v_smfmac_f32_16x16x64_bf16 v[4:7], v[228:231], v[100:107], v10\n\t" \
    "ds_read_b128 v[228:231], v8 offset:8192\n\t" \
    "s_waitcnt lgkmcnt(3)\n\t" \
    "v_smfmac_f32_16x16x64_bf16 v[0:3], v[232:235], v[156:163], v10\n\t" \
    "v_smfmac_f32_16x16x64_bf16 v[4:7], v[232:235], v[164:171], v10\n\t" \
    "ds_read_b128 v[232:235], v8 offset:12288\n\t" \
    "s_waitcnt lgkmcnt(3)\n\t" \
    "v_smfmac_f32_16x16x64_bf16 v[0:3], v[220:223], v[236:243], v10\n\t" \
    "v_xor_b32 v9, 49152, v9\n\t" \
    "ds_read_b128 v[236:239], v9 offset:0\n\t" \
    "v_smfmac_f32_16x16x64_bf16 v[4:7], v[220:223], v[244:251], v10\n\t" \
    "ds_read_b128 v[240:243], v9 offset:1024\n\t" \
    "s_waitcnt lgkmcnt(4)\n\t" \
    "v_smfmac_f32_16x16x64_bf16 v[0:3], v[224:227], v[44:51], v10\n\t" \
    "v_xor_b32 v8, 3072, v8\n\t" \
    "ds_read_b128 v[220:223], v8 offset:4096\n\t" \
    "v_smfmac_f32_16x16x64_bf16 v[4:7], v[224:227], v[52:59], v10\n\t" \
    "ds_read_b128 v[244:247], v9 offset:2048\n\t" \
    "s_waitcnt lgkmcnt(5)\n\t" \
    "v_smfmac_f32_16x16x64_bf16 v[0:3], v[228:231], v[108:115], v10\n\t" \
    "ds_read_b128 v[224:227], v8 offset:8192\n\t" \
    "v_smfmac_f32_16x16x64_bf16 v[4:7], v[228:231], v[116:123], v10\n\t" \
    "ds_read_b128 v[248:251], v9 offset:3072\n\t" \
    "s_waitcnt lgkmcnt(6)\n\t" \
    "v_smfmac_f32_16x16x64_bf16 v[0:3], v[232:235], v[172:179], v10\n\t" \
    "ds_read_b128 v[228:231], v8 offset:0\n\t" \
    "v_smfmac_f32_16x16x64_bf16 v[4:7], v[232:235], v[180:187], v10\n\t" \
    "ds_read_b128 v[232:235], v8 offset:12288\n\t" \
    "s_waitcnt lgkmcnt(5)\n\t" \
    "v_smfmac_f32_16x16x64_bf16 v[0:3], v[220:223], v[60:67], v10\n\t" \
    "v_smfmac_f32_16x16x64_bf16 v[4:7], v[220:223], v[68:75], v10\n\t" \
    "v_xor_b32 v8, 1024, v8\n\t" \
    "ds_read_b128 v[220:223], v8 offset:4096\n\t" \
    "s_waitcnt lgkmcnt(4)\n\t" \
    "v_smfmac_f32_16x16x64_bf16 v[0:3], v[224:227], v[124:131], v10\n\t" \
    "v_smfmac_f32_16x16x64_bf16 v[4:7], v[224:227], v[132:139], v10\n\t" \
    "ds_read_b128 v[224:227], v8 offset:8192\n\t" \
    "s_waitcnt lgkmcnt(3)\n\t" \
    "v_smfmac_f32_16x16x64_bf16 v[0:3], v[228:231], v[236:243], v10\n\t" \
    "v_xor_b32 v9, 16384, v9\n\t" \
    "ds_read_b128 v[236:239], v9 offset:0\n\t" \
    "v_smfmac_f32_16x16x64_bf16 v[4:7], v[228:231], v[244:251], v10\n\t" \
    "ds_read_b128 v[240:243], v9 offset:1024\n\t" \
    "s_waitcnt lgkmcnt(4)\n\t" \
    "v_smfmac_f32_16x16x64_bf16 v[0:3], v[232:235], v[188:195], v10\n\t" \
    "ds_read_b128 v[228:231], v8 offset:12288\n\t" \
    "v_smfmac_f32_16x16x64_bf16 v[4:7], v[232:235], v[196:203], v10\n\t" \
    "ds_read_b128 v[244:247], v9 offset:2048\n\t" \
    "s_waitcnt lgkmcnt(5)\n\t" \
    "v_smfmac_f32_16x16x64_bf16 v[0:3], v[220:223], v[76:83], v10\n\t" \
    "ds_read_b128 v[232:235], v8 offset:0\n\t" \
    "v_xor_b32 v8, 3072, v8\n\t" \
    "v_smfmac_f32_16x16x64_bf16 v[4:7], v[220:223], v[84:91], v10\n\t" \
    "ds_read_b128 v[248:251], v9 offset:3072\n\t" \
    "s_waitcnt lgkmcnt(6)\n\t" \
    "v_smfmac_f32_16x16x64_bf16 v[0:3], v[224:227], v[140:147], v10\n\t" \
    "ds_read_b128 v[220:223], v12 offset:24576\n\t" \
    "v_smfmac_f32_16x16x64_bf16 v[4:7], v[224:227], v[148:155], v10\n\t" \
    "ds_read_b128 v[224:227], v12 offset:28672\n\t" \
    "s_waitcnt lgkmcnt(5)\n\t" \
    "v_smfmac_f32_16x16x64_bf16 v[0:3], v[228:231], v[204:211], v10\n\t" \
    "v_smfmac_f32_16x16x64_bf16 v[4:7], v[228:231], v[212:219], v10\n\t" \
    "ds_read_b128 v[228:231], v12 offset:25600\n\t" \
    "s_waitcnt lgkmcnt(3)\n\t" \
    "v_smfmac_f32_16x16x64_bf16 v[0:3], v[232:235], v[236:243], v10\n\t" \
    "v_xor_b32 v9, 49152, v9\n\t" \
    "v_smfmac_f32_16x16x64_bf16 v[4:7], v[232:235], v[244:251], v10\n\t" \
    VLD(4) \
    "s_nop 5\n\t" \
    "v_add_f32 v0, v0, v1\n\t" \
    "v_add_f32 v2, v2, v3\n\t" \
    "v_add_f32 v4, v4, v5\n\t" \
    "v_add_f32 v6, v6, v7\n\t" \
    "v_mul_f32 v0, %[sl], v0\n\t" \
    "v_mul_f32 v2, %[sl], v2\n\t" \
    "v_mul_f32 v4, %[sl], v4\n\t" \
    "v_mul_f32 v6, %[sl], v6\n\t" \
    "v_cvt_pk_bf16_f32 v0, v0, v4\n\t" \
    "v_cvt_pk_bf16_f32 v2, v2, v6\n\t" \
    "ds_write2_b32 v11, v0, v2 offset0:0 offset1:4\n\t" \
    "s_mov_b64 exec, %[onej]\n\t" \
    "ds_write_b32 v16, v253\n\t" \
    "s_mov_b64 exec, -1\n\t" \
    GSA(1) \
    "s_setprio " STR(QPRIO) "\n\t" \
    "ds_read_b128 v[232:235], v12 offset:29696\n\t" \
    "s_nop 5\n\t" \
    "ds_read_b128 v[0:3], v12 offset:26624\n\t" \
    "s_nop 5\n\t" \
    "ds_read_b128 v[4:7], v12 offset:27648\n\t" \
    "s_nop 5\n\t" \
    "ds_read_b32 v252, v16\n\t" \
    "ds_read_b128 v[236:239], v14 offset:0\n\t" \
    "s_nop 5\n\t" \
    "ds_read_b128 v[240:243], v14 offset:512\n\t" \
    "s_nop 5\n\t" \
    "ds_read_b128 v[244:247], v14 offset:1024\n\t" \
    "s_nop 5\n\t" \
    VLD(3) \
    "ds_read_b128 v[248:251], v14 offset:1536\n\t" \
    "s_nop 5\n\t" \
    "s_setprio 0\n\t" \
    "s_waitcnt lgkmcnt(4)\n\t" \
    "v_cmp_ne_u32 vcc, v253, v252\n\t" \
    "s_cbranch_vccnz RP_%=\n\t" \
    "CP_%=:\n\t" \
    VLD(0) \
    "s_waitcnt lgkmcnt(2)\n\t" \
    "v_mfma_f32_16x16x32_bf16 v[220:223], v[220:223], v[236:239], 0\n\t" \
    "v_mfma_f32_16x16x32_bf16 v[224:227], v[224:227], v[236:239], 0\n\t" \
    "v_mfma_f32_16x16x32_bf16 v[220:223], v[228:231], v[240:243], v[220:223]\n\t" \
    "ds_read_b128 v[228:231], v12 offset:30720\n\t" \
    "v_mfma_f32_16x16x32_bf16 v[224:227], v[232:235], v[240:243], v[224:227]\n\t" \
    "ds_read_b128 v[232:235], v12 offset:31744\n\t" \
    "s_setprio " STR(QPRIO) "\n\t" \
    VLD(1) \
    "s_waitcnt lgkmcnt(2)\n\t" \
    "v_mfma_f32_16x16x32_bf16 v[220:223], v[0:3], v[244:247], v[220:223]\n\t" \
    "v_mfma_f32_16x16x32_bf16 v[220:223], v[4:7], v[248:251], v[220:223]\n\t" \
    "s_waitcnt lgkmcnt(1)\n\t" \
    "v_mfma_f32_16x16x32_bf16 v[224:227], v[228:231], v[244:247], v[224:227]\n\t" \
    "s_waitcnt lgkmcnt(0)\n\t" \
    "v_mfma_f32_16x16x32_bf16 v[224:227], v[232:235], v[248:251], v[224:227]\n\t" \
    GSA(2) \
    "s_setprio 2\n\t" \
    "ds_read_b32 v252, v16 offset:16\n\t" \
    "ds_read_b128 v[0:3], v15 offset:0\n\t" \
    "ds_read_b128 v[4:7], v15 offset:512\n\t" \
    "ds_read_b128 v[228:231], v15 offset:1024\n\t" \
    "ds_read_b128 v[232:235], v15 offset:1536\n\t" \
    VLD(2) \
    "v_exp_f32 v220, v220\n\t" \
    "v_exp_f32 v221, v221\n\t" \
    "v_exp_f32 v222, v222\n\t" \
    "v_exp_f32 v223, v223\n\t" \
    "v_exp_f32 v224, v224\n\t" \
    "v_exp_f32 v225, v225\n\t" \
    "v_exp_f32 v226, v226\n\t" \
    "v_exp_f32 v227, v227\n\t" \
    "v_cvt_pk_bf16_f32 v220, v220, v221\n\t" \
    "v_cvt_pk_bf16_f32 v221, v222, v223\n\t" \
    "s_waitcnt lgkmcnt(4)\n\t" \
    "v_cmp_ne_u32 vcc, %[tag], v252\n\t" \
    "s_cbranch_vccnz RK_%=\n\t" \
    "CK_%=:\n\t" \
    "s_waitcnt lgkmcnt(3)\n\t" \
    "v_mfma_f32_16x16x32_bf16 v[0:3], v[0:3], v[236:239], 0\n\t" \
    "v_cvt_pk_bf16_f32 v222, v224, v225\n\t" \
    "v_cvt_pk_bf16_f32 v223, v226, v227\n\t" \
    "s_waitcnt lgkmcnt(2)\n\t" \
    "v_mfma_f32_16x16x32_bf16 v[0:3], v[4:7], v[240:243], v[0:3]\n\t" \
    "v_mov_b32 v224, %[one]\n\t" \
    "v_mov_b32 v225, %[one]\n\t" \
    "s_waitcnt lgkmcnt(1)\n\t" \
    "v_mfma_f32_16x16x32_bf16 v[0:3], v[228:231], v[244:247], v[0:3]\n\t" \
    "v_mov_b32 v226, %[one]\n\t" \
    "v_mov_b32 v227, %[one]\n\t" \
    "s_waitcnt lgkmcnt(0)\n\t" \
    "v_mfma_f32_16x16x32_bf16 v[0:3], v[232:235], v[248:251], v[0:3]\n\t" \
    "v_mfma_f32_16x16x32_bf16 v[244:247], v[224:227], v[220:223], 0\n\t" \
    "v_mov_b32 v229, 0\n\t" \
    "v_mfma_f32_16x16x32_bf16 v[236:239], v[20:23], v[220:223], 0\n\t" \
    "v_mov_b32 v230, 0\n\t" \
    "v_mfma_f32_16x16x32_bf16 v[240:243], v[24:27], v[220:223], 0\n\t" \
    "s_nop 2\n\t" \
    "v_exp_f32 v0, v0\n\t" \
    "v_exp_f32 v1, v1\n\t" \
    "s_nop 0\n\t" \
    "v_cvt_pk_bf16_f32 v228, v0, v1\n\t" \
    "v_cvt_pk_bf16_f32 v231, v0, v1\n\t" \
    GSA(3) \
    "CM_%=:\n\t" \
    "s_nop 0\n\t" \
    "v_mfma_f32_16x16x16_bf16 v[244:247], v[224:225], v[228:229], v[244:247]\n\t" \
    "s_waitcnt vmcnt(0)\n\t" \
    "v_mfma_f32_16x16x16_bf16 v[236:239], v[254:255], v[228:229], v[236:239]\n\t" \
    "v_max_u32 v7, v254, v255\n\t" \
    "v_mfma_f32_16x16x16_bf16 v[240:243], v[254:255], v[230:231], v[240:243]\n\t" \
    "v_cmp_eq_u32 s[88:89], -1, v7\n\t" \
    "s_not_b64 exec, %[hi]\n\t" \
    "s_nop 1\n\t" \
    "v_rcp_f32 v2, v244\n\t" \
    "v_subrev_u32 v253, 0x21800000, v244\n\t" \
    "v_cmp_gt_u32 vcc, v253, %[rg]\n\t" \
    "v_pk_mul_f32 v[236:237], v[236:237], v[2:3] op_sel_hi:[1,0]\n\t" \
    "v_pk_mul_f32 v[238:239], v[238:239], v[2:3] op_sel_hi:[1,0]\n\t" \
    "v_pk_mul_f32 v[240:241], v[240:241], v[2:3] op_sel_hi:[1,0]\n\t" \
    "v_pk_mul_f32 v[242:243], v[242:243], v[2:3] op_sel_hi:[1,0]\n\t" \
    "s_or_b64 vcc, vcc, s[88:89]\n\t" \
    "v_cvt_pk_bf16_f32 v4, v236, v237\n\t" \
    "v_cvt_pk_bf16_f32 v5, v238, v239\n\t" \
    "v_cvt_pk_bf16_f32 v6, v240, v241\n\t" \
    "v_cvt_pk_bf16_f32 v7, v242, v243\n\t" \
    "s_cbranch_scc1 FX_%=\n\t" \
    "buffer_store_dwordx4 v[4:7], v18, %[rs], %[sb] offen sc1\n\t" \
    "s_memrealtime %[tp]\n\t" \
    DGA \
    GSA(4) \
    "v_max3_u32 v252, v4, v5, v6\n\t" \
    "v_max_u32 v252, v252, v7\n\t" \
    "v_cmp_eq_u32 vcc, -1, v252\n\t" \
    "s_cbranch_vccnz RC_%=\n\t" \
    "CC_%=:\n\t" \
    "s_mov_b64 exec, -1\n\t" \
    "v_add_u32 v252, %[vd], v12\n\t" \
    "v_add_u32 v253, %[md], v17\n\t" \
    "ds_write_b128 v252, v[20:23]\n\t" \
    "ds_write_b128 v252, v[24:27] offset:1024\n\t" \
    "ds_write2st64_b32 v253, v254, v255 offset0:0 offset1:1\n\t" \
    "s_waitcnt lgkmcnt(0)\n\t" \
    "s_cmp_lg_u32 %[sc], 3\n\t" \
    "s_cbranch_scc1 NH_%=\n\t" \
    "buffer_load_dwordx2 v[0:1], off, %[rs], %[fl] sc1\n\t" \
    "s_barrier\n\t" \
    "s_waitcnt vmcnt(0)\n\t" \
    "v_readfirstlane_b32 %[pt], v0\n\t" \
    "v_readfirstlane_b32 %[pd], v1\n\t" \
    "s_nop 3\n\t" \
    "s_branch E_%=\n\t" \
    "NH_%=:\n\t" \
    "s_barrier\n\t" \
    "s_branch E_%=\n\t" \
    "RP_%=:\n\t" \
    "s_waitcnt lgkmcnt(0)\n\t" \
    "ds_read_b32 v252, v16\n\t" \
    "ds_read_b128 v[236:239], v14 offset:0\n\t" \
    "ds_read_b128 v[240:243], v14 offset:512\n\t" \
    "ds_read_b128 v[244:247], v14 offset:1024\n\t" \
    "ds_read_b128 v[248:251], v14 offset:1536\n\t" \
    "s_sub_u32 %[lim], %[lim], " STR(DEC1) "\n\t" \
    "s_cmp_gt_i32 %[lim], 0\n\t" \
    "s_waitcnt lgkmcnt(4)\n\t" \
    "s_cbranch_scc0 CP_%=\n\t" \
    "v_cmp_ne_u32 vcc, %[tag], v252\n\t" \
    "s_cbranch_vccnz RP_%=\n\t" \
    "s_branch CP_%=\n\t" \
    "RK_%=:\n\t" \
    "s_waitcnt lgkmcnt(0)\n\t" \
    "ds_read_b32 v252, v16 offset:16\n\t" \
    "ds_read_b128 v[0:3], v15 offset:0\n\t" \
    "ds_read_b128 v[4:7], v15 offset:512\n\t" \
    "ds_read_b128 v[228:231], v15 offset:1024\n\t" \
    "ds_read_b128 v[232:235], v15 offset:1536\n\t" \
    "s_sub_u32 %[lim], %[lim], " STR(DEC1) "\n\t" \
    "s_cmp_gt_i32 %[lim], 0\n\t" \
    "s_waitcnt lgkmcnt(4)\n\t" \
    "s_cbranch_scc0 CK_%=\n\t" \
    "v_cmp_ne_u32 vcc, %[tag], v252\n\t" \
    "s_cbranch_vccnz RK_%=\n\t" \
    "s_branch CK_%=\n\t" \
    "RC_%=:\n\t" \
    "v_min_u32 v4, -2, v4\n\t" \
    "v_min_u32 v5, -2, v5\n\t" \
    "v_min_u32 v6, -2, v6\n\t" \
    "v_min_u32 v7, -2, v7\n\t" \
    "buffer_store_dwordx4 v[4:7], v18, %[rs], %[sb] offen sc1\n\t" \
    "s_branch CC_%=\n\t" \
    "FX_%=:\n\t" \
    "s_mov_b64 exec, -1\n\t" \
    "s_cmp_eq_u64 s[88:89], 0\n\t" \
    "s_cbranch_scc1 RM_%=\n\t" \
    "buffer_load_dword v254, v17, %[rs], %[sb] offen sc1\n\t" \
    "buffer_load_dword v255, v17, %[rs], %[sb] offen offset:256 sc1\n\t" \
    "v_mfma_f32_16x16x32_bf16 v[244:247], v[224:227], v[220:223], 0\n\t" \
    "v_mfma_f32_16x16x32_bf16 v[236:239], v[20:23], v[220:223], 0\n\t" \
    "v_mfma_f32_16x16x32_bf16 v[240:243], v[24:27], v[220:223], 0\n\t" \
    "s_sub_u32 %[lim], %[lim], " STR(DEC2) "\n\t" \
    "s_cmp_gt_i32 %[lim], 0\n\t" \
    "s_cbranch_scc1 CM_%=\n\t" \
    "s_waitcnt vmcnt(0)\n\t" \
    "v_mov_b32 v254, 0\n\t" \
    "v_mov_b32 v255, 0\n\t" \
    "s_branch CM_%=\n\t" \
    "RM_%=:\n\t" \
    "s_mov_b32 %[rg], -1\n\t" \
    "s_nop 7\n\t" \
    "ds_read_b128 v[236:239], v14 offset:0\n\t" \
    "ds_read_b128 v[240:243], v14 offset:512\n\t" \
    "ds_read_b128 v[244:247], v14 offset:1024\n\t" \
    "ds_read_b128 v[248:251], v14 offset:1536\n\t" \
    "ds_read_b128 v[232:235], v12 offset:24576\n\t" \
    "s_waitcnt lgkmcnt(0)\n\t" \
    "v_mfma_f32_16x16x32_bf16 v[220:223], v[232:235], v[236:239], 0\n\t" \
    "ds_read_b128 v[232:235], v12 offset:25600\n\t" \
    "s_waitcnt lgkmcnt(0)\n\t" \
    "v_mfma_f32_16x16x32_bf16 v[220:223], v[232:235], v[240:243], v[220:223]\n\t" \
    "ds_read_b128 v[232:235], v12 offset:26624\n\t" \
    "s_waitcnt lgkmcnt(0)\n\t" \
    "v_mfma_f32_16x16x32_bf16 v[220:223], v[232:235], v[244:247], v[220:223]\n\t" \
    "ds_read_b128 v[232:235], v12 offset:27648\n\t" \
    "s_waitcnt lgkmcnt(0)\n\t" \
    "v_mfma_f32_16x16x32_bf16 v[220:223], v[232:235], v[248:251], v[220:223]\n\t" \
    "ds_read_b128 v[232:235], v12 offset:28672\n\t" \
    "s_waitcnt lgkmcnt(0)\n\t" \
    "v_mfma_f32_16x16x32_bf16 v[224:227], v[232:235], v[236:239], 0\n\t" \
    "ds_read_b128 v[232:235], v12 offset:29696\n\t" \
    "s_waitcnt lgkmcnt(0)\n\t" \
    "v_mfma_f32_16x16x32_bf16 v[224:227], v[232:235], v[240:243], v[224:227]\n\t" \
    "ds_read_b128 v[232:235], v12 offset:30720\n\t" \
    "s_waitcnt lgkmcnt(0)\n\t" \
    "v_mfma_f32_16x16x32_bf16 v[224:227], v[232:235], v[244:247], v[224:227]\n\t" \
    "ds_read_b128 v[232:235], v12 offset:31744\n\t" \
    "s_waitcnt lgkmcnt(0)\n\t" \
    "v_mfma_f32_16x16x32_bf16 v[224:227], v[232:235], v[248:251], v[224:227]\n\t" \
    "ds_read_b128 v[232:235], v15 offset:0\n\t" \
    "s_waitcnt lgkmcnt(0)\n\t" \
    "v_mfma_f32_16x16x32_bf16 v[0:3], v[232:235], v[236:239], 0\n\t" \
    "ds_read_b128 v[232:235], v15 offset:512\n\t" \
    "s_waitcnt lgkmcnt(0)\n\t" \
    "v_mfma_f32_16x16x32_bf16 v[0:3], v[232:235], v[240:243], v[0:3]\n\t" \
    "ds_read_b128 v[232:235], v15 offset:1024\n\t" \
    "s_waitcnt lgkmcnt(0)\n\t" \
    "v_mfma_f32_16x16x32_bf16 v[0:3], v[232:235], v[244:247], v[0:3]\n\t" \
    "ds_read_b128 v[232:235], v15 offset:1536\n\t" \
    "s_waitcnt lgkmcnt(0)\n\t" \
    "v_mfma_f32_16x16x32_bf16 v[0:3], v[232:235], v[248:251], v[0:3]\n\t" \
    "v_max3_f32 v252, v220, v221, v222\n\t" \
    "v_max3_f32 v253, v223, v224, v225\n\t" \
    "v_max3_f32 v252, v252, v226, v227\n\t" \
    "s_nop 4\n\t" \
    "v_max3_f32 v253, v253, v0, v1\n\t" \
    "v_max_f32 v252, v252, v253\n\t" \
    "v_mov_b32 v253, v252\n\t" \
    "v_mov_b32 v2, v252\n\t" \
    "s_nop 1\n\t" \
    "v_permlane16_swap_b32 v253, v2\n\t" \
    "v_max_f32 v252, v253, v2\n\t" \
    "v_max_f32 v253, v253, v2\n\t" \
    "s_nop 1\n\t" \
    "v_permlane32_swap_b32 v252, v253\n\t" \
    "v_max_f32 v252, v252, v253\n\t" \
    "v_sub_f32 v220, v220, v252\n\t" \
    "v_sub_f32 v221, v221, v252\n\t" \
    "v_sub_f32 v222, v222, v252\n\t" \
    "v_sub_f32 v223, v223, v252\n\t" \
    "v_sub_f32 v224, v224, v252\n\t" \
    "v_sub_f32 v225, v225, v252\n\t" \
    "v_sub_f32 v226, v226, v252\n\t" \
    "v_sub_f32 v227, v227, v252\n\t" \
    "v_sub_f32 v0, v0, v252\n\t" \
    "v_sub_f32 v1, v1, v252\n\t" \
    "v_exp_f32 v220, v220\n\t" \
    "v_exp_f32 v221, v221\n\t" \
    "v_exp_f32 v222, v222\n\t" \
    "v_exp_f32 v223, v223\n\t" \
    "v_exp_f32 v224, v224\n\t" \
    "v_exp_f32 v225, v225\n\t" \
    "v_exp_f32 v226, v226\n\t" \
    "v_exp_f32 v227, v227\n\t" \
    "v_cvt_pk_bf16_f32 v220, v220, v221\n\t" \
    "v_cvt_pk_bf16_f32 v221, v222, v223\n\t" \
    "v_cvt_pk_bf16_f32 v222, v224, v225\n\t" \
    "v_cvt_pk_bf16_f32 v223, v226, v227\n\t" \
    "v_mov_b32 v224, %[one]\n\t" \
    "v_mov_b32 v225, %[one]\n\t" \
    "v_mov_b32 v226, %[one]\n\t" \
    "v_mov_b32 v227, %[one]\n\t" \
    "s_nop 1\n\t" \
    "v_mfma_f32_16x16x32_bf16 v[244:247], v[224:227], v[220:223], 0\n\t" \
    "v_mfma_f32_16x16x32_bf16 v[236:239], v[20:23], v[220:223], 0\n\t" \
    "v_mfma_f32_16x16x32_bf16 v[240:243], v[24:27], v[220:223], 0\n\t" \
    "v_exp_f32 v0, v0\n\t" \
    "v_exp_f32 v1, v1\n\t" \
    "s_nop 0\n\t" \
    "v_cvt_pk_bf16_f32 v228, v0, v1\n\t" \
    "v_cvt_pk_bf16_f32 v231, v0, v1\n\t" \
    "s_nop 1\n\t" \
    "s_branch CM_%=\n\t" \
    "E_%=:\n\t" \
    GSW \
    "s_nop 1\n\t" \
    : [lim] "+s"(lim), [rg] "+s"(rg), [tp] "=&s"(tp), [pt] "=&s"(pt), [pd] "=&s"(pd) DGO DGP GSO(0) GSO(1) GSO(2) GSO(3) GSO(4) \
    : "{v8}"(am_a), "{v9}"(t0_a), "{v10}"(idx), "{v11}"(iw_a), "{v12}"(ab_a), "{v14}"(qr_a), "{v15}"(kr_a), "{v16}"(fr_a), "{v17}"(vo_v), "{v18}"(oo_v), [rs] "s"(rs), [sb] "s"(sb), [tag] "s"(0xffffff81u + s), [hi] "s"(0xff00ff00ff00ff00ull), [tgt] "s"(tgt), [sc] "s"(wid), [onej] "s"(onej), [sl] "s"(0x3e0293ee), [one] "s"(0x3f803f80), [m0] "s"(sbm), [m1] "s"(sbm + 4096u), [vx] "s"(vxo), [fl] "s"(fb + FL_T(s + 1)), [vd] "s"(8192 + j * 2048), [md] "s"(md), "{v[28:35]}"(wr[0][0]), "{v[36:43]}"(wr[0][1]), "{v[44:51]}"(wr[1][0]), "{v[52:59]}"(wr[1][1]), "{v[60:67]}"(wr[2][0]), "{v[68:75]}"(wr[2][1]), "{v[76:83]}"(wr[3][0]), "{v[84:91]}"(wr[3][1]), "{v[92:99]}"(wr[4][0]), "{v[100:107]}"(wr[4][1]), "{v[108:115]}"(wr[5][0]), "{v[116:123]}"(wr[5][1]), "{v[124:131]}"(wr[6][0]), "{v[132:139]}"(wr[6][1]), "{v[140:147]}"(wr[7][0]), "{v[148:155]}"(wr[7][1]), "{v[156:163]}"(wr[8][0]), "{v[164:171]}"(wr[8][1]), "{v[172:179]}"(wr[9][0]), "{v[180:187]}"(wr[9][1]), "{v[188:195]}"(wr[10][0]), "{v[196:203]}"(wr[10][1]), "{v[204:211]}"(wr[11][0]), "{v[212:219]}"(wr[11][1]) \
    : "memory", "vcc", "scc", "s88", "s89", "s90", "s91", "s92", "s93", "v0", "v1", "v2", "v3", "v4", "v5", "v6", "v7", "v20", "v21", "v22", "v23", "v24", "v25", "v26", "v27", "v220", "v221", "v222", "v223", "v224", "v225", "v226", "v227", "v228", "v229", "v230", "v231", "v232", "v233", "v234", "v235", "v236", "v237", "v238", "v239", "v240", "v241", "v242", "v243", "v244", "v245", "v246", "v247", "v248", "v249", "v250", "v251", "v252", "v253", "v254", "v255")

#define A_MAIN_K() \
  asm volatile( \
    "ds_read_b128 v[236:239], v9 offset:0\n\t" \
    "ds_read_b128 v[240:243], v9 offset:1024\n\t" \
    "ds_read_b128 v[244:247], v9 offset:2048\n\t" \
    "ds_read_b128 v[248:251], v9 offset:3072\n\t" \
    "v_mov_b32 v0, 0\n\t" \
    "v_mov_b32 v1, 0\n\t" \
    "v_mov_b32 v2, 0\n\t" \
    "v_mov_b32 v3, 0\n\t" \
    "v_mov_b32 v4, 0\n\t" \
    "v_mov_b32 v5, 0\n\t" \
    "v_mov_b32 v6, 0\n\t" \
    "v_mov_b32 v7, 0\n\t" \
    "s_waitcnt lgkmcnt(0)\n\t" \
    "s_barrier\n\t" \
    "buffer_load_dwordx4 v[220:223], v8, %[rs], %[m0] offen sc1\n\t" \
    "buffer_load_dwordx4 v[224:227], v8, %[rs], %[m1] offen sc1\n\t" \
    GSA(3) \
    "v_mov_b32 v252, v252\n\t" \
    "s_nop 2\n\t" \
    "v_mov_b32 v252, v252\n\t" \
    "s_nop 2\n\t" \
    "v_mov_b32 v252, v252\n\t" \
    "s_nop 2\n\t" \
    "v_mov_b32 v252, v252\n\t" \
    "s_nop 2\n\t" \
    "v_mov_b32 v252, v252\n\t" \
    "s_nop 2\n\t" \
    "v_mov_b32 v252, v252\n\t" \
    "s_nop 2\n\t" \
    "v_mov_b32 v252, v252\n\t" \
    "s_nop 2\n\t" \
    "v_mov_b32 v252, v252\n\t" \
    "s_nop 2\n\t" \
    "v_mov_b32 v252, v252\n\t" \
    "s_nop 2\n\t" \
    "v_mov_b32 v252, v252\n\t" \
    "s_nop 2\n\t" \
    "v_mov_b32 v252, v252\n\t" \
    "s_nop 2\n\t" \
    "v_mov_b32 v252, v252\n\t" \
    "s_nop 2\n\t" \
    "v_mov_b32 v252, v252\n\t" \
    "s_nop 2\n\t" \
    "v_mov_b32 v252, v252\n\t" \
    "s_nop 2\n\t" \
    "v_mov_b32 v252, v252\n\t" \
    "s_nop 2\n\t" \
    "v_mov_b32 v252, v252\n\t" \
    "s_nop 2\n\t" \
    "v_mov_b32 v252, v252\n\t" \
    "s_nop 2\n\t" \
    "v_mov_b32 v252, v252\n\t" \
    "s_nop 2\n\t" \
    "v_mov_b32 v252, v252\n\t" \
    "s_nop 2\n\t" \
    "v_mov_b32 v252, v252\n\t" \
    "s_nop 2\n\t" \
    "v_mov_b32 v252, v252\n\t" \
    "s_nop 2\n\t" \
    "v_mov_b32 v252, v252\n\t" \
    "s_nop 2\n\t" \
    "v_mov_b32 v252, v252\n\t" \
    "s_nop 2\n\t" \
    "v_mov_b32 v252, v252\n\t" \
    "s_nop 2\n\t" \
    "v_mov_b32 v252, v252\n\t" \
    "s_nop 2\n\t" \
    "v_mov_b32 v252, v252\n\t" \
    "s_nop 2\n\t" \
    "v_mov_b32 v252, v252\n\t" \
    "s_nop 2\n\t" \
    "v_mov_b32 v252, v252\n\t" \
    "s_nop 2\n\t" \
    "v_mov_b32 v252, v252\n\t" \
    "s_nop 2\n\t" \
    "v_mov_b32 v252, v252\n\t" \
    "s_nop 2\n\t" \
    "PM_%=:\n\t" \
    "s_waitcnt vmcnt(1)\n\t" \
    "ds_write_b128 v8, v[220:223] offset:8192\n\t" \
    "v_max3_u32 v252, v220, v221, v222\n\t" \
    "s_waitcnt vmcnt(0)\n\t" \
    "ds_write_b128 v8, v[224:227] offset:12288\n\t" \
    "v_max3_u32 v252, v252, v223, v224\n\t" \
    "v_max3_u32 v252, v252, v225, v226\n\t" \
    "v_max_u32 v252, v252, v227\n\t" \
    "v_cmp_eq_u32 vcc, -1, v252\n\t" \
    "s_cbranch_vccz PD_%=\n\t" \
    "s_sub_u32 %[lim], %[lim], 1\n\t" \
    "s_cmp_gt_i32 %[lim], 0\n\t" \
    "s_cbranch_scc0 PD_%=\n\t" \
    "s_sleep " STR(CADM) "\n\t" \
    "buffer_load_dwordx4 v[220:223], v8, %[rs], %[m0] offen sc1\n\t" \
    "buffer_load_dwordx4 v[224:227], v8, %[rs], %[m1] offen sc1\n\t" \
    "s_branch PM_%=\n\t" \
    "PD_%=:\n\t" \
    GSA(0) \
    "v_smfmac_f32_16x16x64_bf16 v[0:3], v[220:223], v[236:243], v10\n\t" \
    "v_xor_b32 v9, 16384, v9\n\t" \
    "ds_read_b128 v[236:239], v9 offset:0\n\t" \
    "ds_read_b128 v[240:243], v9 offset:1024\n\t" \
    "v_smfmac_f32_16x16x64_bf16 v[4:7], v[220:223], v[244:251], v10\n\t" \
    "ds_read_b128 v[244:247], v9 offset:2048\n\t" \
    "ds_read_b128 v[248:251], v9 offset:3072\n\t" \
    "s_waitcnt lgkmcnt(4)\n\t" \
    "s_barrier\n\t" \
    DGA \
    "ds_read_b128 v[228:231], v8 offset:0\n\t" \
    "ds_read_b128 v[232:235], v8 offset:4096\n\t" \
    "v_smfmac_f32_16x16x64_bf16 v[0:3], v[224:227], v[148:155], v10\n\t" \
    "v_xor_b32 v8, 1024, v8\n\t" \
    "ds_read_b128 v[220:223], v8 offset:8192\n\t" \
    "v_smfmac_f32_16x16x64_bf16 v[4:7], v[224:227], v[156:163], v10\n\t" \
    "ds_read_b128 v[224:227], v8 offset:12288\n\t" \
    "buffer_store_dwordx4 v[212:215], v14, %[rs], %[ox] offen offset:0 sc1\n\t" \
    "buffer_store_dwordx4 v[216:219], v14, %[rs], %[ox] offen offset:512 sc1\n\t" \
    "s_waitcnt lgkmcnt(3)\n\t" \
    "v_smfmac_f32_16x16x64_bf16 v[0:3], v[228:231], v[20:27], v10\n\t" \
    "v_smfmac_f32_16x16x64_bf16 v[4:7], v[228:231], v[28:35], v10\n\t" \
    "ds_read_b128 v[228:231], v8 offset:0\n\t" \
    "s_waitcnt lgkmcnt(3)\n\t" \
    "v_smfmac_f32_16x16x64_bf16 v[0:3], v[232:235], v[84:91], v10\n\t" \
    "v_smfmac_f32_16x16x64_bf16 v[4:7], v[232:235], v[92:99], v10\n\t" \
    "ds_read_b128 v[232:235], v8 offset:4096\n\t" \
    "s_waitcnt lgkmcnt(3)\n\t" \
    "v_smfmac_f32_16x16x64_bf16 v[0:3], v[220:223], v[236:243], v10\n\t" \
    "v_xor_b32 v9, 49152, v9\n\t" \
    "ds_read_b128 v[236:239], v9 offset:0\n\t" \
    "v_smfmac_f32_16x16x64_bf16 v[4:7], v[220:223], v[244:251], v10\n\t" \
    "ds_read_b128 v[240:243], v9 offset:1024\n\t" \
    "s_waitcnt lgkmcnt(4)\n\t" \
    "v_smfmac_f32_16x16x64_bf16 v[0:3], v[224:227], v[164:171], v10\n\t" \
    "v_xor_b32 v8, 3072, v8\n\t" \
    "ds_read_b128 v[220:223], v8 offset:12288\n\t" \
    "v_smfmac_f32_16x16x64_bf16 v[4:7], v[224:227], v[172:179], v10\n\t" \
    "ds_read_b128 v[244:247], v9 offset:2048\n\t" \
    "s_waitcnt lgkmcnt(5)\n\t" \
    "v_smfmac_f32_16x16x64_bf16 v[0:3], v[228:231], v[36:43], v10\n\t" \
    "ds_read_b128 v[224:227], v8 offset:0\n\t" \
    "v_smfmac_f32_16x16x64_bf16 v[4:7], v[228:231], v[44:51], v10\n\t" \
    "ds_read_b128 v[248:251], v9 offset:3072\n\t" \
    "s_waitcnt lgkmcnt(6)\n\t" \
    "v_smfmac_f32_16x16x64_bf16 v[0:3], v[232:235], v[100:107], v10\n\t" \
    "ds_read_b128 v[228:231], v8 offset:8192\n\t" \
    "v_smfmac_f32_16x16x64_bf16 v[4:7], v[232:235], v[108:115], v10\n\t" \
    "ds_read_b128 v[232:235], v8 offset:4096\n\t" \
    "s_waitcnt lgkmcnt(5)\n\t" \
    "v_smfmac_f32_16x16x64_bf16 v[0:3], v[220:223], v[180:187], v10\n\t" \
    "v_smfmac_f32_16x16x64_bf16 v[4:7], v[220:223], v[188:195], v10\n\t" \
    "v_xor_b32 v8, 1024, v8\n\t" \
    "ds_read_b128 v[220:223], v8 offset:12288\n\t" \
    "s_waitcnt lgkmcnt(4)\n\t" \
    "v_smfmac_f32_16x16x64_bf16 v[0:3], v[224:227], v[52:59], v10\n\t" \
    "v_smfmac_f32_16x16x64_bf16 v[4:7], v[224:227], v[60:67], v10\n\t" \
    "ds_read_b128 v[224:227], v8 offset:0\n\t" \
    "s_waitcnt lgkmcnt(3)\n\t" \
    "v_smfmac_f32_16x16x64_bf16 v[0:3], v[228:231], v[236:243], v10\n\t" \
    "v_xor_b32 v9, 16384, v9\n\t" \
    "ds_read_b128 v[236:239], v9 offset:0\n\t" \
    "v_smfmac_f32_16x16x64_bf16 v[4:7], v[228:231], v[244:251], v10\n\t" \
    "ds_read_b128 v[240:243], v9 offset:1024\n\t" \
    "s_waitcnt lgkmcnt(4)\n\t" \
    "v_smfmac_f32_16x16x64_bf16 v[0:3], v[232:235], v[116:123], v10\n\t" \
    "ds_read_b128 v[228:231], v8 offset:4096\n\t" \
    "v_smfmac_f32_16x16x64_bf16 v[4:7], v[232:235], v[124:131], v10\n\t" \
    "ds_read_b128 v[244:247], v9 offset:2048\n\t" \
    "s_waitcnt lgkmcnt(5)\n\t" \
    "v_smfmac_f32_16x16x64_bf16 v[0:3], v[220:223], v[196:203], v10\n\t" \
    "ds_read_b128 v[232:235], v8 offset:8192\n\t" \
    "v_xor_b32 v8, 3072, v8\n\t" \
    "v_smfmac_f32_16x16x64_bf16 v[4:7], v[220:223], v[204:211], v10\n\t" \
    "ds_read_b128 v[248:251], v9 offset:3072\n\t" \
    "s_waitcnt lgkmcnt(6)\n\t" \
    "v_smfmac_f32_16x16x64_bf16 v[0:3], v[224:227], v[68:75], v10\n\t" \
    "v_smfmac_f32_16x16x64_bf16 v[4:7], v[224:227], v[76:83], v10\n\t" \
    "s_waitcnt lgkmcnt(3)\n\t" \
    "v_smfmac_f32_16x16x64_bf16 v[0:3], v[228:231], v[132:139], v10\n\t" \
    "v_smfmac_f32_16x16x64_bf16 v[4:7], v[228:231], v[140:147], v10\n\t" \
    "s_waitcnt lgkmcnt(0)\n\t" \
    "v_smfmac_f32_16x16x64_bf16 v[0:3], v[232:235], v[236:243], v10\n\t" \
    "v_xor_b32 v9, 49152, v9\n\t" \
    "v_smfmac_f32_16x16x64_bf16 v[4:7], v[232:235], v[244:251], v10\n\t" \
    "v_mov_b32 v253, %[stag]\n\t" \
    "s_nop 4\n\t" \
    "v_add_f32 v0, v0, v1\n\t" \
    "v_add_f32 v2, v2, v3\n\t" \
    "v_add_f32 v4, v4, v5\n\t" \
    "v_add_f32 v6, v6, v7\n\t" \
    "v_cvt_pk_bf16_f32 v0, v0, v4\n\t" \
    "v_cvt_pk_bf16_f32 v2, v2, v6\n\t" \
    "ds_write2_b32 v11, v0, v2 offset0:0 offset1:4\n\t" \
    "s_mov_b64 exec, %[onej]\n\t" \
    "ds_write_b32 v18, v253 offset:16\n\t" \
    "s_mov_b64 exec, -1\n\t" \
    GSA(1) \
    "s_setprio 0\n\t" \
    "buffer_load_dwordx4 v[236:239], v12, %[rs], %[qx] offen offset:0 sc1\n\t" \
    "buffer_load_dwordx4 v[240:243], v12, %[rs], %[qx] offen offset:1024 sc1\n\t" \
    "buffer_load_dwordx4 v[244:247], v12, %[rs], %[qx] offen offset:2048 sc1\n\t" \
    "buffer_load_dwordx4 v[248:251], v12, %[rs], %[qx] offen offset:3072 sc1\n\t" \
    "v_cmp_le_i32 s[72:73], 0, v16\n\t" \
    "v_cmp_le_i32 s[74:75], 1, v16\n\t" \
    "v_cmp_le_i32 s[76:77], 2, v16\n\t" \
    "v_cmp_le_i32 s[78:79], 3, v16\n\t" \
    "v_cmp_le_i32 s[80:81], 16, v16\n\t" \
    "v_cmp_le_i32 s[82:83], 17, v16\n\t" \
    "v_cmp_le_i32 s[84:85], 18, v16\n\t" \
    "v_cmp_le_i32 s[86:87], 19, v16\n\t" \
    "v_mov_b32 v254, v254\n\t" \
    "s_nop 2\n\t" \
    "v_mov_b32 v254, v254\n\t" \
    "s_nop 2\n\t" \
    "v_mov_b32 v254, v254\n\t" \
    "s_nop 2\n\t" \
    "v_mov_b32 v254, v254\n\t" \
    "s_nop 2\n\t" \
    "v_mov_b32 v254, v254\n\t" \
    "s_nop 2\n\t" \
    "v_mov_b32 v254, v254\n\t" \
    "s_nop 2\n\t" \
    "v_mov_b32 v254, v254\n\t" \
    "s_nop 2\n\t" \
    "v_mov_b32 v254, v254\n\t" \
    "s_nop 2\n\t" \
    "v_mov_b32 v254, v254\n\t" \
    "s_nop 2\n\t" \
    "v_mov_b32 v254, v254\n\t" \
    "s_nop 2\n\t" \
    "v_mov_b32 v254, v254\n\t" \
    "s_nop 2\n\t" \
    "v_mov_b32 v254, v254\n\t" \
    "s_nop 2\n\t" \
    "v_mov_b32 v254, v254\n\t" \
    "s_nop 2\n\t" \
    "v_mov_b32 v254, v254\n\t" \
    "s_nop 2\n\t" \
    "v_mov_b32 v254, v254\n\t" \
    "s_nop 2\n\t" \
    "v_mov_b32 v254, v254\n\t" \
    "s_nop 2\n\t" \
    "v_mov_b32 v254, v254\n\t" \
    "s_nop 2\n\t" \
    "v_mov_b32 v254, v254\n\t" \
    "s_nop 2\n\t" \
    "v_mov_b32 v254, v254\n\t" \
    "s_nop 2\n\t" \
    "v_mov_b32 v254, v254\n\t" \
    "s_nop 2\n\t" \
    "LW_%=:\n\t" \
    "ds_read_b32 v0, v18 offset:16\n\t" \
    "s_sub_u32 %[lim], %[lim], " STR(DEC1) "\n\t" \
    "s_cmp_gt_i32 %[lim], 0\n\t" \
    "v_mov_b32 v254, v254\n\t" \
    "s_nop 2\n\t" \
    "v_mov_b32 v254, v254\n\t" \
    "s_nop 2\n\t" \
    "v_mov_b32 v254, v254\n\t" \
    "s_nop 2\n\t" \
    "v_mov_b32 v254, v254\n\t" \
    "s_nop 2\n\t" \
    "v_mov_b32 v254, v254\n\t" \
    "s_nop 2\n\t" \
    "s_waitcnt lgkmcnt(0)\n\t" \
    "s_cbranch_scc0 CW_%=\n\t" \
    "v_cmp_ne_u32 vcc, v0, v253\n\t" \
    "s_cbranch_vccnz LW_%=\n\t" \
    "CW_%=:\n\t" \
    "ds_read_b128 v[212:215], v12 offset:24576\n\t" \
    "ds_read_b128 v[216:219], v12 offset:28672\n\t" \
    "ds_read_b128 v[220:223], v15 offset:0\n\t" \
    "ds_read_b128 v[224:227], v12 offset:25600\n\t" \
    "ds_read_b128 v[228:231], v12 offset:29696\n\t" \
    "v_add_u32 v253, %[se], v12\n\t" \
    "v_mov_b32 v254, v254\n\t" \
    "s_nop 2\n\t" \
    "v_mov_b32 v254, v254\n\t" \
    "s_nop 2\n\t" \
    "v_mov_b32 v254, v254\n\t" \
    "s_nop 2\n\t" \
    "v_mov_b32 v254, v254\n\t" \
    "s_nop 2\n\t" \
    "s_waitcnt vmcnt(0)\n\t" \
    "s_waitcnt lgkmcnt(4)\n\t" \
    "v_mfma_f32_16x16x32_bf16 v[0:3], v[212:215], v[236:239], 0\n\t" \
    "ds_read_b128 v[212:215], v15 offset:512\n\t" \
    "s_waitcnt lgkmcnt(4)\n\t" \
    "v_mfma_f32_16x16x32_bf16 v[4:7], v[216:219], v[236:239], 0\n\t" \
    "ds_read_b128 v[216:219], v12 offset:26624\n\t" \
    "s_waitcnt lgkmcnt(4)\n\t" \
    "v_mfma_f32_16x16x32_bf16 v[232:235], v[220:223], v[236:239], 0\n\t" \
    "ds_read_b128 v[220:223], v12 offset:30720\n\t" \
    "s_waitcnt lgkmcnt(4)\n\t" \
    "v_mfma_f32_16x16x32_bf16 v[0:3], v[224:227], v[240:243], v[0:3]\n\t" \
    "ds_read_b128 v[224:227], v15 offset:1024\n\t" \
    "s_waitcnt lgkmcnt(4)\n\t" \
    "v_mfma_f32_16x16x32_bf16 v[4:7], v[228:231], v[240:243], v[4:7]\n\t" \
    "ds_read_b128 v[228:231], v12 offset:27648\n\t" \
    "s_waitcnt lgkmcnt(4)\n\t" \
    "v_mfma_f32_16x16x32_bf16 v[232:235], v[212:215], v[240:243], v[232:235]\n\t" \
    "ds_read_b128 v[212:215], v12 offset:31744\n\t" \
    "s_waitcnt lgkmcnt(4)\n\t" \
    "v_mfma_f32_16x16x32_bf16 v[0:3], v[216:219], v[244:247], v[0:3]\n\t" \
    "ds_read_b128 v[216:219], v15 offset:1536\n\t" \
    "s_waitcnt lgkmcnt(4)\n\t" \
    "v_mfma_f32_16x16x32_bf16 v[4:7], v[220:223], v[244:247], v[4:7]\n\t" \
    "s_waitcnt lgkmcnt(3)\n\t" \
    "v_mfma_f32_16x16x32_bf16 v[232:235], v[224:227], v[244:247], v[232:235]\n\t" \
    "s_waitcnt lgkmcnt(2)\n\t" \
    "v_mfma_f32_16x16x32_bf16 v[0:3], v[228:231], v[248:251], v[0:3]\n\t" \
    "s_waitcnt lgkmcnt(1)\n\t" \
    "v_mfma_f32_16x16x32_bf16 v[4:7], v[212:215], v[248:251], v[4:7]\n\t" \
    "s_waitcnt lgkmcnt(0)\n\t" \
    "v_mfma_f32_16x16x32_bf16 v[232:235], v[216:219], v[248:251], v[232:235]\n\t" \
    "s_barrier\n\t" \
    GSA(4) \
    "ds_read_b128 v[236:239], v253 offset:8192\n\t" \
    "ds_read_b128 v[240:243], v253 offset:9216\n\t" \
    "ds_read_b128 v[244:247], v253 offset:10240\n\t" \
    "ds_read_b128 v[248:251], v253 offset:11264\n\t" \
    "ds_read2st64_b32 v[228:229], v17 offset0:0 offset1:1\n\t" \
    "ds_read2st64_b32 v[230:231], v17 offset0:2 offset1:3\n\t" \
    "s_mov_b64 exec, %[fm]\n\t" \
    "v_mov_b32 v220, %[st]\n\t" \
    "v_mov_b32 v221, %[tag]\n\t" \
    "v_mov_b32 v222, 0\n\t" \
    "buffer_store_dwordx2 v[220:221], v222, %[rs], %[po] offen sc1\n\t" \
    "s_mov_b64 exec, -1\n\t" \
    "v_mov_b32 v254, 0xff800000\n\t" \
    "v_cvt_pk_bf16_f32 v0, 0, v0\n\t" \
    "v_cvt_pk_bf16_f32 v1, 0, v1\n\t" \
    "v_cvt_pk_bf16_f32 v2, 0, v2\n\t" \
    "v_cvt_pk_bf16_f32 v3, 0, v3\n\t" \
    "v_cvt_pk_bf16_f32 v4, 0, v4\n\t" \
    "v_cvt_pk_bf16_f32 v5, 0, v5\n\t" \
    "v_cvt_pk_bf16_f32 v6, 0, v6\n\t" \
    "v_cvt_pk_bf16_f32 v7, 0, v7\n\t" \
    "v_cvt_pk_bf16_f32 v232, 0, v232\n\t" \
    "v_cvt_pk_bf16_f32 v233, 0, v233\n\t" \
    "v_mul_f32 v0, 0x3db504f3, v0\n\t" \
    "v_mul_f32 v1, 0x3db504f3, v1\n\t" \
    "v_mul_f32 v2, 0x3db504f3, v2\n\t" \
    "v_mul_f32 v3, 0x3db504f3, v3\n\t" \
    "v_mul_f32 v4, 0x3db504f3, v4\n\t" \
    "v_mul_f32 v5, 0x3db504f3, v5\n\t" \
    "v_mul_f32 v6, 0x3db504f3, v6\n\t" \
    "v_mul_f32 v7, 0x3db504f3, v7\n\t" \
    "v_mul_f32 v232, 0x3db504f3, v232\n\t" \
    "v_mul_f32 v233, 0x3db504f3, v233\n\t" \
    "v_cvt_pk_bf16_f32 v0, 0, v0\n\t" \
    "v_cvt_pk_bf16_f32 v1, 0, v1\n\t" \
    "v_cvt_pk_bf16_f32 v2, 0, v2\n\t" \
    "v_cvt_pk_bf16_f32 v3, 0, v3\n\t" \
    "v_cvt_pk_bf16_f32 v4, 0, v4\n\t" \
    "v_cvt_pk_bf16_f32 v5, 0, v5\n\t" \
    "v_cvt_pk_bf16_f32 v6, 0, v6\n\t" \
    "v_cvt_pk_bf16_f32 v7, 0, v7\n\t" \
    "v_cvt_pk_bf16_f32 v232, 0, v232\n\t" \
    "v_cvt_pk_bf16_f32 v233, 0, v233\n\t" \
    "v_cndmask_b32 v0, v254, v0, s[72:73]\n\t" \
    "v_cndmask_b32 v1, v254, v1, s[74:75]\n\t" \
    "v_cndmask_b32 v2, v254, v2, s[76:77]\n\t" \
    "v_cndmask_b32 v3, v254, v3, s[78:79]\n\t" \
    "v_cndmask_b32 v4, v254, v4, s[80:81]\n\t" \
    "v_cndmask_b32 v5, v254, v5, s[82:83]\n\t" \
    "v_cndmask_b32 v6, v254, v6, s[84:85]\n\t" \
    "v_cndmask_b32 v7, v254, v7, s[86:87]\n\t" \
    "v_max3_f32 v252, v0, v1, v2\n\t" \
    "v_max3_f32 v255, v3, v4, v5\n\t" \
    "v_max3_f32 v254, v6, v7, v232\n\t" \
    "v_max3_f32 v252, v252, v255, v254\n\t" \
    "v_max_f32 v252, v252, v233\n\t" \
    "v_mov_b32 v255, v252\n\t" \
    "v_mov_b32 v254, v252\n\t" \
    "s_nop 1\n\t" \
    "v_permlane16_swap_b32 v255, v254\n\t" \
    "v_max_f32 v252, v255, v254\n\t" \
    "v_max_f32 v255, v255, v254\n\t" \
    "s_nop 1\n\t" \
    "v_permlane32_swap_b32 v252, v255\n\t" \
    "v_max_f32 v252, v252, v255\n\t" \
    "v_mul_f32 v252, 0xbfb8aa3b, v252\n\t" \
    "v_fma_f32 v0, v0, %[l2e], v252\n\t" \
    "v_fma_f32 v1, v1, %[l2e], v252\n\t" \
    "v_fma_f32 v2, v2, %[l2e], v252\n\t" \
    "v_fma_f32 v3, v3, %[l2e], v252\n\t" \
    "v_fma_f32 v4, v4, %[l2e], v252\n\t" \
    "v_fma_f32 v5, v5, %[l2e], v252\n\t" \
    "v_fma_f32 v6, v6, %[l2e], v252\n\t" \
    "v_fma_f32 v7, v7, %[l2e], v252\n\t" \
    "v_fma_f32 v232, v232, %[l2e], v252\n\t" \
    "v_fma_f32 v233, v233, %[l2e], v252\n\t" \
    "v_exp_f32 v0, v0\n\t" \
    "v_exp_f32 v1, v1\n\t" \
    "v_exp_f32 v2, v2\n\t" \
    "v_exp_f32 v3, v3\n\t" \
    "v_exp_f32 v4, v4\n\t" \
    "v_exp_f32 v5, v5\n\t" \
    "v_exp_f32 v6, v6\n\t" \
    "v_exp_f32 v7, v7\n\t" \
    "v_exp_f32 v232, v232\n\t" \
    "v_exp_f32 v233, v233\n\t" \
    "v_add_f32 v252, v0, v1\n\t" \
    "v_add_f32 v235, v2, v3\n\t" \
    "v_add_f32 v254, v4, v5\n\t" \
    "v_add_f32 v255, v6, v7\n\t" \
    "v_add_f32 v234, v232, v233\n\t" \
    "v_add_f32 v252, v252, v235\n\t" \
    "v_add_f32 v254, v254, v255\n\t" \
    "v_add_f32 v252, v252, v254\n\t" \
    "v_add_f32 v252, v252, v234\n\t" \
    "v_mov_b32 v255, v252\n\t" \
    "v_mov_b32 v254, v252\n\t" \
    "s_nop 1\n\t" \
    "v_permlane16_swap_b32 v255, v254\n\t" \
    "v_add_f32 v252, v255, v254\n\t" \
    "v_add_f32 v255, v255, v254\n\t" \
    "s_nop 1\n\t" \
    "v_permlane32_swap_b32 v252, v255\n\t" \
    "v_add_f32 v252, v252, v255\n\t" \
    "v_rcp_f32 v252, v252\n\t" \
    "s_nop 0\n\t" \
    "v_mul_f32 v0, v0, v252\n\t" \
    "v_mul_f32 v1, v1, v252\n\t" \
    "v_mul_f32 v2, v2, v252\n\t" \
    "v_mul_f32 v3, v3, v252\n\t" \
    "v_mul_f32 v4, v4, v252\n\t" \
    "v_mul_f32 v5, v5, v252\n\t" \
    "v_mul_f32 v6, v6, v252\n\t" \
    "v_mul_f32 v7, v7, v252\n\t" \
    "v_mul_f32 v232, v232, v252\n\t" \
    "v_mul_f32 v233, v233, v252\n\t" \
    "v_cvt_pk_bf16_f32 v0, v0, v1\n\t" \
    "v_cvt_pk_bf16_f32 v1, v2, v3\n\t" \
    "v_cvt_pk_bf16_f32 v2, v4, v5\n\t" \
    "v_cvt_pk_bf16_f32 v3, v6, v7\n\t" \
    "v_cvt_pk_bf16_f32 v4, v232, v233\n\t" \
    "v_mov_b32 v5, 0\n\t" \
    "v_mov_b32 v6, 0\n\t" \
    "v_cvt_pk_bf16_f32 v7, v232, v233\n\t" \
    "s_waitcnt lgkmcnt(0)\n\t" \
    "v_mfma_f32_16x16x32_bf16 v[236:239], v[236:239], v[0:3], 0\n\t" \
    "v_mfma_f32_16x16x32_bf16 v[240:243], v[240:243], v[0:3], 0\n\t" \
    "v_mfma_f32_16x16x32_bf16 v[244:247], v[244:247], v[0:3], 0\n\t" \
    "v_mfma_f32_16x16x32_bf16 v[248:251], v[248:251], v[0:3], 0\n\t" \
    "v_mfma_f32_16x16x16_bf16 v[236:239], v[228:229], v[4:5], v[236:239]\n\t" \
    "v_mfma_f32_16x16x16_bf16 v[240:243], v[228:229], v[6:7], v[240:243]\n\t" \
    "v_mfma_f32_16x16x16_bf16 v[244:247], v[230:231], v[4:5], v[244:247]\n\t" \
    "v_mfma_f32_16x16x16_bf16 v[248:251], v[230:231], v[6:7], v[248:251]\n\t" \
    "s_nop 4\n\t" \
    "v_cvt_pk_bf16_f32 v212, v236, v237\n\t" \
    "v_cvt_pk_bf16_f32 v213, v240, v241\n\t" \
    "v_cvt_pk_bf16_f32 v214, v238, v239\n\t" \
    "v_cvt_pk_bf16_f32 v215, v242, v243\n\t" \
    "v_permlane16_swap_b32 v212, v213\n\t" \
    "s_nop 0\n\t" \
    "v_permlane16_swap_b32 v214, v215\n\t" \
    "v_cvt_pk_bf16_f32 v216, v244, v245\n\t" \
    "v_cvt_pk_bf16_f32 v217, v248, v249\n\t" \
    "v_cvt_pk_bf16_f32 v218, v246, v247\n\t" \
    "v_cvt_pk_bf16_f32 v219, v250, v251\n\t" \
    "v_permlane16_swap_b32 v216, v217\n\t" \
    "s_nop 0\n\t" \
    "v_permlane16_swap_b32 v218, v219\n\t" \
    "s_setprio 1\n\t" \
    GSA(2) \
    GSW \
    "s_nop 1\n\t" \
    : [lim] "+s"(lim), "+{v[212:219]}"(oxr) DGO GSO(0) GSO(1) GSO(2) GSO(3) GSO(4) \
    : "{v8}"(am_a), "{v9}"(t0_a), "{v10}"(idx), "{v11}"(iw_a), "{v12}"(ab_a), "{v14}"(xo_v), "{v15}"(kr_a), "{v16}"(dq), "{v17}"(vl_a), "{v18}"(fr_a), [stag] "s"(0xffffff81u + s), [onej] "s"(onej), [rs] "s"(rs), [m0] "s"(sbm + 16384u), [m1] "s"(sbm + 20480u), [st] "s"(s), [tag] "s"(P.tag), [po] "s"(FLG_OFF + F_PRG(4, h) * 128), [fm] "s"(fmask), [qx] "s"(qxo), [qd] "s"(lbase + LA_VM), [qh] "s"((j & 1) * 2048), [ox] "s"(oxo), [se] "s"((j & 1) * 4096), [l2e] "s"(0x3fb8aa3b), "{v[20:27]}"(wr[0][0]), "{v[28:35]}"(wr[0][1]), "{v[36:43]}"(wr[1][0]), "{v[44:51]}"(wr[1][1]), "{v[52:59]}"(wr[2][0]), "{v[60:67]}"(wr[2][1]), "{v[68:75]}"(wr[3][0]), "{v[76:83]}"(wr[3][1]), "{v[84:91]}"(wr[4][0]), "{v[92:99]}"(wr[4][1]), "{v[100:107]}"(wr[5][0]), "{v[108:115]}"(wr[5][1]), "{v[116:123]}"(wr[6][0]), "{v[124:131]}"(wr[6][1]), "{v[132:139]}"(wr[7][0]), "{v[140:147]}"(wr[7][1]), "{v[148:155]}"(wr[8][0]), "{v[156:163]}"(wr[8][1]), "{v[164:171]}"(wr[9][0]), "{v[172:179]}"(wr[9][1]), "{v[180:187]}"(wr[10][0]), "{v[188:195]}"(wr[10][1]), "{v[196:203]}"(wr[11][0]), "{v[204:211]}"(wr[11][1]) \
    : "memory", "scc", "vcc", "s90", "s91", "s72", "s73", "s74", "s75", "s76", "s77", "s78", "s79", "s80", "s81", "s82", "s83", "s84", "s85", "s86", "s87", "v0", "v1", "v2", "v3", "v4", "v5", "v6", "v7", "v220", "v221", "v222", "v223", "v224", "v225", "v226", "v227", "v228", "v229", "v230", "v231", "v232", "v233", "v234", "v235", "v236", "v237", "v238", "v239", "v240", "v241", "v242", "v243", "v244", "v245", "v246", "v247", "v248", "v249", "v250", "v251", "v252", "v253", "v254", "v255")

#define B_POLL() \
  asm volatile( \
    "TWB_%=:\n\t" \
    "s_memrealtime s[88:89]\n\t" \
    "s_memtime s[90:91]\n\t" \
    "s_waitcnt lgkmcnt(0)\n\t" \
    "s_lshl_b32 s88, s88, 2\n\t" \
    "s_sub_u32 s89, %[tgt], s88\n\t" \
    "s_cmp_gt_i32 s89, 16\n\t" \
    "s_cbranch_scc1 TWB_%=\n\t" \
    "s_mul_i32 s89, s89, 11\n\t" \
    "s_ashr_i32 s89, s89, 1\n\t" \
    "s_add_u32 s89, s89, s90\n\t" \
    "TCB_%=:\n\t" \
    "s_memtime s[92:93]\n\t" \
    "s_waitcnt lgkmcnt(0)\n\t" \
    "s_sub_u32 s93, s89, s92\n\t" \
    "s_cmp_gt_i32 s93, 0\n\t" \
    "s_cbranch_scc1 TCB_%=\n\t" \
    "buffer_load_dwordx4 v[220:223], %[l16], %[rs], %[m0] offen offset:0 sc1\n\t" \
    "buffer_load_dwordx4 v[224:227], %[l16], %[rs], %[m0] offen offset:1024 sc1\n\t" \
    "buffer_load_dwordx4 v[228:231], %[l16], %[rs], %[m1] offen offset:0 sc1\n\t" \
    "buffer_load_dwordx4 v[232:235], %[l16], %[rs], %[m1] offen offset:1024 sc1\n\t" \
    "v_mov_b32 v252, v252\n\t" \
    "s_nop 2\n\t" \
    "v_mov_b32 v252, v252\n\t" \
    "s_nop 2\n\t" \
    "v_mov_b32 v252, v252\n\t" \
    "s_nop 2\n\t" \
    "v_mov_b32 v252, v252\n\t" \
    "s_nop 2\n\t" \
    "v_mov_b32 v252, v252\n\t" \
    "s_nop 2\n\t" \
    "v_mov_b32 v252, v252\n\t" \
    "s_nop 2\n\t" \
    "v_mov_b32 v252, v252\n\t" \
    "s_nop 2\n\t" \
    "v_mov_b32 v252, v252\n\t" \
    "s_nop 2\n\t" \
    "v_mov_b32 v252, v252\n\t" \
    "s_nop 2\n\t" \
    "v_mov_b32 v252, v252\n\t" \
    "s_nop 2\n\t" \
    "v_mov_b32 v252, v252\n\t" \
    "s_nop 2\n\t" \
    "v_mov_b32 v252, v252\n\t" \
    "s_nop 2\n\t" \
    "v_mov_b32 v252, v252\n\t" \
    "s_nop 2\n\t" \
    "v_mov_b32 v252, v252\n\t" \
    "s_nop 2\n\t" \
    "v_mov_b32 v252, v252\n\t" \
    "s_nop 2\n\t" \
    "v_mov_b32 v252, v252\n\t" \
    "s_nop 2\n\t" \
    "v_mov_b32 v252, v252\n\t" \
    "s_nop 2\n\t" \
    "v_mov_b32 v252, v252\n\t" \
    "s_nop 2\n\t" \
    "v_mov_b32 v252, v252\n\t" \
    "s_nop 2\n\t" \
    "v_mov_b32 v252, v252\n\t" \
    "s_nop 2\n\t" \
    "v_mov_b32 v252, v252\n\t" \
    "s_nop 2\n\t" \
    "v_mov_b32 v252, v252\n\t" \
    "s_nop 2\n\t" \
    "v_mov_b32 v252, v252\n\t" \
    "s_nop 2\n\t" \
    "v_mov_b32 v252, v252\n\t" \
    "s_nop 2\n\t" \
    "PM_%=:\n\t" \
    "s_waitcnt vmcnt(3)\n\t" \
    "ds_write_b128 %[la], v[220:223] offset:0\n\t" \
    "v_max3_u32 v252, v220, v220, v221\n\t" \
    "v_max3_u32 v252, v252, v222, v223\n\t" \
    "s_waitcnt vmcnt(2)\n\t" \
    "ds_write_b128 %[la], v[224:227] offset:1024\n\t" \
    "v_max3_u32 v252, v252, v224, v225\n\t" \
    "v_max3_u32 v252, v252, v226, v227\n\t" \
    "s_waitcnt vmcnt(1)\n\t" \
    "ds_write_b128 %[la], v[228:231] offset:8192\n\t" \
    "v_max3_u32 v252, v252, v228, v229\n\t" \
    "v_max3_u32 v252, v252, v230, v231\n\t" \
    "s_waitcnt vmcnt(0)\n\t" \
    "ds_write_b128 %[la], v[232:235] offset:9216\n\t" \
    "v_max3_u32 v252, v252, v232, v233\n\t" \
    "v_max3_u32 v252, v252, v234, v235\n\t" \
    "v_cmp_eq_u32 vcc, -1, v252\n\t" \
    "s_cbranch_vccz PD_%=\n\t" \
    "s_sub_u32 %[lim], %[lim], 1\n\t" \
    "s_cmp_gt_i32 %[lim], 0\n\t" \
    "s_cbranch_scc0 PD_%=\n\t" \
    "s_sleep " STR(CADM) "\n\t" \
    "buffer_load_dwordx4 v[220:223], %[l16], %[rs], %[m0] offen offset:0 sc1\n\t" \
    "buffer_load_dwordx4 v[224:227], %[l16], %[rs], %[m0] offen offset:1024 sc1\n\t" \
    "buffer_load_dwordx4 v[228:231], %[l16], %[rs], %[m1] offen offset:0 sc1\n\t" \
    "buffer_load_dwordx4 v[232:235], %[l16], %[rs], %[m1] offen offset:1024 sc1\n\t" \
    "s_branch PM_%=\n\t" \
    "PD_%=:\n\t" \
    GSA(3) \
    "s_waitcnt lgkmcnt(0)\n\t" \
    "s_barrier\n\t" \
    : [lim] "+s"(lim) GSO(3) \
    : [l16] "v"(l16), [la] "v"(la), [rs] "s"(rs), [m0] "s"(mo), [m1] "s"(mo + 16384u), [tgt] "s"(tgt) \
    : "memory", "vcc", "scc", "v252", "s88", "s89", "s90", "s91", "s92", "s93", "v220", "v221", "v222", "v223", "v224", "v225", "v226", "v227", "v228", "v229", "v230", "v231", "v232", "v233", "v234", "v235")

#define T_WAIT() \
  asm volatile( \
    "TWC_%=:\n\t" \
    "s_memrealtime s[88:89]\n\t" \
    "s_memtime s[90:91]\n\t" \
    "s_waitcnt lgkmcnt(0)\n\t" \
    "s_lshl_b32 s88, s88, 2\n\t" \
    "s_sub_u32 s89, %[tgt], s88\n\t" \
    "s_cmp_gt_i32 s89, 16\n\t" \
    "s_cbranch_scc1 TWC_%=\n\t" \
    "s_mul_i32 s89, s89, 11\n\t" \
    "s_ashr_i32 s89, s89, 1\n\t" \
    "s_add_u32 s89, s89, s90\n\t" \
    "TCC_%=:\n\t" \
    "s_memtime s[92:93]\n\t" \
    "s_waitcnt lgkmcnt(0)\n\t" \
    "s_sub_u32 s93, s89, s92\n\t" \
    "s_cmp_gt_i32 s93, 0\n\t" \
    "s_cbranch_scc1 TCC_%=\n\t" \
    :  \
    : [tgt] "s"(tgt) \
    : "s88", "s89", "s90", "s91", "s92", "s93", "memory", "scc")

#define V_PROJ() \
  asm volatile( \
    "v_mov_b32 v0, 0\n\t" \
    "v_mov_b32 v1, 0\n\t" \
    "v_mov_b32 v2, 0\n\t" \
    "v_mov_b32 v3, 0\n\t" \
    "v_mov_b32 v4, 0\n\t" \
    "v_mov_b32 v5, 0\n\t" \
    "v_mov_b32 v6, 0\n\t" \
    "v_mov_b32 v7, 0\n\t" \
    "s_barrier\n\t" \
    "ds_read_b128 v[220:223], %[am] offset:0\n\t" \
    "ds_read_b128 v[224:227], %[am] offset:1024\n\t" \
    "ds_read_b128 v[228:231], %[am] offset:2048\n\t" \
    "ds_read_b128 v[232:235], %[am] offset:3072\n\t" \
    "ds_read_b128 v[236:239], %[am] offset:4096\n\t" \
    "ds_read_b128 v[240:243], %[am] offset:5120\n\t" \
    "ds_read_b128 v[244:247], %[am] offset:6144\n\t" \
    "ds_read_b128 v[248:251], %[am] offset:7168\n\t" \
    "s_waitcnt lgkmcnt(7)\n\t" \
    "v_smfmac_f32_16x16x64_bf16 v[0:3], v[220:223], v[28:35], %[idx]\n\t" \
    "ds_read_b128 v[220:223], %[am] offset:8192\n\t" \
    "s_waitcnt lgkmcnt(7)\n\t" \
    "v_smfmac_f32_16x16x64_bf16 v[4:7], v[224:227], v[36:43], %[idx]\n\t" \
    "ds_read_b128 v[224:227], %[am] offset:9216\n\t" \
    "s_waitcnt lgkmcnt(7)\n\t" \
    "v_smfmac_f32_16x16x64_bf16 v[0:3], v[228:231], v[44:51], %[idx]\n\t" \
    "ds_read_b128 v[228:231], %[am] offset:10240\n\t" \
    "s_waitcnt lgkmcnt(7)\n\t" \
    "v_smfmac_f32_16x16x64_bf16 v[4:7], v[232:235], v[52:59], %[idx]\n\t" \
    "ds_read_b128 v[232:235], %[am] offset:11264\n\t" \
    "s_waitcnt lgkmcnt(7)\n\t" \
    "v_smfmac_f32_16x16x64_bf16 v[0:3], v[236:239], v[60:67], %[idx]\n\t" \
    "ds_read_b128 v[236:239], %[am] offset:12288\n\t" \
    "s_waitcnt lgkmcnt(7)\n\t" \
    "v_smfmac_f32_16x16x64_bf16 v[4:7], v[240:243], v[68:75], %[idx]\n\t" \
    "ds_read_b128 v[240:243], %[am] offset:13312\n\t" \
    "s_waitcnt lgkmcnt(7)\n\t" \
    "v_smfmac_f32_16x16x64_bf16 v[0:3], v[244:247], v[76:83], %[idx]\n\t" \
    "ds_read_b128 v[244:247], %[am] offset:14336\n\t" \
    "s_waitcnt lgkmcnt(7)\n\t" \
    "v_smfmac_f32_16x16x64_bf16 v[4:7], v[248:251], v[84:91], %[idx]\n\t" \
    "ds_read_b128 v[248:251], %[am] offset:15360\n\t" \
    "s_waitcnt lgkmcnt(7)\n\t" \
    "v_smfmac_f32_16x16x64_bf16 v[0:3], v[220:223], v[92:99], %[idx]\n\t" \
    "s_waitcnt lgkmcnt(6)\n\t" \
    "v_smfmac_f32_16x16x64_bf16 v[4:7], v[224:227], v[100:107], %[idx]\n\t" \
    "s_waitcnt lgkmcnt(5)\n\t" \
    "v_smfmac_f32_16x16x64_bf16 v[0:3], v[228:231], v[108:115], %[idx]\n\t" \
    "s_waitcnt lgkmcnt(4)\n\t" \
    "v_smfmac_f32_16x16x64_bf16 v[4:7], v[232:235], v[116:123], %[idx]\n\t" \
    "s_waitcnt lgkmcnt(3)\n\t" \
    "v_smfmac_f32_16x16x64_bf16 v[0:3], v[236:239], v[124:131], %[idx]\n\t" \
    "s_waitcnt lgkmcnt(2)\n\t" \
    "v_smfmac_f32_16x16x64_bf16 v[4:7], v[240:243], v[132:139], %[idx]\n\t" \
    "s_waitcnt lgkmcnt(1)\n\t" \
    "v_smfmac_f32_16x16x64_bf16 v[0:3], v[244:247], v[140:147], %[idx]\n\t" \
    "s_waitcnt lgkmcnt(0)\n\t" \
    "v_smfmac_f32_16x16x64_bf16 v[4:7], v[248:251], v[148:155], %[idx]\n\t" \
    "s_nop 5\n\t" \
    "v_add_f32 v0, v0, v1\n\t" \
    "v_add_f32 v2, v2, v3\n\t" \
    "v_add_f32 v4, v4, v5\n\t" \
    "v_add_f32 v6, v6, v7\n\t" \
    "v_add_f32 v0, v0, v4\n\t" \
    "v_add_f32 v2, v2, v6\n\t" \
    "v_cvt_pk_bf16_f32 v0, v0, v2\n\t" \
    "v_min_u32 v0, -2, v0\n\t" \
    "s_nop 1\n\t" \
    "v_mov_b32_dpp v1, v0 quad_perm:[1,1,1,1] row_mask:0xf bank_mask:0xf\n\t" \
    "v_mov_b32_dpp v2, v0 quad_perm:[2,2,2,2] row_mask:0xf bank_mask:0xf\n\t" \
    "v_mov_b32_dpp v3, v0 quad_perm:[3,3,3,3] row_mask:0xf bank_mask:0xf\n\t" \
    "s_mov_b64 exec, %[qm]\n\t" \
    "buffer_store_dwordx4 v[0:3], %[l4], %[rs], %[vo] offen sc1\n\t" \
    "s_mov_b64 exec, -1\n\t" \
    "s_nop 1\n\t" \
    :  \
    : [am] "v"(am_a), [idx] "v"(idx), [l4] "v"(ln * 4), [rs] "s"(rs), [vo] "s"(sb + vo), [qm] "s"(0x1111111111111111ull), "{v[28:35]}"(wv[0]), "{v[36:43]}"(wv[1]), "{v[44:51]}"(wv[2]), "{v[52:59]}"(wv[3]), "{v[60:67]}"(wv[4]), "{v[68:75]}"(wv[5]), "{v[76:83]}"(wv[6]), "{v[84:91]}"(wv[7]), "{v[92:99]}"(wv[8]), "{v[100:107]}"(wv[9]), "{v[108:115]}"(wv[10]), "{v[116:123]}"(wv[11]), "{v[124:131]}"(wv[12]), "{v[132:139]}"(wv[13]), "{v[140:147]}"(wv[14]), "{v[148:155]}"(wv[15]) \
    : "memory", "v0", "v1", "v2", "v3", "v4", "v5", "v6", "v7", "v220", "v221", "v222", "v223", "v224", "v225", "v226", "v227", "v228", "v229", "v230", "v231", "v232", "v233", "v234", "v235", "v236", "v237", "v238", "v239", "v240", "v241", "v242", "v243", "v244", "v245", "v246", "v247", "v248", "v249", "v250", "v251")

#define WO_V() \
  asm volatile( \
    "v_mov_b32 v0, 0\n\t" \
    "v_mov_b32 v1, 0\n\t" \
    "v_mov_b32 v2, 0\n\t" \
    "v_mov_b32 v3, 0\n\t" \
    "v_mov_b32 v4, 0\n\t" \
    "v_mov_b32 v5, 0\n\t" \
    "v_mov_b32 v6, 0\n\t" \
    "v_mov_b32 v7, 0\n\t" \
    "v_mov_b32 v8, 0\n\t" \
    "v_mov_b32 v9, 0\n\t" \
    "v_mov_b32 v10, 0\n\t" \
    "v_mov_b32 v11, 0\n\t" \
    "v_mov_b32 v12, 0\n\t" \
    "v_mov_b32 v13, 0\n\t" \
    "v_mov_b32 v14, 0\n\t" \
    "v_mov_b32 v15, 0\n\t" \
    "v_mov_b32 v252, 0\n\t" \
    "s_barrier\n\t" \
    GSA(4) \
    DGC \
    "buffer_load_dwordx4 v[220:223], %[l16], %[rs], %[s0] offen sc1\n\t" \
    "buffer_load_dwordx4 v[224:227], %[l16], %[rs], %[s1] offen sc1\n\t" \
    "A_%=:\n\t" \
    "s_waitcnt vmcnt(1)\n\t" \
    "v_smfmac_f32_16x16x64_bf16 v[0:3], v[220:223], v[156:163], %[idx]\n\t" \
    "v_max3_u32 v252, v252, v220, v221\n\t" \
    "v_smfmac_f32_16x16x64_bf16 v[4:7], v[220:223], v[164:171], %[idx]\n\t" \
    "v_max3_u32 v252, v252, v222, v223\n\t" \
    "v_smfmac_f32_16x16x64_bf16 v[8:11], v[220:223], v[172:179], %[idx]\n\t" \
    "v_smfmac_f32_16x16x64_bf16 v[12:15], v[220:223], v[180:187], %[idx]\n\t" \
    "s_waitcnt vmcnt(0)\n\t" \
    "v_smfmac_f32_16x16x64_bf16 v[0:3], v[224:227], v[188:195], %[idx]\n\t" \
    "v_max3_u32 v252, v252, v224, v225\n\t" \
    "v_smfmac_f32_16x16x64_bf16 v[4:7], v[224:227], v[196:203], %[idx]\n\t" \
    "v_max3_u32 v252, v252, v226, v227\n\t" \
    "v_smfmac_f32_16x16x64_bf16 v[8:11], v[224:227], v[204:211], %[idx]\n\t" \
    "v_smfmac_f32_16x16x64_bf16 v[12:15], v[224:227], v[212:219], %[idx]\n\t" \
    "v_cmp_eq_u32 vcc, -1, v252\n\t" \
    "s_cbranch_vccnz SL_%=\n\t" \
    "D_%=:\n\t" \
    GSA(1) \
    "s_nop 2\n\t" \
    "v_add_f32 v0, v0, v1\n\t" \
    "v_add_f32 v2, v2, v3\n\t" \
    "v_add_f32 v4, v4, v5\n\t" \
    "v_add_f32 v6, v6, v7\n\t" \
    "v_add_f32 v8, v8, v9\n\t" \
    "v_add_f32 v10, v10, v11\n\t" \
    "v_add_f32 v12, v12, v13\n\t" \
    "v_add_f32 v14, v14, v15\n\t" \
    "v_permlane32_swap_b32 v0, v4\n\t" \
    "v_permlane32_swap_b32 v2, v6\n\t" \
    "v_permlane32_swap_b32 v8, v12\n\t" \
    "v_permlane32_swap_b32 v10, v14\n\t" \
    "v_add_f32 v0, v0, v12\n\t" \
    "v_add_f32 v2, v2, v14\n\t" \
    "ds_write2_b32 %[rw], v0, v2 offset0:0 offset1:32\n\t" \
    "s_waitcnt lgkmcnt(0)\n\t" \
    "s_barrier\n\t" \
    "s_cmp_eq_u32 %[red], 0\n\t" \
    "s_cbranch_scc0 E_%=\n\t" \
    "ds_read2st64_b64 v[0:3], %[rr] offset0:0 offset1:1\n\t" \
    "ds_read2st64_b64 v[4:7], %[rr] offset0:2 offset1:3\n\t" \
    "ds_read2st64_b64 v[8:11], %[rr] offset0:4 offset1:5\n\t" \
    "ds_read2st64_b64 v[12:15], %[rr] offset0:6 offset1:7\n\t" \
    "s_waitcnt lgkmcnt(3)\n\t" \
    "v_pk_add_f32 v[0:1], v[0:1], v[2:3]\n\t" \
    "s_waitcnt lgkmcnt(2)\n\t" \
    "v_pk_add_f32 v[4:5], v[4:5], v[6:7]\n\t" \
    "s_waitcnt lgkmcnt(1)\n\t" \
    "v_pk_add_f32 v[8:9], v[8:9], v[10:11]\n\t" \
    "v_pk_add_f32 v[0:1], v[0:1], v[4:5]\n\t" \
    "s_waitcnt lgkmcnt(0)\n\t" \
    "v_pk_add_f32 v[12:13], v[12:13], v[14:15]\n\t" \
    "s_nop 0\n\t" \
    "v_pk_add_f32 v[8:9], v[8:9], v[12:13]\n\t" \
    "s_nop 0\n\t" \
    "v_pk_add_f32 v[0:1], v[0:1], v[8:9]\n\t" \
    "s_nop 0\n\t" \
    "v_cvt_pk_bf16_f32 v0, v0, v1\n\t" \
    "v_min_u32 v0, -2, v0\n\t" \
    "buffer_store_dword v0, %[mo], %[rs], %[sn] offen sc1\n\t" \
    "s_memrealtime %[t1]\n\t" \
    DGA \
    "s_waitcnt lgkmcnt(0)\n\t" \
    GSA(2) \
    "s_branch E_%=\n\t" \
    "SL_%=:\n\t" \
    "s_sleep 1\n\t" \
    "buffer_load_dwordx4 v[220:223], %[l16], %[rs], %[s0] offen sc1\n\t" \
    "buffer_load_dwordx4 v[224:227], %[l16], %[rs], %[s1] offen sc1\n\t" \
    "s_sub_u32 %[lim], %[lim], " STR(DEC2) "\n\t" \
    "s_cmp_gt_i32 %[lim], 0\n\t" \
    "s_cbranch_scc0 G_%=\n\t" \
    "s_nop 3\n\t" \
    "v_mov_b32 v0, 0\n\t" \
    "v_mov_b32 v1, 0\n\t" \
    "v_mov_b32 v2, 0\n\t" \
    "v_mov_b32 v3, 0\n\t" \
    "v_mov_b32 v4, 0\n\t" \
    "v_mov_b32 v5, 0\n\t" \
    "v_mov_b32 v6, 0\n\t" \
    "v_mov_b32 v7, 0\n\t" \
    "v_mov_b32 v8, 0\n\t" \
    "v_mov_b32 v9, 0\n\t" \
    "v_mov_b32 v10, 0\n\t" \
    "v_mov_b32 v11, 0\n\t" \
    "v_mov_b32 v12, 0\n\t" \
    "v_mov_b32 v13, 0\n\t" \
    "v_mov_b32 v14, 0\n\t" \
    "v_mov_b32 v15, 0\n\t" \
    "v_mov_b32 v252, 0\n\t" \
    "s_branch A_%=\n\t" \
    "G_%=:\n\t" \
    "s_waitcnt vmcnt(0)\n\t" \
    "s_branch D_%=\n\t" \
    "E_%=:\n\t" \
    GSW \
    : [lim] "+s"(lim) GSO(1) GSO(4), [t1] "=&s"(t1) DGO DGQ GSO(2) \
    : [l16] "v"(l16), [rs] "s"(rs), [idx] "v"(idx), [rw] "v"(rw_a), [s0] "s"(so[0]), [s1] "s"(so[1]), [rr] "v"(rr_a), [mo] "v"(mo_v), [sn] "s"(sn), [red] "s"(wid), "{v[156:163]}"(wo[0][0][0]), "{v[164:171]}"(wo[0][0][1]), "{v[172:179]}"(wo[0][1][0]), "{v[180:187]}"(wo[0][1][1]), "{v[188:195]}"(wo[1][0][0]), "{v[196:203]}"(wo[1][0][1]), "{v[204:211]}"(wo[1][1][0]), "{v[212:219]}"(wo[1][1][1]) \
    : "memory", "vcc", "scc", "v252", "v0", "v1", "v2", "v3", "v4", "v5", "v6", "v7", "v8", "v9", "v10", "v11", "v12", "v13", "v14", "v15", "v220", "v221", "v222", "v223", "v224", "v225", "v226", "v227")

#define WO_W() \
  asm volatile( \
    "v_mov_b32 v0, 0\n\t" \
    "v_mov_b32 v1, 0\n\t" \
    "v_mov_b32 v2, 0\n\t" \
    "v_mov_b32 v3, 0\n\t" \
    "v_mov_b32 v4, 0\n\t" \
    "v_mov_b32 v5, 0\n\t" \
    "v_mov_b32 v6, 0\n\t" \
    "v_mov_b32 v7, 0\n\t" \
    "v_mov_b32 v8, 0\n\t" \
    "v_mov_b32 v9, 0\n\t" \
    "v_mov_b32 v10, 0\n\t" \
    "v_mov_b32 v11, 0\n\t" \
    "v_mov_b32 v12, 0\n\t" \
    "v_mov_b32 v13, 0\n\t" \
    "v_mov_b32 v14, 0\n\t" \
    "v_mov_b32 v15, 0\n\t" \
    "v_mov_b32 v252, 0\n\t" \
    "s_barrier\n\t" \
    GSA(4) \
    "buffer_load_dwordx4 v[220:223], %[l16], %[rs], %[s0] offen sc1\n\t" \
    "s_nop 15\n\t" \
    "buffer_load_dwordx4 v[224:227], %[l16], %[rs], %[s1] offen sc1\n\t" \
    "s_nop 15\n\t" \
    "buffer_load_dwordx4 v[228:231], %[l16], %[rs], %[s2] offen sc1\n\t" \
    "s_nop 15\n\t" \
    "buffer_load_dwordx4 v[232:235], %[l16], %[rs], %[s3] offen sc1\n\t" \
    "s_nop 15\n\t" \
    "buffer_load_dwordx4 v[236:239], %[l16], %[rs], %[s4] offen sc1\n\t" \
    "s_nop 15\n\t" \
    "buffer_load_dwordx4 v[240:243], %[l16], %[rs], %[s5] offen sc1\n\t" \
    "A_%=:\n\t" \
    "s_waitcnt vmcnt(5)\n\t" \
    "v_smfmac_f32_16x16x64_bf16 v[0:3], v[220:223], v[28:35], %[idx]\n\t" \
    "v_max3_u32 v252, v252, v220, v221\n\t" \
    "v_smfmac_f32_16x16x64_bf16 v[4:7], v[220:223], v[36:43], %[idx]\n\t" \
    "v_max3_u32 v252, v252, v222, v223\n\t" \
    "v_smfmac_f32_16x16x64_bf16 v[8:11], v[220:223], v[44:51], %[idx]\n\t" \
    "v_smfmac_f32_16x16x64_bf16 v[12:15], v[220:223], v[52:59], %[idx]\n\t" \
    "s_waitcnt vmcnt(4)\n\t" \
    "v_smfmac_f32_16x16x64_bf16 v[0:3], v[224:227], v[60:67], %[idx]\n\t" \
    "v_max3_u32 v252, v252, v224, v225\n\t" \
    "v_smfmac_f32_16x16x64_bf16 v[4:7], v[224:227], v[68:75], %[idx]\n\t" \
    "v_max3_u32 v252, v252, v226, v227\n\t" \
    "v_smfmac_f32_16x16x64_bf16 v[8:11], v[224:227], v[76:83], %[idx]\n\t" \
    "v_smfmac_f32_16x16x64_bf16 v[12:15], v[224:227], v[84:91], %[idx]\n\t" \
    "s_waitcnt vmcnt(3)\n\t" \
    "v_smfmac_f32_16x16x64_bf16 v[0:3], v[228:231], v[92:99], %[idx]\n\t" \
    "v_max3_u32 v252, v252, v228, v229\n\t" \
    "v_smfmac_f32_16x16x64_bf16 v[4:7], v[228:231], v[100:107], %[idx]\n\t" \
    "v_max3_u32 v252, v252, v230, v231\n\t" \
    "v_smfmac_f32_16x16x64_bf16 v[8:11], v[228:231], v[108:115], %[idx]\n\t" \
    "v_smfmac_f32_16x16x64_bf16 v[12:15], v[228:231], v[116:123], %[idx]\n\t" \
    "s_waitcnt vmcnt(2)\n\t" \
    "v_smfmac_f32_16x16x64_bf16 v[0:3], v[232:235], v[124:131], %[idx]\n\t" \
    "v_max3_u32 v252, v252, v232, v233\n\t" \
    "v_smfmac_f32_16x16x64_bf16 v[4:7], v[232:235], v[132:139], %[idx]\n\t" \
    "v_max3_u32 v252, v252, v234, v235\n\t" \
    "v_smfmac_f32_16x16x64_bf16 v[8:11], v[232:235], v[140:147], %[idx]\n\t" \
    "v_smfmac_f32_16x16x64_bf16 v[12:15], v[232:235], v[148:155], %[idx]\n\t" \
    "s_waitcnt vmcnt(1)\n\t" \
    "v_smfmac_f32_16x16x64_bf16 v[0:3], v[236:239], v[156:163], %[idx]\n\t" \
    "v_max3_u32 v252, v252, v236, v237\n\t" \
    "v_smfmac_f32_16x16x64_bf16 v[4:7], v[236:239], v[164:171], %[idx]\n\t" \
    "v_max3_u32 v252, v252, v238, v239\n\t" \
    "v_smfmac_f32_16x16x64_bf16 v[8:11], v[236:239], v[172:179], %[idx]\n\t" \
    "v_smfmac_f32_16x16x64_bf16 v[12:15], v[236:239], v[180:187], %[idx]\n\t" \
    "s_waitcnt vmcnt(0)\n\t" \
    "v_smfmac_f32_16x16x64_bf16 v[0:3], v[240:243], v[188:195], %[idx]\n\t" \
    "v_max3_u32 v252, v252, v240, v241\n\t" \
    "v_smfmac_f32_16x16x64_bf16 v[4:7], v[240:243], v[196:203], %[idx]\n\t" \
    "v_max3_u32 v252, v252, v242, v243\n\t" \
    "v_smfmac_f32_16x16x64_bf16 v[8:11], v[240:243], v[204:211], %[idx]\n\t" \
    "v_smfmac_f32_16x16x64_bf16 v[12:15], v[240:243], v[212:219], %[idx]\n\t" \
    "v_cmp_eq_u32 vcc, -1, v252\n\t" \
    "s_cbranch_vccnz SL_%=\n\t" \
    "D_%=:\n\t" \
    GSA(1) \
    "s_nop 2\n\t" \
    "v_add_f32 v0, v0, v1\n\t" \
    "v_add_f32 v2, v2, v3\n\t" \
    "v_add_f32 v4, v4, v5\n\t" \
    "v_add_f32 v6, v6, v7\n\t" \
    "v_add_f32 v8, v8, v9\n\t" \
    "v_add_f32 v10, v10, v11\n\t" \
    "v_add_f32 v12, v12, v13\n\t" \
    "v_add_f32 v14, v14, v15\n\t" \
    "v_permlane32_swap_b32 v0, v4\n\t" \
    "v_permlane32_swap_b32 v2, v6\n\t" \
    "v_permlane32_swap_b32 v8, v12\n\t" \
    "v_permlane32_swap_b32 v10, v14\n\t" \
    "v_add_f32 v0, v0, v12\n\t" \
    "v_add_f32 v2, v2, v14\n\t" \
    "ds_write2_b32 %[rw], v0, v2 offset0:0 offset1:32\n\t" \
    "s_waitcnt lgkmcnt(0)\n\t" \
    "s_barrier\n\t" \
    "s_branch E_%=\n\t" \
    "SL_%=:\n\t" \
    "s_sleep 1\n\t" \
    "buffer_load_dwordx4 v[220:223], %[l16], %[rs], %[s0] offen sc1\n\t" \
    "buffer_load_dwordx4 v[224:227], %[l16], %[rs], %[s1] offen sc1\n\t" \
    "buffer_load_dwordx4 v[228:231], %[l16], %[rs], %[s2] offen sc1\n\t" \
    "buffer_load_dwordx4 v[232:235], %[l16], %[rs], %[s3] offen sc1\n\t" \
    "buffer_load_dwordx4 v[236:239], %[l16], %[rs], %[s4] offen sc1\n\t" \
    "buffer_load_dwordx4 v[240:243], %[l16], %[rs], %[s5] offen sc1\n\t" \
    "s_sub_u32 %[lim], %[lim], " STR(DEC2) "\n\t" \
    "s_cmp_gt_i32 %[lim], 0\n\t" \
    "s_cbranch_scc0 G_%=\n\t" \
    "s_nop 3\n\t" \
    "v_mov_b32 v0, 0\n\t" \
    "v_mov_b32 v1, 0\n\t" \
    "v_mov_b32 v2, 0\n\t" \
    "v_mov_b32 v3, 0\n\t" \
    "v_mov_b32 v4, 0\n\t" \
    "v_mov_b32 v5, 0\n\t" \
    "v_mov_b32 v6, 0\n\t" \
    "v_mov_b32 v7, 0\n\t" \
    "v_mov_b32 v8, 0\n\t" \
    "v_mov_b32 v9, 0\n\t" \
    "v_mov_b32 v10, 0\n\t" \
    "v_mov_b32 v11, 0\n\t" \
    "v_mov_b32 v12, 0\n\t" \
    "v_mov_b32 v13, 0\n\t" \
    "v_mov_b32 v14, 0\n\t" \
    "v_mov_b32 v15, 0\n\t" \
    "v_mov_b32 v252, 0\n\t" \
    "s_branch A_%=\n\t" \
    "G_%=:\n\t" \
    "s_waitcnt vmcnt(0)\n\t" \
    "s_branch D_%=\n\t" \
    "E_%=:\n\t" \
    GSW \
    : [lim] "+s"(lim) GSO(1) GSO(4) \
    : [l16] "v"(l16), [rs] "s"(rs), [idx] "v"(idx), [rw] "v"(rw_a), [s0] "s"(so[0]), [s1] "s"(so[1]), [s2] "s"(so[2]), [s3] "s"(so[3]), [s4] "s"(so[4]), [s5] "s"(so[5]), "{v[28:35]}"(wo[0][0][0]), "{v[36:43]}"(wo[0][0][1]), "{v[44:51]}"(wo[0][1][0]), "{v[52:59]}"(wo[0][1][1]), "{v[60:67]}"(wo[1][0][0]), "{v[68:75]}"(wo[1][0][1]), "{v[76:83]}"(wo[1][1][0]), "{v[84:91]}"(wo[1][1][1]), "{v[92:99]}"(wo[2][0][0]), "{v[100:107]}"(wo[2][0][1]), "{v[108:115]}"(wo[2][1][0]), "{v[116:123]}"(wo[2][1][1]), "{v[124:131]}"(wo[3][0][0]), "{v[132:139]}"(wo[3][0][1]), "{v[140:147]}"(wo[3][1][0]), "{v[148:155]}"(wo[3][1][1]), "{v[156:163]}"(wo[4][0][0]), "{v[164:171]}"(wo[4][0][1]), "{v[172:179]}"(wo[4][1][0]), "{v[180:187]}"(wo[4][1][1]), "{v[188:195]}"(wo[5][0][0]), "{v[196:203]}"(wo[5][0][1]), "{v[204:211]}"(wo[5][1][0]), "{v[212:219]}"(wo[5][1][1]) \
    : "memory", "vcc", "scc", "v252", "v0", "v1", "v2", "v3", "v4", "v5", "v6", "v7", "v8", "v9", "v10", "v11", "v12", "v13", "v14", "v15", "v220", "v221", "v222", "v223", "v224", "v225", "v226", "v227", "v228", "v229", "v230", "v231", "v232", "v233", "v234", "v235", "v236", "v237", "v238", "v239", "v240", "v241", "v242", "v243")

#define S_LOOP_K(TE_, FIN_) \
  asm volatile( \
    "s_cmp_eq_u32 %[kh], 0\n\t" \
    "s_cbranch_scc0 L1_%=\n\t" \
    "L0_%=:\n\t" \
    "s_setprio 0\n\t" \
    "s_nop 3\n\t" \
    "ds_write_b128 %[pa], v[196:199] offset:0\n\t" \
    "ds_write_b128 %[pa], v[200:203] offset:1024\n\t" \
    "ds_write_b128 %[pa], v[204:207] offset:2048\n\t" \
    "ds_write_b128 %[pa], v[208:211] offset:3072\n\t" \
    "s_sub_u32 %[a], %[t], 1\n\t" \
    "v_mov_b32 v53, %[tag]\n\t" \
    "v_mov_b32 v52, %[a]\n\t" \
    "s_sub_u32 %[b], %[t], 2\n\t" \
    "s_cmp_lt_u32 %[b], " STR(SQUICK) "\n\t" \
    "s_cselect_b64 exec, %[fm], 0\n\t" \
    "buffer_store_dwordx2 v[52:53], %[l16], %[rs], %[fo] offen sc1\n\t" \
    "s_mov_b64 exec, -1\n\t" \
    "s_add_u32 m0, %[sd], 49152\n\t" \
    "s_add_u32 %[a], %[sx], 81920\n\t" \
    "s_waitcnt lgkmcnt(0)\n\t" \
    "buffer_load_dwordx4 %[l16], %[rs], %[a] offen offset:0 sc1 lds\n\t" \
    "buffer_load_dwordx4 %[l16], %[rs], %[a] offen offset:1024 sc1 lds\n\t" \
    "buffer_load_dwordx4 %[l16], %[rs], %[a] offen offset:2048 sc1 lds\n\t" \
    "buffer_load_dwordx4 %[l16], %[rs], %[a] offen offset:3072 sc1 lds\n\t" \
    "v_mfma_f32_16x16x32_bf16 v[0:3], v[68:71], v[20:23], 0\n\t" \
    "ds_read_b128 v[44:47], %[xp] offset:6144\n\t" \
    "v_mfma_f32_16x16x32_bf16 v[4:7], v[76:79], v[20:23], 0\n\t" \
    "ds_read_b128 v[48:51], %[xp] offset:7168\n\t" \
    "v_mfma_f32_16x16x32_bf16 v[8:11], v[68:71], v[24:27], 0\n\t" \
    "v_mfma_f32_16x16x32_bf16 v[12:15], v[76:79], v[24:27], 0\n\t" \
    "v_mfma_f32_16x16x32_bf16 v[0:3], v[72:75], v[28:31], v[0:3]\n\t" \
    "ds_read_b128 v[20:23], %[xp] offset:8192\n\t" \
    "v_mfma_f32_16x16x32_bf16 v[4:7], v[80:83], v[28:31], v[4:7]\n\t" \
    "ds_read_b128 v[24:27], %[xp] offset:9216\n\t" \
    "v_mfma_f32_16x16x32_bf16 v[8:11], v[72:75], v[32:35], v[8:11]\n\t" \
    "v_mfma_f32_16x16x32_bf16 v[12:15], v[80:83], v[32:35], v[12:15]\n\t" \
    "v_mfma_f32_16x16x32_bf16 v[0:3], v[84:87], v[36:39], v[0:3]\n\t" \
    "ds_read_b128 v[28:31], %[xp] offset:10240\n\t" \
    "v_mfma_f32_16x16x32_bf16 v[4:7], v[92:95], v[36:39], v[4:7]\n\t" \
    "ds_read_b128 v[32:35], %[xp] offset:11264\n\t" \
    "v_mfma_f32_16x16x32_bf16 v[8:11], v[84:87], v[40:43], v[8:11]\n\t" \
    "s_waitcnt lgkmcnt(4)\n\t" \
    "v_mfma_f32_16x16x32_bf16 v[12:15], v[92:95], v[40:43], v[12:15]\n\t" \
    "v_mfma_f32_16x16x32_bf16 v[0:3], v[88:91], v[44:47], v[0:3]\n\t" \
    "ds_read_b128 v[36:39], %[xp] offset:12288\n\t" \
    "v_mfma_f32_16x16x32_bf16 v[4:7], v[96:99], v[44:47], v[4:7]\n\t" \
    "ds_read_b128 v[40:43], %[xp] offset:13312\n\t" \
    "v_mfma_f32_16x16x32_bf16 v[8:11], v[88:91], v[48:51], v[8:11]\n\t" \
    "s_waitcnt lgkmcnt(4)\n\t" \
    "v_mfma_f32_16x16x32_bf16 v[12:15], v[96:99], v[48:51], v[12:15]\n\t" \
    "v_mfma_f32_16x16x32_bf16 v[0:3], v[100:103], v[20:23], v[0:3]\n\t" \
    "ds_read_b128 v[44:47], %[xp] offset:14336\n\t" \
    "v_mfma_f32_16x16x32_bf16 v[4:7], v[108:111], v[20:23], v[4:7]\n\t" \
    "ds_read_b128 v[48:51], %[xp] offset:15360\n\t" \
    "v_mfma_f32_16x16x32_bf16 v[8:11], v[100:103], v[24:27], v[8:11]\n\t" \
    "s_waitcnt lgkmcnt(4)\n\t" \
    "v_mfma_f32_16x16x32_bf16 v[12:15], v[108:111], v[24:27], v[12:15]\n\t" \
    "v_mfma_f32_16x16x32_bf16 v[0:3], v[104:107], v[28:31], v[0:3]\n\t" \
    "ds_read_b128 v[20:23], %[xp] offset:16384\n\t" \
    "v_mfma_f32_16x16x32_bf16 v[4:7], v[112:115], v[28:31], v[4:7]\n\t" \
    "ds_read_b128 v[24:27], %[xp] offset:17408\n\t" \
    "v_mfma_f32_16x16x32_bf16 v[8:11], v[104:107], v[32:35], v[8:11]\n\t" \
    "s_waitcnt lgkmcnt(4)\n\t" \
    "v_mfma_f32_16x16x32_bf16 v[12:15], v[112:115], v[32:35], v[12:15]\n\t" \
    "v_mfma_f32_16x16x32_bf16 v[0:3], v[116:119], v[36:39], v[0:3]\n\t" \
    "ds_read_b128 v[28:31], %[xp] offset:18432\n\t" \
    "v_mfma_f32_16x16x32_bf16 v[4:7], v[124:127], v[36:39], v[4:7]\n\t" \
    "ds_read_b128 v[32:35], %[xp] offset:19456\n\t" \
    "v_mfma_f32_16x16x32_bf16 v[8:11], v[116:119], v[40:43], v[8:11]\n\t" \
    "s_waitcnt lgkmcnt(4)\n\t" \
    "v_mfma_f32_16x16x32_bf16 v[12:15], v[124:127], v[40:43], v[12:15]\n\t" \
    "v_mfma_f32_16x16x32_bf16 v[0:3], v[120:123], v[44:47], v[0:3]\n\t" \
    "ds_read_b128 v[36:39], %[xp] offset:20480\n\t" \
    "v_mfma_f32_16x16x32_bf16 v[4:7], v[128:131], v[44:47], v[4:7]\n\t" \
    "ds_read_b128 v[40:43], %[xp] offset:21504\n\t" \
    "v_mfma_f32_16x16x32_bf16 v[8:11], v[120:123], v[48:51], v[8:11]\n\t" \
    "s_waitcnt lgkmcnt(4)\n\t" \
    "v_mfma_f32_16x16x32_bf16 v[12:15], v[128:131], v[48:51], v[12:15]\n\t" \
    "s_waitcnt vmcnt(4)\n\t" \
    "s_barrier\n\t" \
    "s_setprio 0\n\t" \
    "s_sub_u32 %[a], %[t], 1\n\t" \
    "v_mov_b32 v53, %[tag]\n\t" \
    "v_mov_b32 v52, %[a]\n\t" \
    "s_cmp_gt_u32 %[t], 1\n\t" \
    "s_cselect_b64 exec, %[fm], 0\n\t" \
    "buffer_store_dwordx2 v[52:53], %[l16], %[rs], %[fo] offen sc1\n\t" \
    "s_mov_b64 exec, %[pm]\n\t" \
    "s_mov_b32 m0, %[pd]\n\t" \
    "s_add_u32 %[a], %[sx], %[po]\n\t" \
    "buffer_load_dwordx4 %[l16], %[rs], %[a] offen sc1 lds\n\t" \
    "s_mov_b64 exec, -1\n\t" \
    "s_add_u32 m0, %[sd], 0\n\t" \
    "s_add_u32 %[a], %[sx], 131072\n\t" \
    "buffer_load_dwordx4 %[l16], %[rs], %[a] offen offset:0 sc1 lds\n\t" \
    "buffer_load_dwordx4 %[l16], %[rs], %[a] offen offset:1024 sc1 lds\n\t" \
    "buffer_load_dwordx4 %[l16], %[rs], %[a] offen offset:2048 sc1 lds\n\t" \
    "buffer_load_dwordx4 %[l16], %[rs], %[a] offen offset:3072 sc1 lds\n\t" \
    "v_mfma_f32_16x16x32_bf16 v[0:3], v[132:135], v[20:23], v[0:3]\n\t" \
    "ds_read_b128 v[44:47], %[xp] offset:22528\n\t" \
    "v_mfma_f32_16x16x32_bf16 v[4:7], v[140:143], v[20:23], v[4:7]\n\t" \
    "ds_read_b128 v[48:51], %[xp] offset:23552\n\t" \
    "v_mfma_f32_16x16x32_bf16 v[8:11], v[132:135], v[24:27], v[8:11]\n\t" \
    "s_waitcnt lgkmcnt(4)\n\t" \
    "v_mfma_f32_16x16x32_bf16 v[12:15], v[140:143], v[24:27], v[12:15]\n\t" \
    "v_mfma_f32_16x16x32_bf16 v[0:3], v[136:139], v[28:31], v[0:3]\n\t" \
    "ds_read_b128 v[20:23], %[xp] offset:24576\n\t" \
    "v_mfma_f32_16x16x32_bf16 v[4:7], v[144:147], v[28:31], v[4:7]\n\t" \
    "ds_read_b128 v[24:27], %[xp] offset:25600\n\t" \
    "v_mfma_f32_16x16x32_bf16 v[8:11], v[136:139], v[32:35], v[8:11]\n\t" \
    "s_waitcnt lgkmcnt(4)\n\t" \
    "v_mfma_f32_16x16x32_bf16 v[12:15], v[144:147], v[32:35], v[12:15]\n\t" \
    "v_mfma_f32_16x16x32_bf16 v[0:3], v[148:151], v[36:39], v[0:3]\n\t" \
    "ds_read_b128 v[28:31], %[xp] offset:26624\n\t" \
    "v_mfma_f32_16x16x32_bf16 v[4:7], v[156:159], v[36:39], v[4:7]\n\t" \
    "ds_read_b128 v[32:35], %[xp] offset:27648\n\t" \
    "v_mfma_f32_16x16x32_bf16 v[8:11], v[148:151], v[40:43], v[8:11]\n\t" \
    "s_waitcnt lgkmcnt(4)\n\t" \
    "v_mfma_f32_16x16x32_bf16 v[12:15], v[156:159], v[40:43], v[12:15]\n\t" \
    "v_mfma_f32_16x16x32_bf16 v[0:3], v[152:155], v[44:47], v[0:3]\n\t" \
    "ds_read_b128 v[36:39], %[xp] offset:28672\n\t" \
    "v_mfma_f32_16x16x32_bf16 v[4:7], v[160:163], v[44:47], v[4:7]\n\t" \
    "ds_read_b128 v[40:43], %[xp] offset:29696\n\t" \
    "v_mfma_f32_16x16x32_bf16 v[8:11], v[152:155], v[48:51], v[8:11]\n\t" \
    "s_waitcnt lgkmcnt(4)\n\t" \
    "v_mfma_f32_16x16x32_bf16 v[12:15], v[160:163], v[48:51], v[12:15]\n\t" \
    "v_mfma_f32_16x16x32_bf16 v[0:3], v[164:167], v[20:23], v[0:3]\n\t" \
    "ds_read_b128 v[44:47], %[xp] offset:30720\n\t" \
    "v_mfma_f32_16x16x32_bf16 v[4:7], v[172:175], v[20:23], v[4:7]\n\t" \
    "ds_read_b128 v[48:51], %[xp] offset:31744\n\t" \
    "v_mfma_f32_16x16x32_bf16 v[8:11], v[164:167], v[24:27], v[8:11]\n\t" \
    "s_waitcnt lgkmcnt(4)\n\t" \
    "v_mfma_f32_16x16x32_bf16 v[12:15], v[172:175], v[24:27], v[12:15]\n\t" \
    "v_mfma_f32_16x16x32_bf16 v[0:3], v[168:171], v[28:31], v[0:3]\n\t" \
    "ds_read_b128 v[20:23], %[xp] offset:32768\n\t" \
    "v_mfma_f32_16x16x32_bf16 v[4:7], v[176:179], v[28:31], v[4:7]\n\t" \
    "ds_read_b128 v[24:27], %[xp] offset:33792\n\t" \
    "v_mfma_f32_16x16x32_bf16 v[8:11], v[168:171], v[32:35], v[8:11]\n\t" \
    "s_waitcnt lgkmcnt(4)\n\t" \
    "v_mfma_f32_16x16x32_bf16 v[12:15], v[176:179], v[32:35], v[12:15]\n\t" \
    "v_mfma_f32_16x16x32_bf16 v[0:3], v[180:183], v[36:39], v[0:3]\n\t" \
    "ds_read_b128 v[28:31], %[xp] offset:34816\n\t" \
    "v_mfma_f32_16x16x32_bf16 v[4:7], v[188:191], v[36:39], v[4:7]\n\t" \
    "ds_read_b128 v[32:35], %[xp] offset:35840\n\t" \
    "v_mfma_f32_16x16x32_bf16 v[8:11], v[180:183], v[40:43], v[8:11]\n\t" \
    "s_waitcnt lgkmcnt(4)\n\t" \
    "v_mfma_f32_16x16x32_bf16 v[12:15], v[188:191], v[40:43], v[12:15]\n\t" \
    "v_mfma_f32_16x16x32_bf16 v[0:3], v[184:187], v[44:47], v[0:3]\n\t" \
    "ds_read_b128 v[36:39], %[xp] offset:36864\n\t" \
    "v_mfma_f32_16x16x32_bf16 v[4:7], v[192:195], v[44:47], v[4:7]\n\t" \
    "ds_read_b128 v[40:43], %[xp] offset:37888\n\t" \
    "v_mfma_f32_16x16x32_bf16 v[8:11], v[184:187], v[48:51], v[8:11]\n\t" \
    "s_waitcnt lgkmcnt(4)\n\t" \
    "v_mfma_f32_16x16x32_bf16 v[12:15], v[192:195], v[48:51], v[12:15]\n\t" \
    "s_add_u32 %[sx], %[sx], 65536\n\t" \
    "s_add_u32 %[t], %[t], 1\n\t" \
    "s_waitcnt vmcnt(4)\n\t" \
    "s_barrier\n\t" \
    "s_setprio 0\n\t" \
    "s_nop 3\n\t" \
    "ds_write_b128 %[pa], v[0:3] offset:0\n\t" \
    "ds_write_b128 %[pa], v[4:7] offset:1024\n\t" \
    "ds_write_b128 %[pa], v[8:11] offset:2048\n\t" \
    "ds_write_b128 %[pa], v[12:15] offset:3072\n\t" \
    "s_sub_u32 %[a], %[t], 1\n\t" \
    "v_mov_b32 v53, %[tag]\n\t" \
    "v_mov_b32 v52, %[a]\n\t" \
    "s_sub_u32 %[b], %[t], 2\n\t" \
    "s_cmp_lt_u32 %[b], " STR(SQUICK) "\n\t" \
    "s_cselect_b64 exec, %[fm], 0\n\t" \
    "buffer_store_dwordx2 v[52:53], %[l16], %[rs], %[fo] offen sc1\n\t" \
    "s_mov_b64 exec, -1\n\t" \
    "s_add_u32 m0, %[sd], 16384\n\t" \
    "s_add_u32 %[a], %[sx], 81920\n\t" \
    "s_waitcnt lgkmcnt(0)\n\t" \
    "buffer_load_dwordx4 %[l16], %[rs], %[a] offen offset:0 sc1 lds\n\t" \
    "buffer_load_dwordx4 %[l16], %[rs], %[a] offen offset:1024 sc1 lds\n\t" \
    "buffer_load_dwordx4 %[l16], %[rs], %[a] offen offset:2048 sc1 lds\n\t" \
    "buffer_load_dwordx4 %[l16], %[rs], %[a] offen offset:3072 sc1 lds\n\t" \
    "v_mfma_f32_16x16x32_bf16 v[196:199], v[68:71], v[20:23], 0\n\t" \
    "ds_read_b128 v[44:47], %[xp] offset:38912\n\t" \
    "v_mfma_f32_16x16x32_bf16 v[200:203], v[76:79], v[20:23], 0\n\t" \
    "ds_read_b128 v[48:51], %[xp] offset:39936\n\t" \
    "v_mfma_f32_16x16x32_bf16 v[204:207], v[68:71], v[24:27], 0\n\t" \
    "v_mfma_f32_16x16x32_bf16 v[208:211], v[76:79], v[24:27], 0\n\t" \
    "v_mfma_f32_16x16x32_bf16 v[196:199], v[72:75], v[28:31], v[196:199]\n\t" \
    "ds_read_b128 v[20:23], %[xp] offset:40960\n\t" \
    "v_mfma_f32_16x16x32_bf16 v[200:203], v[80:83], v[28:31], v[200:203]\n\t" \
    "ds_read_b128 v[24:27], %[xp] offset:41984\n\t" \
    "v_mfma_f32_16x16x32_bf16 v[204:207], v[72:75], v[32:35], v[204:207]\n\t" \
    "v_mfma_f32_16x16x32_bf16 v[208:211], v[80:83], v[32:35], v[208:211]\n\t" \
    "v_mfma_f32_16x16x32_bf16 v[196:199], v[84:87], v[36:39], v[196:199]\n\t" \
    "ds_read_b128 v[28:31], %[xp] offset:43008\n\t" \
    "v_mfma_f32_16x16x32_bf16 v[200:203], v[92:95], v[36:39], v[200:203]\n\t" \
    "ds_read_b128 v[32:35], %[xp] offset:44032\n\t" \
    "v_mfma_f32_16x16x32_bf16 v[204:207], v[84:87], v[40:43], v[204:207]\n\t" \
    "s_waitcnt lgkmcnt(4)\n\t" \
    "v_mfma_f32_16x16x32_bf16 v[208:211], v[92:95], v[40:43], v[208:211]\n\t" \
    "v_mfma_f32_16x16x32_bf16 v[196:199], v[88:91], v[44:47], v[196:199]\n\t" \
    "ds_read_b128 v[36:39], %[xp] offset:45056\n\t" \
    "v_mfma_f32_16x16x32_bf16 v[200:203], v[96:99], v[44:47], v[200:203]\n\t" \
    "ds_read_b128 v[40:43], %[xp] offset:46080\n\t" \
    "v_mfma_f32_16x16x32_bf16 v[204:207], v[88:91], v[48:51], v[204:207]\n\t" \
    "s_waitcnt lgkmcnt(4)\n\t" \
    "v_mfma_f32_16x16x32_bf16 v[208:211], v[96:99], v[48:51], v[208:211]\n\t" \
    "v_mfma_f32_16x16x32_bf16 v[196:199], v[100:103], v[20:23], v[196:199]\n\t" \
    "ds_read_b128 v[44:47], %[xp] offset:47104\n\t" \
    "v_mfma_f32_16x16x32_bf16 v[200:203], v[108:111], v[20:23], v[200:203]\n\t" \
    "ds_read_b128 v[48:51], %[xp] offset:48128\n\t" \
    "v_mfma_f32_16x16x32_bf16 v[204:207], v[100:103], v[24:27], v[204:207]\n\t" \
    "s_waitcnt lgkmcnt(4)\n\t" \
    "v_mfma_f32_16x16x32_bf16 v[208:211], v[108:111], v[24:27], v[208:211]\n\t" \
    "v_mfma_f32_16x16x32_bf16 v[196:199], v[104:107], v[28:31], v[196:199]\n\t" \
    "ds_read_b128 v[20:23], %[xp] offset:49152\n\t" \
    "v_mfma_f32_16x16x32_bf16 v[200:203], v[112:115], v[28:31], v[200:203]\n\t" \
    "ds_read_b128 v[24:27], %[xp] offset:50176\n\t" \
    "v_mfma_f32_16x16x32_bf16 v[204:207], v[104:107], v[32:35], v[204:207]\n\t" \
    "s_waitcnt lgkmcnt(4)\n\t" \
    "v_mfma_f32_16x16x32_bf16 v[208:211], v[112:115], v[32:35], v[208:211]\n\t" \
    "v_mfma_f32_16x16x32_bf16 v[196:199], v[116:119], v[36:39], v[196:199]\n\t" \
    "ds_read_b128 v[28:31], %[xp] offset:51200\n\t" \
    "v_mfma_f32_16x16x32_bf16 v[200:203], v[124:127], v[36:39], v[200:203]\n\t" \
    "ds_read_b128 v[32:35], %[xp] offset:52224\n\t" \
    "v_mfma_f32_16x16x32_bf16 v[204:207], v[116:119], v[40:43], v[204:207]\n\t" \
    "s_waitcnt lgkmcnt(4)\n\t" \
    "v_mfma_f32_16x16x32_bf16 v[208:211], v[124:127], v[40:43], v[208:211]\n\t" \
    "v_mfma_f32_16x16x32_bf16 v[196:199], v[120:123], v[44:47], v[196:199]\n\t" \
    "ds_read_b128 v[36:39], %[xp] offset:53248\n\t" \
    "v_mfma_f32_16x16x32_bf16 v[200:203], v[128:131], v[44:47], v[200:203]\n\t" \
    "ds_read_b128 v[40:43], %[xp] offset:54272\n\t" \
    "v_mfma_f32_16x16x32_bf16 v[204:207], v[120:123], v[48:51], v[204:207]\n\t" \
    "s_waitcnt lgkmcnt(4)\n\t" \
    "v_mfma_f32_16x16x32_bf16 v[208:211], v[128:131], v[48:51], v[208:211]\n\t" \
    "s_waitcnt vmcnt(4)\n\t" \
    "s_barrier\n\t" \
    "s_setprio 0\n\t" \
    "s_sub_u32 %[a], %[t], 1\n\t" \
    "v_mov_b32 v53, %[tag]\n\t" \
    "v_mov_b32 v52, %[a]\n\t" \
    "s_cmp_gt_u32 %[t], 1\n\t" \
    "s_cselect_b64 exec, %[fm], 0\n\t" \
    "buffer_store_dwordx2 v[52:53], %[l16], %[rs], %[fo] offen sc1\n\t" \
    "s_mov_b64 exec, %[pm]\n\t" \
    "s_mov_b32 m0, %[pd]\n\t" \
    "s_add_u32 %[a], %[sx], %[po]\n\t" \
    "buffer_load_dwordx4 %[l16], %[rs], %[a] offen sc1 lds\n\t" \
    "s_mov_b64 exec, -1\n\t" \
    "s_add_u32 m0, %[sd], 32768\n\t" \
    "s_add_u32 %[a], %[sx], 131072\n\t" \
    "buffer_load_dwordx4 %[l16], %[rs], %[a] offen offset:0 sc1 lds\n\t" \
    "buffer_load_dwordx4 %[l16], %[rs], %[a] offen offset:1024 sc1 lds\n\t" \
    "buffer_load_dwordx4 %[l16], %[rs], %[a] offen offset:2048 sc1 lds\n\t" \
    "buffer_load_dwordx4 %[l16], %[rs], %[a] offen offset:3072 sc1 lds\n\t" \
    "v_mfma_f32_16x16x32_bf16 v[196:199], v[132:135], v[20:23], v[196:199]\n\t" \
    "ds_read_b128 v[44:47], %[xp] offset:55296\n\t" \
    "v_mfma_f32_16x16x32_bf16 v[200:203], v[140:143], v[20:23], v[200:203]\n\t" \
    "ds_read_b128 v[48:51], %[xp] offset:56320\n\t" \
    "v_mfma_f32_16x16x32_bf16 v[204:207], v[132:135], v[24:27], v[204:207]\n\t" \
    "s_waitcnt lgkmcnt(4)\n\t" \
    "v_mfma_f32_16x16x32_bf16 v[208:211], v[140:143], v[24:27], v[208:211]\n\t" \
    "v_mfma_f32_16x16x32_bf16 v[196:199], v[136:139], v[28:31], v[196:199]\n\t" \
    "ds_read_b128 v[20:23], %[xp] offset:57344\n\t" \
    "v_mfma_f32_16x16x32_bf16 v[200:203], v[144:147], v[28:31], v[200:203]\n\t" \
    "ds_read_b128 v[24:27], %[xp] offset:58368\n\t" \
    "v_mfma_f32_16x16x32_bf16 v[204:207], v[136:139], v[32:35], v[204:207]\n\t" \
    "s_waitcnt lgkmcnt(4)\n\t" \
    "v_mfma_f32_16x16x32_bf16 v[208:211], v[144:147], v[32:35], v[208:211]\n\t" \
    "v_mfma_f32_16x16x32_bf16 v[196:199], v[148:151], v[36:39], v[196:199]\n\t" \
    "ds_read_b128 v[28:31], %[xp] offset:59392\n\t" \
    "v_mfma_f32_16x16x32_bf16 v[200:203], v[156:159], v[36:39], v[200:203]\n\t" \
    "ds_read_b128 v[32:35], %[xp] offset:60416\n\t" \
    "v_mfma_f32_16x16x32_bf16 v[204:207], v[148:151], v[40:43], v[204:207]\n\t" \
    "s_waitcnt lgkmcnt(4)\n\t" \
    "v_mfma_f32_16x16x32_bf16 v[208:211], v[156:159], v[40:43], v[208:211]\n\t" \
    "v_mfma_f32_16x16x32_bf16 v[196:199], v[152:155], v[44:47], v[196:199]\n\t" \
    "ds_read_b128 v[36:39], %[xp] offset:61440\n\t" \
    "v_mfma_f32_16x16x32_bf16 v[200:203], v[160:163], v[44:47], v[200:203]\n\t" \
    "ds_read_b128 v[40:43], %[xp] offset:62464\n\t" \
    "v_mfma_f32_16x16x32_bf16 v[204:207], v[152:155], v[48:51], v[204:207]\n\t" \
    "s_waitcnt lgkmcnt(4)\n\t" \
    "v_mfma_f32_16x16x32_bf16 v[208:211], v[160:163], v[48:51], v[208:211]\n\t" \
    "v_mfma_f32_16x16x32_bf16 v[196:199], v[164:167], v[20:23], v[196:199]\n\t" \
    "ds_read_b128 v[44:47], %[xp] offset:63488\n\t" \
    "v_mfma_f32_16x16x32_bf16 v[200:203], v[172:175], v[20:23], v[200:203]\n\t" \
    "ds_read_b128 v[48:51], %[xp] offset:64512\n\t" \
    "v_mfma_f32_16x16x32_bf16 v[204:207], v[164:167], v[24:27], v[204:207]\n\t" \
    "s_waitcnt lgkmcnt(4)\n\t" \
    "v_mfma_f32_16x16x32_bf16 v[208:211], v[172:175], v[24:27], v[208:211]\n\t" \
    "v_mfma_f32_16x16x32_bf16 v[196:199], v[168:171], v[28:31], v[196:199]\n\t" \
    "ds_read_b128 v[20:23], %[xp] offset:0\n\t" \
    "v_mfma_f32_16x16x32_bf16 v[200:203], v[176:179], v[28:31], v[200:203]\n\t" \
    "ds_read_b128 v[24:27], %[xp] offset:1024\n\t" \
    "v_mfma_f32_16x16x32_bf16 v[204:207], v[168:171], v[32:35], v[204:207]\n\t" \
    "s_waitcnt lgkmcnt(4)\n\t" \
    "v_mfma_f32_16x16x32_bf16 v[208:211], v[176:179], v[32:35], v[208:211]\n\t" \
    "v_mfma_f32_16x16x32_bf16 v[196:199], v[180:183], v[36:39], v[196:199]\n\t" \
    "ds_read_b128 v[28:31], %[xp] offset:2048\n\t" \
    "v_mfma_f32_16x16x32_bf16 v[200:203], v[188:191], v[36:39], v[200:203]\n\t" \
    "ds_read_b128 v[32:35], %[xp] offset:3072\n\t" \
    "v_mfma_f32_16x16x32_bf16 v[204:207], v[180:183], v[40:43], v[204:207]\n\t" \
    "s_waitcnt lgkmcnt(4)\n\t" \
    "v_mfma_f32_16x16x32_bf16 v[208:211], v[188:191], v[40:43], v[208:211]\n\t" \
    "v_mfma_f32_16x16x32_bf16 v[196:199], v[184:187], v[44:47], v[196:199]\n\t" \
    "ds_read_b128 v[36:39], %[xp] offset:4096\n\t" \
    "v_mfma_f32_16x16x32_bf16 v[200:203], v[192:195], v[44:47], v[200:203]\n\t" \
    "ds_read_b128 v[40:43], %[xp] offset:5120\n\t" \
    "v_mfma_f32_16x16x32_bf16 v[204:207], v[184:187], v[48:51], v[204:207]\n\t" \
    "s_waitcnt lgkmcnt(4)\n\t" \
    "v_mfma_f32_16x16x32_bf16 v[208:211], v[192:195], v[48:51], v[208:211]\n\t" \
    "s_add_u32 %[sx], %[sx], 65536\n\t" \
    "s_add_u32 %[t], %[t], 1\n\t" \
    "s_waitcnt vmcnt(4)\n\t" \
    "s_barrier\n\t" \
    "s_cmp_lt_u32 %[t], %[te]\n\t" \
    "s_cbranch_scc1 L0_%=\n\t" \
    "s_cmp_lg_u32 %[fin], 0\n\t" \
    "s_cbranch_scc0 E_%=\n\t" \
    "s_nop 4\n\t" \
    "ds_write_b128 %[pa], v[196:199] offset:0\n\t" \
    "ds_write_b128 %[pa], v[200:203] offset:1024\n\t" \
    "ds_write_b128 %[pa], v[204:207] offset:2048\n\t" \
    "ds_write_b128 %[pa], v[208:211] offset:3072\n\t" \
    "s_waitcnt lgkmcnt(0)\n\t" \
    "s_barrier\n\t" \
    "s_branch E_%=\n\t" \
    "L1_%=:\n\t" \
    "s_setprio 1\n\t" \
    "s_add_u32 m0, %[sd], 49152\n\t" \
    "s_add_u32 %[a], %[sx], 81920\n\t" \
    "v_mfma_f32_16x16x32_bf16 v[0:3], v[68:71], v[20:23], 0\n\t" \
    "ds_read_b128 v[44:47], %[xp] offset:6144\n\t" \
    "v_mfma_f32_16x16x32_bf16 v[4:7], v[76:79], v[20:23], 0\n\t" \
    "ds_read_b128 v[48:51], %[xp] offset:7168\n\t" \
    "v_mfma_f32_16x16x32_bf16 v[8:11], v[68:71], v[24:27], 0\n\t" \
    "s_waitcnt lgkmcnt(4)\n\t" \
    "v_mfma_f32_16x16x32_bf16 v[12:15], v[76:79], v[24:27], 0\n\t" \
    "v_mfma_f32_16x16x32_bf16 v[0:3], v[72:75], v[28:31], v[0:3]\n\t" \
    "ds_read_b128 v[20:23], %[xp] offset:8192\n\t" \
    "v_mfma_f32_16x16x32_bf16 v[4:7], v[80:83], v[28:31], v[4:7]\n\t" \
    "ds_read_b128 v[24:27], %[xp] offset:9216\n\t" \
    "v_mfma_f32_16x16x32_bf16 v[8:11], v[72:75], v[32:35], v[8:11]\n\t" \
    "s_waitcnt lgkmcnt(4)\n\t" \
    "v_mfma_f32_16x16x32_bf16 v[12:15], v[80:83], v[32:35], v[12:15]\n\t" \
    "v_mfma_f32_16x16x32_bf16 v[0:3], v[84:87], v[36:39], v[0:3]\n\t" \
    "ds_read_b128 v[28:31], %[xp] offset:10240\n\t" \
    "v_mfma_f32_16x16x32_bf16 v[4:7], v[92:95], v[36:39], v[4:7]\n\t" \
    "ds_read_b128 v[32:35], %[xp] offset:11264\n\t" \
    "v_mfma_f32_16x16x32_bf16 v[8:11], v[84:87], v[40:43], v[8:11]\n\t" \
    "s_waitcnt lgkmcnt(4)\n\t" \
    "v_mfma_f32_16x16x32_bf16 v[12:15], v[92:95], v[40:43], v[12:15]\n\t" \
    "v_mfma_f32_16x16x32_bf16 v[0:3], v[88:91], v[44:47], v[0:3]\n\t" \
    "ds_read_b128 v[36:39], %[xp] offset:12288\n\t" \
    "v_mfma_f32_16x16x32_bf16 v[4:7], v[96:99], v[44:47], v[4:7]\n\t" \
    "ds_read_b128 v[40:43], %[xp] offset:13312\n\t" \
    "v_mfma_f32_16x16x32_bf16 v[8:11], v[88:91], v[48:51], v[8:11]\n\t" \
    "s_waitcnt lgkmcnt(4)\n\t" \
    "v_mfma_f32_16x16x32_bf16 v[12:15], v[96:99], v[48:51], v[12:15]\n\t" \
    "v_mfma_f32_16x16x32_bf16 v[0:3], v[100:103], v[20:23], v[0:3]\n\t" \
    "ds_read_b128 v[44:47], %[xp] offset:14336\n\t" \
    "v_mfma_f32_16x16x32_bf16 v[4:7], v[108:111], v[20:23], v[4:7]\n\t" \
    "ds_read_b128 v[48:51], %[xp] offset:15360\n\t" \
    "v_mfma_f32_16x16x32_bf16 v[8:11], v[100:103], v[24:27], v[8:11]\n\t" \
    "s_waitcnt lgkmcnt(4)\n\t" \
    "v_mfma_f32_16x16x32_bf16 v[12:15], v[108:111], v[24:27], v[12:15]\n\t" \
    "buffer_load_dwordx4 %[l16], %[rs], %[a] offen offset:0 sc1 lds\n\t" \
    "v_mfma_f32_16x16x32_bf16 v[0:3], v[104:107], v[28:31], v[0:3]\n\t" \
    "ds_read_b128 v[20:23], %[xp] offset:16384\n\t" \
    "v_mfma_f32_16x16x32_bf16 v[4:7], v[112:115], v[28:31], v[4:7]\n\t" \
    "ds_read_b128 v[24:27], %[xp] offset:17408\n\t" \
    "v_mfma_f32_16x16x32_bf16 v[8:11], v[104:107], v[32:35], v[8:11]\n\t" \
    "s_waitcnt lgkmcnt(4)\n\t" \
    "v_mfma_f32_16x16x32_bf16 v[12:15], v[112:115], v[32:35], v[12:15]\n\t" \
    "buffer_load_dwordx4 %[l16], %[rs], %[a] offen offset:1024 sc1 lds\n\t" \
    "v_mfma_f32_16x16x32_bf16 v[0:3], v[116:119], v[36:39], v[0:3]\n\t" \
    "ds_read_b128 v[28:31], %[xp] offset:18432\n\t" \
    "v_mfma_f32_16x16x32_bf16 v[4:7], v[124:127], v[36:39], v[4:7]\n\t" \
    "ds_read_b128 v[32:35], %[xp] offset:19456\n\t" \
    "v_mfma_f32_16x16x32_bf16 v[8:11], v[116:119], v[40:43], v[8:11]\n\t" \
    "s_waitcnt lgkmcnt(4)\n\t" \
    "v_mfma_f32_16x16x32_bf16 v[12:15], v[124:127], v[40:43], v[12:15]\n\t" \
    "buffer_load_dwordx4 %[l16], %[rs], %[a] offen offset:2048 sc1 lds\n\t" \
    "v_mfma_f32_16x16x32_bf16 v[0:3], v[120:123], v[44:47], v[0:3]\n\t" \
    "ds_read_b128 v[36:39], %[xp] offset:20480\n\t" \
    "v_mfma_f32_16x16x32_bf16 v[4:7], v[128:131], v[44:47], v[4:7]\n\t" \
    "ds_read_b128 v[40:43], %[xp] offset:21504\n\t" \
    "v_mfma_f32_16x16x32_bf16 v[8:11], v[120:123], v[48:51], v[8:11]\n\t" \
    "s_waitcnt lgkmcnt(4)\n\t" \
    "v_mfma_f32_16x16x32_bf16 v[12:15], v[128:131], v[48:51], v[12:15]\n\t" \
    "buffer_load_dwordx4 %[l16], %[rs], %[a] offen offset:3072 sc1 lds\n\t" \
    "s_waitcnt vmcnt(4)\n\t" \
    "s_barrier\n\t" \
    "s_setprio 1\n\t" \
    "s_add_u32 m0, %[sd], 0\n\t" \
    "s_add_u32 %[a], %[sx], 131072\n\t" \
    "s_add_u32 %[b], %[oo], 0x1000\n\t" \
    "s_cmp_lg_u32 %[t], 1\n\t" \
    "s_cselect_b64 %[em], -1, 0\n\t" \
    "v_mfma_f32_16x16x32_bf16 v[0:3], v[132:135], v[20:23], v[0:3]\n\t" \
    "ds_read_b128 v[44:47], %[xp] offset:22528\n\t" \
    "v_mfma_f32_16x16x32_bf16 v[4:7], v[140:143], v[20:23], v[4:7]\n\t" \
    "ds_read_b128 v[48:51], %[xp] offset:23552\n\t" \
    "v_mfma_f32_16x16x32_bf16 v[8:11], v[132:135], v[24:27], v[8:11]\n\t" \
    "ds_read_b128 v[52:55], %[pa] offset:0\n\t" \
    "s_waitcnt lgkmcnt(5)\n\t" \
    "v_mfma_f32_16x16x32_bf16 v[12:15], v[140:143], v[24:27], v[12:15]\n\t" \
    "buffer_load_dwordx4 %[l16], %[rs], %[a] offen offset:0 sc1 lds\n\t" \
    "v_mfma_f32_16x16x32_bf16 v[0:3], v[136:139], v[28:31], v[0:3]\n\t" \
    "ds_read_b128 v[20:23], %[xp] offset:24576\n\t" \
    "v_mfma_f32_16x16x32_bf16 v[4:7], v[144:147], v[28:31], v[4:7]\n\t" \
    "ds_read_b128 v[24:27], %[xp] offset:25600\n\t" \
    "v_mfma_f32_16x16x32_bf16 v[8:11], v[136:139], v[32:35], v[8:11]\n\t" \
    "ds_read_b128 v[56:59], %[pa] offset:1024\n\t" \
    "s_waitcnt lgkmcnt(6)\n\t" \
    "v_mfma_f32_16x16x32_bf16 v[12:15], v[144:147], v[32:35], v[12:15]\n\t" \
    "buffer_load_dwordx4 %[l16], %[rs], %[a] offen offset:1024 sc1 lds\n\t" \
    "v_mfma_f32_16x16x32_bf16 v[0:3], v[148:151], v[36:39], v[0:3]\n\t" \
    "ds_read_b128 v[28:31], %[xp] offset:26624\n\t" \
    "v_mfma_f32_16x16x32_bf16 v[4:7], v[156:159], v[36:39], v[4:7]\n\t" \
    "ds_read_b128 v[32:35], %[xp] offset:27648\n\t" \
    "v_mfma_f32_16x16x32_bf16 v[8:11], v[148:151], v[40:43], v[8:11]\n\t" \
    "ds_read_b128 v[60:63], %[pa] offset:2048\n\t" \
    "s_waitcnt lgkmcnt(7)\n\t" \
    "v_mfma_f32_16x16x32_bf16 v[12:15], v[156:159], v[40:43], v[12:15]\n\t" \
    "buffer_load_dwordx4 %[l16], %[rs], %[a] offen offset:2048 sc1 lds\n\t" \
    "v_mfma_f32_16x16x32_bf16 v[0:3], v[152:155], v[44:47], v[0:3]\n\t" \
    "ds_read_b128 v[36:39], %[xp] offset:28672\n\t" \
    "s_waitcnt lgkmcnt(7)\n\t" \
    "v_add_f32 v196, v196, v52\n\t" \
    "v_mfma_f32_16x16x32_bf16 v[4:7], v[160:163], v[44:47], v[4:7]\n\t" \
    "ds_read_b128 v[40:43], %[xp] offset:29696\n\t" \
    "v_add_f32 v197, v197, v53\n\t" \
    "v_mfma_f32_16x16x32_bf16 v[8:11], v[152:155], v[48:51], v[8:11]\n\t" \
    "ds_read_b128 v[64:67], %[pa] offset:3072\n\t" \
    "s_waitcnt lgkmcnt(7)\n\t" \
    "v_mfma_f32_16x16x32_bf16 v[12:15], v[160:163], v[48:51], v[12:15]\n\t" \
    "buffer_load_dwordx4 %[l16], %[rs], %[a] offen offset:3072 sc1 lds\n\t" \
    "v_add_f32 v198, v198, v54\n\t" \
    "v_add_f32 v199, v199, v55\n\t" \
    "v_mfma_f32_16x16x32_bf16 v[0:3], v[164:167], v[20:23], v[0:3]\n\t" \
    "ds_read_b128 v[44:47], %[xp] offset:30720\n\t" \
    "s_waitcnt lgkmcnt(7)\n\t" \
    "v_add_f32 v200, v200, v56\n\t" \
    "v_mfma_f32_16x16x32_bf16 v[4:7], v[172:175], v[20:23], v[4:7]\n\t" \
    "ds_read_b128 v[48:51], %[xp] offset:31744\n\t" \
    "v_add_f32 v201, v201, v57\n\t" \
    "v_mfma_f32_16x16x32_bf16 v[8:11], v[164:167], v[24:27], v[8:11]\n\t" \
    "v_add_f32 v202, v202, v58\n\t" \
    "s_waitcnt lgkmcnt(6)\n\t" \
    "v_mfma_f32_16x16x32_bf16 v[12:15], v[172:175], v[24:27], v[12:15]\n\t" \
    "v_add_f32 v203, v203, v59\n\t" \
    "s_waitcnt lgkmcnt(5)\n\t" \
    "v_add_f32 v204, v204, v60\n\t" \
    "v_add_f32 v205, v205, v61\n\t" \
    "v_mfma_f32_16x16x32_bf16 v[0:3], v[168:171], v[28:31], v[0:3]\n\t" \
    "ds_read_b128 v[20:23], %[xp] offset:32768\n\t" \
    "v_add_f32 v206, v206, v62\n\t" \
    "v_mfma_f32_16x16x32_bf16 v[4:7], v[176:179], v[28:31], v[4:7]\n\t" \
    "ds_read_b128 v[24:27], %[xp] offset:33792\n\t" \
    "v_add_f32 v207, v207, v63\n\t" \
    "v_mfma_f32_16x16x32_bf16 v[8:11], v[168:171], v[32:35], v[8:11]\n\t" \
    "s_waitcnt lgkmcnt(4)\n\t" \
    "v_add_f32 v208, v208, v64\n\t" \
    "v_mfma_f32_16x16x32_bf16 v[12:15], v[176:179], v[32:35], v[12:15]\n\t" \
    "v_add_f32 v209, v209, v65\n\t" \
    "v_add_f32 v210, v210, v66\n\t" \
    "v_add_f32 v211, v211, v67\n\t" \
    "v_mfma_f32_16x16x32_bf16 v[0:3], v[180:183], v[36:39], v[0:3]\n\t" \
    "ds_read_b128 v[28:31], %[xp] offset:34816\n\t" \
    "v_cvt_pk_bf16_f32 v52, v196, v200\n\t" \
    "v_mfma_f32_16x16x32_bf16 v[4:7], v[188:191], v[36:39], v[4:7]\n\t" \
    "ds_read_b128 v[32:35], %[xp] offset:35840\n\t" \
    "v_cvt_pk_bf16_f32 v53, v197, v201\n\t" \
    "v_mfma_f32_16x16x32_bf16 v[8:11], v[180:183], v[40:43], v[8:11]\n\t" \
    "v_cvt_pk_bf16_f32 v54, v198, v202\n\t" \
    "s_waitcnt lgkmcnt(4)\n\t" \
    "v_mfma_f32_16x16x32_bf16 v[12:15], v[188:191], v[40:43], v[12:15]\n\t" \
    "v_cvt_pk_bf16_f32 v55, v199, v203\n\t" \
    "v_cvt_pk_bf16_f32 v56, v204, v208\n\t" \
    "v_cvt_pk_bf16_f32 v57, v205, v209\n\t" \
    "v_mfma_f32_16x16x32_bf16 v[0:3], v[184:187], v[44:47], v[0:3]\n\t" \
    "ds_read_b128 v[36:39], %[xp] offset:36864\n\t" \
    "v_cvt_pk_bf16_f32 v58, v206, v210\n\t" \
    "v_mfma_f32_16x16x32_bf16 v[4:7], v[192:195], v[44:47], v[4:7]\n\t" \
    "ds_read_b128 v[40:43], %[xp] offset:37888\n\t" \
    "v_cvt_pk_bf16_f32 v59, v207, v211\n\t" \
    "v_mfma_f32_16x16x32_bf16 v[8:11], v[184:187], v[48:51], v[8:11]\n\t" \
    "s_waitcnt lgkmcnt(4)\n\t" \
    "v_mfma_f32_16x16x32_bf16 v[12:15], v[192:195], v[48:51], v[12:15]\n\t" \
    "s_mov_b64 exec, %[em]\n\t" \
    "buffer_store_dwordx4 v[52:55], %[l16], %[rs], %[oo] offen sc1\n\t" \
    "buffer_store_dwordx4 v[56:59], %[l16], %[rs], %[b] offen sc1\n\t" \
    "s_mov_b64 exec, -1\n\t" \
    "s_add_u32 %[sx], %[sx], 65536\n\t" \
    "s_add_u32 %[t], %[t], 1\n\t" \
    "s_add_u32 %[oo], %[oo], 262144\n\t" \
    "s_waitcnt vmcnt(6)\n\t" \
    "s_cmp_gt_u32 %[t], " STR(SQUICK) " + 1\n\t" \
    "s_cbranch_scc1 B0_%=\n\t" \
    "s_waitcnt vmcnt(0)\n\t" \
    "B0_%=:\n\t" \
    "s_barrier\n\t" \
    "s_setprio 1\n\t" \
    "s_add_u32 m0, %[sd], 16384\n\t" \
    "s_add_u32 %[a], %[sx], 81920\n\t" \
    "v_mfma_f32_16x16x32_bf16 v[196:199], v[68:71], v[20:23], 0\n\t" \
    "ds_read_b128 v[44:47], %[xp] offset:38912\n\t" \
    "v_mfma_f32_16x16x32_bf16 v[200:203], v[76:79], v[20:23], 0\n\t" \
    "ds_read_b128 v[48:51], %[xp] offset:39936\n\t" \
    "v_mfma_f32_16x16x32_bf16 v[204:207], v[68:71], v[24:27], 0\n\t" \
    "s_waitcnt lgkmcnt(4)\n\t" \
    "v_mfma_f32_16x16x32_bf16 v[208:211], v[76:79], v[24:27], 0\n\t" \
    "v_mfma_f32_16x16x32_bf16 v[196:199], v[72:75], v[28:31], v[196:199]\n\t" \
    "ds_read_b128 v[20:23], %[xp] offset:40960\n\t" \
    "v_mfma_f32_16x16x32_bf16 v[200:203], v[80:83], v[28:31], v[200:203]\n\t" \
    "ds_read_b128 v[24:27], %[xp] offset:41984\n\t" \
    "v_mfma_f32_16x16x32_bf16 v[204:207], v[72:75], v[32:35], v[204:207]\n\t" \
    "s_waitcnt lgkmcnt(4)\n\t" \
    "v_mfma_f32_16x16x32_bf16 v[208:211], v[80:83], v[32:35], v[208:211]\n\t" \
    "v_mfma_f32_16x16x32_bf16 v[196:199], v[84:87], v[36:39], v[196:199]\n\t" \
    "ds_read_b128 v[28:31], %[xp] offset:43008\n\t" \
    "v_mfma_f32_16x16x32_bf16 v[200:203], v[92:95], v[36:39], v[200:203]\n\t" \
    "ds_read_b128 v[32:35], %[xp] offset:44032\n\t" \
    "v_mfma_f32_16x16x32_bf16 v[204:207], v[84:87], v[40:43], v[204:207]\n\t" \
    "s_waitcnt lgkmcnt(4)\n\t" \
    "v_mfma_f32_16x16x32_bf16 v[208:211], v[92:95], v[40:43], v[208:211]\n\t" \
    "v_mfma_f32_16x16x32_bf16 v[196:199], v[88:91], v[44:47], v[196:199]\n\t" \
    "ds_read_b128 v[36:39], %[xp] offset:45056\n\t" \
    "v_mfma_f32_16x16x32_bf16 v[200:203], v[96:99], v[44:47], v[200:203]\n\t" \
    "ds_read_b128 v[40:43], %[xp] offset:46080\n\t" \
    "v_mfma_f32_16x16x32_bf16 v[204:207], v[88:91], v[48:51], v[204:207]\n\t" \
    "s_waitcnt lgkmcnt(4)\n\t" \
    "v_mfma_f32_16x16x32_bf16 v[208:211], v[96:99], v[48:51], v[208:211]\n\t" \
    "v_mfma_f32_16x16x32_bf16 v[196:199], v[100:103], v[20:23], v[196:199]\n\t" \
    "ds_read_b128 v[44:47], %[xp] offset:47104\n\t" \
    "v_mfma_f32_16x16x32_bf16 v[200:203], v[108:111], v[20:23], v[200:203]\n\t" \
    "ds_read_b128 v[48:51], %[xp] offset:48128\n\t" \
    "v_mfma_f32_16x16x32_bf16 v[204:207], v[100:103], v[24:27], v[204:207]\n\t" \
    "s_waitcnt lgkmcnt(4)\n\t" \
    "v_mfma_f32_16x16x32_bf16 v[208:211], v[108:111], v[24:27], v[208:211]\n\t" \
    "buffer_load_dwordx4 %[l16], %[rs], %[a] offen offset:0 sc1 lds\n\t" \
    "v_mfma_f32_16x16x32_bf16 v[196:199], v[104:107], v[28:31], v[196:199]\n\t" \
    "ds_read_b128 v[20:23], %[xp] offset:49152\n\t" \
    "v_mfma_f32_16x16x32_bf16 v[200:203], v[112:115], v[28:31], v[200:203]\n\t" \
    "ds_read_b128 v[24:27], %[xp] offset:50176\n\t" \
    "v_mfma_f32_16x16x32_bf16 v[204:207], v[104:107], v[32:35], v[204:207]\n\t" \
    "s_waitcnt lgkmcnt(4)\n\t" \
    "v_mfma_f32_16x16x32_bf16 v[208:211], v[112:115], v[32:35], v[208:211]\n\t" \
    "buffer_load_dwordx4 %[l16], %[rs], %[a] offen offset:1024 sc1 lds\n\t" \
    "v_mfma_f32_16x16x32_bf16 v[196:199], v[116:119], v[36:39], v[196:199]\n\t" \
    "ds_read_b128 v[28:31], %[xp] offset:51200\n\t" \
    "v_mfma_f32_16x16x32_bf16 v[200:203], v[124:127], v[36:39], v[200:203]\n\t" \
    "ds_read_b128 v[32:35], %[xp] offset:52224\n\t" \
    "v_mfma_f32_16x16x32_bf16 v[204:207], v[116:119], v[40:43], v[204:207]\n\t" \
    "s_waitcnt lgkmcnt(4)\n\t" \
    "v_mfma_f32_16x16x32_bf16 v[208:211], v[124:127], v[40:43], v[208:211]\n\t" \
    "buffer_load_dwordx4 %[l16], %[rs], %[a] offen offset:2048 sc1 lds\n\t" \
    "v_mfma_f32_16x16x32_bf16 v[196:199], v[120:123], v[44:47], v[196:199]\n\t" \
    "ds_read_b128 v[36:39], %[xp] offset:53248\n\t" \
    "v_mfma_f32_16x16x32_bf16 v[200:203], v[128:131], v[44:47], v[200:203]\n\t" \
    "ds_read_b128 v[40:43], %[xp] offset:54272\n\t" \
    "v_mfma_f32_16x16x32_bf16 v[204:207], v[120:123], v[48:51], v[204:207]\n\t" \
    "s_waitcnt lgkmcnt(4)\n\t" \
    "v_mfma_f32_16x16x32_bf16 v[208:211], v[128:131], v[48:51], v[208:211]\n\t" \
    "buffer_load_dwordx4 %[l16], %[rs], %[a] offen offset:3072 sc1 lds\n\t" \
    "s_waitcnt vmcnt(4)\n\t" \
    "s_barrier\n\t" \
    "s_setprio 1\n\t" \
    "s_add_u32 m0, %[sd], 32768\n\t" \
    "s_add_u32 %[a], %[sx], 131072\n\t" \
    "s_add_u32 %[b], %[oo], 0x1000\n\t" \
    "s_cmp_lg_u32 %[t], 1\n\t" \
    "s_cselect_b64 %[em], -1, 0\n\t" \
    "v_mfma_f32_16x16x32_bf16 v[196:199], v[132:135], v[20:23], v[196:199]\n\t" \
    "ds_read_b128 v[44:47], %[xp] offset:55296\n\t" \
    "v_mfma_f32_16x16x32_bf16 v[200:203], v[140:143], v[20:23], v[200:203]\n\t" \
    "ds_read_b128 v[48:51], %[xp] offset:56320\n\t" \
    "v_mfma_f32_16x16x32_bf16 v[204:207], v[132:135], v[24:27], v[204:207]\n\t" \
    "ds_read_b128 v[52:55], %[pa] offset:0\n\t" \
    "s_waitcnt lgkmcnt(5)\n\t" \
    "v_mfma_f32_16x16x32_bf16 v[208:211], v[140:143], v[24:27], v[208:211]\n\t" \
    "buffer_load_dwordx4 %[l16], %[rs], %[a] offen offset:0 sc1 lds\n\t" \
    "v_mfma_f32_16x16x32_bf16 v[196:199], v[136:139], v[28:31], v[196:199]\n\t" \
    "ds_read_b128 v[20:23], %[xp] offset:57344\n\t" \
    "v_mfma_f32_16x16x32_bf16 v[200:203], v[144:147], v[28:31], v[200:203]\n\t" \
    "ds_read_b128 v[24:27], %[xp] offset:58368\n\t" \
    "v_mfma_f32_16x16x32_bf16 v[204:207], v[136:139], v[32:35], v[204:207]\n\t" \
    "ds_read_b128 v[56:59], %[pa] offset:1024\n\t" \
    "s_waitcnt lgkmcnt(6)\n\t" \
    "v_mfma_f32_16x16x32_bf16 v[208:211], v[144:147], v[32:35], v[208:211]\n\t" \
    "buffer_load_dwordx4 %[l16], %[rs], %[a] offen offset:1024 sc1 lds\n\t" \
    "v_mfma_f32_16x16x32_bf16 v[196:199], v[148:151], v[36:39], v[196:199]\n\t" \
    "ds_read_b128 v[28:31], %[xp] offset:59392\n\t" \
    "v_mfma_f32_16x16x32_bf16 v[200:203], v[156:159], v[36:39], v[200:203]\n\t" \
    "ds_read_b128 v[32:35], %[xp] offset:60416\n\t" \
    "v_mfma_f32_16x16x32_bf16 v[204:207], v[148:151], v[40:43], v[204:207]\n\t" \
    "ds_read_b128 v[60:63], %[pa] offset:2048\n\t" \
    "s_waitcnt lgkmcnt(7)\n\t" \
    "v_mfma_f32_16x16x32_bf16 v[208:211], v[156:159], v[40:43], v[208:211]\n\t" \
    "buffer_load_dwordx4 %[l16], %[rs], %[a] offen offset:2048 sc1 lds\n\t" \
    "v_mfma_f32_16x16x32_bf16 v[196:199], v[152:155], v[44:47], v[196:199]\n\t" \
    "ds_read_b128 v[36:39], %[xp] offset:61440\n\t" \
    "s_waitcnt lgkmcnt(7)\n\t" \
    "v_add_f32 v0, v0, v52\n\t" \
    "v_mfma_f32_16x16x32_bf16 v[200:203], v[160:163], v[44:47], v[200:203]\n\t" \
    "ds_read_b128 v[40:43], %[xp] offset:62464\n\t" \
    "v_add_f32 v1, v1, v53\n\t" \
    "v_mfma_f32_16x16x32_bf16 v[204:207], v[152:155], v[48:51], v[204:207]\n\t" \
    "ds_read_b128 v[64:67], %[pa] offset:3072\n\t" \
    "s_waitcnt lgkmcnt(7)\n\t" \
    "v_mfma_f32_16x16x32_bf16 v[208:211], v[160:163], v[48:51], v[208:211]\n\t" \
    "buffer_load_dwordx4 %[l16], %[rs], %[a] offen offset:3072 sc1 lds\n\t" \
    "v_add_f32 v2, v2, v54\n\t" \
    "v_add_f32 v3, v3, v55\n\t" \
    "v_mfma_f32_16x16x32_bf16 v[196:199], v[164:167], v[20:23], v[196:199]\n\t" \
    "ds_read_b128 v[44:47], %[xp] offset:63488\n\t" \
    "s_waitcnt lgkmcnt(7)\n\t" \
    "v_add_f32 v4, v4, v56\n\t" \
    "v_mfma_f32_16x16x32_bf16 v[200:203], v[172:175], v[20:23], v[200:203]\n\t" \
    "ds_read_b128 v[48:51], %[xp] offset:64512\n\t" \
    "v_add_f32 v5, v5, v57\n\t" \
    "v_mfma_f32_16x16x32_bf16 v[204:207], v[164:167], v[24:27], v[204:207]\n\t" \
    "v_add_f32 v6, v6, v58\n\t" \
    "s_waitcnt lgkmcnt(6)\n\t" \
    "v_mfma_f32_16x16x32_bf16 v[208:211], v[172:175], v[24:27], v[208:211]\n\t" \
    "v_add_f32 v7, v7, v59\n\t" \
    "s_waitcnt lgkmcnt(5)\n\t" \
    "v_add_f32 v8, v8, v60\n\t" \
    "v_add_f32 v9, v9, v61\n\t" \
    "v_mfma_f32_16x16x32_bf16 v[196:199], v[168:171], v[28:31], v[196:199]\n\t" \
    "ds_read_b128 v[20:23], %[xp] offset:0\n\t" \
    "v_add_f32 v10, v10, v62\n\t" \
    "v_mfma_f32_16x16x32_bf16 v[200:203], v[176:179], v[28:31], v[200:203]\n\t" \
    "ds_read_b128 v[24:27], %[xp] offset:1024\n\t" \
    "v_add_f32 v11, v11, v63\n\t" \
    "v_mfma_f32_16x16x32_bf16 v[204:207], v[168:171], v[32:35], v[204:207]\n\t" \
    "s_waitcnt lgkmcnt(4)\n\t" \
    "v_add_f32 v12, v12, v64\n\t" \
    "v_mfma_f32_16x16x32_bf16 v[208:211], v[176:179], v[32:35], v[208:211]\n\t" \
    "v_add_f32 v13, v13, v65\n\t" \
    "v_add_f32 v14, v14, v66\n\t" \
    "v_add_f32 v15, v15, v67\n\t" \
    "v_mfma_f32_16x16x32_bf16 v[196:199], v[180:183], v[36:39], v[196:199]\n\t" \
    "ds_read_b128 v[28:31], %[xp] offset:2048\n\t" \
    "v_cvt_pk_bf16_f32 v52, v0, v4\n\t" \
    "v_mfma_f32_16x16x32_bf16 v[200:203], v[188:191], v[36:39], v[200:203]\n\t" \
    "ds_read_b128 v[32:35], %[xp] offset:3072\n\t" \
    "v_cvt_pk_bf16_f32 v53, v1, v5\n\t" \
    "v_mfma_f32_16x16x32_bf16 v[204:207], v[180:183], v[40:43], v[204:207]\n\t" \
    "v_cvt_pk_bf16_f32 v54, v2, v6\n\t" \
    "s_waitcnt lgkmcnt(4)\n\t" \
    "v_mfma_f32_16x16x32_bf16 v[208:211], v[188:191], v[40:43], v[208:211]\n\t" \
    "v_cvt_pk_bf16_f32 v55, v3, v7\n\t" \
    "v_cvt_pk_bf16_f32 v56, v8, v12\n\t" \
    "v_cvt_pk_bf16_f32 v57, v9, v13\n\t" \
    "v_mfma_f32_16x16x32_bf16 v[196:199], v[184:187], v[44:47], v[196:199]\n\t" \
    "ds_read_b128 v[36:39], %[xp] offset:4096\n\t" \
    "v_cvt_pk_bf16_f32 v58, v10, v14\n\t" \
    "v_mfma_f32_16x16x32_bf16 v[200:203], v[192:195], v[44:47], v[200:203]\n\t" \
    "ds_read_b128 v[40:43], %[xp] offset:5120\n\t" \
    "v_cvt_pk_bf16_f32 v59, v11, v15\n\t" \
    "v_mfma_f32_16x16x32_bf16 v[204:207], v[184:187], v[48:51], v[204:207]\n\t" \
    "s_waitcnt lgkmcnt(4)\n\t" \
    "v_mfma_f32_16x16x32_bf16 v[208:211], v[192:195], v[48:51], v[208:211]\n\t" \
    "s_mov_b64 exec, %[em]\n\t" \
    "buffer_store_dwordx4 v[52:55], %[l16], %[rs], %[oo] offen sc1\n\t" \
    "buffer_store_dwordx4 v[56:59], %[l16], %[rs], %[b] offen sc1\n\t" \
    "s_mov_b64 exec, -1\n\t" \
    "s_add_u32 %[sx], %[sx], 65536\n\t" \
    "s_add_u32 %[t], %[t], 1\n\t" \
    "s_add_u32 %[oo], %[oo], 262144\n\t" \
    "s_waitcnt vmcnt(6)\n\t" \
    "s_cmp_gt_u32 %[t], " STR(SQUICK) " + 1\n\t" \
    "s_cbranch_scc1 B1_%=\n\t" \
    "s_waitcnt vmcnt(0)\n\t" \
    "B1_%=:\n\t" \
    "s_barrier\n\t" \
    "s_cmp_lt_u32 %[t], %[te]\n\t" \
    "s_cbranch_scc1 L1_%=\n\t" \
    "s_cmp_lg_u32 %[fin], 0\n\t" \
    "s_cbranch_scc0 E_%=\n\t" \
    "s_nop 4\n\t" \
    "s_barrier\n\t" \
    "ds_read_b128 v[52:55], %[pa] offset:0\n\t" \
    "ds_read_b128 v[56:59], %[pa] offset:1024\n\t" \
    "ds_read_b128 v[60:63], %[pa] offset:2048\n\t" \
    "ds_read_b128 v[64:67], %[pa] offset:3072\n\t" \
    "s_add_u32 %[b], %[oo], 0x1000\n\t" \
    "s_waitcnt lgkmcnt(0)\n\t" \
    "v_add_f32 v196, v196, v52\n\t" \
    "v_add_f32 v197, v197, v53\n\t" \
    "v_add_f32 v198, v198, v54\n\t" \
    "v_add_f32 v199, v199, v55\n\t" \
    "v_add_f32 v200, v200, v56\n\t" \
    "v_add_f32 v201, v201, v57\n\t" \
    "v_add_f32 v202, v202, v58\n\t" \
    "v_add_f32 v203, v203, v59\n\t" \
    "v_add_f32 v204, v204, v60\n\t" \
    "v_add_f32 v205, v205, v61\n\t" \
    "v_add_f32 v206, v206, v62\n\t" \
    "v_add_f32 v207, v207, v63\n\t" \
    "v_add_f32 v208, v208, v64\n\t" \
    "v_add_f32 v209, v209, v65\n\t" \
    "v_add_f32 v210, v210, v66\n\t" \
    "v_add_f32 v211, v211, v67\n\t" \
    "v_cvt_pk_bf16_f32 v52, v196, v200\n\t" \
    "v_cvt_pk_bf16_f32 v53, v197, v201\n\t" \
    "v_cvt_pk_bf16_f32 v54, v198, v202\n\t" \
    "v_cvt_pk_bf16_f32 v55, v199, v203\n\t" \
    "v_cvt_pk_bf16_f32 v56, v204, v208\n\t" \
    "v_cvt_pk_bf16_f32 v57, v205, v209\n\t" \
    "v_cvt_pk_bf16_f32 v58, v206, v210\n\t" \
    "v_cvt_pk_bf16_f32 v59, v207, v211\n\t" \
    "buffer_store_dwordx4 v[52:55], %[l16], %[rs], %[oo] offen sc1\n\t" \
    "buffer_store_dwordx4 v[56:59], %[l16], %[rs], %[b] offen sc1\n\t" \
    "E_%=:\n\t" \
    "s_setprio 0\n\t" \
    "s_waitcnt lgkmcnt(0)\n\t" \
    "s_nop 7\n\t" \
    : [t] "+s"(t), [sx] "+s"(sx), [oo] "+s"(oo), [a] "=&s"(tmp), [b] "=&s"(tmp2), [em] "=&s"(em), "+{v[20:27]}"(xf[0]), "+{v[28:35]}"(xf[1]), "+{v[36:43]}"(xf[2]), "+{v[196:203]}"(ac[0]), "+{v[204:211]}"(ac[1]) \
    : [te] "s"(TE_), [fin] "s"(FIN_), [kh] "s"(kh), [sd] "s"(sd), [rs] "s"(rs), [l16] "v"(l16), [xp] "v"(xp), [pa] "v"(pa), [fm] "s"(fm), [pm] "s"(pm), [fo] "s"(fo), [po] "s"(po), [pd] "s"(pd), [tag] "s"(P.tag), "{v[68:75]}"(w[0][0]), "{v[76:83]}"(w[0][1]), "{v[84:91]}"(w[1][0]), "{v[92:99]}"(w[1][1]), "{v[100:107]}"(w[2][0]), "{v[108:115]}"(w[2][1]), "{v[116:123]}"(w[3][0]), "{v[124:131]}"(w[3][1]), "{v[132:139]}"(w[4][0]), "{v[140:147]}"(w[4][1]), "{v[148:155]}"(w[5][0]), "{v[156:163]}"(w[5][1]), "{v[164:171]}"(w[6][0]), "{v[172:179]}"(w[6][1]), "{v[180:187]}"(w[7][0]), "{v[188:195]}"(w[7][1]) \
    : "memory", "scc", "v0", "v1", "v2", "v3", "v4", "v5", "v6", "v7", "v8", "v9", "v10", "v11", "v12", "v13", "v14", "v15", "v44", "v45", "v46", "v47", "v48", "v49", "v50", "v51", "v52", "v53", "v54", "v55", "v56", "v57", "v58", "v59", "v60", "v61", "v62", "v63", "v64", "v65", "v66", "v67")

#define S_LOOP_V(TE_, FIN_) \
  asm volatile( \
    "s_cmp_eq_u32 %[kh], 0\n\t" \
    "s_cbranch_scc0 L1_%=\n\t" \
    "L0_%=:\n\t" \
    "s_setprio 0\n\t" \
    "s_nop 3\n\t" \
    "ds_write_b128 %[pa], v[196:199] offset:0\n\t" \
    "ds_write_b128 %[pa], v[200:203] offset:1024\n\t" \
    "ds_write_b128 %[pa], v[204:207] offset:2048\n\t" \
    "ds_write_b128 %[pa], v[208:211] offset:3072\n\t" \
    "s_sub_u32 %[a], %[t], 1\n\t" \
    "v_mov_b32 v53, %[tag]\n\t" \
    "v_mov_b32 v52, %[a]\n\t" \
    "s_sub_u32 %[b], %[t], 2\n\t" \
    "s_cmp_lt_u32 %[b], " STR(SQUICK) "\n\t" \
    "s_cselect_b64 exec, %[fm], 0\n\t" \
    "buffer_store_dwordx2 v[52:53], %[l16], %[rs], %[fo] offen sc1\n\t" \
    "s_mov_b64 exec, -1\n\t" \
    "s_add_u32 m0, %[sd], 49152\n\t" \
    "s_add_u32 %[a], %[sx], 81920\n\t" \
    "s_waitcnt lgkmcnt(0)\n\t" \
    "buffer_load_dwordx4 %[l16], %[rs], %[a] offen offset:0 sc1 lds\n\t" \
    "buffer_load_dwordx4 %[l16], %[rs], %[a] offen offset:1024 sc1 lds\n\t" \
    "buffer_load_dwordx4 %[l16], %[rs], %[a] offen offset:2048 sc1 lds\n\t" \
    "buffer_load_dwordx4 %[l16], %[rs], %[a] offen offset:3072 sc1 lds\n\t" \
    "v_mfma_f32_16x16x32_bf16 v[0:3], v[20:23], v[68:71], 0\n\t" \
    "ds_read_b128 v[44:47], %[xp] offset:6144\n\t" \
    "v_mfma_f32_16x16x32_bf16 v[4:7], v[20:23], v[76:79], 0\n\t" \
    "ds_read_b128 v[48:51], %[xp] offset:7168\n\t" \
    "v_mfma_f32_16x16x32_bf16 v[8:11], v[24:27], v[68:71], 0\n\t" \
    "v_mfma_f32_16x16x32_bf16 v[12:15], v[24:27], v[76:79], 0\n\t" \
    "v_mfma_f32_16x16x32_bf16 v[0:3], v[28:31], v[72:75], v[0:3]\n\t" \
    "ds_read_b128 v[20:23], %[xp] offset:8192\n\t" \
    "v_mfma_f32_16x16x32_bf16 v[4:7], v[28:31], v[80:83], v[4:7]\n\t" \
    "ds_read_b128 v[24:27], %[xp] offset:9216\n\t" \
    "v_mfma_f32_16x16x32_bf16 v[8:11], v[32:35], v[72:75], v[8:11]\n\t" \
    "v_mfma_f32_16x16x32_bf16 v[12:15], v[32:35], v[80:83], v[12:15]\n\t" \
    "v_mfma_f32_16x16x32_bf16 v[0:3], v[36:39], v[84:87], v[0:3]\n\t" \
    "ds_read_b128 v[28:31], %[xp] offset:10240\n\t" \
    "v_mfma_f32_16x16x32_bf16 v[4:7], v[36:39], v[92:95], v[4:7]\n\t" \
    "ds_read_b128 v[32:35], %[xp] offset:11264\n\t" \
    "v_mfma_f32_16x16x32_bf16 v[8:11], v[40:43], v[84:87], v[8:11]\n\t" \
    "s_waitcnt lgkmcnt(4)\n\t" \
    "v_mfma_f32_16x16x32_bf16 v[12:15], v[40:43], v[92:95], v[12:15]\n\t" \
    "v_mfma_f32_16x16x32_bf16 v[0:3], v[44:47], v[88:91], v[0:3]\n\t" \
    "ds_read_b128 v[36:39], %[xp] offset:12288\n\t" \
    "v_mfma_f32_16x16x32_bf16 v[4:7], v[44:47], v[96:99], v[4:7]\n\t" \
    "ds_read_b128 v[40:43], %[xp] offset:13312\n\t" \
    "v_mfma_f32_16x16x32_bf16 v[8:11], v[48:51], v[88:91], v[8:11]\n\t" \
    "s_waitcnt lgkmcnt(4)\n\t" \
    "v_mfma_f32_16x16x32_bf16 v[12:15], v[48:51], v[96:99], v[12:15]\n\t" \
    "v_mfma_f32_16x16x32_bf16 v[0:3], v[20:23], v[100:103], v[0:3]\n\t" \
    "ds_read_b128 v[44:47], %[xp] offset:14336\n\t" \
    "v_mfma_f32_16x16x32_bf16 v[4:7], v[20:23], v[108:111], v[4:7]\n\t" \
    "ds_read_b128 v[48:51], %[xp] offset:15360\n\t" \
    "v_mfma_f32_16x16x32_bf16 v[8:11], v[24:27], v[100:103], v[8:11]\n\t" \
    "s_waitcnt lgkmcnt(4)\n\t" \
    "v_mfma_f32_16x16x32_bf16 v[12:15], v[24:27], v[108:111], v[12:15]\n\t" \
    "v_mfma_f32_16x16x32_bf16 v[0:3], v[28:31], v[104:107], v[0:3]\n\t" \
    "ds_read_b128 v[20:23], %[xp] offset:16384\n\t" \
    "v_mfma_f32_16x16x32_bf16 v[4:7], v[28:31], v[112:115], v[4:7]\n\t" \
    "ds_read_b128 v[24:27], %[xp] offset:17408\n\t" \
    "v_mfma_f32_16x16x32_bf16 v[8:11], v[32:35], v[104:107], v[8:11]\n\t" \
    "s_waitcnt lgkmcnt(4)\n\t" \
    "v_mfma_f32_16x16x32_bf16 v[12:15], v[32:35], v[112:115], v[12:15]\n\t" \
    "v_mfma_f32_16x16x32_bf16 v[0:3], v[36:39], v[116:119], v[0:3]\n\t" \
    "ds_read_b128 v[28:31], %[xp] offset:18432\n\t" \
    "v_mfma_f32_16x16x32_bf16 v[4:7], v[36:39], v[124:127], v[4:7]\n\t" \
    "ds_read_b128 v[32:35], %[xp] offset:19456\n\t" \
    "v_mfma_f32_16x16x32_bf16 v[8:11], v[40:43], v[116:119], v[8:11]\n\t" \
    "s_waitcnt lgkmcnt(4)\n\t" \
    "v_mfma_f32_16x16x32_bf16 v[12:15], v[40:43], v[124:127], v[12:15]\n\t" \
    "v_mfma_f32_16x16x32_bf16 v[0:3], v[44:47], v[120:123], v[0:3]\n\t" \
    "ds_read_b128 v[36:39], %[xp] offset:20480\n\t" \
    "v_mfma_f32_16x16x32_bf16 v[4:7], v[44:47], v[128:131], v[4:7]\n\t" \
    "ds_read_b128 v[40:43], %[xp] offset:21504\n\t" \
    "v_mfma_f32_16x16x32_bf16 v[8:11], v[48:51], v[120:123], v[8:11]\n\t" \
    "s_waitcnt lgkmcnt(4)\n\t" \
    "v_mfma_f32_16x16x32_bf16 v[12:15], v[48:51], v[128:131], v[12:15]\n\t" \
    "s_waitcnt vmcnt(4)\n\t" \
    "s_barrier\n\t" \
    "s_setprio 0\n\t" \
    "s_sub_u32 %[a], %[t], 1\n\t" \
    "v_mov_b32 v53, %[tag]\n\t" \
    "v_mov_b32 v52, %[a]\n\t" \
    "s_cmp_gt_u32 %[t], 1\n\t" \
    "s_cselect_b64 exec, %[fm], 0\n\t" \
    "buffer_store_dwordx2 v[52:53], %[l16], %[rs], %[fo] offen sc1\n\t" \
    "s_mov_b64 exec, %[pm]\n\t" \
    "s_mov_b32 m0, %[pd]\n\t" \
    "s_add_u32 %[a], %[sx], %[po]\n\t" \
    "buffer_load_dwordx4 %[l16], %[rs], %[a] offen sc1 lds\n\t" \
    "s_mov_b64 exec, -1\n\t" \
    "s_add_u32 m0, %[sd], 0\n\t" \
    "s_add_u32 %[a], %[sx], 131072\n\t" \
    "buffer_load_dwordx4 %[l16], %[rs], %[a] offen offset:0 sc1 lds\n\t" \
    "buffer_load_dwordx4 %[l16], %[rs], %[a] offen offset:1024 sc1 lds\n\t" \
    "buffer_load_dwordx4 %[l16], %[rs], %[a] offen offset:2048 sc1 lds\n\t" \
    "buffer_load_dwordx4 %[l16], %[rs], %[a] offen offset:3072 sc1 lds\n\t" \
    "v_mfma_f32_16x16x32_bf16 v[0:3], v[20:23], v[132:135], v[0:3]\n\t" \
    "ds_read_b128 v[44:47], %[xp] offset:22528\n\t" \
    "v_mfma_f32_16x16x32_bf16 v[4:7], v[20:23], v[140:143], v[4:7]\n\t" \
    "ds_read_b128 v[48:51], %[xp] offset:23552\n\t" \
    "v_mfma_f32_16x16x32_bf16 v[8:11], v[24:27], v[132:135], v[8:11]\n\t" \
    "s_waitcnt lgkmcnt(4)\n\t" \
    "v_mfma_f32_16x16x32_bf16 v[12:15], v[24:27], v[140:143], v[12:15]\n\t" \
    "v_mfma_f32_16x16x32_bf16 v[0:3], v[28:31], v[136:139], v[0:3]\n\t" \
    "ds_read_b128 v[20:23], %[xp] offset:24576\n\t" \
    "v_mfma_f32_16x16x32_bf16 v[4:7], v[28:31], v[144:147], v[4:7]\n\t" \
    "ds_read_b128 v[24:27], %[xp] offset:25600\n\t" \
    "v_mfma_f32_16x16x32_bf16 v[8:11], v[32:35], v[136:139], v[8:11]\n\t" \
    "s_waitcnt lgkmcnt(4)\n\t" \
    "v_mfma_f32_16x16x32_bf16 v[12:15], v[32:35], v[144:147], v[12:15]\n\t" \
    "v_mfma_f32_16x16x32_bf16 v[0:3], v[36:39], v[148:151], v[0:3]\n\t" \
    "ds_read_b128 v[28:31], %[xp] offset:26624\n\t" \
    "v_mfma_f32_16x16x32_bf16 v[4:7], v[36:39], v[156:159], v[4:7]\n\t" \
    "ds_read_b128 v[32:35], %[xp] offset:27648\n\t" \
    "v_mfma_f32_16x16x32_bf16 v[8:11], v[40:43], v[148:151], v[8:11]\n\t" \
    "s_waitcnt lgkmcnt(4)\n\t" \
    "v_mfma_f32_16x16x32_bf16 v[12:15], v[40:43], v[156:159], v[12:15]\n\t" \
    "v_mfma_f32_16x16x32_bf16 v[0:3], v[44:47], v[152:155], v[0:3]\n\t" \
    "ds_read_b128 v[36:39], %[xp] offset:28672\n\t" \
    "v_mfma_f32_16x16x32_bf16 v[4:7], v[44:47], v[160:163], v[4:7]\n\t" \
    "ds_read_b128 v[40:43], %[xp] offset:29696\n\t" \
    "v_mfma_f32_16x16x32_bf16 v[8:11], v[48:51], v[152:155], v[8:11]\n\t" \
    "s_waitcnt lgkmcnt(4)\n\t" \
    "v_mfma_f32_16x16x32_bf16 v[12:15], v[48:51], v[160:163], v[12:15]\n\t" \
    "v_mfma_f32_16x16x32_bf16 v[0:3], v[20:23], v[164:167], v[0:3]\n\t" \
    "ds_read_b128 v[44:47], %[xp] offset:30720\n\t" \
    "v_mfma_f32_16x16x32_bf16 v[4:7], v[20:23], v[172:175], v[4:7]\n\t" \
    "ds_read_b128 v[48:51], %[xp] offset:31744\n\t" \
    "v_mfma_f32_16x16x32_bf16 v[8:11], v[24:27], v[164:167], v[8:11]\n\t" \
    "s_waitcnt lgkmcnt(4)\n\t" \
    "v_mfma_f32_16x16x32_bf16 v[12:15], v[24:27], v[172:175], v[12:15]\n\t" \
    "v_mfma_f32_16x16x32_bf16 v[0:3], v[28:31], v[168:171], v[0:3]\n\t" \
    "ds_read_b128 v[20:23], %[xp] offset:32768\n\t" \
    "v_mfma_f32_16x16x32_bf16 v[4:7], v[28:31], v[176:179], v[4:7]\n\t" \
    "ds_read_b128 v[24:27], %[xp] offset:33792\n\t" \
    "v_mfma_f32_16x16x32_bf16 v[8:11], v[32:35], v[168:171], v[8:11]\n\t" \
    "s_waitcnt lgkmcnt(4)\n\t" \
    "v_mfma_f32_16x16x32_bf16 v[12:15], v[32:35], v[176:179], v[12:15]\n\t" \
    "v_mfma_f32_16x16x32_bf16 v[0:3], v[36:39], v[180:183], v[0:3]\n\t" \
    "ds_read_b128 v[28:31], %[xp] offset:34816\n\t" \
    "v_mfma_f32_16x16x32_bf16 v[4:7], v[36:39], v[188:191], v[4:7]\n\t" \
    "ds_read_b128 v[32:35], %[xp] offset:35840\n\t" \
    "v_mfma_f32_16x16x32_bf16 v[8:11], v[40:43], v[180:183], v[8:11]\n\t" \
    "s_waitcnt lgkmcnt(4)\n\t" \
    "v_mfma_f32_16x16x32_bf16 v[12:15], v[40:43], v[188:191], v[12:15]\n\t" \
    "v_mfma_f32_16x16x32_bf16 v[0:3], v[44:47], v[184:187], v[0:3]\n\t" \
    "ds_read_b128 v[36:39], %[xp] offset:36864\n\t" \
    "v_mfma_f32_16x16x32_bf16 v[4:7], v[44:47], v[192:195], v[4:7]\n\t" \
    "ds_read_b128 v[40:43], %[xp] offset:37888\n\t" \
    "v_mfma_f32_16x16x32_bf16 v[8:11], v[48:51], v[184:187], v[8:11]\n\t" \
    "s_waitcnt lgkmcnt(4)\n\t" \
    "v_mfma_f32_16x16x32_bf16 v[12:15], v[48:51], v[192:195], v[12:15]\n\t" \
    "s_add_u32 %[sx], %[sx], 65536\n\t" \
    "s_add_u32 %[t], %[t], 1\n\t" \
    "s_waitcnt vmcnt(4)\n\t" \
    "s_barrier\n\t" \
    "s_setprio 0\n\t" \
    "s_nop 3\n\t" \
    "ds_write_b128 %[pa], v[0:3] offset:0\n\t" \
    "ds_write_b128 %[pa], v[4:7] offset:1024\n\t" \
    "ds_write_b128 %[pa], v[8:11] offset:2048\n\t" \
    "ds_write_b128 %[pa], v[12:15] offset:3072\n\t" \
    "s_sub_u32 %[a], %[t], 1\n\t" \
    "v_mov_b32 v53, %[tag]\n\t" \
    "v_mov_b32 v52, %[a]\n\t" \
    "s_sub_u32 %[b], %[t], 2\n\t" \
    "s_cmp_lt_u32 %[b], " STR(SQUICK) "\n\t" \
    "s_cselect_b64 exec, %[fm], 0\n\t" \
    "buffer_store_dwordx2 v[52:53], %[l16], %[rs], %[fo] offen sc1\n\t" \
    "s_mov_b64 exec, -1\n\t" \
    "s_add_u32 m0, %[sd], 16384\n\t" \
    "s_add_u32 %[a], %[sx], 81920\n\t" \
    "s_waitcnt lgkmcnt(0)\n\t" \
    "buffer_load_dwordx4 %[l16], %[rs], %[a] offen offset:0 sc1 lds\n\t" \
    "buffer_load_dwordx4 %[l16], %[rs], %[a] offen offset:1024 sc1 lds\n\t" \
    "buffer_load_dwordx4 %[l16], %[rs], %[a] offen offset:2048 sc1 lds\n\t" \
    "buffer_load_dwordx4 %[l16], %[rs], %[a] offen offset:3072 sc1 lds\n\t" \
    "v_mfma_f32_16x16x32_bf16 v[196:199], v[20:23], v[68:71], 0\n\t" \
    "ds_read_b128 v[44:47], %[xp] offset:38912\n\t" \
    "v_mfma_f32_16x16x32_bf16 v[200:203], v[20:23], v[76:79], 0\n\t" \
    "ds_read_b128 v[48:51], %[xp] offset:39936\n\t" \
    "v_mfma_f32_16x16x32_bf16 v[204:207], v[24:27], v[68:71], 0\n\t" \
    "v_mfma_f32_16x16x32_bf16 v[208:211], v[24:27], v[76:79], 0\n\t" \
    "v_mfma_f32_16x16x32_bf16 v[196:199], v[28:31], v[72:75], v[196:199]\n\t" \
    "ds_read_b128 v[20:23], %[xp] offset:40960\n\t" \
    "v_mfma_f32_16x16x32_bf16 v[200:203], v[28:31], v[80:83], v[200:203]\n\t" \
    "ds_read_b128 v[24:27], %[xp] offset:41984\n\t" \
    "v_mfma_f32_16x16x32_bf16 v[204:207], v[32:35], v[72:75], v[204:207]\n\t" \
    "v_mfma_f32_16x16x32_bf16 v[208:211], v[32:35], v[80:83], v[208:211]\n\t" \
    "v_mfma_f32_16x16x32_bf16 v[196:199], v[36:39], v[84:87], v[196:199]\n\t" \
    "ds_read_b128 v[28:31], %[xp] offset:43008\n\t" \
    "v_mfma_f32_16x16x32_bf16 v[200:203], v[36:39], v[92:95], v[200:203]\n\t" \
    "ds_read_b128 v[32:35], %[xp] offset:44032\n\t" \
    "v_mfma_f32_16x16x32_bf16 v[204:207], v[40:43], v[84:87], v[204:207]\n\t" \
    "s_waitcnt lgkmcnt(4)\n\t" \
    "v_mfma_f32_16x16x32_bf16 v[208:211], v[40:43], v[92:95], v[208:211]\n\t" \
    "v_mfma_f32_16x16x32_bf16 v[196:199], v[44:47], v[88:91], v[196:199]\n\t" \
    "ds_read_b128 v[36:39], %[xp] offset:45056\n\t" \
    "v_mfma_f32_16x16x32_bf16 v[200:203], v[44:47], v[96:99], v[200:203]\n\t" \
    "ds_read_b128 v[40:43], %[xp] offset:46080\n\t" \
    "v_mfma_f32_16x16x32_bf16 v[204:207], v[48:51], v[88:91], v[204:207]\n\t" \
    "s_waitcnt lgkmcnt(4)\n\t" \
    "v_mfma_f32_16x16x32_bf16 v[208:211], v[48:51], v[96:99], v[208:211]\n\t" \
    "v_mfma_f32_16x16x32_bf16 v[196:199], v[20:23], v[100:103], v[196:199]\n\t" \
    "ds_read_b128 v[44:47], %[xp] offset:47104\n\t" \
    "v_mfma_f32_16x16x32_bf16 v[200:203], v[20:23], v[108:111], v[200:203]\n\t" \
    "ds_read_b128 v[48:51], %[xp] offset:48128\n\t" \
    "v_mfma_f32_16x16x32_bf16 v[204:207], v[24:27], v[100:103], v[204:207]\n\t" \
    "s_waitcnt lgkmcnt(4)\n\t" \
    "v_mfma_f32_16x16x32_bf16 v[208:211], v[24:27], v[108:111], v[208:211]\n\t" \
    "v_mfma_f32_16x16x32_bf16 v[196:199], v[28:31], v[104:107], v[196:199]\n\t" \
    "ds_read_b128 v[20:23], %[xp] offset:49152\n\t" \
    "v_mfma_f32_16x16x32_bf16 v[200:203], v[28:31], v[112:115], v[200:203]\n\t" \
    "ds_read_b128 v[24:27], %[xp] offset:50176\n\t" \
    "v_mfma_f32_16x16x32_bf16 v[204:207], v[32:35], v[104:107], v[204:207]\n\t" \
    "s_waitcnt lgkmcnt(4)\n\t" \
    "v_mfma_f32_16x16x32_bf16 v[208:211], v[32:35], v[112:115], v[208:211]\n\t" \
    "v_mfma_f32_16x16x32_bf16 v[196:199], v[36:39], v[116:119], v[196:199]\n\t" \
    "ds_read_b128 v[28:31], %[xp] offset:51200\n\t" \
    "v_mfma_f32_16x16x32_bf16 v[200:203], v[36:39], v[124:127], v[200:203]\n\t" \
    "ds_read_b128 v[32:35], %[xp] offset:52224\n\t" \
    "v_mfma_f32_16x16x32_bf16 v[204:207], v[40:43], v[116:119], v[204:207]\n\t" \
    "s_waitcnt lgkmcnt(4)\n\t" \
    "v_mfma_f32_16x16x32_bf16 v[208:211], v[40:43], v[124:127], v[208:211]\n\t" \
    "v_mfma_f32_16x16x32_bf16 v[196:199], v[44:47], v[120:123], v[196:199]\n\t" \
    "ds_read_b128 v[36:39], %[xp] offset:53248\n\t" \
    "v_mfma_f32_16x16x32_bf16 v[200:203], v[44:47], v[128:131], v[200:203]\n\t" \
    "ds_read_b128 v[40:43], %[xp] offset:54272\n\t" \
    "v_mfma_f32_16x16x32_bf16 v[204:207], v[48:51], v[120:123], v[204:207]\n\t" \
    "s_waitcnt lgkmcnt(4)\n\t" \
    "v_mfma_f32_16x16x32_bf16 v[208:211], v[48:51], v[128:131], v[208:211]\n\t" \
    "s_waitcnt vmcnt(4)\n\t" \
    "s_barrier\n\t" \
    "s_setprio 0\n\t" \
    "s_sub_u32 %[a], %[t], 1\n\t" \
    "v_mov_b32 v53, %[tag]\n\t" \
    "v_mov_b32 v52, %[a]\n\t" \
    "s_cmp_gt_u32 %[t], 1\n\t" \
    "s_cselect_b64 exec, %[fm], 0\n\t" \
    "buffer_store_dwordx2 v[52:53], %[l16], %[rs], %[fo] offen sc1\n\t" \
    "s_mov_b64 exec, %[pm]\n\t" \
    "s_mov_b32 m0, %[pd]\n\t" \
    "s_add_u32 %[a], %[sx], %[po]\n\t" \
    "buffer_load_dwordx4 %[l16], %[rs], %[a] offen sc1 lds\n\t" \
    "s_mov_b64 exec, -1\n\t" \
    "s_add_u32 m0, %[sd], 32768\n\t" \
    "s_add_u32 %[a], %[sx], 131072\n\t" \
    "buffer_load_dwordx4 %[l16], %[rs], %[a] offen offset:0 sc1 lds\n\t" \
    "buffer_load_dwordx4 %[l16], %[rs], %[a] offen offset:1024 sc1 lds\n\t" \
    "buffer_load_dwordx4 %[l16], %[rs], %[a] offen offset:2048 sc1 lds\n\t" \
    "buffer_load_dwordx4 %[l16], %[rs], %[a] offen offset:3072 sc1 lds\n\t" \
    "v_mfma_f32_16x16x32_bf16 v[196:199], v[20:23], v[132:135], v[196:199]\n\t" \
    "ds_read_b128 v[44:47], %[xp] offset:55296\n\t" \
    "v_mfma_f32_16x16x32_bf16 v[200:203], v[20:23], v[140:143], v[200:203]\n\t" \
    "ds_read_b128 v[48:51], %[xp] offset:56320\n\t" \
    "v_mfma_f32_16x16x32_bf16 v[204:207], v[24:27], v[132:135], v[204:207]\n\t" \
    "s_waitcnt lgkmcnt(4)\n\t" \
    "v_mfma_f32_16x16x32_bf16 v[208:211], v[24:27], v[140:143], v[208:211]\n\t" \
    "v_mfma_f32_16x16x32_bf16 v[196:199], v[28:31], v[136:139], v[196:199]\n\t" \
    "ds_read_b128 v[20:23], %[xp] offset:57344\n\t" \
    "v_mfma_f32_16x16x32_bf16 v[200:203], v[28:31], v[144:147], v[200:203]\n\t" \
    "ds_read_b128 v[24:27], %[xp] offset:58368\n\t" \
    "v_mfma_f32_16x16x32_bf16 v[204:207], v[32:35], v[136:139], v[204:207]\n\t" \
    "s_waitcnt lgkmcnt(4)\n\t" \
    "v_mfma_f32_16x16x32_bf16 v[208:211], v[32:35], v[144:147], v[208:211]\n\t" \
    "v_mfma_f32_16x16x32_bf16 v[196:199], v[36:39], v[148:151], v[196:199]\n\t" \
    "ds_read_b128 v[28:31], %[xp] offset:59392\n\t" \
    "v_mfma_f32_16x16x32_bf16 v[200:203], v[36:39], v[156:159], v[200:203]\n\t" \
    "ds_read_b128 v[32:35], %[xp] offset:60416\n\t" \
    "v_mfma_f32_16x16x32_bf16 v[204:207], v[40:43], v[148:151], v[204:207]\n\t" \
    "s_waitcnt lgkmcnt(4)\n\t" \
    "v_mfma_f32_16x16x32_bf16 v[208:211], v[40:43], v[156:159], v[208:211]\n\t" \
    "v_mfma_f32_16x16x32_bf16 v[196:199], v[44:47], v[152:155], v[196:199]\n\t" \
    "ds_read_b128 v[36:39], %[xp] offset:61440\n\t" \
    "v_mfma_f32_16x16x32_bf16 v[200:203], v[44:47], v[160:163], v[200:203]\n\t" \
    "ds_read_b128 v[40:43], %[xp] offset:62464\n\t" \
    "v_mfma_f32_16x16x32_bf16 v[204:207], v[48:51], v[152:155], v[204:207]\n\t" \
    "s_waitcnt lgkmcnt(4)\n\t" \
    "v_mfma_f32_16x16x32_bf16 v[208:211], v[48:51], v[160:163], v[208:211]\n\t" \
    "v_mfma_f32_16x16x32_bf16 v[196:199], v[20:23], v[164:167], v[196:199]\n\t" \
    "ds_read_b128 v[44:47], %[xp] offset:63488\n\t" \
    "v_mfma_f32_16x16x32_bf16 v[200:203], v[20:23], v[172:175], v[200:203]\n\t" \
    "ds_read_b128 v[48:51], %[xp] offset:64512\n\t" \
    "v_mfma_f32_16x16x32_bf16 v[204:207], v[24:27], v[164:167], v[204:207]\n\t" \
    "s_waitcnt lgkmcnt(4)\n\t" \
    "v_mfma_f32_16x16x32_bf16 v[208:211], v[24:27], v[172:175], v[208:211]\n\t" \
    "v_mfma_f32_16x16x32_bf16 v[196:199], v[28:31], v[168:171], v[196:199]\n\t" \
    "ds_read_b128 v[20:23], %[xp] offset:0\n\t" \
    "v_mfma_f32_16x16x32_bf16 v[200:203], v[28:31], v[176:179], v[200:203]\n\t" \
    "ds_read_b128 v[24:27], %[xp] offset:1024\n\t" \
    "v_mfma_f32_16x16x32_bf16 v[204:207], v[32:35], v[168:171], v[204:207]\n\t" \
    "s_waitcnt lgkmcnt(4)\n\t" \
    "v_mfma_f32_16x16x32_bf16 v[208:211], v[32:35], v[176:179], v[208:211]\n\t" \
    "v_mfma_f32_16x16x32_bf16 v[196:199], v[36:39], v[180:183], v[196:199]\n\t" \
    "ds_read_b128 v[28:31], %[xp] offset:2048\n\t" \
    "v_mfma_f32_16x16x32_bf16 v[200:203], v[36:39], v[188:191], v[200:203]\n\t" \
    "ds_read_b128 v[32:35], %[xp] offset:3072\n\t" \
    "v_mfma_f32_16x16x32_bf16 v[204:207], v[40:43], v[180:183], v[204:207]\n\t" \
    "s_waitcnt lgkmcnt(4)\n\t" \
    "v_mfma_f32_16x16x32_bf16 v[208:211], v[40:43], v[188:191], v[208:211]\n\t" \
    "v_mfma_f32_16x16x32_bf16 v[196:199], v[44:47], v[184:187], v[196:199]\n\t" \
    "ds_read_b128 v[36:39], %[xp] offset:4096\n\t" \
    "v_mfma_f32_16x16x32_bf16 v[200:203], v[44:47], v[192:195], v[200:203]\n\t" \
    "ds_read_b128 v[40:43], %[xp] offset:5120\n\t" \
    "v_mfma_f32_16x16x32_bf16 v[204:207], v[48:51], v[184:187], v[204:207]\n\t" \
    "s_waitcnt lgkmcnt(4)\n\t" \
    "v_mfma_f32_16x16x32_bf16 v[208:211], v[48:51], v[192:195], v[208:211]\n\t" \
    "s_add_u32 %[sx], %[sx], 65536\n\t" \
    "s_add_u32 %[t], %[t], 1\n\t" \
    "s_waitcnt vmcnt(4)\n\t" \
    "s_barrier\n\t" \
    "s_cmp_lt_u32 %[t], %[te]\n\t" \
    "s_cbranch_scc1 L0_%=\n\t" \
    "s_cmp_lg_u32 %[fin], 0\n\t" \
    "s_cbranch_scc0 E_%=\n\t" \
    "s_nop 4\n\t" \
    "ds_write_b128 %[pa], v[196:199] offset:0\n\t" \
    "ds_write_b128 %[pa], v[200:203] offset:1024\n\t" \
    "ds_write_b128 %[pa], v[204:207] offset:2048\n\t" \
    "ds_write_b128 %[pa], v[208:211] offset:3072\n\t" \
    "s_waitcnt lgkmcnt(0)\n\t" \
    "s_barrier\n\t" \
    "s_branch E_%=\n\t" \
    "L1_%=:\n\t" \
    "s_setprio 1\n\t" \
    "s_add_u32 m0, %[sd], 49152\n\t" \
    "s_add_u32 %[a], %[sx], 81920\n\t" \
    "v_mfma_f32_16x16x32_bf16 v[0:3], v[20:23], v[68:71], 0\n\t" \
    "ds_read_b128 v[44:47], %[xp] offset:6144\n\t" \
    "v_mfma_f32_16x16x32_bf16 v[4:7], v[20:23], v[76:79], 0\n\t" \
    "ds_read_b128 v[48:51], %[xp] offset:7168\n\t" \
    "v_mfma_f32_16x16x32_bf16 v[8:11], v[24:27], v[68:71], 0\n\t" \
    "s_waitcnt lgkmcnt(4)\n\t" \
    "v_mfma_f32_16x16x32_bf16 v[12:15], v[24:27], v[76:79], 0\n\t" \
    "v_mfma_f32_16x16x32_bf16 v[0:3], v[28:31], v[72:75], v[0:3]\n\t" \
    "ds_read_b128 v[20:23], %[xp] offset:8192\n\t" \
    "v_mfma_f32_16x16x32_bf16 v[4:7], v[28:31], v[80:83], v[4:7]\n\t" \
    "ds_read_b128 v[24:27], %[xp] offset:9216\n\t" \
    "v_mfma_f32_16x16x32_bf16 v[8:11], v[32:35], v[72:75], v[8:11]\n\t" \
    "s_waitcnt lgkmcnt(4)\n\t" \
    "v_mfma_f32_16x16x32_bf16 v[12:15], v[32:35], v[80:83], v[12:15]\n\t" \
    "v_mfma_f32_16x16x32_bf16 v[0:3], v[36:39], v[84:87], v[0:3]\n\t" \
    "ds_read_b128 v[28:31], %[xp] offset:10240\n\t" \
    "v_mfma_f32_16x16x32_bf16 v[4:7], v[36:39], v[92:95], v[4:7]\n\t" \
    "ds_read_b128 v[32:35], %[xp] offset:11264\n\t" \
    "v_mfma_f32_16x16x32_bf16 v[8:11], v[40:43], v[84:87], v[8:11]\n\t" \
    "s_waitcnt lgkmcnt(4)\n\t" \
    "v_mfma_f32_16x16x32_bf16 v[12:15], v[40:43], v[92:95], v[12:15]\n\t" \
    "v_mfma_f32_16x16x32_bf16 v[0:3], v[44:47], v[88:91], v[0:3]\n\t" \
    "ds_read_b128 v[36:39], %[xp] offset:12288\n\t" \
    "v_mfma_f32_16x16x32_bf16 v[4:7], v[44:47], v[96:99], v[4:7]\n\t" \
    "ds_read_b128 v[40:43], %[xp] offset:13312\n\t" \
    "v_mfma_f32_16x16x32_bf16 v[8:11], v[48:51], v[88:91], v[8:11]\n\t" \
    "s_waitcnt lgkmcnt(4)\n\t" \
    "v_mfma_f32_16x16x32_bf16 v[12:15], v[48:51], v[96:99], v[12:15]\n\t" \
    "v_mfma_f32_16x16x32_bf16 v[0:3], v[20:23], v[100:103], v[0:3]\n\t" \
    "ds_read_b128 v[44:47], %[xp] offset:14336\n\t" \
    "v_mfma_f32_16x16x32_bf16 v[4:7], v[20:23], v[108:111], v[4:7]\n\t" \
    "ds_read_b128 v[48:51], %[xp] offset:15360\n\t" \
    "v_mfma_f32_16x16x32_bf16 v[8:11], v[24:27], v[100:103], v[8:11]\n\t" \
    "s_waitcnt lgkmcnt(4)\n\t" \
    "v_mfma_f32_16x16x32_bf16 v[12:15], v[24:27], v[108:111], v[12:15]\n\t" \
    "buffer_load_dwordx4 %[l16], %[rs], %[a] offen offset:0 sc1 lds\n\t" \
    "v_mfma_f32_16x16x32_bf16 v[0:3], v[28:31], v[104:107], v[0:3]\n\t" \
    "ds_read_b128 v[20:23], %[xp] offset:16384\n\t" \
    "v_mfma_f32_16x16x32_bf16 v[4:7], v[28:31], v[112:115], v[4:7]\n\t" \
    "ds_read_b128 v[24:27], %[xp] offset:17408\n\t" \
    "v_mfma_f32_16x16x32_bf16 v[8:11], v[32:35], v[104:107], v[8:11]\n\t" \
    "s_waitcnt lgkmcnt(4)\n\t" \
    "v_mfma_f32_16x16x32_bf16 v[12:15], v[32:35], v[112:115], v[12:15]\n\t" \
    "buffer_load_dwordx4 %[l16], %[rs], %[a] offen offset:1024 sc1 lds\n\t" \
    "v_mfma_f32_16x16x32_bf16 v[0:3], v[36:39], v[116:119], v[0:3]\n\t" \
    "ds_read_b128 v[28:31], %[xp] offset:18432\n\t" \
    "v_mfma_f32_16x16x32_bf16 v[4:7], v[36:39], v[124:127], v[4:7]\n\t" \
    "ds_read_b128 v[32:35], %[xp] offset:19456\n\t" \
    "v_mfma_f32_16x16x32_bf16 v[8:11], v[40:43], v[116:119], v[8:11]\n\t" \
    "s_waitcnt lgkmcnt(4)\n\t" \
    "v_mfma_f32_16x16x32_bf16 v[12:15], v[40:43], v[124:127], v[12:15]\n\t" \
    "buffer_load_dwordx4 %[l16], %[rs], %[a] offen offset:2048 sc1 lds\n\t" \
    "v_mfma_f32_16x16x32_bf16 v[0:3], v[44:47], v[120:123], v[0:3]\n\t" \
    "ds_read_b128 v[36:39], %[xp] offset:20480\n\t" \
    "v_mfma_f32_16x16x32_bf16 v[4:7], v[44:47], v[128:131], v[4:7]\n\t" \
    "ds_read_b128 v[40:43], %[xp] offset:21504\n\t" \
    "v_mfma_f32_16x16x32_bf16 v[8:11], v[48:51], v[120:123], v[8:11]\n\t" \
    "s_waitcnt lgkmcnt(4)\n\t" \
    "v_mfma_f32_16x16x32_bf16 v[12:15], v[48:51], v[128:131], v[12:15]\n\t" \
    "buffer_load_dwordx4 %[l16], %[rs], %[a] offen offset:3072 sc1 lds\n\t" \
    "s_waitcnt vmcnt(4)\n\t" \
    "s_barrier\n\t" \
    "s_setprio 1\n\t" \
    "s_add_u32 m0, %[sd], 0\n\t" \
    "s_add_u32 %[a], %[sx], 131072\n\t" \
    "s_add_u32 %[b], %[oo], 0x1000\n\t" \
    "s_cmp_lg_u32 %[t], 1\n\t" \
    "s_cselect_b64 %[em], -1, 0\n\t" \
    "v_mfma_f32_16x16x32_bf16 v[0:3], v[20:23], v[132:135], v[0:3]\n\t" \
    "ds_read_b128 v[44:47], %[xp] offset:22528\n\t" \
    "v_mfma_f32_16x16x32_bf16 v[4:7], v[20:23], v[140:143], v[4:7]\n\t" \
    "ds_read_b128 v[48:51], %[xp] offset:23552\n\t" \
    "v_mfma_f32_16x16x32_bf16 v[8:11], v[24:27], v[132:135], v[8:11]\n\t" \
    "ds_read_b128 v[52:55], %[pa] offset:0\n\t" \
    "s_waitcnt lgkmcnt(5)\n\t" \
    "v_mfma_f32_16x16x32_bf16 v[12:15], v[24:27], v[140:143], v[12:15]\n\t" \
    "buffer_load_dwordx4 %[l16], %[rs], %[a] offen offset:0 sc1 lds\n\t" \
    "v_mfma_f32_16x16x32_bf16 v[0:3], v[28:31], v[136:139], v[0:3]\n\t" \
    "ds_read_b128 v[20:23], %[xp] offset:24576\n\t" \
    "v_mfma_f32_16x16x32_bf16 v[4:7], v[28:31], v[144:147], v[4:7]\n\t" \
    "ds_read_b128 v[24:27], %[xp] offset:25600\n\t" \
    "v_mfma_f32_16x16x32_bf16 v[8:11], v[32:35], v[136:139], v[8:11]\n\t" \
    "ds_read_b128 v[56:59], %[pa] offset:1024\n\t" \
    "s_waitcnt lgkmcnt(6)\n\t" \
    "v_mfma_f32_16x16x32_bf16 v[12:15], v[32:35], v[144:147], v[12:15]\n\t" \
    "buffer_load_dwordx4 %[l16], %[rs], %[a] offen offset:1024 sc1 lds\n\t" \
    "v_mfma_f32_16x16x32_bf16 v[0:3], v[36:39], v[148:151], v[0:3]\n\t" \
    "ds_read_b128 v[28:31], %[xp] offset:26624\n\t" \
    "v_mfma_f32_16x16x32_bf16 v[4:7], v[36:39], v[156:159], v[4:7]\n\t" \
    "ds_read_b128 v[32:35], %[xp] offset:27648\n\t" \
    "v_mfma_f32_16x16x32_bf16 v[8:11], v[40:43], v[148:151], v[8:11]\n\t" \
    "ds_read_b128 v[60:63], %[pa] offset:2048\n\t" \
    "s_waitcnt lgkmcnt(7)\n\t" \
    "v_mfma_f32_16x16x32_bf16 v[12:15], v[40:43], v[156:159], v[12:15]\n\t" \
    "buffer_load_dwordx4 %[l16], %[rs], %[a] offen offset:2048 sc1 lds\n\t" \
    "v_mfma_f32_16x16x32_bf16 v[0:3], v[44:47], v[152:155], v[0:3]\n\t" \
    "ds_read_b128 v[36:39], %[xp] offset:28672\n\t" \
    "s_waitcnt lgkmcnt(7)\n\t" \
    "v_add_f32 v196, v196, v52\n\t" \
    "v_mfma_f32_16x16x32_bf16 v[4:7], v[44:47], v[160:163], v[4:7]\n\t" \
    "ds_read_b128 v[40:43], %[xp] offset:29696\n\t" \
    "v_add_f32 v197, v197, v53\n\t" \
    "v_mfma_f32_16x16x32_bf16 v[8:11], v[48:51], v[152:155], v[8:11]\n\t" \
    "ds_read_b128 v[64:67], %[pa] offset:3072\n\t" \
    "s_waitcnt lgkmcnt(7)\n\t" \
    "v_mfma_f32_16x16x32_bf16 v[12:15], v[48:51], v[160:163], v[12:15]\n\t" \
    "buffer_load_dwordx4 %[l16], %[rs], %[a] offen offset:3072 sc1 lds\n\t" \
    "v_add_f32 v198, v198, v54\n\t" \
    "v_add_f32 v199, v199, v55\n\t" \
    "v_mfma_f32_16x16x32_bf16 v[0:3], v[20:23], v[164:167], v[0:3]\n\t" \
    "ds_read_b128 v[44:47], %[xp] offset:30720\n\t" \
    "s_waitcnt lgkmcnt(7)\n\t" \
    "v_add_f32 v200, v200, v56\n\t" \
    "v_mfma_f32_16x16x32_bf16 v[4:7], v[20:23], v[172:175], v[4:7]\n\t" \
    "ds_read_b128 v[48:51], %[xp] offset:31744\n\t" \
    "v_add_f32 v201, v201, v57\n\t" \
    "v_mfma_f32_16x16x32_bf16 v[8:11], v[24:27], v[164:167], v[8:11]\n\t" \
    "v_add_f32 v202, v202, v58\n\t" \
    "s_waitcnt lgkmcnt(6)\n\t" \
    "v_mfma_f32_16x16x32_bf16 v[12:15], v[24:27], v[172:175], v[12:15]\n\t" \
    "v_add_f32 v203, v203, v59\n\t" \
    "s_waitcnt lgkmcnt(5)\n\t" \
    "v_add_f32 v204, v204, v60\n\t" \
    "v_add_f32 v205, v205, v61\n\t" \
    "v_mfma_f32_16x16x32_bf16 v[0:3], v[28:31], v[168:171], v[0:3]\n\t" \
    "ds_read_b128 v[20:23], %[xp] offset:32768\n\t" \
    "v_add_f32 v206, v206, v62\n\t" \
    "v_mfma_f32_16x16x32_bf16 v[4:7], v[28:31], v[176:179], v[4:7]\n\t" \
    "ds_read_b128 v[24:27], %[xp] offset:33792\n\t" \
    "v_add_f32 v207, v207, v63\n\t" \
    "v_mfma_f32_16x16x32_bf16 v[8:11], v[32:35], v[168:171], v[8:11]\n\t" \
    "s_waitcnt lgkmcnt(4)\n\t" \
    "v_add_f32 v208, v208, v64\n\t" \
    "v_mfma_f32_16x16x32_bf16 v[12:15], v[32:35], v[176:179], v[12:15]\n\t" \
    "v_add_f32 v209, v209, v65\n\t" \
    "v_add_f32 v210, v210, v66\n\t" \
    "v_add_f32 v211, v211, v67\n\t" \
    "v_mfma_f32_16x16x32_bf16 v[0:3], v[36:39], v[180:183], v[0:3]\n\t" \
    "ds_read_b128 v[28:31], %[xp] offset:34816\n\t" \
    "v_cvt_pk_bf16_f32 v52, v196, v197\n\t" \
    "v_mfma_f32_16x16x32_bf16 v[4:7], v[36:39], v[188:191], v[4:7]\n\t" \
    "ds_read_b128 v[32:35], %[xp] offset:35840\n\t" \
    "v_cvt_pk_bf16_f32 v53, v198, v199\n\t" \
    "v_mfma_f32_16x16x32_bf16 v[8:11], v[40:43], v[180:183], v[8:11]\n\t" \
    "v_cvt_pk_bf16_f32 v54, v204, v205\n\t" \
    "s_waitcnt lgkmcnt(4)\n\t" \
    "v_mfma_f32_16x16x32_bf16 v[12:15], v[40:43], v[188:191], v[12:15]\n\t" \
    "v_cvt_pk_bf16_f32 v55, v206, v207\n\t" \
    "v_cvt_pk_bf16_f32 v56, v200, v201\n\t" \
    "v_cvt_pk_bf16_f32 v57, v202, v203\n\t" \
    "v_mfma_f32_16x16x32_bf16 v[0:3], v[44:47], v[184:187], v[0:3]\n\t" \
    "ds_read_b128 v[36:39], %[xp] offset:36864\n\t" \
    "v_cvt_pk_bf16_f32 v58, v208, v209\n\t" \
    "v_mfma_f32_16x16x32_bf16 v[4:7], v[44:47], v[192:195], v[4:7]\n\t" \
    "ds_read_b128 v[40:43], %[xp] offset:37888\n\t" \
    "v_cvt_pk_bf16_f32 v59, v210, v211\n\t" \
    "v_mfma_f32_16x16x32_bf16 v[8:11], v[48:51], v[184:187], v[8:11]\n\t" \
    "s_waitcnt lgkmcnt(4)\n\t" \
    "v_mfma_f32_16x16x32_bf16 v[12:15], v[48:51], v[192:195], v[12:15]\n\t" \
    "s_mov_b64 exec, %[em]\n\t" \
    "buffer_store_dwordx4 v[52:55], %[l16], %[rs], %[oo] offen sc1\n\t" \
    "buffer_store_dwordx4 v[56:59], %[l16], %[rs], %[oo] offen offset:1024 sc1\n\t" \
    "s_mov_b64 exec, -1\n\t" \
    "s_add_u32 %[sx], %[sx], 65536\n\t" \
    "s_add_u32 %[t], %[t], 1\n\t" \
    "s_add_u32 %[oo], %[oo], 262144\n\t" \
    "s_waitcnt vmcnt(6)\n\t" \
    "s_cmp_gt_u32 %[t], " STR(SQUICK) " + 1\n\t" \
    "s_cbranch_scc1 B0_%=\n\t" \
    "s_waitcnt vmcnt(0)\n\t" \
    "B0_%=:\n\t" \
    "s_barrier\n\t" \
    "s_setprio 1\n\t" \
    "s_add_u32 m0, %[sd], 16384\n\t" \
    "s_add_u32 %[a], %[sx], 81920\n\t" \
    "v_mfma_f32_16x16x32_bf16 v[196:199], v[20:23], v[68:71], 0\n\t" \
    "ds_read_b128 v[44:47], %[xp] offset:38912\n\t" \
    "v_mfma_f32_16x16x32_bf16 v[200:203], v[20:23], v[76:79], 0\n\t" \
    "ds_read_b128 v[48:51], %[xp] offset:39936\n\t" \
    "v_mfma_f32_16x16x32_bf16 v[204:207], v[24:27], v[68:71], 0\n\t" \
    "s_waitcnt lgkmcnt(4)\n\t" \
    "v_mfma_f32_16x16x32_bf16 v[208:211], v[24:27], v[76:79], 0\n\t" \
    "v_mfma_f32_16x16x32_bf16 v[196:199], v[28:31], v[72:75], v[196:199]\n\t" \
    "ds_read_b128 v[20:23], %[xp] offset:40960\n\t" \
    "v_mfma_f32_16x16x32_bf16 v[200:203], v[28:31], v[80:83], v[200:203]\n\t" \
    "ds_read_b128 v[24:27], %[xp] offset:41984\n\t" \
    "v_mfma_f32_16x16x32_bf16 v[204:207], v[32:35], v[72:75], v[204:207]\n\t" \
    "s_waitcnt lgkmcnt(4)\n\t" \
    "v_mfma_f32_16x16x32_bf16 v[208:211], v[32:35], v[80:83], v[208:211]\n\t" \
    "v_mfma_f32_16x16x32_bf16 v[196:199], v[36:39], v[84:87], v[196:199]\n\t" \
    "ds_read_b128 v[28:31], %[xp] offset:43008\n\t" \
    "v_mfma_f32_16x16x32_bf16 v[200:203], v[36:39], v[92:95], v[200:203]\n\t" \
    "ds_read_b128 v[32:35], %[xp] offset:44032\n\t" \
    "v_mfma_f32_16x16x32_bf16 v[204:207], v[40:43], v[84:87], v[204:207]\n\t" \
    "s_waitcnt lgkmcnt(4)\n\t" \
    "v_mfma_f32_16x16x32_bf16 v[208:211], v[40:43], v[92:95], v[208:211]\n\t" \
    "v_mfma_f32_16x16x32_bf16 v[196:199], v[44:47], v[88:91], v[196:199]\n\t" \
    "ds_read_b128 v[36:39], %[xp] offset:45056\n\t" \
    "v_mfma_f32_16x16x32_bf16 v[200:203], v[44:47], v[96:99], v[200:203]\n\t" \
    "ds_read_b128 v[40:43], %[xp] offset:46080\n\t" \
    "v_mfma_f32_16x16x32_bf16 v[204:207], v[48:51], v[88:91], v[204:207]\n\t" \
    "s_waitcnt lgkmcnt(4)\n\t" \
    "v_mfma_f32_16x16x32_bf16 v[208:211], v[48:51], v[96:99], v[208:211]\n\t" \
    "v_mfma_f32_16x16x32_bf16 v[196:199], v[20:23], v[100:103], v[196:199]\n\t" \
    "ds_read_b128 v[44:47], %[xp] offset:47104\n\t" \
    "v_mfma_f32_16x16x32_bf16 v[200:203], v[20:23], v[108:111], v[200:203]\n\t" \
    "ds_read_b128 v[48:51], %[xp] offset:48128\n\t" \
    "v_mfma_f32_16x16x32_bf16 v[204:207], v[24:27], v[100:103], v[204:207]\n\t" \
    "s_waitcnt lgkmcnt(4)\n\t" \
    "v_mfma_f32_16x16x32_bf16 v[208:211], v[24:27], v[108:111], v[208:211]\n\t" \
    "buffer_load_dwordx4 %[l16], %[rs], %[a] offen offset:0 sc1 lds\n\t" \
    "v_mfma_f32_16x16x32_bf16 v[196:199], v[28:31], v[104:107], v[196:199]\n\t" \
    "ds_read_b128 v[20:23], %[xp] offset:49152\n\t" \
    "v_mfma_f32_16x16x32_bf16 v[200:203], v[28:31], v[112:115], v[200:203]\n\t" \
    "ds_read_b128 v[24:27], %[xp] offset:50176\n\t" \
    "v_mfma_f32_16x16x32_bf16 v[204:207], v[32:35], v[104:107], v[204:207]\n\t" \
    "s_waitcnt lgkmcnt(4)\n\t" \
    "v_mfma_f32_16x16x32_bf16 v[208:211], v[32:35], v[112:115], v[208:211]\n\t" \
    "buffer_load_dwordx4 %[l16], %[rs], %[a] offen offset:1024 sc1 lds\n\t" \
    "v_mfma_f32_16x16x32_bf16 v[196:199], v[36:39], v[116:119], v[196:199]\n\t" \
    "ds_read_b128 v[28:31], %[xp] offset:51200\n\t" \
    "v_mfma_f32_16x16x32_bf16 v[200:203], v[36:39], v[124:127], v[200:203]\n\t" \
    "ds_read_b128 v[32:35], %[xp] offset:52224\n\t" \
    "v_mfma_f32_16x16x32_bf16 v[204:207], v[40:43], v[116:119], v[204:207]\n\t" \
    "s_waitcnt lgkmcnt(4)\n\t" \
    "v_mfma_f32_16x16x32_bf16 v[208:211], v[40:43], v[124:127], v[208:211]\n\t" \
    "buffer_load_dwordx4 %[l16], %[rs], %[a] offen offset:2048 sc1 lds\n\t" \
    "v_mfma_f32_16x16x32_bf16 v[196:199], v[44:47], v[120:123], v[196:199]\n\t" \
    "ds_read_b128 v[36:39], %[xp] offset:53248\n\t" \
    "v_mfma_f32_16x16x32_bf16 v[200:203], v[44:47], v[128:131], v[200:203]\n\t" \
    "ds_read_b128 v[40:43], %[xp] offset:54272\n\t" \
    "v_mfma_f32_16x16x32_bf16 v[204:207], v[48:51], v[120:123], v[204:207]\n\t" \
    "s_waitcnt lgkmcnt(4)\n\t" \
    "v_mfma_f32_16x16x32_bf16 v[208:211], v[48:51], v[128:131], v[208:211]\n\t" \
    "buffer_load_dwordx4 %[l16], %[rs], %[a] offen offset:3072 sc1 lds\n\t" \
    "s_waitcnt vmcnt(4)\n\t" \
    "s_barrier\n\t" \
    "s_setprio 1\n\t" \
    "s_add_u32 m0, %[sd], 32768\n\t" \
    "s_add_u32 %[a], %[sx], 131072\n\t" \
    "s_add_u32 %[b], %[oo], 0x1000\n\t" \
    "s_cmp_lg_u32 %[t], 1\n\t" \
    "s_cselect_b64 %[em], -1, 0\n\t" \
    "v_mfma_f32_16x16x32_bf16 v[196:199], v[20:23], v[132:135], v[196:199]\n\t" \
    "ds_read_b128 v[44:47], %[xp] offset:55296\n\t" \
    "v_mfma_f32_16x16x32_bf16 v[200:203], v[20:23], v[140:143], v[200:203]\n\t" \
    "ds_read_b128 v[48:51], %[xp] offset:56320\n\t" \
    "v_mfma_f32_16x16x32_bf16 v[204:207], v[24:27], v[132:135], v[204:207]\n\t" \
    "ds_read_b128 v[52:55], %[pa] offset:0\n\t" \
    "s_waitcnt lgkmcnt(5)\n\t" \
    "v_mfma_f32_16x16x32_bf16 v[208:211], v[24:27], v[140:143], v[208:211]\n\t" \
    "buffer_load_dwordx4 %[l16], %[rs], %[a] offen offset:0 sc1 lds\n\t" \
    "v_mfma_f32_16x16x32_bf16 v[196:199], v[28:31], v[136:139], v[196:199]\n\t" \
    "ds_read_b128 v[20:23], %[xp] offset:57344\n\t" \
    "v_mfma_f32_16x16x32_bf16 v[200:203], v[28:31], v[144:147], v[200:203]\n\t" \
    "ds_read_b128 v[24:27], %[xp] offset:58368\n\t" \
    "v_mfma_f32_16x16x32_bf16 v[204:207], v[32:35], v[136:139], v[204:207]\n\t" \
    "ds_read_b128 v[56:59], %[pa] offset:1024\n\t" \
    "s_waitcnt lgkmcnt(6)\n\t" \
    "v_mfma_f32_16x16x32_bf16 v[208:211], v[32:35], v[144:147], v[208:211]\n\t" \
    "buffer_load_dwordx4 %[l16], %[rs], %[a] offen offset:1024 sc1 lds\n\t" \
    "v_mfma_f32_16x16x32_bf16 v[196:199], v[36:39], v[148:151], v[196:199]\n\t" \
    "ds_read_b128 v[28:31], %[xp] offset:59392\n\t" \
    "v_mfma_f32_16x16x32_bf16 v[200:203], v[36:39], v[156:159], v[200:203]\n\t" \
    "ds_read_b128 v[32:35], %[xp] offset:60416\n\t" \
    "v_mfma_f32_16x16x32_bf16 v[204:207], v[40:43], v[148:151], v[204:207]\n\t" \
    "ds_read_b128 v[60:63], %[pa] offset:2048\n\t" \
    "s_waitcnt lgkmcnt(7)\n\t" \
    "v_mfma_f32_16x16x32_bf16 v[208:211], v[40:43], v[156:159], v[208:211]\n\t" \
    "buffer_load_dwordx4 %[l16], %[rs], %[a] offen offset:2048 sc1 lds\n\t" \
    "v_mfma_f32_16x16x32_bf16 v[196:199], v[44:47], v[152:155], v[196:199]\n\t" \
    "ds_read_b128 v[36:39], %[xp] offset:61440\n\t" \
    "s_waitcnt lgkmcnt(7)\n\t" \
    "v_add_f32 v0, v0, v52\n\t" \
    "v_mfma_f32_16x16x32_bf16 v[200:203], v[44:47], v[160:163], v[200:203]\n\t" \
    "ds_read_b128 v[40:43], %[xp] offset:62464\n\t" \
    "v_add_f32 v1, v1, v53\n\t" \
    "v_mfma_f32_16x16x32_bf16 v[204:207], v[48:51], v[152:155], v[204:207]\n\t" \
    "ds_read_b128 v[64:67], %[pa] offset:3072\n\t" \
    "s_waitcnt lgkmcnt(7)\n\t" \
    "v_mfma_f32_16x16x32_bf16 v[208:211], v[48:51], v[160:163], v[208:211]\n\t" \
    "buffer_load_dwordx4 %[l16], %[rs], %[a] offen offset:3072 sc1 lds\n\t" \
    "v_add_f32 v2, v2, v54\n\t" \
    "v_add_f32 v3, v3, v55\n\t" \
    "v_mfma_f32_16x16x32_bf16 v[196:199], v[20:23], v[164:167], v[196:199]\n\t" \
    "ds_read_b128 v[44:47], %[xp] offset:63488\n\t" \
    "s_waitcnt lgkmcnt(7)\n\t" \
    "v_add_f32 v4, v4, v56\n\t" \
    "v_mfma_f32_16x16x32_bf16 v[200:203], v[20:23], v[172:175], v[200:203]\n\t" \
    "ds_read_b128 v[48:51], %[xp] offset:64512\n\t" \
    "v_add_f32 v5, v5, v57\n\t" \
    "v_mfma_f32_16x16x32_bf16 v[204:207], v[24:27], v[164:167], v[204:207]\n\t" \
    "v_add_f32 v6, v6, v58\n\t" \
    "s_waitcnt lgkmcnt(6)\n\t" \
    "v_mfma_f32_16x16x32_bf16 v[208:211], v[24:27], v[172:175], v[208:211]\n\t" \
    "v_add_f32 v7, v7, v59\n\t" \
    "s_waitcnt lgkmcnt(5)\n\t" \
    "v_add_f32 v8, v8, v60\n\t" \
    "v_add_f32 v9, v9, v61\n\t" \
    "v_mfma_f32_16x16x32_bf16 v[196:199], v[28:31], v[168:171], v[196:199]\n\t" \
    "ds_read_b128 v[20:23], %[xp] offset:0\n\t" \
    "v_add_f32 v10, v10, v62\n\t" \
    "v_mfma_f32_16x16x32_bf16 v[200:203], v[28:31], v[176:179], v[200:203]\n\t" \
    "ds_read_b128 v[24:27], %[xp] offset:1024\n\t" \
    "v_add_f32 v11, v11, v63\n\t" \
    "v_mfma_f32_16x16x32_bf16 v[204:207], v[32:35], v[168:171], v[204:207]\n\t" \
    "s_waitcnt lgkmcnt(4)\n\t" \
    "v_add_f32 v12, v12, v64\n\t" \
    "v_mfma_f32_16x16x32_bf16 v[208:211], v[32:35], v[176:179], v[208:211]\n\t" \
    "v_add_f32 v13, v13, v65\n\t" \
    "v_add_f32 v14, v14, v66\n\t" \
    "v_add_f32 v15, v15, v67\n\t" \
    "v_mfma_f32_16x16x32_bf16 v[196:199], v[36:39], v[180:183], v[196:199]\n\t" \
    "ds_read_b128 v[28:31], %[xp] offset:2048\n\t" \
    "v_cvt_pk_bf16_f32 v52, v0, v1\n\t" \
    "v_mfma_f32_16x16x32_bf16 v[200:203], v[36:39], v[188:191], v[200:203]\n\t" \
    "ds_read_b128 v[32:35], %[xp] offset:3072\n\t" \
    "v_cvt_pk_bf16_f32 v53, v2, v3\n\t" \
    "v_mfma_f32_16x16x32_bf16 v[204:207], v[40:43], v[180:183], v[204:207]\n\t" \
    "v_cvt_pk_bf16_f32 v54, v8, v9\n\t" \
    "s_waitcnt lgkmcnt(4)\n\t" \
    "v_mfma_f32_16x16x32_bf16 v[208:211], v[40:43], v[188:191], v[208:211]\n\t" \
    "v_cvt_pk_bf16_f32 v55, v10, v11\n\t" \
    "v_cvt_pk_bf16_f32 v56, v4, v5\n\t" \
    "v_cvt_pk_bf16_f32 v57, v6, v7\n\t" \
    "v_mfma_f32_16x16x32_bf16 v[196:199], v[44:47], v[184:187], v[196:199]\n\t" \
    "ds_read_b128 v[36:39], %[xp] offset:4096\n\t" \
    "v_cvt_pk_bf16_f32 v58, v12, v13\n\t" \
    "v_mfma_f32_16x16x32_bf16 v[200:203], v[44:47], v[192:195], v[200:203]\n\t" \
    "ds_read_b128 v[40:43], %[xp] offset:5120\n\t" \
    "v_cvt_pk_bf16_f32 v59, v14, v15\n\t" \
    "v_mfma_f32_16x16x32_bf16 v[204:207], v[48:51], v[184:187], v[204:207]\n\t" \
    "s_waitcnt lgkmcnt(4)\n\t" \
    "v_mfma_f32_16x16x32_bf16 v[208:211], v[48:51], v[192:195], v[208:211]\n\t" \
    "s_mov_b64 exec, %[em]\n\t" \
    "buffer_store_dwordx4 v[52:55], %[l16], %[rs], %[oo] offen sc1\n\t" \
    "buffer_store_dwordx4 v[56:59], %[l16], %[rs], %[oo] offen offset:1024 sc1\n\t" \
    "s_mov_b64 exec, -1\n\t" \
    "s_add_u32 %[sx], %[sx], 65536\n\t" \
    "s_add_u32 %[t], %[t], 1\n\t" \
    "s_add_u32 %[oo], %[oo], 262144\n\t" \
    "s_waitcnt vmcnt(6)\n\t" \
    "s_cmp_gt_u32 %[t], " STR(SQUICK) " + 1\n\t" \
    "s_cbranch_scc1 B1_%=\n\t" \
    "s_waitcnt vmcnt(0)\n\t" \
    "B1_%=:\n\t" \
    "s_barrier\n\t" \
    "s_cmp_lt_u32 %[t], %[te]\n\t" \
    "s_cbranch_scc1 L1_%=\n\t" \
    "s_cmp_lg_u32 %[fin], 0\n\t" \
    "s_cbranch_scc0 E_%=\n\t" \
    "s_nop 4\n\t" \
    "s_barrier\n\t" \
    "ds_read_b128 v[52:55], %[pa] offset:0\n\t" \
    "ds_read_b128 v[56:59], %[pa] offset:1024\n\t" \
    "ds_read_b128 v[60:63], %[pa] offset:2048\n\t" \
    "ds_read_b128 v[64:67], %[pa] offset:3072\n\t" \
    "s_add_u32 %[b], %[oo], 0x1000\n\t" \
    "s_waitcnt lgkmcnt(0)\n\t" \
    "v_add_f32 v196, v196, v52\n\t" \
    "v_add_f32 v197, v197, v53\n\t" \
    "v_add_f32 v198, v198, v54\n\t" \
    "v_add_f32 v199, v199, v55\n\t" \
    "v_add_f32 v200, v200, v56\n\t" \
    "v_add_f32 v201, v201, v57\n\t" \
    "v_add_f32 v202, v202, v58\n\t" \
    "v_add_f32 v203, v203, v59\n\t" \
    "v_add_f32 v204, v204, v60\n\t" \
    "v_add_f32 v205, v205, v61\n\t" \
    "v_add_f32 v206, v206, v62\n\t" \
    "v_add_f32 v207, v207, v63\n\t" \
    "v_add_f32 v208, v208, v64\n\t" \
    "v_add_f32 v209, v209, v65\n\t" \
    "v_add_f32 v210, v210, v66\n\t" \
    "v_add_f32 v211, v211, v67\n\t" \
    "v_cvt_pk_bf16_f32 v52, v196, v197\n\t" \
    "v_cvt_pk_bf16_f32 v53, v198, v199\n\t" \
    "v_cvt_pk_bf16_f32 v54, v204, v205\n\t" \
    "v_cvt_pk_bf16_f32 v55, v206, v207\n\t" \
    "v_cvt_pk_bf16_f32 v56, v200, v201\n\t" \
    "v_cvt_pk_bf16_f32 v57, v202, v203\n\t" \
    "v_cvt_pk_bf16_f32 v58, v208, v209\n\t" \
    "v_cvt_pk_bf16_f32 v59, v210, v211\n\t" \
    "buffer_store_dwordx4 v[52:55], %[l16], %[rs], %[oo] offen sc1\n\t" \
    "buffer_store_dwordx4 v[56:59], %[l16], %[rs], %[oo] offen offset:1024 sc1\n\t" \
    "E_%=:\n\t" \
    "s_setprio 0\n\t" \
    "s_waitcnt lgkmcnt(0)\n\t" \
    "s_nop 7\n\t" \
    : [t] "+s"(t), [sx] "+s"(sx), [oo] "+s"(oo), [a] "=&s"(tmp), [b] "=&s"(tmp2), [em] "=&s"(em), "+{v[20:27]}"(xf[0]), "+{v[28:35]}"(xf[1]), "+{v[36:43]}"(xf[2]), "+{v[196:203]}"(ac[0]), "+{v[204:211]}"(ac[1]) \
    : [te] "s"(TE_), [fin] "s"(FIN_), [kh] "s"(kh), [sd] "s"(sd), [rs] "s"(rs), [l16] "v"(l16), [xp] "v"(xp), [pa] "v"(pa), [fm] "s"(fm), [pm] "s"(pm), [fo] "s"(fo), [po] "s"(po), [pd] "s"(pd), [tag] "s"(P.tag), "{v[68:75]}"(w[0][0]), "{v[76:83]}"(w[0][1]), "{v[84:91]}"(w[1][0]), "{v[92:99]}"(w[1][1]), "{v[100:107]}"(w[2][0]), "{v[108:115]}"(w[2][1]), "{v[116:123]}"(w[3][0]), "{v[124:131]}"(w[3][1]), "{v[132:139]}"(w[4][0]), "{v[140:147]}"(w[4][1]), "{v[148:155]}"(w[5][0]), "{v[156:163]}"(w[5][1]), "{v[164:171]}"(w[6][0]), "{v[172:179]}"(w[6][1]), "{v[180:187]}"(w[7][0]), "{v[188:195]}"(w[7][1]) \
    : "memory", "scc", "v0", "v1", "v2", "v3", "v4", "v5", "v6", "v7", "v8", "v9", "v10", "v11", "v12", "v13", "v14", "v15", "v44", "v45", "v46", "v47", "v48", "v49", "v50", "v51", "v52", "v53", "v54", "v55", "v56", "v57", "v58", "v59", "v60", "v61", "v62", "v63", "v64", "v65", "v66", "v67")

#define S_LOOP_X(TE_, FIN_) \
  asm volatile( \
    "s_cmp_eq_u32 %[kh], 0\n\t" \
    "s_cbranch_scc0 L1_%=\n\t" \
    "L0_%=:\n\t" \
    "s_setprio 0\n\t" \
    "s_nop 3\n\t" \
    "ds_write_b128 %[pa], v[196:199] offset:0\n\t" \
    "ds_write_b128 %[pa], v[200:203] offset:1024\n\t" \
    "ds_write_b128 %[pa], v[204:207] offset:2048\n\t" \
    "ds_write_b128 %[pa], v[208:211] offset:3072\n\t" \
    "s_sub_u32 %[a], %[t], 1\n\t" \
    "v_mov_b32 v53, %[tag]\n\t" \
    "v_mov_b32 v52, %[a]\n\t" \
    "s_sub_u32 %[b], %[t], 2\n\t" \
    "s_cmp_lt_u32 %[b], " STR(SQUICK) "\n\t" \
    "s_cselect_b64 exec, %[fm], 0\n\t" \
    "buffer_store_dwordx2 v[52:53], %[l16], %[rs], %[fo] offen sc1\n\t" \
    "s_mov_b64 exec, -1\n\t" \
    "s_add_u32 m0, %[sd], 49152\n\t" \
    "s_add_u32 %[a], %[sx], 278528\n\t" \
    "s_waitcnt lgkmcnt(0)\n\t" \
    "buffer_load_dwordx4 %[l16], %[rs], %[a] offen offset:0 sc1 lds\n\t" \
    "buffer_load_dwordx4 %[l16], %[rs], %[a] offen offset:1024 sc1 lds\n\t" \
    "buffer_load_dwordx4 %[l16], %[rs], %[a] offen offset:2048 sc1 lds\n\t" \
    "buffer_load_dwordx4 %[l16], %[rs], %[a] offen offset:3072 sc1 lds\n\t" \
    "v_mfma_f32_16x16x32_bf16 v[0:3], v[68:71], v[20:23], 0\n\t" \
    "ds_read_b128 v[44:47], %[xp] offset:6144\n\t" \
    "v_mfma_f32_16x16x32_bf16 v[4:7], v[76:79], v[20:23], 0\n\t" \
    "ds_read_b128 v[48:51], %[xp] offset:7168\n\t" \
    "v_mfma_f32_16x16x32_bf16 v[8:11], v[68:71], v[24:27], 0\n\t" \
    "v_mfma_f32_16x16x32_bf16 v[12:15], v[76:79], v[24:27], 0\n\t" \
    "v_mfma_f32_16x16x32_bf16 v[0:3], v[72:75], v[28:31], v[0:3]\n\t" \
    "ds_read_b128 v[20:23], %[xp] offset:8192\n\t" \
    "v_mfma_f32_16x16x32_bf16 v[4:7], v[80:83], v[28:31], v[4:7]\n\t" \
    "ds_read_b128 v[24:27], %[xp] offset:9216\n\t" \
    "v_mfma_f32_16x16x32_bf16 v[8:11], v[72:75], v[32:35], v[8:11]\n\t" \
    "v_mfma_f32_16x16x32_bf16 v[12:15], v[80:83], v[32:35], v[12:15]\n\t" \
    "v_mfma_f32_16x16x32_bf16 v[0:3], v[84:87], v[36:39], v[0:3]\n\t" \
    "ds_read_b128 v[28:31], %[xp] offset:10240\n\t" \
    "v_mfma_f32_16x16x32_bf16 v[4:7], v[92:95], v[36:39], v[4:7]\n\t" \
    "ds_read_b128 v[32:35], %[xp] offset:11264\n\t" \
    "v_mfma_f32_16x16x32_bf16 v[8:11], v[84:87], v[40:43], v[8:11]\n\t" \
    "s_waitcnt lgkmcnt(4)\n\t" \
    "v_mfma_f32_16x16x32_bf16 v[12:15], v[92:95], v[40:43], v[12:15]\n\t" \
    "v_mfma_f32_16x16x32_bf16 v[0:3], v[88:91], v[44:47], v[0:3]\n\t" \
    "ds_read_b128 v[36:39], %[xp] offset:12288\n\t" \
    "v_mfma_f32_16x16x32_bf16 v[4:7], v[96:99], v[44:47], v[4:7]\n\t" \
    "ds_read_b128 v[40:43], %[xp] offset:13312\n\t" \
    "v_mfma_f32_16x16x32_bf16 v[8:11], v[88:91], v[48:51], v[8:11]\n\t" \
    "s_waitcnt lgkmcnt(4)\n\t" \
    "v_mfma_f32_16x16x32_bf16 v[12:15], v[96:99], v[48:51], v[12:15]\n\t" \
    "v_mfma_f32_16x16x32_bf16 v[0:3], v[100:103], v[20:23], v[0:3]\n\t" \
    "ds_read_b128 v[44:47], %[xp] offset:14336\n\t" \
    "v_mfma_f32_16x16x32_bf16 v[4:7], v[108:111], v[20:23], v[4:7]\n\t" \
    "ds_read_b128 v[48:51], %[xp] offset:15360\n\t" \
    "v_mfma_f32_16x16x32_bf16 v[8:11], v[100:103], v[24:27], v[8:11]\n\t" \
    "s_waitcnt lgkmcnt(4)\n\t" \
    "v_mfma_f32_16x16x32_bf16 v[12:15], v[108:111], v[24:27], v[12:15]\n\t" \
    "v_mfma_f32_16x16x32_bf16 v[0:3], v[104:107], v[28:31], v[0:3]\n\t" \
    "ds_read_b128 v[20:23], %[xp] offset:16384\n\t" \
    "v_mfma_f32_16x16x32_bf16 v[4:7], v[112:115], v[28:31], v[4:7]\n\t" \
    "ds_read_b128 v[24:27], %[xp] offset:17408\n\t" \
    "v_mfma_f32_16x16x32_bf16 v[8:11], v[104:107], v[32:35], v[8:11]\n\t" \
    "s_waitcnt lgkmcnt(4)\n\t" \
    "v_mfma_f32_16x16x32_bf16 v[12:15], v[112:115], v[32:35], v[12:15]\n\t" \
    "v_mfma_f32_16x16x32_bf16 v[0:3], v[116:119], v[36:39], v[0:3]\n\t" \
    "ds_read_b128 v[28:31], %[xp] offset:18432\n\t" \
    "v_mfma_f32_16x16x32_bf16 v[4:7], v[124:127], v[36:39], v[4:7]\n\t" \
    "ds_read_b128 v[32:35], %[xp] offset:19456\n\t" \
    "v_mfma_f32_16x16x32_bf16 v[8:11], v[116:119], v[40:43], v[8:11]\n\t" \
    "s_waitcnt lgkmcnt(4)\n\t" \
    "v_mfma_f32_16x16x32_bf16 v[12:15], v[124:127], v[40:43], v[12:15]\n\t" \
    "v_mfma_f32_16x16x32_bf16 v[0:3], v[120:123], v[44:47], v[0:3]\n\t" \
    "ds_read_b128 v[36:39], %[xp] offset:20480\n\t" \
    "v_mfma_f32_16x16x32_bf16 v[4:7], v[128:131], v[44:47], v[4:7]\n\t" \
    "ds_read_b128 v[40:43], %[xp] offset:21504\n\t" \
    "v_mfma_f32_16x16x32_bf16 v[8:11], v[120:123], v[48:51], v[8:11]\n\t" \
    "s_waitcnt lgkmcnt(4)\n\t" \
    "v_mfma_f32_16x16x32_bf16 v[12:15], v[128:131], v[48:51], v[12:15]\n\t" \
    "s_waitcnt vmcnt(4)\n\t" \
    "s_barrier\n\t" \
    "s_setprio 0\n\t" \
    "s_sub_u32 %[a], %[t], 1\n\t" \
    "v_mov_b32 v53, %[tag]\n\t" \
    "v_mov_b32 v52, %[a]\n\t" \
    "s_cmp_gt_u32 %[t], 1\n\t" \
    "s_cselect_b64 exec, %[fm], 0\n\t" \
    "buffer_store_dwordx2 v[52:53], %[l16], %[rs], %[fo] offen sc1\n\t" \
    "s_mov_b64 exec, %[pm]\n\t" \
    "s_mov_b32 m0, %[pd]\n\t" \
    "s_add_u32 %[a], %[sx], %[po]\n\t" \
    "buffer_load_dwordx4 %[l16], %[rs], %[a] offen sc1 lds\n\t" \
    "s_mov_b64 exec, -1\n\t" \
    "s_add_u32 m0, %[sd], 0\n\t" \
    "s_add_u32 %[a], %[sx], 524288\n\t" \
    "buffer_load_dwordx4 %[l16], %[rs], %[a] offen offset:0 sc1 lds\n\t" \
    "buffer_load_dwordx4 %[l16], %[rs], %[a] offen offset:1024 sc1 lds\n\t" \
    "buffer_load_dwordx4 %[l16], %[rs], %[a] offen offset:2048 sc1 lds\n\t" \
    "buffer_load_dwordx4 %[l16], %[rs], %[a] offen offset:3072 sc1 lds\n\t" \
    "v_mfma_f32_16x16x32_bf16 v[0:3], v[132:135], v[20:23], v[0:3]\n\t" \
    "ds_read_b128 v[44:47], %[xp] offset:22528\n\t" \
    "v_mfma_f32_16x16x32_bf16 v[4:7], v[140:143], v[20:23], v[4:7]\n\t" \
    "ds_read_b128 v[48:51], %[xp] offset:23552\n\t" \
    "v_mfma_f32_16x16x32_bf16 v[8:11], v[132:135], v[24:27], v[8:11]\n\t" \
    "s_waitcnt lgkmcnt(4)\n\t" \
    "v_mfma_f32_16x16x32_bf16 v[12:15], v[140:143], v[24:27], v[12:15]\n\t" \
    "v_mfma_f32_16x16x32_bf16 v[0:3], v[136:139], v[28:31], v[0:3]\n\t" \
    "ds_read_b128 v[20:23], %[xp] offset:24576\n\t" \
    "v_mfma_f32_16x16x32_bf16 v[4:7], v[144:147], v[28:31], v[4:7]\n\t" \
    "ds_read_b128 v[24:27], %[xp] offset:25600\n\t" \
    "v_mfma_f32_16x16x32_bf16 v[8:11], v[136:139], v[32:35], v[8:11]\n\t" \
    "s_waitcnt lgkmcnt(4)\n\t" \
    "v_mfma_f32_16x16x32_bf16 v[12:15], v[144:147], v[32:35], v[12:15]\n\t" \
    "v_mfma_f32_16x16x32_bf16 v[0:3], v[148:151], v[36:39], v[0:3]\n\t" \
    "ds_read_b128 v[28:31], %[xp] offset:26624\n\t" \
    "v_mfma_f32_16x16x32_bf16 v[4:7], v[156:159], v[36:39], v[4:7]\n\t" \
    "ds_read_b128 v[32:35], %[xp] offset:27648\n\t" \
    "v_mfma_f32_16x16x32_bf16 v[8:11], v[148:151], v[40:43], v[8:11]\n\t" \
    "s_waitcnt lgkmcnt(4)\n\t" \
    "v_mfma_f32_16x16x32_bf16 v[12:15], v[156:159], v[40:43], v[12:15]\n\t" \
    "v_mfma_f32_16x16x32_bf16 v[0:3], v[152:155], v[44:47], v[0:3]\n\t" \
    "ds_read_b128 v[36:39], %[xp] offset:28672\n\t" \
    "v_mfma_f32_16x16x32_bf16 v[4:7], v[160:163], v[44:47], v[4:7]\n\t" \
    "ds_read_b128 v[40:43], %[xp] offset:29696\n\t" \
    "v_mfma_f32_16x16x32_bf16 v[8:11], v[152:155], v[48:51], v[8:11]\n\t" \
    "s_waitcnt lgkmcnt(4)\n\t" \
    "v_mfma_f32_16x16x32_bf16 v[12:15], v[160:163], v[48:51], v[12:15]\n\t" \
    "v_mfma_f32_16x16x32_bf16 v[0:3], v[164:167], v[20:23], v[0:3]\n\t" \
    "ds_read_b128 v[44:47], %[xp] offset:30720\n\t" \
    "v_mfma_f32_16x16x32_bf16 v[4:7], v[172:175], v[20:23], v[4:7]\n\t" \
    "ds_read_b128 v[48:51], %[xp] offset:31744\n\t" \
    "v_mfma_f32_16x16x32_bf16 v[8:11], v[164:167], v[24:27], v[8:11]\n\t" \
    "s_waitcnt lgkmcnt(4)\n\t" \
    "v_mfma_f32_16x16x32_bf16 v[12:15], v[172:175], v[24:27], v[12:15]\n\t" \
    "v_mfma_f32_16x16x32_bf16 v[0:3], v[168:171], v[28:31], v[0:3]\n\t" \
    "ds_read_b128 v[20:23], %[xp] offset:32768\n\t" \
    "v_mfma_f32_16x16x32_bf16 v[4:7], v[176:179], v[28:31], v[4:7]\n\t" \
    "ds_read_b128 v[24:27], %[xp] offset:33792\n\t" \
    "v_mfma_f32_16x16x32_bf16 v[8:11], v[168:171], v[32:35], v[8:11]\n\t" \
    "s_waitcnt lgkmcnt(4)\n\t" \
    "v_mfma_f32_16x16x32_bf16 v[12:15], v[176:179], v[32:35], v[12:15]\n\t" \
    "v_mfma_f32_16x16x32_bf16 v[0:3], v[180:183], v[36:39], v[0:3]\n\t" \
    "ds_read_b128 v[28:31], %[xp] offset:34816\n\t" \
    "v_mfma_f32_16x16x32_bf16 v[4:7], v[188:191], v[36:39], v[4:7]\n\t" \
    "ds_read_b128 v[32:35], %[xp] offset:35840\n\t" \
    "v_mfma_f32_16x16x32_bf16 v[8:11], v[180:183], v[40:43], v[8:11]\n\t" \
    "s_waitcnt lgkmcnt(4)\n\t" \
    "v_mfma_f32_16x16x32_bf16 v[12:15], v[188:191], v[40:43], v[12:15]\n\t" \
    "v_mfma_f32_16x16x32_bf16 v[0:3], v[184:187], v[44:47], v[0:3]\n\t" \
    "ds_read_b128 v[36:39], %[xp] offset:36864\n\t" \
    "v_mfma_f32_16x16x32_bf16 v[4:7], v[192:195], v[44:47], v[4:7]\n\t" \
    "ds_read_b128 v[40:43], %[xp] offset:37888\n\t" \
    "v_mfma_f32_16x16x32_bf16 v[8:11], v[184:187], v[48:51], v[8:11]\n\t" \
    "s_waitcnt lgkmcnt(4)\n\t" \
    "v_mfma_f32_16x16x32_bf16 v[12:15], v[192:195], v[48:51], v[12:15]\n\t" \
    "s_add_u32 %[sx], %[sx], 262144\n\t" \
    "s_add_u32 %[t], %[t], 1\n\t" \
    "s_waitcnt vmcnt(4)\n\t" \
    "s_barrier\n\t" \
    "s_setprio 0\n\t" \
    "s_nop 3\n\t" \
    "ds_write_b128 %[pa], v[0:3] offset:0\n\t" \
    "ds_write_b128 %[pa], v[4:7] offset:1024\n\t" \
    "ds_write_b128 %[pa], v[8:11] offset:2048\n\t" \
    "ds_write_b128 %[pa], v[12:15] offset:3072\n\t" \
    "s_sub_u32 %[a], %[t], 1\n\t" \
    "v_mov_b32 v53, %[tag]\n\t" \
    "v_mov_b32 v52, %[a]\n\t" \
    "s_sub_u32 %[b], %[t], 2\n\t" \
    "s_cmp_lt_u32 %[b], " STR(SQUICK) "\n\t" \
    "s_cselect_b64 exec, %[fm], 0\n\t" \
    "buffer_store_dwordx2 v[52:53], %[l16], %[rs], %[fo] offen sc1\n\t" \
    "s_mov_b64 exec, -1\n\t" \
    "s_add_u32 m0, %[sd], 16384\n\t" \
    "s_add_u32 %[a], %[sx], 278528\n\t" \
    "s_waitcnt lgkmcnt(0)\n\t" \
    "buffer_load_dwordx4 %[l16], %[rs], %[a] offen offset:0 sc1 lds\n\t" \
    "buffer_load_dwordx4 %[l16], %[rs], %[a] offen offset:1024 sc1 lds\n\t" \
    "buffer_load_dwordx4 %[l16], %[rs], %[a] offen offset:2048 sc1 lds\n\t" \
    "buffer_load_dwordx4 %[l16], %[rs], %[a] offen offset:3072 sc1 lds\n\t" \
    "v_mfma_f32_16x16x32_bf16 v[196:199], v[68:71], v[20:23], 0\n\t" \
    "ds_read_b128 v[44:47], %[xp] offset:38912\n\t" \
    "v_mfma_f32_16x16x32_bf16 v[200:203], v[76:79], v[20:23], 0\n\t" \
    "ds_read_b128 v[48:51], %[xp] offset:39936\n\t" \
    "v_mfma_f32_16x16x32_bf16 v[204:207], v[68:71], v[24:27], 0\n\t" \
    "v_mfma_f32_16x16x32_bf16 v[208:211], v[76:79], v[24:27], 0\n\t" \
    "v_mfma_f32_16x16x32_bf16 v[196:199], v[72:75], v[28:31], v[196:199]\n\t" \
    "ds_read_b128 v[20:23], %[xp] offset:40960\n\t" \
    "v_mfma_f32_16x16x32_bf16 v[200:203], v[80:83], v[28:31], v[200:203]\n\t" \
    "ds_read_b128 v[24:27], %[xp] offset:41984\n\t" \
    "v_mfma_f32_16x16x32_bf16 v[204:207], v[72:75], v[32:35], v[204:207]\n\t" \
    "v_mfma_f32_16x16x32_bf16 v[208:211], v[80:83], v[32:35], v[208:211]\n\t" \
    "v_mfma_f32_16x16x32_bf16 v[196:199], v[84:87], v[36:39], v[196:199]\n\t" \
    "ds_read_b128 v[28:31], %[xp] offset:43008\n\t" \
    "v_mfma_f32_16x16x32_bf16 v[200:203], v[92:95], v[36:39], v[200:203]\n\t" \
    "ds_read_b128 v[32:35], %[xp] offset:44032\n\t" \
    "v_mfma_f32_16x16x32_bf16 v[204:207], v[84:87], v[40:43], v[204:207]\n\t" \
    "s_waitcnt lgkmcnt(4)\n\t" \
    "v_mfma_f32_16x16x32_bf16 v[208:211], v[92:95], v[40:43], v[208:211]\n\t" \
    "v_mfma_f32_16x16x32_bf16 v[196:199], v[88:91], v[44:47], v[196:199]\n\t" \
    "ds_read_b128 v[36:39], %[xp] offset:45056\n\t" \
    "v_mfma_f32_16x16x32_bf16 v[200:203], v[96:99], v[44:47], v[200:203]\n\t" \
    "ds_read_b128 v[40:43], %[xp] offset:46080\n\t" \
    "v_mfma_f32_16x16x32_bf16 v[204:207], v[88:91], v[48:51], v[204:207]\n\t" \
    "s_waitcnt lgkmcnt(4)\n\t" \
    "v_mfma_f32_16x16x32_bf16 v[208:211], v[96:99], v[48:51], v[208:211]\n\t" \
    "v_mfma_f32_16x16x32_bf16 v[196:199], v[100:103], v[20:23], v[196:199]\n\t" \
    "ds_read_b128 v[44:47], %[xp] offset:47104\n\t" \
    "v_mfma_f32_16x16x32_bf16 v[200:203], v[108:111], v[20:23], v[200:203]\n\t" \
    "ds_read_b128 v[48:51], %[xp] offset:48128\n\t" \
    "v_mfma_f32_16x16x32_bf16 v[204:207], v[100:103], v[24:27], v[204:207]\n\t" \
    "s_waitcnt lgkmcnt(4)\n\t" \
    "v_mfma_f32_16x16x32_bf16 v[208:211], v[108:111], v[24:27], v[208:211]\n\t" \
    "v_mfma_f32_16x16x32_bf16 v[196:199], v[104:107], v[28:31], v[196:199]\n\t" \
    "ds_read_b128 v[20:23], %[xp] offset:49152\n\t" \
    "v_mfma_f32_16x16x32_bf16 v[200:203], v[112:115], v[28:31], v[200:203]\n\t" \
    "ds_read_b128 v[24:27], %[xp] offset:50176\n\t" \
    "v_mfma_f32_16x16x32_bf16 v[204:207], v[104:107], v[32:35], v[204:207]\n\t" \
    "s_waitcnt lgkmcnt(4)\n\t" \
    "v_mfma_f32_16x16x32_bf16 v[208:211], v[112:115], v[32:35], v[208:211]\n\t" \
    "v_mfma_f32_16x16x32_bf16 v[196:199], v[116:119], v[36:39], v[196:199]\n\t" \
    "ds_read_b128 v[28:31], %[xp] offset:51200\n\t" \
    "v_mfma_f32_16x16x32_bf16 v[200:203], v[124:127], v[36:39], v[200:203]\n\t" \
    "ds_read_b128 v[32:35], %[xp] offset:52224\n\t" \
    "v_mfma_f32_16x16x32_bf16 v[204:207], v[116:119], v[40:43], v[204:207]\n\t" \
    "s_waitcnt lgkmcnt(4)\n\t" \
    "v_mfma_f32_16x16x32_bf16 v[208:211], v[124:127], v[40:43], v[208:211]\n\t" \
    "v_mfma_f32_16x16x32_bf16 v[196:199], v[120:123], v[44:47], v[196:199]\n\t" \
    "ds_read_b128 v[36:39], %[xp] offset:53248\n\t" \
    "v_mfma_f32_16x16x32_bf16 v[200:203], v[128:131], v[44:47], v[200:203]\n\t" \
    "ds_read_b128 v[40:43], %[xp] offset:54272\n\t" \
    "v_mfma_f32_16x16x32_bf16 v[204:207], v[120:123], v[48:51], v[204:207]\n\t" \
    "s_waitcnt lgkmcnt(4)\n\t" \
    "v_mfma_f32_16x16x32_bf16 v[208:211], v[128:131], v[48:51], v[208:211]\n\t" \
    "s_waitcnt vmcnt(4)\n\t" \
    "s_barrier\n\t" \
    "s_setprio 0\n\t" \
    "s_sub_u32 %[a], %[t], 1\n\t" \
    "v_mov_b32 v53, %[tag]\n\t" \
    "v_mov_b32 v52, %[a]\n\t" \
    "s_cmp_gt_u32 %[t], 1\n\t" \
    "s_cselect_b64 exec, %[fm], 0\n\t" \
    "buffer_store_dwordx2 v[52:53], %[l16], %[rs], %[fo] offen sc1\n\t" \
    "s_mov_b64 exec, %[pm]\n\t" \
    "s_mov_b32 m0, %[pd]\n\t" \
    "s_add_u32 %[a], %[sx], %[po]\n\t" \
    "buffer_load_dwordx4 %[l16], %[rs], %[a] offen sc1 lds\n\t" \
    "s_mov_b64 exec, -1\n\t" \
    "s_add_u32 m0, %[sd], 32768\n\t" \
    "s_add_u32 %[a], %[sx], 524288\n\t" \
    "buffer_load_dwordx4 %[l16], %[rs], %[a] offen offset:0 sc1 lds\n\t" \
    "buffer_load_dwordx4 %[l16], %[rs], %[a] offen offset:1024 sc1 lds\n\t" \
    "buffer_load_dwordx4 %[l16], %[rs], %[a] offen offset:2048 sc1 lds\n\t" \
    "buffer_load_dwordx4 %[l16], %[rs], %[a] offen offset:3072 sc1 lds\n\t" \
    "v_mfma_f32_16x16x32_bf16 v[196:199], v[132:135], v[20:23], v[196:199]\n\t" \
    "ds_read_b128 v[44:47], %[xp] offset:55296\n\t" \
    "v_mfma_f32_16x16x32_bf16 v[200:203], v[140:143], v[20:23], v[200:203]\n\t" \
    "ds_read_b128 v[48:51], %[xp] offset:56320\n\t" \
    "v_mfma_f32_16x16x32_bf16 v[204:207], v[132:135], v[24:27], v[204:207]\n\t" \
    "s_waitcnt lgkmcnt(4)\n\t" \
    "v_mfma_f32_16x16x32_bf16 v[208:211], v[140:143], v[24:27], v[208:211]\n\t" \
    "v_mfma_f32_16x16x32_bf16 v[196:199], v[136:139], v[28:31], v[196:199]\n\t" \
    "ds_read_b128 v[20:23], %[xp] offset:57344\n\t" \
    "v_mfma_f32_16x16x32_bf16 v[200:203], v[144:147], v[28:31], v[200:203]\n\t" \
    "ds_read_b128 v[24:27], %[xp] offset:58368\n\t" \
    "v_mfma_f32_16x16x32_bf16 v[204:207], v[136:139], v[32:35], v[204:207]\n\t" \
    "s_waitcnt lgkmcnt(4)\n\t" \
    "v_mfma_f32_16x16x32_bf16 v[208:211], v[144:147], v[32:35], v[208:211]\n\t" \
    "v_mfma_f32_16x16x32_bf16 v[196:199], v[148:151], v[36:39], v[196:199]\n\t" \
    "ds_read_b128 v[28:31], %[xp] offset:59392\n\t" \
    "v_mfma_f32_16x16x32_bf16 v[200:203], v[156:159], v[36:39], v[200:203]\n\t" \
    "ds_read_b128 v[32:35], %[xp] offset:60416\n\t" \
    "v_mfma_f32_16x16x32_bf16 v[204:207], v[148:151], v[40:43], v[204:207]\n\t" \
    "s_waitcnt lgkmcnt(4)\n\t" \
    "v_mfma_f32_16x16x32_bf16 v[208:211], v[156:159], v[40:43], v[208:211]\n\t" \
    "v_mfma_f32_16x16x32_bf16 v[196:199], v[152:155], v[44:47], v[196:199]\n\t" \
    "ds_read_b128 v[36:39], %[xp] offset:61440\n\t" \
    "v_mfma_f32_16x16x32_bf16 v[200:203], v[160:163], v[44:47], v[200:203]\n\t" \
    "ds_read_b128 v[40:43], %[xp] offset:62464\n\t" \
    "v_mfma_f32_16x16x32_bf16 v[204:207], v[152:155], v[48:51], v[204:207]\n\t" \
    "s_waitcnt lgkmcnt(4)\n\t" \
    "v_mfma_f32_16x16x32_bf16 v[208:211], v[160:163], v[48:51], v[208:211]\n\t" \
    "v_mfma_f32_16x16x32_bf16 v[196:199], v[164:167], v[20:23], v[196:199]\n\t" \
    "ds_read_b128 v[44:47], %[xp] offset:63488\n\t" \
    "v_mfma_f32_16x16x32_bf16 v[200:203], v[172:175], v[20:23], v[200:203]\n\t" \
    "ds_read_b128 v[48:51], %[xp] offset:64512\n\t" \
    "v_mfma_f32_16x16x32_bf16 v[204:207], v[164:167], v[24:27], v[204:207]\n\t" \
    "s_waitcnt lgkmcnt(4)\n\t" \
    "v_mfma_f32_16x16x32_bf16 v[208:211], v[172:175], v[24:27], v[208:211]\n\t" \
    "v_mfma_f32_16x16x32_bf16 v[196:199], v[168:171], v[28:31], v[196:199]\n\t" \
    "ds_read_b128 v[20:23], %[xp] offset:0\n\t" \
    "v_mfma_f32_16x16x32_bf16 v[200:203], v[176:179], v[28:31], v[200:203]\n\t" \
    "ds_read_b128 v[24:27], %[xp] offset:1024\n\t" \
    "v_mfma_f32_16x16x32_bf16 v[204:207], v[168:171], v[32:35], v[204:207]\n\t" \
    "s_waitcnt lgkmcnt(4)\n\t" \
    "v_mfma_f32_16x16x32_bf16 v[208:211], v[176:179], v[32:35], v[208:211]\n\t" \
    "v_mfma_f32_16x16x32_bf16 v[196:199], v[180:183], v[36:39], v[196:199]\n\t" \
    "ds_read_b128 v[28:31], %[xp] offset:2048\n\t" \
    "v_mfma_f32_16x16x32_bf16 v[200:203], v[188:191], v[36:39], v[200:203]\n\t" \
    "ds_read_b128 v[32:35], %[xp] offset:3072\n\t" \
    "v_mfma_f32_16x16x32_bf16 v[204:207], v[180:183], v[40:43], v[204:207]\n\t" \
    "s_waitcnt lgkmcnt(4)\n\t" \
    "v_mfma_f32_16x16x32_bf16 v[208:211], v[188:191], v[40:43], v[208:211]\n\t" \
    "v_mfma_f32_16x16x32_bf16 v[196:199], v[184:187], v[44:47], v[196:199]\n\t" \
    "ds_read_b128 v[36:39], %[xp] offset:4096\n\t" \
    "v_mfma_f32_16x16x32_bf16 v[200:203], v[192:195], v[44:47], v[200:203]\n\t" \
    "ds_read_b128 v[40:43], %[xp] offset:5120\n\t" \
    "v_mfma_f32_16x16x32_bf16 v[204:207], v[184:187], v[48:51], v[204:207]\n\t" \
    "s_waitcnt lgkmcnt(4)\n\t" \
    "v_mfma_f32_16x16x32_bf16 v[208:211], v[192:195], v[48:51], v[208:211]\n\t" \
    "s_add_u32 %[sx], %[sx], 262144\n\t" \
    "s_add_u32 %[t], %[t], 1\n\t" \
    "s_waitcnt vmcnt(4)\n\t" \
    "s_barrier\n\t" \
    "s_cmp_lt_u32 %[t], %[te]\n\t" \
    "s_cbranch_scc1 L0_%=\n\t" \
    "s_cmp_lg_u32 %[fin], 0\n\t" \
    "s_cbranch_scc0 E_%=\n\t" \
    "s_nop 4\n\t" \
    "ds_write_b128 %[pa], v[196:199] offset:0\n\t" \
    "ds_write_b128 %[pa], v[200:203] offset:1024\n\t" \
    "ds_write_b128 %[pa], v[204:207] offset:2048\n\t" \
    "ds_write_b128 %[pa], v[208:211] offset:3072\n\t" \
    "s_waitcnt lgkmcnt(0)\n\t" \
    "s_barrier\n\t" \
    "s_branch E_%=\n\t" \
    "L1_%=:\n\t" \
    "s_setprio 1\n\t" \
    "s_add_u32 m0, %[sd], 49152\n\t" \
    "s_add_u32 %[a], %[sx], 278528\n\t" \
    "v_mfma_f32_16x16x32_bf16 v[0:3], v[68:71], v[20:23], 0\n\t" \
    "ds_read_b128 v[44:47], %[xp] offset:6144\n\t" \
    "v_mfma_f32_16x16x32_bf16 v[4:7], v[76:79], v[20:23], 0\n\t" \
    "ds_read_b128 v[48:51], %[xp] offset:7168\n\t" \
    "v_mfma_f32_16x16x32_bf16 v[8:11], v[68:71], v[24:27], 0\n\t" \
    "s_waitcnt lgkmcnt(4)\n\t" \
    "v_mfma_f32_16x16x32_bf16 v[12:15], v[76:79], v[24:27], 0\n\t" \
    "v_mfma_f32_16x16x32_bf16 v[0:3], v[72:75], v[28:31], v[0:3]\n\t" \
    "ds_read_b128 v[20:23], %[xp] offset:8192\n\t" \
    "v_mfma_f32_16x16x32_bf16 v[4:7], v[80:83], v[28:31], v[4:7]\n\t" \
    "ds_read_b128 v[24:27], %[xp] offset:9216\n\t" \
    "v_mfma_f32_16x16x32_bf16 v[8:11], v[72:75], v[32:35], v[8:11]\n\t" \
    "s_waitcnt lgkmcnt(4)\n\t" \
    "v_mfma_f32_16x16x32_bf16 v[12:15], v[80:83], v[32:35], v[12:15]\n\t" \
    "v_mfma_f32_16x16x32_bf16 v[0:3], v[84:87], v[36:39], v[0:3]\n\t" \
    "ds_read_b128 v[28:31], %[xp] offset:10240\n\t" \
    "v_mfma_f32_16x16x32_bf16 v[4:7], v[92:95], v[36:39], v[4:7]\n\t" \
    "ds_read_b128 v[32:35], %[xp] offset:11264\n\t" \
    "v_mfma_f32_16x16x32_bf16 v[8:11], v[84:87], v[40:43], v[8:11]\n\t" \
    "s_waitcnt lgkmcnt(4)\n\t" \
    "v_mfma_f32_16x16x32_bf16 v[12:15], v[92:95], v[40:43], v[12:15]\n\t" \
    "v_mfma_f32_16x16x32_bf16 v[0:3], v[88:91], v[44:47], v[0:3]\n\t" \
    "ds_read_b128 v[36:39], %[xp] offset:12288\n\t" \
    "v_mfma_f32_16x16x32_bf16 v[4:7], v[96:99], v[44:47], v[4:7]\n\t" \
    "ds_read_b128 v[40:43], %[xp] offset:13312\n\t" \
    "v_mfma_f32_16x16x32_bf16 v[8:11], v[88:91], v[48:51], v[8:11]\n\t" \
    "s_waitcnt lgkmcnt(4)\n\t" \
    "v_mfma_f32_16x16x32_bf16 v[12:15], v[96:99], v[48:51], v[12:15]\n\t" \
    "v_mfma_f32_16x16x32_bf16 v[0:3], v[100:103], v[20:23], v[0:3]\n\t" \
    "ds_read_b128 v[44:47], %[xp] offset:14336\n\t" \
    "v_mfma_f32_16x16x32_bf16 v[4:7], v[108:111], v[20:23], v[4:7]\n\t" \
    "ds_read_b128 v[48:51], %[xp] offset:15360\n\t" \
    "v_mfma_f32_16x16x32_bf16 v[8:11], v[100:103], v[24:27], v[8:11]\n\t" \
    "s_waitcnt lgkmcnt(4)\n\t" \
    "v_mfma_f32_16x16x32_bf16 v[12:15], v[108:111], v[24:27], v[12:15]\n\t" \
    "buffer_load_dwordx4 %[l16], %[rs], %[a] offen offset:0 sc1 lds\n\t" \
    "v_mfma_f32_16x16x32_bf16 v[0:3], v[104:107], v[28:31], v[0:3]\n\t" \
    "ds_read_b128 v[20:23], %[xp] offset:16384\n\t" \
    "v_mfma_f32_16x16x32_bf16 v[4:7], v[112:115], v[28:31], v[4:7]\n\t" \
    "ds_read_b128 v[24:27], %[xp] offset:17408\n\t" \
    "v_mfma_f32_16x16x32_bf16 v[8:11], v[104:107], v[32:35], v[8:11]\n\t" \
    "s_waitcnt lgkmcnt(4)\n\t" \
    "v_mfma_f32_16x16x32_bf16 v[12:15], v[112:115], v[32:35], v[12:15]\n\t" \
    "buffer_load_dwordx4 %[l16], %[rs], %[a] offen offset:1024 sc1 lds\n\t" \
    "v_mfma_f32_16x16x32_bf16 v[0:3], v[116:119], v[36:39], v[0:3]\n\t" \
    "ds_read_b128 v[28:31], %[xp] offset:18432\n\t" \
    "v_mfma_f32_16x16x32_bf16 v[4:7], v[124:127], v[36:39], v[4:7]\n\t" \
    "ds_read_b128 v[32:35], %[xp] offset:19456\n\t" \
    "v_mfma_f32_16x16x32_bf16 v[8:11], v[116:119], v[40:43], v[8:11]\n\t" \
    "s_waitcnt lgkmcnt(4)\n\t" \
    "v_mfma_f32_16x16x32_bf16 v[12:15], v[124:127], v[40:43], v[12:15]\n\t" \
    "buffer_load_dwordx4 %[l16], %[rs], %[a] offen offset:2048 sc1 lds\n\t" \
    "v_mfma_f32_16x16x32_bf16 v[0:3], v[120:123], v[44:47], v[0:3]\n\t" \
    "ds_read_b128 v[36:39], %[xp] offset:20480\n\t" \
    "v_mfma_f32_16x16x32_bf16 v[4:7], v[128:131], v[44:47], v[4:7]\n\t" \
    "ds_read_b128 v[40:43], %[xp] offset:21504\n\t" \
    "v_mfma_f32_16x16x32_bf16 v[8:11], v[120:123], v[48:51], v[8:11]\n\t" \
    "s_waitcnt lgkmcnt(4)\n\t" \
    "v_mfma_f32_16x16x32_bf16 v[12:15], v[128:131], v[48:51], v[12:15]\n\t" \
    "buffer_load_dwordx4 %[l16], %[rs], %[a] offen offset:3072 sc1 lds\n\t" \
    "s_waitcnt vmcnt(4)\n\t" \
    "s_barrier\n\t" \
    "s_setprio 1\n\t" \
    "s_add_u32 m0, %[sd], 0\n\t" \
    "s_add_u32 %[a], %[sx], 524288\n\t" \
    "s_add_u32 %[b], %[oo], 0x1000\n\t" \
    "s_cmp_lg_u32 %[t], 0\n\t" \
    "s_cselect_b64 %[em], -1, 0\n\t" \
    "v_mfma_f32_16x16x32_bf16 v[0:3], v[132:135], v[20:23], v[0:3]\n\t" \
    "ds_read_b128 v[44:47], %[xp] offset:22528\n\t" \
    "v_mfma_f32_16x16x32_bf16 v[4:7], v[140:143], v[20:23], v[4:7]\n\t" \
    "ds_read_b128 v[48:51], %[xp] offset:23552\n\t" \
    "v_mfma_f32_16x16x32_bf16 v[8:11], v[132:135], v[24:27], v[8:11]\n\t" \
    "ds_read_b128 v[52:55], %[pa] offset:0\n\t" \
    "s_waitcnt lgkmcnt(5)\n\t" \
    "v_mfma_f32_16x16x32_bf16 v[12:15], v[140:143], v[24:27], v[12:15]\n\t" \
    "buffer_load_dwordx4 %[l16], %[rs], %[a] offen offset:0 sc1 lds\n\t" \
    "v_mfma_f32_16x16x32_bf16 v[0:3], v[136:139], v[28:31], v[0:3]\n\t" \
    "ds_read_b128 v[20:23], %[xp] offset:24576\n\t" \
    "v_mfma_f32_16x16x32_bf16 v[4:7], v[144:147], v[28:31], v[4:7]\n\t" \
    "ds_read_b128 v[24:27], %[xp] offset:25600\n\t" \
    "v_mfma_f32_16x16x32_bf16 v[8:11], v[136:139], v[32:35], v[8:11]\n\t" \
    "ds_read_b128 v[56:59], %[pa] offset:1024\n\t" \
    "s_waitcnt lgkmcnt(6)\n\t" \
    "v_mfma_f32_16x16x32_bf16 v[12:15], v[144:147], v[32:35], v[12:15]\n\t" \
    "buffer_load_dwordx4 %[l16], %[rs], %[a] offen offset:1024 sc1 lds\n\t" \
    "v_mfma_f32_16x16x32_bf16 v[0:3], v[148:151], v[36:39], v[0:3]\n\t" \
    "ds_read_b128 v[28:31], %[xp] offset:26624\n\t" \
    "v_mfma_f32_16x16x32_bf16 v[4:7], v[156:159], v[36:39], v[4:7]\n\t" \
    "ds_read_b128 v[32:35], %[xp] offset:27648\n\t" \
    "v_mfma_f32_16x16x32_bf16 v[8:11], v[148:151], v[40:43], v[8:11]\n\t" \
    "ds_read_b128 v[60:63], %[pa] offset:2048\n\t" \
    "s_waitcnt lgkmcnt(7)\n\t" \
    "v_mfma_f32_16x16x32_bf16 v[12:15], v[156:159], v[40:43], v[12:15]\n\t" \
    "buffer_load_dwordx4 %[l16], %[rs], %[a] offen offset:2048 sc1 lds\n\t" \
    "v_mfma_f32_16x16x32_bf16 v[0:3], v[152:155], v[44:47], v[0:3]\n\t" \
    "ds_read_b128 v[36:39], %[xp] offset:28672\n\t" \
    "s_waitcnt lgkmcnt(7)\n\t" \
    "v_add_f32 v196, v196, v52\n\t" \
    "v_mfma_f32_16x16x32_bf16 v[4:7], v[160:163], v[44:47], v[4:7]\n\t" \
    "ds_read_b128 v[40:43], %[xp] offset:29696\n\t" \
    "v_add_f32 v197, v197, v53\n\t" \
    "v_mfma_f32_16x16x32_bf16 v[8:11], v[152:155], v[48:51], v[8:11]\n\t" \
    "ds_read_b128 v[64:67], %[pa] offset:3072\n\t" \
    "s_waitcnt lgkmcnt(7)\n\t" \
    "v_mfma_f32_16x16x32_bf16 v[12:15], v[160:163], v[48:51], v[12:15]\n\t" \
    "buffer_load_dwordx4 %[l16], %[rs], %[a] offen offset:3072 sc1 lds\n\t" \
    "v_add_f32 v198, v198, v54\n\t" \
    "v_add_f32 v199, v199, v55\n\t" \
    "v_mfma_f32_16x16x32_bf16 v[0:3], v[164:167], v[20:23], v[0:3]\n\t" \
    "ds_read_b128 v[44:47], %[xp] offset:30720\n\t" \
    "s_waitcnt lgkmcnt(7)\n\t" \
    "v_add_f32 v200, v200, v56\n\t" \
    "v_mfma_f32_16x16x32_bf16 v[4:7], v[172:175], v[20:23], v[4:7]\n\t" \
    "ds_read_b128 v[48:51], %[xp] offset:31744\n\t" \
    "v_add_f32 v201, v201, v57\n\t" \
    "v_mfma_f32_16x16x32_bf16 v[8:11], v[164:167], v[24:27], v[8:11]\n\t" \
    "v_add_f32 v202, v202, v58\n\t" \
    "s_waitcnt lgkmcnt(6)\n\t" \
    "v_mfma_f32_16x16x32_bf16 v[12:15], v[172:175], v[24:27], v[12:15]\n\t" \
    "v_add_f32 v203, v203, v59\n\t" \
    "s_waitcnt lgkmcnt(5)\n\t" \
    "v_add_f32 v204, v204, v60\n\t" \
    "v_add_f32 v205, v205, v61\n\t" \
    "v_mfma_f32_16x16x32_bf16 v[0:3], v[168:171], v[28:31], v[0:3]\n\t" \
    "ds_read_b128 v[20:23], %[xp] offset:32768\n\t" \
    "v_add_f32 v206, v206, v62\n\t" \
    "v_mfma_f32_16x16x32_bf16 v[4:7], v[176:179], v[28:31], v[4:7]\n\t" \
    "ds_read_b128 v[24:27], %[xp] offset:33792\n\t" \
    "v_add_f32 v207, v207, v63\n\t" \
    "v_mfma_f32_16x16x32_bf16 v[8:11], v[168:171], v[32:35], v[8:11]\n\t" \
    "s_waitcnt lgkmcnt(4)\n\t" \
    "v_add_f32 v208, v208, v64\n\t" \
    "v_mfma_f32_16x16x32_bf16 v[12:15], v[176:179], v[32:35], v[12:15]\n\t" \
    "v_add_f32 v209, v209, v65\n\t" \
    "v_add_f32 v210, v210, v66\n\t" \
    "v_add_f32 v211, v211, v67\n\t" \
    "v_mfma_f32_16x16x32_bf16 v[0:3], v[180:183], v[36:39], v[0:3]\n\t" \
    "ds_read_b128 v[28:31], %[xp] offset:34816\n\t" \
    "v_mfma_f32_16x16x32_bf16 v[4:7], v[188:191], v[36:39], v[4:7]\n\t" \
    "ds_read_b128 v[32:35], %[xp] offset:35840\n\t" \
    "s_mov_b64 exec, %[em]\n\t" \
    "buffer_store_dwordx4 v[196:199], %[l16], %[rs], %[oo] offen offset:0 sc1\n\t" \
    "s_mov_b64 exec, -1\n\t" \
    "v_mfma_f32_16x16x32_bf16 v[8:11], v[180:183], v[40:43], v[8:11]\n\t" \
    "s_waitcnt lgkmcnt(4)\n\t" \
    "v_mfma_f32_16x16x32_bf16 v[12:15], v[188:191], v[40:43], v[12:15]\n\t" \
    "s_mov_b64 exec, %[em]\n\t" \
    "buffer_store_dwordx4 v[200:203], %[l16], %[rs], %[oo] offen offset:1024 sc1\n\t" \
    "s_mov_b64 exec, -1\n\t" \
    "v_mfma_f32_16x16x32_bf16 v[0:3], v[184:187], v[44:47], v[0:3]\n\t" \
    "ds_read_b128 v[36:39], %[xp] offset:36864\n\t" \
    "v_mfma_f32_16x16x32_bf16 v[4:7], v[192:195], v[44:47], v[4:7]\n\t" \
    "ds_read_b128 v[40:43], %[xp] offset:37888\n\t" \
    "s_mov_b64 exec, %[em]\n\t" \
    "buffer_store_dwordx4 v[204:207], %[l16], %[rs], %[oo] offen offset:2048 sc1\n\t" \
    "s_mov_b64 exec, -1\n\t" \
    "v_mfma_f32_16x16x32_bf16 v[8:11], v[184:187], v[48:51], v[8:11]\n\t" \
    "s_waitcnt lgkmcnt(4)\n\t" \
    "v_mfma_f32_16x16x32_bf16 v[12:15], v[192:195], v[48:51], v[12:15]\n\t" \
    "s_mov_b64 exec, %[em]\n\t" \
    "buffer_store_dwordx4 v[208:211], %[l16], %[rs], %[oo] offen offset:3072 sc1\n\t" \
    "s_mov_b64 exec, -1\n\t" \
    "s_add_u32 %[sx], %[sx], 262144\n\t" \
    "s_add_u32 %[t], %[t], 1\n\t" \
    "s_add_u32 %[oo], %[oo], 524288\n\t" \
    "s_waitcnt vmcnt(8)\n\t" \
    "s_cmp_gt_u32 %[t], " STR(SQUICK) " + 1\n\t" \
    "s_cbranch_scc1 B0_%=\n\t" \
    "s_waitcnt vmcnt(0)\n\t" \
    "B0_%=:\n\t" \
    "s_barrier\n\t" \
    "s_setprio 1\n\t" \
    "s_add_u32 m0, %[sd], 16384\n\t" \
    "s_add_u32 %[a], %[sx], 278528\n\t" \
    "v_mfma_f32_16x16x32_bf16 v[196:199], v[68:71], v[20:23], 0\n\t" \
    "ds_read_b128 v[44:47], %[xp] offset:38912\n\t" \
    "v_mfma_f32_16x16x32_bf16 v[200:203], v[76:79], v[20:23], 0\n\t" \
    "ds_read_b128 v[48:51], %[xp] offset:39936\n\t" \
    "v_mfma_f32_16x16x32_bf16 v[204:207], v[68:71], v[24:27], 0\n\t" \
    "s_waitcnt lgkmcnt(4)\n\t" \
    "v_mfma_f32_16x16x32_bf16 v[208:211], v[76:79], v[24:27], 0\n\t" \
    "v_mfma_f32_16x16x32_bf16 v[196:199], v[72:75], v[28:31], v[196:199]\n\t" \
    "ds_read_b128 v[20:23], %[xp] offset:40960\n\t" \
    "v_mfma_f32_16x16x32_bf16 v[200:203], v[80:83], v[28:31], v[200:203]\n\t" \
    "ds_read_b128 v[24:27], %[xp] offset:41984\n\t" \
    "v_mfma_f32_16x16x32_bf16 v[204:207], v[72:75], v[32:35], v[204:207]\n\t" \
    "s_waitcnt lgkmcnt(4)\n\t" \
    "v_mfma_f32_16x16x32_bf16 v[208:211], v[80:83], v[32:35], v[208:211]\n\t" \
    "v_mfma_f32_16x16x32_bf16 v[196:199], v[84:87], v[36:39], v[196:199]\n\t" \
    "ds_read_b128 v[28:31], %[xp] offset:43008\n\t" \
    "v_mfma_f32_16x16x32_bf16 v[200:203], v[92:95], v[36:39], v[200:203]\n\t" \
    "ds_read_b128 v[32:35], %[xp] offset:44032\n\t" \
    "v_mfma_f32_16x16x32_bf16 v[204:207], v[84:87], v[40:43], v[204:207]\n\t" \
    "s_waitcnt lgkmcnt(4)\n\t" \
    "v_mfma_f32_16x16x32_bf16 v[208:211], v[92:95], v[40:43], v[208:211]\n\t" \
    "v_mfma_f32_16x16x32_bf16 v[196:199], v[88:91], v[44:47], v[196:199]\n\t" \
    "ds_read_b128 v[36:39], %[xp] offset:45056\n\t" \
    "v_mfma_f32_16x16x32_bf16 v[200:203], v[96:99], v[44:47], v[200:203]\n\t" \
    "ds_read_b128 v[40:43], %[xp] offset:46080\n\t" \
    "v_mfma_f32_16x16x32_bf16 v[204:207], v[88:91], v[48:51], v[204:207]\n\t" \
    "s_waitcnt lgkmcnt(4)\n\t" \
    "v_mfma_f32_16x16x32_bf16 v[208:211], v[96:99], v[48:51], v[208:211]\n\t" \
    "v_mfma_f32_16x16x32_bf16 v[196:199], v[100:103], v[20:23], v[196:199]\n\t" \
    "ds_read_b128 v[44:47], %[xp] offset:47104\n\t" \
    "v_mfma_f32_16x16x32_bf16 v[200:203], v[108:111], v[20:23], v[200:203]\n\t" \
    "ds_read_b128 v[48:51], %[xp] offset:48128\n\t" \
    "v_mfma_f32_16x16x32_bf16 v[204:207], v[100:103], v[24:27], v[204:207]\n\t" \
    "s_waitcnt lgkmcnt(4)\n\t" \
    "v_mfma_f32_16x16x32_bf16 v[208:211], v[108:111], v[24:27], v[208:211]\n\t" \
    "buffer_load_dwordx4 %[l16], %[rs], %[a] offen offset:0 sc1 lds\n\t" \
    "v_mfma_f32_16x16x32_bf16 v[196:199], v[104:107], v[28:31], v[196:199]\n\t" \
    "ds_read_b128 v[20:23], %[xp] offset:49152\n\t" \
    "v_mfma_f32_16x16x32_bf16 v[200:203], v[112:115], v[28:31], v[200:203]\n\t" \
    "ds_read_b128 v[24:27], %[xp] offset:50176\n\t" \
    "v_mfma_f32_16x16x32_bf16 v[204:207], v[104:107], v[32:35], v[204:207]\n\t" \
    "s_waitcnt lgkmcnt(4)\n\t" \
    "v_mfma_f32_16x16x32_bf16 v[208:211], v[112:115], v[32:35], v[208:211]\n\t" \
    "buffer_load_dwordx4 %[l16], %[rs], %[a] offen offset:1024 sc1 lds\n\t" \
    "v_mfma_f32_16x16x32_bf16 v[196:199], v[116:119], v[36:39], v[196:199]\n\t" \
    "ds_read_b128 v[28:31], %[xp] offset:51200\n\t" \
    "v_mfma_f32_16x16x32_bf16 v[200:203], v[124:127], v[36:39], v[200:203]\n\t" \
    "ds_read_b128 v[32:35], %[xp] offset:52224\n\t" \
    "v_mfma_f32_16x16x32_bf16 v[204:207], v[116:119], v[40:43], v[204:207]\n\t" \
    "s_waitcnt lgkmcnt(4)\n\t" \
    "v_mfma_f32_16x16x32_bf16 v[208:211], v[124:127], v[40:43], v[208:211]\n\t" \
    "buffer_load_dwordx4 %[l16], %[rs], %[a] offen offset:2048 sc1 lds\n\t" \
    "v_mfma_f32_16x16x32_bf16 v[196:199], v[120:123], v[44:47], v[196:199]\n\t" \
    "ds_read_b128 v[36:39], %[xp] offset:53248\n\t" \
    "v_mfma_f32_16x16x32_bf16 v[200:203], v[128:131], v[44:47], v[200:203]\n\t" \
    "ds_read_b128 v[40:43], %[xp] offset:54272\n\t" \
    "v_mfma_f32_16x16x32_bf16 v[204:207], v[120:123], v[48:51], v[204:207]\n\t" \
    "s_waitcnt lgkmcnt(4)\n\t" \
    "v_mfma_f32_16x16x32_bf16 v[208:211], v[128:131], v[48:51], v[208:211]\n\t" \
    "buffer_load_dwordx4 %[l16], %[rs], %[a] offen offset:3072 sc1 lds\n\t" \
    "s_waitcnt vmcnt(4)\n\t" \
    "s_barrier\n\t" \
    "s_setprio 1\n\t" \
    "s_add_u32 m0, %[sd], 32768\n\t" \
    "s_add_u32 %[a], %[sx], 524288\n\t" \
    "s_add_u32 %[b], %[oo], 0x1000\n\t" \
    "s_cmp_lg_u32 %[t], 0\n\t" \
    "s_cselect_b64 %[em], -1, 0\n\t" \
    "v_mfma_f32_16x16x32_bf16 v[196:199], v[132:135], v[20:23], v[196:199]\n\t" \
    "ds_read_b128 v[44:47], %[xp] offset:55296\n\t" \
    "v_mfma_f32_16x16x32_bf16 v[200:203], v[140:143], v[20:23], v[200:203]\n\t" \
    "ds_read_b128 v[48:51], %[xp] offset:56320\n\t" \
    "v_mfma_f32_16x16x32_bf16 v[204:207], v[132:135], v[24:27], v[204:207]\n\t" \
    "ds_read_b128 v[52:55], %[pa] offset:0\n\t" \
    "s_waitcnt lgkmcnt(5)\n\t" \
    "v_mfma_f32_16x16x32_bf16 v[208:211], v[140:143], v[24:27], v[208:211]\n\t" \
    "buffer_load_dwordx4 %[l16], %[rs], %[a] offen offset:0 sc1 lds\n\t" \
    "v_mfma_f32_16x16x32_bf16 v[196:199], v[136:139], v[28:31], v[196:199]\n\t" \
    "ds_read_b128 v[20:23], %[xp] offset:57344\n\t" \
    "v_mfma_f32_16x16x32_bf16 v[200:203], v[144:147], v[28:31], v[200:203]\n\t" \
    "ds_read_b128 v[24:27], %[xp] offset:58368\n\t" \
    "v_mfma_f32_16x16x32_bf16 v[204:207], v[136:139], v[32:35], v[204:207]\n\t" \
    "ds_read_b128 v[56:59], %[pa] offset:1024\n\t" \
    "s_waitcnt lgkmcnt(6)\n\t" \
    "v_mfma_f32_16x16x32_bf16 v[208:211], v[144:147], v[32:35], v[208:211]\n\t" \
    "buffer_load_dwordx4 %[l16], %[rs], %[a] offen offset:1024 sc1 lds\n\t" \
    "v_mfma_f32_16x16x32_bf16 v[196:199], v[148:151], v[36:39], v[196:199]\n\t" \
    "ds_read_b128 v[28:31], %[xp] offset:59392\n\t" \
    "v_mfma_f32_16x16x32_bf16 v[200:203], v[156:159], v[36:39], v[200:203]\n\t" \
    "ds_read_b128 v[32:35], %[xp] offset:60416\n\t" \
    "v_mfma_f32_16x16x32_bf16 v[204:207], v[148:151], v[40:43], v[204:207]\n\t" \
    "ds_read_b128 v[60:63], %[pa] offset:2048\n\t" \
    "s_waitcnt lgkmcnt(7)\n\t" \
    "v_mfma_f32_16x16x32_bf16 v[208:211], v[156:159], v[40:43], v[208:211]\n\t" \
    "buffer_load_dwordx4 %[l16], %[rs], %[a] offen offset:2048 sc1 lds\n\t" \
    "v_mfma_f32_16x16x32_bf16 v[196:199], v[152:155], v[44:47], v[196:199]\n\t" \
    "ds_read_b128 v[36:39], %[xp] offset:61440\n\t" \
    "s_waitcnt lgkmcnt(7)\n\t" \
    "v_add_f32 v0, v0, v52\n\t" \
    "v_mfma_f32_16x16x32_bf16 v[200:203], v[160:163], v[44:47], v[200:203]\n\t" \
    "ds_read_b128 v[40:43], %[xp] offset:62464\n\t" \
    "v_add_f32 v1, v1, v53\n\t" \
    "v_mfma_f32_16x16x32_bf16 v[204:207], v[152:155], v[48:51], v[204:207]\n\t" \
    "ds_read_b128 v[64:67], %[pa] offset:3072\n\t" \
    "s_waitcnt lgkmcnt(7)\n\t" \
    "v_mfma_f32_16x16x32_bf16 v[208:211], v[160:163], v[48:51], v[208:211]\n\t" \
    "buffer_load_dwordx4 %[l16], %[rs], %[a] offen offset:3072 sc1 lds\n\t" \
    "v_add_f32 v2, v2, v54\n\t" \
    "v_add_f32 v3, v3, v55\n\t" \
    "v_mfma_f32_16x16x32_bf16 v[196:199], v[164:167], v[20:23], v[196:199]\n\t" \
    "ds_read_b128 v[44:47], %[xp] offset:63488\n\t" \
    "s_waitcnt lgkmcnt(7)\n\t" \
    "v_add_f32 v4, v4, v56\n\t" \
    "v_mfma_f32_16x16x32_bf16 v[200:203], v[172:175], v[20:23], v[200:203]\n\t" \
    "ds_read_b128 v[48:51], %[xp] offset:64512\n\t" \
    "v_add_f32 v5, v5, v57\n\t" \
    "v_mfma_f32_16x16x32_bf16 v[204:207], v[164:167], v[24:27], v[204:207]\n\t" \
    "v_add_f32 v6, v6, v58\n\t" \
    "s_waitcnt lgkmcnt(6)\n\t" \
    "v_mfma_f32_16x16x32_bf16 v[208:211], v[172:175], v[24:27], v[208:211]\n\t" \
    "v_add_f32 v7, v7, v59\n\t" \
    "s_waitcnt lgkmcnt(5)\n\t" \
    "v_add_f32 v8, v8, v60\n\t" \
    "v_add_f32 v9, v9, v61\n\t" \
    "v_mfma_f32_16x16x32_bf16 v[196:199], v[168:171], v[28:31], v[196:199]\n\t" \
    "ds_read_b128 v[20:23], %[xp] offset:0\n\t" \
    "v_add_f32 v10, v10, v62\n\t" \
    "v_mfma_f32_16x16x32_bf16 v[200:203], v[176:179], v[28:31], v[200:203]\n\t" \
    "ds_read_b128 v[24:27], %[xp] offset:1024\n\t" \
    "v_add_f32 v11, v11, v63\n\t" \
    "v_mfma_f32_16x16x32_bf16 v[204:207], v[168:171], v[32:35], v[204:207]\n\t" \
    "s_waitcnt lgkmcnt(4)\n\t" \
    "v_add_f32 v12, v12, v64\n\t" \
    "v_mfma_f32_16x16x32_bf16 v[208:211], v[176:179], v[32:35], v[208:211]\n\t" \
    "v_add_f32 v13, v13, v65\n\t" \
    "v_add_f32 v14, v14, v66\n\t" \
    "v_add_f32 v15, v15, v67\n\t" \
    "v_mfma_f32_16x16x32_bf16 v[196:199], v[180:183], v[36:39], v[196:199]\n\t" \
    "ds_read_b128 v[28:31], %[xp] offset:2048\n\t" \
    "v_mfma_f32_16x16x32_bf16 v[200:203], v[188:191], v[36:39], v[200:203]\n\t" \
    "ds_read_b128 v[32:35], %[xp] offset:3072\n\t" \
    "s_mov_b64 exec, %[em]\n\t" \
    "buffer_store_dwordx4 v[0:3], %[l16], %[rs], %[oo] offen offset:0 sc1\n\t" \
    "s_mov_b64 exec, -1\n\t" \
    "v_mfma_f32_16x16x32_bf16 v[204:207], v[180:183], v[40:43], v[204:207]\n\t" \
    "s_waitcnt lgkmcnt(4)\n\t" \
    "v_mfma_f32_16x16x32_bf16 v[208:211], v[188:191], v[40:43], v[208:211]\n\t" \
    "s_mov_b64 exec, %[em]\n\t" \
    "buffer_store_dwordx4 v[4:7], %[l16], %[rs], %[oo] offen offset:1024 sc1\n\t" \
    "s_mov_b64 exec, -1\n\t" \
    "v_mfma_f32_16x16x32_bf16 v[196:199], v[184:187], v[44:47], v[196:199]\n\t" \
    "ds_read_b128 v[36:39], %[xp] offset:4096\n\t" \
    "v_mfma_f32_16x16x32_bf16 v[200:203], v[192:195], v[44:47], v[200:203]\n\t" \
    "ds_read_b128 v[40:43], %[xp] offset:5120\n\t" \
    "s_mov_b64 exec, %[em]\n\t" \
    "buffer_store_dwordx4 v[8:11], %[l16], %[rs], %[oo] offen offset:2048 sc1\n\t" \
    "s_mov_b64 exec, -1\n\t" \
    "v_mfma_f32_16x16x32_bf16 v[204:207], v[184:187], v[48:51], v[204:207]\n\t" \
    "s_waitcnt lgkmcnt(4)\n\t" \
    "v_mfma_f32_16x16x32_bf16 v[208:211], v[192:195], v[48:51], v[208:211]\n\t" \
    "s_mov_b64 exec, %[em]\n\t" \
    "buffer_store_dwordx4 v[12:15], %[l16], %[rs], %[oo] offen offset:3072 sc1\n\t" \
    "s_mov_b64 exec, -1\n\t" \
    "s_add_u32 %[sx], %[sx], 262144\n\t" \
    "s_add_u32 %[t], %[t], 1\n\t" \
    "s_add_u32 %[oo], %[oo], 524288\n\t" \
    "s_waitcnt vmcnt(8)\n\t" \
    "s_cmp_gt_u32 %[t], " STR(SQUICK) " + 1\n\t" \
    "s_cbranch_scc1 B1_%=\n\t" \
    "s_waitcnt vmcnt(0)\n\t" \
    "B1_%=:\n\t" \
    "s_barrier\n\t" \
    "s_cmp_lt_u32 %[t], %[te]\n\t" \
    "s_cbranch_scc1 L1_%=\n\t" \
    "s_cmp_lg_u32 %[fin], 0\n\t" \
    "s_cbranch_scc0 E_%=\n\t" \
    "s_nop 4\n\t" \
    "s_barrier\n\t" \
    "ds_read_b128 v[52:55], %[pa] offset:0\n\t" \
    "ds_read_b128 v[56:59], %[pa] offset:1024\n\t" \
    "ds_read_b128 v[60:63], %[pa] offset:2048\n\t" \
    "ds_read_b128 v[64:67], %[pa] offset:3072\n\t" \
    "s_add_u32 %[b], %[oo], 0x1000\n\t" \
    "s_waitcnt lgkmcnt(0)\n\t" \
    "v_add_f32 v196, v196, v52\n\t" \
    "v_add_f32 v197, v197, v53\n\t" \
    "v_add_f32 v198, v198, v54\n\t" \
    "v_add_f32 v199, v199, v55\n\t" \
    "v_add_f32 v200, v200, v56\n\t" \
    "v_add_f32 v201, v201, v57\n\t" \
    "v_add_f32 v202, v202, v58\n\t" \
    "v_add_f32 v203, v203, v59\n\t" \
    "v_add_f32 v204, v204, v60\n\t" \
    "v_add_f32 v205, v205, v61\n\t" \
    "v_add_f32 v206, v206, v62\n\t" \
    "v_add_f32 v207, v207, v63\n\t" \
    "v_add_f32 v208, v208, v64\n\t" \
    "v_add_f32 v209, v209, v65\n\t" \
    "v_add_f32 v210, v210, v66\n\t" \
    "v_add_f32 v211, v211, v67\n\t" \
    "buffer_store_dwordx4 v[196:199], %[l16], %[rs], %[oo] offen offset:0 sc1\n\t" \
    "buffer_store_dwordx4 v[200:203], %[l16], %[rs], %[oo] offen offset:1024 sc1\n\t" \
    "buffer_store_dwordx4 v[204:207], %[l16], %[rs], %[oo] offen offset:2048 sc1\n\t" \
    "buffer_store_dwordx4 v[208:211], %[l16], %[rs], %[oo] offen offset:3072 sc1\n\t" \
    "E_%=:\n\t" \
    "s_setprio 0\n\t" \
    "s_waitcnt lgkmcnt(0)\n\t" \
    "s_nop 7\n\t" \
    : [t] "+s"(t), [sx] "+s"(sx), [oo] "+s"(oo), [a] "=&s"(tmp), [b] "=&s"(tmp2), [em] "=&s"(em), "+{v[20:27]}"(xf[0]), "+{v[28:35]}"(xf[1]), "+{v[36:43]}"(xf[2]), "+{v[196:203]}"(ac[0]), "+{v[204:211]}"(ac[1]) \
    : [te] "s"(TE_), [fin] "s"(FIN_), [kh] "s"(kh), [sd] "s"(sd), [rs] "s"(rs), [l16] "v"(l16), [xp] "v"(xp), [pa] "v"(pa), [fm] "s"(fm), [pm] "s"(pm), [fo] "s"(fo), [po] "s"(po), [pd] "s"(pd), [tag] "s"(P.tag), "{v[68:75]}"(w[0][0]), "{v[76:83]}"(w[0][1]), "{v[84:91]}"(w[1][0]), "{v[92:99]}"(w[1][1]), "{v[100:107]}"(w[2][0]), "{v[108:115]}"(w[2][1]), "{v[116:123]}"(w[3][0]), "{v[124:131]}"(w[3][1]), "{v[132:139]}"(w[4][0]), "{v[140:147]}"(w[4][1]), "{v[148:155]}"(w[5][0]), "{v[156:163]}"(w[5][1]), "{v[164:171]}"(w[6][0]), "{v[172:179]}"(w[6][1]), "{v[180:187]}"(w[7][0]), "{v[188:195]}"(w[7][1]) \
    : "memory", "scc", "v0", "v1", "v2", "v3", "v4", "v5", "v6", "v7", "v8", "v9", "v10", "v11", "v12", "v13", "v14", "v15", "v44", "v45", "v46", "v47", "v48", "v49", "v50", "v51", "v52", "v53", "v54", "v55", "v56", "v57", "v58", "v59", "v60", "v61", "v62", "v63", "v64", "v65", "v66", "v67")
#define AQ_PIN0() asm volatile("" : "+{v[28:35]}"(wr[0][0]), "+{v[36:43]}"(wr[0][1]))
#define AQ_PIN1() asm volatile("" : "+{v[44:51]}"(wr[1][0]), "+{v[52:59]}"(wr[1][1]))
#define AQ_PIN2() asm volatile("" : "+{v[60:67]}"(wr[2][0]), "+{v[68:75]}"(wr[2][1]))
#define AQ_PIN3() asm volatile("" : "+{v[76:83]}"(wr[3][0]), "+{v[84:91]}"(wr[3][1]))
#define AQ_PIN4() asm volatile("" : "+{v[92:99]}"(wr[4][0]), "+{v[100:107]}"(wr[4][1]))
#define AQ_PIN5() asm volatile("" : "+{v[108:115]}"(wr[5][0]), "+{v[116:123]}"(wr[5][1]))
#define AQ_PIN6() asm volatile("" : "+{v[124:131]}"(wr[6][0]), "+{v[132:139]}"(wr[6][1]))
#define AQ_PIN7() asm volatile("" : "+{v[140:147]}"(wr[7][0]), "+{v[148:155]}"(wr[7][1]))
#define AQ_PIN8() asm volatile("" : "+{v[156:163]}"(wr[8][0]), "+{v[164:171]}"(wr[8][1]))
#define AQ_PIN9() asm volatile("" : "+{v[172:179]}"(wr[9][0]), "+{v[180:187]}"(wr[9][1]))
#define AQ_PIN10() asm volatile("" : "+{v[188:195]}"(wr[10][0]), "+{v[196:203]}"(wr[10][1]))
#define AQ_PIN11() asm volatile("" : "+{v[204:211]}"(wr[11][0]), "+{v[212:219]}"(wr[11][1]))
#define AK_PIN0() asm volatile("" : "+{v[20:27]}"(wr[0][0]), "+{v[28:35]}"(wr[0][1]))
#define AK_PIN1() asm volatile("" : "+{v[36:43]}"(wr[1][0]), "+{v[44:51]}"(wr[1][1]))
#define AK_PIN2() asm volatile("" : "+{v[52:59]}"(wr[2][0]), "+{v[60:67]}"(wr[2][1]))
#define AK_PIN3() asm volatile("" : "+{v[68:75]}"(wr[3][0]), "+{v[76:83]}"(wr[3][1]))
#define AK_PIN4() asm volatile("" : "+{v[84:91]}"(wr[4][0]), "+{v[92:99]}"(wr[4][1]))
#define AK_PIN5() asm volatile("" : "+{v[100:107]}"(wr[5][0]), "+{v[108:115]}"(wr[5][1]))
#define AK_PIN6() asm volatile("" : "+{v[116:123]}"(wr[6][0]), "+{v[124:131]}"(wr[6][1]))
#define AK_PIN7() asm volatile("" : "+{v[132:139]}"(wr[7][0]), "+{v[140:147]}"(wr[7][1]))
#define AK_PIN8() asm volatile("" : "+{v[148:155]}"(wr[8][0]), "+{v[156:163]}"(wr[8][1]))
#define AK_PIN9() asm volatile("" : "+{v[164:171]}"(wr[9][0]), "+{v[172:179]}"(wr[9][1]))
#define AK_PIN10() asm volatile("" : "+{v[180:187]}"(wr[10][0]), "+{v[188:195]}"(wr[10][1]))
#define AK_PIN11() asm volatile("" : "+{v[196:203]}"(wr[11][0]), "+{v[204:211]}"(wr[11][1]))
#define V_PIN() asm volatile("" : "+{v[28:35]}"(wv[0]), "+{v[36:43]}"(wv[1]), "+{v[44:51]}"(wv[2]), "+{v[52:59]}"(wv[3]), "+{v[60:67]}"(wv[4]), "+{v[68:75]}"(wv[5]), "+{v[76:83]}"(wv[6]), "+{v[84:91]}"(wv[7]), "+{v[92:99]}"(wv[8]), "+{v[100:107]}"(wv[9]), "+{v[108:115]}"(wv[10]), "+{v[116:123]}"(wv[11]), "+{v[124:131]}"(wv[12]), "+{v[132:139]}"(wv[13]), "+{v[140:147]}"(wv[14]), "+{v[148:155]}"(wv[15]))
#define WOV_PIN0() asm volatile("" : "+{v[156:163]}"(wo[0][0][0]), "+{v[164:171]}"(wo[0][0][1]), "+{v[172:179]}"(wo[0][1][0]), "+{v[180:187]}"(wo[0][1][1]))
#define WOV_PIN1() asm volatile("" : "+{v[188:195]}"(wo[1][0][0]), "+{v[196:203]}"(wo[1][0][1]), "+{v[204:211]}"(wo[1][1][0]), "+{v[212:219]}"(wo[1][1][1]))
#define WOW_PIN0() asm volatile("" : "+{v[28:35]}"(wo[0][0][0]), "+{v[36:43]}"(wo[0][0][1]), "+{v[44:51]}"(wo[0][1][0]), "+{v[52:59]}"(wo[0][1][1]))
#define WOW_PIN1() asm volatile("" : "+{v[60:67]}"(wo[1][0][0]), "+{v[68:75]}"(wo[1][0][1]), "+{v[76:83]}"(wo[1][1][0]), "+{v[84:91]}"(wo[1][1][1]))
#define WOW_PIN2() asm volatile("" : "+{v[92:99]}"(wo[2][0][0]), "+{v[100:107]}"(wo[2][0][1]), "+{v[108:115]}"(wo[2][1][0]), "+{v[116:123]}"(wo[2][1][1]))
#define WOW_PIN3() asm volatile("" : "+{v[124:131]}"(wo[3][0][0]), "+{v[132:139]}"(wo[3][0][1]), "+{v[140:147]}"(wo[3][1][0]), "+{v[148:155]}"(wo[3][1][1]))
#define WOW_PIN4() asm volatile("" : "+{v[156:163]}"(wo[4][0][0]), "+{v[164:171]}"(wo[4][0][1]), "+{v[172:179]}"(wo[4][1][0]), "+{v[180:187]}"(wo[4][1][1]))
#define WOW_PIN5() asm volatile("" : "+{v[188:195]}"(wo[5][0][0]), "+{v[196:203]}"(wo[5][0][1]), "+{v[204:211]}"(wo[5][1][0]), "+{v[212:219]}"(wo[5][1][1]))
#define A_PIN(QW, i) do { switch (i) { case 0: if (QW) AQ_PIN0(); else AK_PIN0(); break; case 1: if (QW) AQ_PIN1(); else AK_PIN1(); break; case 2: if (QW) AQ_PIN2(); else AK_PIN2(); break; case 3: if (QW) AQ_PIN3(); else AK_PIN3(); break; case 4: if (QW) AQ_PIN4(); else AK_PIN4(); break; case 5: if (QW) AQ_PIN5(); else AK_PIN5(); break; case 6: if (QW) AQ_PIN6(); else AK_PIN6(); break; case 7: if (QW) AQ_PIN7(); else AK_PIN7(); break; case 8: if (QW) AQ_PIN8(); else AK_PIN8(); break; case 9: if (QW) AQ_PIN9(); else AK_PIN9(); break; case 10: if (QW) AQ_PIN10(); else AK_PIN10(); break; case 11: if (QW) AQ_PIN11(); else AK_PIN11(); break; } } while (0)
#define WO_PIN(VW, i) do { switch (i) { case 0: if (VW) WOV_PIN0(); else WOW_PIN0(); break; case 1: if (VW) WOV_PIN1(); else WOW_PIN1(); break; case 2: if (!VW) WOW_PIN2(); break; case 3: if (!VW) WOW_PIN3(); break; case 4: if (!VW) WOW_PIN4(); break; case 5: if (!VW) WOW_PIN5(); break; } } while (0)

__device__ __forceinline__ u32x4 ldsv(LAS unsigned char* p) { return *(volatile LAS u32x4*)p; }
#define DMA_A dman
#define DMA_V dman
#define DMA_O dmaw
#define TOUCH 0
#define TAUX 0
#define POISON4 ((u32x4){~0u, ~0u, ~0u, ~0u})
#define DLYM 0
#define DLYA 8
#define DLYK (DLYA + 8)
#define DLYO 8
#define FLA 0
#define FLB 0
#define CADM 1
#define CADO 1

#define RTC(id)
#define DCA 3
#define DCB 2
#define DL1 10
#define DL2 17
#define DLB 18
#define ADL 0
#define SN0 150
#define HELP 256
#define BORD 0
#define QT 0
__device__ __forceinline__ void track(int& est, int d, int up, int dn) { est += max(min(d - est, up), min(-dn, (d - est) >> 1)); }
__device__ __forceinline__ unsigned rfl(unsigned v) { unsigned r; asm volatile("s_nop 0\n\tv_readfirstlane_b32 %0, %1\n\ts_nop 4" : "=s"(r) : "v"(v)); return r; }
__device__ __forceinline__ void plan_wait(rsrc_t rs, unsigned fo, int s, int& lim, unsigned& tm, unsigned& d) {
  while ((d & 255u) != (unsigned)s && (lim -= DEC1) > 0) {
    __builtin_amdgcn_s_sleep(1);
    const u32x2 v = __builtin_amdgcn_raw_buffer_load_b64(rs, 0, fo, AUX_SC1);
    tm = rfl(v.x); d = rfl(v.y);
  }
  const unsigned now = (unsigned)__builtin_amdgcn_s_memrealtime() * 4u;
  tm = now + min((int)(tm - now), 2000);
  d = min(d >> 8, 1000u);
}
#define SPIN_BUDGET (1 << 21)

__device__ __forceinline__ unsigned rec_go(LAS unsigned char* w, const Ptrs& P, u32x2 f, int wid, int lane) {
  if (wid >= 6) {
    if (__any(f.y != P.tag)) f.x = flag_wait(P, wid == 6 ? F_LN0(lane) : F_CAL(lane), 0, lane);
    if (wid == 7) { const unsigned m = near_half(f.x); if (lane == 0) *(volatile LAS unsigned*)w = m; }
  }
  LBARRIER();
  return __builtin_amdgcn_readfirstlane(*(volatile LAS unsigned*)w);
}

#define SLOT(i) ((i) * 16384)
#define LA_MEM SLOT(8)
#define LA_Q SLOT(9)
#define LA_K (SLOT(9) + 2048)
#define LA_P (SLOT(9) + 4096)
#define LA_VM (SLOT(9) + 6144)
#define LA_KX (SLOT(9) + 8192)

template <bool QW>
__device__ __forceinline__ void a_wave(LAS unsigned char* lds, Ptrs& P, int h, int wid) {
  char* ws = P.ws;
  const int lane = opq(0), tid = wid * 64 + lane;
  const int j = wid & 3, g = lane >> 4, q4 = (lane & 15) >> 2, p4 = lane & 3;
  const rsrc_t rs = WS_RSRC(P);
  const rsrc_t rq = __builtin_amdgcn_make_buffer_rsrc((void*)P.Wq, 0, HD * DM * 2, RSRC_W3);
  const rsrc_t rk = __builtin_amdgcn_make_buffer_rsrc((void*)P.Wk, 0, HD * DM * 2, RSRC_W3);
  const unsigned lbase = (unsigned)(size_t)lds;
  int lim = SPIN_BUDGET;
  u32x8 wr[12][2];
  const int trow = 16 * (g >> 1) + 4 * (g & 1) + 8 * (q4 >> 1) + (q4 & 1);
  const int tsw = ((q4 & 1) | ((g & 1) << 1) | ((q4 >> 1) << 2)) << 1;
  const int toff = trow * 256 + (((4 * j + (p4 >> 1)) ^ tsw) << 4) + 8 * (p4 & 1);
  {
    unsigned dv[2];
    #pragma unroll
    for (int x = 0; x < 2; ++x) {
      const int row = 8 * wid + 4 * x + (lane >> 4);
      const int sw = ((row & 1) | (((row >> 2) & 1) << 1) | (((row >> 3) & 1) << 2)) << 1;
      dv[x] = (unsigned)(row * (HD * 2) + h * 256 + (((lane & 15) ^ sw) << 4));
    }
#define A_GRP(n) do { _Pragma("unroll") for (int i = 0; i < 8; ++i) \
      DMA_A(((n) & 1) ? rk : rq, dv[i & 1], (4 * (((n) & 1) ? ((n) >> 1) ^ ((n) >> 2) : (((n) >> 1) + 1) & 3) + (i >> 1)) * 64 * HD * 2, lbase + SLOT(4 * ((n) & 1) + (i >> 1)) + wid * 2048 + (i & 1) * 1024); } while (0)
    A_GRP(0); A_GRP(1);
    #pragma unroll
    for (int n = 0; n < 6; ++n) {
      VMW(8);
      BARRIER();
      if ((n & 1) == (QW ? 0 : 1)) {
        #pragma unroll
        for (int b = 0; b < 4; ++b) {
          LAS unsigned char* t = lds + SLOT(QW ? 0 : 4) + ((b ^ j) << 14);
          wr[4 * (n >> 1) + b][0] = rd_frag<8192, 512>(t + toff);
          wr[4 * (n >> 1) + b][1] = rd_frag<8192, 512>(t + (toff ^ 32));
        }
        #pragma unroll
        for (int b = 0; b < 4; ++b) A_PIN(QW, 4 * (n >> 1) + b);
      }
      LBARRIER();
      A_GRP(n + 2);
    }
  }
  const unsigned cb = comm_base(ws);
  u32x2 f0 = {0u, 0u};
  #pragma unroll
  for (int rd = 0; rd < 2; ++rd) {
    if (rd == 0) VMW(8);
    else {
      if (wid >= 6) f0 = ld8(rs, FLG_OFF + (wid == 6 ? F_LN0(lane) : F_CAL(lane)) * 128);
      const unsigned fv = wid == 3 ? FLG_OFF + F_PRG(min((lane >> 1) & 3, 2), h) * 128 + (lane & 1) * 4 : WS_QF + j * (16u << 20) + h * 8192 + lane * 128;
      if (QW) asm volatile("s_mov_b32 m0, %2\n\ts_nop 0\n\tbuffer_load_dword %0, %1, 0 offen sc1 lds" :: "v"(fv), "s"(rs), "s"(lbase + LA_P + (wid == 3 ? 640 : 1024)) : "memory");
      get_tag(P);
      VMWAIT();
    }
    BARRIER();
    u32x8 f[2][2];
    #pragma unroll
    for (int i = 0; i < 2; ++i) {
      f[i][0] = rd_frag<8192, 512>(lds + SLOT((QW ? 0 : 2) + 4 * rd + i) + toff);
      f[i][1] = rd_frag<8192, 512>(lds + SLOT((QW ? 0 : 2) + 4 * rd + i) + (toff ^ 32));
    }
    asm volatile("" : "+v"(f[0][0]), "+v"(f[0][1]), "+v"(f[1][0]), "+v"(f[1][1]));
    LBARRIER();
    #pragma unroll
    for (int i = 0; i < 2; ++i)
      #pragma unroll
      for (int t = 0; t < 2; ++t) {
        LAS unsigned char* p = lds + SLOT((QW ? 0 : 2) + 4 * rd + i) + j * 4096 + t * 2048 + lane * 16;
        *(LAS u32x4*)p = (u32x4){f[i][t][0], f[i][t][1], f[i][t][2], f[i][t][3]};
        *(LAS u32x4*)(p + 1024) = (u32x4){f[i][t][4], f[i][t][5], f[i][t][6], f[i][t][7]};
      }
  }
  const unsigned mask = rec_go(lds + LA_P + 128, P, f0, wid, lane);
  const int idx = (lane & 1) ? 0xEEEE : 0x4444;
  const unsigned ab_a = lbase + LA_MEM + lane * 16, am_a = ab_a + j * 1024;
  const unsigned t0_a = lbase + SLOT((QW ? 0 : 4) + j) + j * 4096 + lane * 16;
  const unsigned iw_a = lbase + (QW ? LA_Q : LA_K) + j * 512 + q4 * 128 + g * 32 + p4 * 4;
  const unsigned qr_a = lbase + LA_Q + g * 128 + (lane & 7) * 16;
  const unsigned kr_a = lbase + LA_K + g * 128 + (2 * q4 + (lane & 1)) * 16;
  const unsigned fr_a = lbase + LA_P + p4 * 4;
  if (tid < 12) *(LAS unsigned*)(lds + LA_P + tid * 4) = 0u;
  const unsigned vo_v = VB_AT(h) + j * 512 + lane * 4, md = lbase + LA_VM - VB_AT(h);
  const unsigned oo_v = OB_AT((lane >> 2) & 1, h) + (j * 16 + (g >> 1) * 8 + p4 * 2 + (g & 1)) * 16;
  const unsigned long long onej = 1ull << j;
  const unsigned lkx = lbase + LA_KX + j * 2048;
  u32x8 oxr = {};
  const unsigned xo_v = (lane >> 5) * 2048 + (lane & 31) * 16, vl_a = lbase + LA_VM + (j & 1) * 1024 + lane * 4;
  const int dq = 16 * (j >> 1) + (lane & 15) - 4 * g;
  const unsigned fb = step_base(cb, mask, 0);
  unsigned tgt = (unsigned)__builtin_amdgcn_s_memrealtime() * 4u;
  unsigned long long tp = 0;
  unsigned pt = 0, pd = 0;
  const unsigned long long fmask = __builtin_amdgcn_readfirstlane((16u >> wid) & 1u);
  int known = 0;
  __builtin_amdgcn_s_setprio(QW ? 2 : 1);
  LBARRIER();
  report(P, wid);
  TL(72);
  for (int s = 0; s < NSEG; ++s) {
    const unsigned sb = step_base(cb, mask, s);
    const unsigned sbm = sb - (lbase + LA_MEM);
    const unsigned fo = (unsigned)(s * NH + h) * 8192u + j * 2048 - (lbase + LA_MEM);
    GSDECL;
    RTC(h);
    lim = __builtin_amdgcn_readfirstlane(lim);
    if (QW) {
      if (wid == 3 && s > 0) {
        if (opq(0) == 0) __builtin_amdgcn_raw_buffer_store_b32((unsigned)tp * 4u - tgt, rs, 0, fb + FL_O(h), AUX_SC1);
        plan_wait(rs, fb + FL_T(s), s, lim, pt, pd);
        tgt = pt;
      }
      if (s >= known) {
        const u32x2 v = *(volatile LAS u32x2*)(lds + LA_P + 640 + (opq(0) & 3) * 8);
        if (!__any(v.y != P.tag)) known = rfl(min(min(__builtin_amdgcn_readlane(v.x, 0), __builtin_amdgcn_readlane(v.x, 1)), __builtin_amdgcn_readlane(v.x, 2)));
      }
      kv_wait(P, s, h, known, opq(0));
      const unsigned kxo = WS_KF + fo, vxo = WS_VF + fo;
      if (wid < 3) for (int i = 0; i < ADL; ++i) __builtin_amdgcn_s_sleep(1);
      dmas(rs, ab_a, kxo, lkx);
      dmas(rs, ab_a, kxo + 1024, lkx + 1024);
      dmas(rs, ab_a, WS_QF + fo, lbase + LA_P + 1024);
      dmas(rs, ab_a, WS_QF + fo + 1024, lbase + LA_P + 1024);
      if (wid == 2 && s + 1 >= known) {
        const int ln = opq(0);
        const unsigned fv = FLG_OFF + F_PRG(min((ln >> 1) & 3, 2), h) * 128 + (ln & 1) * 4;
        asm volatile("s_mov_b32 m0, %2\n\ts_nop 0\n\tbuffer_load_dword %0, %1, 0 offen sc1 lds" :: "v"(fv), "s"(rs), "s"(lbase + LA_P + 640) : "memory");
      }
      unsigned rg = 0x3c000000u;
      A_MAIN_Q();
      TL(s);
      GSFLUSH(0, 24 + wid); GSFLUSH(1, 4 + wid); GSFLUSH(2, 12 + j); GSFLUSH(3, 16 + j); GSFLUSH(4, 20 + j);
      if (s + PD < NSEG && !(opq(0) & 8)) __builtin_amdgcn_raw_buffer_store_b128(poison4(), rs, oo_v, step_base(cb, mask, s + PD), AUX_SC1);
    } else {
      const unsigned qxo = WS_QF + fo - (j & 1) * 2048, oxo = WS_OX + (unsigned)(max(s - 1, 0) * NH + h) * 8192u + (j & 1) * 4096 + (j >> 1) * 1024;
      A_MAIN_K();
      if (wid == 7) GSFLUSH(0, 28);
      GSFLUSH(1, 4 + wid);
    }
  }
  if (!QW) {
    const unsigned oxo = WS_OX + (unsigned)((NSEG - 1) * NH + h) * 8192u + (j & 1) * 4096 + (j >> 1) * 1024 + xo_v;
    st16(rs, oxo, (u32x4){oxr[0], oxr[1], oxr[2], oxr[3]});
    st16(rs, oxo + 512, (u32x4){oxr[4], oxr[5], oxr[6], oxr[7]});
  }
  VMWAIT();
  BARRIER();
  if (wid == 4) flag_set(P, F_PRG(4, h), NSEG, opq(0));
}

#define TSL 16
#define LB_MEM 0
#define LB_RED 16384
template <bool VW>
__device__ __forceinline__ void b_wave(LAS unsigned char* lds, Ptrs& P, int r, int c, int wid, int bs, int xg) {
  constexpr int NHD = VW ? 2 : 6;
  char* ws = P.ws;
  const int lane = opq(0), tid = wid * 64 + lane;
  const int j = wid & 3, g = lane >> 4, q4 = (lane & 15) >> 2, p4 = lane & 3;
  const int b = r * 32 + c;
  const rsrc_t rs = WS_RSRC(P);
  const rsrc_t rv = __builtin_amdgcn_make_buffer_rsrc((void*)P.Wv, 0, HD * DM * 2, RSRC_W3);
  const rsrc_t ro = __builtin_amdgcn_make_buffer_rsrc((void*)P.Wo, 0, HD * DM * 2, RSRC_W3);
  const unsigned lbase = (unsigned)(size_t)lds;
  int lim = SPIN_BUDGET;
  const int h0 = ((VW ? 2 * j : 8 + 6 * j) + 5 * c) & 31;
  u32x8 wv[16], wo[6][2][2];
  {
    if ((wid & 3) < 2 && xg < 2) {
      const unsigned nk = 8 + 2 * xg, M = (bs * 2 + (wid & 1)) * 64 + lane, row = M / nk, k = M - row * nk;
      if (row < HELP) touch(VW ? P.Wq : P.Wk, HD * DM * 2, (row < 256 ? row + (VW ? 512 : 256) : row + 512) * (HD * 2) + min(xg * 4096 + (k >> 1) * 1024 + 256, 7936u) + (k & 1) * 128, lds);
    }
    const int nl = xg == 2 ? 6 : 5;
    #pragma unroll
    for (int i = 0; i < 2; ++i) {
      const unsigned L = bs * 1024 + wid * 128 + i * 64 + lane, row = L / nl;
      touch(P.Wo, HD * DM * 2, row * (DM * 2) + (c - (bs >> 1)) * 64 + (L - row * nl) * 128, lds);
    }
    VMWAIT();
    BARRIER();
    const int pos = lane >> 2;
    const int prow = (pos & 9) | ((pos & 2) << 1) | ((pos & 4) >> 1);
    const unsigned dv0 = (unsigned)(prow * (DM * 2) + c * 64 + ((lane & 3) << 4));
    const unsigned dv1 = (unsigned)(prow * (DM * 2) + c * 64 + (((lane & 3) ^ 2) << 4));
    const int toff = wid * 16384 + (g * 16 + q4) * 64 + (((p4 >> 1) ^ ((g & 1) << 1)) << 4) + 8 * (p4 & 1);
    #pragma unroll
    for (int rd = 0; rd < NHD / 2; ++rd) {
      #pragma unroll
      for (int hh = 0; hh < 2; ++hh) {
        const int h = (h0 + 2 * rd + hh) & 31;
        #pragma unroll
        for (int i = 0; i < 8; ++i)
          DMA_O(ro, (i & 1) ? dv1 : dv0, (unsigned)(h * 128 + i * 16) * (DM * 2), lbase + wid * 16384 + hh * 8192 + i * 1024);
      }
      VMWAIT();
      #pragma unroll
      for (int hh = 0; hh < 2; ++hh)
        #pragma unroll
        for (int ks = 0; ks < 2; ++ks)
          #pragma unroll
          for (int t = 0; t < 2; ++t)
            wo[2 * rd + hh][ks][t] = rd_frag<4096, 256>(lds + (toff ^ (t * 32)) + hh * 8192 + ks * 512);
      #pragma unroll
      for (int hh = 0; hh < 2; ++hh)
        WO_PIN(VW, 2 * rd + hh);
      asm volatile("s_waitcnt lgkmcnt(0)" ::: "memory");
    }
    const int lr = lane >> 3;
    #pragma unroll
    for (int i = 0; i < 16; ++i) {
      const int sw = (((lr >> 2) & 1) | ((i & 1) << 1)) << 1;
      const unsigned dv = (unsigned)(lr * (HD * 2) + b * 128 + (((lane & 7) ^ sw) << 4));
      DMA_V(rv, dv, (unsigned)(wid * 128 + i * 8) * (HD * 2), lbase + wid * 16384 + i * 1024);
    }
    VMWAIT();
    BARRIER();
    if (VW) {
      const int trow = 16 * (g >> 1) + 4 * (g & 1) + 8 * (q4 >> 1) + (q4 & 1);
      const int tsw = ((g & 1) | ((q4 >> 1) << 1)) << 1;
      LAS unsigned char* tv = lds + trow * 128 + (((2 * j + (p4 >> 1)) ^ tsw) << 4) + 8 * (p4 & 1);
      #pragma unroll
      for (int ks = 0; ks < 16; ++ks) wv[ks] = rd_frag<4096, 256>(tv + ks * 8192);
      V_PIN();
    }
    LBARRIER();
  }
  const unsigned cb = comm_base(ws);
  get_tag(P);
  const unsigned mask = rec_go(lds + LB_RED + 4096 + 64, P, (u32x2){0u, P.tag - 1u}, wid, lane);
  const int idx = (lane & 1) ? 0xEEEE : 0x4444;
  const unsigned am_a = lbase + LB_MEM + lane * 16;
  const unsigned fb = step_base(cb, mask, 0);
  volatile LAS unsigned* tsl = (volatile LAS unsigned*)(lds + LB_RED + 4096);
  if (tid < 8) tsl[tid] = tid ? 0u : (unsigned)__builtin_amdgcn_s_memrealtime() * 4u;
  unsigned dn = 600;
  int da = 570, gb = 360;
  if (VW) __builtin_amdgcn_s_setprio(2);
  LBARRIER();
  report(P, wid);
  TL(72);
  for (int s = 0; s < NSEG; ++s) {
    const unsigned sb = step_base(cb, mask, s);
    const int ln = opq(lane), gg = ln >> 4;
    GSDECL;
    RTC(32 + b);
    const int l16 = ln * 16;
    const int xc = 32 * (c & 1) + 2 * (ln & 15);
    const unsigned mo_v = MF_AT(c >> 1) + ((xc >> 4) * 16 + (4 * r + gg) * 2 + ((xc >> 3) & 1)) * 16 + (xc & 7) * 2;
    if (!VW) {
      const unsigned tm = __builtin_amdgcn_readfirstlane(*tsl), tgt = tm + DLB, la = lbase + LB_MEM + j * 2048 + l16, mo = sb + j * 2048;
      B_POLL();
      if (wid == 7) {
        const int late = (int)((unsigned)__builtin_amdgcn_s_memrealtime() * 4u - tgt) - SN0;
        const unsigned to = tm + dn + (late > 80 ? late : 0);
        if (ln == 0) tsl[1] = to;
        if (b == 0) {
          dn = da + DL1;
          if (ln == 0) { *tsl = to + gb + DL2; st8(rs, fb + FL_T(s + 1), (u32x2){to + gb + DL2, (dn << 8) | (s + 1)}); }
          const unsigned va = ld4(rs, fb + FL_O(ln & 31)), vb = ld4(rs, fb + FL_B(ln));
          if (s > 2) {
            track(da, rfl(wmax(va)), DCA, DCB);
            track(gb, rfl(wmax(vb)), DCA, DCB);
          }
        }
      }
      if (wid == 7 && s > 0 && s + 2 < NSEG) __builtin_amdgcn_raw_buffer_store_b32(~0u, rs, mo_v, step_base(cb, mask, s + 2), AUX_SC1);
    } else {
      GS(3);
      const unsigned vo = VB_AT(b >> 1) + ((b & 1) * 4 + j) * 256;
      V_PROJ();
      GS(0); GSFLUSH(0, 4 + j);
      if (s + PD < NSEG && ln < 16) st16(rs, step_base(cb, mask, s + PD) + vo + l16, poison4());
    }
    GSFLUSH(3, 24 + wid);
    if (s + 1 == NSEG) break;
    unsigned so[6];
    #pragma unroll
    for (int i = 0; i < NHD; ++i) { const int h = (h0 + i) & 31; so[i] = __builtin_amdgcn_readfirstlane(sb + OB_AT(r, h)); }
    const unsigned sn = __builtin_amdgcn_readfirstlane(step_base(cb, mask, s + 1));
    const unsigned rw_a = lbase + LB_RED + (wid * 128 + (gg & 1) * 64 + (gg >> 1) * 16 + (ln & 15)) * 4;
    const unsigned rr_a = lbase + LB_RED + ln * 8;
    if (wid == 7) {
      if (b != 0) {
        __builtin_amdgcn_s_sleep(4);
        unsigned tm = 0;
        dn = 0;
        plan_wait(rs, fb + FL_T(s + 1), s + 1, lim, tm, dn);
        if (ln == 0) *tsl = tm;
      }
      const unsigned tgt = rfl(tsl[1]);
      T_WAIT();
    }
    lim = __builtin_amdgcn_readfirstlane(lim);
    unsigned long long t1;
    if (VW) WO_V(); else WO_W();
    if (wid == 0) {
      if (ln == 0) st4(rs, fb + FL_B(b), (unsigned)t1 * 4u - tsl[1]);
    }
    if (wid == 0) {
      GSFLUSH(2, 16);
      TL(s);
    }
    GSFLUSH(1, 8 + wid);
    if (wid == 0 || wid == 4) { GSFLUSH(4, 17 + (wid >> 2)); GSFLUSH(0, 19 + (wid >> 2)); }
  }
  __builtin_amdgcn_s_setprio(0);
  const int ln = opq(0);
  TL(64);
  volatile LAS int* rdy = (volatile LAS int*)(lds + 98304 + 3 * 16384);
  if (tid == 0) *rdy = XS3 + 1;
  LBARRIER();
#define T_SRC(sg, i) (WS_OX + (unsigned)((sg) * NH + ((h0 + (i)) & 31)) * 8192u + r * 1024)
#define T_MMA(i, k, f) do { const u32x8 w0 = wo[i][(k) & 1][0], w1 = wo[i][(k) & 1][1]; const int e = ((k) >> 1) * 4; \
    a0 = MFMA32(((u32x4){w0[e], w0[e + 1], w0[e + 2], w0[e + 3]}), f, a0); a1 = MFMA32(((u32x4){w1[e], w1[e + 1], w1[e + 2], w1[e + 3]}), f, a1); } while (0)
#define T_DMA(sg, i) do { _Pragma("unroll") for (int k = 0; k < 4; ++k) dmas(rs, ln * 16, T_SRC(sg, i) + k * 2048, lbase + j * 24576 + (i) * 4096 + k * 1024); } while (0)
  bool ahead = false;
  f32x4 k0, k1;
  #pragma unroll 1
  for (int sg = XS3; sg < NSEG; ++sg) {
    f32x4 a0 = zero4(), a1 = zero4();
    if (VW) {
      if (wid == 0) {
        if (__builtin_amdgcn_readfirstlane(*rdy) < min(sg + 2, NSEG)) {
          const int w = -wmax(-(int)flag_wait<TSL>(P, F_PRG(4, ln & 31), min(sg + 2, NSEG), ln));
          if (ln == 0) *rdy = max(w, min(sg + 2, NSEG));
        }
      } else while (__builtin_amdgcn_readfirstlane(*rdy) <= sg) __builtin_amdgcn_s_sleep(1);
      u32x4 of[8];
      #pragma unroll
      for (int i = 0; i < 8; ++i) of[i] = __builtin_amdgcn_raw_buffer_load_b128(rs, ln * 16 + (i & 3) * 2048, T_SRC(sg, i >> 2), AUX_SC1);
      #pragma unroll
      for (int i = 0; i < 8; ++i) T_MMA(i >> 2, i & 3, of[i]);
    } else {
      if (!ahead) {
        while (__builtin_amdgcn_readfirstlane(*rdy) <= sg) __builtin_amdgcn_s_sleep(1);
        #pragma unroll
        for (int i = 0; i < NHD; ++i) T_DMA(sg, i);
      }
      VMW(20);
      ahead = __builtin_amdgcn_readfirstlane(*rdy) > sg + 1;
      u32x4 of[2][4];
#define T_RD(i) do { _Pragma("unroll") for (int k = 0; k < 4; ++k) of[(i) & 1][k] = *(const LAS u32x4*)(lds + j * 24576 + (i) * 4096 + k * 1024 + ln * 16); } while (0)
      T_RD(0);
      #pragma unroll
      for (int i = 0; i < NHD; ++i) {
        asm volatile("s_waitcnt lgkmcnt(0)" : "+v"(of[i & 1][0]), "+v"(of[i & 1][1]), "+v"(of[i & 1][2]), "+v"(of[i & 1][3]) :: "memory");
        if (i + 1 < NHD) {
          if (ahead) VMW(16); else G_WAIT_V(16 - 4 * (i < 4 ? i : 4));
          T_RD(i + 1);
        }
        #pragma unroll
        for (int k = 0; k < 4; ++k) {
          if (ahead) dmas(rs, ln * 16, T_SRC(sg + 1, i) + k * 2048, lbase + j * 24576 + i * 4096 + k * 1024);
          T_MMA(i, k, of[i & 1][k]);
          __builtin_amdgcn_sched_barrier(0);
        }
      }
    }
    LAS f32x4* pw = (LAS f32x4*)(lds + 98304 + ((sg & 3) * 8 + wid) * 2048) + ln;
    if (wid == 0 && sg == NSEG - 1) { k0 = a0; k1 = a1; } else { pw[0] = a0; pw[64] = a1; }
  }
  if (VW) {
    const int fq = ln >> 4, cb2 = 16 * (fq & 1) + 8 * (fq >> 1);
    const unsigned eo = (unsigned)((NSEG - 1 - j) * 32 + r * 16 + (ln & 15)) * 2048u + (unsigned)(c * 32 + cb2) * 2u;
    cnt_wait(P, C_W4(7), 8, ln);
    const u32x4 gv = ld16(rs, WS_G + (unsigned)((NSEG - 1 - j) * 2 + r) * 32768u + c * 1024 + blk_in(ln & 15, cb2));
    const u32x4 xv = *(const u32x4*)((const char*)P.x + eo);
    LBARRIER();
    const LAS f32x4* pr = (const LAS f32x4*)(lds + 98304 + (3 - j) * 16384) + ln;
    f32x4 t[2];
    #pragma unroll
    for (int k = 0; k < 2; ++k)
      t[k] = (((wid ? pr[k * 64] : k ? k1 : k0) + pr[128 + k * 64]) + (pr[256 + k * 64] + pr[384 + k * 64])) + ((pr[512 + k * 64] + pr[640 + k * 64]) + (pr[768 + k * 64] + pr[896 + k * 64]));
    u32x4 v = epi_pack(t[0], t[1]);
    #pragma unroll
    for (int k = 0; k < 4; ++k) {
      v[k] = pack2(lo16(v[k]) * lo16(gv[k]), hi16(v[k]) * hi16(gv[k]));
      v[k] = pack2(lo16(xv[k]) + lo16(v[k]), hi16(xv[k]) + hi16(v[k]));
    }
    *(u32x4*)((char*)P.out + eo) = v;
  } else LBARRIER();
}

#define DMA_S dman
#define XDL 2
#define XPC 0
#define LS_P 131072
template <int MODE>
__device__ __forceinline__ void s_role(LAS unsigned char* lds, Ptrs& P, const u16* W, int h, unsigned obase, int nseg, int fl, int wid, int s0) {
  constexpr bool XM = MODE == 2;
  constexpr unsigned RS = XM ? DM * 2 : HD * 2, SS = XM ? 262144u : 65536u;
  const int lane = opq(0);
  const int kh = wid >> 2, tp = wid & 3;
  const rsrc_t rs = WS_RSRC(P);
  const rsrc_t rw = __builtin_amdgcn_make_buffer_rsrc((void*)W, 0, HD * DM * 2, RSRC_W3);
  const unsigned lbase = (unsigned)(size_t)lds;
  u32x8 w[8][2];
  if (XM && XDL) {
    if (wid == 0) flag_wait(P, F_PRG(4, h), max(s0 - XDL, 0), lane);
    BARRIER();
  }
  {
    const int g = lane >> 4, q4 = (lane & 15) >> 2, p4 = lane & 3;
    const int trow = XM ? 16 * g + 4 * (q4 >> 1) + (q4 & 1) : 8 * g + q4;
#define S_SW(row) ((((row) & 1) | ((((row) >> (XM ? 2 : 1)) & 1) << 1) | ((((row) >> (XM ? 4 : 3)) & 1) << 2)) << 1)
    const int toff = trow * 256 + (((4 * tp + (p4 >> 1)) ^ S_SW(trow)) << 4) + 8 * (p4 & 1);
    unsigned dv[2];
    #pragma unroll
    for (int x = 0; x < 2; ++x) {
      const int row = 8 * wid + 4 * x + (lane >> 4);
      dv[x] = (unsigned)row * RS + (XM ? (h >> 3) * 1024 * RS + (h & 7) * 256 : h * 256) + (((lane & 15) ^ S_SW(row)) << 4);
    }
#define S_IMG(i) do { DMA_S(rw, dv[0], (i) * 64 * RS, lbase + ((i) & 7) * 16384 + wid * 2048); \
                      DMA_S(rw, dv[1], (i) * 64 * RS, lbase + ((i) & 7) * 16384 + wid * 2048 + 1024); if (XM && XPC) __builtin_amdgcn_s_sleep(XPC); } while (0)
    #pragma unroll
    for (int i = 0; i < 8; ++i) S_IMG(i);
    #pragma unroll
    for (int b = 0; b < 4; ++b) {
      if (b == 3 && !XM) get_tag(P);
      if (b < 3) VMW(8); else VMWAIT();
      BARRIER();
      if (kh == (b >> 1)) {
        #pragma unroll
        for (int i = 4 * (b & 1); i < 4 * (b & 1) + 4; ++i) {
          w[i][0] = rd_frag<XM ? 2048 : 8192, XM ? 512 : 1024>(lds + toff + i * 16384);
          w[i][1] = rd_frag<XM ? 2048 : 8192, XM ? 512 : 1024>(lds + (toff ^ 32) + i * 16384);
        }
      }
      LBARRIER();
      if (b < 2) {
        #pragma unroll
        for (int i = 4 * b + 8; i < 4 * b + 12; ++i) S_IMG(i);
      }
    }
  }
  TL(XM ? 74 : 72);
  const unsigned l16 = lane * 16;
  volatile LAS int* rd = (volatile LAS int*)(lds + LS_P + 20480);
#define X_READY(want) do { \
    if (wid == 0) { \
      const int n_ = -wmax(-(int)flag_wait(P, F_PRG(4, (h & 24) + (lane & 7)), s0 + (want), lane)) - s0; \
      if (lane == 0) *rd = max(n_, want); \
    } \
    LBARRIER(); } while (0)
  if (XM) X_READY(2);
  else {
    if (wid == 0) flag_wait(P, F_LN0(lane & 7), 0, lane);
    BARRIER();
  }
  unsigned sx = (XM ? WS_OX + s0 * SS + (h >> 3) * 65536 : WS_XN) + kh * 32768 + tp * 4096;
  const unsigned sd = lbase + kh * 65536 + tp * 4096;
  const unsigned xl = kh * 65536 + (XM ? l16 : blk_in(lane & 15, (lane >> 4) * 8));
  const unsigned xp = lbase + xl, pa = lbase + LS_P + tp * 4096 + l16;
  if (XM) {
    #pragma unroll
    for (int i = 0; i < 12; ++i) dmas(rs, l16, sx + (i >> 3) * SS + ((i >> 2) & 1) * 16384 + (i & 3) * 1024, sd + (i >> 2) * 16384 + (i & 3) * 1024);
    VMWAIT();
    BARRIER();
  } else {
    #pragma unroll
    for (int i = 0; i < 16; ++i) dmas(rs, l16, sx + (i >> 3) * SS + ((i >> 2) & 1) * 16384 + (i & 3) * 1024, sd + ((i >> 2) ^ 2) * 16384 + (i & 3) * 1024);
    VMW(8);
    BARRIER();
    f32x4 a[4] = {};
    #pragma unroll
    for (int ks = 0; ks < 16; ++ks)
      #pragma unroll
      for (int i = 0; i < 4; ++i) {
        const u32x4 x = *(const LAS u32x4*)(lds + xl + 32768 + ks * 2048 + (i >> 1) * 1024);
        const u32x8 wf = w[ks >> 1][i & 1];
        const u32x4 wh = {wf[(ks & 1) * 4], wf[(ks & 1) * 4 + 1], wf[(ks & 1) * 4 + 2], wf[(ks & 1) * 4 + 3]};
        a[i] = MODE ? MFMA32(x, wh, a[i]) : MFMA32(wh, x, a[i]);
      }
    LAS f32x4* pq = (LAS f32x4*)(lds + LS_P + tp * 4096 + l16);
    if (!kh) {
      #pragma unroll
      for (int i = 0; i < 4; ++i) pq[i * 64] = a[i];
    }
    LBARRIER();
    if (kh) {
      #pragma unroll
      for (int i = 0; i < 4; ++i) a[i] += pq[i * 64];
      #pragma unroll
      for (int i = 0; i < 2; ++i) {
        const f32x4 a0 = a[MODE ? i : 2 * i], a1 = a[MODE ? 2 + i : 2 * i + 1];
        if (MODE) st16(rs, obase + h * 8192 + (2 * tp + i) * 1024 + l16, (u32x4){pack2(a0[0], a0[1]), pack2(a0[2], a0[3]), pack2(a1[0], a1[1]), pack2(a1[2], a1[3])});
        else st16(rs, obase + h * 8192 + i * 4096 + tp * 1024 + l16, (u32x4){pack2(a0[0], a1[0]), pack2(a0[1], a1[1]), pack2(a0[2], a1[2]), pack2(a0[3], a1[3])});
      }
    }
    sx += SS;
    #pragma unroll
    for (int i = 0; i < 4; ++i) dmas(rs, l16, sx + SS + i * 1024, sd + 32768 + i * 1024);
    VMW(4);
    BARRIER();
    if (wid == 1) flag_set(P, fl, 1, lane);
    TL(62);
  }
  u32x8 xf[3], ac[2] = {};
  #pragma unroll
  for (int i = 0; i < 3; ++i) {
    const u32x4 a = *(const LAS u32x4*)(lds + xl + i * 2048), b = *(const LAS u32x4*)(lds + xl + i * 2048 + 1024);
    xf[i] = (u32x8){a[0], a[1], a[2], a[3], b[0], b[1], b[2], b[3]};
  }
  unsigned long long fm, pm;
  asm volatile("s_cmp_eq_u32 %2, 1\n\ts_cselect_b64 %0, 1, 0\n\ts_cmp_eq_u32 %3, 1\n\ts_cselect_b64 %1, -1, 0" : "=&s"(fm), "=&s"(pm) : "s"(wid), "s"(XM ? 0 : wid >> 1) : "scc");
  const unsigned fo = FLG_OFF + fl * 128, pd = lbase + LS_P + 16384;
  const unsigned po = 4 * 65536 + h * 2048 + (wid & 1) * 1024 - tp * 4096 - kh * 32768;
  unsigned oo = XM ? obase + (unsigned)h * 16384u + tp * 4096 + (s0 - 1) * 0x80000u : obase + (unsigned)h * 8192u + tp * (MODE ? 2048 : 1024), tmp, tmp2;
  unsigned long long em;
  int t = XM ? 0 : 1;
  if (MODE < 2) {
    #pragma unroll 1
    for (int part = 0; part < 2; ++part) {
      if (part == 1) { TL(63); if (wid == 0) setup_wait(P, lane); }
      if (MODE) S_LOOP_V(part ? nseg + 1 : 3, 0); else S_LOOP_K(part ? nseg + 1 : 3, 0);
    }
  } else {
    #pragma unroll 1
    while (t < nseg) {
      X_READY(min(t + 4, nseg));
      const int n = __builtin_amdgcn_readfirstlane(*rd), te = n >= nseg ? nseg : (n - 2) & ~1;
      TL(t);
      S_LOOP_X(te, (nseg - 1 - te) >> 31);
    }
  }
  TL(XM ? 65 : 64);
  VMWAIT();
  BARRIER();
  if (wid == 1) flag_set(P, fl, nseg, lane);
}

#define LND 32
#define CALN 6
__device__ __forceinline__ void setup_role(LAS unsigned char* lds, Ptrs& P, int cu, int wid) {
  const int lane = opq(0), tid = wid * 64 + lane;
  const rsrc_t rs = WS_RSRC(P);
  u32x4 v[8];
  const u32x4* px = (const u32x4*)(P.x + (size_t)(cu * 32 + wid * 4 + (lane >> 4)) * DM) + (lane & 15);
  #pragma unroll
  for (int i = 0; i < 8; ++i) v[i] = __builtin_nontemporal_load(px + i * 16);
  const unsigned epo = __hip_atomic_load((gu32*)(P.ws + EPO_OFF), RLX_AGENT);
  const unsigned cb = comm_base(P.ws);
  const u32x4 z = {0u, 0u, 0u, 0u};
  const int cr = 63 - cu;
  if (wid == 0 && cr < 16) st16(rs, CNT_OFF + cr * 1024 + lane * 16, z);
  if (cr < 4) st16(rs, cb + FL_B(0) + cr * 8192 + tid * 16, z);
  if (cr == 4 && tid < 256) {
    const int g4 = tid >> 6, k = (tid >> 5) & 1 & (g4 >> 1);
    st16(rs, WS_H1 + ((g4 & 1) ? 127u * 32768u : 0u) + (g4 * 8 + ((tid >> 2) & 7)) * 1024 + blk_in((g4 & 1) ? 15 - k : k, (tid & 3) * 8), z);
  }
  #pragma unroll
  for (int i = 0; i < 2; ++i) {
    const int f = i * 54 + cr;
    if (cr < PD * NCHUNK) {
      u32x4 m = {~0u, ~0u, ~0u, ~0u};
      if (f < 4) {
        const int ks = (f >> 1) * 8 + wid;
        m = *(const u32x4*)(P.mem0 + ((lane >> 1) & 7) * DM + 64 * ks + 16 * (lane >> 4) + 8 * (lane & 1));
        m.x = canon1(m.x); m.y = canon1(m.y); m.z = canon1(m.z); m.w = canon1(m.w);
      }
      st16(rs, cb + (unsigned)(f / (NCHUNK * 2)) * CB_STEP + (f % (NCHUNK * 2)) * 8192u + tid * 16, m);
    }
  }
  const unsigned bi = blk_in((wid & 3) * 4 + (lane >> 4), (lane & 3) * 8);
  const int c0 = (lane >> 2) & 3;
  ln_rows(v);
  TL(69);
  if (cu >= 8) __builtin_amdgcn_s_sleep(LND);
  #pragma unroll
  for (int i = 0; i < 8; ++i) st16(rs, WS_XN + xn_blk(cu * 2 + (wid >> 2), 4 * i + c0) + bi, v[i]);
  P.tag = __builtin_amdgcn_readfirstlane(epo) + 1u;
  ln_rows(v);
  if (cu < 8) {
    VMWAIT();
    BARRIER();
    if (wid == 0) flag_set(P, F_LN0(cu), 1, lane);
    TL(66);
  }
  asm volatile("" ::: "memory");
  #pragma unroll
  for (int i = 0; i < 8; ++i) st16(rs, WS_XN2 + (unsigned)(cu * 2 + (wid >> 2)) * 32768u + (4 * i + c0) * 1024 + bi, v[i]);
  touch(P.w1, DM * DM * 2, ((cu & 31) * 512 + tid) * 128, lds);
  VMW(9);
  BARRIER();
  if (wid == 0) flag_set(P, F_LN0(cu), 1, lane);
  TL(70);
  if (wid == 0) VMW(2); else VMW(1);
  BARRIER();
  if (wid == 0) flag_set(P, F_LN1(cu), 1, lane);
  TL(71);
  if (cu < 32 && (wid >> 1) == 1) {
    const int id = cu * 2 + (wid & 1);
    unsigned best[2] = {65535u, 65535u};
    #pragma unroll
    for (int it = 0; it < CALN; ++it) {
      const unsigned long long c0 = __builtin_amdgcn_s_memtime();
      asm volatile("s_waitcnt lgkmcnt(0)" ::: "memory");
      if (lane == 0) st4(rs, cb + (id & 3) * CB_STEP + (30 + ((id >> 2) & 1)) * 16384u + (it & 1) * 8192u + (id >> 3) * 1024u, 0u);
      VMWAIT();
      const unsigned d = (unsigned)(__builtin_amdgcn_s_memtime() - c0);
      if (it >= 2) best[it & 1] = min(best[it & 1], d);
    }
    flag_set(P, F_CAL(id), (best[0] << 16) | best[1], lane);
  }
}

#define CPF 1
__global__ __launch_bounds__(512, 2) void mega_kernel(const u16* x, const u16* mem0, const u16* Wq, const u16* Wk, const u16* Wv, const u16* Wo,
                                                     const u16* w1, const u16* w2, const u16* w3, const u16* w4, u16* out, char* ws) {
  __shared__ __attribute__((aligned(16))) unsigned char smem[163840];
  LAS unsigned char* lds = (LAS unsigned char*)smem;
  Ptrs P;
  P.x = x; P.mem0 = mem0; P.Wq = Wq; P.Wk = Wk; P.Wv = Wv; P.Wo = Wo; P.w1 = w1; P.w2 = w2; P.w3 = w3; P.w4 = w4; P.out = out; P.ws = ws;
  const int xcd = blockIdx.x & 7, slot = blockIdx.x >> 3;
  const int wid = __builtin_amdgcn_readfirstlane(threadIdx.x >> 6);
  TL(79);
  P.tag = 0;
  if (wid == 7) {
    unsigned long long pc;
    unsigned len;
    asm volatile("s_getpc_b64 %0\nMK_BEG:\n\ts_mov_b32 %1, MK_END-MK_BEG" : "=s"(pc), "=s"(len));
    touch((const void*)pc, len, (slot * 64 + opq(0)) * 128, lds);
  }
  const int na = xcd == 2 ? 8 : 12;
  const bool isa = xcd < 3 && slot < na;
  const int ah = xcd == 2 ? slot * 4 + 2 : xcd * 16 + (slot & 3) * 4 + (slot >> 2) + (slot >> 3), bs = slot - na, br = bs & 1, bc = (xcd == 1 ? 22 : xcd * 5) + (bs >> 1);
  if (isa) {
    if (wid < 4) a_wave<true>(lds, P, ah, wid); else a_wave<false>(lds, P, ah, wid);
  } else if (xcd < 3) {
    if (wid < 4) b_wave<true>(lds, P, br, bc, wid, bs, xcd); else b_wave<false>(lds, P, br, bc, wid, bs, xcd);
  } else {
    if ((xcd & 3) == 3) {
      setup_role(lds, P, (xcd >> 2) * 32 + slot, wid);
    } else if (xcd == 6) s_role<1>(lds, P, Wv, slot, WS_VF, NSEG, F_PRG(2, slot), wid, 0);
    else s_role<0>(lds, P, xcd == 4 ? Wk : Wq, slot, xcd == 4 ? WS_KF : WS_QF, NSEG, F_PRG(xcd - 4, slot), wid, 0);
    if ((xcd & 3) == 3) mlp_role(lds, P, xcd >> 2, slot, wid);
    else s_role<2>(lds, P, Wo, slot, WS_PT, xcd == 4 ? XS1 : xcd == 5 ? XS2 - XS1 : XS3 - XS2, F_PRG(xcd + 1, slot), wid, xcd == 4 ? 0 : xcd == 5 ? XS1 : XS2);
    report(P, wid);
    fin_role(lds, P, wid);
    if (blockIdx.x == 3 && wid == 0) {
      cnt_wait(P, C_ARR, 256, threadIdx.x);
      if (threadIdx.x == 0) __hip_atomic_store((gu32*)(ws + EPO_OFF), P.tag, RLX_AGENT);
    }
  }
  asm volatile("MK_END:");
  TL(78);
}

extern "C" void kernel_launch(void* const* d_in, const int* in_sizes, int n_in, void* d_out, int out_size,
                              void* d_ws, size_t ws_size, hipStream_t stream) {
  mega_kernel<<<256, 512, 0, stream>>>((const u16*)d_in[0], (const u16*)d_in[1], (const u16*)d_in[2], (const u16*)d_in[3], (const u16*)d_in[4],
                                       (const u16*)d_in[5], (const u16*)d_in[8], (const u16*)d_in[9], (const u16*)d_in[10], (const u16*)d_in[11],
                                       (u16*)d_out, (char*)d_ws);
}
```

```cpp
#include <hip/hip_runtime.h>
#include <hip/hip_bf16.h>
#include <stdint.h>

#define SEQ 2048
#define DM 1024
#define NH 32
#define DH 128
#define HD (NH * DH)
#define MM 8
#define SEG 32
#define NSEG (SEQ / SEG)
#define FF 3584

typedef unsigned short u16;
typedef __attribute__((ext_vector_type(8))) short bf16x8;
typedef __attribute__((ext_vector_type(4))) short bf16x4;
typedef __attribute__((ext_vector_type(4))) float f32x4;
typedef __attribute__((ext_vector_type(4))) unsigned u32x4;
typedef __attribute__((ext_vector_type(2))) unsigned u32x2;
typedef __attribute__((ext_vector_type(2))) float f32x2;
typedef __attribute__((ext_vector_type(2))) __bf16 bf16v2;
typedef __attribute__((ext_vector_type(8))) unsigned u32x8;
#define LAS __attribute__((address_space(3)))
#define GAS __attribute__((address_space(1)))

__device__ __forceinline__ unsigned pack2(float lo, float hi) {
  return __builtin_bit_cast(unsigned, __builtin_convertvector((f32x2){lo, hi}, bf16v2));
}
__device__ __forceinline__ float rb1(float x) { return __uint_as_float(pack2(0.f, x)); }

typedef GAS unsigned gu32;
#define RLX_AGENT __ATOMIC_RELAXED, __HIP_MEMORY_SCOPE_AGENT
#define SPIN_CAP (1u << 20)
#define AUX_SC1 16
#define RSRC_W3 0x00020000
typedef __amdgpu_buffer_rsrc_t rsrc_t;

__device__ __forceinline__ u32x4 ld16(rsrc_t rs, unsigned off) { return __builtin_amdgcn_raw_buffer_load_b128(rs, off, 0, AUX_SC1); }
__device__ __forceinline__ u32x2 ld8(rsrc_t rs, unsigned off) { return __builtin_amdgcn_raw_buffer_load_b64(rs, off, 0, AUX_SC1); }
__device__ __forceinline__ unsigned ld4(rsrc_t rs, unsigned off) { return __builtin_amdgcn_raw_buffer_load_b32(rs, off, 0, AUX_SC1); }
__device__ __forceinline__ void st16(rsrc_t rs, unsigned off, u32x4 v) { __builtin_amdgcn_raw_buffer_store_b128(v, rs, off, 0, AUX_SC1); }
__device__ __forceinline__ void st8(rsrc_t rs, unsigned off, u32x2 v) { __builtin_amdgcn_raw_buffer_store_b64(v, rs, off, 0, AUX_SC1); }
__device__ __forceinline__ void st4(rsrc_t rs, unsigned off, unsigned v) { __builtin_amdgcn_raw_buffer_store_b32(v, rs, off, 0, AUX_SC1); }
__device__ __forceinline__ float lo16(unsigned w) { return __uint_as_float(w << 16); }
__device__ __forceinline__ float hi16(unsigned w) { return __uint_as_float(w & 0xffff0000u); }

__device__ __forceinline__ float xsum16_32(float v) {
  unsigned u = __float_as_uint(v);
  auto a = __builtin_amdgcn_permlane16_swap(u, u, false, false);
  v = __uint_as_float(a[0]) + __uint_as_float(a[1]);
  u = __float_as_uint(v);
  auto b = __builtin_amdgcn_permlane32_swap(u, u, false, false);
  return __uint_as_float(b[0]) + __uint_as_float(b[1]);
}

__device__ __forceinline__ unsigned canon1(unsigned w) { return min(w, 0xfffffffeu); }
__device__ __forceinline__ unsigned max4(u32x4 v) { return max(max(v.x, v.y), max(v.z, v.w)); }
__device__ __forceinline__ int opq(int) { int v; asm volatile("v_mbcnt_lo_u32_b32 %0, -1, 0\n\tv_mbcnt_hi_u32_b32 %0, -1, %0" : "=v"(v)); return v; }
__device__ __forceinline__ u32x4 poison4() { unsigned m = ~0u; asm volatile("" : "+v"(m)); return (u32x4){m, m, m, m}; }
__device__ __forceinline__ f32x4 zero4() { float m = 0.f; asm volatile("" : "+v"(m)); return (f32x4){m, m, m, m}; }
#define MFMA32(a, b, c) __builtin_amdgcn_mfma_f32_16x16x32_bf16(__builtin_bit_cast(bf16x8, a), __builtin_bit_cast(bf16x8, b), c, 0, 0, 0)
#define MFMA16(a, b, c) __builtin_amdgcn_mfma_f32_16x16x16bf16_1k(__builtin_bit_cast(bf16x4, a), __builtin_bit_cast(bf16x4, b), c, 0, 0, 0)
#define LBARRIER() do { asm volatile("s_waitcnt lgkmcnt(0)" ::: "memory"); __builtin_amdgcn_s_barrier(); asm volatile("" ::: "memory"); } while (0)
#define BARRIER() do { asm volatile("" ::: "memory"); __builtin_amdgcn_s_barrier(); asm volatile("" ::: "memory"); } while (0)
#define VMWAIT() asm volatile("s_waitcnt vmcnt(0)" ::: "memory")
#define VMW(n) asm volatile("s_waitcnt vmcnt(" #n ")" ::: "memory")
#define DELAY(n) do { if ((n) > 0) __builtin_amdgcn_s_sleep(n); } while (0)

#define EPO_OFF 0
#define TMO_OFF 128
#define FLG_OFF 256
#define F_LN0(i) (i)
#define F_LN1(i) (64 + (i))
#define F_CAL(i) (128 + (i))
#define F_PRG(t, h) (192 + (t) * 32 + (h))
#define CNT_OFF 65536
#define C_W1(p2) (p2)
#define C_W2(p2) (16 + (p2))
#define C_LN(pm) (32 + (pm))
#define C_W3(pm, hf) (40 + (pm) * 2 + (hf))
#define C_W4(pm) (56 + (pm))
#define C_HEAD 64
#define C_ARR 72
#define C_P4(pm, pc) (96 + (pm) * 4 + (pc))
#define NCNT 128
#define WS_XN (1u << 20)
#define WS_XN2 (WS_XN + (4u << 20))
#define WS_QF (WS_XN2 + (4u << 20))
#define WS_KF (WS_QF + (16u << 20))
#define WS_VF (WS_KF + (16u << 20))
#define WS_OX (WS_VF + (16u << 20))
#define WS_H1 (WS_OX + (16u << 20) + (1u << 20))
#define WS_X1 (WS_H1 + (4u << 20) + 65536u)
#define WS_X1N (WS_X1 + (4u << 20))
#define WS_H3 (WS_X1N + (4u << 20))
#define WS_P4 (WS_H3 + (14u << 20))
#define WS_G (WS_P4 + (8u << 20))
#define WS_PT (WS_G + (4u << 20))
#define WS_COMM (WS_PT + (32u << 20))
#define WS_END (WS_COMM + (4u << 20))
#define CB_STEP 524288u
#define PD 3
__device__ __forceinline__ unsigned comm_base(const char* ws) { return WS_COMM + ((0u - ((unsigned)(size_t)ws + WS_COMM)) & 0x1fffffu); }
__device__ __forceinline__ unsigned step_base(unsigned cb, unsigned mask, int s) { return cb + (unsigned)(s & 3) * CB_STEP + mask; }
#define MF_AT(ks) (((ks) >> 3) * 16384u + ((ks) & 7) * 1024u)
#define OB_AT(r, h) ((2u + (r) * 4u + ((h) >> 3)) * 16384u + ((h) & 7) * 1024u)
#define VB_AT(h) ((10u + ((h) >> 2)) * 16384u + ((h) & 3) * 2048u)
#define XS1 22
#define XS2 44
#define XS3 62
#define CSL 8
#define FSL 6
#define SQUICK 3
#define NCHUNK 18
#define FL_O(h) (29u * 16384u + (h) * 128u)
#define FL_B(b) (28u * 16384u + (b) * 128u)
#define FL_T(s) FL_O(32 + ((s) & 3))

struct Ptrs { const u16 *x, *mem0, *Wq, *Wk, *Wv, *Wo, *w1, *w2, *w3, *w4; u16* out; char* ws; unsigned tag; };
#define WS_RSRC(P) __builtin_amdgcn_make_buffer_rsrc((void*)(P).ws, 0, WS_END, RSRC_W3)

__device__ __forceinline__ bool spin_fail(unsigned& spins, const Ptrs& P, int lane) {
  asm volatile("" ::: "memory");
  if (++spins > SPIN_CAP) {
    unsigned tv;
    asm volatile("v_mov_b32 %0, %1" : "=v"(tv) : "s"(P.tag));
    if (lane == 0) __builtin_amdgcn_raw_buffer_store_b32(tv, WS_RSRC(P), 0, TMO_OFF, AUX_SC1);
    return true;
  }
  return (spins & 255u) == 0u && (unsigned)__builtin_amdgcn_readfirstlane(__builtin_amdgcn_raw_buffer_load_b32(WS_RSRC(P), 0, TMO_OFF, AUX_SC1)) == P.tag;
}
__device__ __forceinline__ unsigned cnt_load(char* ws, int idx) {
  return __builtin_amdgcn_readfirstlane(__hip_atomic_load((gu32*)(ws + CNT_OFF + idx * 128), RLX_AGENT));
}
__device__ __forceinline__ void cnt_add(char* ws, int idx, int lane) {
  asm volatile("s_waitcnt vmcnt(0)" ::: "memory");
  if (lane == 0) __hip_atomic_fetch_add((gu32*)(ws + CNT_OFF + idx * 128), 1u, RLX_AGENT);
}
__device__ __forceinline__ void cnt_wait(const Ptrs& P, int idx, unsigned want, int lane) {
  for (unsigned spins = 0;;) {
    if (cnt_load(P.ws, idx) >= want) break;
    __builtin_amdgcn_s_sleep(CSL);
    if (spin_fail(spins, P, lane)) break;
  }
}
template <int SL = FSL>
__device__ __forceinline__ unsigned flag_wait(const Ptrs& P, int f, unsigned want, int lane) {
  const rsrc_t rs = WS_RSRC(P);
  u32x2 v;
  for (unsigned spins = 0;;) {
    v = ld8(rs, FLG_OFF + f * 128);
    if (!__any(v.y != P.tag || v.x < want)) break;
    __builtin_amdgcn_s_sleep(SL);
    if (spin_fail(spins, P, lane)) break;
  }
  asm volatile("" ::: "memory");
  return v.x;
}
__device__ __forceinline__ void flag_set(const Ptrs& P, int f, unsigned val, int lane) {
  if (lane == 0) st8(WS_RSRC(P), FLG_OFF + f * 128, (u32x2){val, P.tag});
}
__device__ __forceinline__ void get_tag(Ptrs& P) { P.tag = __builtin_amdgcn_readfirstlane(__hip_atomic_load((gu32*)(P.ws + EPO_OFF), RLX_AGENT)) + 1u; }
__device__ __forceinline__ void setup_wait(const Ptrs& P, int lane) { flag_wait(P, F_LN0(lane), 0, lane); }
__device__ __forceinline__ void clock_wait(unsigned tgt) {
  while ((int)(tgt - (unsigned)__builtin_amdgcn_s_memrealtime()) > 0) {}
}
__device__ __forceinline__ void report(const Ptrs& P, int wid) {
  if (wid == 0 && opq(0) == 0) __hip_atomic_fetch_add((gu32*)(P.ws + CNT_OFF + C_ARR * 128), 1u, RLX_AGENT);
}
__device__ __forceinline__ void kv_wait(const Ptrs& P, int s, int h, int& known, int lane) {
  if (s < known) return;
  const unsigned v = flag_wait(P, F_PRG(min(lane, 2), h), s + 1, lane);
  known = __builtin_amdgcn_readfirstlane(min(min(__builtin_amdgcn_readlane(v, 0), __builtin_amdgcn_readlane(v, 1)), __builtin_amdgcn_readlane(v, 2)));
}
__device__ __forceinline__ int wmax(int v) {
  v = max(v, __builtin_amdgcn_update_dpp(0, v, 0x128, 0xf, 0xf, false));
  v = max(v, __builtin_amdgcn_update_dpp(0, v, 0x124, 0xf, 0xf, false));
  v = max(v, __builtin_amdgcn_update_dpp(0, v, 0x122, 0xf, 0xf, false));
  v = max(v, __builtin_amdgcn_update_dpp(0, v, 0x121, 0xf, 0xf, false));
  return max(max(__builtin_amdgcn_readlane(v, 0), __builtin_amdgcn_readlane(v, 16)), max(__builtin_amdgcn_readlane(v, 32), __builtin_amdgcn_readlane(v, 48)));
}
__device__ __forceinline__ unsigned near_half(unsigned v) { return wmax(-(int)(v & 65535u)) > wmax(-(int)(v >> 16)) ? 8192u : 0u; }
__device__ __forceinline__ void dmaw(rsrc_t rs, unsigned voff, unsigned soff, unsigned ldsaddr) {
  asm volatile("s_mov_b32 m0, %3\n\ts_nop 0\n\tbuffer_load_dwordx4 %0, %1, %2 offen lds" :: "v"(voff), "s"(rs), "s"(soff), "s"(ldsaddr) : "memory");
}
__device__ __forceinline__ void dman(rsrc_t rs, unsigned voff, unsigned soff, unsigned ldsaddr) {
  asm volatile("s_mov_b32 m0, %3\n\ts_nop 0\n\tbuffer_load_dwordx4 %0, %1, %2 offen nt lds" :: "v"(voff), "s"(rs), "s"(soff), "s"(ldsaddr) : "memory");
}
__device__ __forceinline__ void dmas(rsrc_t rs, unsigned voff, unsigned soff, unsigned ldsaddr) {
  asm volatile("s_mov_b32 m0, %3\n\ts_nop 0\n\tbuffer_load_dwordx4 %0, %1, %2 offen sc1 lds" :: "v"(voff), "s"(rs), "s"(soff), "s"(ldsaddr) : "memory");
}
__device__ __forceinline__ void touch(const void* w, unsigned bytes, unsigned voff, LAS unsigned char* lds) {
  asm volatile("s_mov_b32 m0, %2\n\ts_nop 0\n\tbuffer_load_dword %0, %1, 0 offen lds"
               :: "v"(voff), "s"(__builtin_amdgcn_make_buffer_rsrc((void*)w, 0, bytes, RSRC_W3)), "s"((unsigned)(size_t)lds + 148480) : "memory");
}
__device__ __forceinline__ u32x2 trd(LAS unsigned char* p) {
  return __builtin_bit_cast(u32x2, __builtin_amdgcn_ds_read_tr16_b64_v4i16((LAS bf16x4*)p));
}
template <int BO, int UO>
__device__ __forceinline__ u32x8 rd_frag(LAS unsigned char* p) {
  const u32x2 a = trd(p), b = trd(p + UO), c = trd(p + BO), d = trd(p + BO + UO);
  return (u32x8){a.x, a.y, b.x, b.y, c.x, c.y, d.x, d.y};
}

#define TL(i) do {} while (0)
#define TLW(i) do {} while (0)
#define GSDECL
#define GS(i) do {} while (0)
#define GSFLUSH(k, i) do {} while (0)

__device__ __forceinline__ int lds_byte(int r, int c) {
  const int st = (r >> 4) * 2 + (c >> 5), rr = r & 15, cc = c & 31, ob = rr * 64 + cc * 2;
  return st * 1024 + (ob ^ (((ob >> 9) & 1) << 5));
}
#define PFNT ""
#define G_DvoffB dmaw
#define HSP 3
#define PFD 3
#define PFH 4
struct Pf { unsigned v, step; bool on, a; };
#define G_PF(t) do { if (pf.on && (t) < nt) asm volatile("s_mov_b32 m0, %3\n\ts_nop 0\n\tbuffer_load_dword %0, %1, %2 offen sc1 " PFNT "lds" \
    :: "v"(pf.v), "s"(pf.a ? ra : rb), "s"((t) * pf.step), "s"((unsigned)(size_t)lds + 148480) : "memory"); } while (0)

__device__ __forceinline__ void gemm_loop(LAS unsigned char* lds, rsrc_t ra, rsrc_t rb, unsigned cA, unsigned cB, int lda, int ldb, int nt,
                                          const Pf& pf, f32x4 (&acc)[2][2][4][2]) {
  const int wid = __builtin_amdgcn_readfirstlane(threadIdx.x >> 6), lane = opq(0), tid = wid * 64 + lane;
  const int wr = wid >> 2, wc = wid & 3, fr = lane & 15, fq = lane >> 4;
  unsigned voffA[2], voffB[2];
  #pragma unroll
  for (int i = 0; i < 2; ++i) {
    voffA[i] = (unsigned)(((wid >> 1) + 4 * i) * 16 * lda + (wid & 1) * 1024 + lane * 16);
    const int b = tid * 16 + i * 8192, r = b >> 8, pos = (b >> 4) & 15;
    const int c = pos ^ (((r & 3) << 1) | (((r >> 3) & 1) << 3));
    voffB[i] = (unsigned)(r * ldb + c * 16);
  }
  const unsigned hA = 128 * lda;
  const unsigned kB = 64 * ldb;
  const unsigned ldsw = (unsigned)(size_t)lds + (unsigned)wid * 1024u;
  const int aoff = 65536 + lds_byte(wr * 64 + fr, fq * 8);
  const int q = (lane & 15) >> 2, p = lane & 3;
  const unsigned boff0 = (unsigned)(size_t)lds + (8 * fq + q) * 256 + (((wc * 4 + (p >> 1)) ^ ((q << 1) | ((fq & 1) << 3))) << 4) + 8 * (p & 1);
  const unsigned boff1 = boff0 ^ 32;
#define G_SB(b, h) (((b) * 2 + (h)) * 16384)
#define G_SA(b, h) (65536 + ((b) * 2 + (h)) * 16384)
#define G_ST1(bufoff, r, so, voff, i) G_D##voff(r, (voff)[i], so, ldsw + (bufoff) + (i) * 8192)
#define G_DvoffA dmas
#define G_STAGE(bufoff, so, voff) do { G_ST1(bufoff, G_R##voff, so, voff, 0); G_ST1(bufoff, G_R##voff, so, voff, 1); } while (0)
#define G_RvoffA ra
#define G_RvoffB rb
#define G_LDA(dst, b, h) do { _Pragma("unroll") for (int m = 0; m < 4; ++m) _Pragma("unroll") for (int k = 0; k < 2; ++k) \
    dst[m][k] = *(const LAS bf16x8*)(lds + (G_SA(b, h) - 65536) + aoff + m * 2048 + k * 1024); } while (0)
#define G_TR(dst, base, off) asm volatile("ds_read_b64_tr_b16 %0, %1 offset:%2" : "=v"(dst) : "v"(base), "i"(off))
#define G_LDB(dst, b, h) do { _Pragma("unroll") for (int n = 0; n < 2; ++n) _Pragma("unroll") for (int k = 0; k < 2; ++k) { \
    bf16x4 _v0, _v1; \
    G_TR(_v0, (n ? boff1 : boff0), G_SB(b, h) + k * 8192); \
    G_TR(_v1, (n ? boff1 : boff0), G_SB(b, h) + k * 8192 + 1024); \
    dst[n][k] = __builtin_shufflevector(_v0, _v1, 0, 1, 2, 3, 4, 5, 6, 7); } } while (0)
#define G_WAIT_B(Bt) asm volatile("s_waitcnt lgkmcnt(0)" : "+v"(Bt[0][0]), "+v"(Bt[0][1]), "+v"(Bt[1][0]), "+v"(Bt[1][1]) :: "memory")
#define G_MMA(ai, bj, At, Bt) do { __builtin_amdgcn_s_setprio(1); _Pragma("unroll") for (int m = 0; m < 4; ++m) _Pragma("unroll") for (int n = 0; n < 2; ++n) _Pragma("unroll") for (int k = 0; k < 2; ++k) \
    acc[ai][bj][m][n] = __builtin_amdgcn_mfma_f32_16x16x32_bf16(Bt[n][k], At[m][k], acc[ai][bj][m][n], 0, 0, 0); \
    __builtin_amdgcn_s_setprio(0); } while (0)
#define G_WAIT_V(n) asm volatile("s_waitcnt vmcnt(%0)" :: "n"(n) : "memory")
#define G_WAIT_L(n) asm volatile("s_waitcnt lgkmcnt(" #n ")" ::: "memory")
#define G_BAR __builtin_amdgcn_s_barrier()
#define G_SCHED __builtin_amdgcn_sched_barrier(0)
#define G_APTR(t) (cA + (t) * 2048)
  #pragma unroll
  for (int a = 0; a < 2; ++a)
    #pragma unroll
    for (int b = 0; b < 2; ++b)
      #pragma unroll
      for (int m = 0; m < 4; ++m)
        #pragma unroll
        for (int n = 0; n < 2; ++n) acc[a][b][m][n] = (f32x4){0.f, 0.f, 0.f, 0.f};
  bf16x8 At[4][2], B0[2][2], B1[2][2];
  {
    const unsigned a0 = G_APTR(0);
    #pragma unroll
    for (int t = 2; t < PFD; ++t) G_PF(t);
    G_STAGE(G_SB(0, 0), cB, voffB); G_STAGE(G_SA(0, 0), a0, voffA); G_STAGE(G_SB(0, 1), cB + 256, voffB); G_STAGE(G_SA(0, 1), a0 + hA, voffA);
    if (wr == 1) G_BAR;
    G_WAIT_V(4); G_BAR;
    G_STAGE(G_SB(1, 0), cB + kB, voffB); G_STAGE(G_SA(1, 0), a0 + 2048, voffA); G_STAGE(G_SB(1, 1), cB + kB + 256, voffB);
    G_WAIT_V(6); G_BAR;
  }
  for (int t = 0; t < nt; t += 2) {
    const int t2 = (t == nt - 2) ? 0 : t + 2;
    const unsigned a1 = G_APTR(t + 1);
    const unsigned a2 = G_APTR(t2);
    const unsigned b2 = cB + t2 * kB;
    const unsigned a3 = a2 + 2048;
    const unsigned b3 = b2 + kB;
    G_LDB(B0, 0, 0); G_SCHED; G_LDA(At, 0, 0); G_STAGE(G_SA(1, 1), a1 + hA, voffA);
    G_WAIT_L(8); G_BAR; G_WAIT_B(B0); G_MMA(0, 0, At, B0); G_BAR; G_SCHED;
    G_LDB(B1, 0, 1); G_STAGE(G_SB(0, 0), b2, voffB);
    G_BAR; G_WAIT_B(B1); G_MMA(0, 1, At, B1); G_BAR;
    G_LDA(At, 0, 1); G_STAGE(G_SA(0, 0), a2, voffA);
    G_BAR; G_WAIT_L(0); G_MMA(1, 0, At, B0); G_BAR; G_SCHED;
    G_STAGE(G_SB(0, 1), b2 + 256, voffB);
    G_WAIT_V(6); G_PF(t + PFD); G_BAR; G_MMA(1, 1, At, B1); G_BAR;
    G_LDB(B0, 1, 0); G_SCHED; G_LDA(At, 1, 0); G_STAGE(G_SA(0, 1), a2 + hA, voffA);
    G_WAIT_L(8); G_BAR; G_WAIT_B(B0); G_MMA(0, 0, At, B0); G_BAR; G_SCHED;
    G_LDB(B1, 1, 1); G_STAGE(G_SB(1, 0), b3, voffB);
    G_BAR; G_WAIT_B(B1); G_MMA(0, 1, At, B1); G_BAR;
    G_LDA(At, 1, 1); G_STAGE(G_SA(1, 0), a3, voffA);
    G_BAR; G_WAIT_L(0); G_MMA(1, 0, At, B0); G_BAR; G_SCHED;
    G_STAGE(G_SB(1, 1), b3 + 256, voffB);
    G_WAIT_V(6); G_PF(t + PFD + 1); G_BAR; G_MMA(1, 1, At, B1); G_BAR;
  }
  if (wr == 0) G_BAR;
}

__device__ __forceinline__ void gemm_h(LAS unsigned char* lds, rsrc_t ra, rsrc_t rb, unsigned cA, unsigned cB, int lda, int ldb, int nt,
                                       const Pf& pf, f32x4 (&acc)[1][2][4][2]) {
  const int wid = __builtin_amdgcn_readfirstlane(threadIdx.x >> 6), lane = opq(0), tid = wid * 64 + lane;
  const int wr = wid >> 2, wc = wid & 3, fr = lane & 15, fq = lane >> 4;
  unsigned voffA[2], voffB[2];
  #pragma unroll
  for (int i = 0; i < 2; ++i) {
    voffA[i] = (unsigned)(((wid >> 1) + 4 * i) * 16 * lda + (wid & 1) * 1024 + lane * 16);
    const int b = tid * 16 + i * 8192, r = b >> 8, pos = (b >> 4) & 15;
    const int c = pos ^ (((r & 3) << 1) | (((r >> 3) & 1) << 3));
    voffB[i] = (unsigned)(r * ldb + c * 16);
  }
  const unsigned kB = 64 * ldb;
  const unsigned ldsw = (unsigned)(size_t)lds + (unsigned)wid * 1024u;
  const int q = (lane & 15) >> 2, p = lane & 3;
  const int aoffs = 32768 + lds_byte(wr * 64 + fr, fq * 8);
  const unsigned boffs = (unsigned)(size_t)lds + (8 * fq + q) * 256 + (((wc * 4 + (p >> 1)) ^ ((q << 1) | ((fq & 1) << 3))) << 4) + 8 * (p & 1);
  #pragma unroll
  for (int i = 0; i < 16; ++i) acc[0][i >> 3][(i >> 1) & 3][i & 1] = (f32x4){0.f, 0.f, 0.f, 0.f};
  bf16x8 At[4][2], B0[2][2], B1[2][2];
#define H_STAGE(so, t) do { G_STAGE(so, cB + (t) * kB, voffB); G_STAGE((so) + 32768, G_APTR(t), voffA); G_STAGE((so) + 16384, cB + (t) * kB + 256, voffB); } while (0)
  #pragma unroll
  for (int t = 2; t < PFH; ++t) G_PF(t);
  H_STAGE(0, 0); H_STAGE(49152, 1);
  G_WAIT_V(6); G_BAR;
  if (wr == 1) G_BAR;
  int cur = 0, nxt = 98304;
#define H_M4(bj, m, Bt) do { _Pragma("unroll") for (int n = 0; n < 2; ++n) _Pragma("unroll") for (int k = 0; k < 2; ++k) \
    acc[0][bj][m][n] = __builtin_amdgcn_mfma_f32_16x16x32_bf16(Bt[n][k], At[m][k], acc[0][bj][m][n], 0, 0, 0); G_SCHED; } while (0)
  for (int t = 0; t < nt; ++t) {
    const int aoff = aoffs + cur;
    const unsigned boff0 = boffs + cur, boff1 = boff0 ^ 32;
    const int t2 = t + 2 < nt ? t + 2 : 0;
    const unsigned a2 = G_APTR(t2), b2 = cB + t2 * kB;
    G_LDB(B0, 0, 0); G_ST1(nxt, rb, b2, voffB, 0); G_LDA(At, 0, 0); G_SCHED; G_ST1(nxt, rb, b2, voffB, 1); G_LDB(B1, 0, 1); G_SCHED;
    if (HSP > 2) G_ST1(nxt + 32768, ra, a2, voffA, 0);
    G_WAIT_V(HSP); G_WAIT_B(B0); G_WAIT_B(B1); G_BAR; G_SCHED;
    __builtin_amdgcn_s_setprio(1);
    H_M4(0, 0, B0); if (HSP < 3) G_ST1(nxt + 32768, ra, a2, voffA, 0);
    H_M4(0, 1, B0); G_ST1(nxt + 32768, ra, a2, voffA, 1);
    H_M4(0, 2, B0); G_ST1(nxt + 16384, rb, b2 + 256, voffB, 0);
    H_M4(0, 3, B0); G_ST1(nxt + 16384, rb, b2 + 256, voffB, 1);
    H_M4(1, 0, B1); G_PF(t + PFH);
    H_M4(1, 1, B1); H_M4(1, 2, B1); H_M4(1, 3, B1);
    __builtin_amdgcn_s_setprio(0);
    G_BAR; G_SCHED;
    nxt = cur;
    cur = cur == 98304 ? 0 : cur + 49152;
  }
  if (wr == 0) G_BAR;
}

__device__ __forceinline__ u32x4 epi_pack(const f32x4 a0, const f32x4 a1) {
  const unsigned x0 = pack2(a0[0], a0[1]), x1 = pack2(a0[2], a0[3]);
  const unsigned y0 = pack2(a1[0], a1[1]), y1 = pack2(a1[2], a1[3]);
  auto s0 = __builtin_amdgcn_permlane16_swap(x0, y0, false, false);
  auto s1 = __builtin_amdgcn_permlane16_swap(x1, y1, false, false);
  return (u32x4){s0[0], s1[0], s0[1], s1[1]};
}
__device__ __forceinline__ unsigned gelu2(unsigned w) {
  const f32x2 x = {lo16(w), hi16(w)};
  const float c0 = -2.f * 1.4426950408889634f * 0.7978845608028654f, c1 = c0 * 0.044715f;
  const f32x2 a = x * (x * x * c1 + c0);
  const f32x2 d = (f32x2){__builtin_amdgcn_exp2f(a[0]), __builtin_amdgcn_exp2f(a[1])} + 1.f;
  const f32x2 y = x * (f32x2){__builtin_amdgcn_rcpf(d[0]), __builtin_amdgcn_rcpf(d[1])};
  return pack2(y[0], y[1]);
}
__device__ __forceinline__ float silu_f(float b) { return b * __builtin_amdgcn_rcpf(1.f + __expf(-b)); }

__device__ __forceinline__ unsigned blk_in(int r, int c) { const int ob = r * 64 + c * 2; return ob ^ (((ob >> 9) & 1) << 5); }
__device__ __forceinline__ unsigned xn_blk(int rb, int cb) { return (unsigned)(((rb >> 1) * 32 + cb) * 2 + (rb & 1)) * 1024u; }

template <int EPI, int NA>
__device__ __forceinline__ void epi_rows(const f32x4 (&acc)[NA][2][4][2], rsrc_t rs, unsigned obase, int ldc, int pm2, int pn2, int sh, int wr, int wc, int ln) {
  const int fq = ln >> 4, cb = 16 * (fq & 1) + 8 * (fq >> 1), fr = (ln & 15) + sh;
  const unsigned lo = (unsigned)((fr >> 4) * 16 * ldc) + blk_in(fr & 15, cb);
  u32x4 r[8];
  if (EPI == 2) {
    #pragma unroll
    for (int i = 0; i < 8; ++i) r[i] = ld16(rs, WS_XN + xn_blk(pm2 * 8 + 4 * wr + (i >> 1), pn2 * 4 + 4 * (i & 1) + wc) + lo);
  }
  #pragma unroll
  for (int ai = 0; ai < NA; ++ai)
    #pragma unroll
    for (int m = 0; m < 4; ++m)
      #pragma unroll
      for (int bj = 0; bj < 2; ++bj) {
        u32x4 w = epi_pack(acc[ai][bj][m][0], acc[ai][bj][m][1]);
        #pragma unroll
        for (int i = 0; i < 4; ++i) w[i] = EPI < 2 ? gelu2(w[i]) : pack2(lo16(r[2 * m + bj][i]) + lo16(w[i]), hi16(r[2 * m + bj][i]) + hi16(w[i]));
        st16(rs, obase + (unsigned)(pm2 * 128 + 128 * ai + 64 * wr + 16 * m) * (unsigned)ldc + (unsigned)(pn2 * 4 + 4 * bj + wc) * 1024u + lo, w);
      }
}

__device__ __forceinline__ float row_sum16(float v) {
  v += __uint_as_float(__builtin_amdgcn_update_dpp(0, (int)__float_as_uint(v), 0x128, 0xf, 0xf, false));
  v += __uint_as_float(__builtin_amdgcn_update_dpp(0, (int)__float_as_uint(v), 0x124, 0xf, 0xf, false));
  v += __uint_as_float(__builtin_amdgcn_update_dpp(0, (int)__float_as_uint(v), 0x122, 0xf, 0xf, false));
  v += __uint_as_float(__builtin_amdgcn_update_dpp(0, (int)__float_as_uint(v), 0x121, 0xf, 0xf, false));
  return v;
}
__device__ __forceinline__ void ln_rows(u32x4 (&a)[8]) {
  f32x2 v[32];
  #pragma unroll
  for (int i = 0; i < 32; ++i) v[i] = (f32x2){lo16(a[i >> 2][i & 3]), hi16(a[i >> 2][i & 3])};
  f32x2 s[4] = {v[0], v[1], v[2], v[3]};
  #pragma unroll
  for (int i = 4; i < 32; ++i) s[i & 3] += v[i];
  s[0] = (s[0] + s[1]) + (s[2] + s[3]);
  const float mean = row_sum16(s[0][0] + s[0][1]) * (1.f / DM);
  #pragma unroll
  for (int i = 0; i < 32; ++i) { const f32x2 d = v[i] - mean; s[i & 3] = i < 4 ? d * d : s[i & 3] + d * d; }
  s[0] = (s[0] + s[1]) + (s[2] + s[3]);
  const float var = row_sum16(s[0][0] + s[0][1]) * (1.f / DM);
  const float mb = rb1(mean), vb = rb1(var);
  const float rden = __builtin_amdgcn_rcpf(rb1(__builtin_amdgcn_sqrtf(rb1(vb + 1e-5f))));
  #pragma unroll
  for (int i = 0; i < 32; ++i) {
    const f32x2 d = v[i] - mb;
    const unsigned w = pack2(d[0], d[1]);
    const f32x2 y = (f32x2){lo16(w), hi16(w)} * rden;
    a[i >> 2][i & 3] = pack2(y[0], y[1]);
  }
}

template <int N>
__device__ __forceinline__ void fin_out(rsrc_t rs, const Ptrs& P, unsigned po, unsigned go, unsigned eo, int fw = -1, int want = 0) {
  u32x4 pv[N][8], gv[N], xv[N];
  #pragma unroll
  for (int c = 0; c < N; ++c) {
    gv[c] = ld16(rs, go + c * 4096);
    xv[c] = __builtin_nontemporal_load((const u32x4*)((const char*)P.x + eo + c * 256));
    if (N == 1) {
      if (fw >= 0) flag_wait<3>(P, fw, want, opq(0));
      BARRIER();
    }
    #pragma unroll
    for (int i = 0; i < 8; ++i) pv[c][i] = ld16(rs, po + c * 16384 + (i >> 1) * 131072 + (i & 1) * 1024);
  }
  #pragma unroll
  for (int c = 0; c < N; ++c) {
#define PF4(i) __builtin_bit_cast(f32x4, pv[c][i])
    u32x4 v = epi_pack((PF4(0) + PF4(2)) + (PF4(4) + PF4(6)), (PF4(1) + PF4(3)) + (PF4(5) + PF4(7)));
    #pragma unroll
    for (int k = 0; k < 4; ++k) {
      v[k] = pack2(lo16(v[k]) * lo16(gv[c][k]), hi16(v[k]) * hi16(gv[c][k]));
      v[k] = pack2(lo16(xv[c][k]) + lo16(v[k]), hi16(xv[c][k]) + hi16(v[k]));
    }
    *(u32x4*)((char*)P.out + eo + c * 256) = v;
  }
}

template <int KH>
__device__ __forceinline__ void epi_w4(const f32x4 (&acc)[2][2][4][2], rsrc_t rs, const Ptrs& P, int pm, int pc, int wid, int ln) {
  char* ws = P.ws;
  const int wr = wid >> 2, wc = wid & 3, fr = ln & 15, fq = ln >> 4;
  const unsigned pb = WS_P4 + (unsigned)(pm * 4 + pc) * 262144u + wid * 16384 + ln * 16;
  #pragma unroll
  for (int i = 0; i < 16; ++i) st16(rs, pb + KH * 131072 + i * 1024, __builtin_bit_cast(u32x4, acc[KH ^ 1][(i >> 1) & 1][i >> 2][i & 1]));
  const unsigned eb = (unsigned)(pm * 256 + 128 * KH + 64 * wr) * 2048u + (unsigned)(pc * 8 + wc) * 1024u + blk_in(fr, 16 * (fq & 1) + 8 * (fq >> 1));
  u32x4 pp[16], r1[8];
  #pragma unroll
  for (int i = 0; i < 8; ++i) r1[i] = ld16(rs, WS_X1 + eb + (i >> 1) * 32768 + (i & 1) * 4096);
  VMWAIT();
  BARRIER();
  if (wid == 0) {
    if (ln == 0) __hip_atomic_fetch_add((gu32*)(ws + CNT_OFF + C_P4(pm, pc) * 128), 1u, RLX_AGENT);
    cnt_wait(P, C_P4(pm, pc), 2, ln);
  }
  BARRIER();
  #pragma unroll
  for (int i = 0; i < 16; ++i) pp[i] = ld16(rs, pb + (KH ^ 1) * 131072 + i * 1024);
  asm volatile("" ::: "memory");
  #pragma unroll
  for (int i = 0; i < 8; ++i) {
    u32x4 v = epi_pack(acc[KH][i & 1][i >> 1][0] + __builtin_bit_cast(f32x4, pp[2 * i]), acc[KH][i & 1][i >> 1][1] + __builtin_bit_cast(f32x4, pp[2 * i + 1]));
    #pragma unroll
    for (int k = 0; k < 4; ++k) {
      v[k] = pack2(lo16(r1[i][k]) + lo16(v[k]), hi16(r1[i][k]) + hi16(v[k]));
      v[k] = pack2(silu_f(lo16(v[k])), silu_f(hi16(v[k])));
    }
    st16(rs, WS_G + eb + (i >> 1) * 32768 + (i & 1) * 4096, v);
  }
}

#define W4S 1
__device__ __forceinline__ void mlp_role(LAS unsigned char* lds, const Ptrs& P, int hf, int slot, int wid) {
  const int wr = wid >> 2, wc = wid & 3;
  char* ws = P.ws;
  const rsrc_t rs = WS_RSRC(P);
#define M_DONE(c) do { VMWAIT(); BARRIER(); if (wid == 0) cnt_add(ws, c, opq(0)); } while (0)
  #pragma unroll 1
  for (int code = 0; code < 2; ++code) {
    const int p2 = hf * 8 + (slot >> 2), pn = slot & 3;
    int ln = opq(0);
    if (wid == 0) {
      if (code == 0) flag_wait(P, F_LN1(p2 * 4 + (ln & 3)), 0, ln);
      else {
        const unsigned co = CNT_OFF + C_W1(min(max(p2 - 1 + min(ln, 2), 0), 15)) * 128;
        for (unsigned spins = 0; __any(ld4(rs, co) < 4u);) {
          __builtin_amdgcn_s_sleep(4);
          if (spin_fail(spins, P, ln)) break;
        }
      }
    }
    BARRIER();
    TLW(3 * code);
    const rsrc_t rb = __builtin_amdgcn_make_buffer_rsrc((void*)(code ? P.w2 : P.w1), 0, DM * DM * 2, RSRC_W3);
    const unsigned cA = (code ? WS_H1 : WS_XN2) + p2 * 128 * 2048, cB = pn * 512;
    Pf pf;
    pf.on = wc == 0; pf.a = wr;
    ln &= 31;
    pf.v = wr ? cA + (pn * 2 + (ln >> 4)) * 32768 + (ln & 15) * 128 : cB + ((slot >> 2) * 8 + (ln >> 2)) * 2048 + (ln & 3) * 128;
    pf.step = wr ? 2048 : 64 * 2048;
    f32x4 acq[1][2][4][2];
    gemm_h(lds, rs, rb, cA, cB, 2048, 2048, 16, pf, acq);
    TLW(3 * code + 1);
    ln = opq(0);
    if (code == 0) {
      if (wid < 4) touch(P.w2, DM * DM * 2, ((hf * 32 + slot) * 256 + wid * 64 + ln) * 128, lds);
      epi_rows<0, 1>(acq, rs, WS_H1, 2048, p2, 2 * pn, (pn & 1) ? -1 - (pn >> 1) : 1 + (pn >> 1), wr, wc, ln);
      if (wid == 0) setup_wait(P, ln);
      M_DONE(C_W1(p2));
    } else {
      epi_rows<2, 1>(acq, rs, WS_X1, 2048, p2, 2 * pn, 0, wr, wc, ln);
      M_DONE(C_W2(p2));
      if (wid == 0) cnt_wait(P, C_W2(p2), 4, ln);
      BARRIER();
      const unsigned ro = (unsigned)(p2 * 8 + pn * 2 + wr) * 32768u + ((ln >> 2) & 3) * 1024 + blk_in(wc * 4 + (ln >> 4), (ln & 3) * 8);
      u32x4 v[8];
      #pragma unroll
      for (int i = 0; i < 8; ++i) v[i] = ld16(rs, WS_X1 + ro + i * 4096);
      ln_rows(v);
      #pragma unroll
      for (int i = 0; i < 8; ++i) st16(rs, WS_X1N + ro + i * 4096, v[i]);
      M_DONE(C_LN(p2 >> 1));
    }
    TLW(3 * code + 2);
  }
  #pragma unroll 1
  for (int code = 2; code < 5; ++code) {
    const int pm = hf * 4 + (slot & 3), pn = (slot >> 2) + (code == 3 ? 8 : 0);
    int ln = opq(0);
    if (pn >= 14) {
      #pragma unroll 1
      for (int i = 0; i < 7 * W4S; ++i) {
        if ((wid & (W4S - 1)) == (i & (W4S - 1))) touch(P.w4, DM * FF * 2, (((i / W4S) * 16 + hf * 8 + (slot & 7)) * 512 + wid * 64 + ln) * 128, lds);
        __builtin_amdgcn_s_sleep(96 / W4S);
      }
      continue;
    }
    if (code != 3) {
      if (wid == 0) cnt_wait(P, code == 2 ? C_LN(pm) : C_W3(pm, pn >> 2), code == 2 ? 8 : 7, ln);
      BARRIER();
    }
    TLW(3 * code);
    const bool w4 = code == 4;
    const int lda = w4 ? 7168 : 2048, ldb = w4 ? 2048 : 7168, nt = w4 ? 28 : 16, na = w4 ? 4 : 8;
    const rsrc_t rb = __builtin_amdgcn_make_buffer_rsrc((void*)(w4 ? P.w4 : P.w3), 0, DM * FF * 2, RSRC_W3);
    const unsigned cA = (w4 ? WS_H3 + (pn >> 2) * 28 * 2048 : WS_X1N) + pm * 256 * lda, cB = w4 ? (pn >> 2) * 28 * 64 * 2048 + (pn & 3) * 512 : pn * 512;
    Pf pf;
    pf.on = wc == 0 && (!wr || code != 3); pf.a = wr;
    pf.v = wr ? cA + ((pn & (na - 1)) * (16 / na) + ((ln >> 4) & (16 / na - 1))) * 16 * lda + (ln & 15) * 128 : cB + ((slot & 3) * 16 + (ln >> 2)) * ldb + (ln & 3) * 128;
    pf.step = wr ? 2048 : 64 * ldb;
    f32x4 acc[2][2][4][2];
    gemm_loop(lds, rs, rb, cA, cB, lda, ldb, nt, pf, acc);
    TLW(3 * code + 1);
    ln = opq(0);
    if (!w4) epi_rows<1, 2>(acc, rs, WS_H3, 7168, 2 * pm, 2 * pn, 0, wr, wc, ln);
    else if (pn >> 2) epi_w4<1>(acc, rs, P, pm, pn & 3, wid, ln);
    else epi_w4<0>(acc, rs, P, pm, pn & 3, wid, ln);
    M_DONE(w4 ? C_W4(pm) : C_W3(pm, pn >= 7));
    TLW(3 * code + 2);
  }
}

#define FINE 52
__device__ __forceinline__ void fin_role(LAS unsigned char* lds, const Ptrs& P, int wid) {
  const int wr = wid >> 2, wc = wid & 3;
  const rsrc_t rs = WS_RSRC(P);
  volatile LAS int* tk = (volatile LAS int*)(lds + 147456);
  unsigned tks = 0;
#define POP() do { if (wid == 0 && opq(0) == 0) tks = __hip_atomic_fetch_add((gu32*)(P.ws + CNT_OFF + C_HEAD * 128), 1u, RLX_AGENT); } while (0)
  POP();
  for (int round = 0;; ++round) {
    if (wid == 0 && opq(0) == 0) tk[round & 1] = tks;
    LBARRIER();
    const int u = __builtin_amdgcn_readfirstlane(tk[round & 1]);
    if (u >= 2 * FINE + 8 * (XS3 - FINE)) break;
    const bool big = u < 2 * FINE;
    const int sg = big ? u >> 1 : FINE + ((u - 2 * FINE) >> 3), cc0 = big ? (u & 1) * 4 : (u - 2 * FINE) & 7;
    const int ln = opq(0), fq = ln >> 4, cb = 16 * (fq & 1) + 8 * (fq >> 1);
    const unsigned eo = (unsigned)(sg * 32 + wr * 16 + (ln & 15)) * 2048u + (unsigned)(cc0 * 128 + wc * 32 + cb) * 2u;
    const unsigned go = WS_G + (unsigned)(sg * 2 + wr) * 32768u + (cc0 * 4 + wc) * 1024 + blk_in(ln & 15, cb);
    const unsigned po = WS_PT + (unsigned)sg * 524288u + cc0 * 16384 + wc * 4096 + wr * 2048 + ln * 16;
    if (wid == 1) cnt_wait(P, C_W4(sg >> 3), 8, ln);
    const int xg = (sg >= XS1) + (sg >= XS2), want = sg + 1 - (xg == 2 ? XS2 : xg * XS1);
    if (wid == 2 && big) flag_wait(P, F_PRG(5 + xg, ln & 31), want, ln);
    BARRIER();
    if (!big) fin_out<1>(rs, P, po, go, eo, wid == 2 ? F_PRG(5 + xg, (ln & 3) * 8 + cc0) : -1, want);
    else { fin_out<2>(rs, P, po, go, eo); fin_out<2>(rs, P, po + 32768, go + 8192, eo + 512); }
    POP();
  }
}

#define QPRIO 0
#define VPOS 3
#define VLDI "buffer_load_dword v254, v17, %[rs], %[sb] offen sc1\n\tbuffer_load_dword v255, v17, %[rs], %[sb] offen offset:256 sc1\n\t"
#define VLD(p) VLD##p
#define VLD0
#define VLD1
#define VLD2
#define VLD3 VLDI
#define VLD4
#define DEC1 1
#define DEC2 1
#define DGA
#define DGB
#define DGC
#define DGO
#define DGP
#define DGQ
#define PACE "s_sleep 1\n\t"
#define STR_(x) #x
#define STR(x) STR_(x)
#define GSW
#define GSA(k)
#define GSO(k)
#define A_MAIN_Q() \
  asm volatile( \
    "v_mov_b32 v253, %[tag]\n\t" \
    "buffer_load_dwordx4 v[20:23], v12, %[rs], %[vx] offen sc1\n\t" \
    "buffer_load_dwordx4 v[24:27], v12, %[rs], %[vx] offen offset:1024 sc1\n\t" \
    "ds_read_b128 v[236:239], v9 offset:0\n\t" \
    "ds_read_b128 v[240:243], v9 offset:1024\n\t" \
    "ds_read_b128 v[244:247], v9 offset:2048\n\t" \
    "ds_read_b128 v[248:251], v9 offset:3072\n\t" \
    "v_mov_b32 v0, 0\n\t" \
    "v_mov_b32 v1, 0\n\t" \
    "v_mov_b32 v2, 0\n\t" \
    "v_mov_b32 v3, 0\n\t" \
    "v_mov_b32 v4, 0\n\t" \
    "v_mov_b32 v5, 0\n\t" \
    "v_mov_b32 v6, 0\n\t" \
    "v_mov_b32 v7, 0\n\t" \
    "s_cmp_lg_u32 %[sc], 3\n\t" \
    "s_cbranch_scc1 PB_%=\n\t" \
    "TWA_%=:\n\t" \
    "s_memrealtime s[88:89]\n\t" \
    "s_memtime s[90:91]\n\t" \
    "s_waitcnt lgkmcnt(0)\n\t" \
    "s_lshl_b32 s88, s88, 2\n\t" \
    "s_sub_u32 s89, %[tgt], s88\n\t" \
    "s_cmp_gt_i32 s89, 16\n\t" \
    "s_cbranch_scc1 TWA_%=\n\t" \
    "s_mul_i32 s89, s89, 11\n\t" \
    "s_ashr_i32 s89, s89, 1\n\t" \
    "s_add_u32 s89, s89, s90\n\t" \
    DGB \
    "TCA_%=:\n\t" \
    "s_memtime s[92:93]\n\t" \
    "s_waitcnt lgkmcnt(0)\n\t" \
    "s_sub_u32 s93, s89, s92\n\t" \
    "s_cmp_gt_i32 s93, 0\n\t" \
    "s_cbranch_scc1 TCA_%=\n\t" \
    "PB_%=:\n\t" \
    "s_waitcnt lgkmcnt(0)\n\t" \
    "s_barrier\n\t" \
    "buffer_load_dwordx4 v[220:223], v8, %[rs], %[m0] offen sc1\n\t" \
    "buffer_load_dwordx4 v[224:227], v8, %[rs], %[m1] offen sc1\n\t" \
    "PM_%=:\n\t" \
    "s_waitcnt vmcnt(1)\n\t" \
    "ds_write_b128 v8, v[220:223] offset:0\n\t" \
    "v_max3_u32 v252, v220, v221, v222\n\t" \
    "s_waitcnt vmcnt(0)\n\t" \
    "ds_write_b128 v8, v[224:227] offset:4096\n\t" \
    "v_max3_u32 v252, v252, v223, v224\n\t" \
    "v_max3_u32 v252, v252, v225, v226\n\t" \
    "v_max_u32 v252, v252, v227\n\t" \
    "v_cmp_eq_u32 vcc, -1, v252\n\t" \
    "s_cbranch_vccz PD_%=\n\t" \
    "s_sub_u32 %[lim], %[lim], 1\n\t" \
    "s_cmp_gt_i32 %[lim], 0\n\t" \
    "s_cbranch_scc0 PD_%=\n\t" \
    "s_sleep " STR(CADM) "\n\t" \
    "buffer_load_dwordx4 v[220:223], v8, %[rs], %[m0] offen sc1\n\t" \
    "buffer_load_dwordx4 v[224:227], v8, %[rs], %[m1] offen sc1\n\t" \
    "s_branch PM_%=\n\t" \
    "PD_%=:\n\t" \
    GSA(0) \
    "v_smfmac_f32_16x16x64_bf16 v[0:3], v[220:223], v[236:243], v10\n\t" \
    "v_xor_b32 v9, 16384, v9\n\t" \
    "ds_read_b128 v[236:239], v9 offset:0\n\t" \
    "ds_read_b128 v[240:243], v9 offset:1024\n\t" \
    "v_smfmac_f32_16x16x64_bf16 v[4:7], v[220:223], v[244:251], v10\n\t" \
    "ds_read_b128 v[244:247], v9 offset:2048\n\t" \
    "ds_read_b128 v[248:251], v9 offset:3072\n\t" \
    "s_waitcnt lgkmcnt(4)\n\t" \
    "s_barrier\n\t" \
    "ds_read_b128 v[228:231], v8 offset:8192\n\t" \
    "ds_read_b128 v[232:235], v8 offset:12288\n\t" \
    "v_xor_b32 v8, 1024, v8\n\t" \
    "ds_read_b128 v[220:223], v8 offset:0\n\t" \
    "v_smfmac_f32_16x16x64_bf16 v[0:3], v[224:227], v[28:35], v10\n\t" \
    "v_smfmac_f32_16x16x64_bf16 v[4:7], v[224:227], v[36:43], v10\n\t" \
    "ds_read_b128 v[224:227], v8 offset:4096\n\t" \
    "s_waitcnt lgkmcnt(3)\n\t" \
    "v_smfmac_f32_16x16x64_bf16 v[0:3], v[228:231], v[92:99], v10\n\t" \
    "v_smfmac_f32_16x16x64_bf16 v[4:7], v[228:231], v[100:107], v10\n\t" \
    "ds_read_b128 v[228:231], v8 offset:8192\n\t" \
    "s_waitcnt lgkmcnt(3)\n\t" \
    "v_smfmac_f32_16x16x64_bf16 v[0:3], v[232:235], v[156:163], v10\n\t" \
    "v_smfmac_f32_16x16x64_bf16 v[4:7], v[232:235], v[164:171], v10\n\t" \
    "ds_read_b128 v[232:235], v8 offset:12288\n\t" \
    "s_waitcnt lgkmcnt(3)\n\t" \
    "v_smfmac_f32_16x16x64_bf16 v[0:3], v[220:223], v[236:243], v10\n\t" \
    "v_xor_b32 v9, 49152, v9\n\t" \
    "ds_read_b128 v[236:239], v9 offset:0\n\t" \
    "v_smfmac_f32_16x16x64_bf16 v[4:7], v[220:223], v[244:251], v10\n\t" \
    "ds_read_b128 v[240:243], v9 offset:1024\n\t" \
    "s_waitcnt lgkmcnt(4)\n\t" \
    "v_smfmac_f32_16x16x64_bf16 v[0:3], v[224:227], v[44:51], v10\n\t" \
    "v_xor_b32 v8, 3072, v8\n\t" \
    "ds_read_b128 v[220:223], v8 offset:4096\n\t" \
    "v_smfmac_f32_16x16x64_bf16 v[4:7], v[224:227], v[52:59], v10\n\t" \
    "ds_read_b128 v[244:247], v9 offset:2048\n\t" \
    "s_waitcnt lgkmcnt(5)\n\t" \
    "v_smfmac_f32_16x16x64_bf16 v[0:3], v[228:231], v[108:115], v10\n\t" \
    "ds_read_b128 v[224:227], v8 offset:8192\n\t" \
    "v_smfmac_f32_16x16x64_bf16 v[4:7], v[228:231], v[116:123], v10\n\t" \
    "ds_read_b128 v[248:251], v9 offset:3072\n\t" \
    "s_waitcnt lgkmcnt(6)\n\t" \
    "v_smfmac_f32_16x16x64_bf16 v[0:3], v[232:235], v[172:179], v10\n\t" \
    "ds_read_b128 v[228:231], v8 offset:0\n\t" \
    "v_smfmac_f32_16x16x64_bf16 v[4:7], v[232:235], v[180:187], v10\n\t" \
    "ds_read_b128 v[232:235], v8 offset:12288\n\t" \
    "s_waitcnt lgkmcnt(5)\n\t" \
    "v_smfmac_f32_16x16x64_bf16 v[0:3], v[220:223], v[60:67], v10\n\t" \
    "v_smfmac_f32_16x16x64_bf16 v[4:7], v[220:223], v[68:75], v10\n\t" \
    "v_xor_b32 v8, 1024, v8\n\t" \
    "ds_read_b128 v[220:223], v8 offset:4096\n\t" \
    "s_waitcnt lgkmcnt(4)\n\t" \
    "v_smfmac_f32_16x16x64_bf16 v[0:3], v[224:227], v[124:131], v10\n\t" \
    "v_smfmac_f32_16x16x64_bf16 v[4:7], v[224:227], v[132:139], v10\n\t" \
    "ds_read_b128 v[224:227], v8 offset:8192\n\t" \
    "s_waitcnt lgkmcnt(3)\n\t" \
    "v_smfmac_f32_16x16x64_bf16 v[0:3], v[228:231], v[236:243], v10\n\t" \
    "v_xor_b32 v9, 16384, v9\n\t" \
    "ds_read_b128 v[236:239], v9 offset:0\n\t" \
    "v_smfmac_f32_16x16x64_bf16 v[4:7], v[228:231], v[244:251], v10\n\t" \
    "ds_read_b128 v[240:243], v9 offset:1024\n\t" \
    "s_waitcnt lgkmcnt(4)\n\t" \
    "v_smfmac_f32_16x16x64_bf16 v[0:3], v[232:235], v[188:195], v10\n\t" \
    "ds_read_b128 v[228:231], v8 offset:12288\n\t" \
    "v_smfmac_f32_16x16x64_bf16 v[4:7], v[232:235], v[196:203], v10\n\t" \
    "ds_read_b128 v[244:247], v9 offset:2048\n\t" \
    "s_waitcnt lgkmcnt(5)\n\t" \
    "v_smfmac_f32_16x16x64_bf16 v[0:3], v[220:223], v[76:83], v10\n\t" \
    "ds_read_b128 v[232:235], v8 offset:0\n\t" \
    "v_xor_b32 v8, 3072, v8\n\t" \
    "v_smfmac_f32_16x16x64_bf16 v[4:7], v[220:223], v[84:91], v10\n\t" \
    "ds_read_b128 v[248:251], v9 offset:3072\n\t" \
    "s_waitcnt lgkmcnt(6)\n\t" \
    "v_smfmac_f32_16x16x64_bf16 v[0:3], v[224:227], v[140:147], v10\n\t" \
    "ds_read_b128 v[220:223], v12 offset:24576\n\t" \
    "v_smfmac_f32_16x16x64_bf16 v[4:7], v[224:227], v[148:155], v10\n\t" \
    "ds_read_b128 v[224:227], v12 offset:28672\n\t" \
    "s_waitcnt lgkmcnt(5)\n\t" \
    "v_smfmac_f32_16x16x64_bf16 v[0:3], v[228:231], v[204:211], v10\n\t" \
    "v_smfmac_f32_16x16x64_bf16 v[4:7], v[228:231], v[212:219], v10\n\t" \
    "ds_read_b128 v[228:231], v12 offset:25600\n\t" \
    "s_waitcnt lgkmcnt(3)\n\t" \
    "v_smfmac_f32_16x16x64_bf16 v[0:3], v[232:235], v[236:243], v10\n\t" \
    "v_xor_b32 v9, 49152, v9\n\t" \
    "v_smfmac_f32_16x16x64_bf16 v[4:7], v[232:235], v[244:251], v10\n\t" \
    VLD(4) \
    "s_nop 5\n\t" \
    "v_add_f32 v0, v0, v1\n\t" \
    "v_add_f32 v2, v2, v3\n\t" \
    "v_add_f32 v4, v4, v5\n\t" \
    "v_add_f32 v6, v6, v7\n\t" \
    "v_mul_f32 v0, %[sl], v0\n\t" \
    "v_mul_f32 v2, %[sl], v2\n\t" \
    "v_mul_f32 v4, %[sl], v4\n\t" \
    "v_mul_f32 v6, %[sl], v6\n\t" \
    "v_cvt_pk_bf16_f32 v0, v0, v4\n\t" \
    "v_cvt_pk_bf16_f32 v2, v2, v6\n\t" \
    "ds_write2_b32 v11, v0, v2 offset0:0 offset1:4\n\t" \
    "s_mov_b64 exec, %[onej]\n\t" \
    "ds_write_b32 v16, v253\n\t" \
    "s_mov_b64 exec, -1\n\t" \
    GSA(1) \
    "s_setprio " STR(QPRIO) "\n\t" \
    "ds_read_b128 v[232:235], v12 offset:29696\n\t" \
    "s_nop 5\n\t" \
    "ds_read_b128 v[0:3], v12 offset:26624\n\t" \
    "s_nop 5\n\t" \
    "ds_read_b128 v[4:7], v12 offset:27648\n\t" \
    "s_nop 5\n\t" \
    "ds_read_b32 v252, v16\n\t" \
    "ds_read_b128 v[236:239], v14 offset:0\n\t" \
    "s_nop 5\n\t" \
    "ds_read_b128 v[240:243], v14 offset:512\n\t" \
    "s_nop 5\n\t" \
    "ds_read_b128 v[244:247], v14 offset:1024\n\t" \
    "s_nop 5\n\t" \
    VLD(3) \
    "ds_read_b128 v[248:251], v14 offset:1536\n\t" \
    "s_nop 5\n\t" \
    "s_setprio 0\n\t" \
    "s_waitcnt lgkmcnt(4)\n\t" \
    "v_cmp_ne_u32 vcc, v253, v252\n\t" \
    "s_cbranch_vccnz RP_%=\n\t" \
    "CP_%=:\n\t" \
    VLD(0) \
    "s_waitcnt lgkmcnt(2)\n\t" \
    "v_mfma_f32_16x16x32_bf16 v[220:223], v[220:223], v[236:239], 0\n\t" \
    "v_mfma_f32_16x16x32_bf16 v[224:227], v[224:227], v[236:239], 0\n\t" \
    "v_mfma_f32_16x16x32_bf16 v[220:223], v[228:231], v[240:243], v[220:223]\n\t" \
    "ds_read_b128 v[228:231], v12 offset:30720\n\t" \
    "v_mfma_f32_16x16x32_bf16 v[224:227], v[232:235], v[240:243], v[224:227]\n\t" \
    "ds_read_b128 v[232:235], v12 offset:31744\n\t" \
    "s_setprio " STR(QPRIO) "\n\t" \
    VLD(1) \
    "s_waitcnt lgkmcnt(2)\n\t" \
    "v_mfma_f32_16x16x32_bf16 v[220:223], v[0:3], v[244:247], v[220:223]\n\t" \
    "v_mfma_f32_16x16x32_bf16 v[220:223], v[4:7], v[248:251], v[220:223]\n\t" \
    "s_waitcnt lgkmcnt(1)\n\t" \
    "v_mfma_f32_16x16x32_bf16 v[224:227], v[228:231], v[244:247], v[224:227]\n\t" \
    "s_waitcnt lgkmcnt(0)\n\t" \
    "v_mfma_f32_16x16x32_bf16 v[224:227], v[232:235], v[248:251], v[224:227]\n\t" \
    GSA(2) \
    "s_setprio 2\n\t" \
    "ds_read_b32 v252, v16 offset:16\n\t" \
    "ds_read_b128 v[0:3], v15 offset:0\n\t" \
    "ds_read_b128 v[4:7], v15 offset:512\n\t" \
    "ds_read_b128 v[228:231], v15 offset:1024\n\t" \
    "ds_read_b128 v[232:235], v15 offset:1536\n\t" \
    VLD(2) \
    "v_exp_f32 v220, v220\n\t" \
    "v_exp_f32 v221, v221\n\t" \
    "v_exp_f32 v222, v222\n\t" \
    "v_exp_f32 v223, v223\n\t" \
    "v_exp_f32 v224, v224\n\t" \
    "v_exp_f32 v225, v225\n\t" \
    "v_exp_f32 v226, v226\n\t" \
    "v_exp_f32 v227, v227\n\t" \
    "v_cvt_pk_bf16_f32 v220, v220, v221\n\t" \
    "v_cvt_pk_bf16_f32 v221, v222, v223\n\t" \
    "s_waitcnt lgkmcnt(4)\n\t" \
    "v_cmp_ne_u32 vcc, %[tag], v252\n\t" \
    "s_cbranch_vccnz RK_%=\n\t" \
    "CK_%=:\n\t" \
    "s_waitcnt lgkmcnt(3)\n\t" \
    "v_mfma_f32_16x16x32_bf16 v[0:3], v[0:3], v[236:239], 0\n\t" \
    "v_cvt_pk_bf16_f32 v222, v224, v225\n\t" \
    "v_cvt_pk_bf16_f32 v223, v226, v227\n\t" \
    "s_waitcnt lgkmcnt(2)\n\t" \
    "v_mfma_f32_16x16x32_bf16 v[0:3], v[4:7], v[240:243], v[0:3]\n\t" \
    "v_mov_b32 v224, %[one]\n\t" \
    "v_mov_b32 v225, %[one]\n\t" \
    "s_waitcnt lgkmcnt(1)\n\t" \
    "v_mfma_f32_16x16x32_bf16 v[0:3], v[228:231], v[244:247], v[0:3]\n\t" \
    "v_mov_b32 v226, %[one]\n\t" \
    "v_mov_b32 v227, %[one]\n\t" \
    "s_waitcnt lgkmcnt(0)\n\t" \
    "v_mfma_f32_16x16x32_bf16 v[0:3], v[232:235], v[248:251], v[0:3]\n\t" \
    "v_mfma_f32_16x16x32_bf16 v[244:247], v[224:227], v[220:223], 0\n\t" \
    "v_mov_b32 v229, 0\n\t" \
    "v_mfma_f32_16x16x32_bf16 v[236:239], v[20:23], v[220:223], 0\n\t" \
    "v_mov_b32 v230, 0\n\t" \
    "v_mfma_f32_16x16x32_bf16 v[240:243], v[24:27], v[220:223], 0\n\t" \
    "s_nop 2\n\t" \
    "v_exp_f32 v0, v0\n\t" \
    "v_exp_f32 v1, v1\n\t" \
    "s_nop 0\n\t" \
    "v_cvt_pk_bf16_f32 v228, v0, v1\n\t" \
    "v_cvt_pk_bf16_f32 v231, v0, v1\n\t" \
    GSA(3) \
    "CM_%=:\n\t" \
    "s_nop 0\n\t" \
    "v_mfma_f32_16x16x16_bf16 v[244:247], v[224:225], v[228:229], v[244:247]\n\t" \
    "s_waitcnt vmcnt(0)\n\t" \
    "v_mfma_f32_16x16x16_bf16 v[236:239], v[254:255], v[228:229], v[236:239]\n\t" \
    "v_max_u32 v7, v254, v255\n\t" \
    "v_mfma_f32_16x16x16_bf16 v[240:243], v[254:255], v[230:231], v[240:243]\n\t" \
    "v_cmp_eq_u32 s[88:89], -1, v7\n\t" \
    "s_not_b64 exec, %[hi]\n\t" \
    "s_nop 1\n\t" \
    "v_rcp_f32 v2, v244\n\t" \
    "v_subrev_u32 v253, 0x21800000, v244\n\t" \
    "v_cmp_gt_u32 vcc, v253, %[rg]\n\t" \
    "v_pk_mul_f32 v[236:237], v[236:237], v[2:3] op_sel_hi:[1,0]\n\t" \
    "v_pk_mul_f32 v[238:239], v[238:239], v[2:3] op_sel_hi:[1,0]\n\t" \
    "v_pk_mul_f32 v[240:241], v[240:241], v[2:3] op_sel_hi:[1,0]\n\t" \
    "v_pk_mul_f32 v[242:243], v[242:243], v[2:3] op_sel_hi:[1,0]\n\t" \
    "s_or_b64 vcc, vcc, s[88:89]\n\t" \
    "v_cvt_pk_bf16_f32 v4, v236, v237\n\t" \
    "v_cvt_pk_bf16_f32 v5, v238, v239\n\t" \
    "v_cvt_pk_bf16_f32 v6, v240, v241\n\t" \
    "v_cvt_pk_bf16_f32 v7, v242, v243\n\t" \
    "s_cbranch_scc1 FX_%=\n\t" \
    "buffer_store_dwordx4 v[4:7], v18, %[rs], %[sb] offen sc1\n\t" \
    "s_memrealtime %[tp]\n\t" \
    DGA \
    GSA(4) \
    "v_max3_u32 v252, v4, v5, v6\n\t" \
    "v_max_u32 v252, v252, v7\n\t" \
    "v_cmp_eq_u32 vcc, -1, v252\n\t" \
    "s_cbranch_vccnz RC_%=\n\t" \
    "CC_%=:\n\t" \
    "s_mov_b64 exec, -1\n\t" \
    "v_add_u32 v252, %[vd], v12\n\t" \
    "v_add_u32 v253, %[md], v17\n\t" \
    "ds_write_b128 v252, v[20:23]\n\t" \
    "ds_write_b128 v252, v[24:27] offset:1024\n\t" \
    "ds_write2st64_b32 v253, v254, v255 offset0:0 offset1:1\n\t" \
    "s_waitcnt lgkmcnt(0)\n\t" \
    "s_cmp_lg_u32 %[sc], 3\n\t" \
    "s_cbranch_scc1 NH_%=\n\t" \
    "buffer_load_dwordx2 v[0:1], off, %[rs], %[fl] sc1\n\t" \
    "s_barrier\n\t" \
    "s_waitcnt vmcnt(0)\n\t" \
    "v_readfirstlane_b32 %[pt], v0\n\t" \
    "v_readfirstlane_b32 %[pd], v1\n\t" \
    "s_nop 3\n\t" \
    "s_branch E_%=\n\t" \
    "NH_%=:\n\t" \
    "s_barrier\n\t" \
    "s_branch E_%=\n\t" \
    "RP_%=:\n\t" \
    "s_waitcnt lgkmcnt(0)\n\t" \
    "ds_read_b32 v252, v16\n\t" \
    "ds_read_b128 v[236:239], v14 offset:0\n\t" \
    "ds_read_b128 v[240:243], v14 offset:512\n\t" \
    "ds_read_b128 v[244:247], v14 offset:1024\n\t" \
    "ds_read_b128 v[248:251], v14 offset:1536\n\t" \
    "s_sub_u32 %[lim], %[lim], " STR(DEC1) "\n\t" \
    "s_cmp_gt_i32 %[lim], 0\n\t" \
    "s_waitcnt lgkmcnt(4)\n\t" \
    "s_cbranch_scc0 CP_%=\n\t" \
    "v_cmp_ne_u32 vcc, %[tag], v252\n\t" \
    "s_cbranch_vccnz RP_%=\n\t" \
    "s_branch CP_%=\n\t" \
    "RK_%=:\n\t" \
    "s_waitcnt lgkmcnt(0)\n\t" \
    "ds_read_b32 v252, v16 offset:16\n\t" \
    "ds_read_b128 v[0:3], v15 offset:0\n\t" \
    "ds_read_b128 v[4:7], v15 offset:512\n\t" \
    "ds_read_b128 v[228:231], v15 offset:1024\n\t" \
    "ds_read_b128 v[232:235], v15 offset:1536\n\t" \
    "s_sub_u32 %[lim], %[lim], " STR(DEC1) "\n\t" \
    "s_cmp_gt_i32 %[lim], 0\n\t" \
    "s_waitcnt lgkmcnt(4)\n\t" \
    "s_cbranch_scc0 CK_%=\n\t" \
    "v_cmp_ne_u32 vcc, %[tag], v252\n\t" \
    "s_cbranch_vccnz RK_%=\n\t" \
    "s_branch CK_%=\n\t" \
    "RC_%=:\n\t" \
    "v_min_u32 v4, -2, v4\n\t" \
    "v_min_u32 v5, -2, v5\n\t" \
    "v_min_u32 v6, -2, v6\n\t" \
    "v_min_u32 v7, -2, v7\n\t" \
    "buffer_store_dwordx4 v[4:7], v18, %[rs], %[sb] offen sc1\n\t" \
    "s_branch CC_%=\n\t" \
    "FX_%=:\n\t" \
    "s_mov_b64 exec, -1\n\t" \
    "s_cmp_eq_u64 s[88:89], 0\n\t" \
    "s_cbranch_scc1 RM_%=\n\t" \
    "buffer_load_dword v254, v17, %[rs], %[sb] offen sc1\n\t" \
    "buffer_load_dword v255, v17, %[rs], %[sb] offen offset:256 sc1\n\t" \
    "v_mfma_f32_16x16x32_bf16 v[244:247], v[224:227], v[220:223], 0\n\t" \
    "v_mfma_f32_16x16x32_bf16 v[236:239], v[20:23], v[220:223], 0\n\t" \
    "v_mfma_f32_16x16x32_bf16 v[240:243], v[24:27], v[220:223], 0\n\t" \
    "s_sub_u32 %[lim], %[lim], " STR(DEC2) "\n\t" \
    "s_cmp_gt_i32 %[lim], 0\n\t" \
    "s_cbranch_scc1 CM_%=\n\t" \
    "s_waitcnt vmcnt(0)\n\t" \
    "v_mov_b32 v254, 0\n\t" \
    "v_mov_b32 v255, 0\n\t" \
    "s_branch CM_%=\n\t" \
    "RM_%=:\n\t" \
    "s_mov_b32 %[rg], -1\n\t" \
    "s_nop 7\n\t" \
    "ds_read_b128 v[236:239], v14 offset:0\n\t" \
    "ds_read_b128 v[240:243], v14 offset:512\n\t" \
    "ds_read_b128 v[244:247], v14 offset:1024\n\t" \
    "ds_read_b128 v[248:251], v14 offset:1536\n\t" \
    "ds_read_b128 v[232:235], v12 offset:24576\n\t" \
    "s_waitcnt lgkmcnt(0)\n\t" \
    "v_mfma_f32_16x16x32_bf16 v[220:223], v[232:235], v[236:239], 0\n\t" \
    "ds_read_b128 v[232:235], v12 offset:25600\n\t" \
    "s_waitcnt lgkmcnt(0)\n\t" \
    "v_mfma_f32_16x16x32_bf16 v[220:223], v[232:235], v[240:243], v[220:223]\n\t" \
    "ds_read_b128 v[232:235], v12 offset:26624\n\t" \
    "s_waitcnt lgkmcnt(0)\n\t" \
    "v_mfma_f32_16x16x32_bf16 v[220:223], v[232:235], v[244:247], v[220:223]\n\t" \
    "ds_read_b128 v[232:235], v12 offset:27648\n\t" \
    "s_waitcnt lgkmcnt(0)\n\t" \
    "v_mfma_f32_16x16x32_bf16 v[220:223], v[232:235], v[248:251], v[220:223]\n\t" \
    "ds_read_b128 v[232:235], v12 offset:28672\n\t" \
    "s_waitcnt lgkmcnt(0)\n\t" \
    "v_mfma_f32_16x16x32_bf16 v[224:227], v[232:235], v[236:239], 0\n\t" \
    "ds_read_b128 v[232:235], v12 offset:29696\n\t" \
    "s_waitcnt lgkmcnt(0)\n\t" \
    "v_mfma_f32_16x16x32_bf16 v[224:227], v[232:235], v[240:243], v[224:227]\n\t" \
    "ds_read_b128 v[232:235], v12 offset:30720\n\t" \
    "s_waitcnt lgkmcnt(0)\n\t" \
    "v_mfma_f32_16x16x32_bf16 v[224:227], v[232:235], v[244:247], v[224:227]\n\t" \
    "ds_read_b128 v[232:235], v12 offset:31744\n\t" \
    "s_waitcnt lgkmcnt(0)\n\t" \
    "v_mfma_f32_16x16x32_bf16 v[224:227], v[232:235], v[248:251], v[224:227]\n\t" \
    "ds_read_b128 v[232:235], v15 offset:0\n\t" \
    "s_waitcnt lgkmcnt(0)\n\t" \
    "v_mfma_f32_16x16x32_bf16 v[0:3], v[232:235], v[236:239], 0\n\t" \
    "ds_read_b128 v[232:235], v15 offset:512\n\t" \
    "s_waitcnt lgkmcnt(0)\n\t" \
    "v_mfma_f32_16x16x32_bf16 v[0:3], v[232:235], v[240:243], v[0:3]\n\t" \
    "ds_read_b128 v[232:235], v15 offset:1024\n\t" \
    "s_waitcnt lgkmcnt(0)\n\t" \
    "v_mfma_f32_16x16x32_bf16 v[0:3], v[232:235], v[244:247], v[0:3]\n\t" \
    "ds_read_b128 v[232:235], v15 offset:1536\n\t" \
    "s_waitcnt lgkmcnt(0)\n\t" \
    "v_mfma_f32_16x16x32_bf16 v[0:3], v[232:235], v[248:251], v[0:3]\n\t" \
    "v_max3_f32 v252, v220, v221, v222\n\t" \
    "v_max3_f32 v253, v223, v224, v225\n\t" \
    "v_max3_f32 v252, v252, v226, v227\n\t" \
    "s_nop 4\n\t" \
    "v_max3_f32 v253, v253, v0, v1\n\t" \
    "v_max_f32 v252, v252, v253\n\t" \
    "v_mov_b32 v253, v252\n\t" \
    "v_mov_b32 v2, v252\n\t" \
    "s_nop 1\n\t" \
    "v_permlane16_swap_b32 v253, v2\n\t" \
    "v_max_f32 v252, v253, v2\n\t" \
    "v_max_f32 v253, v253, v2\n\t" \
    "s_nop 1\n\t" \
    "v_permlane32_swap_b32 v252, v253\n\t" \
    "v_max_f32 v252, v252, v253\n\t" \
    "v_sub_f32 v220, v220, v252\n\t" \
    "v_sub_f32 v221, v221, v252\n\t" \
    "v_sub_f32 v222, v222, v252\n\t" \
    "v_sub_f32 v223, v223, v252\n\t" \
    "v_sub_f32 v224, v224, v252\n\t" \
    "v_sub_f32 v225, v225, v252\n\t" \
    "v_sub_f32 v226, v226, v252\n\t" \
    "v_sub_f32 v227, v227, v252\n\t" \
    "v_sub_f32 v0, v0, v252\n\t" \
    "v_sub_f32 v1, v1, v252\n\t" \
    "v_exp_f32 v220, v220\n\t" \
    "v_exp_f32 v221, v221\n\t" \
    "v_exp_f32 v222, v222\n\t" \
    "v_exp_f32 v223, v223\n\t" \
    "v_exp_f32 v224, v224\n\t" \
    "v_exp_f32 v225, v225\n\t" \
    "v_exp_f32 v226, v226\n\t" \
    "v_exp_f32 v227, v227\n\t" \
    "v_cvt_pk_bf16_f32 v220, v220, v221\n\t" \
    "v_cvt_pk_bf16_f32 v221, v222, v223\n\t" \
    "v_cvt_pk_bf16_f32 v222, v224, v225\n\t" \
    "v_cvt_pk_bf16_f32 v223, v226, v227\n\t" \
    "v_mov_b32 v224, %[one]\n\t" \
    "v_mov_b32 v225, %[one]\n\t" \
    "v_mov_b32 v226, %[one]\n\t" \
    "v_mov_b32 v227, %[one]\n\t" \
    "s_nop 1\n\t" \
    "v_mfma_f32_16x16x32_bf16 v[244:247], v[224:227], v[220:223], 0\n\t" \
    "v_mfma_f32_16x16x32_bf16 v[236:239], v[20:23], v[220:223], 0\n\t" \
    "v_mfma_f32_16x16x32_bf16 v[240:243], v[24:27], v[220:223], 0\n\t" \
    "v_exp_f32 v0, v0\n\t" \
    "v_exp_f32 v1, v1\n\t" \
    "s_nop 0\n\t" \
    "v_cvt_pk_bf16_f32 v228, v0, v1\n\t" \
    "v_cvt_pk_bf16_f32 v231, v0, v1\n\t" \
    "s_nop 1\n\t" \
    "s_branch CM_%=\n\t" \
    "E_%=:\n\t" \
    GSW \
    "s_nop 1\n\t" \
    : [lim] "+s"(lim), [rg] "+s"(rg), [tp] "=&s"(tp), [pt] "=&s"(pt), [pd] "=&s"(pd) DGO DGP GSO(0) GSO(1) GSO(2) GSO(3) GSO(4) \
    : "{v8}"(am_a), "{v9}"(t0_a), "{v10}"(idx), "{v11}"(iw_a), "{v12}"(ab_a), "{v14}"(qr_a), "{v15}"(kr_a), "{v16}"(fr_a), "{v17}"(vo_v), "{v18}"(oo_v), [rs] "s"(rs), [sb] "s"(sb), [tag] "s"(0xffffff81u + s), [hi] "s"(0xff00ff00ff00ff00ull), [tgt] "s"(tgt), [sc] "s"(wid), [onej] "s"(onej), [sl] "s"(0x3e0293ee), [one] "s"(0x3f803f80), [m0] "s"(sbm), [m1] "s"(sbm + 4096u), [vx] "s"(vxo), [fl] "s"(fb + FL_T(s + 1)), [vd] "s"(8192 + j * 2048), [md] "s"(md), "{v[28:35]}"(wr[0][0]), "{v[36:43]}"(wr[0][1]), "{v[44:51]}"(wr[1][0]), "{v[52:59]}"(wr[1][1]), "{v[60:67]}"(wr[2][0]), "{v[68:75]}"(wr[2][1]), "{v[76:83]}"(wr[3][0]), "{v[84:91]}"(wr[3][1]), "{v[92:99]}"(wr[4][0]), "{v[100:107]}"(wr[4][1]), "{v[108:115]}"(wr[5][0]), "{v[116:123]}"(wr[5][1]), "{v[124:131]}"(wr[6][0]), "{v[132:139]}"(wr[6][1]), "{v[140:147]}"(wr[7][0]), "{v[148:155]}"(wr[7][1]), "{v[156:163]}"(wr[8][0]), "{v[164:171]}"(wr[8][1]), "{v[172:179]}"(wr[9][0]), "{v[180:187]}"(wr[9][1]), "{v[188:195]}"(wr[10][0]), "{v[196:203]}"(wr[10][1]), "{v[204:211]}"(wr[11][0]), "{v[212:219]}"(wr[11][1]) \
    : "memory", "vcc", "scc", "s88", "s89", "s90", "s91", "s92", "s93", "v0", "v1", "v2", "v3", "v4", "v5", "v6", "v7", "v20", "v21", "v22", "v23", "v24", "v25", "v26", "v27", "v220", "v221", "v222", "v223", "v224", "v225", "v226", "v227", "v228", "v229", "v230", "v231", "v232", "v233", "v234", "v235", "v236", "v237", "v238", "v239", "v240", "v241", "v242", "v243", "v244", "v245", "v246", "v247", "v248", "v249", "v250", "v251", "v252", "v253", "v254", "v255")

#define A_MAIN_K() \
  asm volatile( \
    "ds_read_b128 v[236:239], v9 offset:0\n\t" \
    "ds_read_b128 v[240:243], v9 offset:1024\n\t" \
    "ds_read_b128 v[244:247], v9 offset:2048\n\t" \
    "ds_read_b128 v[248:251], v9 offset:3072\n\t" \
    "v_mov_b32 v0, 0\n\t" \
    "v_mov_b32 v1, 0\n\t" \
    "v_mov_b32 v2, 0\n\t" \
    "v_mov_b32 v3, 0\n\t" \
    "v_mov_b32 v4, 0\n\t" \
    "v_mov_b32 v5, 0\n\t" \
    "v_mov_b32 v6, 0\n\t" \
    "v_mov_b32 v7, 0\n\t" \
    "s_waitcnt lgkmcnt(0)\n\t" \
    "s_barrier\n\t" \
    "buffer_load_dwordx4 v[220:223], v8, %[rs], %[m0] offen sc1\n\t" \
    "buffer_load_dwordx4 v[224:227], v8, %[rs], %[m1] offen sc1\n\t" \
    GSA(3) \
    "v_mov_b32 v252, v252\n\t" \
    "s_nop 2\n\t" \
    "v_mov_b32 v252, v252\n\t" \
    "s_nop 2\n\t" \
    "v_mov_b32 v252, v252\n\t" \
    "s_nop 2\n\t" \
    "v_mov_b32 v252, v252\n\t" \
    "s_nop 2\n\t" \
    "v_mov_b32 v252, v252\n\t" \
    "s_nop 2\n\t" \
    "v_mov_b32 v252, v252\n\t" \
    "s_nop 2\n\t" \
    "v_mov_b32 v252, v252\n\t" \
    "s_nop 2\n\t" \
    "v_mov_b32 v252, v252\n\t" \
    "s_nop 2\n\t" \
    "v_mov_b32 v252, v252\n\t" \
    "s_nop 2\n\t" \
    "v_mov_b32 v252, v252\n\t" \
    "s_nop 2\n\t" \
    "v_mov_b32 v252, v252\n\t" \
    "s_nop 2\n\t" \
    "v_mov_b32 v252, v252\n\t" \
    "s_nop 2\n\t" \
    "v_mov_b32 v252, v252\n\t" \
    "s_nop 2\n\t" \
    "v_mov_b32 v252, v252\n\t" \
    "s_nop 2\n\t" \
    "v_mov_b32 v252, v252\n\t" \
    "s_nop 2\n\t" \
    "v_mov_b32 v252, v252\n\t" \
    "s_nop 2\n\t" \
    "v_mov_b32 v252, v252\n\t" \
    "s_nop 2\n\t" \
    "v_mov_b32 v252, v252\n\t" \
    "s_nop 2\n\t" \
    "v_mov_b32 v252, v252\n\t" \
    "s_nop 2\n\t" \
    "v_mov_b32 v252, v252\n\t" \
    "s_nop 2\n\t" \
    "v_mov_b32 v252, v252\n\t" \
    "s_nop 2\n\t" \
    "v_mov_b32 v252, v252\n\t" \
    "s_nop 2\n\t" \
    "v_mov_b32 v252, v252\n\t" \
    "s_nop 2\n\t" \
    "v_mov_b32 v252, v252\n\t" \
    "s_nop 2\n\t" \
    "v_mov_b32 v252, v252\n\t" \
    "s_nop 2\n\t" \
    "v_mov_b32 v252, v252\n\t" \
    "s_nop 2\n\t" \
    "v_mov_b32 v252, v252\n\t" \
    "s_nop 2\n\t" \
    "v_mov_b32 v252, v252\n\t" \
    "s_nop 2\n\t" \
    "v_mov_b32 v252, v252\n\t" \
    "s_nop 2\n\t" \
    "v_mov_b32 v252, v252\n\t" \
    "s_nop 2\n\t" \
    "PM_%=:\n\t" \
    "s_waitcnt vmcnt(1)\n\t" \
    "ds_write_b128 v8, v[220:223] offset:8192\n\t" \
    "v_max3_u32 v252, v220, v221, v222\n\t" \
    "s_waitcnt vmcnt(0)\n\t" \
    "ds_write_b128 v8, v[224:227] offset:12288\n\t" \
    "v_max3_u32 v252, v252, v223, v224\n\t" \
    "v_max3_u32 v252, v252, v225, v226\n\t" \
    "v_max_u32 v252, v252, v227\n\t" \
    "v_cmp_eq_u32 vcc, -1, v252\n\t" \
    "s_cbranch_vccz PD_%=\n\t" \
    "s_sub_u32 %[lim], %[lim], 1\n\t" \
    "s_cmp_gt_i32 %[lim], 0\n\t" \
    "s_cbranch_scc0 PD_%=\n\t" \
    "s_sleep " STR(CADM) "\n\t" \
    "buffer_load_dwordx4 v[220:223], v8, %[rs], %[m0] offen sc1\n\t" \
    "buffer_load_dwordx4 v[224:227], v8, %[rs], %[m1] offen sc1\n\t" \
    "s_branch PM_%=\n\t" \
    "PD_%=:\n\t" \
    GSA(0) \
    "v_smfmac_f32_16x16x64_bf16 v[0:3], v[220:223], v[236:243], v10\n\t" \
    "v_xor_b32 v9, 16384, v9\n\t" \
    "ds_read_b128 v[236:239], v9 offset:0\n\t" \
    "ds_read_b128 v[240:243], v9 offset:1024\n\t" \
    "v_smfmac_f32_16x16x64_bf16 v[4:7], v[220:223], v[244:251], v10\n\t" \
    "ds_read_b128 v[244:247], v9 offset:2048\n\t" \
    "ds_read_b128 v[248:251], v9 offset:3072\n\t" \
    "s_waitcnt lgkmcnt(4)\n\t" \
    "s_barrier\n\t" \
    DGA \
    "s_cmp_lg_u32 %[st], 63\n\t" \
    "s_cbranch_scc1 NL_%=\n\t" \
    "s_mov_b64 exec, %[fm]\n\t" \
    "v_mov_b32 v252, %[st]\n\t" \
    "v_mov_b32 v253, %[tag]\n\t" \
    "v_mov_b32 v254, 0\n\t" \
    "buffer_store_dwordx2 v[252:253], v254, %[rs], %[po] offen sc1\n\t" \
    "s_mov_b64 exec, -1\n\t" \
    "NL_%=:\n\t" \
    "ds_read_b128 v[228:231], v8 offset:0\n\t" \
    "ds_read_b128 v[232:235], v8 offset:4096\n\t" \
    "v_smfmac_f32_16x16x64_bf16 v[0:3], v[224:227], v[148:155], v10\n\t" \
    "v_xor_b32 v8, 1024, v8\n\t" \
    "ds_read_b128 v[220:223], v8 offset:8192\n\t" \
    "v_smfmac_f32_16x16x64_bf16 v[4:7], v[224:227], v[156:163], v10\n\t" \
    "ds_read_b128 v[224:227], v8 offset:12288\n\t" \
    "buffer_store_dwordx4 v[212:215], v14, %[rs], %[ox] offen offset:0 sc1\n\t" \
    "buffer_store_dwordx4 v[216:219], v14, %[rs], %[ox] offen offset:512 sc1\n\t" \
    "s_waitcnt lgkmcnt(3)\n\t" \
    "v_smfmac_f32_16x16x64_bf16 v[0:3], v[228:231], v[20:27], v10\n\t" \
    "v_smfmac_f32_16x16x64_bf16 v[4:7], v[228:231], v[28:35], v10\n\t" \
    "ds_read_b128 v[228:231], v8 offset:0\n\t" \
    "s_waitcnt lgkmcnt(3)\n\t" \
    "v_smfmac_f32_16x16x64_bf16 v[0:3], v[232:235], v[84:91], v10\n\t" \
    "v_smfmac_f32_16x16x64_bf16 v[4:7], v[232:235], v[92:99], v10\n\t" \
    "ds_read_b128 v[232:235], v8 offset:4096\n\t" \
    "s_waitcnt lgkmcnt(3)\n\t" \
    "v_smfmac_f32_16x16x64_bf16 v[0:3], v[220:223], v[236:243], v10\n\t" \
    "v_xor_b32 v9, 49152, v9\n\t" \
    "ds_read_b128 v[236:239], v9 offset:0\n\t" \
    "v_smfmac_f32_16x16x64_bf16 v[4:7], v[220:223], v[244:251], v10\n\t" \
    "ds_read_b128 v[240:243], v9 offset:1024\n\t" \
    "s_waitcnt lgkmcnt(4)\n\t" \
    "v_smfmac_f32_16x16x64_bf16 v[0:3], v[224:227], v[164:171], v10\n\t" \
    "v_xor_b32 v8, 3072, v8\n\t" \
    "ds_read_b128 v[220:223], v8 offset:12288\n\t" \
    "v_smfmac_f32_16x16x64_bf16 v[4:7], v[224:227], v[172:179], v10\n\t" \
    "ds_read_b128 v[244:247], v9 offset:2048\n\t" \
    "s_waitcnt lgkmcnt(5)\n\t" \
    "v_smfmac_f32_16x16x64_bf16 v[0:3], v[228:231], v[36:43], v10\n\t" \
    "ds_read_b128 v[224:227], v8 offset:0\n\t" \
    "v_smfmac_f32_16x16x64_bf16 v[4:7], v[228:231], v[44:51], v10\n\t" \
    "ds_read_b128 v[248:251], v9 offset:3072\n\t" \
    "s_waitcnt lgkmcnt(6)\n\t" \
    "v_smfmac_f32_16x16x64_bf16 v[0:3], v[232:235], v[100:107], v10\n\t" \
    "ds_read_b128 v[228:231], v8 offset:8192\n\t" \
    "v_smfmac_f32_16x16x64_bf16 v[4:7], v[232:235], v[108:115], v10\n\t" \
    "ds_read_b128 v[232:235], v8 offset:4096\n\t" \
    "s_waitcnt lgkmcnt(5)\n\t" \
    "v_smfmac_f32_16x16x64_bf16 v[0:3], v[220:223], v[180:187], v10\n\t" \
    "v_smfmac_f32_16x16x64_bf16 v[4:7], v[220:223], v[188:195], v10\n\t" \
    "v_xor_b32 v8, 1024, v8\n\t" \
    "ds_read_b128 v[220:223], v8 offset:12288\n\t" \
    "s_waitcnt lgkmcnt(4)\n\t" \
    "v_smfmac_f32_16x16x64_bf16 v[0:3], v[224:227], v[52:59], v10\n\t" \
    "v_smfmac_f32_16x16x64_bf16 v[4:7], v[224:227], v[60:67], v10\n\t" \
    "ds_read_b128 v[224:227], v8 offset:0\n\t" \
    "s_waitcnt lgkmcnt(3)\n\t" \
    "v_smfmac_f32_16x16x64_bf16 v[0:3], v[228:231], v[236:243], v10\n\t" \
    "v_xor_b32 v9, 16384, v9\n\t" \
    "ds_read_b128 v[236:239], v9 offset:0\n\t" \
    "v_smfmac_f32_16x16x64_bf16 v[4:7], v[228:231], v[244:251], v10\n\t" \
    "ds_read_b128 v[240:243], v9 offset:1024\n\t" \
    "s_waitcnt lgkmcnt(4)\n\t" \
    "v_smfmac_f32_16x16x64_bf16 v[0:3], v[232:235], v[116:123], v10\n\t" \
    "ds_read_b128 v[228:231], v8 offset:4096\n\t" \
    "v_smfmac_f32_16x16x64_bf16 v[4:7], v[232:235], v[124:131], v10\n\t" \
    "ds_read_b128 v[244:247], v9 offset:2048\n\t" \
    "s_waitcnt lgkmcnt(5)\n\t" \
    "v_smfmac_f32_16x16x64_bf16 v[0:3], v[220:223], v[196:203], v10\n\t" \
    "ds_read_b128 v[232:235], v8 offset:8192\n\t" \
    "v_xor_b32 v8, 3072, v8\n\t" \
    "v_smfmac_f32_16x16x64_bf16 v[4:7], v[220:223], v[204:211], v10\n\t" \
    "ds_read_b128 v[248:251], v9 offset:3072\n\t" \
    "s_waitcnt lgkmcnt(6)\n\t" \
    "v_smfmac_f32_16x16x64_bf16 v[0:3], v[224:227], v[68:75], v10\n\t" \
    "v_smfmac_f32_16x16x64_bf16 v[4:7], v[224:227], v[76:83], v10\n\t" \
    "s_waitcnt lgkmcnt(3)\n\t" \
    "v_smfmac_f32_16x16x64_bf16 v[0:3], v[228:231], v[132:139], v10\n\t" \
    "v_smfmac_f32_16x16x64_bf16 v[4:7], v[228:231], v[140:147], v10\n\t" \
    "s_waitcnt lgkmcnt(0)\n\t" \
    "v_smfmac_f32_16x16x64_bf16 v[0:3], v[232:235], v[236:243], v10\n\t" \
    "v_xor_b32 v9, 49152, v9\n\t" \
    "v_smfmac_f32_16x16x64_bf16 v[4:7], v[232:235], v[244:251], v10\n\t" \
    "v_mov_b32 v253, %[stag]\n\t" \
    "s_nop 4\n\t" \
    "v_add_f32 v0, v0, v1\n\t" \
    "v_add_f32 v2, v2, v3\n\t" \
    "v_add_f32 v4, v4, v5\n\t" \
    "v_add_f32 v6, v6, v7\n\t" \
    "v_cvt_pk_bf16_f32 v0, v0, v4\n\t" \
    "v_cvt_pk_bf16_f32 v2, v2, v6\n\t" \
    "ds_write2_b32 v11, v0, v2 offset0:0 offset1:4\n\t" \
    "s_mov_b64 exec, %[onej]\n\t" \
    "ds_write_b32 v18, v253 offset:16\n\t" \
    "s_mov_b64 exec, -1\n\t" \
    GSA(1) \
    "s_setprio 0\n\t" \
    "buffer_load_dwordx4 v[236:239], v12, %[rs], %[qx] offen offset:0 sc1\n\t" \
    "buffer_load_dwordx4 v[240:243], v12, %[rs], %[qx] offen offset:1024 sc1\n\t" \
    "buffer_load_dwordx4 v[244:247], v12, %[rs], %[qx] offen offset:2048 sc1\n\t" \
    "buffer_load_dwordx4 v[248:251], v12, %[rs], %[qx] offen offset:3072 sc1\n\t" \
    "v_cmp_le_i32 s[72:73], 0, v16\n\t" \
    "v_cmp_le_i32 s[74:75], 1, v16\n\t" \
    "v_cmp_le_i32 s[76:77], 2, v16\n\t" \
    "v_cmp_le_i32 s[78:79], 3, v16\n\t" \
    "v_cmp_le_i32 s[80:81], 16, v16\n\t" \
    "v_cmp_le_i32 s[82:83], 17, v16\n\t" \
    "v_cmp_le_i32 s[84:85], 18, v16\n\t" \
    "v_cmp_le_i32 s[86:87], 19, v16\n\t" \
    "v_mov_b32 v254, v254\n\t" \
    "s_nop 2\n\t" \
    "v_mov_b32 v254, v254\n\t" \
    "s_nop 2\n\t" \
    "v_mov_b32 v254, v254\n\t" \
    "s_nop 2\n\t" \
    "v_mov_b32 v254, v254\n\t" \
    "s_nop 2\n\t" \
    "v_mov_b32 v254, v254\n\t" \
    "s_nop 2\n\t" \
    "v_mov_b32 v254, v254\n\t" \
    "s_nop 2\n\t" \
    "v_mov_b32 v254, v254\n\t" \
    "s_nop 2\n\t" \
    "v_mov_b32 v254, v254\n\t" \
    "s_nop 2\n\t" \
    "v_mov_b32 v254, v254\n\t" \
    "s_nop 2\n\t" \
    "v_mov_b32 v254, v254\n\t" \
    "s_nop 2\n\t" \
    "v_mov_b32 v254, v254\n\t" \
    "s_nop 2\n\t" \
    "v_mov_b32 v254, v254\n\t" \
    "s_nop 2\n\t" \
    "v_mov_b32 v254, v254\n\t" \
    "s_nop 2\n\t" \
    "v_mov_b32 v254, v254\n\t" \
    "s_nop 2\n\t" \
    "v_mov_b32 v254, v254\n\t" \
    "s_nop 2\n\t" \
    "v_mov_b32 v254, v254\n\t" \
    "s_nop 2\n\t" \
    "v_mov_b32 v254, v254\n\t" \
    "s_nop 2\n\t" \
    "v_mov_b32 v254, v254\n\t" \
    "s_nop 2\n\t" \
    "v_mov_b32 v254, v254\n\t" \
    "s_nop 2\n\t" \
    "v_mov_b32 v254, v254\n\t" \
    "s_nop 2\n\t" \
    "LW_%=:\n\t" \
    "ds_read_b32 v0, v18 offset:16\n\t" \
    "s_sub_u32 %[lim], %[lim], " STR(DEC1) "\n\t" \
    "s_cmp_gt_i32 %[lim], 0\n\t" \
    "v_mov_b32 v254, v254\n\t" \
    "s_nop 2\n\t" \
    "v_mov_b32 v254, v254\n\t" \
    "s_nop 2\n\t" \
    "v_mov_b32 v254, v254\n\t" \
    "s_nop 2\n\t" \
    "v_mov_b32 v254, v254\n\t" \
    "s_nop 2\n\t" \
    "v_mov_b32 v254, v254\n\t" \
    "s_nop 2\n\t" \
    "s_waitcnt lgkmcnt(0)\n\t" \
    "s_cbranch_scc0 CW_%=\n\t" \
    "v_cmp_ne_u32 vcc, v0, v253\n\t" \
    "s_cbranch_vccnz LW_%=\n\t" \
    "CW_%=:\n\t" \
    "ds_read_b128 v[212:215], v12 offset:24576\n\t" \
    "ds_read_b128 v[216:219], v12 offset:28672\n\t" \
    "ds_read_b128 v[220:223], v15 offset:0\n\t" \
    "ds_read_b128 v[224:227], v12 offset:25600\n\t" \
    "ds_read_b128 v[228:231], v12 offset:29696\n\t" \
    "v_add_u32 v253, %[se], v12\n\t" \
    "v_mov_b32 v254, v254\n\t" \
    "s_nop 2\n\t" \
    "v_mov_b32 v254, v254\n\t" \
    "s_nop 2\n\t" \
    "v_mov_b32 v254, v254\n\t" \
    "s_nop 2\n\t" \
    "v_mov_b32 v254, v254\n\t" \
    "s_nop 2\n\t" \
    "s_waitcnt vmcnt(0)\n\t" \
    "s_waitcnt lgkmcnt(4)\n\t" \
    "v_mfma_f32_16x16x32_bf16 v[0:3], v[212:215], v[236:239], 0\n\t" \
    "ds_read_b128 v[212:215], v15 offset:512\n\t" \
    "s_waitcnt lgkmcnt(4)\n\t" \
    "v_mfma_f32_16x16x32_bf16 v[4:7], v[216:219], v[236:239], 0\n\t" \
    "ds_read_b128 v[216:219], v12 offset:26624\n\t" \
    "s_waitcnt lgkmcnt(4)\n\t" \
    "v_mfma_f32_16x16x32_bf16 v[232:235], v[220:223], v[236:239], 0\n\t" \
    "ds_read_b128 v[220:223], v12 offset:30720\n\t" \
    "s_waitcnt lgkmcnt(4)\n\t" \
    "v_mfma_f32_16x16x32_bf16 v[0:3], v[224:227], v[240:243], v[0:3]\n\t" \
    "ds_read_b128 v[224:227], v15 offset:1024\n\t" \
    "s_waitcnt lgkmcnt(4)\n\t" \
    "v_mfma_f32_16x16x32_bf16 v[4:7], v[228:231], v[240:243], v[4:7]\n\t" \
    "ds_read_b128 v[228:231], v12 offset:27648\n\t" \
    "s_waitcnt lgkmcnt(4)\n\t" \
    "v_mfma_f32_16x16x32_bf16 v[232:235], v[212:215], v[240:243], v[232:235]\n\t" \
    "ds_read_b128 v[212:215], v12 offset:31744\n\t" \
    "s_waitcnt lgkmcnt(4)\n\t" \
    "v_mfma_f32_16x16x32_bf16 v[0:3], v[216:219], v[244:247], v[0:3]\n\t" \
    "ds_read_b128 v[216:219], v15 offset:1536\n\t" \
    "s_waitcnt lgkmcnt(4)\n\t" \
    "v_mfma_f32_16x16x32_bf16 v[4:7], v[220:223], v[244:247], v[4:7]\n\t" \
    "s_waitcnt lgkmcnt(3)\n\t" \
    "v_mfma_f32_16x16x32_bf16 v[232:235], v[224:227], v[244:247], v[232:235]\n\t" \
    "s_waitcnt lgkmcnt(2)\n\t" \
    "v_mfma_f32_16x16x32_bf16 v[0:3], v[228:231], v[248:251], v[0:3]\n\t" \
    "s_waitcnt lgkmcnt(1)\n\t" \
    "v_mfma_f32_16x16x32_bf16 v[4:7], v[212:215], v[248:251], v[4:7]\n\t" \
    "s_waitcnt lgkmcnt(0)\n\t" \
    "v_mfma_f32_16x16x32_bf16 v[232:235], v[216:219], v[248:251], v[232:235]\n\t" \
    "s_barrier\n\t" \
    GSA(4) \
    "ds_read_b128 v[236:239], v253 offset:8192\n\t" \
    "ds_read_b128 v[240:243], v253 offset:9216\n\t" \
    "ds_read_b128 v[244:247], v253 offset:10240\n\t" \
    "ds_read_b128 v[248:251], v253 offset:11264\n\t" \
    "ds_read2st64_b32 v[228:229], v17 offset0:0 offset1:1\n\t" \
    "ds_read2st64_b32 v[230:231], v17 offset0:2 offset1:3\n\t" \
    "s_mov_b64 exec, %[fm]\n\t" \
    "v_mov_b32 v220, %[st]\n\t" \
    "v_mov_b32 v221, %[tag]\n\t" \
    "v_mov_b32 v222, 0\n\t" \
    "buffer_store_dwordx2 v[220:221], v222, %[rs], %[po] offen sc1\n\t" \
    "s_mov_b64 exec, -1\n\t" \
    "v_mov_b32 v254, 0xff800000\n\t" \
    "v_cvt_pk_bf16_f32 v0, 0, v0\n\t" \
    "v_cvt_pk_bf16_f32 v1, 0, v1\n\t" \
    "v_cvt_pk_bf16_f32 v2, 0, v2\n\t" \
    "v_cvt_pk_bf16_f32 v3, 0, v3\n\t" \
    "v_cvt_pk_bf16_f32 v4, 0, v4\n\t" \
    "v_cvt_pk_bf16_f32 v5, 0, v5\n\t" \
    "v_cvt_pk_bf16_f32 v6, 0, v6\n\t" \
    "v_cvt_pk_bf16_f32 v7, 0, v7\n\t" \
    "v_cvt_pk_bf16_f32 v232, 0, v232\n\t" \
    "v_cvt_pk_bf16_f32 v233, 0, v233\n\t" \
    "v_mul_f32 v0, 0x3db504f3, v0\n\t" \
    "v_mul_f32 v1, 0x3db504f3, v1\n\t" \
    "v_mul_f32 v2, 0x3db504f3, v2\n\t" \
    "v_mul_f32 v3, 0x3db504f3, v3\n\t" \
    "v_mul_f32 v4, 0x3db504f3, v4\n\t" \
    "v_mul_f32 v5, 0x3db504f3, v5\n\t" \
    "v_mul_f32 v6, 0x3db504f3, v6\n\t" \
    "v_mul_f32 v7, 0x3db504f3, v7\n\t" \
    "v_mul_f32 v232, 0x3db504f3, v232\n\t" \
    "v_mul_f32 v233, 0x3db504f3, v233\n\t" \
    "v_cvt_pk_bf16_f32 v0, 0, v0\n\t" \
    "v_cvt_pk_bf16_f32 v1, 0, v1\n\t" \
    "v_cvt_pk_bf16_f32 v2, 0, v2\n\t" \
    "v_cvt_pk_bf16_f32 v3, 0, v3\n\t" \
    "v_cvt_pk_bf16_f32 v4, 0, v4\n\t" \
    "v_cvt_pk_bf16_f32 v5, 0, v5\n\t" \
    "v_cvt_pk_bf16_f32 v6, 0, v6\n\t" \
    "v_cvt_pk_bf16_f32 v7, 0, v7\n\t" \
    "v_cvt_pk_bf16_f32 v232, 0, v232\n\t" \
    "v_cvt_pk_bf16_f32 v233, 0, v233\n\t" \
    "v_cndmask_b32 v0, v254, v0, s[72:73]\n\t" \
    "v_cndmask_b32 v1, v254, v1, s[74:75]\n\t" \
    "v_cndmask_b32 v2, v254, v2, s[76:77]\n\t" \
    "v_cndmask_b32 v3, v254, v3, s[78:79]\n\t" \
    "v_cndmask_b32 v4, v254, v4, s[80:81]\n\t" \
    "v_cndmask_b32 v5, v254, v5, s[82:83]\n\t" \
    "v_cndmask_b32 v6, v254, v6, s[84:85]\n\t" \
    "v_cndmask_b32 v7, v254, v7, s[86:87]\n\t" \
    "v_max3_f32 v252, v0, v1, v2\n\t" \
    "v_max3_f32 v255, v3, v4, v5\n\t" \
    "v_max3_f32 v254, v6, v7, v232\n\t" \
    "v_max3_f32 v252, v252, v255, v254\n\t" \
    "v_max_f32 v252, v252, v233\n\t" \
    "v_mov_b32 v255, v252\n\t" \
    "v_mov_b32 v254, v252\n\t" \
    "s_nop 1\n\t" \
    "v_permlane16_swap_b32 v255, v254\n\t" \
    "v_max_f32 v252, v255, v254\n\t" \
    "v_max_f32 v255, v255, v254\n\t" \
    "s_nop 1\n\t" \
    "v_permlane32_swap_b32 v252, v255\n\t" \
    "v_max_f32 v252, v252, v255\n\t" \
    "v_mul_f32 v252, 0xbfb8aa3b, v252\n\t" \
    "v_fma_f32 v0, v0, %[l2e], v252\n\t" \
    "v_fma_f32 v1, v1, %[l2e], v252\n\t" \
    "v_fma_f32 v2, v2, %[l2e], v252\n\t" \
    "v_fma_f32 v3, v3, %[l2e], v252\n\t" \
    "v_fma_f32 v4, v4, %[l2e], v252\n\t" \
    "v_fma_f32 v5, v5, %[l2e], v252\n\t" \
    "v_fma_f32 v6, v6, %[l2e], v252\n\t" \
    "v_fma_f32 v7, v7, %[l2e], v252\n\t" \
    "v_fma_f32 v232, v232, %[l2e], v252\n\t" \
    "v_fma_f32 v233, v233, %[l2e], v252\n\t" \
    "v_exp_f32 v0, v0\n\t" \
    "v_exp_f32 v1, v1\n\t" \
    "v_exp_f32 v2, v2\n\t" \
    "v_exp_f32 v3, v3\n\t" \
    "v_exp_f32 v4, v4\n\t" \
    "v_exp_f32 v5, v5\n\t" \
    "v_exp_f32 v6, v6\n\t" \
    "v_exp_f32 v7, v7\n\t" \
    "v_exp_f32 v232, v232\n\t" \
    "v_exp_f32 v233, v233\n\t" \
    "v_add_f32 v252, v0, v1\n\t" \
    "v_add_f32 v235, v2, v3\n\t" \
    "v_add_f32 v254, v4, v5\n\t" \
    "v_add_f32 v255, v6, v7\n\t" \
    "v_add_f32 v234, v232, v233\n\t" \
    "v_add_f32 v252, v252, v235\n\t" \
    "v_add_f32 v254, v254, v255\n\t" \
    "v_add_f32 v252, v252, v254\n\t" \
    "v_add_f32 v252, v252, v234\n\t" \
    "v_mov_b32 v255, v252\n\t" \
    "v_mov_b32 v254, v252\n\t" \
    "s_nop 1\n\t" \
    "v_permlane16_swap_b32 v255, v254\n\t" \
    "v_add_f32 v252, v255, v254\n\t" \
    "v_add_f32 v255, v255, v254\n\t" \
    "s_nop 1\n\t" \
    "v_permlane32_swap_b32 v252, v255\n\t" \
    "v_add_f32 v252, v252, v255\n\t" \
    "v_rcp_f32 v252, v252\n\t" \
    "s_nop 0\n\t" \
    "v_mul_f32 v0, v0, v252\n\t" \
    "v_mul_f32 v1, v1, v252\n\t" \
    "v_mul_f32 v2, v2, v252\n\t" \
    "v_mul_f32 v3, v3, v252\n\t" \
    "v_mul_f32 v4, v4, v252\n\t" \
    "v_mul_f32 v5, v5, v252\n\t" \
    "v_mul_f32 v6, v6, v252\n\t" \
    "v_mul_f32 v7, v7, v252\n\t" \
    "v_mul_f32 v232, v232, v252\n\t" \
    "v_mul_f32 v233, v233, v252\n\t" \
    "v_cvt_pk_bf16_f32 v0, v0, v1\n\t" \
    "v_cvt_pk_bf16_f32 v1, v2, v3\n\t" \
    "v_cvt_pk_bf16_f32 v2, v4, v5\n\t" \
    "v_cvt_pk_bf16_f32 v3, v6, v7\n\t" \
    "v_cvt_pk_bf16_f32 v4, v232, v233\n\t" \
    "v_mov_b32 v5, 0\n\t" \
    "v_mov_b32 v6, 0\n\t" \
    "v_cvt_pk_bf16_f32 v7, v232, v233\n\t" \
    "s_waitcnt lgkmcnt(0)\n\t" \
    "v_mfma_f32_16x16x32_bf16 v[236:239], v[236:239], v[0:3], 0\n\t" \
    "v_mfma_f32_16x16x32_bf16 v[240:243], v[240:243], v[0:3], 0\n\t" \
    "v_mfma_f32_16x16x32_bf16 v[244:247], v[244:247], v[0:3], 0\n\t" \
    "v_mfma_f32_16x16x32_bf16 v[248:251], v[248:251], v[0:3], 0\n\t" \
    "v_mfma_f32_16x16x16_bf16 v[236:239], v[228:229], v[4:5], v[236:239]\n\t" \
    "v_mfma_f32_16x16x16_bf16 v[240:243], v[228:229], v[6:7], v[240:243]\n\t" \
    "v_mfma_f32_16x16x16_bf16 v[244:247], v[230:231], v[4:5], v[244:247]\n\t" \
    "v_mfma_f32_16x16x16_bf16 v[248:251], v[230:231], v[6:7], v[248:251]\n\t" \
    "s_nop 4\n\t" \
    "v_cvt_pk_bf16_f32 v212, v236, v237\n\t" \
    "v_cvt_pk_bf16_f32 v213, v240, v241\n\t" \
    "v_cvt_pk_bf16_f32 v214, v238, v239\n\t" \
    "v_cvt_pk_bf16_f32 v215, v242, v243\n\t" \
    "v_permlane16_swap_b32 v212, v213\n\t" \
    "s_nop 0\n\t" \
    "v_permlane16_swap_b32 v214, v215\n\t" \
    "v_cvt_pk_bf16_f32 v216, v244, v245\n\t" \
    "v_cvt_pk_bf16_f32 v217, v248, v249\n\t" \
    "v_cvt_pk_bf16_f32 v218, v246, v247\n\t" \
    "v_cvt_pk_bf16_f32 v219, v250, v251\n\t" \
    "v_permlane16_swap_b32 v216, v217\n\t" \
    "s_nop 0\n\t" \
    "v_permlane16_swap_b32 v218, v219\n\t" \
    "s_setprio 1\n\t" \
    GSA(2) \
    GSW \
    "s_nop 1\n\t" \
    : [lim] "+s"(lim), "+{v[212:219]}"(oxr) DGO GSO(0) GSO(1) GSO(2) GSO(3) GSO(4) \
    : "{v8}"(am_a), "{v9}"(t0_a), "{v10}"(idx), "{v11}"(iw_a), "{v12}"(ab_a), "{v14}"(xo_v), "{v15}"(kr_a), "{v16}"(dq), "{v17}"(vl_a), "{v18}"(fr_a), [stag] "s"(0xffffff81u + s), [onej] "s"(onej), [rs] "s"(rs), [m0] "s"(sbm + 16384u), [m1] "s"(sbm + 20480u), [st] "s"(s), [tag] "s"(P.tag), [po] "s"(FLG_OFF + F_PRG(4, h) * 128), [fm] "s"(fmask), [qx] "s"(qxo), [qd] "s"(lbase + LA_VM), [qh] "s"((j & 1) * 2048), [ox] "s"(oxo), [se] "s"((j & 1) * 4096), [l2e] "s"(0x3fb8aa3b), "{v[20:27]}"(wr[0][0]), "{v[28:35]}"(wr[0][1]), "{v[36:43]}"(wr[1][0]), "{v[44:51]}"(wr[1][1]), "{v[52:59]}"(wr[2][0]), "{v[60:67]}"(wr[2][1]), "{v[68:75]}"(wr[3][0]), "{v[76:83]}"(wr[3][1]), "{v[84:91]}"(wr[4][0]), "{v[92:99]}"(wr[4][1]), "{v[100:107]}"(wr[5][0]), "{v[108:115]}"(wr[5][1]), "{v[116:123]}"(wr[6][0]), "{v[124:131]}"(wr[6][1]), "{v[132:139]}"(wr[7][0]), "{v[140:147]}"(wr[7][1]), "{v[148:155]}"(wr[8][0]), "{v[156:163]}"(wr[8][1]), "{v[164:171]}"(wr[9][0]), "{v[172:179]}"(wr[9][1]), "{v[180:187]}"(wr[10][0]), "{v[188:195]}"(wr[10][1]), "{v[196:203]}"(wr[11][0]), "{v[204:211]}"(wr[11][1]) \
    : "memory", "scc", "vcc", "s90", "s91", "s72", "s73", "s74", "s75", "s76", "s77", "s78", "s79", "s80", "s81", "s82", "s83", "s84", "s85", "s86", "s87", "v0", "v1", "v2", "v3", "v4", "v5", "v6", "v7", "v220", "v221", "v222", "v223", "v224", "v225", "v226", "v227", "v228", "v229", "v230", "v231", "v232", "v233", "v234", "v235", "v236", "v237", "v238", "v239", "v240", "v241", "v242", "v243", "v244", "v245", "v246", "v247", "v248", "v249", "v250", "v251", "v252", "v253", "v254", "v255")

#define B_POLL() \
  asm volatile( \
    "TWB_%=:\n\t" \
    "s_memrealtime s[88:89]\n\t" \
    "s_memtime s[90:91]\n\t" \
    "s_waitcnt lgkmcnt(0)\n\t" \
    "s_lshl_b32 s88, s88, 2\n\t" \
    "s_sub_u32 s89, %[tgt], s88\n\t" \
    "s_cmp_gt_i32 s89, 16\n\t" \
    "s_cbranch_scc1 TWB_%=\n\t" \
    "s_mul_i32 s89, s89, 11\n\t" \
    "s_ashr_i32 s89, s89, 1\n\t" \
    "s_add_u32 s89, s89, s90\n\t" \
    "TCB_%=:\n\t" \
    "s_memtime s[92:93]\n\t" \
    "s_waitcnt lgkmcnt(0)\n\t" \
    "s_sub_u32 s93, s89, s92\n\t" \
    "s_cmp_gt_i32 s93, 0\n\t" \
    "s_cbranch_scc1 TCB_%=\n\t" \
    "buffer_load_dwordx4 v[220:223], %[l16], %[rs], %[m0] offen offset:0 sc1\n\t" \
    "buffer_load_dwordx4 v[224:227], %[l16], %[rs], %[m0] offen offset:1024 sc1\n\t" \
    "buffer_load_dwordx4 v[228:231], %[l16], %[rs], %[m1] offen offset:0 sc1\n\t" \
    "buffer_load_dwordx4 v[232:235], %[l16], %[rs], %[m1] offen offset:1024 sc1\n\t" \
    "v_mov_b32 v252, v252\n\t" \
    "s_nop 2\n\t" \
    "v_mov_b32 v252, v252\n\t" \
    "s_nop 2\n\t" \
    "v_mov_b32 v252, v252\n\t" \
    "s_nop 2\n\t" \
    "v_mov_b32 v252, v252\n\t" \
    "s_nop 2\n\t" \
    "v_mov_b32 v252, v252\n\t" \
    "s_nop 2\n\t" \
    "v_mov_b32 v252, v252\n\t" \
    "s_nop 2\n\t" \
    "v_mov_b32 v252, v252\n\t" \
    "s_nop 2\n\t" \
    "v_mov_b32 v252, v252\n\t" \
    "s_nop 2\n\t" \
    "v_mov_b32 v252, v252\n\t" \
    "s_nop 2\n\t" \
    "v_mov_b32 v252, v252\n\t" \
    "s_nop 2\n\t" \
    "v_mov_b32 v252, v252\n\t" \
    "s_nop 2\n\t" \
    "v_mov_b32 v252, v252\n\t" \
    "s_nop 2\n\t" \
    "v_mov_b32 v252, v252\n\t" \
    "s_nop 2\n\t" \
    "v_mov_b32 v252, v252\n\t" \
    "s_nop 2\n\t" \
    "v_mov_b32 v252, v252\n\t" \
    "s_nop 2\n\t" \
    "v_mov_b32 v252, v252\n\t" \
    "s_nop 2\n\t" \
    "v_mov_b32 v252, v252\n\t" \
    "s_nop 2\n\t" \
    "v_mov_b32 v252, v252\n\t" \
    "s_nop 2\n\t" \
    "v_mov_b32 v252, v252\n\t" \
    "s_nop 2\n\t" \
    "v_mov_b32 v252, v252\n\t" \
    "s_nop 2\n\t" \
    "v_mov_b32 v252, v252\n\t" \
    "s_nop 2\n\t" \
    "v_mov_b32 v252, v252\n\t" \
    "s_nop 2\n\t" \
    "v_mov_b32 v252, v252\n\t" \
    "s_nop 2\n\t" \
    "v_mov_b32 v252, v252\n\t" \
    "s_nop 2\n\t" \
    "PM_%=:\n\t" \
    "s_waitcnt vmcnt(3)\n\t" \
    "ds_write_b128 %[la], v[220:223] offset:0\n\t" \
    "v_max3_u32 v252, v220, v220, v221\n\t" \
    "v_max3_u32 v252, v252, v222, v223\n\t" \
    "s_waitcnt vmcnt(2)\n\t" \
    "ds_write_b128 %[la], v[224:227] offset:1024\n\t" \
    "v_max3_u32 v252, v252, v224, v225\n\t" \
    "v_max3_u32 v252, v252, v226, v227\n\t" \
    "s_waitcnt vmcnt(1)\n\t" \
    "ds_write_b128 %[la], v[228:231] offset:8192\n\t" \
    "v_max3_u32 v252, v252, v228, v229\n\t" \
    "v_max3_u32 v252, v252, v230, v231\n\t" \
    "s_waitcnt vmcnt(0)\n\t" \
    "ds_write_b128 %[la], v[232:235] offset:9216\n\t" \
    "v_max3_u32 v252, v252, v232, v233\n\t" \
    "v_max3_u32 v252, v252, v234, v235\n\t" \
    "v_cmp_eq_u32 vcc, -1, v252\n\t" \
    "s_cbranch_vccz PD_%=\n\t" \
    "s_sub_u32 %[lim], %[lim], 1\n\t" \
    "s_cmp_gt_i32 %[lim], 0\n\t" \
    "s_cbranch_scc0 PD_%=\n\t" \
    "s_sleep " STR(CADM) "\n\t" \
    "buffer_load_dwordx4 v[220:223], %[l16], %[rs], %[m0] offen offset:0 sc1\n\t" \
    "buffer_load_dwordx4 v[224:227], %[l16], %[rs], %[m0] offen offset:1024 sc1\n\t" \
    "buffer_load_dwordx4 v[228:231], %[l16], %[rs], %[m1] offen offset:0 sc1\n\t" \
    "buffer_load_dwordx4 v[232:235], %[l16], %[rs], %[m1] offen offset:1024 sc1\n\t" \
    "s_branch PM_%=\n\t" \
    "PD_%=:\n\t" \
    GSA(3) \
    "s_waitcnt lgkmcnt(0)\n\t" \
    "s_barrier\n\t" \
    : [lim] "+s"(lim) GSO(3) \
    : [l16] "v"(l16), [la] "v"(la), [rs] "s"(rs), [m0] "s"(mo), [m1] "s"(mo + 16384u), [tgt] "s"(tgt) \
    : "memory", "vcc", "scc", "v252", "s88", "s89", "s90", "s91", "s92", "s93", "v220", "v221", "v222", "v223", "v224", "v225", "v226", "v227", "v228", "v229", "v230", "v231", "v232", "v233", "v234", "v235")

#define T_WAIT() \
  asm volatile( \
    "TWC_%=:\n\t" \
    "s_memrealtime s[88:89]\n\t" \
    "s_memtime s[90:91]\n\t" \
    "s_waitcnt lgkmcnt(0)\n\t" \
    "s_lshl_b32 s88, s88, 2\n\t" \
    "s_sub_u32 s89, %[tgt], s88\n\t" \
    "s_cmp_gt_i32 s89, 16\n\t" \
    "s_cbranch_scc1 TWC_%=\n\t" \
    "s_mul_i32 s89, s89, 11\n\t" \
    "s_ashr_i32 s89, s89, 1\n\t" \
    "s_add_u32 s89, s89, s90\n\t" \
    "TCC_%=:\n\t" \
    "s_memtime s[92:93]\n\t" \
    "s_waitcnt lgkmcnt(0)\n\t" \
    "s_sub_u32 s93, s89, s92\n\t" \
    "s_cmp_gt_i32 s93, 0\n\t" \
    "s_cbranch_scc1 TCC_%=\n\t" \
    :  \
    : [tgt] "s"(tgt) \
    : "s88", "s89", "s90", "s91", "s92", "s93", "memory", "scc")

#define V_PROJ() \
  asm volatile( \
    "v_mov_b32 v0, 0\n\t" \
    "v_mov_b32 v1, 0\n\t" \
    "v_mov_b32 v2, 0\n\t" \
    "v_mov_b32 v3, 0\n\t" \
    "v_mov_b32 v4, 0\n\t" \
    "v_mov_b32 v5, 0\n\t" \
    "v_mov_b32 v6, 0\n\t" \
    "v_mov_b32 v7, 0\n\t" \
    "s_barrier\n\t" \
    "ds_read_b128 v[220:223], %[am] offset:0\n\t" \
    "ds_read_b128 v[224:227], %[am] offset:1024\n\t" \
    "ds_read_b128 v[228:231], %[am] offset:2048\n\t" \
    "ds_read_b128 v[232:235], %[am] offset:3072\n\t" \
    "ds_read_b128 v[236:239], %[am] offset:4096\n\t" \
    "ds_read_b128 v[240:243], %[am] offset:5120\n\t" \
    "ds_read_b128 v[244:247], %[am] offset:6144\n\t" \
    "ds_read_b128 v[248:251], %[am] offset:7168\n\t" \
    "s_waitcnt lgkmcnt(7)\n\t" \
    "v_smfmac_f32_16x16x64_bf16 v[0:3], v[220:223], v[28:35], %[idx]\n\t" \
    "ds_read_b128 v[220:223], %[am] offset:8192\n\t" \
    "s_waitcnt lgkmcnt(7)\n\t" \
    "v_smfmac_f32_16x16x64_bf16 v[4:7], v[224:227], v[36:43], %[idx]\n\t" \
    "ds_read_b128 v[224:227], %[am] offset:9216\n\t" \
    "s_waitcnt lgkmcnt(7)\n\t" \
    "v_smfmac_f32_16x16x64_bf16 v[0:3], v[228:231], v[44:51], %[idx]\n\t" \
    "ds_read_b128 v[228:231], %[am] offset:10240\n\t" \
    "s_waitcnt lgkmcnt(7)\n\t" \
    "v_smfmac_f32_16x16x64_bf16 v[4:7], v[232:235], v[52:59], %[idx]\n\t" \
    "ds_read_b128 v[232:235], %[am] offset:11264\n\t" \
    "s_waitcnt lgkmcnt(7)\n\t" \
    "v_smfmac_f32_16x16x64_bf16 v[0:3], v[236:239], v[60:67], %[idx]\n\t" \
    "ds_read_b128 v[236:239], %[am] offset:12288\n\t" \
    "s_waitcnt lgkmcnt(7)\n\t" \
    "v_smfmac_f32_16x16x64_bf16 v[4:7], v[240:243], v[68:75], %[idx]\n\t" \
    "ds_read_b128 v[240:243], %[am] offset:13312\n\t" \
    "s_waitcnt lgkmcnt(7)\n\t" \
    "v_smfmac_f32_16x16x64_bf16 v[0:3], v[244:247], v[76:83], %[idx]\n\t" \
    "ds_read_b128 v[244:247], %[am] offset:14336\n\t" \
    "s_waitcnt lgkmcnt(7)\n\t" \
    "v_smfmac_f32_16x16x64_bf16 v[4:7], v[248:251], v[84:91], %[idx]\n\t" \
    "ds_read_b128 v[248:251], %[am] offset:15360\n\t" \
    "s_waitcnt lgkmcnt(7)\n\t" \
    "v_smfmac_f32_16x16x64_bf16 v[0:3], v[220:223], v[92:99], %[idx]\n\t" \
    "s_waitcnt lgkmcnt(6)\n\t" \
    "v_smfmac_f32_16x16x64_bf16 v[4:7], v[224:227], v[100:107], %[idx]\n\t" \
    "s_waitcnt lgkmcnt(5)\n\t" \
    "v_smfmac_f32_16x16x64_bf16 v[0:3], v[228:231], v[108:115], %[idx]\n\t" \
    "s_waitcnt lgkmcnt(4)\n\t" \
    "v_smfmac_f32_16x16x64_bf16 v[4:7], v[232:235], v[116:123], %[idx]\n\t" \
    "s_waitcnt lgkmcnt(3)\n\t" \
    "v_smfmac_f32_16x16x64_bf16 v[0:3], v[236:239], v[124:131], %[idx]\n\t" \
    "s_waitcnt lgkmcnt(2)\n\t" \
    "v_smfmac_f32_16x16x64_bf16 v[4:7], v[240:243], v[132:139], %[idx]\n\t" \
    "s_waitcnt lgkmcnt(1)\n\t" \
    "v_smfmac_f32_16x16x64_bf16 v[0:3], v[244:247], v[140:147], %[idx]\n\t" \
    "s_waitcnt lgkmcnt(0)\n\t" \
    "v_smfmac_f32_16x16x64_bf16 v[4:7], v[248:251], v[148:155], %[idx]\n\t" \
    "s_nop 5\n\t" \
    "v_add_f32 v0, v0, v1\n\t" \
    "v_add_f32 v2, v2, v3\n\t" \
    "v_add_f32 v4, v4, v5\n\t" \
    "v_add_f32 v6, v6, v7\n\t" \
    "v_add_f32 v0, v0, v4\n\t" \
    "v_add_f32 v2, v2, v6\n\t" \
    "v_cvt_pk_bf16_f32 v0, v0, v2\n\t" \
    "v_min_u32 v0, -2, v0\n\t" \
    "s_nop 1\n\t" \
    "v_mov_b32_dpp v1, v0 quad_perm:[1,1,1,1] row_mask:0xf bank_mask:0xf\n\t" \
    "v_mov_b32_dpp v2, v0 quad_perm:[2,2,2,2] row_mask:0xf bank_mask:0xf\n\t" \
    "v_mov_b32_dpp v3, v0 quad_perm:[3,3,3,3] row_mask:0xf bank_mask:0xf\n\t" \
    "s_mov_b64 exec, %[qm]\n\t" \
    "buffer_store_dwordx4 v[0:3], %[l4], %[rs], %[vo] offen sc1\n\t" \
    "s_mov_b64 exec, -1\n\t" \
    "s_nop 1\n\t" \
    :  \
    : [am] "v"(am_a), [idx] "v"(idx), [l4] "v"(ln * 4), [rs] "s"(rs), [vo] "s"(sb + vo), [qm] "s"(0x1111111111111111ull), "{v[28:35]}"(wv[0]), "{v[36:43]}"(wv[1]), "{v[44:51]}"(wv[2]), "{v[52:59]}"(wv[3]), "{v[60:67]}"(wv[4]), "{v[68:75]}"(wv[5]), "{v[76:83]}"(wv[6]), "{v[84:91]}"(wv[7]), "{v[92:99]}"(wv[8]), "{v[100:107]}"(wv[9]), "{v[108:115]}"(wv[10]), "{v[116:123]}"(wv[11]), "{v[124:131]}"(wv[12]), "{v[132:139]}"(wv[13]), "{v[140:147]}"(wv[14]), "{v[148:155]}"(wv[15]) \
    : "memory", "v0", "v1", "v2", "v3", "v4", "v5", "v6", "v7", "v220", "v221", "v222", "v223", "v224", "v225", "v226", "v227", "v228", "v229", "v230", "v231", "v232", "v233", "v234", "v235", "v236", "v237", "v238", "v239", "v240", "v241", "v242", "v243", "v244", "v245", "v246", "v247", "v248", "v249", "v250", "v251")

#define WO_V() \
  asm volatile( \
    "v_mov_b32 v0, 0\n\t" \
    "v_mov_b32 v1, 0\n\t" \
    "v_mov_b32 v2, 0\n\t" \
    "v_mov_b32 v3, 0\n\t" \
    "v_mov_b32 v4, 0\n\t" \
    "v_mov_b32 v5, 0\n\t" \
    "v_mov_b32 v6, 0\n\t" \
    "v_mov_b32 v7, 0\n\t" \
    "v_mov_b32 v8, 0\n\t" \
    "v_mov_b32 v9, 0\n\t" \
    "v_mov_b32 v10, 0\n\t" \
    "v_mov_b32 v11, 0\n\t" \
    "v_mov_b32 v12, 0\n\t" \
    "v_mov_b32 v13, 0\n\t" \
    "v_mov_b32 v14, 0\n\t" \
    "v_mov_b32 v15, 0\n\t" \
    "v_mov_b32 v252, 0\n\t" \
    "s_barrier\n\t" \
    GSA(4) \
    DGC \
    "buffer_load_dwordx4 v[220:223], %[l16], %[rs], %[s0] offen sc1\n\t" \
    "buffer_load_dwordx4 v[224:227], %[l16], %[rs], %[s1] offen sc1\n\t" \
    "A_%=:\n\t" \
    "s_waitcnt vmcnt(1)\n\t" \
    "v_smfmac_f32_16x16x64_bf16 v[0:3], v[220:223], v[156:163], %[idx]\n\t" \
    "v_max3_u32 v252, v252, v220, v221\n\t" \
    "v_smfmac_f32_16x16x64_bf16 v[4:7], v[220:223], v[164:171], %[idx]\n\t" \
    "v_max3_u32 v252, v252, v222, v223\n\t" \
    "v_smfmac_f32_16x16x64_bf16 v[8:11], v[220:223], v[172:179], %[idx]\n\t" \
    "v_smfmac_f32_16x16x64_bf16 v[12:15], v[220:223], v[180:187], %[idx]\n\t" \
    "s_waitcnt vmcnt(0)\n\t" \
    "v_smfmac_f32_16x16x64_bf16 v[0:3], v[224:227], v[188:195], %[idx]\n\t" \
    "v_max3_u32 v252, v252, v224, v225\n\t" \
    "v_smfmac_f32_16x16x64_bf16 v[4:7], v[224:227], v[196:203], %[idx]\n\t" \
    "v_max3_u32 v252, v252, v226, v227\n\t" \
    "v_smfmac_f32_16x16x64_bf16 v[8:11], v[224:227], v[204:211], %[idx]\n\t" \
    "v_smfmac_f32_16x16x64_bf16 v[12:15], v[224:227], v[212:219], %[idx]\n\t" \
    "v_cmp_eq_u32 vcc, -1, v252\n\t" \
    "s_cbranch_vccnz SL_%=\n\t" \
    "D_%=:\n\t" \
    GSA(1) \
    "s_nop 2\n\t" \
    "v_add_f32 v0, v0, v1\n\t" \
    "v_add_f32 v2, v2, v3\n\t" \
    "v_add_f32 v4, v4, v5\n\t" \
    "v_add_f32 v6, v6, v7\n\t" \
    "v_add_f32 v8, v8, v9\n\t" \
    "v_add_f32 v10, v10, v11\n\t" \
    "v_add_f32 v12, v12, v13\n\t" \
    "v_add_f32 v14, v14, v15\n\t" \
    "v_permlane32_swap_b32 v0, v4\n\t" \
    "v_permlane32_swap_b32 v2, v6\n\t" \
    "v_permlane32_swap_b32 v8, v12\n\t" \
    "v_permlane32_swap_b32 v10, v14\n\t" \
    "v_add_f32 v0, v0, v12\n\t" \
    "v_add_f32 v2, v2, v14\n\t" \
    "ds_write2_b32 %[rw], v0, v2 offset0:0 offset1:32\n\t" \
    "s_waitcnt lgkmcnt(0)\n\t" \
    "s_barrier\n\t" \
    "s_cmp_eq_u32 %[red], 0\n\t" \
    "s_cbranch_scc0 E_%=\n\t" \
    "ds_read2st64_b64 v[0:3], %[rr] offset0:0 offset1:1\n\t" \
    "ds_read2st64_b64 v[4:7], %[rr] offset0:2 offset1:3\n\t" \
    "ds_read2st64_b64 v[8:11], %[rr] offset0:4 offset1:5\n\t" \
    "ds_read2st64_b64 v[12:15], %[rr] offset0:6 offset1:7\n\t" \
    "s_waitcnt lgkmcnt(3)\n\t" \
    "v_pk_add_f32 v[0:1], v[0:1], v[2:3]\n\t" \
    "s_waitcnt lgkmcnt(2)\n\t" \
    "v_pk_add_f32 v[4:5], v[4:5], v[6:7]\n\t" \
    "s_waitcnt lgkmcnt(1)\n\t" \
    "v_pk_add_f32 v[8:9], v[8:9], v[10:11]\n\t" \
    "v_pk_add_f32 v[0:1], v[0:1], v[4:5]\n\t" \
    "s_waitcnt lgkmcnt(0)\n\t" \
    "v_pk_add_f32 v[12:13], v[12:13], v[14:15]\n\t" \
    "s_nop 0\n\t" \
    "v_pk_add_f32 v[8:9], v[8:9], v[12:13]\n\t" \
    "s_nop 0\n\t" \
    "v_pk_add_f32 v[0:1], v[0:1], v[8:9]\n\t" \
    "s_nop 0\n\t" \
    "v_cvt_pk_bf16_f32 v0, v0, v1\n\t" \
    "v_min_u32 v0, -2, v0\n\t" \
    "buffer_store_dword v0, %[mo], %[rs], %[sn] offen sc1\n\t" \
    "s_memrealtime %[t1]\n\t" \
    DGA \
    "s_waitcnt lgkmcnt(0)\n\t" \
    GSA(2) \
    "s_branch E_%=\n\t" \
    "SL_%=:\n\t" \
    "s_sleep 1\n\t" \
    "buffer_load_dwordx4 v[220:223], %[l16], %[rs], %[s0] offen sc1\n\t" \
    "buffer_load_dwordx4 v[224:227], %[l16], %[rs], %[s1] offen sc1\n\t" \
    "s_sub_u32 %[lim], %[lim], " STR(DEC2) "\n\t" \
    "s_cmp_gt_i32 %[lim], 0\n\t" \
    "s_cbranch_scc0 G_%=\n\t" \
    "s_nop 3\n\t" \
    "v_mov_b32 v0, 0\n\t" \
    "v_mov_b32 v1, 0\n\t" \
    "v_mov_b32 v2, 0\n\t" \
    "v_mov_b32 v3, 0\n\t" \
    "v_mov_b32 v4, 0\n\t" \
    "v_mov_b32 v5, 0\n\t" \
    "v_mov_b32 v6, 0\n\t" \
    "v_mov_b32 v7, 0\n\t" \
    "v_mov_b32 v8, 0\n\t" \
    "v_mov_b32 v9, 0\n\t" \
    "v_mov_b32 v10, 0\n\t" \
    "v_mov_b32 v11, 0\n\t" \
    "v_mov_b32 v12, 0\n\t" \
    "v_mov_b32 v13, 0\n\t" \
    "v_mov_b32 v14, 0\n\t" \
    "v_mov_b32 v15, 0\n\t" \
    "v_mov_b32 v252, 0\n\t" \
    "s_branch A_%=\n\t" \
    "G_%=:\n\t" \
    "s_waitcnt vmcnt(0)\n\t" \
    "s_branch D_%=\n\t" \
    "E_%=:\n\t" \
    GSW \
    : [lim] "+s"(lim) GSO(1) GSO(4), [t1] "=&s"(t1) DGO DGQ GSO(2) \
    : [l16] "v"(l16), [rs] "s"(rs), [idx] "v"(idx), [rw] "v"(rw_a), [s0] "s"(so[0]), [s1] "s"(so[1]), [rr] "v"(rr_a), [mo] "v"(mo_v), [sn] "s"(sn), [red] "s"(wid), "{v[156:163]}"(wo[0][0][0]), "{v[164:171]}"(wo[0][0][1]), "{v[172:179]}"(wo[0][1][0]), "{v[180:187]}"(wo[0][1][1]), "{v[188:195]}"(wo[1][0][0]), "{v[196:203]}"(wo[1][0][1]), "{v[204:211]}"(wo[1][1][0]), "{v[212:219]}"(wo[1][1][1]) \
    : "memory", "vcc", "scc", "v252", "v0", "v1", "v2", "v3", "v4", "v5", "v6", "v7", "v8", "v9", "v10", "v11", "v12", "v13", "v14", "v15", "v220", "v221", "v222", "v223", "v224", "v225", "v226", "v227")

#define WO_W() \
  asm volatile( \
    "v_mov_b32 v0, 0\n\t" \
    "v_mov_b32 v1, 0\n\t" \
    "v_mov_b32 v2, 0\n\t" \
    "v_mov_b32 v3, 0\n\t" \
    "v_mov_b32 v4, 0\n\t" \
    "v_mov_b32 v5, 0\n\t" \
    "v_mov_b32 v6, 0\n\t" \
    "v_mov_b32 v7, 0\n\t" \
    "v_mov_b32 v8, 0\n\t" \
    "v_mov_b32 v9, 0\n\t" \
    "v_mov_b32 v10, 0\n\t" \
    "v_mov_b32 v11, 0\n\t" \
    "v_mov_b32 v12, 0\n\t" \
    "v_mov_b32 v13, 0\n\t" \
    "v_mov_b32 v14, 0\n\t" \
    "v_mov_b32 v15, 0\n\t" \
    "v_mov_b32 v252, 0\n\t" \
    "s_barrier\n\t" \
    GSA(4) \
    "buffer_load_dwordx4 v[220:223], %[l16], %[rs], %[s0] offen sc1\n\t" \
    "s_nop 15\n\t" \
    "buffer_load_dwordx4 v[224:227], %[l16], %[rs], %[s1] offen sc1\n\t" \
    "s_nop 15\n\t" \
    "buffer_load_dwordx4 v[228:231], %[l16], %[rs], %[s2] offen sc1\n\t" \
    "s_nop 15\n\t" \
    "buffer_load_dwordx4 v[232:235], %[l16], %[rs], %[s3] offen sc1\n\t" \
    "s_nop 15\n\t" \
    "buffer_load_dwordx4 v[236:239], %[l16], %[rs], %[s4] offen sc1\n\t" \
    "s_nop 15\n\t" \
    "buffer_load_dwordx4 v[240:243], %[l16], %[rs], %[s5] offen sc1\n\t" \
    "A_%=:\n\t" \
    "s_waitcnt vmcnt(5)\n\t" \
    "v_smfmac_f32_16x16x64_bf16 v[0:3], v[220:223], v[28:35], %[idx]\n\t" \
    "v_max3_u32 v252, v252, v220, v221\n\t" \
    "v_smfmac_f32_16x16x64_bf16 v[4:7], v[220:223], v[36:43], %[idx]\n\t" \
    "v_max3_u32 v252, v252, v222, v223\n\t" \
    "v_smfmac_f32_16x16x64_bf16 v[8:11], v[220:223], v[44:51], %[idx]\n\t" \
    "v_smfmac_f32_16x16x64_bf16 v[12:15], v[220:223], v[52:59], %[idx]\n\t" \
    "s_waitcnt vmcnt(4)\n\t" \
    "v_smfmac_f32_16x16x64_bf16 v[0:3], v[224:227], v[60:67], %[idx]\n\t" \
    "v_max3_u32 v252, v252, v224, v225\n\t" \
    "v_smfmac_f32_16x16x64_bf16 v[4:7], v[224:227], v[68:75], %[idx]\n\t" \
    "v_max3_u32 v252, v252, v226, v227\n\t" \
    "v_smfmac_f32_16x16x64_bf16 v[8:11], v[224:227], v[76:83], %[idx]\n\t" \
    "v_smfmac_f32_16x16x64_bf16 v[12:15], v[224:227], v[84:91], %[idx]\n\t" \
    "s_waitcnt vmcnt(3)\n\t" \
    "v_smfmac_f32_16x16x64_bf16 v[0:3], v[228:231], v[92:99], %[idx]\n\t" \
    "v_max3_u32 v252, v252, v228, v229\n\t" \
    "v_smfmac_f32_16x16x64_bf16 v[4:7], v[228:231], v[100:107], %[idx]\n\t" \
    "v_max3_u32 v252, v252, v230, v231\n\t" \
    "v_smfmac_f32_16x16x64_bf16 v[8:11], v[228:231], v[108:115], %[idx]\n\t" \
    "v_smfmac_f32_16x16x64_bf16 v[12:15], v[228:231], v[116:123], %[idx]\n\t" \
    "s_waitcnt vmcnt(2)\n\t" \
    "v_smfmac_f32_16x16x64_bf16 v[0:3], v[232:235], v[124:131], %[idx]\n\t" \
    "v_max3_u32 v252, v252, v232, v233\n\t" \
    "v_smfmac_f32_16x16x64_bf16 v[4:7], v[232:235], v[132:139], %[idx]\n\t" \
    "v_max3_u32 v252, v252, v234, v235\n\t" \
    "v_smfmac_f32_16x16x64_bf16 v[8:11], v[232:235], v[140:147], %[idx]\n\t" \
    "v_smfmac_f32_16x16x64_bf16 v[12:15], v[232:235], v[148:155], %[idx]\n\t" \
    "s_waitcnt vmcnt(1)\n\t" \
    "v_smfmac_f32_16x16x64_bf16 v[0:3], v[236:239], v[156:163], %[idx]\n\t" \
    "v_max3_u32 v252, v252, v236, v237\n\t" \
    "v_smfmac_f32_16x16x64_bf16 v[4:7], v[236:239], v[164:171], %[idx]\n\t" \
    "v_max3_u32 v252, v252, v238, v239\n\t" \
    "v_smfmac_f32_16x16x64_bf16 v[8:11], v[236:239], v[172:179], %[idx]\n\t" \
    "v_smfmac_f32_16x16x64_bf16 v[12:15], v[236:239], v[180:187], %[idx]\n\t" \
    "s_waitcnt vmcnt(0)\n\t" \
    "v_smfmac_f32_16x16x64_bf16 v[0:3], v[240:243], v[188:195], %[idx]\n\t" \
    "v_max3_u32 v252, v252, v240, v241\n\t" \
    "v_smfmac_f32_16x16x64_bf16 v[4:7], v[240:243], v[196:203], %[idx]\n\t" \
    "v_max3_u32 v252, v252, v242, v243\n\t" \
    "v_smfmac_f32_16x16x64_bf16 v[8:11], v[240:243], v[204:211], %[idx]\n\t" \
    "v_smfmac_f32_16x16x64_bf16 v[12:15], v[240:243], v[212:219], %[idx]\n\t" \
    "v_cmp_eq_u32 vcc, -1, v252\n\t" \
    "s_cbranch_vccnz SL_%=\n\t" \
    "D_%=:\n\t" \
    GSA(1) \
    "s_nop 2\n\t" \
    "v_add_f32 v0, v0, v1\n\t" \
    "v_add_f32 v2, v2, v3\n\t" \
    "v_add_f32 v4, v4, v5\n\t" \
    "v_add_f32 v6, v6, v7\n\t" \
    "v_add_f32 v8, v8, v9\n\t" \
    "v_add_f32 v10, v10, v11\n\t" \
    "v_add_f32 v12, v12, v13\n\t" \
    "v_add_f32 v14, v14, v15\n\t" \
    "v_permlane32_swap_b32 v0, v4\n\t" \
    "v_permlane32_swap_b32 v2, v6\n\t" \
    "v_permlane32_swap_b32 v8, v12\n\t" \
    "v_permlane32_swap_b32 v10, v14\n\t" \
    "v_add_f32 v0, v0, v12\n\t" \
    "v_add_f32 v2, v2, v14\n\t" \
    "ds_write2_b32 %[rw], v0, v2 offset0:0 offset1:32\n\t" \
    "s_waitcnt lgkmcnt(0)\n\t" \
    "s_barrier\n\t" \
    "s_branch E_%=\n\t" \
    "SL_%=:\n\t" \
    "s_sleep 1\n\t" \
    "buffer_load_dwordx4 v[220:223], %[l16], %[rs], %[s0] offen sc1\n\t" \
    "buffer_load_dwordx4 v[224:227], %[l16], %[rs], %[s1] offen sc1\n\t" \
    "buffer_load_dwordx4 v[228:231], %[l16], %[rs], %[s2] offen sc1\n\t" \
    "buffer_load_dwordx4 v[232:235], %[l16], %[rs], %[s3] offen sc1\n\t" \
    "buffer_load_dwordx4 v[236:239], %[l16], %[rs], %[s4] offen sc1\n\t" \
    "buffer_load_dwordx4 v[240:243], %[l16], %[rs], %[s5] offen sc1\n\t" \
    "s_sub_u32 %[lim], %[lim], " STR(DEC2) "\n\t" \
    "s_cmp_gt_i32 %[lim], 0\n\t" \
    "s_cbranch_scc0 G_%=\n\t" \
    "s_nop 3\n\t" \
    "v_mov_b32 v0, 0\n\t" \
    "v_mov_b32 v1, 0\n\t" \
    "v_mov_b32 v2, 0\n\t" \
    "v_mov_b32 v3, 0\n\t" \
    "v_mov_b32 v4, 0\n\t" \
    "v_mov_b32 v5, 0\n\t" \
    "v_mov_b32 v6, 0\n\t" \
    "v_mov_b32 v7, 0\n\t" \
    "v_mov_b32 v8, 0\n\t" \
    "v_mov_b32 v9, 0\n\t" \
    "v_mov_b32 v10, 0\n\t" \
    "v_mov_b32 v11, 0\n\t" \
    "v_mov_b32 v12, 0\n\t" \
    "v_mov_b32 v13, 0\n\t" \
    "v_mov_b32 v14, 0\n\t" \
    "v_mov_b32 v15, 0\n\t" \
    "v_mov_b32 v252, 0\n\t" \
    "s_branch A_%=\n\t" \
    "G_%=:\n\t" \
    "s_waitcnt vmcnt(0)\n\t" \
    "s_branch D_%=\n\t" \
    "E_%=:\n\t" \
    GSW \
    : [lim] "+s"(lim) GSO(1) GSO(4) \
    : [l16] "v"(l16), [rs] "s"(rs), [idx] "v"(idx), [rw] "v"(rw_a), [s0] "s"(so[0]), [s1] "s"(so[1]), [s2] "s"(so[2]), [s3] "s"(so[3]), [s4] "s"(so[4]), [s5] "s"(so[5]), "{v[28:35]}"(wo[0][0][0]), "{v[36:43]}"(wo[0][0][1]), "{v[44:51]}"(wo[0][1][0]), "{v[52:59]}"(wo[0][1][1]), "{v[60:67]}"(wo[1][0][0]), "{v[68:75]}"(wo[1][0][1]), "{v[76:83]}"(wo[1][1][0]), "{v[84:91]}"(wo[1][1][1]), "{v[92:99]}"(wo[2][0][0]), "{v[100:107]}"(wo[2][0][1]), "{v[108:115]}"(wo[2][1][0]), "{v[116:123]}"(wo[2][1][1]), "{v[124:131]}"(wo[3][0][0]), "{v[132:139]}"(wo[3][0][1]), "{v[140:147]}"(wo[3][1][0]), "{v[148:155]}"(wo[3][1][1]), "{v[156:163]}"(wo[4][0][0]), "{v[164:171]}"(wo[4][0][1]), "{v[172:179]}"(wo[4][1][0]), "{v[180:187]}"(wo[4][1][1]), "{v[188:195]}"(wo[5][0][0]), "{v[196:203]}"(wo[5][0][1]), "{v[204:211]}"(wo[5][1][0]), "{v[212:219]}"(wo[5][1][1]) \
    : "memory", "vcc", "scc", "v252", "v0", "v1", "v2", "v3", "v4", "v5", "v6", "v7", "v8", "v9", "v10", "v11", "v12", "v13", "v14", "v15", "v220", "v221", "v222", "v223", "v224", "v225", "v226", "v227", "v228", "v229", "v230", "v231", "v232", "v233", "v234", "v235", "v236", "v237", "v238", "v239", "v240", "v241", "v242", "v243")

#define S_LOOP_K(TE_, FIN_) \
  asm volatile( \
    "s_cmp_eq_u32 %[kh], 0\n\t" \
    "s_cbranch_scc0 L1_%=\n\t" \
    "L0_%=:\n\t" \
    "s_setprio 0\n\t" \
    "s_nop 3\n\t" \
    "ds_write_b128 %[pa], v[196:199] offset:0\n\t" \
    "ds_write_b128 %[pa], v[200:203] offset:1024\n\t" \
    "ds_write_b128 %[pa], v[204:207] offset:2048\n\t" \
    "ds_write_b128 %[pa], v[208:211] offset:3072\n\t" \
    "s_sub_u32 %[a], %[t], 1\n\t" \
    "v_mov_b32 v53, %[tag]\n\t" \
    "v_mov_b32 v52, %[a]\n\t" \
    "s_sub_u32 %[b], %[t], 2\n\t" \
    "s_cmp_lt_u32 %[b], " STR(SQUICK) "\n\t" \
    "s_cselect_b64 exec, %[fm], 0\n\t" \
    "buffer_store_dwordx2 v[52:53], %[l16], %[rs], %[fo] offen sc1\n\t" \
    "s_mov_b64 exec, -1\n\t" \
    "s_add_u32 m0, %[sd], 49152\n\t" \
    "s_add_u32 %[a], %[sx], 81920\n\t" \
    "s_waitcnt lgkmcnt(0)\n\t" \
    "buffer_load_dwordx4 %[l16], %[rs], %[a] offen offset:0 sc1 lds\n\t" \
    "buffer_load_dwordx4 %[l16], %[rs], %[a] offen offset:1024 sc1 lds\n\t" \
    "buffer_load_dwordx4 %[l16], %[rs], %[a] offen offset:2048 sc1 lds\n\t" \
    "buffer_load_dwordx4 %[l16], %[rs], %[a] offen offset:3072 sc1 lds\n\t" \
    "v_mfma_f32_16x16x32_bf16 v[0:3], v[68:71], v[20:23], 0\n\t" \
    "ds_read_b128 v[44:47], %[xp] offset:6144\n\t" \
    "v_mfma_f32_16x16x32_bf16 v[4:7], v[76:79], v[20:23], 0\n\t" \
    "ds_read_b128 v[48:51], %[xp] offset:7168\n\t" \
    "v_mfma_f32_16x16x32_bf16 v[8:11], v[68:71], v[24:27], 0\n\t" \
    "v_mfma_f32_16x16x32_bf16 v[12:15], v[76:79], v[24:27], 0\n\t" \
    "v_mfma_f32_16x16x32_bf16 v[0:3], v[72:75], v[28:31], v[0:3]\n\t" \
    "ds_read_b128 v[20:23], %[xp] offset:8192\n\t" \
    "v_mfma_f32_16x16x32_bf16 v[4:7], v[80:83], v[28:31], v[4:7]\n\t" \
    "ds_read_b128 v[24:27], %[xp] offset:9216\n\t" \
    "v_mfma_f32_16x16x32_bf16 v[8:11], v[72:75], v[32:35], v[8:11]\n\t" \
    "v_mfma_f32_16x16x32_bf16 v[12:15], v[80:83], v[32:35], v[12:15]\n\t" \
    "v_mfma_f32_16x16x32_bf16 v[0:3], v[84:87], v[36:39], v[0:3]\n\t" \
    "ds_read_b128 v[28:31], %[xp] offset:10240\n\t" \
    "v_mfma_f32_16x16x32_bf16 v[4:7], v[92:95], v[36:39], v[4:7]\n\t" \
    "ds_read_b128 v[32:35], %[xp] offset:11264\n\t" \
    "v_mfma_f32_16x16x32_bf16 v[8:11], v[84:87], v[40:43], v[8:11]\n\t" \
    "s_waitcnt lgkmcnt(4)\n\t" \
    "v_mfma_f32_16x16x32_bf16 v[12:15], v[92:95], v[40:43], v[12:15]\n\t" \
    "v_mfma_f32_16x16x32_bf16 v[0:3], v[88:91], v[44:47], v[0:3]\n\t" \
    "ds_read_b128 v[36:39], %[xp] offset:12288\n\t" \
    "v_mfma_f32_16x16x32_bf16 v[4:7], v[96:99], v[44:47], v[4:7]\n\t" \
    "ds_read_b128 v[40:43], %[xp] offset:13312\n\t" \
    "v_mfma_f32_16x16x32_bf16 v[8:11], v[88:91], v[48:51], v[8:11]\n\t" \
    "s_waitcnt lgkmcnt(4)\n\t" \
    "v_mfma_f32_16x16x32_bf16 v[12:15], v[96:99], v[48:51], v[12:15]\n\t" \
    "v_mfma_f32_16x16x32_bf16 v[0:3], v[100:103], v[20:23], v[0:3]\n\t" \
    "ds_read_b128 v[44:47], %[xp] offset:14336\n\t" \
    "v_mfma_f32_16x16x32_bf16 v[4:7], v[108:111], v[20:23], v[4:7]\n\t" \
    "ds_read_b128 v[48:51], %[xp] offset:15360\n\t" \
    "v_mfma_f32_16x16x32_bf16 v[8:11], v[100:103], v[24:27], v[8:11]\n\t" \
    "s_waitcnt lgkmcnt(4)\n\t" \
    "v_mfma_f32_16x16x32_bf16 v[12:15], v[108:111], v[24:27], v[12:15]\n\t" \
    "v_mfma_f32_16x16x32_bf16 v[0:3], v[104:107], v[28:31], v[0:3]\n\t" \
    "ds_read_b128 v[20:23], %[xp] offset:16384\n\t" \
    "v_mfma_f32_16x16x32_bf16 v[4:7], v[112:115], v[28:31], v[4:7]\n\t" \
    "ds_read_b128 v[24:27], %[xp] offset:17408\n\t" \
    "v_mfma_f32_16x16x32_bf16 v[8:11], v[104:107], v[32:35], v[8:11]\n\t" \
    "s_waitcnt lgkmcnt(4)\n\t" \
    "v_mfma_f32_16x16x32_bf16 v[12:15], v[112:115], v[32:35], v[12:15]\n\t" \
    "v_mfma_f32_16x16x32_bf16 v[0:3], v[116:119], v[36:39], v[0:3]\n\t" \
    "ds_read_b128 v[28:31], %[xp] offset:18432\n\t" \
    "v_mfma_f32_16x16x32_bf16 v[4:7], v[124:127], v[36:39], v[4:7]\n\t" \
    "ds_read_b128 v[32:35], %[xp] offset:19456\n\t" \
    "v_mfma_f32_16x16x32_bf16 v[8:11], v[116:119], v[40:43], v[8:11]\n\t" \
    "s_waitcnt lgkmcnt(4)\n\t" \
    "v_mfma_f32_16x16x32_bf16 v[12:15], v[124:127], v[40:43], v[12:15]\n\t" \
    "v_mfma_f32_16x16x32_bf16 v[0:3], v[120:123], v[44:47], v[0:3]\n\t" \
    "ds_read_b128 v[36:39], %[xp] offset:20480\n\t" \
    "v_mfma_f32_16x16x32_bf16 v[4:7], v[128:131], v[44:47], v[4:7]\n\t" \
    "ds_read_b128 v[40:43], %[xp] offset:21504\n\t" \
    "v_mfma_f32_16x16x32_bf16 v[8:11], v[120:123], v[48:51], v[8:11]\n\t" \
    "s_waitcnt lgkmcnt(4)\n\t" \
    "v_mfma_f32_16x16x32_bf16 v[12:15], v[128:131], v[48:51], v[12:15]\n\t" \
    "s_waitcnt vmcnt(4)\n\t" \
    "s_barrier\n\t" \
    "s_setprio 0\n\t" \
    "s_sub_u32 %[a], %[t], 1\n\t" \
    "v_mov_b32 v53, %[tag]\n\t" \
    "v_mov_b32 v52, %[a]\n\t" \
    "s_cmp_gt_u32 %[t], 1\n\t" \
    "s_cselect_b64 exec, %[fm], 0\n\t" \
    "buffer_store_dwordx2 v[52:53], %[l16], %[rs], %[fo] offen sc1\n\t" \
    "s_mov_b64 exec, %[pm]\n\t" \
    "s_mov_b32 m0, %[pd]\n\t" \
    "s_add_u32 %[a], %[sx], %[po]\n\t" \
    "buffer_load_dwordx4 %[l16], %[rs], %[a] offen sc1 lds\n\t" \
    "s_mov_b64 exec, -1\n\t" \
    "s_add_u32 m0, %[sd], 0\n\t" \
    "s_add_u32 %[a], %[sx], 131072\n\t" \
    "buffer_load_dwordx4 %[l16], %[rs], %[a] offen offset:0 sc1 lds\n\t" \
    "buffer_load_dwordx4 %[l16], %[rs], %[a] offen offset:1024 sc1 lds\n\t" \
    "buffer_load_dwordx4 %[l16], %[rs], %[a] offen offset:2048 sc1 lds\n\t" \
    "buffer_load_dwordx4 %[l16], %[rs], %[a] offen offset:3072 sc1 lds\n\t" \
    "v_mfma_f32_16x16x32_bf16 v[0:3], v[132:135], v[20:23], v[0:3]\n\t" \
    "ds_read_b128 v[44:47], %[xp] offset:22528\n\t" \
    "v_mfma_f32_16x16x32_bf16 v[4:7], v[140:143], v[20:23], v[4:7]\n\t" \
    "ds_read_b128 v[48:51], %[xp] offset:23552\n\t" \
    "v_mfma_f32_16x16x32_bf16 v[8:11], v[132:135], v[24:27], v[8:11]\n\t" \
    "s_waitcnt lgkmcnt(4)\n\t" \
    "v_mfma_f32_16x16x32_bf16 v[12:15], v[140:143], v[24:27], v[12:15]\n\t" \
    "v_mfma_f32_16x16x32_bf16 v[0:3], v[136:139], v[28:31], v[0:3]\n\t" \
    "ds_read_b128 v[20:23], %[xp] offset:24576\n\t" \
    "v_mfma_f32_16x16x32_bf16 v[4:7], v[144:147], v[28:31], v[4:7]\n\t" \
    "ds_read_b128 v[24:27], %[xp] offset:25600\n\t" \
    "v_mfma_f32_16x16x32_bf16 v[8:11], v[136:139], v[32:35], v[8:11]\n\t" \
    "s_waitcnt lgkmcnt(4)\n\t" \
    "v_mfma_f32_16x16x32_bf16 v[12:15], v[144:147], v[32:35], v[12:15]\n\t" \
    "v_mfma_f32_16x16x32_bf16 v[0:3], v[148:151], v[36:39], v[0:3]\n\t" \
    "ds_read_b128 v[28:31], %[xp] offset:26624\n\t" \
    "v_mfma_f32_16x16x32_bf16 v[4:7], v[156:159], v[36:39], v[4:7]\n\t" \
    "ds_read_b128 v[32:35], %[xp] offset:27648\n\t" \
    "v_mfma_f32_16x16x32_bf16 v[8:11], v[148:151], v[40:43], v[8:11]\n\t" \
    "s_waitcnt lgkmcnt(4)\n\t" \
    "v_mfma_f32_16x16x32_bf16 v[12:15], v[156:159], v[40:43], v[12:15]\n\t" \
    "v_mfma_f32_16x16x32_bf16 v[0:3], v[152:155], v[44:47], v[0:3]\n\t" \
    "ds_read_b128 v[36:39], %[xp] offset:28672\n\t" \
    "v_mfma_f32_16x16x32_bf16 v[4:7], v[160:163], v[44:47], v[4:7]\n\t" \
    "ds_read_b128 v[40:43], %[xp] offset:29696\n\t" \
    "v_mfma_f32_16x16x32_bf16 v[8:11], v[152:155], v[48:51], v[8:11]\n\t" \
    "s_waitcnt lgkmcnt(4)\n\t" \
    "v_mfma_f32_16x16x32_bf16 v[12:15], v[160:163], v[48:51], v[12:15]\n\t" \
    "v_mfma_f32_16x16x32_bf16 v[0:3], v[164:167], v[20:23], v[0:3]\n\t" \
    "ds_read_b128 v[44:47], %[xp] offset:30720\n\t" \
    "v_mfma_f32_16x16x32_bf16 v[4:7], v[172:175], v[20:23], v[4:7]\n\t" \
    "ds_read_b128 v[48:51], %[xp] offset:31744\n\t" \
    "v_mfma_f32_16x16x32_bf16 v[8:11], v[164:167], v[24:27], v[8:11]\n\t" \
    "s_waitcnt lgkmcnt(4)\n\t" \
    "v_mfma_f32_16x16x32_bf16 v[12:15], v[172:175], v[24:27], v[12:15]\n\t" \
    "v_mfma_f32_16x16x32_bf16 v[0:3], v[168:171], v[28:31], v[0:3]\n\t" \
    "ds_read_b128 v[20:23], %[xp] offset:32768\n\t" \
    "v_mfma_f32_16x16x32_bf16 v[4:7], v[176:179], v[28:31], v[4:7]\n\t" \
    "ds_read_b128 v[24:27], %[xp] offset:33792\n\t" \
    "v_mfma_f32_16x16x32_bf16 v[8:11], v[168:171], v[32:35], v[8:11]\n\t" \
    "s_waitcnt lgkmcnt(4)\n\t" \
    "v_mfma_f32_16x16x32_bf16 v[12:15], v[176:179], v[32:35], v[12:15]\n\t" \
    "v_mfma_f32_16x16x32_bf16 v[0:3], v[180:183], v[36:39], v[0:3]\n\t" \
    "ds_read_b128 v[28:31], %[xp] offset:34816\n\t" \
    "v_mfma_f32_16x16x32_bf16 v[4:7], v[188:191], v[36:39], v[4:7]\n\t" \
    "ds_read_b128 v[32:35], %[xp] offset:35840\n\t" \
    "v_mfma_f32_16x16x32_bf16 v[8:11], v[180:183], v[40:43], v[8:11]\n\t" \
    "s_waitcnt lgkmcnt(4)\n\t" \
    "v_mfma_f32_16x16x32_bf16 v[12:15], v[188:191], v[40:43], v[12:15]\n\t" \
    "v_mfma_f32_16x16x32_bf16 v[0:3], v[184:187], v[44:47], v[0:3]\n\t" \
    "ds_read_b128 v[36:39], %[xp] offset:36864\n\t" \
    "v_mfma_f32_16x16x32_bf16 v[4:7], v[192:195], v[44:47], v[4:7]\n\t" \
    "ds_read_b128 v[40:43], %[xp] offset:37888\n\t" \
    "v_mfma_f32_16x16x32_bf16 v[8:11], v[184:187], v[48:51], v[8:11]\n\t" \
    "s_waitcnt lgkmcnt(4)\n\t" \
    "v_mfma_f32_16x16x32_bf16 v[12:15], v[192:195], v[48:51], v[12:15]\n\t" \
    "s_add_u32 %[sx], %[sx], 65536\n\t" \
    "s_add_u32 %[t], %[t], 1\n\t" \
    "s_waitcnt vmcnt(4)\n\t" \
    "s_barrier\n\t" \
    "s_setprio 0\n\t" \
    "s_nop 3\n\t" \
    "ds_write_b128 %[pa], v[0:3] offset:0\n\t" \
    "ds_write_b128 %[pa], v[4:7] offset:1024\n\t" \
    "ds_write_b128 %[pa], v[8:11] offset:2048\n\t" \
    "ds_write_b128 %[pa], v[12:15] offset:3072\n\t" \
    "s_sub_u32 %[a], %[t], 1\n\t" \
    "v_mov_b32 v53, %[tag]\n\t" \
    "v_mov_b32 v52, %[a]\n\t" \
    "s_sub_u32 %[b], %[t], 2\n\t" \
    "s_cmp_lt_u32 %[b], " STR(SQUICK) "\n\t" \
    "s_cselect_b64 exec, %[fm], 0\n\t" \
    "buffer_store_dwordx2 v[52:53], %[l16], %[rs], %[fo] offen sc1\n\t" \
    "s_mov_b64 exec, -1\n\t" \
    "s_add_u32 m0, %[sd], 16384\n\t" \
    "s_add_u32 %[a], %[sx], 81920\n\t" \
    "s_waitcnt lgkmcnt(0)\n\t" \
    "buffer_load_dwordx4 %[l16], %[rs], %[a] offen offset:0 sc1 lds\n\t" \
    "buffer_load_dwordx4 %[l16], %[rs], %[a] offen offset:1024 sc1 lds\n\t" \
    "buffer_load_dwordx4 %[l16], %[rs], %[a] offen offset:2048 sc1 lds\n\t" \
    "buffer_load_dwordx4 %[l16], %[rs], %[a] offen offset:3072 sc1 lds\n\t" \
    "v_mfma_f32_16x16x32_bf16 v[196:199], v[68:71], v[20:23], 0\n\t" \
    "ds_read_b128 v[44:47], %[xp] offset:38912\n\t" \
    "v_mfma_f32_16x16x32_bf16 v[200:203], v[76:79], v[20:23], 0\n\t" \
    "ds_read_b128 v[48:51], %[xp] offset:39936\n\t" \
    "v_mfma_f32_16x16x32_bf16 v[204:207], v[68:71], v[24:27], 0\n\t" \
    "v_mfma_f32_16x16x32_bf16 v[208:211], v[76:79], v[24:27], 0\n\t" \
    "v_mfma_f32_16x16x32_bf16 v[196:199], v[72:75], v[28:31], v[196:199]\n\t" \
    "ds_read_b128 v[20:23], %[xp] offset:40960\n\t" \
    "v_mfma_f32_16x16x32_bf16 v[200:203], v[80:83], v[28:31], v[200:203]\n\t" \
    "ds_read_b128 v[24:27], %[xp] offset:41984\n\t" \
    "v_mfma_f32_16x16x32_bf16 v[204:207], v[72:75], v[32:35], v[204:207]\n\t" \
    "v_mfma_f32_16x16x32_bf16 v[208:211], v[80:83], v[32:35], v[208:211]\n\t" \
    "v_mfma_f32_16x16x32_bf16 v[196:199], v[84:87], v[36:39], v[196:199]\n\t" \
    "ds_read_b128 v[28:31], %[xp] offset:43008\n\t" \
    "v_mfma_f32_16x16x32_bf16 v[200:203], v[92:95], v[36:39], v[200:203]\n\t" \
    "ds_read_b128 v[32:35], %[xp] offset:44032\n\t" \
    "v_mfma_f32_16x16x32_bf16 v[204:207], v[84:87], v[40:43], v[204:207]\n\t" \
    "s_waitcnt lgkmcnt(4)\n\t" \
    "v_mfma_f32_16x16x32_bf16 v[208:211], v[92:95], v[40:43], v[208:211]\n\t" \
    "v_mfma_f32_16x16x32_bf16 v[196:199], v[88:91], v[44:47], v[196:199]\n\t" \
    "ds_read_b128 v[36:39], %[xp] offset:45056\n\t" \
    "v_mfma_f32_16x16x32_bf16 v[200:203], v[96:99], v[44:47], v[200:203]\n\t" \
    "ds_read_b128 v[40:43], %[xp] offset:46080\n\t" \
    "v_mfma_f32_16x16x32_bf16 v[204:207], v[88:91], v[48:51], v[204:207]\n\t" \
    "s_waitcnt lgkmcnt(4)\n\t" \
    "v_mfma_f32_16x16x32_bf16 v[208:211], v[96:99], v[48:51], v[208:211]\n\t" \
    "v_mfma_f32_16x16x32_bf16 v[196:199], v[100:103], v[20:23], v[196:199]\n\t" \
    "ds_read_b128 v[44:47], %[xp] offset:47104\n\t" \
    "v_mfma_f32_16x16x32_bf16 v[200:203], v[108:111], v[20:23], v[200:203]\n\t" \
    "ds_read_b128 v[48:51], %[xp] offset:48128\n\t" \
    "v_mfma_f32_16x16x32_bf16 v[204:207], v[100:103], v[24:27], v[204:207]\n\t" \
    "s_waitcnt lgkmcnt(4)\n\t" \
    "v_mfma_f32_16x16x32_bf16 v[208:211], v[108:111], v[24:27], v[208:211]\n\t" \
    "v_mfma_f32_16x16x32_bf16 v[196:199], v[104:107], v[28:31], v[196:199]\n\t" \
    "ds_read_b128 v[20:23], %[xp] offset:49152\n\t" \
    "v_mfma_f32_16x16x32_bf16 v[200:203], v[112:115], v[28:31], v[200:203]\n\t" \
    "ds_read_b128 v[24:27], %[xp] offset:50176\n\t" \
    "v_mfma_f32_16x16x32_bf16 v[204:207], v[104:107], v[32:35], v[204:207]\n\t" \
    "s_waitcnt lgkmcnt(4)\n\t" \
    "v_mfma_f32_16x16x32_bf16 v[208:211], v[112:115], v[32:35], v[208:211]\n\t" \
    "v_mfma_f32_16x16x32_bf16 v[196:199], v[116:119], v[36:39], v[196:199]\n\t" \
    "ds_read_b128 v[28:31], %[xp] offset:51200\n\t" \
    "v_mfma_f32_16x16x32_bf16 v[200:203], v[124:127], v[36:39], v[200:203]\n\t" \
    "ds_read_b128 v[32:35], %[xp] offset:52224\n\t" \
    "v_mfma_f32_16x16x32_bf16 v[204:207], v[116:119], v[40:43], v[204:207]\n\t" \
    "s_waitcnt lgkmcnt(4)\n\t" \
    "v_mfma_f32_16x16x32_bf16 v[208:211], v[124:127], v[40:43], v[208:211]\n\t" \
    "v_mfma_f32_16x16x32_bf16 v[196:199], v[120:123], v[44:47], v[196:199]\n\t" \
    "ds_read_b128 v[36:39], %[xp] offset:53248\n\t" \
    "v_mfma_f32_16x16x32_bf16 v[200:203], v[128:131], v[44:47], v[200:203]\n\t" \
    "ds_read_b128 v[40:43], %[xp] offset:54272\n\t" \
    "v_mfma_f32_16x16x32_bf16 v[204:207], v[120:123], v[48:51], v[204:207]\n\t" \
    "s_waitcnt lgkmcnt(4)\n\t" \
    "v_mfma_f32_16x16x32_bf16 v[208:211], v[128:131], v[48:51], v[208:211]\n\t" \
    "s_waitcnt vmcnt(4)\n\t" \
    "s_barrier\n\t" \
    "s_setprio 0\n\t" \
    "s_sub_u32 %[a], %[t], 1\n\t" \
    "v_mov_b32 v53, %[tag]\n\t" \
    "v_mov_b32 v52, %[a]\n\t" \
    "s_cmp_gt_u32 %[t], 1\n\t" \
    "s_cselect_b64 exec, %[fm], 0\n\t" \
    "buffer_store_dwordx2 v[52:53], %[l16], %[rs], %[fo] offen sc1\n\t" \
    "s_mov_b64 exec, %[pm]\n\t" \
    "s_mov_b32 m0, %[pd]\n\t" \
    "s_add_u32 %[a], %[sx], %[po]\n\t" \
    "buffer_load_dwordx4 %[l16], %[rs], %[a] offen sc1 lds\n\t" \
    "s_mov_b64 exec, -1\n\t" \
    "s_add_u32 m0, %[sd], 32768\n\t" \
    "s_add_u32 %[a], %[sx], 131072\n\t" \
    "buffer_load_dwordx4 %[l16], %[rs], %[a] offen offset:0 sc1 lds\n\t" \
    "buffer_load_dwordx4 %[l16], %[rs], %[a] offen offset:1024 sc1 lds\n\t" \
    "buffer_load_dwordx4 %[l16], %[rs], %[a] offen offset:2048 sc1 lds\n\t" \
    "buffer_load_dwordx4 %[l16], %[rs], %[a] offen offset:3072 sc1 lds\n\t" \
    "v_mfma_f32_16x16x32_bf16 v[196:199], v[132:135], v[20:23], v[196:199]\n\t" \
    "ds_read_b128 v[44:47], %[xp] offset:55296\n\t" \
    "v_mfma_f32_16x16x32_bf16 v[200:203], v[140:143], v[20:23], v[200:203]\n\t" \
    "ds_read_b128 v[48:51], %[xp] offset:56320\n\t" \
    "v_mfma_f32_16x16x32_bf16 v[204:207], v[132:135], v[24:27], v[204:207]\n\t" \
    "s_waitcnt lgkmcnt(4)\n\t" \
    "v_mfma_f32_16x16x32_bf16 v[208:211], v[140:143], v[24:27], v[208:211]\n\t" \
    "v_mfma_f32_16x16x32_bf16 v[196:199], v[136:139], v[28:31], v[196:199]\n\t" \
    "ds_read_b128 v[20:23], %[xp] offset:57344\n\t" \
    "v_mfma_f32_16x16x32_bf16 v[200:203], v[144:147], v[28:31], v[200:203]\n\t" \
    "ds_read_b128 v[24:27], %[xp] offset:58368\n\t" \
    "v_mfma_f32_16x16x32_bf16 v[204:207], v[136:139], v[32:35], v[204:207]\n\t" \
    "s_waitcnt lgkmcnt(4)\n\t" \
    "v_mfma_f32_16x16x32_bf16 v[208:211], v[144:147], v[32:35], v[208:211]\n\t" \
    "v_mfma_f32_16x16x32_bf16 v[196:199], v[148:151], v[36:39], v[196:199]\n\t" \
    "ds_read_b128 v[28:31], %[xp] offset:59392\n\t" \
    "v_mfma_f32_16x16x32_bf16 v[200:203], v[156:159], v[36:39], v[200:203]\n\t" \
    "ds_read_b128 v[32:35], %[xp] offset:60416\n\t" \
    "v_mfma_f32_16x16x32_bf16 v[204:207], v[148:151], v[40:43], v[204:207]\n\t" \
    "s_waitcnt lgkmcnt(4)\n\t" \
    "v_mfma_f32_16x16x32_bf16 v[208:211], v[156:159], v[40:43], v[208:211]\n\t" \
    "v_mfma_f32_16x16x32_bf16 v[196:199], v[152:155], v[44:47], v[196:199]\n\t" \
    "ds_read_b128 v[36:39], %[xp] offset:61440\n\t" \
    "v_mfma_f32_16x16x32_bf16 v[200:203], v[160:163], v[44:47], v[200:203]\n\t" \
    "ds_read_b128 v[40:43], %[xp] offset:62464\n\t" \
    "v_mfma_f32_16x16x32_bf16 v[204:207], v[152:155], v[48:51], v[204:207]\n\t" \
    "s_waitcnt lgkmcnt(4)\n\t" \
    "v_mfma_f32_16x16x32_bf16 v[208:211], v[160:163], v[48:51], v[208:211]\n\t" \
    "v_mfma_f32_16x16x32_bf16 v[196:199], v[164:167], v[20:23], v[196:199]\n\t" \
    "ds_read_b128 v[44:47], %[xp] offset:63488\n\t" \
    "v_mfma_f32_16x16x32_bf16 v[200:203], v[172:175], v[20:23], v[200:203]\n\t" \
    "ds_read_b128 v[48:51], %[xp] offset:64512\n\t" \
    "v_mfma_f32_16x16x32_bf16 v[204:207], v[164:167], v[24:27], v[204:207]\n\t" \
    "s_waitcnt lgkmcnt(4)\n\t" \
    "v_mfma_f32_16x16x32_bf16 v[208:211], v[172:175], v[24:27], v[208:211]\n\t" \
    "v_mfma_f32_16x16x32_bf16 v[196:199], v[168:171], v[28:31], v[196:199]\n\t" \
    "ds_read_b128 v[20:23], %[xp] offset:0\n\t" \
    "v_mfma_f32_16x16x32_bf16 v[200:203], v[176:179], v[28:31], v[200:203]\n\t" \
    "ds_read_b128 v[24:27], %[xp] offset:1024\n\t" \
    "v_mfma_f32_16x16x32_bf16 v[204:207], v[168:171], v[32:35], v[204:207]\n\t" \
    "s_waitcnt lgkmcnt(4)\n\t" \
    "v_mfma_f32_16x16x32_bf16 v[208:211], v[176:179], v[32:35], v[208:211]\n\t" \
    "v_mfma_f32_16x16x32_bf16 v[196:199], v[180:183], v[36:39], v[196:199]\n\t" \
    "ds_read_b128 v[28:31], %[xp] offset:2048\n\t" \
    "v_mfma_f32_16x16x32_bf16 v[200:203], v[188:191], v[36:39], v[200:203]\n\t" \
    "ds_read_b128 v[32:35], %[xp] offset:3072\n\t" \
    "v_mfma_f32_16x16x32_bf16 v[204:207], v[180:183], v[40:43], v[204:207]\n\t" \
    "s_waitcnt lgkmcnt(4)\n\t" \
    "v_mfma_f32_16x16x32_bf16 v[208:211], v[188:191], v[40:43], v[208:211]\n\t" \
    "v_mfma_f32_16x16x32_bf16 v[196:199], v[184:187], v[44:47], v[196:199]\n\t" \
    "ds_read_b128 v[36:39], %[xp] offset:4096\n\t" \
    "v_mfma_f32_16x16x32_bf16 v[200:203], v[192:195], v[44:47], v[200:203]\n\t" \
    "ds_read_b128 v[40:43], %[xp] offset:5120\n\t" \
    "v_mfma_f32_16x16x32_bf16 v[204:207], v[184:187], v[48:51], v[204:207]\n\t" \
    "s_waitcnt lgkmcnt(4)\n\t" \
    "v_mfma_f32_16x16x32_bf16 v[208:211], v[192:195], v[48:51], v[208:211]\n\t" \
    "s_add_u32 %[sx], %[sx], 65536\n\t" \
    "s_add_u32 %[t], %[t], 1\n\t" \
    "s_waitcnt vmcnt(4)\n\t" \
    "s_barrier\n\t" \
    "s_cmp_lt_u32 %[t], %[te]\n\t" \
    "s_cbranch_scc1 L0_%=\n\t" \
    "s_cmp_eq_u32 %[fin], %[te]\n\t" \
    "s_cbranch_scc0 E_%=\n\t" \
    "s_nop 4\n\t" \
    "ds_write_b128 %[pa], v[196:199] offset:0\n\t" \
    "ds_write_b128 %[pa], v[200:203] offset:1024\n\t" \
    "ds_write_b128 %[pa], v[204:207] offset:2048\n\t" \
    "ds_write_b128 %[pa], v[208:211] offset:3072\n\t" \
    "s_waitcnt lgkmcnt(0)\n\t" \
    "s_barrier\n\t" \
    "s_branch E_%=\n\t" \
    "L1_%=:\n\t" \
    "s_setprio 1\n\t" \
    "s_add_u32 m0, %[sd], 49152\n\t" \
    "s_add_u32 %[a], %[sx], 81920\n\t" \
    "v_mfma_f32_16x16x32_bf16 v[0:3], v[68:71], v[20:23], 0\n\t" \
    "ds_read_b128 v[44:47], %[xp] offset:6144\n\t" \
    "v_mfma_f32_16x16x32_bf16 v[4:7], v[76:79], v[20:23], 0\n\t" \
    "ds_read_b128 v[48:51], %[xp] offset:7168\n\t" \
    "v_mfma_f32_16x16x32_bf16 v[8:11], v[68:71], v[24:27], 0\n\t" \
    "s_waitcnt lgkmcnt(4)\n\t" \
    "v_mfma_f32_16x16x32_bf16 v[12:15], v[76:79], v[24:27], 0\n\t" \
    "v_mfma_f32_16x16x32_bf16 v[0:3], v[72:75], v[28:31], v[0:3]\n\t" \
    "ds_read_b128 v[20:23], %[xp] offset:8192\n\t" \
    "v_mfma_f32_16x16x32_bf16 v[4:7], v[80:83], v[28:31], v[4:7]\n\t" \
    "ds_read_b128 v[24:27], %[xp] offset:9216\n\t" \
    "v_mfma_f32_16x16x32_bf16 v[8:11], v[72:75], v[32:35], v[8:11]\n\t" \
    "s_waitcnt lgkmcnt(4)\n\t" \
    "v_mfma_f32_16x16x32_bf16 v[12:15], v[80:83], v[32:35], v[12:15]\n\t" \
    "v_mfma_f32_16x16x32_bf16 v[0:3], v[84:87], v[36:39], v[0:3]\n\t" \
    "ds_read_b128 v[28:31], %[xp] offset:10240\n\t" \
    "v_mfma_f32_16x16x32_bf16 v[4:7], v[92:95], v[36:39], v[4:7]\n\t" \
    "ds_read_b128 v[32:35], %[xp] offset:11264\n\t" \
    "v_mfma_f32_16x16x32_bf16 v[8:11], v[84:87], v[40:43], v[8:11]\n\t" \
    "s_waitcnt lgkmcnt(4)\n\t" \
    "v_mfma_f32_16x16x32_bf16 v[12:15], v[92:95], v[40:43], v[12:15]\n\t" \
    "v_mfma_f32_16x16x32_bf16 v[0:3], v[88:91], v[44:47], v[0:3]\n\t" \
    "ds_read_b128 v[36:39], %[xp] offset:12288\n\t" \
    "v_mfma_f32_16x16x32_bf16 v[4:7], v[96:99], v[44:47], v[4:7]\n\t" \
    "ds_read_b128 v[40:43], %[xp] offset:13312\n\t" \
    "v_mfma_f32_16x16x32_bf16 v[8:11], v[88:91], v[48:51], v[8:11]\n\t" \
    "s_waitcnt lgkmcnt(4)\n\t" \
    "v_mfma_f32_16x16x32_bf16 v[12:15], v[96:99], v[48:51], v[12:15]\n\t" \
    "v_mfma_f32_16x16x32_bf16 v[0:3], v[100:103], v[20:23], v[0:3]\n\t" \
    "ds_read_b128 v[44:47], %[xp] offset:14336\n\t" \
    "v_mfma_f32_16x16x32_bf16 v[4:7], v[108:111], v[20:23], v[4:7]\n\t" \
    "ds_read_b128 v[48:51], %[xp] offset:15360\n\t" \
    "v_mfma_f32_16x16x32_bf16 v[8:11], v[100:103], v[24:27], v[8:11]\n\t" \
    "s_waitcnt lgkmcnt(4)\n\t" \
    "v_mfma_f32_16x16x32_bf16 v[12:15], v[108:111], v[24:27], v[12:15]\n\t" \
    "buffer_load_dwordx4 %[l16], %[rs], %[a] offen offset:0 sc1 lds\n\t" \
    "v_mfma_f32_16x16x32_bf16 v[0:3], v[104:107], v[28:31], v[0:3]\n\t" \
    "ds_read_b128 v[20:23], %[xp] offset:16384\n\t" \
    "v_mfma_f32_16x16x32_bf16 v[4:7], v[112:115], v[28:31], v[4:7]\n\t" \
    "ds_read_b128 v[24:27], %[xp] offset:17408\n\t" \
    "v_mfma_f32_16x16x32_bf16 v[8:11], v[104:107], v[32:35], v[8:11]\n\t" \
    "s_waitcnt lgkmcnt(4)\n\t" \
    "v_mfma_f32_16x16x32_bf16 v[12:15], v[112:115], v[32:35], v[12:15]\n\t" \
    "buffer_load_dwordx4 %[l16], %[rs], %[a] offen offset:1024 sc1 lds\n\t" \
    "v_mfma_f32_16x16x32_bf16 v[0:3], v[116:119], v[36:39], v[0:3]\n\t" \
    "ds_read_b128 v[28:31], %[xp] offset:18432\n\t" \
    "v_mfma_f32_16x16x32_bf16 v[4:7], v[124:127], v[36:39], v[4:7]\n\t" \
    "ds_read_b128 v[32:35], %[xp] offset:19456\n\t" \
    "v_mfma_f32_16x16x32_bf16 v[8:11], v[116:119], v[40:43], v[8:11]\n\t" \
    "s_waitcnt lgkmcnt(4)\n\t" \
    "v_mfma_f32_16x16x32_bf16 v[12:15], v[124:127], v[40:43], v[12:15]\n\t" \
    "buffer_load_dwordx4 %[l16], %[rs], %[a] offen offset:2048 sc1 lds\n\t" \
    "v_mfma_f32_16x16x32_bf16 v[0:3], v[120:123], v[44:47], v[0:3]\n\t" \
    "ds_read_b128 v[36:39], %[xp] offset:20480\n\t" \
    "v_mfma_f32_16x16x32_bf16 v[4:7], v[128:131], v[44:47], v[4:7]\n\t" \
    "ds_read_b128 v[40:43], %[xp] offset:21504\n\t" \
    "v_mfma_f32_16x16x32_bf16 v[8:11], v[120:123], v[48:51], v[8:11]\n\t" \
    "s_waitcnt lgkmcnt(4)\n\t" \
    "v_mfma_f32_16x16x32_bf16 v[12:15], v[128:131], v[48:51], v[12:15]\n\t" \
    "buffer_load_dwordx4 %[l16], %[rs], %[a] offen offset:3072 sc1 lds\n\t" \
    "s_waitcnt vmcnt(4)\n\t" \
    "s_barrier\n\t" \
    "s_setprio 1\n\t" \
    "s_add_u32 m0, %[sd], 0\n\t" \
    "s_add_u32 %[a], %[sx], 131072\n\t" \
    "s_add_u32 %[b], %[oo], 0x1000\n\t" \
    "s_cmp_lg_u32 %[t], 1\n\t" \
    "s_cselect_b64 %[em], -1, 0\n\t" \
    "v_mfma_f32_16x16x32_bf16 v[0:3], v[132:135], v[20:23], v[0:3]\n\t" \
    "ds_read_b128 v[44:47], %[xp] offset:22528\n\t" \
    "v_mfma_f32_16x16x32_bf16 v[4:7], v[140:143], v[20:23], v[4:7]\n\t" \
    "ds_read_b128 v[48:51], %[xp] offset:23552\n\t" \
    "v_mfma_f32_16x16x32_bf16 v[8:11], v[132:135], v[24:27], v[8:11]\n\t" \
    "ds_read_b128 v[52:55], %[pa] offset:0\n\t" \
    "s_waitcnt lgkmcnt(5)\n\t" \
    "v_mfma_f32_16x16x32_bf16 v[12:15], v[140:143], v[24:27], v[12:15]\n\t" \
    "buffer_load_dwordx4 %[l16], %[rs], %[a] offen offset:0 sc1 lds\n\t" \
    "v_mfma_f32_16x16x32_bf16 v[0:3], v[136:139], v[28:31], v[0:3]\n\t" \
    "ds_read_b128 v[20:23], %[xp] offset:24576\n\t" \
    "v_mfma_f32_16x16x32_bf16 v[4:7], v[144:147], v[28:31], v[4:7]\n\t" \
    "ds_read_b128 v[24:27], %[xp] offset:25600\n\t" \
    "v_mfma_f32_16x16x32_bf16 v[8:11], v[136:139], v[32:35], v[8:11]\n\t" \
    "ds_read_b128 v[56:59], %[pa] offset:1024\n\t" \
    "s_waitcnt lgkmcnt(6)\n\t" \
    "v_mfma_f32_16x16x32_bf16 v[12:15], v[144:147], v[32:35], v[12:15]\n\t" \
    "buffer_load_dwordx4 %[l16], %[rs], %[a] offen offset:1024 sc1 lds\n\t" \
    "v_mfma_f32_16x16x32_bf16 v[0:3], v[148:151], v[36:39], v[0:3]\n\t" \
    "ds_read_b128 v[28:31], %[xp] offset:26624\n\t" \
    "v_mfma_f32_16x16x32_bf16 v[4:7], v[156:159], v[36:39], v[4:7]\n\t" \
    "ds_read_b128 v[32:35], %[xp] offset:27648\n\t" \
    "v_mfma_f32_16x16x32_bf16 v[8:11], v[148:151], v[40:43], v[8:11]\n\t" \
    "ds_read_b128 v[60:63], %[pa] offset:2048\n\t" \
    "s_waitcnt lgkmcnt(7)\n\t" \
    "v_mfma_f32_16x16x32_bf16 v[12:15], v[156:159], v[40:43], v[12:15]\n\t" \
    "buffer_load_dwordx4 %[l16], %[rs], %[a] offen offset:2048 sc1 lds\n\t" \
    "v_mfma_f32_16x16x32_bf16 v[0:3], v[152:155], v[44:47], v[0:3]\n\t" \
    "ds_read_b128 v[36:39], %[xp] offset:28672\n\t" \
    "s_waitcnt lgkmcnt(7)\n\t" \
    "v_add_f32 v196, v196, v52\n\t" \
    "v_mfma_f32_16x16x32_bf16 v[4:7], v[160:163], v[44:47], v[4:7]\n\t" \
    "ds_read_b128 v[40:43], %[xp] offset:29696\n\t" \
    "v_add_f32 v197, v197, v53\n\t" \
    "v_mfma_f32_16x16x32_bf16 v[8:11], v[152:155], v[48:51], v[8:11]\n\t" \
    "ds_read_b128 v[64:67], %[pa] offset:3072\n\t" \
    "s_waitcnt lgkmcnt(7)\n\t" \
    "v_mfma_f32_16x16x32_bf16 v[12:15], v[160:163], v[48:51], v[12:15]\n\t" \
    "buffer_load_dwordx4 %[l16], %[rs], %[a] offen offset:3072 sc1 lds\n\t" \
    "v_add_f32 v198, v198, v54\n\t" \
    "v_add_f32 v199, v199, v55\n\t" \
    "v_mfma_f32_16x16x32_bf16 v[0:3], v[164:167], v[20:23], v[0:3]\n\t" \
    "ds_read_b128 v[44:47], %[xp] offset:30720\n\t" \
    "s_waitcnt lgkmcnt(7)\n\t" \
    "v_add_f32 v200, v200, v56\n\t" \
    "v_mfma_f32_16x16x32_bf16 v[4:7], v[172:175], v[20:23], v[4:7]\n\t" \
    "ds_read_b128 v[48:51], %[xp] offset:31744\n\t" \
    "v_add_f32 v201, v201, v57\n\t" \
    "v_mfma_f32_16x16x32_bf16 v[8:11], v[164:167], v[24:27], v[8:11]\n\t" \
    "v_add_f32 v202, v202, v58\n\t" \
    "s_waitcnt lgkmcnt(6)\n\t" \
    "v_mfma_f32_16x16x32_bf16 v[12:15], v[172:175], v[24:27], v[12:15]\n\t" \
    "v_add_f32 v203, v203, v59\n\t" \
    "s_waitcnt lgkmcnt(5)\n\t" \
    "v_add_f32 v204, v204, v60\n\t" \
    "v_add_f32 v205, v205, v61\n\t" \
    "v_mfma_f32_16x16x32_bf16 v[0:3], v[168:171], v[28:31], v[0:3]\n\t" \
    "ds_read_b128 v[20:23], %[xp] offset:32768\n\t" \
    "v_add_f32 v206, v206, v62\n\t" \
    "v_mfma_f32_16x16x32_bf16 v[4:7], v[176:179], v[28:31], v[4:7]\n\t" \
    "ds_read_b128 v[24:27], %[xp] offset:33792\n\t" \
    "v_add_f32 v207, v207, v63\n\t" \
    "v_mfma_f32_16x16x32_bf16 v[8:11], v[168:171], v[32:35], v[8:11]\n\t" \
    "s_waitcnt lgkmcnt(4)\n\t" \
    "v_add_f32 v208, v208, v64\n\t" \
    "v_mfma_f32_16x16x32_bf16 v[12:15], v[176:179], v[32:35], v[12:15]\n\t" \
    "v_add_f32 v209, v209, v65\n\t" \
    "v_add_f32 v210, v210, v66\n\t" \
    "v_add_f32 v211, v211, v67\n\t" \
    "v_mfma_f32_16x16x32_bf16 v[0:3], v[180:183], v[36:39], v[0:3]\n\t" \
    "ds_read_b128 v[28:31], %[xp] offset:34816\n\t" \
    "v_cvt_pk_bf16_f32 v52, v196, v200\n\t" \
    "v_mfma_f32_16x16x32_bf16 v[4:7], v[188:191], v[36:39], v[4:7]\n\t" \
    "ds_read_b128 v[32:35], %[xp] offset:35840\n\t" \
    "v_cvt_pk_bf16_f32 v53, v197, v201\n\t" \
    "v_mfma_f32_16x16x32_bf16 v[8:11], v[180:183], v[40:43], v[8:11]\n\t" \
    "v_cvt_pk_bf16_f32 v54, v198, v202\n\t" \
    "s_waitcnt lgkmcnt(4)\n\t" \
    "v_mfma_f32_16x16x32_bf16 v[12:15], v[188:191], v[40:43], v[12:15]\n\t" \
    "v_cvt_pk_bf16_f32 v55, v199, v203\n\t" \
    "v_cvt_pk_bf16_f32 v56, v204, v208\n\t" \
    "v_cvt_pk_bf16_f32 v57, v205, v209\n\t" \
    "v_mfma_f32_16x16x32_bf16 v[0:3], v[184:187], v[44:47], v[0:3]\n\t" \
    "ds_read_b128 v[36:39], %[xp] offset:36864\n\t" \
    "v_cvt_pk_bf16_f32 v58, v206, v210\n\t" \
    "v_mfma_f32_16x16x32_bf16 v[4:7], v[192:195], v[44:47], v[4:7]\n\t" \
    "ds_read_b128 v[40:43], %[xp] offset:37888\n\t" \
    "v_cvt_pk_bf16_f32 v59, v207, v211\n\t" \
    "v_mfma_f32_16x16x32_bf16 v[8:11], v[184:187], v[48:51], v[8:11]\n\t" \
    "s_waitcnt lgkmcnt(4)\n\t" \
    "v_mfma_f32_16x16x32_bf16 v[12:15], v[192:195], v[48:51], v[12:15]\n\t" \
    "s_mov_b64 exec, %[em]\n\t" \
    "buffer_store_dwordx4 v[52:55], %[l16], %[rs], %[oo] offen sc1\n\t" \
    "buffer_store_dwordx4 v[56:59], %[l16], %[rs], %[b] offen sc1\n\t" \
    "s_mov_b64 exec, -1\n\t" \
    "s_add_u32 %[sx], %[sx], 65536\n\t" \
    "s_add_u32 %[t], %[t], 1\n\t" \
    "s_add_u32 %[oo], %[oo], 262144\n\t" \
    "s_waitcnt vmcnt(6)\n\t" \
    "s_cmp_gt_u32 %[t], " STR(SQUICK) " + 1\n\t" \
    "s_cbranch_scc1 B0_%=\n\t" \
    "s_waitcnt vmcnt(0)\n\t" \
    "B0_%=:\n\t" \
    "s_barrier\n\t" \
    "s_setprio 1\n\t" \
    "s_add_u32 m0, %[sd], 16384\n\t" \
    "s_add_u32 %[a], %[sx], 81920\n\t" \
    "v_mfma_f32_16x16x32_bf16 v[196:199], v[68:71], v[20:23], 0\n\t" \
    "ds_read_b128 v[44:47], %[xp] offset:38912\n\t" \
    "v_mfma_f32_16x16x32_bf16 v[200:203], v[76:79], v[20:23], 0\n\t" \
    "ds_read_b128 v[48:51], %[xp] offset:39936\n\t" \
    "v_mfma_f32_16x16x32_bf16 v[204:207], v[68:71], v[24:27], 0\n\t" \
    "s_waitcnt lgkmcnt(4)\n\t" \
    "v_mfma_f32_16x16x32_bf16 v[208:211], v[76:79], v[24:27], 0\n\t" \
    "v_mfma_f32_16x16x32_bf16 v[196:199], v[72:75], v[28:31], v[196:199]\n\t" \
    "ds_read_b128 v[20:23], %[xp] offset:40960\n\t" \
    "v_mfma_f32_16x16x32_bf16 v[200:203], v[80:83], v[28:31], v[200:203]\n\t" \
    "ds_read_b128 v[24:27], %[xp] offset:41984\n\t" \
    "v_mfma_f32_16x16x32_bf16 v[204:207], v[72:75], v[32:35], v[204:207]\n\t" \
    "s_waitcnt lgkmcnt(4)\n\t" \
    "v_mfma_f32_16x16x32_bf16 v[208:211], v[80:83], v[32:35], v[208:211]\n\t" \
    "v_mfma_f32_16x16x32_bf16 v[196:199], v[84:87], v[36:39], v[196:199]\n\t" \
    "ds_read_b128 v[28:31], %[xp] offset:43008\n\t" \
    "v_mfma_f32_16x16x32_bf16 v[200:203], v[92:95], v[36:39], v[200:203]\n\t" \
    "ds_read_b128 v[32:35], %[xp] offset:44032\n\t" \
    "v_mfma_f32_16x16x32_bf16 v[204:207], v[84:87], v[40:43], v[204:207]\n\t" \
    "s_waitcnt lgkmcnt(4)\n\t" \
    "v_mfma_f32_16x16x32_bf16 v[208:211], v[92:95], v[40:43], v[208:211]\n\t" \
    "v_mfma_f32_16x16x32_bf16 v[196:199], v[88:91], v[44:47], v[196:199]\n\t" \
    "ds_read_b128 v[36:39], %[xp] offset:45056\n\t" \
    "v_mfma_f32_16x16x32_bf16 v[200:203], v[96:99], v[44:47], v[200:203]\n\t" \
    "ds_read_b128 v[40:43], %[xp] offset:46080\n\t" \
    "v_mfma_f32_16x16x32_bf16 v[204:207], v[88:91], v[48:51], v[204:207]\n\t" \
    "s_waitcnt lgkmcnt(4)\n\t" \
    "v_mfma_f32_16x16x32_bf16 v[208:211], v[96:99], v[48:51], v[208:211]\n\t" \
    "v_mfma_f32_16x16x32_bf16 v[196:199], v[100:103], v[20:23], v[196:199]\n\t" \
    "ds_read_b128 v[44:47], %[xp] offset:47104\n\t" \
    "v_mfma_f32_16x16x32_bf16 v[200:203], v[108:111], v[20:23], v[200:203]\n\t" \
    "ds_read_b128 v[48:51], %[xp] offset:48128\n\t" \
    "v_mfma_f32_16x16x32_bf16 v[204:207], v[100:103], v[24:27], v[204:207]\n\t" \
    "s_waitcnt lgkmcnt(4)\n\t" \
    "v_mfma_f32_16x16x32_bf16 v[208:211], v[108:111], v[24:27], v[208:211]\n\t" \
    "buffer_load_dwordx4 %[l16], %[rs], %[a] offen offset:0 sc1 lds\n\t" \
    "v_mfma_f32_16x16x32_bf16 v[196:199], v[104:107], v[28:31], v[196:199]\n\t" \
    "ds_read_b128 v[20:23], %[xp] offset:49152\n\t" \
    "v_mfma_f32_16x16x32_bf16 v[200:203], v[112:115], v[28:31], v[200:203]\n\t" \
    "ds_read_b128 v[24:27], %[xp] offset:50176\n\t" \
    "v_mfma_f32_16x16x32_bf16 v[204:207], v[104:107], v[32:35], v[204:207]\n\t" \
    "s_waitcnt lgkmcnt(4)\n\t" \
    "v_mfma_f32_16x16x32_bf16 v[208:211], v[112:115], v[32:35], v[208:211]\n\t" \
    "buffer_load_dwordx4 %[l16], %[rs], %[a] offen offset:1024 sc1 lds\n\t" \
    "v_mfma_f32_16x16x32_bf16 v[196:199], v[116:119], v[36:39], v[196:199]\n\t" \
    "ds_read_b128 v[28:31], %[xp] offset:51200\n\t" \
    "v_mfma_f32_16x16x32_bf16 v[200:203], v[124:127], v[36:39], v[200:203]\n\t" \
    "ds_read_b128 v[32:35], %[xp] offset:52224\n\t" \
    "v_mfma_f32_16x16x32_bf16 v[204:207], v[116:119], v[40:43], v[204:207]\n\t" \
    "s_waitcnt lgkmcnt(4)\n\t" \
    "v_mfma_f32_16x16x32_bf16 v[208:211], v[124:127], v[40:43], v[208:211]\n\t" \
    "buffer_load_dwordx4 %[l16], %[rs], %[a] offen offset:2048 sc1 lds\n\t" \
    "v_mfma_f32_16x16x32_bf16 v[196:199], v[120:123], v[44:47], v[196:199]\n\t" \
    "ds_read_b128 v[36:39], %[xp] offset:53248\n\t" \
    "v_mfma_f32_16x16x32_bf16 v[200:203], v[128:131], v[44:47], v[200:203]\n\t" \
    "ds_read_b128 v[40:43], %[xp] offset:54272\n\t" \
    "v_mfma_f32_16x16x32_bf16 v[204:207], v[120:123], v[48:51], v[204:207]\n\t" \
    "s_waitcnt lgkmcnt(4)\n\t" \
    "v_mfma_f32_16x16x32_bf16 v[208:211], v[128:131], v[48:51], v[208:211]\n\t" \
    "buffer_load_dwordx4 %[l16], %[rs], %[a] offen offset:3072 sc1 lds\n\t" \
    "s_waitcnt vmcnt(4)\n\t" \
    "s_barrier\n\t" \
    "s_setprio 1\n\t" \
    "s_add_u32 m0, %[sd], 32768\n\t" \
    "s_add_u32 %[a], %[sx], 131072\n\t" \
    "s_add_u32 %[b], %[oo], 0x1000\n\t" \
    "s_cmp_lg_u32 %[t], 1\n\t" \
    "s_cselect_b64 %[em], -1, 0\n\t" \
    "v_mfma_f32_16x16x32_bf16 v[196:199], v[132:135], v[20:23], v[196:199]\n\t" \
    "ds_read_b128 v[44:47], %[xp] offset:55296\n\t" \
    "v_mfma_f32_16x16x32_bf16 v[200:203], v[140:143], v[20:23], v[200:203]\n\t" \
    "ds_read_b128 v[48:51], %[xp] offset:56320\n\t" \
    "v_mfma_f32_16x16x32_bf16 v[204:207], v[132:135], v[24:27], v[204:207]\n\t" \
    "ds_read_b128 v[52:55], %[pa] offset:0\n\t" \
    "s_waitcnt lgkmcnt(5)\n\t" \
    "v_mfma_f32_16x16x32_bf16 v[208:211], v[140:143], v[24:27], v[208:211]\n\t" \
    "buffer_load_dwordx4 %[l16], %[rs], %[a] offen offset:0 sc1 lds\n\t" \
    "v_mfma_f32_16x16x32_bf16 v[196:199], v[136:139], v[28:31], v[196:199]\n\t" \
    "ds_read_b128 v[20:23], %[xp] offset:57344\n\t" \
    "v_mfma_f32_16x16x32_bf16 v[200:203], v[144:147], v[28:31], v[200:203]\n\t" \
    "ds_read_b128 v[24:27], %[xp] offset:58368\n\t" \
    "v_mfma_f32_16x16x32_bf16 v[204:207], v[136:139], v[32:35], v[204:207]\n\t" \
    "ds_read_b128 v[56:59], %[pa] offset:1024\n\t" \
    "s_waitcnt lgkmcnt(6)\n\t" \
    "v_mfma_f32_16x16x32_bf16 v[208:211], v[144:147], v[32:35], v[208:211]\n\t" \
    "buffer_load_dwordx4 %[l16], %[rs], %[a] offen offset:1024 sc1 lds\n\t" \
    "v_mfma_f32_16x16x32_bf16 v[196:199], v[148:151], v[36:39], v[196:199]\n\t" \
    "ds_read_b128 v[28:31], %[xp] offset:59392\n\t" \
    "v_mfma_f32_16x16x32_bf16 v[200:203], v[156:159], v[36:39], v[200:203]\n\t" \
    "ds_read_b128 v[32:35], %[xp] offset:60416\n\t" \
    "v_mfma_f32_16x16x32_bf16 v[204:207], v[148:151], v[40:43], v[204:207]\n\t" \
    "ds_read_b128 v[60:63], %[pa] offset:2048\n\t" \
    "s_waitcnt lgkmcnt(7)\n\t" \
    "v_mfma_f32_16x16x32_bf16 v[208:211], v[156:159], v[40:43], v[208:211]\n\t" \
    "buffer_load_dwordx4 %[l16], %[rs], %[a] offen offset:2048 sc1 lds\n\t" \
    "v_mfma_f32_16x16x32_bf16 v[196:199], v[152:155], v[44:47], v[196:199]\n\t" \
    "ds_read_b128 v[36:39], %[xp] offset:61440\n\t" \
    "s_waitcnt lgkmcnt(7)\n\t" \
    "v_add_f32 v0, v0, v52\n\t" \
    "v_mfma_f32_16x16x32_bf16 v[200:203], v[160:163], v[44:47], v[200:203]\n\t" \
    "ds_read_b128 v[40:43], %[xp] offset:62464\n\t" \
    "v_add_f32 v1, v1, v53\n\t" \
    "v_mfma_f32_16x16x32_bf16 v[204:207], v[152:155], v[48:51], v[204:207]\n\t" \
    "ds_read_b128 v[64:67], %[pa] offset:3072\n\t" \
    "s_waitcnt lgkmcnt(7)\n\t" \
    "v_mfma_f32_16x16x32_bf16 v[208:211], v[160:163], v[48:51], v[208:211]\n\t" \
    "buffer_load_dwordx4 %[l16], %[rs], %[a] offen offset:3072 sc1 lds\n\t" \
    "v_add_f32 v2, v2, v54\n\t" \
    "v_add_f32 v3, v3, v55\n\t" \
    "v_mfma_f32_16x16x32_bf16 v[196:199], v[164:167], v[20:23], v[196:199]\n\t" \
    "ds_read_b128 v[44:47], %[xp] offset:63488\n\t" \
    "s_waitcnt lgkmcnt(7)\n\t" \
    "v_add_f32 v4, v4, v56\n\t" \
    "v_mfma_f32_16x16x32_bf16 v[200:203], v[172:175], v[20:23], v[200:203]\n\t" \
    "ds_read_b128 v[48:51], %[xp] offset:64512\n\t" \
    "v_add_f32 v5, v5, v57\n\t" \
    "v_mfma_f32_16x16x32_bf16 v[204:207], v[164:167], v[24:27], v[204:207]\n\t" \
    "v_add_f32 v6, v6, v58\n\t" \
    "s_waitcnt lgkmcnt(6)\n\t" \
    "v_mfma_f32_16x16x32_bf16 v[208:211], v[172:175], v[24:27], v[208:211]\n\t" \
    "v_add_f32 v7, v7, v59\n\t" \
    "s_waitcnt lgkmcnt(5)\n\t" \
    "v_add_f32 v8, v8, v60\n\t" \
    "v_add_f32 v9, v9, v61\n\t" \
    "v_mfma_f32_16x16x32_bf16 v[196:199], v[168:171], v[28:31], v[196:199]\n\t" \
    "ds_read_b128 v[20:23], %[xp] offset:0\n\t" \
    "v_add_f32 v10, v10, v62\n\t" \
    "v_mfma_f32_16x16x32_bf16 v[200:203], v[176:179], v[28:31], v[200:203]\n\t" \
    "ds_read_b128 v[24:27], %[xp] offset:1024\n\t" \
    "v_add_f32 v11, v11, v63\n\t" \
    "v_mfma_f32_16x16x32_bf16 v[204:207], v[168:171], v[32:35], v[204:207]\n\t" \
    "s_waitcnt lgkmcnt(4)\n\t" \
    "v_add_f32 v12, v12, v64\n\t" \
    "v_mfma_f32_16x16x32_bf16 v[208:211], v[176:179], v[32:35], v[208:211]\n\t" \
    "v_add_f32 v13, v13, v65\n\t" \
    "v_add_f32 v14, v14, v66\n\t" \
    "v_add_f32 v15, v15, v67\n\t" \
    "v_mfma_f32_16x16x32_bf16 v[196:199], v[180:183], v[36:39], v[196:199]\n\t" \
    "ds_read_b128 v[28:31], %[xp] offset:2048\n\t" \
    "v_cvt_pk_bf16_f32 v52, v0, v4\n\t" \
    "v_mfma_f32_16x16x32_bf16 v[200:203], v[188:191], v[36:39], v[200:203]\n\t" \
    "ds_read_b128 v[32:35], %[xp] offset:3072\n\t" \
    "v_cvt_pk_bf16_f32 v53, v1, v5\n\t" \
    "v_mfma_f32_16x16x32_bf16 v[204:207], v[180:183], v[40:43], v[204:207]\n\t" \
    "v_cvt_pk_bf16_f32 v54, v2, v6\n\t" \
    "s_waitcnt lgkmcnt(4)\n\t" \
    "v_mfma_f32_16x16x32_bf16 v[208:211], v[188:191], v[40:43], v[208:211]\n\t" \
    "v_cvt_pk_bf16_f32 v55, v3, v7\n\t" \
    "v_cvt_pk_bf16_f32 v56, v8, v12\n\t" \
    "v_cvt_pk_bf16_f32 v57, v9, v13\n\t" \
    "v_mfma_f32_16x16x32_bf16 v[196:199], v[184:187], v[44:47], v[196:199]\n\t" \
    "ds_read_b128 v[36:39], %[xp] offset:4096\n\t" \
    "v_cvt_pk_bf16_f32 v58, v10, v14\n\t" \
    "v_mfma_f32_16x16x32_bf16 v[200:203], v[192:195], v[44:47], v[200:203]\n\t" \
    "ds_read_b128 v[40:43], %[xp] offset:5120\n\t" \
    "v_cvt_pk_bf16_f32 v59, v11, v15\n\t" \
    "v_mfma_f32_16x16x32_bf16 v[204:207], v[184:187], v[48:51], v[204:207]\n\t" \
    "s_waitcnt lgkmcnt(4)\n\t" \
    "v_mfma_f32_16x16x32_bf16 v[208:211], v[192:195], v[48:51], v[208:211]\n\t" \
    "s_mov_b64 exec, %[em]\n\t" \
    "buffer_store_dwordx4 v[52:55], %[l16], %[rs], %[oo] offen sc1\n\t" \
    "buffer_store_dwordx4 v[56:59], %[l16], %[rs], %[b] offen sc1\n\t" \
    "s_mov_b64 exec, -1\n\t" \
    "s_add_u32 %[sx], %[sx], 65536\n\t" \
    "s_add_u32 %[t], %[t], 1\n\t" \
    "s_add_u32 %[oo], %[oo], 262144\n\t" \
    "s_waitcnt vmcnt(6)\n\t" \
    "s_cmp_gt_u32 %[t], " STR(SQUICK) " + 1\n\t" \
    "s_cbranch_scc1 B1_%=\n\t" \
    "s_waitcnt vmcnt(0)\n\t" \
    "B1_%=:\n\t" \
    "s_barrier\n\t" \
    "s_cmp_lt_u32 %[t], %[te]\n\t" \
    "s_cbranch_scc1 L1_%=\n\t" \
    "s_cmp_eq_u32 %[fin], %[te]\n\t" \
    "s_cbranch_scc0 E_%=\n\t" \
    "s_nop 4\n\t" \
    "s_barrier\n\t" \
    "ds_read_b128 v[52:55], %[pa] offset:0\n\t" \
    "ds_read_b128 v[56:59], %[pa] offset:1024\n\t" \
    "ds_read_b128 v[60:63], %[pa] offset:2048\n\t" \
    "ds_read_b128 v[64:67], %[pa] offset:3072\n\t" \
    "s_add_u32 %[b], %[oo], 0x1000\n\t" \
    "s_waitcnt lgkmcnt(0)\n\t" \
    "v_add_f32 v196, v196, v52\n\t" \
    "v_add_f32 v197, v197, v53\n\t" \
    "v_add_f32 v198, v198, v54\n\t" \
    "v_add_f32 v199, v199, v55\n\t" \
    "v_add_f32 v200, v200, v56\n\t" \
    "v_add_f32 v201, v201, v57\n\t" \
    "v_add_f32 v202, v202, v58\n\t" \
    "v_add_f32 v203, v203, v59\n\t" \
    "v_add_f32 v204, v204, v60\n\t" \
    "v_add_f32 v205, v205, v61\n\t" \
    "v_add_f32 v206, v206, v62\n\t" \
    "v_add_f32 v207, v207, v63\n\t" \
    "v_add_f32 v208, v208, v64\n\t" \
    "v_add_f32 v209, v209, v65\n\t" \
    "v_add_f32 v210, v210, v66\n\t" \
    "v_add_f32 v211, v211, v67\n\t" \
    "v_cvt_pk_bf16_f32 v52, v196, v200\n\t" \
    "v_cvt_pk_bf16_f32 v53, v197, v201\n\t" \
    "v_cvt_pk_bf16_f32 v54, v198, v202\n\t" \
    "v_cvt_pk_bf16_f32 v55, v199, v203\n\t" \
    "v_cvt_pk_bf16_f32 v56, v204, v208\n\t" \
    "v_cvt_pk_bf16_f32 v57, v205, v209\n\t" \
    "v_cvt_pk_bf16_f32 v58, v206, v210\n\t" \
    "v_cvt_pk_bf16_f32 v59, v207, v211\n\t" \
    "buffer_store_dwordx4 v[52:55], %[l16], %[rs], %[oo] offen sc1\n\t" \
    "buffer_store_dwordx4 v[56:59], %[l16], %[rs], %[b] offen sc1\n\t" \
    "E_%=:\n\t" \
    "s_setprio 0\n\t" \
    "s_waitcnt lgkmcnt(0)\n\t" \
    "s_nop 7\n\t" \
    : [t] "+s"(t), [sx] "+s"(sx), [oo] "+s"(oo), [a] "=&s"(tmp), [b] "=&s"(tmp2), [em] "=&s"(em), "+{v[20:27]}"(xf[0]), "+{v[28:35]}"(xf[1]), "+{v[36:43]}"(xf[2]), "+{v[196:203]}"(ac[0]), "+{v[204:211]}"(ac[1]) \
    : [te] "s"(TE_), [fin] "s"(FIN_), [kh] "s"(kh), [sd] "s"(sd), [rs] "s"(rs), [l16] "v"(l16), [xp] "v"(xp), [pa] "v"(pa), [fm] "s"(fm), [pm] "s"(pm), [fo] "s"(fo), [po] "s"(po), [pd] "s"(pd), [tag] "s"(P.tag), "{v[68:75]}"(w[0][0]), "{v[76:83]}"(w[0][1]), "{v[84:91]}"(w[1][0]), "{v[92:99]}"(w[1][1]), "{v[100:107]}"(w[2][0]), "{v[108:115]}"(w[2][1]), "{v[116:123]}"(w[3][0]), "{v[124:131]}"(w[3][1]), "{v[132:139]}"(w[4][0]), "{v[140:147]}"(w[4][1]), "{v[148:155]}"(w[5][0]), "{v[156:163]}"(w[5][1]), "{v[164:171]}"(w[6][0]), "{v[172:179]}"(w[6][1]), "{v[180:187]}"(w[7][0]), "{v[188:195]}"(w[7][1]) \
    : "memory", "scc", "v0", "v1", "v2", "v3", "v4", "v5", "v6", "v7", "v8", "v9", "v10", "v11", "v12", "v13", "v14", "v15", "v44", "v45", "v46", "v47", "v48", "v49", "v50", "v51", "v52", "v53", "v54", "v55", "v56", "v57", "v58", "v59", "v60", "v61", "v62", "v63", "v64", "v65", "v66", "v67")

#define S_LOOP_V(TE_, FIN_) \
  asm volatile( \
    "s_cmp_eq_u32 %[kh], 0\n\t" \
    "s_cbranch_scc0 L1_%=\n\t" \
    "L0_%=:\n\t" \
    "s_setprio 0\n\t" \
    "s_nop 3\n\t" \
    "ds_write_b128 %[pa], v[196:199] offset:0\n\t" \
    "ds_write_b128 %[pa], v[200:203] offset:1024\n\t" \
    "ds_write_b128 %[pa], v[204:207] offset:2048\n\t" \
    "ds_write_b128 %[pa], v[208:211] offset:3072\n\t" \
    "s_sub_u32 %[a], %[t], 1\n\t" \
    "v_mov_b32 v53, %[tag]\n\t" \
    "v_mov_b32 v52, %[a]\n\t" \
    "s_sub_u32 %[b], %[t], 2\n\t" \
    "s_cmp_lt_u32 %[b], " STR(SQUICK) "\n\t" \
    "s_cselect_b64 exec, %[fm], 0\n\t" \
    "buffer_store_dwordx2 v[52:53], %[l16], %[rs], %[fo] offen sc1\n\t" \
    "s_mov_b64 exec, -1\n\t" \
    "s_add_u32 m0, %[sd], 49152\n\t" \
    "s_add_u32 %[a], %[sx], 81920\n\t" \
    "s_waitcnt lgkmcnt(0)\n\t" \
    "buffer_load_dwordx4 %[l16], %[rs], %[a] offen offset:0 sc1 lds\n\t" \
    "buffer_load_dwordx4 %[l16], %[rs], %[a] offen offset:1024 sc1 lds\n\t" \
    "buffer_load_dwordx4 %[l16], %[rs], %[a] offen offset:2048 sc1 lds\n\t" \
    "buffer_load_dwordx4 %[l16], %[rs], %[a] offen offset:3072 sc1 lds\n\t" \
    "v_mfma_f32_16x16x32_bf16 v[0:3], v[20:23], v[68:71], 0\n\t" \
    "ds_read_b128 v[44:47], %[xp] offset:6144\n\t" \
    "v_mfma_f32_16x16x32_bf16 v[4:7], v[20:23], v[76:79], 0\n\t" \
    "ds_read_b128 v[48:51], %[xp] offset:7168\n\t" \
    "v_mfma_f32_16x16x32_bf16 v[8:11], v[24:27], v[68:71], 0\n\t" \
    "v_mfma_f32_16x16x32_bf16 v[12:15], v[24:27], v[76:79], 0\n\t" \
    "v_mfma_f32_16x16x32_bf16 v[0:3], v[28:31], v[72:75], v[0:3]\n\t" \
    "ds_read_b128 v[20:23], %[xp] offset:8192\n\t" \
    "v_mfma_f32_16x16x32_bf16 v[4:7], v[28:31], v[80:83], v[4:7]\n\t" \
    "ds_read_b128 v[24:27], %[xp] offset:9216\n\t" \
    "v_mfma_f32_16x16x32_bf16 v[8:11], v[32:35], v[72:75], v[8:11]\n\t" \
    "v_mfma_f32_16x16x32_bf16 v[12:15], v[32:35], v[80:83], v[12:15]\n\t" \
    "v_mfma_f32_16x16x32_bf16 v[0:3], v[36:39], v[84:87], v[0:3]\n\t" \
    "ds_read_b128 v[28:31], %[xp] offset:10240\n\t" \
    "v_mfma_f32_16x16x32_bf16 v[4:7], v[36:39], v[92:95], v[4:7]\n\t" \
    "ds_read_b128 v[32:35], %[xp] offset:11264\n\t" \
    "v_mfma_f32_16x16x32_bf16 v[8:11], v[40:43], v[84:87], v[8:11]\n\t" \
    "s_waitcnt lgkmcnt(4)\n\t" \
    "v_mfma_f32_16x16x32_bf16 v[12:15], v[40:43], v[92:95], v[12:15]\n\t" \
    "v_mfma_f32_16x16x32_bf16 v[0:3], v[44:47], v[88:91], v[0:3]\n\t" \
    "ds_read_b128 v[36:39], %[xp] offset:12288\n\t" \
    "v_mfma_f32_16x16x32_bf16 v[4:7], v[44:47], v[96:99], v[4:7]\n\t" \
    "ds_read_b128 v[40:43], %[xp] offset:13312\n\t" \
    "v_mfma_f32_16x16x32_bf16 v[8:11], v[48:51], v[88:91], v[8:11]\n\t" \
    "s_waitcnt lgkmcnt(4)\n\t" \
    "v_mfma_f32_16x16x32_bf16 v[12:15], v[48:51], v[96:99], v[12:15]\n\t" \
    "v_mfma_f32_16x16x32_bf16 v[0:3], v[20:23], v[100:103], v[0:3]\n\t" \
    "ds_read_b128 v[44:47], %[xp] offset:14336\n\t" \
    "v_mfma_f32_16x16x32_bf16 v[4:7], v[20:23], v[108:111], v[4:7]\n\t" \
    "ds_read_b128 v[48:51], %[xp] offset:15360\n\t" \
    "v_mfma_f32_16x16x32_bf16 v[8:11], v[24:27], v[100:103], v[8:11]\n\t" \
    "s_waitcnt lgkmcnt(4)\n\t" \
    "v_mfma_f32_16x16x32_bf16 v[12:15], v[24:27], v[108:111], v[12:15]\n\t" \
    "v_mfma_f32_16x16x32_bf16 v[0:3], v[28:31], v[104:107], v[0:3]\n\t" \
    "ds_read_b128 v[20:23], %[xp] offset:16384\n\t" \
    "v_mfma_f32_16x16x32_bf16 v[4:7], v[28:31], v[112:115], v[4:7]\n\t" \
    "ds_read_b128 v[24:27], %[xp] offset:17408\n\t" \
    "v_mfma_f32_16x16x32_bf16 v[8:11], v[32:35], v[104:107], v[8:11]\n\t" \
    "s_waitcnt lgkmcnt(4)\n\t" \
    "v_mfma_f32_16x16x32_bf16 v[12:15], v[32:35], v[112:115], v[12:15]\n\t" \
    "v_mfma_f32_16x16x32_bf16 v[0:3], v[36:39], v[116:119], v[0:3]\n\t" \
    "ds_read_b128 v[28:31], %[xp] offset:18432\n\t" \
    "v_mfma_f32_16x16x32_bf16 v[4:7], v[36:39], v[124:127], v[4:7]\n\t" \
    "ds_read_b128 v[32:35], %[xp] offset:19456\n\t" \
    "v_mfma_f32_16x16x32_bf16 v[8:11], v[40:43], v[116:119], v[8:11]\n\t" \
    "s_waitcnt lgkmcnt(4)\n\t" \
    "v_mfma_f32_16x16x32_bf16 v[12:15], v[40:43], v[124:127], v[12:15]\n\t" \
    "v_mfma_f32_16x16x32_bf16 v[0:3], v[44:47], v[120:123], v[0:3]\n\t" \
    "ds_read_b128 v[36:39], %[xp] offset:20480\n\t" \
    "v_mfma_f32_16x16x32_bf16 v[4:7], v[44:47], v[128:131], v[4:7]\n\t" \
    "ds_read_b128 v[40:43], %[xp] offset:21504\n\t" \
    "v_mfma_f32_16x16x32_bf16 v[8:11], v[48:51], v[120:123], v[8:11]\n\t" \
    "s_waitcnt lgkmcnt(4)\n\t" \
    "v_mfma_f32_16x16x32_bf16 v[12:15], v[48:51], v[128:131], v[12:15]\n\t" \
    "s_waitcnt vmcnt(4)\n\t" \
    "s_barrier\n\t" \
    "s_setprio 0\n\t" \
    "s_sub_u32 %[a], %[t], 1\n\t" \
    "v_mov_b32 v53, %[tag]\n\t" \
    "v_mov_b32 v52, %[a]\n\t" \
    "s_cmp_gt_u32 %[t], 1\n\t" \
    "s_cselect_b64 exec, %[fm], 0\n\t" \
    "buffer_store_dwordx2 v[52:53], %[l16], %[rs], %[fo] offen sc1\n\t" \
    "s_mov_b64 exec, %[pm]\n\t" \
    "s_mov_b32 m0, %[pd]\n\t" \
    "s_add_u32 %[a], %[sx], %[po]\n\t" \
    "buffer_load_dwordx4 %[l16], %[rs], %[a] offen sc1 lds\n\t" \
    "s_mov_b64 exec, -1\n\t" \
    "s_add_u32 m0, %[sd], 0\n\t" \
    "s_add_u32 %[a], %[sx], 131072\n\t" \
    "buffer_load_dwordx4 %[l16], %[rs], %[a] offen offset:0 sc1 lds\n\t" \
    "buffer_load_dwordx4 %[l16], %[rs], %[a] offen offset:1024 sc1 lds\n\t" \
    "buffer_load_dwordx4 %[l16], %[rs], %[a] offen offset:2048 sc1 lds\n\t" \
    "buffer_load_dwordx4 %[l16], %[rs], %[a] offen offset:3072 sc1 lds\n\t" \
    "v_mfma_f32_16x16x32_bf16 v[0:3], v[20:23], v[132:135], v[0:3]\n\t" \
    "ds_read_b128 v[44:47], %[xp] offset:22528\n\t" \
    "v_mfma_f32_16x16x32_bf16 v[4:7], v[20:23], v[140:143], v[4:7]\n\t" \
    "ds_read_b128 v[48:51], %[xp] offset:23552\n\t" \
    "v_mfma_f32_16x16x32_bf16 v[8:11], v[24:27], v[132:135], v[8:11]\n\t" \
    "s_waitcnt lgkmcnt(4)\n\t" \
    "v_mfma_f32_16x16x32_bf16 v[12:15], v[24:27], v[140:143], v[12:15]\n\t" \
    "v_mfma_f32_16x16x32_bf16 v[0:3], v[28:31], v[136:139], v[0:3]\n\t" \
    "ds_read_b128 v[20:23], %[xp] offset:24576\n\t" \
    "v_mfma_f32_16x16x32_bf16 v[4:7], v[28:31], v[144:147], v[4:7]\n\t" \
    "ds_read_b128 v[24:27], %[xp] offset:25600\n\t" \
    "v_mfma_f32_16x16x32_bf16 v[8:11], v[32:35], v[136:139], v[8:11]\n\t" \
    "s_waitcnt lgkmcnt(4)\n\t" \
    "v_mfma_f32_16x16x32_bf16 v[12:15], v[32:35], v[144:147], v[12:15]\n\t" \
    "v_mfma_f32_16x16x32_bf16 v[0:3], v[36:39], v[148:151], v[0:3]\n\t" \
    "ds_read_b128 v[28:31], %[xp] offset:26624\n\t" \
    "v_mfma_f32_16x16x32_bf16 v[4:7], v[36:39], v[156:159], v[4:7]\n\t" \
    "ds_read_b128 v[32:35], %[xp] offset:27648\n\t" \
    "v_mfma_f32_16x16x32_bf16 v[8:11], v[40:43], v[148:151], v[8:11]\n\t" \
    "s_waitcnt lgkmcnt(4)\n\t" \
    "v_mfma_f32_16x16x32_bf16 v[12:15], v[40:43], v[156:159], v[12:15]\n\t" \
    "v_mfma_f32_16x16x32_bf16 v[0:3], v[44:47], v[152:155], v[0:3]\n\t" \
    "ds_read_b128 v[36:39], %[xp] offset:28672\n\t" \
    "v_mfma_f32_16x16x32_bf16 v[4:7], v[44:47], v[160:163], v[4:7]\n\t" \
    "ds_read_b128 v[40:43], %[xp] offset:29696\n\t" \
    "v_mfma_f32_16x16x32_bf16 v[8:11], v[48:51], v[152:155], v[8:11]\n\t" \
    "s_waitcnt lgkmcnt(4)\n\t" \
    "v_mfma_f32_16x16x32_bf16 v[12:15], v[48:51], v[160:163], v[12:15]\n\t" \
    "v_mfma_f32_16x16x32_bf16 v[0:3], v[20:23], v[164:167], v[0:3]\n\t" \
    "ds_read_b128 v[44:47], %[xp] offset:30720\n\t" \
    "v_mfma_f32_16x16x32_bf16 v[4:7], v[20:23], v[172:175], v[4:7]\n\t" \
    "ds_read_b128 v[48:51], %[xp] offset:31744\n\t" \
    "v_mfma_f32_16x16x32_bf16 v[8:11], v[24:27], v[164:167], v[8:11]\n\t" \
    "s_waitcnt lgkmcnt(4)\n\t" \
    "v_mfma_f32_16x16x32_bf16 v[12:15], v[24:27], v[172:175], v[12:15]\n\t" \
    "v_mfma_f32_16x16x32_bf16 v[0:3], v[28:31], v[168:171], v[0:3]\n\t" \
    "ds_read_b128 v[20:23], %[xp] offset:32768\n\t" \
    "v_mfma_f32_16x16x32_bf16 v[4:7], v[28:31], v[176:179], v[4:7]\n\t" \
    "ds_read_b128 v[24:27], %[xp] offset:33792\n\t" \
    "v_mfma_f32_16x16x32_bf16 v[8:11], v[32:35], v[168:171], v[8:11]\n\t" \
    "s_waitcnt lgkmcnt(4)\n\t" \
    "v_mfma_f32_16x16x32_bf16 v[12:15], v[32:35], v[176:179], v[12:15]\n\t" \
    "v_mfma_f32_16x16x32_bf16 v[0:3], v[36:39], v[180:183], v[0:3]\n\t" \
    "ds_read_b128 v[28:31], %[xp] offset:34816\n\t" \
    "v_mfma_f32_16x16x32_bf16 v[4:7], v[36:39], v[188:191], v[4:7]\n\t" \
    "ds_read_b128 v[32:35], %[xp] offset:35840\n\t" \
    "v_mfma_f32_16x16x32_bf16 v[8:11], v[40:43], v[180:183], v[8:11]\n\t" \
    "s_waitcnt lgkmcnt(4)\n\t" \
    "v_mfma_f32_16x16x32_bf16 v[12:15], v[40:43], v[188:191], v[12:15]\n\t" \
    "v_mfma_f32_16x16x32_bf16 v[0:3], v[44:47], v[184:187], v[0:3]\n\t" \
    "ds_read_b128 v[36:39], %[xp] offset:36864\n\t" \
    "v_mfma_f32_16x16x32_bf16 v[4:7], v[44:47], v[192:195], v[4:7]\n\t" \
    "ds_read_b128 v[40:43], %[xp] offset:37888\n\t" \
    "v_mfma_f32_16x16x32_bf16 v[8:11], v[48:51], v[184:187], v[8:11]\n\t" \
    "s_waitcnt lgkmcnt(4)\n\t" \
    "v_mfma_f32_16x16x32_bf16 v[12:15], v[48:51], v[192:195], v[12:15]\n\t" \
    "s_add_u32 %[sx], %[sx], 65536\n\t" \
    "s_add_u32 %[t], %[t], 1\n\t" \
    "s_waitcnt vmcnt(4)\n\t" \
    "s_barrier\n\t" \
    "s_setprio 0\n\t" \
    "s_nop 3\n\t" \
    "ds_write_b128 %[pa], v[0:3] offset:0\n\t" \
    "ds_write_b128 %[pa], v[4:7] offset:1024\n\t" \
    "ds_write_b128 %[pa], v[8:11] offset:2048\n\t" \
    "ds_write_b128 %[pa], v[12:15] offset:3072\n\t" \
    "s_sub_u32 %[a], %[t], 1\n\t" \
    "v_mov_b32 v53, %[tag]\n\t" \
    "v_mov_b32 v52, %[a]\n\t" \
    "s_sub_u32 %[b], %[t], 2\n\t" \
    "s_cmp_lt_u32 %[b], " STR(SQUICK) "\n\t" \
    "s_cselect_b64 exec, %[fm], 0\n\t" \
    "buffer_store_dwordx2 v[52:53], %[l16], %[rs], %[fo] offen sc1\n\t" \
    "s_mov_b64 exec, -1\n\t" \
    "s_add_u32 m0, %[sd], 16384\n\t" \
    "s_add_u32 %[a], %[sx], 81920\n\t" \
    "s_waitcnt lgkmcnt(0)\n\t" \
    "buffer_load_dwordx4 %[l16], %[rs], %[a] offen offset:0 sc1 lds\n\t" \
    "buffer_load_dwordx4 %[l16], %[rs], %[a] offen offset:1024 sc1 lds\n\t" \
    "buffer_load_dwordx4 %[l16], %[rs], %[a] offen offset:2048 sc1 lds\n\t" \
    "buffer_load_dwordx4 %[l16], %[rs], %[a] offen offset:3072 sc1 lds\n\t" \
    "v_mfma_f32_16x16x32_bf16 v[196:199], v[20:23], v[68:71], 0\n\t" \
    "ds_read_b128 v[44:47], %[xp] offset:38912\n\t" \
    "v_mfma_f32_16x16x32_bf16 v[200:203], v[20:23], v[76:79], 0\n\t" \
    "ds_read_b128 v[48:51], %[xp] offset:39936\n\t" \
    "v_mfma_f32_16x16x32_bf16 v[204:207], v[24:27], v[68:71], 0\n\t" \
    "v_mfma_f32_16x16x32_bf16 v[208:211], v[24:27], v[76:79], 0\n\t" \
    "v_mfma_f32_16x16x32_bf16 v[196:199], v[28:31], v[72:75], v[196:199]\n\t" \
    "ds_read_b128 v[20:23], %[xp] offset:40960\n\t" \
    "v_mfma_f32_16x16x32_bf16 v[200:203], v[28:31], v[80:83], v[200:203]\n\t" \
    "ds_read_b128 v[24:27], %[xp] offset:41984\n\t" \
    "v_mfma_f32_16x16x32_bf16 v[204:207], v[32:35], v[72:75], v[204:207]\n\t" \
    "v_mfma_f32_16x16x32_bf16 v[208:211], v[32:35], v[80:83], v[208:211]\n\t" \
    "v_mfma_f32_16x16x32_bf16 v[196:199], v[36:39], v[84:87], v[196:199]\n\t" \
    "ds_read_b128 v[28:31], %[xp] offset:43008\n\t" \
    "v_mfma_f32_16x16x32_bf16 v[200:203], v[36:39], v[92:95], v[200:203]\n\t" \
    "ds_read_b128 v[32:35], %[xp] offset:44032\n\t" \
    "v_mfma_f32_16x16x32_bf16 v[204:207], v[40:43], v[84:87], v[204:207]\n\t" \
    "s_waitcnt lgkmcnt(4)\n\t" \
    "v_mfma_f32_16x16x32_bf16 v[208:211], v[40:43], v[92:95], v[208:211]\n\t" \
    "v_mfma_f32_16x16x32_bf16 v[196:199], v[44:47], v[88:91], v[196:199]\n\t" \
    "ds_read_b128 v[36:39], %[xp] offset:45056\n\t" \
    "v_mfma_f32_16x16x32_bf16 v[200:203], v[44:47], v[96:99], v[200:203]\n\t" \
    "ds_read_b128 v[40:43], %[xp] offset:46080\n\t" \
    "v_mfma_f32_16x16x32_bf16 v[204:207], v[48:51], v[88:91], v[204:207]\n\t" \
    "s_waitcnt lgkmcnt(4)\n\t" \
    "v_mfma_f32_16x16x32_bf16 v[208:211], v[48:51], v[96:99], v[208:211]\n\t" \
    "v_mfma_f32_16x16x32_bf16 v[196:199], v[20:23], v[100:103], v[196:199]\n\t" \
    "ds_read_b128 v[44:47], %[xp] offset:47104\n\t" \
    "v_mfma_f32_16x16x32_bf16 v[200:203], v[20:23], v[108:111], v[200:203]\n\t" \
    "ds_read_b128 v[48:51], %[xp] offset:48128\n\t" \
    "v_mfma_f32_16x16x32_bf16 v[204:207], v[24:27], v[100:103], v[204:207]\n\t" \
    "s_waitcnt lgkmcnt(4)\n\t" \
    "v_mfma_f32_16x16x32_bf16 v[208:211], v[24:27], v[108:111], v[208:211]\n\t" \
    "v_mfma_f32_16x16x32_bf16 v[196:199], v[28:31], v[104:107], v[196:199]\n\t" \
    "ds_read_b128 v[20:23], %[xp] offset:49152\n\t" \
    "v_mfma_f32_16x16x32_bf16 v[200:203], v[28:31], v[112:115], v[200:203]\n\t" \
    "ds_read_b128 v[24:27], %[xp] offset:50176\n\t" \
    "v_mfma_f32_16x16x32_bf16 v[204:207], v[32:35], v[104:107], v[204:207]\n\t" \
    "s_waitcnt lgkmcnt(4)\n\t" \
    "v_mfma_f32_16x16x32_bf16 v[208:211], v[32:35], v[112:115], v[208:211]\n\t" \
    "v_mfma_f32_16x16x32_bf16 v[196:199], v[36:39], v[116:119], v[196:199]\n\t" \
    "ds_read_b128 v[28:31], %[xp] offset:51200\n\t" \
    "v_mfma_f32_16x16x32_bf16 v[200:203], v[36:39], v[124:127], v[200:203]\n\t" \
    "ds_read_b128 v[32:35], %[xp] offset:52224\n\t" \
    "v_mfma_f32_16x16x32_bf16 v[204:207], v[40:43], v[116:119], v[204:207]\n\t" \
    "s_waitcnt lgkmcnt(4)\n\t" \
    "v_mfma_f32_16x16x32_bf16 v[208:211], v[40:43], v[124:127], v[208:211]\n\t" \
    "v_mfma_f32_16x16x32_bf16 v[196:199], v[44:47], v[120:123], v[196:199]\n\t" \
    "ds_read_b128 v[36:39], %[xp] offset:53248\n\t" \
    "v_mfma_f32_16x16x32_bf16 v[200:203], v[44:47], v[128:131], v[200:203]\n\t" \
    "ds_read_b128 v[40:43], %[xp] offset:54272\n\t" \
    "v_mfma_f32_16x16x32_bf16 v[204:207], v[48:51], v[120:123], v[204:207]\n\t" \
    "s_waitcnt lgkmcnt(4)\n\t" \
    "v_mfma_f32_16x16x32_bf16 v[208:211], v[48:51], v[128:131], v[208:211]\n\t" \
    "s_waitcnt vmcnt(4)\n\t" \
    "s_barrier\n\t" \
    "s_setprio 0\n\t" \
    "s_sub_u32 %[a], %[t], 1\n\t" \
    "v_mov_b32 v53, %[tag]\n\t" \
    "v_mov_b32 v52, %[a]\n\t" \
    "s_cmp_gt_u32 %[t], 1\n\t" \
    "s_cselect_b64 exec, %[fm], 0\n\t" \
    "buffer_store_dwordx2 v[52:53], %[l16], %[rs], %[fo] offen sc1\n\t" \
    "s_mov_b64 exec, %[pm]\n\t" \
    "s_mov_b32 m0, %[pd]\n\t" \
    "s_add_u32 %[a], %[sx], %[po]\n\t" \
    "buffer_load_dwordx4 %[l16], %[rs], %[a] offen sc1 lds\n\t" \
    "s_mov_b64 exec, -1\n\t" \
    "s_add_u32 m0, %[sd], 32768\n\t" \
    "s_add_u32 %[a], %[sx], 131072\n\t" \
    "buffer_load_dwordx4 %[l16], %[rs], %[a] offen offset:0 sc1 lds\n\t" \
    "buffer_load_dwordx4 %[l16], %[rs], %[a] offen offset:1024 sc1 lds\n\t" \
    "buffer_load_dwordx4 %[l16], %[rs], %[a] offen offset:2048 sc1 lds\n\t" \
    "buffer_load_dwordx4 %[l16], %[rs], %[a] offen offset:3072 sc1 lds\n\t" \
    "v_mfma_f32_16x16x32_bf16 v[196:199], v[20:23], v[132:135], v[196:199]\n\t" \
    "ds_read_b128 v[44:47], %[xp] offset:55296\n\t" \
    "v_mfma_f32_16x16x32_bf16 v[200:203], v[20:23], v[140:143], v[200:203]\n\t" \
    "ds_read_b128 v[48:51], %[xp] offset:56320\n\t" \
    "v_mfma_f32_16x16x32_bf16 v[204:207], v[24:27], v[132:135], v[204:207]\n\t" \
    "s_waitcnt lgkmcnt(4)\n\t" \
    "v_mfma_f32_16x16x32_bf16 v[208:211], v[24:27], v[140:143], v[208:211]\n\t" \
    "v_mfma_f32_16x16x32_bf16 v[196:199], v[28:31], v[136:139], v[196:199]\n\t" \
    "ds_read_b128 v[20:23], %[xp] offset:57344\n\t" \
    "v_mfma_f32_16x16x32_bf16 v[200:203], v[28:31], v[144:147], v[200:203]\n\t" \
    "ds_read_b128 v[24:27], %[xp] offset:58368\n\t" \
    "v_mfma_f32_16x16x32_bf16 v[204:207], v[32:35], v[136:139], v[204:207]\n\t" \
    "s_waitcnt lgkmcnt(4)\n\t" \
    "v_mfma_f32_16x16x32_bf16 v[208:211], v[32:35], v[144:147], v[208:211]\n\t" \
    "v_mfma_f32_16x16x32_bf16 v[196:199], v[36:39], v[148:151], v[196:199]\n\t" \
    "ds_read_b128 v[28:31], %[xp] offset:59392\n\t" \
    "v_mfma_f32_16x16x32_bf16 v[200:203], v[36:39], v[156:159], v[200:203]\n\t" \
    "ds_read_b128 v[32:35], %[xp] offset:60416\n\t" \
    "v_mfma_f32_16x16x32_bf16 v[204:207], v[40:43], v[148:151], v[204:207]\n\t" \
    "s_waitcnt lgkmcnt(4)\n\t" \
    "v_mfma_f32_16x16x32_bf16 v[208:211], v[40:43], v[156:159], v[208:211]\n\t" \
    "v_mfma_f32_16x16x32_bf16 v[196:199], v[44:47], v[152:155], v[196:199]\n\t" \
    "ds_read_b128 v[36:39], %[xp] offset:61440\n\t" \
    "v_mfma_f32_16x16x32_bf16 v[200:203], v[44:47], v[160:163], v[200:203]\n\t" \
    "ds_read_b128 v[40:43], %[xp] offset:62464\n\t" \
    "v_mfma_f32_16x16x32_bf16 v[204:207], v[48:51], v[152:155], v[204:207]\n\t" \
    "s_waitcnt lgkmcnt(4)\n\t" \
    "v_mfma_f32_16x16x32_bf16 v[208:211], v[48:51], v[160:163], v[208:211]\n\t" \
    "v_mfma_f32_16x16x32_bf16 v[196:199], v[20:23], v[164:167], v[196:199]\n\t" \
    "ds_read_b128 v[44:47], %[xp] offset:63488\n\t" \
    "v_mfma_f32_16x16x32_bf16 v[200:203], v[20:23], v[172:175], v[200:203]\n\t" \
    "ds_read_b128 v[48:51], %[xp] offset:64512\n\t" \
    "v_mfma_f32_16x16x32_bf16 v[204:207], v[24:27], v[164:167], v[204:207]\n\t" \
    "s_waitcnt lgkmcnt(4)\n\t" \
    "v_mfma_f32_16x16x32_bf16 v[208:211], v[24:27], v[172:175], v[208:211]\n\t" \
    "v_mfma_f32_16x16x32_bf16 v[196:199], v[28:31], v[168:171], v[196:199]\n\t" \
    "ds_read_b128 v[20:23], %[xp] offset:0\n\t" \
    "v_mfma_f32_16x16x32_bf16 v[200:203], v[28:31], v[176:179], v[200:203]\n\t" \
    "ds_read_b128 v[24:27], %[xp] offset:1024\n\t" \
    "v_mfma_f32_16x16x32_bf16 v[204:207], v[32:35], v[168:171], v[204:207]\n\t" \
    "s_waitcnt lgkmcnt(4)\n\t" \
    "v_mfma_f32_16x16x32_bf16 v[208:211], v[32:35], v[176:179], v[208:211]\n\t" \
    "v_mfma_f32_16x16x32_bf16 v[196:199], v[36:39], v[180:183], v[196:199]\n\t" \
    "ds_read_b128 v[28:31], %[xp] offset:2048\n\t" \
    "v_mfma_f32_16x16x32_bf16 v[200:203], v[36:39], v[188:191], v[200:203]\n\t" \
    "ds_read_b128 v[32:35], %[xp] offset:3072\n\t" \
    "v_mfma_f32_16x16x32_bf16 v[204:207], v[40:43], v[180:183], v[204:207]\n\t" \
    "s_waitcnt lgkmcnt(4)\n\t" \
    "v_mfma_f32_16x16x32_bf16 v[208:211], v[40:43], v[188:191], v[208:211]\n\t" \
    "v_mfma_f32_16x16x32_bf16 v[196:199], v[44:47], v[184:187], v[196:199]\n\t" \
    "ds_read_b128 v[36:39], %[xp] offset:4096\n\t" \
    "v_mfma_f32_16x16x32_bf16 v[200:203], v[44:47], v[192:195], v[200:203]\n\t" \
    "ds_read_b128 v[40:43], %[xp] offset:5120\n\t" \
    "v_mfma_f32_16x16x32_bf16 v[204:207], v[48:51], v[184:187], v[204:207]\n\t" \
    "s_waitcnt lgkmcnt(4)\n\t" \
    "v_mfma_f32_16x16x32_bf16 v[208:211], v[48:51], v[192:195], v[208:211]\n\t" \
    "s_add_u32 %[sx], %[sx], 65536\n\t" \
    "s_add_u32 %[t], %[t], 1\n\t" \
    "s_waitcnt vmcnt(4)\n\t" \
    "s_barrier\n\t" \
    "s_cmp_lt_u32 %[t], %[te]\n\t" \
    "s_cbranch_scc1 L0_%=\n\t" \
    "s_cmp_eq_u32 %[fin], %[te]\n\t" \
    "s_cbranch_scc0 E_%=\n\t" \
    "s_nop 4\n\t" \
    "ds_write_b128 %[pa], v[196:199] offset:0\n\t" \
    "ds_write_b128 %[pa], v[200:203] offset:1024\n\t" \
    "ds_write_b128 %[pa], v[204:207] offset:2048\n\t" \
    "ds_write_b128 %[pa], v[208:211] offset:3072\n\t" \
    "s_waitcnt lgkmcnt(0)\n\t" \
    "s_barrier\n\t" \
    "s_branch E_%=\n\t" \
    "L1_%=:\n\t" \
    "s_setprio 1\n\t" \
    "s_add_u32 m0, %[sd], 49152\n\t" \
    "s_add_u32 %[a], %[sx], 81920\n\t" \
    "v_mfma_f32_16x16x32_bf16 v[0:3], v[20:23], v[68:71], 0\n\t" \
    "ds_read_b128 v[44:47], %[xp] offset:6144\n\t" \
    "v_mfma_f32_16x16x32_bf16 v[4:7], v[20:23], v[76:79], 0\n\t" \
    "ds_read_b128 v[48:51], %[xp] offset:7168\n\t" \
    "v_mfma_f32_16x16x32_bf16 v[8:11], v[24:27], v[68:71], 0\n\t" \
    "s_waitcnt lgkmcnt(4)\n\t" \
    "v_mfma_f32_16x16x32_bf16 v[12:15], v[24:27], v[76:79], 0\n\t" \
    "v_mfma_f32_16x16x32_bf16 v[0:3], v[28:31], v[72:75], v[0:3]\n\t" \
    "ds_read_b128 v[20:23], %[xp] offset:8192\n\t" \
    "v_mfma_f32_16x16x32_bf16 v[4:7], v[28:31], v[80:83], v[4:7]\n\t" \
    "ds_read_b128 v[24:27], %[xp] offset:9216\n\t" \
    "v_mfma_f32_16x16x32_bf16 v[8:11], v[32:35], v[72:75], v[8:11]\n\t" \
    "s_waitcnt lgkmcnt(4)\n\t" \
    "v_mfma_f32_16x16x32_bf16 v[12:15], v[32:35], v[80:83], v[12:15]\n\t" \
    "v_mfma_f32_16x16x32_bf16 v[0:3], v[36:39], v[84:87], v[0:3]\n\t" \
    "ds_read_b128 v[28:31], %[xp] offset:10240\n\t" \
    "v_mfma_f32_16x16x32_bf16 v[4:7], v[36:39], v[92:95], v[4:7]\n\t" \
    "ds_read_b128 v[32:35], %[xp] offset:11264\n\t" \
    "v_mfma_f32_16x16x32_bf16 v[8:11], v[40:43], v[84:87], v[8:11]\n\t" \
    "s_waitcnt lgkmcnt(4)\n\t" \
    "v_mfma_f32_16x16x32_bf16 v[12:15], v[40:43], v[92:95], v[12:15]\n\t" \
    "v_mfma_f32_16x16x32_bf16 v[0:3], v[44:47], v[88:91], v[0:3]\n\t" \
    "ds_read_b128 v[36:39], %[xp] offset:12288\n\t" \
    "v_mfma_f32_16x16x32_bf16 v[4:7], v[44:47], v[96:99], v[4:7]\n\t" \
    "ds_read_b128 v[40:43], %[xp] offset:13312\n\t" \
    "v_mfma_f32_16x16x32_bf16 v[8:11], v[48:51], v[88:91], v[8:11]\n\t" \
    "s_waitcnt lgkmcnt(4)\n\t" \
    "v_mfma_f32_16x16x32_bf16 v[12:15], v[48:51], v[96:99], v[12:15]\n\t" \
    "v_mfma_f32_16x16x32_bf16 v[0:3], v[20:23], v[100:103], v[0:3]\n\t" \
    "ds_read_b128 v[44:47], %[xp] offset:14336\n\t" \
    "v_mfma_f32_16x16x32_bf16 v[4:7], v[20:23], v[108:111], v[4:7]\n\t" \
    "ds_read_b128 v[48:51], %[xp] offset:15360\n\t" \
    "v_mfma_f32_16x16x32_bf16 v[8:11], v[24:27], v[100:103], v[8:11]\n\t" \
    "s_waitcnt lgkmcnt(4)\n\t" \
    "v_mfma_f32_16x16x32_bf16 v[12:15], v[24:27], v[108:111], v[12:15]\n\t" \
    "buffer_load_dwordx4 %[l16], %[rs], %[a] offen offset:0 sc1 lds\n\t" \
    "v_mfma_f32_16x16x32_bf16 v[0:3], v[28:31], v[104:107], v[0:3]\n\t" \
    "ds_read_b128 v[20:23], %[xp] offset:16384\n\t" \
    "v_mfma_f32_16x16x32_bf16 v[4:7], v[28:31], v[112:115], v[4:7]\n\t" \
    "ds_read_b128 v[24:27], %[xp] offset:17408\n\t" \
    "v_mfma_f32_16x16x32_bf16 v[8:11], v[32:35], v[104:107], v[8:11]\n\t" \
    "s_waitcnt lgkmcnt(4)\n\t" \
    "v_mfma_f32_16x16x32_bf16 v[12:15], v[32:35], v[112:115], v[12:15]\n\t" \
    "buffer_load_dwordx4 %[l16], %[rs], %[a] offen offset:1024 sc1 lds\n\t" \
    "v_mfma_f32_16x16x32_bf16 v[0:3], v[36:39], v[116:119], v[0:3]\n\t" \
    "ds_read_b128 v[28:31], %[xp] offset:18432\n\t" \
    "v_mfma_f32_16x16x32_bf16 v[4:7], v[36:39], v[124:127], v[4:7]\n\t" \
    "ds_read_b128 v[32:35], %[xp] offset:19456\n\t" \
    "v_mfma_f32_16x16x32_bf16 v[8:11], v[40:43], v[116:119], v[8:11]\n\t" \
    "s_waitcnt lgkmcnt(4)\n\t" \
    "v_mfma_f32_16x16x32_bf16 v[12:15], v[40:43], v[124:127], v[12:15]\n\t" \
    "buffer_load_dwordx4 %[l16], %[rs], %[a] offen offset:2048 sc1 lds\n\t" \
    "v_mfma_f32_16x16x32_bf16 v[0:3], v[44:47], v[120:123], v[0:3]\n\t" \
    "ds_read_b128 v[36:39], %[xp] offset:20480\n\t" \
    "v_mfma_f32_16x16x32_bf16 v[4:7], v[44:47], v[128:131], v[4:7]\n\t" \
    "ds_read_b128 v[40:43], %[xp] offset:21504\n\t" \
    "v_mfma_f32_16x16x32_bf16 v[8:11], v[48:51], v[120:123], v[8:11]\n\t" \
    "s_waitcnt lgkmcnt(4)\n\t" \
    "v_mfma_f32_16x16x32_bf16 v[12:15], v[48:51], v[128:131], v[12:15]\n\t" \
    "buffer_load_dwordx4 %[l16], %[rs], %[a] offen offset:3072 sc1 lds\n\t" \
    "s_waitcnt vmcnt(4)\n\t" \
    "s_barrier\n\t" \
    "s_setprio 1\n\t" \
    "s_add_u32 m0, %[sd], 0\n\t" \
    "s_add_u32 %[a], %[sx], 131072\n\t" \
    "s_add_u32 %[b], %[oo], 0x1000\n\t" \
    "s_cmp_lg_u32 %[t], 1\n\t" \
    "s_cselect_b64 %[em], -1, 0\n\t" \
    "v_mfma_f32_16x16x32_bf16 v[0:3], v[20:23], v[132:135], v[0:3]\n\t" \
    "ds_read_b128 v[44:47], %[xp] offset:22528\n\t" \
    "v_mfma_f32_16x16x32_bf16 v[4:7], v[20:23], v[140:143], v[4:7]\n\t" \
    "ds_read_b128 v[48:51], %[xp] offset:23552\n\t" \
    "v_mfma_f32_16x16x32_bf16 v[8:11], v[24:27], v[132:135], v[8:11]\n\t" \
    "ds_read_b128 v[52:55], %[pa] offset:0\n\t" \
    "s_waitcnt lgkmcnt(5)\n\t" \
    "v_mfma_f32_16x16x32_bf16 v[12:15], v[24:27], v[140:143], v[12:15]\n\t" \
    "buffer_load_dwordx4 %[l16], %[rs], %[a] offen offset:0 sc1 lds\n\t" \
    "v_mfma_f32_16x16x32_bf16 v[0:3], v[28:31], v[136:139], v[0:3]\n\t" \
    "ds_read_b128 v[20:23], %[xp] offset:24576\n\t" \
    "v_mfma_f32_16x16x32_bf16 v[4:7], v[28:31], v[144:147], v[4:7]\n\t" \
    "ds_read_b128 v[24:27], %[xp] offset:25600\n\t" \
    "v_mfma_f32_16x16x32_bf16 v[8:11], v[32:35], v[136:139], v[8:11]\n\t" \
    "ds_read_b128 v[56:59], %[pa] offset:1024\n\t" \
    "s_waitcnt lgkmcnt(6)\n\t" \
    "v_mfma_f32_16x16x32_bf16 v[12:15], v[32:35], v[144:147], v[12:15]\n\t" \
    "buffer_load_dwordx4 %[l16], %[rs], %[a] offen offset:1024 sc1 lds\n\t" \
    "v_mfma_f32_16x16x32_bf16 v[0:3], v[36:39], v[148:151], v[0:3]\n\t" \
    "ds_read_b128 v[28:31], %[xp] offset:26624\n\t" \
    "v_mfma_f32_16x16x32_bf16 v[4:7], v[36:39], v[156:159], v[4:7]\n\t" \
    "ds_read_b128 v[32:35], %[xp] offset:27648\n\t" \
    "v_mfma_f32_16x16x32_bf16 v[8:11], v[40:43], v[148:151], v[8:11]\n\t" \
    "ds_read_b128 v[60:63], %[pa] offset:2048\n\t" \
    "s_waitcnt lgkmcnt(7)\n\t" \
    "v_mfma_f32_16x16x32_bf16 v[12:15], v[40:43], v[156:159], v[12:15]\n\t" \
    "buffer_load_dwordx4 %[l16], %[rs], %[a] offen offset:2048 sc1 lds\n\t" \
    "v_mfma_f32_16x16x32_bf16 v[0:3], v[44:47], v[152:155], v[0:3]\n\t" \
    "ds_read_b128 v[36:39], %[xp] offset:28672\n\t" \
    "s_waitcnt lgkmcnt(7)\n\t" \
    "v_add_f32 v196, v196, v52\n\t" \
    "v_mfma_f32_16x16x32_bf16 v[4:7], v[44:47], v[160:163], v[4:7]\n\t" \
    "ds_read_b128 v[40:43], %[xp] offset:29696\n\t" \
    "v_add_f32 v197, v197, v53\n\t" \
    "v_mfma_f32_16x16x32_bf16 v[8:11], v[48:51], v[152:155], v[8:11]\n\t" \
    "ds_read_b128 v[64:67], %[pa] offset:3072\n\t" \
    "s_waitcnt lgkmcnt(7)\n\t" \
    "v_mfma_f32_16x16x32_bf16 v[12:15], v[48:51], v[160:163], v[12:15]\n\t" \
    "buffer_load_dwordx4 %[l16], %[rs], %[a] offen offset:3072 sc1 lds\n\t" \
    "v_add_f32 v198, v198, v54\n\t" \
    "v_add_f32 v199, v199, v55\n\t" \
    "v_mfma_f32_16x16x32_bf16 v[0:3], v[20:23], v[164:167], v[0:3]\n\t" \
    "ds_read_b128 v[44:47], %[xp] offset:30720\n\t" \
    "s_waitcnt lgkmcnt(7)\n\t" \
    "v_add_f32 v200, v200, v56\n\t" \
    "v_mfma_f32_16x16x32_bf16 v[4:7], v[20:23], v[172:175], v[4:7]\n\t" \
    "ds_read_b128 v[48:51], %[xp] offset:31744\n\t" \
    "v_add_f32 v201, v201, v57\n\t" \
    "v_mfma_f32_16x16x32_bf16 v[8:11], v[24:27], v[164:167], v[8:11]\n\t" \
    "v_add_f32 v202, v202, v58\n\t" \
    "s_waitcnt lgkmcnt(6)\n\t" \
    "v_mfma_f32_16x16x32_bf16 v[12:15], v[24:27], v[172:175], v[12:15]\n\t" \
    "v_add_f32 v203, v203, v59\n\t" \
    "s_waitcnt lgkmcnt(5)\n\t" \
    "v_add_f32 v204, v204, v60\n\t" \
    "v_add_f32 v205, v205, v61\n\t" \
    "v_mfma_f32_16x16x32_bf16 v[0:3], v[28:31], v[168:171], v[0:3]\n\t" \
    "ds_read_b128 v[20:23], %[xp] offset:32768\n\t" \
    "v_add_f32 v206, v206, v62\n\t" \
    "v_mfma_f32_16x16x32_bf16 v[4:7], v[28:31], v[176:179], v[4:7]\n\t" \
    "ds_read_b128 v[24:27], %[xp] offset:33792\n\t" \
    "v_add_f32 v207, v207, v63\n\t" \
    "v_mfma_f32_16x16x32_bf16 v[8:11], v[32:35], v[168:171], v[8:11]\n\t" \
    "s_waitcnt lgkmcnt(4)\n\t" \
    "v_add_f32 v208, v208, v64\n\t" \
    "v_mfma_f32_16x16x32_bf16 v[12:15], v[32:35], v[176:179], v[12:15]\n\t" \
    "v_add_f32 v209, v209, v65\n\t" \
    "v_add_f32 v210, v210, v66\n\t" \
    "v_add_f32 v211, v211, v67\n\t" \
    "v_mfma_f32_16x16x32_bf16 v[0:3], v[36:39], v[180:183], v[0:3]\n\t" \
    "ds_read_b128 v[28:31], %[xp] offset:34816\n\t" \
    "v_cvt_pk_bf16_f32 v52, v196, v197\n\t" \
    "v_mfma_f32_16x16x32_bf16 v[4:7], v[36:39], v[188:191], v[4:7]\n\t" \
    "ds_read_b128 v[32:35], %[xp] offset:35840\n\t" \
    "v_cvt_pk_bf16_f32 v53, v198, v199\n\t" \
    "v_mfma_f32_16x16x32_bf16 v[8:11], v[40:43], v[180:183], v[8:11]\n\t" \
    "v_cvt_pk_bf16_f32 v54, v204, v205\n\t" \
    "s_waitcnt lgkmcnt(4)\n\t" \
    "v_mfma_f32_16x16x32_bf16 v[12:15], v[40:43], v[188:191], v[12:15]\n\t" \
    "v_cvt_pk_bf16_f32 v55, v206, v207\n\t" \
    "v_cvt_pk_bf16_f32 v56, v200, v201\n\t" \
    "v_cvt_pk_bf16_f32 v57, v202, v203\n\t" \
    "v_mfma_f32_16x16x32_bf16 v[0:3], v[44:47], v[184:187], v[0:3]\n\t" \
    "ds_read_b128 v[36:39], %[xp] offset:36864\n\t" \
    "v_cvt_pk_bf16_f32 v58, v208, v209\n\t" \
    "v_mfma_f32_16x16x32_bf16 v[4:7], v[44:47], v[192:195], v[4:7]\n\t" \
    "ds_read_b128 v[40:43], %[xp] offset:37888\n\t" \
    "v_cvt_pk_bf16_f32 v59, v210, v211\n\t" \
    "v_mfma_f32_16x16x32_bf16 v[8:11], v[48:51], v[184:187], v[8:11]\n\t" \
    "s_waitcnt lgkmcnt(4)\n\t" \
    "v_mfma_f32_16x16x32_bf16 v[12:15], v[48:51], v[192:195], v[12:15]\n\t" \
    "s_mov_b64 exec, %[em]\n\t" \
    "buffer_store_dwordx4 v[52:55], %[l16], %[rs], %[oo] offen sc1\n\t" \
    "buffer_store_dwordx4 v[56:59], %[l16], %[rs], %[oo] offen offset:1024 sc1\n\t" \
    "s_mov_b64 exec, -1\n\t" \
    "s_add_u32 %[sx], %[sx], 65536\n\t" \
    "s_add_u32 %[t], %[t], 1\n\t" \
    "s_add_u32 %[oo], %[oo], 262144\n\t" \
    "s_waitcnt vmcnt(6)\n\t" \
    "s_cmp_gt_u32 %[t], " STR(SQUICK) " + 1\n\t" \
    "s_cbranch_scc1 B0_%=\n\t" \
    "s_waitcnt vmcnt(0)\n\t" \
    "B0_%=:\n\t" \
    "s_barrier\n\t" \
    "s_setprio 1\n\t" \
    "s_add_u32 m0, %[sd], 16384\n\t" \
    "s_add_u32 %[a], %[sx], 81920\n\t" \
    "v_mfma_f32_16x16x32_bf16 v[196:199], v[20:23], v[68:71], 0\n\t" \
    "ds_read_b128 v[44:47], %[xp] offset:38912\n\t" \
    "v_mfma_f32_16x16x32_bf16 v[200:203], v[20:23], v[76:79], 0\n\t" \
    "ds_read_b128 v[48:51], %[xp] offset:39936\n\t" \
    "v_mfma_f32_16x16x32_bf16 v[204:207], v[24:27], v[68:71], 0\n\t" \
    "s_waitcnt lgkmcnt(4)\n\t" \
    "v_mfma_f32_16x16x32_bf16 v[208:211], v[24:27], v[76:79], 0\n\t" \
    "v_mfma_f32_16x16x32_bf16 v[196:199], v[28:31], v[72:75], v[196:199]\n\t" \
    "ds_read_b128 v[20:23], %[xp] offset:40960\n\t" \
    "v_mfma_f32_16x16x32_bf16 v[200:203], v[28:31], v[80:83], v[200:203]\n\t" \
    "ds_read_b128 v[24:27], %[xp] offset:41984\n\t" \
    "v_mfma_f32_16x16x32_bf16 v[204:207], v[32:35], v[72:75], v[204:207]\n\t" \
    "s_waitcnt lgkmcnt(4)\n\t" \
    "v_mfma_f32_16x16x32_bf16 v[208:211], v[32:35], v[80:83], v[208:211]\n\t" \
    "v_mfma_f32_16x16x32_bf16 v[196:199], v[36:39], v[84:87], v[196:199]\n\t" \
    "ds_read_b128 v[28:31], %[xp] offset:43008\n\t" \
    "v_mfma_f32_16x16x32_bf16 v[200:203], v[36:39], v[92:95], v[200:203]\n\t" \
    "ds_read_b128 v[32:35], %[xp] offset:44032\n\t" \
    "v_mfma_f32_16x16x32_bf16 v[204:207], v[40:43], v[84:87], v[204:207]\n\t" \
    "s_waitcnt lgkmcnt(4)\n\t" \
    "v_mfma_f32_16x16x32_bf16 v[208:211], v[40:43], v[92:95], v[208:211]\n\t" \
    "v_mfma_f32_16x16x32_bf16 v[196:199], v[44:47], v[88:91], v[196:199]\n\t" \
    "ds_read_b128 v[36:39], %[xp] offset:45056\n\t" \
    "v_mfma_f32_16x16x32_bf16 v[200:203], v[44:47], v[96:99], v[200:203]\n\t" \
    "ds_read_b128 v[40:43], %[xp] offset:46080\n\t" \
    "v_mfma_f32_16x16x32_bf16 v[204:207], v[48:51], v[88:91], v[204:207]\n\t" \
    "s_waitcnt lgkmcnt(4)\n\t" \
    "v_mfma_f32_16x16x32_bf16 v[208:211], v[48:51], v[96:99], v[208:211]\n\t" \
    "v_mfma_f32_16x16x32_bf16 v[196:199], v[20:23], v[100:103], v[196:199]\n\t" \
    "ds_read_b128 v[44:47], %[xp] offset:47104\n\t" \
    "v_mfma_f32_16x16x32_bf16 v[200:203], v[20:23], v[108:111], v[200:203]\n\t" \
    "ds_read_b128 v[48:51], %[xp] offset:48128\n\t" \
    "v_mfma_f32_16x16x32_bf16 v[204:207], v[24:27], v[100:103], v[204:207]\n\t" \
    "s_waitcnt lgkmcnt(4)\n\t" \
    "v_mfma_f32_16x16x32_bf16 v[208:211], v[24:27], v[108:111], v[208:211]\n\t" \
    "buffer_load_dwordx4 %[l16], %[rs], %[a] offen offset:0 sc1 lds\n\t" \
    "v_mfma_f32_16x16x32_bf16 v[196:199], v[28:31], v[104:107], v[196:199]\n\t" \
    "ds_read_b128 v[20:23], %[xp] offset:49152\n\t" \
    "v_mfma_f32_16x16x32_bf16 v[200:203], v[28:31], v[112:115], v[200:203]\n\t" \
    "ds_read_b128 v[24:27], %[xp] offset:50176\n\t" \
    "v_mfma_f32_16x16x32_bf16 v[204:207], v[32:35], v[104:107], v[204:207]\n\t" \
    "s_waitcnt lgkmcnt(4)\n\t" \
    "v_mfma_f32_16x16x32_bf16 v[208:211], v[32:35], v[112:115], v[208:211]\n\t" \
    "buffer_load_dwordx4 %[l16], %[rs], %[a] offen offset:1024 sc1 lds\n\t" \
    "v_mfma_f32_16x16x32_bf16 v[196:199], v[36:39], v[116:119], v[196:199]\n\t" \
    "ds_read_b128 v[28:31], %[xp] offset:51200\n\t" \
    "v_mfma_f32_16x16x32_bf16 v[200:203], v[36:39], v[124:127], v[200:203]\n\t" \
    "ds_read_b128 v[32:35], %[xp] offset:52224\n\t" \
    "v_mfma_f32_16x16x32_bf16 v[204:207], v[40:43], v[116:119], v[204:207]\n\t" \
    "s_waitcnt lgkmcnt(4)\n\t" \
    "v_mfma_f32_16x16x32_bf16 v[208:211], v[40:43], v[124:127], v[208:211]\n\t" \
    "buffer_load_dwordx4 %[l16], %[rs], %[a] offen offset:2048 sc1 lds\n\t" \
    "v_mfma_f32_16x16x32_bf16 v[196:199], v[44:47], v[120:123], v[196:199]\n\t" \
    "ds_read_b128 v[36:39], %[xp] offset:53248\n\t" \
    "v_mfma_f32_16x16x32_bf16 v[200:203], v[44:47], v[128:131], v[200:203]\n\t" \
    "ds_read_b128 v[40:43], %[xp] offset:54272\n\t" \
    "v_mfma_f32_16x16x32_bf16 v[204:207], v[48:51], v[120:123], v[204:207]\n\t" \
    "s_waitcnt lgkmcnt(4)\n\t" \
    "v_mfma_f32_16x16x32_bf16 v[208:211], v[48:51], v[128:131], v[208:211]\n\t" \
    "buffer_load_dwordx4 %[l16], %[rs], %[a] offen offset:3072 sc1 lds\n\t" \
    "s_waitcnt vmcnt(4)\n\t" \
    "s_barrier\n\t" \
    "s_setprio 1\n\t" \
    "s_add_u32 m0, %[sd], 32768\n\t" \
    "s_add_u32 %[a], %[sx], 131072\n\t" \
    "s_add_u32 %[b], %[oo], 0x1000\n\t" \
    "s_cmp_lg_u32 %[t], 1\n\t" \
    "s_cselect_b64 %[em], -1, 0\n\t" \
    "v_mfma_f32_16x16x32_bf16 v[196:199], v[20:23], v[132:135], v[196:199]\n\t" \
    "ds_read_b128 v[44:47], %[xp] offset:55296\n\t" \
    "v_mfma_f32_16x16x32_bf16 v[200:203], v[20:23], v[140:143], v[200:203]\n\t" \
    "ds_read_b128 v[48:51], %[xp] offset:56320\n\t" \
    "v_mfma_f32_16x16x32_bf16 v[204:207], v[24:27], v[132:135], v[204:207]\n\t" \
    "ds_read_b128 v[52:55], %[pa] offset:0\n\t" \
    "s_waitcnt lgkmcnt(5)\n\t" \
    "v_mfma_f32_16x16x32_bf16 v[208:211], v[24:27], v[140:143], v[208:211]\n\t" \
    "buffer_load_dwordx4 %[l16], %[rs], %[a] offen offset:0 sc1 lds\n\t" \
    "v_mfma_f32_16x16x32_bf16 v[196:199], v[28:31], v[136:139], v[196:199]\n\t" \
    "ds_read_b128 v[20:23], %[xp] offset:57344\n\t" \
    "v_mfma_f32_16x16x32_bf16 v[200:203], v[28:31], v[144:147], v[200:203]\n\t" \
    "ds_read_b128 v[24:27], %[xp] offset:58368\n\t" \
    "v_mfma_f32_16x16x32_bf16 v[204:207], v[32:35], v[136:139], v[204:207]\n\t" \
    "ds_read_b128 v[56:59], %[pa] offset:1024\n\t" \
    "s_waitcnt lgkmcnt(6)\n\t" \
    "v_mfma_f32_16x16x32_bf16 v[208:211], v[32:35], v[144:147], v[208:211]\n\t" \
    "buffer_load_dwordx4 %[l16], %[rs], %[a] offen offset:1024 sc1 lds\n\t" \
    "v_mfma_f32_16x16x32_bf16 v[196:199], v[36:39], v[148:151], v[196:199]\n\t" \
    "ds_read_b128 v[28:31], %[xp] offset:59392\n\t" \
    "v_mfma_f32_16x16x32_bf16 v[200:203], v[36:39], v[156:159], v[200:203]\n\t" \
    "ds_read_b128 v[32:35], %[xp] offset:60416\n\t" \
    "v_mfma_f32_16x16x32_bf16 v[204:207], v[40:43], v[148:151], v[204:207]\n\t" \
    "ds_read_b128 v[60:63], %[pa] offset:2048\n\t" \
    "s_waitcnt lgkmcnt(7)\n\t" \
    "v_mfma_f32_16x16x32_bf16 v[208:211], v[40:43], v[156:159], v[208:211]\n\t" \
    "buffer_load_dwordx4 %[l16], %[rs], %[a] offen offset:2048 sc1 lds\n\t" \
    "v_mfma_f32_16x16x32_bf16 v[196:199], v[44:47], v[152:155], v[196:199]\n\t" \
    "ds_read_b128 v[36:39], %[xp] offset:61440\n\t" \
    "s_waitcnt lgkmcnt(7)\n\t" \
    "v_add_f32 v0, v0, v52\n\t" \
    "v_mfma_f32_16x16x32_bf16 v[200:203], v[44:47], v[160:163], v[200:203]\n\t" \
    "ds_read_b128 v[40:43], %[xp] offset:62464\n\t" \
    "v_add_f32 v1, v1, v53\n\t" \
    "v_mfma_f32_16x16x32_bf16 v[204:207], v[48:51], v[152:155], v[204:207]\n\t" \
    "ds_read_b128 v[64:67], %[pa] offset:3072\n\t" \
    "s_waitcnt lgkmcnt(7)\n\t" \
    "v_mfma_f32_16x16x32_bf16 v[208:211], v[48:51], v[160:163], v[208:211]\n\t" \
    "buffer_load_dwordx4 %[l16], %[rs], %[a] offen offset:3072 sc1 lds\n\t" \
    "v_add_f32 v2, v2, v54\n\t" \
    "v_add_f32 v3, v3, v55\n\t" \
    "v_mfma_f32_16x16x32_bf16 v[196:199], v[20:23], v[164:167], v[196:199]\n\t" \
    "ds_read_b128 v[44:47], %[xp] offset:63488\n\t" \
    "s_waitcnt lgkmcnt(7)\n\t" \
    "v_add_f32 v4, v4, v56\n\t" \
    "v_mfma_f32_16x16x32_bf16 v[200:203], v[20:23], v[172:175], v[200:203]\n\t" \
    "ds_read_b128 v[48:51], %[xp] offset:64512\n\t" \
    "v_add_f32 v5, v5, v57\n\t" \
    "v_mfma_f32_16x16x32_bf16 v[204:207], v[24:27], v[164:167], v[204:207]\n\t" \
    "v_add_f32 v6, v6, v58\n\t" \
    "s_waitcnt lgkmcnt(6)\n\t" \
    "v_mfma_f32_16x16x32_bf16 v[208:211], v[24:27], v[172:175], v[208:211]\n\t" \
    "v_add_f32 v7, v7, v59\n\t" \
    "s_waitcnt lgkmcnt(5)\n\t" \
    "v_add_f32 v8, v8, v60\n\t" \
    "v_add_f32 v9, v9, v61\n\t" \
    "v_mfma_f32_16x16x32_bf16 v[196:199], v[28:31], v[168:171], v[196:199]\n\t" \
    "ds_read_b128 v[20:23], %[xp] offset:0\n\t" \
    "v_add_f32 v10, v10, v62\n\t" \
    "v_mfma_f32_16x16x32_bf16 v[200:203], v[28:31], v[176:179], v[200:203]\n\t" \
    "ds_read_b128 v[24:27], %[xp] offset:1024\n\t" \
    "v_add_f32 v11, v11, v63\n\t" \
    "v_mfma_f32_16x16x32_bf16 v[204:207], v[32:35], v[168:171], v[204:207]\n\t" \
    "s_waitcnt lgkmcnt(4)\n\t" \
    "v_add_f32 v12, v12, v64\n\t" \
    "v_mfma_f32_16x16x32_bf16 v[208:211], v[32:35], v[176:179], v[208:211]\n\t" \
    "v_add_f32 v13, v13, v65\n\t" \
    "v_add_f32 v14, v14, v66\n\t" \
    "v_add_f32 v15, v15, v67\n\t" \
    "v_mfma_f32_16x16x32_bf16 v[196:199], v[36:39], v[180:183], v[196:199]\n\t" \
    "ds_read_b128 v[28:31], %[xp] offset:2048\n\t" \
    "v_cvt_pk_bf16_f32 v52, v0, v1\n\t" \
    "v_mfma_f32_16x16x32_bf16 v[200:203], v[36:39], v[188:191], v[200:203]\n\t" \
    "ds_read_b128 v[32:35], %[xp] offset:3072\n\t" \
    "v_cvt_pk_bf16_f32 v53, v2, v3\n\t" \
    "v_mfma_f32_16x16x32_bf16 v[204:207], v[40:43], v[180:183], v[204:207]\n\t" \
    "v_cvt_pk_bf16_f32 v54, v8, v9\n\t" \
    "s_waitcnt lgkmcnt(4)\n\t" \
    "v_mfma_f32_16x16x32_bf16 v[208:211], v[40:43], v[188:191], v[208:211]\n\t" \
    "v_cvt_pk_bf16_f32 v55, v10, v11\n\t" \
    "v_cvt_pk_bf16_f32 v56, v4, v5\n\t" \
    "v_cvt_pk_bf16_f32 v57, v6, v7\n\t" \
    "v_mfma_f32_16x16x32_bf16 v[196:199], v[44:47], v[184:187], v[196:199]\n\t" \
    "ds_read_b128 v[36:39], %[xp] offset:4096\n\t" \
    "v_cvt_pk_bf16_f32 v58, v12, v13\n\t" \
    "v_mfma_f32_16x16x32_bf16 v[200:203], v[44:47], v[192:195], v[200:203]\n\t" \
    "ds_read_b128 v[40:43], %[xp] offset:5120\n\t" \
    "v_cvt_pk_bf16_f32 v59, v14, v15\n\t" \
    "v_mfma_f32_16x16x32_bf16 v[204:207], v[48:51], v[184:187], v[204:207]\n\t" \
    "s_waitcnt lgkmcnt(4)\n\t" \
    "v_mfma_f32_16x16x32_bf16 v[208:211], v[48:51], v[192:195], v[208:211]\n\t" \
    "s_mov_b64 exec, %[em]\n\t" \
    "buffer_store_dwordx4 v[52:55], %[l16], %[rs], %[oo] offen sc1\n\t" \
    "buffer_store_dwordx4 v[56:59], %[l16], %[rs], %[oo] offen offset:1024 sc1\n\t" \
    "s_mov_b64 exec, -1\n\t" \
    "s_add_u32 %[sx], %[sx], 65536\n\t" \
    "s_add_u32 %[t], %[t], 1\n\t" \
    "s_add_u32 %[oo], %[oo], 262144\n\t" \
    "s_waitcnt vmcnt(6)\n\t" \
    "s_cmp_gt_u32 %[t], " STR(SQUICK) " + 1\n\t" \
    "s_cbranch_scc1 B1_%=\n\t" \
    "s_waitcnt vmcnt(0)\n\t" \
    "B1_%=:\n\t" \
    "s_barrier\n\t" \
    "s_cmp_lt_u32 %[t], %[te]\n\t" \
    "s_cbranch_scc1 L1_%=\n\t" \
    "s_cmp_eq_u32 %[fin], %[te]\n\t" \
    "s_cbranch_scc0 E_%=\n\t" \
    "s_nop 4\n\t" \
    "s_barrier\n\t" \
    "ds_read_b128 v[52:55], %[pa] offset:0\n\t" \
    "ds_read_b128 v[56:59], %[pa] offset:1024\n\t" \
    "ds_read_b128 v[60:63], %[pa] offset:2048\n\t" \
    "ds_read_b128 v[64:67], %[pa] offset:3072\n\t" \
    "s_add_u32 %[b], %[oo], 0x1000\n\t" \
    "s_waitcnt lgkmcnt(0)\n\t" \
    "v_add_f32 v196, v196, v52\n\t" \
    "v_add_f32 v197, v197, v53\n\t" \
    "v_add_f32 v198, v198, v54\n\t" \
    "v_add_f32 v199, v199, v55\n\t" \
    "v_add_f32 v200, v200, v56\n\t" \
    "v_add_f32 v201, v201, v57\n\t" \
    "v_add_f32 v202, v202, v58\n\t" \
    "v_add_f32 v203, v203, v59\n\t" \
    "v_add_f32 v204, v204, v60\n\t" \
    "v_add_f32 v205, v205, v61\n\t" \
    "v_add_f32 v206, v206, v62\n\t" \
    "v_add_f32 v207, v207, v63\n\t" \
    "v_add_f32 v208, v208, v64\n\t" \
    "v_add_f32 v209, v209, v65\n\t" \
    "v_add_f32 v210, v210, v66\n\t" \
    "v_add_f32 v211, v211, v67\n\t" \
    "v_cvt_pk_bf16_f32 v52, v196, v197\n\t" \
    "v_cvt_pk_bf16_f32 v53, v198, v199\n\t" \
    "v_cvt_pk_bf16_f32 v54, v204, v205\n\t" \
    "v_cvt_pk_bf16_f32 v55, v206, v207\n\t" \
    "v_cvt_pk_bf16_f32 v56, v200, v201\n\t" \
    "v_cvt_pk_bf16_f32 v57, v202, v203\n\t" \
    "v_cvt_pk_bf16_f32 v58, v208, v209\n\t" \
    "v_cvt_pk_bf16_f32 v59, v210, v211\n\t" \
    "buffer_store_dwordx4 v[52:55], %[l16], %[rs], %[oo] offen sc1\n\t" \
    "buffer_store_dwordx4 v[56:59], %[l16], %[rs], %[oo] offen offset:1024 sc1\n\t" \
    "E_%=:\n\t" \
    "s_setprio 0\n\t" \
    "s_waitcnt lgkmcnt(0)\n\t" \
    "s_nop 7\n\t" \
    : [t] "+s"(t), [sx] "+s"(sx), [oo] "+s"(oo), [a] "=&s"(tmp), [b] "=&s"(tmp2), [em] "=&s"(em), "+{v[20:27]}"(xf[0]), "+{v[28:35]}"(xf[1]), "+{v[36:43]}"(xf[2]), "+{v[196:203]}"(ac[0]), "+{v[204:211]}"(ac[1]) \
    : [te] "s"(TE_), [fin] "s"(FIN_), [kh] "s"(kh), [sd] "s"(sd), [rs] "s"(rs), [l16] "v"(l16), [xp] "v"(xp), [pa] "v"(pa), [fm] "s"(fm), [pm] "s"(pm), [fo] "s"(fo), [po] "s"(po), [pd] "s"(pd), [tag] "s"(P.tag), "{v[68:75]}"(w[0][0]), "{v[76:83]}"(w[0][1]), "{v[84:91]}"(w[1][0]), "{v[92:99]}"(w[1][1]), "{v[100:107]}"(w[2][0]), "{v[108:115]}"(w[2][1]), "{v[116:123]}"(w[3][0]), "{v[124:131]}"(w[3][1]), "{v[132:139]}"(w[4][0]), "{v[140:147]}"(w[4][1]), "{v[148:155]}"(w[5][0]), "{v[156:163]}"(w[5][1]), "{v[164:171]}"(w[6][0]), "{v[172:179]}"(w[6][1]), "{v[180:187]}"(w[7][0]), "{v[188:195]}"(w[7][1]) \
    : "memory", "scc", "v0", "v1", "v2", "v3", "v4", "v5", "v6", "v7", "v8", "v9", "v10", "v11", "v12", "v13", "v14", "v15", "v44", "v45", "v46", "v47", "v48", "v49", "v50", "v51", "v52", "v53", "v54", "v55", "v56", "v57", "v58", "v59", "v60", "v61", "v62", "v63", "v64", "v65", "v66", "v67")

#define S_LOOP_X(TE_, FIN_) \
  asm volatile( \
    "s_cmp_eq_u32 %[kh], 0\n\t" \
    "s_cbranch_scc0 L1_%=\n\t" \
    "L0_%=:\n\t" \
    "s_setprio 0\n\t" \
    "s_nop 3\n\t" \
    "ds_write_b128 %[pa], v[196:199] offset:0\n\t" \
    "ds_write_b128 %[pa], v[200:203] offset:1024\n\t" \
    "ds_write_b128 %[pa], v[204:207] offset:2048\n\t" \
    "ds_write_b128 %[pa], v[208:211] offset:3072\n\t" \
    "s_sub_u32 %[a], %[t], 1\n\t" \
    "v_mov_b32 v53, %[tag]\n\t" \
    "v_mov_b32 v52, %[a]\n\t" \
    "s_sub_u32 %[b], %[t], 2\n\t" \
    "s_cmp_lt_u32 %[b], " STR(SQUICK) "\n\t" \
    "s_cselect_b64 exec, %[fm], 0\n\t" \
    "buffer_store_dwordx2 v[52:53], %[l16], %[rs], %[fo] offen sc1\n\t" \
    "s_mov_b64 exec, -1\n\t" \
    "s_add_u32 m0, %[sd], 49152\n\t" \
    "s_add_u32 %[a], %[sx], 278528\n\t" \
    "s_waitcnt lgkmcnt(0)\n\t" \
    "buffer_load_dwordx4 %[l16], %[rs], %[a] offen offset:0 sc1 lds\n\t" \
    "buffer_load_dwordx4 %[l16], %[rs], %[a] offen offset:1024 sc1 lds\n\t" \
    "buffer_load_dwordx4 %[l16], %[rs], %[a] offen offset:2048 sc1 lds\n\t" \
    "buffer_load_dwordx4 %[l16], %[rs], %[a] offen offset:3072 sc1 lds\n\t" \
    "v_mfma_f32_16x16x32_bf16 v[0:3], v[68:71], v[20:23], 0\n\t" \
    "ds_read_b128 v[44:47], %[xp] offset:6144\n\t" \
    "v_mfma_f32_16x16x32_bf16 v[4:7], v[76:79], v[20:23], 0\n\t" \
    "ds_read_b128 v[48:51], %[xp] offset:7168\n\t" \
    "v_mfma_f32_16x16x32_bf16 v[8:11], v[68:71], v[24:27], 0\n\t" \
    "v_mfma_f32_16x16x32_bf16 v[12:15], v[76:79], v[24:27], 0\n\t" \
    "v_mfma_f32_16x16x32_bf16 v[0:3], v[72:75], v[28:31], v[0:3]\n\t" \
    "ds_read_b128 v[20:23], %[xp] offset:8192\n\t" \
    "v_mfma_f32_16x16x32_bf16 v[4:7], v[80:83], v[28:31], v[4:7]\n\t" \
    "ds_read_b128 v[24:27], %[xp] offset:9216\n\t" \
    "v_mfma_f32_16x16x32_bf16 v[8:11], v[72:75], v[32:35], v[8:11]\n\t" \
    "v_mfma_f32_16x16x32_bf16 v[12:15], v[80:83], v[32:35], v[12:15]\n\t" \
    "v_mfma_f32_16x16x32_bf16 v[0:3], v[84:87], v[36:39], v[0:3]\n\t" \
    "ds_read_b128 v[28:31], %[xp] offset:10240\n\t" \
    "v_mfma_f32_16x16x32_bf16 v[4:7], v[92:95], v[36:39], v[4:7]\n\t" \
    "ds_read_b128 v[32:35], %[xp] offset:11264\n\t" \
    "v_mfma_f32_16x16x32_bf16 v[8:11], v[84:87], v[40:43], v[8:11]\n\t" \
    "s_waitcnt lgkmcnt(4)\n\t" \
    "v_mfma_f32_16x16x32_bf16 v[12:15], v[92:95], v[40:43], v[12:15]\n\t" \
    "v_mfma_f32_16x16x32_bf16 v[0:3], v[88:91], v[44:47], v[0:3]\n\t" \
    "ds_read_b128 v[36:39], %[xp] offset:12288\n\t" \
    "v_mfma_f32_16x16x32_bf16 v[4:7], v[96:99], v[44:47], v[4:7]\n\t" \
    "ds_read_b128 v[40:43], %[xp] offset:13312\n\t" \
    "v_mfma_f32_16x16x32_bf16 v[8:11], v[88:91], v[48:51], v[8:11]\n\t" \
    "s_waitcnt lgkmcnt(4)\n\t" \
    "v_mfma_f32_16x16x32_bf16 v[12:15], v[96:99], v[48:51], v[12:15]\n\t" \
    "v_mfma_f32_16x16x32_bf16 v[0:3], v[100:103], v[20:23], v[0:3]\n\t" \
    "ds_read_b128 v[44:47], %[xp] offset:14336\n\t" \
    "v_mfma_f32_16x16x32_bf16 v[4:7], v[108:111], v[20:23], v[4:7]\n\t" \
    "ds_read_b128 v[48:51], %[xp] offset:15360\n\t" \
    "v_mfma_f32_16x16x32_bf16 v[8:11], v[100:103], v[24:27], v[8:11]\n\t" \
    "s_waitcnt lgkmcnt(4)\n\t" \
    "v_mfma_f32_16x16x32_bf16 v[12:15], v[108:111], v[24:27], v[12:15]\n\t" \
    "v_mfma_f32_16x16x32_bf16 v[0:3], v[104:107], v[28:31], v[0:3]\n\t" \
    "ds_read_b128 v[20:23], %[xp] offset:16384\n\t" \
    "v_mfma_f32_16x16x32_bf16 v[4:7], v[112:115], v[28:31], v[4:7]\n\t" \
    "ds_read_b128 v[24:27], %[xp] offset:17408\n\t" \
    "v_mfma_f32_16x16x32_bf16 v[8:11], v[104:107], v[32:35], v[8:11]\n\t" \
    "s_waitcnt lgkmcnt(4)\n\t" \
    "v_mfma_f32_16x16x32_bf16 v[12:15], v[112:115], v[32:35], v[12:15]\n\t" \
    "v_mfma_f32_16x16x32_bf16 v[0:3], v[116:119], v[36:39], v[0:3]\n\t" \
    "ds_read_b128 v[28:31], %[xp] offset:18432\n\t" \
    "v_mfma_f32_16x16x32_bf16 v[4:7], v[124:127], v[36:39], v[4:7]\n\t" \
    "ds_read_b128 v[32:35], %[xp] offset:19456\n\t" \
    "v_mfma_f32_16x16x32_bf16 v[8:11], v[116:119], v[40:43], v[8:11]\n\t" \
    "s_waitcnt lgkmcnt(4)\n\t" \
    "v_mfma_f32_16x16x32_bf16 v[12:15], v[124:127], v[40:43], v[12:15]\n\t" \
    "v_mfma_f32_16x16x32_bf16 v[0:3], v[120:123], v[44:47], v[0:3]\n\t" \
    "ds_read_b128 v[36:39], %[xp] offset:20480\n\t" \
    "v_mfma_f32_16x16x32_bf16 v[4:7], v[128:131], v[44:47], v[4:7]\n\t" \
    "ds_read_b128 v[40:43], %[xp] offset:21504\n\t" \
    "v_mfma_f32_16x16x32_bf16 v[8:11], v[120:123], v[48:51], v[8:11]\n\t" \
    "s_waitcnt lgkmcnt(4)\n\t" \
    "v_mfma_f32_16x16x32_bf16 v[12:15], v[128:131], v[48:51], v[12:15]\n\t" \
    "s_waitcnt vmcnt(4)\n\t" \
    "s_barrier\n\t" \
    "s_setprio 0\n\t" \
    "s_sub_u32 %[a], %[t], 1\n\t" \
    "v_mov_b32 v53, %[tag]\n\t" \
    "v_mov_b32 v52, %[a]\n\t" \
    "s_cmp_gt_u32 %[t], 1\n\t" \
    "s_cselect_b64 exec, %[fm], 0\n\t" \
    "buffer_store_dwordx2 v[52:53], %[l16], %[rs], %[fo] offen sc1\n\t" \
    "s_mov_b64 exec, %[pm]\n\t" \
    "s_mov_b32 m0, %[pd]\n\t" \
    "s_add_u32 %[a], %[sx], %[po]\n\t" \
    "buffer_load_dwordx4 %[l16], %[rs], %[a] offen sc1 lds\n\t" \
    "s_mov_b64 exec, -1\n\t" \
    "s_add_u32 m0, %[sd], 0\n\t" \
    "s_add_u32 %[a], %[sx], 524288\n\t" \
    "buffer_load_dwordx4 %[l16], %[rs], %[a] offen offset:0 sc1 lds\n\t" \
    "buffer_load_dwordx4 %[l16], %[rs], %[a] offen offset:1024 sc1 lds\n\t" \
    "buffer_load_dwordx4 %[l16], %[rs], %[a] offen offset:2048 sc1 lds\n\t" \
    "buffer_load_dwordx4 %[l16], %[rs], %[a] offen offset:3072 sc1 lds\n\t" \
    "v_mfma_f32_16x16x32_bf16 v[0:3], v[132:135], v[20:23], v[0:3]\n\t" \
    "ds_read_b128 v[44:47], %[xp] offset:22528\n\t" \
    "v_mfma_f32_16x16x32_bf16 v[4:7], v[140:143], v[20:23], v[4:7]\n\t" \
    "ds_read_b128 v[48:51], %[xp] offset:23552\n\t" \
    "v_mfma_f32_16x16x32_bf16 v[8:11], v[132:135], v[24:27], v[8:11]\n\t" \
    "s_waitcnt lgkmcnt(4)\n\t" \
    "v_mfma_f32_16x16x32_bf16 v[12:15], v[140:143], v[24:27], v[12:15]\n\t" \
    "v_mfma_f32_16x16x32_bf16 v[0:3], v[136:139], v[28:31], v[0:3]\n\t" \
    "ds_read_b128 v[20:23], %[xp] offset:24576\n\t" \
    "v_mfma_f32_16x16x32_bf16 v[4:7], v[144:147], v[28:31], v[4:7]\n\t" \
    "ds_read_b128 v[24:27], %[xp] offset:25600\n\t" \
    "v_mfma_f32_16x16x32_bf16 v[8:11], v[136:139], v[32:35], v[8:11]\n\t" \
    "s_waitcnt lgkmcnt(4)\n\t" \
    "v_mfma_f32_16x16x32_bf16 v[12:15], v[144:147], v[32:35], v[12:15]\n\t" \
    "v_mfma_f32_16x16x32_bf16 v[0:3], v[148:151], v[36:39], v[0:3]\n\t" \
    "ds_read_b128 v[28:31], %[xp] offset:26624\n\t" \
    "v_mfma_f32_16x16x32_bf16 v[4:7], v[156:159], v[36:39], v[4:7]\n\t" \
    "ds_read_b128 v[32:35], %[xp] offset:27648\n\t" \
    "v_mfma_f32_16x16x32_bf16 v[8:11], v[148:151], v[40:43], v[8:11]\n\t" \
    "s_waitcnt lgkmcnt(4)\n\t" \
    "v_mfma_f32_16x16x32_bf16 v[12:15], v[156:159], v[40:43], v[12:15]\n\t" \
    "v_mfma_f32_16x16x32_bf16 v[0:3], v[152:155], v[44:47], v[0:3]\n\t" \
    "ds_read_b128 v[36:39], %[xp] offset:28672\n\t" \
    "v_mfma_f32_16x16x32_bf16 v[4:7], v[160:163], v[44:47], v[4:7]\n\t" \
    "ds_read_b128 v[40:43], %[xp] offset:29696\n\t" \
    "v_mfma_f32_16x16x32_bf16 v[8:11], v[152:155], v[48:51], v[8:11]\n\t" \
    "s_waitcnt lgkmcnt(4)\n\t" \
    "v_mfma_f32_16x16x32_bf16 v[12:15], v[160:163], v[48:51], v[12:15]\n\t" \
    "v_mfma_f32_16x16x32_bf16 v[0:3], v[164:167], v[20:23], v[0:3]\n\t" \
    "ds_read_b128 v[44:47], %[xp] offset:30720\n\t" \
    "v_mfma_f32_16x16x32_bf16 v[4:7], v[172:175], v[20:23], v[4:7]\n\t" \
    "ds_read_b128 v[48:51], %[xp] offset:31744\n\t" \
    "v_mfma_f32_16x16x32_bf16 v[8:11], v[164:167], v[24:27], v[8:11]\n\t" \
    "s_waitcnt lgkmcnt(4)\n\t" \
    "v_mfma_f32_16x16x32_bf16 v[12:15], v[172:175], v[24:27], v[12:15]\n\t" \
    "v_mfma_f32_16x16x32_bf16 v[0:3], v[168:171], v[28:31], v[0:3]\n\t" \
    "ds_read_b128 v[20:23], %[xp] offset:32768\n\t" \
    "v_mfma_f32_16x16x32_bf16 v[4:7], v[176:179], v[28:31], v[4:7]\n\t" \
    "ds_read_b128 v[24:27], %[xp] offset:33792\n\t" \
    "v_mfma_f32_16x16x32_bf16 v[8:11], v[168:171], v[32:35], v[8:11]\n\t" \
    "s_waitcnt lgkmcnt(4)\n\t" \
    "v_mfma_f32_16x16x32_bf16 v[12:15], v[176:179], v[32:35], v[12:15]\n\t" \
    "v_mfma_f32_16x16x32_bf16 v[0:3], v[180:183], v[36:39], v[0:3]\n\t" \
    "ds_read_b128 v[28:31], %[xp] offset:34816\n\t" \
    "v_mfma_f32_16x16x32_bf16 v[4:7], v[188:191], v[36:39], v[4:7]\n\t" \
    "ds_read_b128 v[32:35], %[xp] offset:35840\n\t" \
    "v_mfma_f32_16x16x32_bf16 v[8:11], v[180:183], v[40:43], v[8:11]\n\t" \
    "s_waitcnt lgkmcnt(4)\n\t" \
    "v_mfma_f32_16x16x32_bf16 v[12:15], v[188:191], v[40:43], v[12:15]\n\t" \
    "v_mfma_f32_16x16x32_bf16 v[0:3], v[184:187], v[44:47], v[0:3]\n\t" \
    "ds_read_b128 v[36:39], %[xp] offset:36864\n\t" \
    "v_mfma_f32_16x16x32_bf16 v[4:7], v[192:195], v[44:47], v[4:7]\n\t" \
    "ds_read_b128 v[40:43], %[xp] offset:37888\n\t" \
    "v_mfma_f32_16x16x32_bf16 v[8:11], v[184:187], v[48:51], v[8:11]\n\t" \
    "s_waitcnt lgkmcnt(4)\n\t" \
    "v_mfma_f32_16x16x32_bf16 v[12:15], v[192:195], v[48:51], v[12:15]\n\t" \
    "s_add_u32 %[sx], %[sx], 262144\n\t" \
    "s_add_u32 %[t], %[t], 1\n\t" \
    "s_waitcnt vmcnt(4)\n\t" \
    "s_barrier\n\t" \
    "s_setprio 0\n\t" \
    "s_nop 3\n\t" \
    "ds_write_b128 %[pa], v[0:3] offset:0\n\t" \
    "ds_write_b128 %[pa], v[4:7] offset:1024\n\t" \
    "ds_write_b128 %[pa], v[8:11] offset:2048\n\t" \
    "ds_write_b128 %[pa], v[12:15] offset:3072\n\t" \
    "s_sub_u32 %[a], %[t], 1\n\t" \
    "v_mov_b32 v53, %[tag]\n\t" \
    "v_mov_b32 v52, %[a]\n\t" \
    "s_sub_u32 %[b], %[t], 2\n\t" \
    "s_cmp_lt_u32 %[b], " STR(SQUICK) "\n\t" \
    "s_cselect_b64 exec, %[fm], 0\n\t" \
    "buffer_store_dwordx2 v[52:53], %[l16], %[rs], %[fo] offen sc1\n\t" \
    "s_mov_b64 exec, -1\n\t" \
    "s_add_u32 m0, %[sd], 16384\n\t" \
    "s_add_u32 %[a], %[sx], 278528\n\t" \
    "s_waitcnt lgkmcnt(0)\n\t" \
    "buffer_load_dwordx4 %[l16], %[rs], %[a] offen offset:0 sc1 lds\n\t" \
    "buffer_load_dwordx4 %[l16], %[rs], %[a] offen offset:1024 sc1 lds\n\t" \
    "buffer_load_dwordx4 %[l16], %[rs], %[a] offen offset:2048 sc1 lds\n\t" \
    "buffer_load_dwordx4 %[l16], %[rs], %[a] offen offset:3072 sc1 lds\n\t" \
    "v_mfma_f32_16x16x32_bf16 v[196:199], v[68:71], v[20:23], 0\n\t" \
    "ds_read_b128 v[44:47], %[xp] offset:38912\n\t" \
    "v_mfma_f32_16x16x32_bf16 v[200:203], v[76:79], v[20:23], 0\n\t" \
    "ds_read_b128 v[48:51], %[xp] offset:39936\n\t" \
    "v_mfma_f32_16x16x32_bf16 v[204:207], v[68:71], v[24:27], 0\n\t" \
    "v_mfma_f32_16x16x32_bf16 v[208:211], v[76:79], v[24:27], 0\n\t" \
    "v_mfma_f32_16x16x32_bf16 v[196:199], v[72:75], v[28:31], v[196:199]\n\t" \
    "ds_read_b128 v[20:23], %[xp] offset:40960\n\t" \
    "v_mfma_f32_16x16x32_bf16 v[200:203], v[80:83], v[28:31], v[200:203]\n\t" \
    "ds_read_b128 v[24:27], %[xp] offset:41984\n\t" \
    "v_mfma_f32_16x16x32_bf16 v[204:207], v[72:75], v[32:35], v[204:207]\n\t" \
    "v_mfma_f32_16x16x32_bf16 v[208:211], v[80:83], v[32:35], v[208:211]\n\t" \
    "v_mfma_f32_16x16x32_bf16 v[196:199], v[84:87], v[36:39], v[196:199]\n\t" \
    "ds_read_b128 v[28:31], %[xp] offset:43008\n\t" \
    "v_mfma_f32_16x16x32_bf16 v[200:203], v[92:95], v[36:39], v[200:203]\n\t" \
    "ds_read_b128 v[32:35], %[xp] offset:44032\n\t" \
    "v_mfma_f32_16x16x32_bf16 v[204:207], v[84:87], v[40:43], v[204:207]\n\t" \
    "s_waitcnt lgkmcnt(4)\n\t" \
    "v_mfma_f32_16x16x32_bf16 v[208:211], v[92:95], v[40:43], v[208:211]\n\t" \
    "v_mfma_f32_16x16x32_bf16 v[196:199], v[88:91], v[44:47], v[196:199]\n\t" \
    "ds_read_b128 v[36:39], %[xp] offset:45056\n\t" \
    "v_mfma_f32_16x16x32_bf16 v[200:203], v[96:99], v[44:47], v[200:203]\n\t" \
    "ds_read_b128 v[40:43], %[xp] offset:46080\n\t" \
    "v_mfma_f32_16x16x32_bf16 v[204:207], v[88:91], v[48:51], v[204:207]\n\t" \
    "s_waitcnt lgkmcnt(4)\n\t" \
    "v_mfma_f32_16x16x32_bf16 v[208:211], v[96:99], v[48:51], v[208:211]\n\t" \
    "v_mfma_f32_16x16x32_bf16 v[196:199], v[100:103], v[20:23], v[196:199]\n\t" \
    "ds_read_b128 v[44:47], %[xp] offset:47104\n\t" \
    "v_mfma_f32_16x16x32_bf16 v[200:203], v[108:111], v[20:23], v[200:203]\n\t" \
    "ds_read_b128 v[48:51], %[xp] offset:48128\n\t" \
    "v_mfma_f32_16x16x32_bf16 v[204:207], v[100:103], v[24:27], v[204:207]\n\t" \
    "s_waitcnt lgkmcnt(4)\n\t" \
    "v_mfma_f32_16x16x32_bf16 v[208:211], v[108:111], v[24:27], v[208:211]\n\t" \
    "v_mfma_f32_16x16x32_bf16 v[196:199], v[104:107], v[28:31], v[196:199]\n\t" \
    "ds_read_b128 v[20:23], %[xp] offset:49152\n\t" \
    "v_mfma_f32_16x16x32_bf16 v[200:203], v[112:115], v[28:31], v[200:203]\n\t" \
    "ds_read_b128 v[24:27], %[xp] offset:50176\n\t" \
    "v_mfma_f32_16x16x32_bf16 v[204:207], v[104:107], v[32:35], v[204:207]\n\t" \
    "s_waitcnt lgkmcnt(4)\n\t" \
    "v_mfma_f32_16x16x32_bf16 v[208:211], v[112:115], v[32:35], v[208:211]\n\t" \
    "v_mfma_f32_16x16x32_bf16 v[196:199], v[116:119], v[36:39], v[196:199]\n\t" \
    "ds_read_b128 v[28:31], %[xp] offset:51200\n\t" \
    "v_mfma_f32_16x16x32_bf16 v[200:203], v[124:127], v[36:39], v[200:203]\n\t" \
    "ds_read_b128 v[32:35], %[xp] offset:52224\n\t" \
    "v_mfma_f32_16x16x32_bf16 v[204:207], v[116:119], v[40:43], v[204:207]\n\t" \
    "s_waitcnt lgkmcnt(4)\n\t" \
    "v_mfma_f32_16x16x32_bf16 v[208:211], v[124:127], v[40:43], v[208:211]\n\t" \
    "v_mfma_f32_16x16x32_bf16 v[196:199], v[120:123], v[44:47], v[196:199]\n\t" \
    "ds_read_b128 v[36:39], %[xp] offset:53248\n\t" \
    "v_mfma_f32_16x16x32_bf16 v[200:203], v[128:131], v[44:47], v[200:203]\n\t" \
    "ds_read_b128 v[40:43], %[xp] offset:54272\n\t" \
    "v_mfma_f32_16x16x32_bf16 v[204:207], v[120:123], v[48:51], v[204:207]\n\t" \
    "s_waitcnt lgkmcnt(4)\n\t" \
    "v_mfma_f32_16x16x32_bf16 v[208:211], v[128:131], v[48:51], v[208:211]\n\t" \
    "s_waitcnt vmcnt(4)\n\t" \
    "s_barrier\n\t" \
    "s_setprio 0\n\t" \
    "s_sub_u32 %[a], %[t], 1\n\t" \
    "v_mov_b32 v53, %[tag]\n\t" \
    "v_mov_b32 v52, %[a]\n\t" \
    "s_cmp_gt_u32 %[t], 1\n\t" \
    "s_cselect_b64 exec, %[fm], 0\n\t" \
    "buffer_store_dwordx2 v[52:53], %[l16], %[rs], %[fo] offen sc1\n\t" \
    "s_mov_b64 exec, %[pm]\n\t" \
    "s_mov_b32 m0, %[pd]\n\t" \
    "s_add_u32 %[a], %[sx], %[po]\n\t" \
    "buffer_load_dwordx4 %[l16], %[rs], %[a] offen sc1 lds\n\t" \
    "s_mov_b64 exec, -1\n\t" \
    "s_add_u32 m0, %[sd], 32768\n\t" \
    "s_add_u32 %[a], %[sx], 524288\n\t" \
    "buffer_load_dwordx4 %[l16], %[rs], %[a] offen offset:0 sc1 lds\n\t" \
    "buffer_load_dwordx4 %[l16], %[rs], %[a] offen offset:1024 sc1 lds\n\t" \
    "buffer_load_dwordx4 %[l16], %[rs], %[a] offen offset:2048 sc1 lds\n\t" \
    "buffer_load_dwordx4 %[l16], %[rs], %[a] offen offset:3072 sc1 lds\n\t" \
    "v_mfma_f32_16x16x32_bf16 v[196:199], v[132:135], v[20:23], v[196:199]\n\t" \
    "ds_read_b128 v[44:47], %[xp] offset:55296\n\t" \
    "v_mfma_f32_16x16x32_bf16 v[200:203], v[140:143], v[20:23], v[200:203]\n\t" \
    "ds_read_b128 v[48:51], %[xp] offset:56320\n\t" \
    "v_mfma_f32_16x16x32_bf16 v[204:207], v[132:135], v[24:27], v[204:207]\n\t" \
    "s_waitcnt lgkmcnt(4)\n\t" \
    "v_mfma_f32_16x16x32_bf16 v[208:211], v[140:143], v[24:27], v[208:211]\n\t" \
    "v_mfma_f32_16x16x32_bf16 v[196:199], v[136:139], v[28:31], v[196:199]\n\t" \
    "ds_read_b128 v[20:23], %[xp] offset:57344\n\t" \
    "v_mfma_f32_16x16x32_bf16 v[200:203], v[144:147], v[28:31], v[200:203]\n\t" \
    "ds_read_b128 v[24:27], %[xp] offset:58368\n\t" \
    "v_mfma_f32_16x16x32_bf16 v[204:207], v[136:139], v[32:35], v[204:207]\n\t" \
    "s_waitcnt lgkmcnt(4)\n\t" \
    "v_mfma_f32_16x16x32_bf16 v[208:211], v[144:147], v[32:35], v[208:211]\n\t" \
    "v_mfma_f32_16x16x32_bf16 v[196:199], v[148:151], v[36:39], v[196:199]\n\t" \
    "ds_read_b128 v[28:31], %[xp] offset:59392\n\t" \
    "v_mfma_f32_16x16x32_bf16 v[200:203], v[156:159], v[36:39], v[200:203]\n\t" \
    "ds_read_b128 v[32:35], %[xp] offset:60416\n\t" \
    "v_mfma_f32_16x16x32_bf16 v[204:207], v[148:151], v[40:43], v[204:207]\n\t" \
    "s_waitcnt lgkmcnt(4)\n\t" \
    "v_mfma_f32_16x16x32_bf16 v[208:211], v[156:159], v[40:43], v[208:211]\n\t" \
    "v_mfma_f32_16x16x32_bf16 v[196:199], v[152:155], v[44:47], v[196:199]\n\t" \
    "ds_read_b128 v[36:39], %[xp] offset:61440\n\t" \
    "v_mfma_f32_16x16x32_bf16 v[200:203], v[160:163], v[44:47], v[200:203]\n\t" \
    "ds_read_b128 v[40:43], %[xp] offset:62464\n\t" \
    "v_mfma_f32_16x16x32_bf16 v[204:207], v[152:155], v[48:51], v[204:207]\n\t" \
    "s_waitcnt lgkmcnt(4)\n\t" \
    "v_mfma_f32_16x16x32_bf16 v[208:211], v[160:163], v[48:51], v[208:211]\n\t" \
    "v_mfma_f32_16x16x32_bf16 v[196:199], v[164:167], v[20:23], v[196:199]\n\t" \
    "ds_read_b128 v[44:47], %[xp] offset:63488\n\t" \
    "v_mfma_f32_16x16x32_bf16 v[200:203], v[172:175], v[20:23], v[200:203]\n\t" \
    "ds_read_b128 v[48:51], %[xp] offset:64512\n\t" \
    "v_mfma_f32_16x16x32_bf16 v[204:207], v[164:167], v[24:27], v[204:207]\n\t" \
    "s_waitcnt lgkmcnt(4)\n\t" \
    "v_mfma_f32_16x16x32_bf16 v[208:211], v[172:175], v[24:27], v[208:211]\n\t" \
    "v_mfma_f32_16x16x32_bf16 v[196:199], v[168:171], v[28:31], v[196:199]\n\t" \
    "ds_read_b128 v[20:23], %[xp] offset:0\n\t" \
    "v_mfma_f32_16x16x32_bf16 v[200:203], v[176:179], v[28:31], v[200:203]\n\t" \
    "ds_read_b128 v[24:27], %[xp] offset:1024\n\t" \
    "v_mfma_f32_16x16x32_bf16 v[204:207], v[168:171], v[32:35], v[204:207]\n\t" \
    "s_waitcnt lgkmcnt(4)\n\t" \
    "v_mfma_f32_16x16x32_bf16 v[208:211], v[176:179], v[32:35], v[208:211]\n\t" \
    "v_mfma_f32_16x16x32_bf16 v[196:199], v[180:183], v[36:39], v[196:199]\n\t" \
    "ds_read_b128 v[28:31], %[xp] offset:2048\n\t" \
    "v_mfma_f32_16x16x32_bf16 v[200:203], v[188:191], v[36:39], v[200:203]\n\t" \
    "ds_read_b128 v[32:35], %[xp] offset:3072\n\t" \
    "v_mfma_f32_16x16x32_bf16 v[204:207], v[180:183], v[40:43], v[204:207]\n\t" \
    "s_waitcnt lgkmcnt(4)\n\t" \
    "v_mfma_f32_16x16x32_bf16 v[208:211], v[188:191], v[40:43], v[208:211]\n\t" \
    "v_mfma_f32_16x16x32_bf16 v[196:199], v[184:187], v[44:47], v[196:199]\n\t" \
    "ds_read_b128 v[36:39], %[xp] offset:4096\n\t" \
    "v_mfma_f32_16x16x32_bf16 v[200:203], v[192:195], v[44:47], v[200:203]\n\t" \
    "ds_read_b128 v[40:43], %[xp] offset:5120\n\t" \
    "v_mfma_f32_16x16x32_bf16 v[204:207], v[184:187], v[48:51], v[204:207]\n\t" \
    "s_waitcnt lgkmcnt(4)\n\t" \
    "v_mfma_f32_16x16x32_bf16 v[208:211], v[192:195], v[48:51], v[208:211]\n\t" \
    "s_add_u32 %[sx], %[sx], 262144\n\t" \
    "s_add_u32 %[t], %[t], 1\n\t" \
    "s_waitcnt vmcnt(4)\n\t" \
    "s_barrier\n\t" \
    "s_cmp_lt_u32 %[t], %[te]\n\t" \
    "s_cbranch_scc1 L0_%=\n\t" \
    "s_cmp_eq_u32 %[fin], %[te]\n\t" \
    "s_cbranch_scc0 E_%=\n\t" \
    "s_nop 4\n\t" \
    "ds_write_b128 %[pa], v[196:199] offset:0\n\t" \
    "ds_write_b128 %[pa], v[200:203] offset:1024\n\t" \
    "ds_write_b128 %[pa], v[204:207] offset:2048\n\t" \
    "ds_write_b128 %[pa], v[208:211] offset:3072\n\t" \
    "s_waitcnt lgkmcnt(0)\n\t" \
    "s_barrier\n\t" \
    "s_branch E_%=\n\t" \
    "L1_%=:\n\t" \
    "s_setprio 1\n\t" \
    "s_add_u32 m0, %[sd], 49152\n\t" \
    "s_add_u32 %[a], %[sx], 278528\n\t" \
    "v_mfma_f32_16x16x32_bf16 v[0:3], v[68:71], v[20:23], 0\n\t" \
    "ds_read_b128 v[44:47], %[xp] offset:6144\n\t" \
    "v_mfma_f32_16x16x32_bf16 v[4:7], v[76:79], v[20:23], 0\n\t" \
    "ds_read_b128 v[48:51], %[xp] offset:7168\n\t" \
    "v_mfma_f32_16x16x32_bf16 v[8:11], v[68:71], v[24:27], 0\n\t" \
    "s_waitcnt lgkmcnt(4)\n\t" \
    "v_mfma_f32_16x16x32_bf16 v[12:15], v[76:79], v[24:27], 0\n\t" \
    "v_mfma_f32_16x16x32_bf16 v[0:3], v[72:75], v[28:31], v[0:3]\n\t" \
    "ds_read_b128 v[20:23], %[xp] offset:8192\n\t" \
    "v_mfma_f32_16x16x32_bf16 v[4:7], v[80:83], v[28:31], v[4:7]\n\t" \
    "ds_read_b128 v[24:27], %[xp] offset:9216\n\t" \
    "v_mfma_f32_16x16x32_bf16 v[8:11], v[72:75], v[32:35], v[8:11]\n\t" \
    "s_waitcnt lgkmcnt(4)\n\t" \
    "v_mfma_f32_16x16x32_bf16 v[12:15], v[80:83], v[32:35], v[12:15]\n\t" \
    "v_mfma_f32_16x16x32_bf16 v[0:3], v[84:87], v[36:39], v[0:3]\n\t" \
    "ds_read_b128 v[28:31], %[xp] offset:10240\n\t" \
    "v_mfma_f32_16x16x32_bf16 v[4:7], v[92:95], v[36:39], v[4:7]\n\t" \
    "ds_read_b128 v[32:35], %[xp] offset:11264\n\t" \
    "v_mfma_f32_16x16x32_bf16 v[8:11], v[84:87], v[40:43], v[8:11]\n\t" \
    "s_waitcnt lgkmcnt(4)\n\t" \
    "v_mfma_f32_16x16x32_bf16 v[12:15], v[92:95], v[40:43], v[12:15]\n\t" \
    "v_mfma_f32_16x16x32_bf16 v[0:3], v[88:91], v[44:47], v[0:3]\n\t" \
    "ds_read_b128 v[36:39], %[xp] offset:12288\n\t" \
    "v_mfma_f32_16x16x32_bf16 v[4:7], v[96:99], v[44:47], v[4:7]\n\t" \
    "ds_read_b128 v[40:43], %[xp] offset:13312\n\t" \
    "v_mfma_f32_16x16x32_bf16 v[8:11], v[88:91], v[48:51], v[8:11]\n\t" \
    "s_waitcnt lgkmcnt(4)\n\t" \
    "v_mfma_f32_16x16x32_bf16 v[12:15], v[96:99], v[48:51], v[12:15]\n\t" \
    "v_mfma_f32_16x16x32_bf16 v[0:3], v[100:103], v[20:23], v[0:3]\n\t" \
    "ds_read_b128 v[44:47], %[xp] offset:14336\n\t" \
    "v_mfma_f32_16x16x32_bf16 v[4:7], v[108:111], v[20:23], v[4:7]\n\t" \
    "ds_read_b128 v[48:51], %[xp] offset:15360\n\t" \
    "v_mfma_f32_16x16x32_bf16 v[8:11], v[100:103], v[24:27], v[8:11]\n\t" \
    "s_waitcnt lgkmcnt(4)\n\t" \
    "v_mfma_f32_16x16x32_bf16 v[12:15], v[108:111], v[24:27], v[12:15]\n\t" \
    "buffer_load_dwordx4 %[l16], %[rs], %[a] offen offset:0 sc1 lds\n\t" \
    "v_mfma_f32_16x16x32_bf16 v[0:3], v[104:107], v[28:31], v[0:3]\n\t" \
    "ds_read_b128 v[20:23], %[xp] offset:16384\n\t" \
    "v_mfma_f32_16x16x32_bf16 v[4:7], v[112:115], v[28:31], v[4:7]\n\t" \
    "ds_read_b128 v[24:27], %[xp] offset:17408\n\t" \
    "v_mfma_f32_16x16x32_bf16 v[8:11], v[104:107], v[32:35], v[8:11]\n\t" \
    "s_waitcnt lgkmcnt(4)\n\t" \
    "v_mfma_f32_16x16x32_bf16 v[12:15], v[112:115], v[32:35], v[12:15]\n\t" \
    "buffer_load_dwordx4 %[l16], %[rs], %[a] offen offset:1024 sc1 lds\n\t" \
    "v_mfma_f32_16x16x32_bf16 v[0:3], v[116:119], v[36:39], v[0:3]\n\t" \
    "ds_read_b128 v[28:31], %[xp] offset:18432\n\t" \
    "v_mfma_f32_16x16x32_bf16 v[4:7], v[124:127], v[36:39], v[4:7]\n\t" \
    "ds_read_b128 v[32:35], %[xp] offset:19456\n\t" \
    "v_mfma_f32_16x16x32_bf16 v[8:11], v[116:119], v[40:43], v[8:11]\n\t" \
    "s_waitcnt lgkmcnt(4)\n\t" \
    "v_mfma_f32_16x16x32_bf16 v[12:15], v[124:127], v[40:43], v[12:15]\n\t" \
    "buffer_load_dwordx4 %[l16], %[rs], %[a] offen offset:2048 sc1 lds\n\t" \
    "v_mfma_f32_16x16x32_bf16 v[0:3], v[120:123], v[44:47], v[0:3]\n\t" \
    "ds_read_b128 v[36:39], %[xp] offset:20480\n\t" \
    "v_mfma_f32_16x16x32_bf16 v[4:7], v[128:131], v[44:47], v[4:7]\n\t" \
    "ds_read_b128 v[40:43], %[xp] offset:21504\n\t" \
    "v_mfma_f32_16x16x32_bf16 v[8:11], v[120:123], v[48:51], v[8:11]\n\t" \
    "s_waitcnt lgkmcnt(4)\n\t" \
    "v_mfma_f32_16x16x32_bf16 v[12:15], v[128:131], v[48:51], v[12:15]\n\t" \
    "buffer_load_dwordx4 %[l16], %[rs], %[a] offen offset:3072 sc1 lds\n\t" \
    "s_waitcnt vmcnt(4)\n\t" \
    "s_barrier\n\t" \
    "s_setprio 1\n\t" \
    "s_add_u32 m0, %[sd], 0\n\t" \
    "s_add_u32 %[a], %[sx], 524288\n\t" \
    "s_add_u32 %[b], %[oo], 0x1000\n\t" \
    "s_cmp_lg_u32 %[t], 0\n\t" \
    "s_cselect_b64 %[em], -1, 0\n\t" \
    "v_mfma_f32_16x16x32_bf16 v[0:3], v[132:135], v[20:23], v[0:3]\n\t" \
    "ds_read_b128 v[44:47], %[xp] offset:22528\n\t" \
    "v_mfma_f32_16x16x32_bf16 v[4:7], v[140:143], v[20:23], v[4:7]\n\t" \
    "ds_read_b128 v[48:51], %[xp] offset:23552\n\t" \
    "v_mfma_f32_16x16x32_bf16 v[8:11], v[132:135], v[24:27], v[8:11]\n\t" \
    "ds_read_b128 v[52:55], %[pa] offset:0\n\t" \
    "s_waitcnt lgkmcnt(5)\n\t" \
    "v_mfma_f32_16x16x32_bf16 v[12:15], v[140:143], v[24:27], v[12:15]\n\t" \
    "buffer_load_dwordx4 %[l16], %[rs], %[a] offen offset:0 sc1 lds\n\t" \
    "v_mfma_f32_16x16x32_bf16 v[0:3], v[136:139], v[28:31], v[0:3]\n\t" \
    "ds_read_b128 v[20:23], %[xp] offset:24576\n\t" \
    "v_mfma_f32_16x16x32_bf16 v[4:7], v[144:147], v[28:31], v[4:7]\n\t" \
    "ds_read_b128 v[24:27], %[xp] offset:25600\n\t" \
    "v_mfma_f32_16x16x32_bf16 v[8:11], v[136:139], v[32:35], v[8:11]\n\t" \
    "ds_read_b128 v[56:59], %[pa] offset:1024\n\t" \
    "s_waitcnt lgkmcnt(6)\n\t" \
    "v_mfma_f32_16x16x32_bf16 v[12:15], v[144:147], v[32:35], v[12:15]\n\t" \
    "buffer_load_dwordx4 %[l16], %[rs], %[a] offen offset:1024 sc1 lds\n\t" \
    "v_mfma_f32_16x16x32_bf16 v[0:3], v[148:151], v[36:39], v[0:3]\n\t" \
    "ds_read_b128 v[28:31], %[xp] offset:26624\n\t" \
    "v_mfma_f32_16x16x32_bf16 v[4:7], v[156:159], v[36:39], v[4:7]\n\t" \
    "ds_read_b128 v[32:35], %[xp] offset:27648\n\t" \
    "v_mfma_f32_16x16x32_bf16 v[8:11], v[148:151], v[40:43], v[8:11]\n\t" \
    "ds_read_b128 v[60:63], %[pa] offset:2048\n\t" \
    "s_waitcnt lgkmcnt(7)\n\t" \
    "v_mfma_f32_16x16x32_bf16 v[12:15], v[156:159], v[40:43], v[12:15]\n\t" \
    "buffer_load_dwordx4 %[l16], %[rs], %[a] offen offset:2048 sc1 lds\n\t" \
    "v_mfma_f32_16x16x32_bf16 v[0:3], v[152:155], v[44:47], v[0:3]\n\t" \
    "ds_read_b128 v[36:39], %[xp] offset:28672\n\t" \
    "s_waitcnt lgkmcnt(7)\n\t" \
    "v_add_f32 v196, v196, v52\n\t" \
    "v_mfma_f32_16x16x32_bf16 v[4:7], v[160:163], v[44:47], v[4:7]\n\t" \
    "ds_read_b128 v[40:43], %[xp] offset:29696\n\t" \
    "v_add_f32 v197, v197, v53\n\t" \
    "v_mfma_f32_16x16x32_bf16 v[8:11], v[152:155], v[48:51], v[8:11]\n\t" \
    "ds_read_b128 v[64:67], %[pa] offset:3072\n\t" \
    "s_waitcnt lgkmcnt(7)\n\t" \
    "v_mfma_f32_16x16x32_bf16 v[12:15], v[160:163], v[48:51], v[12:15]\n\t" \
    "buffer_load_dwordx4 %[l16], %[rs], %[a] offen offset:3072 sc1 lds\n\t" \
    "v_add_f32 v198, v198, v54\n\t" \
    "v_add_f32 v199, v199, v55\n\t" \
    "v_mfma_f32_16x16x32_bf16 v[0:3], v[164:167], v[20:23], v[0:3]\n\t" \
    "ds_read_b128 v[44:47], %[xp] offset:30720\n\t" \
    "s_waitcnt lgkmcnt(7)\n\t" \
    "v_add_f32 v200, v200, v56\n\t" \
    "v_mfma_f32_16x16x32_bf16 v[4:7], v[172:175], v[20:23], v[4:7]\n\t" \
    "ds_read_b128 v[48:51], %[xp] offset:31744\n\t" \
    "v_add_f32 v201, v201, v57\n\t" \
    "v_mfma_f32_16x16x32_bf16 v[8:11], v[164:167], v[24:27], v[8:11]\n\t" \
    "v_add_f32 v202, v202, v58\n\t" \
    "s_waitcnt lgkmcnt(6)\n\t" \
    "v_mfma_f32_16x16x32_bf16 v[12:15], v[172:175], v[24:27], v[12:15]\n\t" \
    "v_add_f32 v203, v203, v59\n\t" \
    "s_waitcnt lgkmcnt(5)\n\t" \
    "v_add_f32 v204, v204, v60\n\t" \
    "v_add_f32 v205, v205, v61\n\t" \
    "v_mfma_f32_16x16x32_bf16 v[0:3], v[168:171], v[28:31], v[0:3]\n\t" \
    "ds_read_b128 v[20:23], %[xp] offset:32768\n\t" \
    "v_add_f32 v206, v206, v62\n\t" \
    "v_mfma_f32_16x16x32_bf16 v[4:7], v[176:179], v[28:31], v[4:7]\n\t" \
    "ds_read_b128 v[24:27], %[xp] offset:33792\n\t" \
    "v_add_f32 v207, v207, v63\n\t" \
    "v_mfma_f32_16x16x32_bf16 v[8:11], v[168:171], v[32:35], v[8:11]\n\t" \
    "s_waitcnt lgkmcnt(4)\n\t" \
    "v_add_f32 v208, v208, v64\n\t" \
    "v_mfma_f32_16x16x32_bf16 v[12:15], v[176:179], v[32:35], v[12:15]\n\t" \
    "v_add_f32 v209, v209, v65\n\t" \
    "v_add_f32 v210, v210, v66\n\t" \
    "v_add_f32 v211, v211, v67\n\t" \
    "v_mfma_f32_16x16x32_bf16 v[0:3], v[180:183], v[36:39], v[0:3]\n\t" \
    "ds_read_b128 v[28:31], %[xp] offset:34816\n\t" \
    "v_mfma_f32_16x16x32_bf16 v[4:7], v[188:191], v[36:39], v[4:7]\n\t" \
    "ds_read_b128 v[32:35], %[xp] offset:35840\n\t" \
    "s_mov_b64 exec, %[em]\n\t" \
    "buffer_store_dwordx4 v[196:199], %[l16], %[rs], %[oo] offen offset:0 sc1\n\t" \
    "s_mov_b64 exec, -1\n\t" \
    "v_mfma_f32_16x16x32_bf16 v[8:11], v[180:183], v[40:43], v[8:11]\n\t" \
    "s_waitcnt lgkmcnt(4)\n\t" \
    "v_mfma_f32_16x16x32_bf16 v[12:15], v[188:191], v[40:43], v[12:15]\n\t" \
    "s_mov_b64 exec, %[em]\n\t" \
    "buffer_store_dwordx4 v[200:203], %[l16], %[rs], %[oo] offen offset:1024 sc1\n\t" \
    "s_mov_b64 exec, -1\n\t" \
    "v_mfma_f32_16x16x32_bf16 v[0:3], v[184:187], v[44:47], v[0:3]\n\t" \
    "ds_read_b128 v[36:39], %[xp] offset:36864\n\t" \
    "v_mfma_f32_16x16x32_bf16 v[4:7], v[192:195], v[44:47], v[4:7]\n\t" \
    "ds_read_b128 v[40:43], %[xp] offset:37888\n\t" \
    "s_mov_b64 exec, %[em]\n\t" \
    "buffer_store_dwordx4 v[204:207], %[l16], %[rs], %[oo] offen offset:2048 sc1\n\t" \
    "s_mov_b64 exec, -1\n\t" \
    "v_mfma_f32_16x16x32_bf16 v[8:11], v[184:187], v[48:51], v[8:11]\n\t" \
    "s_waitcnt lgkmcnt(4)\n\t" \
    "v_mfma_f32_16x16x32_bf16 v[12:15], v[192:195], v[48:51], v[12:15]\n\t" \
    "s_mov_b64 exec, %[em]\n\t" \
    "buffer_store_dwordx4 v[208:211], %[l16], %[rs], %[oo] offen offset:3072 sc1\n\t" \
    "s_mov_b64 exec, -1\n\t" \
    "s_add_u32 %[sx], %[sx], 262144\n\t" \
    "s_add_u32 %[t], %[t], 1\n\t" \
    "s_add_u32 %[oo], %[oo], 524288\n\t" \
    "s_waitcnt vmcnt(8)\n\t" \
    "s_cmp_gt_u32 %[t], " STR(SQUICK) " + 1\n\t" \
    "s_cbranch_scc1 B0_%=\n\t" \
    "s_waitcnt vmcnt(0)\n\t" \
    "B0_%=:\n\t" \
    "s_barrier\n\t" \
    "s_setprio 1\n\t" \
    "s_add_u32 m0, %[sd], 16384\n\t" \
    "s_add_u32 %[a], %[sx], 278528\n\t" \
    "v_mfma_f32_16x16x32_bf16 v[196:199], v[68:71], v[20:23], 0\n\t" \
    "ds_read_b128 v[44:47], %[xp] offset:38912\n\t" \
    "v_mfma_f32_16x16x32_bf16 v[200:203], v[76:79], v[20:23], 0\n\t" \
    "ds_read_b128 v[48:51], %[xp] offset:39936\n\t" \
    "v_mfma_f32_16x16x32_bf16 v[204:207], v[68:71], v[24:27], 0\n\t" \
    "s_waitcnt lgkmcnt(4)\n\t" \
    "v_mfma_f32_16x16x32_bf16 v[208:211], v[76:79], v[24:27], 0\n\t" \
    "v_mfma_f32_16x16x32_bf16 v[196:199], v[72:75], v[28:31], v[196:199]\n\t" \
    "ds_read_b128 v[20:23], %[xp] offset:40960\n\t" \
    "v_mfma_f32_16x16x32_bf16 v[200:203], v[80:83], v[28:31], v[200:203]\n\t" \
    "ds_read_b128 v[24:27], %[xp] offset:41984\n\t" \
    "v_mfma_f32_16x16x32_bf16 v[204:207], v[72:75], v[32:35], v[204:207]\n\t" \
    "s_waitcnt lgkmcnt(4)\n\t" \
    "v_mfma_f32_16x16x32_bf16 v[208:211], v[80:83], v[32:35], v[208:211]\n\t" \
    "v_mfma_f32_16x16x32_bf16 v[196:199], v[84:87], v[36:39], v[196:199]\n\t" \
    "ds_read_b128 v[28:31], %[xp] offset:43008\n\t" \
    "v_mfma_f32_16x16x32_bf16 v[200:203], v[92:95], v[36:39], v[200:203]\n\t" \
    "ds_read_b128 v[32:35], %[xp] offset:44032\n\t" \
    "v_mfma_f32_16x16x32_bf16 v[204:207], v[84:87], v[40:43], v[204:207]\n\t" \
    "s_waitcnt lgkmcnt(4)\n\t" \
    "v_mfma_f32_16x16x32_bf16 v[208:211], v[92:95], v[40:43], v[208:211]\n\t" \
    "v_mfma_f32_16x16x32_bf16 v[196:199], v[88:91], v[44:47], v[196:199]\n\t" \
    "ds_read_b128 v[36:39], %[xp] offset:45056\n\t" \
    "v_mfma_f32_16x16x32_bf16 v[200:203], v[96:99], v[44:47], v[200:203]\n\t" \
    "ds_read_b128 v[40:43], %[xp] offset:46080\n\t" \
    "v_mfma_f32_16x16x32_bf16 v[204:207], v[88:91], v[48:51], v[204:207]\n\t" \
    "s_waitcnt lgkmcnt(4)\n\t" \
    "v_mfma_f32_16x16x32_bf16 v[208:211], v[96:99], v[48:51], v[208:211]\n\t" \
    "v_mfma_f32_16x16x32_bf16 v[196:199], v[100:103], v[20:23], v[196:199]\n\t" \
    "ds_read_b128 v[44:47], %[xp] offset:47104\n\t" \
    "v_mfma_f32_16x16x32_bf16 v[200:203], v[108:111], v[20:23], v[200:203]\n\t" \
    "ds_read_b128 v[48:51], %[xp] offset:48128\n\t" \
    "v_mfma_f32_16x16x32_bf16 v[204:207], v[100:103], v[24:27], v[204:207]\n\t" \
    "s_waitcnt lgkmcnt(4)\n\t" \
    "v_mfma_f32_16x16x32_bf16 v[208:211], v[108:111], v[24:27], v[208:211]\n\t" \
    "buffer_load_dwordx4 %[l16], %[rs], %[a] offen offset:0 sc1 lds\n\t" \
    "v_mfma_f32_16x16x32_bf16 v[196:199], v[104:107], v[28:31], v[196:199]\n\t" \
    "ds_read_b128 v[20:23], %[xp] offset:49152\n\t" \
    "v_mfma_f32_16x16x32_bf16 v[200:203], v[112:115], v[28:31], v[200:203]\n\t" \
    "ds_read_b128 v[24:27], %[xp] offset:50176\n\t" \
    "v_mfma_f32_16x16x32_bf16 v[204:207], v[104:107], v[32:35], v[204:207]\n\t" \
    "s_waitcnt lgkmcnt(4)\n\t" \
    "v_mfma_f32_16x16x32_bf16 v[208:211], v[112:115], v[32:35], v[208:211]\n\t" \
    "buffer_load_dwordx4 %[l16], %[rs], %[a] offen offset:1024 sc1 lds\n\t" \
    "v_mfma_f32_16x16x32_bf16 v[196:199], v[116:119], v[36:39], v[196:199]\n\t" \
    "ds_read_b128 v[28:31], %[xp] offset:51200\n\t" \
    "v_mfma_f32_16x16x32_bf16 v[200:203], v[124:127], v[36:39], v[200:203]\n\t" \
    "ds_read_b128 v[32:35], %[xp] offset:52224\n\t" \
    "v_mfma_f32_16x16x32_bf16 v[204:207], v[116:119], v[40:43], v[204:207]\n\t" \
    "s_waitcnt lgkmcnt(4)\n\t" \
    "v_mfma_f32_16x16x32_bf16 v[208:211], v[124:127], v[40:43], v[208:211]\n\t" \
    "buffer_load_dwordx4 %[l16], %[rs], %[a] offen offset:2048 sc1 lds\n\t" \
    "v_mfma_f32_16x16x32_bf16 v[196:199], v[120:123], v[44:47], v[196:199]\n\t" \
    "ds_read_b128 v[36:39], %[xp] offset:53248\n\t" \
    "v_mfma_f32_16x16x32_bf16 v[200:203], v[128:131], v[44:47], v[200:203]\n\t" \
    "ds_read_b128 v[40:43], %[xp] offset:54272\n\t" \
    "v_mfma_f32_16x16x32_bf16 v[204:207], v[120:123], v[48:51], v[204:207]\n\t" \
    "s_waitcnt lgkmcnt(4)\n\t" \
    "v_mfma_f32_16x16x32_bf16 v[208:211], v[128:131], v[48:51], v[208:211]\n\t" \
    "buffer_load_dwordx4 %[l16], %[rs], %[a] offen offset:3072 sc1 lds\n\t" \
    "s_waitcnt vmcnt(4)\n\t" \
    "s_barrier\n\t" \
    "s_setprio 1\n\t" \
    "s_add_u32 m0, %[sd], 32768\n\t" \
    "s_add_u32 %[a], %[sx], 524288\n\t" \
    "s_add_u32 %[b], %[oo], 0x1000\n\t" \
    "s_cmp_lg_u32 %[t], 0\n\t" \
    "s_cselect_b64 %[em], -1, 0\n\t" \
    "v_mfma_f32_16x16x32_bf16 v[196:199], v[132:135], v[20:23], v[196:199]\n\t" \
    "ds_read_b128 v[44:47], %[xp] offset:55296\n\t" \
    "v_mfma_f32_16x16x32_bf16 v[200:203], v[140:143], v[20:23], v[200:203]\n\t" \
    "ds_read_b128 v[48:51], %[xp] offset:56320\n\t" \
    "v_mfma_f32_16x16x32_bf16 v[204:207], v[132:135], v[24:27], v[204:207]\n\t" \
    "ds_read_b128 v[52:55], %[pa] offset:0\n\t" \
    "s_waitcnt lgkmcnt(5)\n\t" \
    "v_mfma_f32_16x16x32_bf16 v[208:211], v[140:143], v[24:27], v[208:211]\n\t" \
    "buffer_load_dwordx4 %[l16], %[rs], %[a] offen offset:0 sc1 lds\n\t" \
    "v_mfma_f32_16x16x32_bf16 v[196:199], v[136:139], v[28:31], v[196:199]\n\t" \
    "ds_read_b128 v[20:23], %[xp] offset:57344\n\t" \
    "v_mfma_f32_16x16x32_bf16 v[200:203], v[144:147], v[28:31], v[200:203]\n\t" \
    "ds_read_b128 v[24:27], %[xp] offset:58368\n\t" \
    "v_mfma_f32_16x16x32_bf16 v[204:207], v[136:139], v[32:35], v[204:207]\n\t" \
    "ds_read_b128 v[56:59], %[pa] offset:1024\n\t" \
    "s_waitcnt lgkmcnt(6)\n\t" \
    "v_mfma_f32_16x16x32_bf16 v[208:211], v[144:147], v[32:35], v[208:211]\n\t" \
    "buffer_load_dwordx4 %[l16], %[rs], %[a] offen offset:1024 sc1 lds\n\t" \
    "v_mfma_f32_16x16x32_bf16 v[196:199], v[148:151], v[36:39], v[196:199]\n\t" \
    "ds_read_b128 v[28:31], %[xp] offset:59392\n\t" \
    "v_mfma_f32_16x16x32_bf16 v[200:203], v[156:159], v[36:39], v[200:203]\n\t" \
    "ds_read_b128 v[32:35], %[xp] offset:60416\n\t" \
    "v_mfma_f32_16x16x32_bf16 v[204:207], v[148:151], v[40:43], v[204:207]\n\t" \
    "ds_read_b128 v[60:63], %[pa] offset:2048\n\t" \
    "s_waitcnt lgkmcnt(7)\n\t" \
    "v_mfma_f32_16x16x32_bf16 v[208:211], v[156:159], v[40:43], v[208:211]\n\t" \
    "buffer_load_dwordx4 %[l16], %[rs], %[a] offen offset:2048 sc1 lds\n\t" \
    "v_mfma_f32_16x16x32_bf16 v[196:199], v[152:155], v[44:47], v[196:199]\n\t" \
    "ds_read_b128 v[36:39], %[xp] offset:61440\n\t" \
    "s_waitcnt lgkmcnt(7)\n\t" \
    "v_add_f32 v0, v0, v52\n\t" \
    "v_mfma_f32_16x16x32_bf16 v[200:203], v[160:163], v[44:47], v[200:203]\n\t" \
    "ds_read_b128 v[40:43], %[xp] offset:62464\n\t" \
    "v_add_f32 v1, v1, v53\n\t" \
    "v_mfma_f32_16x16x32_bf16 v[204:207], v[152:155], v[48:51], v[204:207]\n\t" \
    "ds_read_b128 v[64:67], %[pa] offset:3072\n\t" \
    "s_waitcnt lgkmcnt(7)\n\t" \
    "v_mfma_f32_16x16x32_bf16 v[208:211], v[160:163], v[48:51], v[208:211]\n\t" \
    "buffer_load_dwordx4 %[l16], %[rs], %[a] offen offset:3072 sc1 lds\n\t" \
    "v_add_f32 v2, v2, v54\n\t" \
    "v_add_f32 v3, v3, v55\n\t" \
    "v_mfma_f32_16x16x32_bf16 v[196:199], v[164:167], v[20:23], v[196:199]\n\t" \
    "ds_read_b128 v[44:47], %[xp] offset:63488\n\t" \
    "s_waitcnt lgkmcnt(7)\n\t" \
    "v_add_f32 v4, v4, v56\n\t" \
    "v_mfma_f32_16x16x32_bf16 v[200:203], v[172:175], v[20:23], v[200:203]\n\t" \
    "ds_read_b128 v[48:51], %[xp] offset:64512\n\t" \
    "v_add_f32 v5, v5, v57\n\t" \
    "v_mfma_f32_16x16x32_bf16 v[204:207], v[164:167], v[24:27], v[204:207]\n\t" \
    "v_add_f32 v6, v6, v58\n\t" \
    "s_waitcnt lgkmcnt(6)\n\t" \
    "v_mfma_f32_16x16x32_bf16 v[208:211], v[172:175], v[24:27], v[208:211]\n\t" \
    "v_add_f32 v7, v7, v59\n\t" \
    "s_waitcnt lgkmcnt(5)\n\t" \
    "v_add_f32 v8, v8, v60\n\t" \
    "v_add_f32 v9, v9, v61\n\t" \
    "v_mfma_f32_16x16x32_bf16 v[196:199], v[168:171], v[28:31], v[196:199]\n\t" \
    "ds_read_b128 v[20:23], %[xp] offset:0\n\t" \
    "v_add_f32 v10, v10, v62\n\t" \
    "v_mfma_f32_16x16x32_bf16 v[200:203], v[176:179], v[28:31], v[200:203]\n\t" \
    "ds_read_b128 v[24:27], %[xp] offset:1024\n\t" \
    "v_add_f32 v11, v11, v63\n\t" \
    "v_mfma_f32_16x16x32_bf16 v[204:207], v[168:171], v[32:35], v[204:207]\n\t" \
    "s_waitcnt lgkmcnt(4)\n\t" \
    "v_add_f32 v12, v12, v64\n\t" \
    "v_mfma_f32_16x16x32_bf16 v[208:211], v[176:179], v[32:35], v[208:211]\n\t" \
    "v_add_f32 v13, v13, v65\n\t" \
    "v_add_f32 v14, v14, v66\n\t" \
    "v_add_f32 v15, v15, v67\n\t" \
    "v_mfma_f32_16x16x32_bf16 v[196:199], v[180:183], v[36:39], v[196:199]\n\t" \
    "ds_read_b128 v[28:31], %[xp] offset:2048\n\t" \
    "v_mfma_f32_16x16x32_bf16 v[200:203], v[188:191], v[36:39], v[200:203]\n\t" \
    "ds_read_b128 v[32:35], %[xp] offset:3072\n\t" \
    "s_mov_b64 exec, %[em]\n\t" \
    "buffer_store_dwordx4 v[0:3], %[l16], %[rs], %[oo] offen offset:0 sc1\n\t" \
    "s_mov_b64 exec, -1\n\t" \
    "v_mfma_f32_16x16x32_bf16 v[204:207], v[180:183], v[40:43], v[204:207]\n\t" \
    "s_waitcnt lgkmcnt(4)\n\t" \
    "v_mfma_f32_16x16x32_bf16 v[208:211], v[188:191], v[40:43], v[208:211]\n\t" \
    "s_mov_b64 exec, %[em]\n\t" \
    "buffer_store_dwordx4 v[4:7], %[l16], %[rs], %[oo] offen offset:1024 sc1\n\t" \
    "s_mov_b64 exec, -1\n\t" \
    "v_mfma_f32_16x16x32_bf16 v[196:199], v[184:187], v[44:47], v[196:199]\n\t" \
    "ds_read_b128 v[36:39], %[xp] offset:4096\n\t" \
    "v_mfma_f32_16x16x32_bf16 v[200:203], v[192:195], v[44:47], v[200:203]\n\t" \
    "ds_read_b128 v[40:43], %[xp] offset:5120\n\t" \
    "s_mov_b64 exec, %[em]\n\t" \
    "buffer_store_dwordx4 v[8:11], %[l16], %[rs], %[oo] offen offset:2048 sc1\n\t" \
    "s_mov_b64 exec, -1\n\t" \
    "v_mfma_f32_16x16x32_bf16 v[204:207], v[184:187], v[48:51], v[204:207]\n\t" \
    "s_waitcnt lgkmcnt(4)\n\t" \
    "v_mfma_f32_16x16x32_bf16 v[208:211], v[192:195], v[48:51], v[208:211]\n\t" \
    "s_mov_b64 exec, %[em]\n\t" \
    "buffer_store_dwordx4 v[12:15], %[l16], %[rs], %[oo] offen offset:3072 sc1\n\t" \
    "s_mov_b64 exec, -1\n\t" \
    "s_add_u32 %[sx], %[sx], 262144\n\t" \
    "s_add_u32 %[t], %[t], 1\n\t" \
    "s_add_u32 %[oo], %[oo], 524288\n\t" \
    "s_waitcnt vmcnt(8)\n\t" \
    "s_cmp_gt_u32 %[t], " STR(SQUICK) " + 1\n\t" \
    "s_cbranch_scc1 B1_%=\n\t" \
    "s_waitcnt vmcnt(0)\n\t" \
    "B1_%=:\n\t" \
    "s_barrier\n\t" \
    "s_cmp_lt_u32 %[t], %[te]\n\t" \
    "s_cbranch_scc1 L1_%=\n\t" \
    "s_cmp_eq_u32 %[fin], %[te]\n\t" \
    "s_cbranch_scc0 E_%=\n\t" \
    "s_nop 4\n\t" \
    "s_barrier\n\t" \
    "ds_read_b128 v[52:55], %[pa] offset:0\n\t" \
    "ds_read_b128 v[56:59], %[pa] offset:1024\n\t" \
    "ds_read_b128 v[60:63], %[pa] offset:2048\n\t" \
    "ds_read_b128 v[64:67], %[pa] offset:3072\n\t" \
    "s_add_u32 %[b], %[oo], 0x1000\n\t" \
    "s_waitcnt lgkmcnt(0)\n\t" \
    "v_add_f32 v196, v196, v52\n\t" \
    "v_add_f32 v197, v197, v53\n\t" \
    "v_add_f32 v198, v198, v54\n\t" \
    "v_add_f32 v199, v199, v55\n\t" \
    "v_add_f32 v200, v200, v56\n\t" \
    "v_add_f32 v201, v201, v57\n\t" \
    "v_add_f32 v202, v202, v58\n\t" \
    "v_add_f32 v203, v203, v59\n\t" \
    "v_add_f32 v204, v204, v60\n\t" \
    "v_add_f32 v205, v205, v61\n\t" \
    "v_add_f32 v206, v206, v62\n\t" \
    "v_add_f32 v207, v207, v63\n\t" \
    "v_add_f32 v208, v208, v64\n\t" \
    "v_add_f32 v209, v209, v65\n\t" \
    "v_add_f32 v210, v210, v66\n\t" \
    "v_add_f32 v211, v211, v67\n\t" \
    "buffer_store_dwordx4 v[196:199], %[l16], %[rs], %[oo] offen offset:0 sc1\n\t" \
    "buffer_store_dwordx4 v[200:203], %[l16], %[rs], %[oo] offen offset:1024 sc1\n\t" \
    "buffer_store_dwordx4 v[204:207], %[l16], %[rs], %[oo] offen offset:2048 sc1\n\t" \
    "buffer_store_dwordx4 v[208:211], %[l16], %[rs], %[oo] offen offset:3072 sc1\n\t" \
    "E_%=:\n\t" \
    "s_setprio 0\n\t" \
    "s_waitcnt lgkmcnt(0)\n\t" \
    "s_nop 7\n\t" \
    : [t] "+s"(t), [sx] "+s"(sx), [oo] "+s"(oo), [a] "=&s"(tmp), [b] "=&s"(tmp2), [em] "=&s"(em), "+{v[20:27]}"(xf[0]), "+{v[28:35]}"(xf[1]), "+{v[36:43]}"(xf[2]), "+{v[196:203]}"(ac[0]), "+{v[204:211]}"(ac[1]) \
    : [te] "s"(TE_), [fin] "s"(FIN_), [kh] "s"(kh), [sd] "s"(sd), [rs] "s"(rs), [l16] "v"(l16), [xp] "v"(xp), [pa] "v"(pa), [fm] "s"(fm), [pm] "s"(pm), [fo] "s"(fo), [po] "s"(po), [pd] "s"(pd), [tag] "s"(P.tag), "{v[68:75]}"(w[0][0]), "{v[76:83]}"(w[0][1]), "{v[84:91]}"(w[1][0]), "{v[92:99]}"(w[1][1]), "{v[100:107]}"(w[2][0]), "{v[108:115]}"(w[2][1]), "{v[116:123]}"(w[3][0]), "{v[124:131]}"(w[3][1]), "{v[132:139]}"(w[4][0]), "{v[140:147]}"(w[4][1]), "{v[148:155]}"(w[5][0]), "{v[156:163]}"(w[5][1]), "{v[164:171]}"(w[6][0]), "{v[172:179]}"(w[6][1]), "{v[180:187]}"(w[7][0]), "{v[188:195]}"(w[7][1]) \
    : "memory", "scc", "v0", "v1", "v2", "v3", "v4", "v5", "v6", "v7", "v8", "v9", "v10", "v11", "v12", "v13", "v14", "v15", "v44", "v45", "v46", "v47", "v48", "v49", "v50", "v51", "v52", "v53", "v54", "v55", "v56", "v57", "v58", "v59", "v60", "v61", "v62", "v63", "v64", "v65", "v66", "v67")

#define X_ONE() \
  asm volatile( \
    "ds_read_b128 v[20:23], %[xp] offset:0\n\t" \
    "ds_read_b128 v[24:27], %[xp] offset:1024\n\t" \
    "ds_read_b128 v[28:31], %[xp] offset:2048\n\t" \
    "ds_read_b128 v[32:35], %[xp] offset:3072\n\t" \
    "ds_read_b128 v[36:39], %[xp] offset:4096\n\t" \
    "ds_read_b128 v[40:43], %[xp] offset:5120\n\t" \
    "s_waitcnt lgkmcnt(4)\n\t" \
    "v_mfma_f32_16x16x32_bf16 v[0:3], v[68:71], v[20:23], 0\n\t" \
    "ds_read_b128 v[44:47], %[xp] offset:6144\n\t" \
    "v_mfma_f32_16x16x32_bf16 v[4:7], v[76:79], v[20:23], 0\n\t" \
    "ds_read_b128 v[48:51], %[xp] offset:7168\n\t" \
    "v_mfma_f32_16x16x32_bf16 v[8:11], v[68:71], v[24:27], 0\n\t" \
    "s_waitcnt lgkmcnt(4)\n\t" \
    "v_mfma_f32_16x16x32_bf16 v[12:15], v[76:79], v[24:27], 0\n\t" \
    "v_mfma_f32_16x16x32_bf16 v[0:3], v[72:75], v[28:31], v[0:3]\n\t" \
    "ds_read_b128 v[20:23], %[xp] offset:8192\n\t" \
    "v_mfma_f32_16x16x32_bf16 v[4:7], v[80:83], v[28:31], v[4:7]\n\t" \
    "ds_read_b128 v[24:27], %[xp] offset:9216\n\t" \
    "v_mfma_f32_16x16x32_bf16 v[8:11], v[72:75], v[32:35], v[8:11]\n\t" \
    "s_waitcnt lgkmcnt(4)\n\t" \
    "v_mfma_f32_16x16x32_bf16 v[12:15], v[80:83], v[32:35], v[12:15]\n\t" \
    "v_mfma_f32_16x16x32_bf16 v[0:3], v[84:87], v[36:39], v[0:3]\n\t" \
    "ds_read_b128 v[28:31], %[xp] offset:10240\n\t" \
    "v_mfma_f32_16x16x32_bf16 v[4:7], v[92:95], v[36:39], v[4:7]\n\t" \
    "ds_read_b128 v[32:35], %[xp] offset:11264\n\t" \
    "v_mfma_f32_16x16x32_bf16 v[8:11], v[84:87], v[40:43], v[8:11]\n\t" \
    "s_waitcnt lgkmcnt(4)\n\t" \
    "v_mfma_f32_16x16x32_bf16 v[12:15], v[92:95], v[40:43], v[12:15]\n\t" \
    "v_mfma_f32_16x16x32_bf16 v[0:3], v[88:91], v[44:47], v[0:3]\n\t" \
    "ds_read_b128 v[36:39], %[xp] offset:12288\n\t" \
    "v_mfma_f32_16x16x32_bf16 v[4:7], v[96:99], v[44:47], v[4:7]\n\t" \
    "ds_read_b128 v[40:43], %[xp] offset:13312\n\t" \
    "v_mfma_f32_16x16x32_bf16 v[8:11], v[88:91], v[48:51], v[8:11]\n\t" \
    "s_waitcnt lgkmcnt(4)\n\t" \
    "v_mfma_f32_16x16x32_bf16 v[12:15], v[96:99], v[48:51], v[12:15]\n\t" \
    "v_mfma_f32_16x16x32_bf16 v[0:3], v[100:103], v[20:23], v[0:3]\n\t" \
    "ds_read_b128 v[44:47], %[xp] offset:14336\n\t" \
    "v_mfma_f32_16x16x32_bf16 v[4:7], v[108:111], v[20:23], v[4:7]\n\t" \
    "ds_read_b128 v[48:51], %[xp] offset:15360\n\t" \
    "v_mfma_f32_16x16x32_bf16 v[8:11], v[100:103], v[24:27], v[8:11]\n\t" \
    "s_waitcnt lgkmcnt(4)\n\t" \
    "v_mfma_f32_16x16x32_bf16 v[12:15], v[108:111], v[24:27], v[12:15]\n\t" \
    "s_waitcnt vmcnt(0)\n\t" \
    "s_barrier\n\t" \
    "v_mfma_f32_16x16x32_bf16 v[0:3], v[104:107], v[28:31], v[0:3]\n\t" \
    "ds_read_b128 v[20:23], %[xp] offset:16384\n\t" \
    "v_mfma_f32_16x16x32_bf16 v[4:7], v[112:115], v[28:31], v[4:7]\n\t" \
    "ds_read_b128 v[24:27], %[xp] offset:17408\n\t" \
    "v_mfma_f32_16x16x32_bf16 v[8:11], v[104:107], v[32:35], v[8:11]\n\t" \
    "s_waitcnt lgkmcnt(4)\n\t" \
    "v_mfma_f32_16x16x32_bf16 v[12:15], v[112:115], v[32:35], v[12:15]\n\t" \
    "buffer_load_dword v52, %[pv], %[rs], 0 offen sc1\n\t" \
    "v_mfma_f32_16x16x32_bf16 v[0:3], v[116:119], v[36:39], v[0:3]\n\t" \
    "ds_read_b128 v[28:31], %[xp] offset:18432\n\t" \
    "v_mfma_f32_16x16x32_bf16 v[4:7], v[124:127], v[36:39], v[4:7]\n\t" \
    "ds_read_b128 v[32:35], %[xp] offset:19456\n\t" \
    "v_mfma_f32_16x16x32_bf16 v[8:11], v[116:119], v[40:43], v[8:11]\n\t" \
    "s_waitcnt lgkmcnt(4)\n\t" \
    "v_mfma_f32_16x16x32_bf16 v[12:15], v[124:127], v[40:43], v[12:15]\n\t" \
    "v_mfma_f32_16x16x32_bf16 v[0:3], v[120:123], v[44:47], v[0:3]\n\t" \
    "ds_read_b128 v[36:39], %[xp] offset:20480\n\t" \
    "v_mfma_f32_16x16x32_bf16 v[4:7], v[128:131], v[44:47], v[4:7]\n\t" \
    "ds_read_b128 v[40:43], %[xp] offset:21504\n\t" \
    "v_mfma_f32_16x16x32_bf16 v[8:11], v[120:123], v[48:51], v[8:11]\n\t" \
    "s_waitcnt lgkmcnt(4)\n\t" \
    "v_mfma_f32_16x16x32_bf16 v[12:15], v[128:131], v[48:51], v[12:15]\n\t" \
    "v_mfma_f32_16x16x32_bf16 v[0:3], v[132:135], v[20:23], v[0:3]\n\t" \
    "ds_read_b128 v[44:47], %[xp] offset:22528\n\t" \
    "v_mfma_f32_16x16x32_bf16 v[4:7], v[140:143], v[20:23], v[4:7]\n\t" \
    "ds_read_b128 v[48:51], %[xp] offset:23552\n\t" \
    "v_mfma_f32_16x16x32_bf16 v[8:11], v[132:135], v[24:27], v[8:11]\n\t" \
    "s_waitcnt lgkmcnt(4)\n\t" \
    "v_mfma_f32_16x16x32_bf16 v[12:15], v[140:143], v[24:27], v[12:15]\n\t" \
    "v_mfma_f32_16x16x32_bf16 v[0:3], v[136:139], v[28:31], v[0:3]\n\t" \
    "ds_read_b128 v[20:23], %[xp] offset:24576\n\t" \
    "v_mfma_f32_16x16x32_bf16 v[4:7], v[144:147], v[28:31], v[4:7]\n\t" \
    "ds_read_b128 v[24:27], %[xp] offset:25600\n\t" \
    "v_mfma_f32_16x16x32_bf16 v[8:11], v[136:139], v[32:35], v[8:11]\n\t" \
    "s_waitcnt lgkmcnt(4)\n\t" \
    "v_mfma_f32_16x16x32_bf16 v[12:15], v[144:147], v[32:35], v[12:15]\n\t" \
    "v_mfma_f32_16x16x32_bf16 v[0:3], v[148:151], v[36:39], v[0:3]\n\t" \
    "ds_read_b128 v[28:31], %[xp] offset:26624\n\t" \
    "v_mfma_f32_16x16x32_bf16 v[4:7], v[156:159], v[36:39], v[4:7]\n\t" \
    "ds_read_b128 v[32:35], %[xp] offset:27648\n\t" \
    "v_mfma_f32_16x16x32_bf16 v[8:11], v[148:151], v[40:43], v[8:11]\n\t" \
    "s_waitcnt lgkmcnt(4)\n\t" \
    "v_mfma_f32_16x16x32_bf16 v[12:15], v[156:159], v[40:43], v[12:15]\n\t" \
    "v_mfma_f32_16x16x32_bf16 v[0:3], v[152:155], v[44:47], v[0:3]\n\t" \
    "ds_read_b128 v[36:39], %[xp] offset:28672\n\t" \
    "v_mfma_f32_16x16x32_bf16 v[4:7], v[160:163], v[44:47], v[4:7]\n\t" \
    "ds_read_b128 v[40:43], %[xp] offset:29696\n\t" \
    "v_mfma_f32_16x16x32_bf16 v[8:11], v[152:155], v[48:51], v[8:11]\n\t" \
    "s_waitcnt lgkmcnt(4)\n\t" \
    "v_mfma_f32_16x16x32_bf16 v[12:15], v[160:163], v[48:51], v[12:15]\n\t" \
    "v_mfma_f32_16x16x32_bf16 v[0:3], v[164:167], v[20:23], v[0:3]\n\t" \
    "ds_read_b128 v[44:47], %[xp] offset:30720\n\t" \
    "v_mfma_f32_16x16x32_bf16 v[4:7], v[172:175], v[20:23], v[4:7]\n\t" \
    "ds_read_b128 v[48:51], %[xp] offset:31744\n\t" \
    "v_mfma_f32_16x16x32_bf16 v[8:11], v[164:167], v[24:27], v[8:11]\n\t" \
    "s_waitcnt lgkmcnt(4)\n\t" \
    "v_mfma_f32_16x16x32_bf16 v[12:15], v[172:175], v[24:27], v[12:15]\n\t" \
    "v_mfma_f32_16x16x32_bf16 v[0:3], v[168:171], v[28:31], v[0:3]\n\t" \
    "v_mfma_f32_16x16x32_bf16 v[4:7], v[176:179], v[28:31], v[4:7]\n\t" \
    "v_mfma_f32_16x16x32_bf16 v[8:11], v[168:171], v[32:35], v[8:11]\n\t" \
    "s_waitcnt lgkmcnt(2)\n\t" \
    "v_mfma_f32_16x16x32_bf16 v[12:15], v[176:179], v[32:35], v[12:15]\n\t" \
    "v_mfma_f32_16x16x32_bf16 v[0:3], v[180:183], v[36:39], v[0:3]\n\t" \
    "v_mfma_f32_16x16x32_bf16 v[4:7], v[188:191], v[36:39], v[4:7]\n\t" \
    "v_mfma_f32_16x16x32_bf16 v[8:11], v[180:183], v[40:43], v[8:11]\n\t" \
    "s_waitcnt lgkmcnt(0)\n\t" \
    "v_mfma_f32_16x16x32_bf16 v[12:15], v[188:191], v[40:43], v[12:15]\n\t" \
    "v_mfma_f32_16x16x32_bf16 v[0:3], v[184:187], v[44:47], v[0:3]\n\t" \
    "v_mfma_f32_16x16x32_bf16 v[4:7], v[192:195], v[44:47], v[4:7]\n\t" \
    "v_mfma_f32_16x16x32_bf16 v[8:11], v[184:187], v[48:51], v[8:11]\n\t" \
    "v_mfma_f32_16x16x32_bf16 v[12:15], v[192:195], v[48:51], v[12:15]\n\t" \
    "s_waitcnt vmcnt(0)\n\t" \
    "s_nop 7\n\t" \
    : "=&{v52}"(pg), "=&{v[0:3]}"(a[0]), "=&{v[4:7]}"(a[1]), "=&{v[8:11]}"(a[2]), "=&{v[12:15]}"(a[3]) \
    : [xp] "v"(xp), [pv] "v"(pv), [rs] "s"(rs), "{v[68:75]}"(w[0][0]), "{v[76:83]}"(w[0][1]), "{v[84:91]}"(w[1][0]), "{v[92:99]}"(w[1][1]), "{v[100:107]}"(w[2][0]), "{v[108:115]}"(w[2][1]), "{v[116:123]}"(w[3][0]), "{v[124:131]}"(w[3][1]), "{v[132:139]}"(w[4][0]), "{v[140:147]}"(w[4][1]), "{v[148:155]}"(w[5][0]), "{v[156:163]}"(w[5][1]), "{v[164:171]}"(w[6][0]), "{v[172:179]}"(w[6][1]), "{v[180:187]}"(w[7][0]), "{v[188:195]}"(w[7][1]) \
    : "memory", "v20", "v21", "v22", "v23", "v24", "v25", "v26", "v27", "v28", "v29", "v30", "v31", "v32", "v33", "v34", "v35", "v36", "v37", "v38", "v39", "v40", "v41", "v42", "v43", "v44", "v45", "v46", "v47", "v48", "v49", "v50", "v51")
#define AQ_PIN0() asm volatile("" : "+{v[28:35]}"(wr[0][0]), "+{v[36:43]}"(wr[0][1]))
#define AQ_PIN1() asm volatile("" : "+{v[44:51]}"(wr[1][0]), "+{v[52:59]}"(wr[1][1]))
#define AQ_PIN2() asm volatile("" : "+{v[60:67]}"(wr[2][0]), "+{v[68:75]}"(wr[2][1]))
#define AQ_PIN3() asm volatile("" : "+{v[76:83]}"(wr[3][0]), "+{v[84:91]}"(wr[3][1]))
#define AQ_PIN4() asm volatile("" : "+{v[92:99]}"(wr[4][0]), "+{v[100:107]}"(wr[4][1]))
#define AQ_PIN5() asm volatile("" : "+{v[108:115]}"(wr[5][0]), "+{v[116:123]}"(wr[5][1]))
#define AQ_PIN6() asm volatile("" : "+{v[124:131]}"(wr[6][0]), "+{v[132:139]}"(wr[6][1]))
#define AQ_PIN7() asm volatile("" : "+{v[140:147]}"(wr[7][0]), "+{v[148:155]}"(wr[7][1]))
#define AQ_PIN8() asm volatile("" : "+{v[156:163]}"(wr[8][0]), "+{v[164:171]}"(wr[8][1]))
#define AQ_PIN9() asm volatile("" : "+{v[172:179]}"(wr[9][0]), "+{v[180:187]}"(wr[9][1]))
#define AQ_PIN10() asm volatile("" : "+{v[188:195]}"(wr[10][0]), "+{v[196:203]}"(wr[10][1]))
#define AQ_PIN11() asm volatile("" : "+{v[204:211]}"(wr[11][0]), "+{v[212:219]}"(wr[11][1]))
#define AK_PIN0() asm volatile("" : "+{v[20:27]}"(wr[0][0]), "+{v[28:35]}"(wr[0][1]))
#define AK_PIN1() asm volatile("" : "+{v[36:43]}"(wr[1][0]), "+{v[44:51]}"(wr[1][1]))
#define AK_PIN2() asm volatile("" : "+{v[52:59]}"(wr[2][0]), "+{v[60:67]}"(wr[2][1]))
#define AK_PIN3() asm volatile("" : "+{v[68:75]}"(wr[3][0]), "+{v[76:83]}"(wr[3][1]))
#define AK_PIN4() asm volatile("" : "+{v[84:91]}"(wr[4][0]), "+{v[92:99]}"(wr[4][1]))
#define AK_PIN5() asm volatile("" : "+{v[100:107]}"(wr[5][0]), "+{v[108:115]}"(wr[5][1]))
#define AK_PIN6() asm volatile("" : "+{v[116:123]}"(wr[6][0]), "+{v[124:131]}"(wr[6][1]))
#define AK_PIN7() asm volatile("" : "+{v[132:139]}"(wr[7][0]), "+{v[140:147]}"(wr[7][1]))
#define AK_PIN8() asm volatile("" : "+{v[148:155]}"(wr[8][0]), "+{v[156:163]}"(wr[8][1]))
#define AK_PIN9() asm volatile("" : "+{v[164:171]}"(wr[9][0]), "+{v[172:179]}"(wr[9][1]))
#define AK_PIN10() asm volatile("" : "+{v[180:187]}"(wr[10][0]), "+{v[188:195]}"(wr[10][1]))
#define AK_PIN11() asm volatile("" : "+{v[196:203]}"(wr[11][0]), "+{v[204:211]}"(wr[11][1]))
#define V_PIN() asm volatile("" : "+{v[28:35]}"(wv[0]), "+{v[36:43]}"(wv[1]), "+{v[44:51]}"(wv[2]), "+{v[52:59]}"(wv[3]), "+{v[60:67]}"(wv[4]), "+{v[68:75]}"(wv[5]), "+{v[76:83]}"(wv[6]), "+{v[84:91]}"(wv[7]), "+{v[92:99]}"(wv[8]), "+{v[100:107]}"(wv[9]), "+{v[108:115]}"(wv[10]), "+{v[116:123]}"(wv[11]), "+{v[124:131]}"(wv[12]), "+{v[132:139]}"(wv[13]), "+{v[140:147]}"(wv[14]), "+{v[148:155]}"(wv[15]))
#define WOV_PIN0() asm volatile("" : "+{v[156:163]}"(wo[0][0][0]), "+{v[164:171]}"(wo[0][0][1]), "+{v[172:179]}"(wo[0][1][0]), "+{v[180:187]}"(wo[0][1][1]))
#define WOV_PIN1() asm volatile("" : "+{v[188:195]}"(wo[1][0][0]), "+{v[196:203]}"(wo[1][0][1]), "+{v[204:211]}"(wo[1][1][0]), "+{v[212:219]}"(wo[1][1][1]))
#define WOW_PIN0() asm volatile("" : "+{v[28:35]}"(wo[0][0][0]), "+{v[36:43]}"(wo[0][0][1]), "+{v[44:51]}"(wo[0][1][0]), "+{v[52:59]}"(wo[0][1][1]))
#define WOW_PIN1() asm volatile("" : "+{v[60:67]}"(wo[1][0][0]), "+{v[68:75]}"(wo[1][0][1]), "+{v[76:83]}"(wo[1][1][0]), "+{v[84:91]}"(wo[1][1][1]))
#define WOW_PIN2() asm volatile("" : "+{v[92:99]}"(wo[2][0][0]), "+{v[100:107]}"(wo[2][0][1]), "+{v[108:115]}"(wo[2][1][0]), "+{v[116:123]}"(wo[2][1][1]))
#define WOW_PIN3() asm volatile("" : "+{v[124:131]}"(wo[3][0][0]), "+{v[132:139]}"(wo[3][0][1]), "+{v[140:147]}"(wo[3][1][0]), "+{v[148:155]}"(wo[3][1][1]))
#define WOW_PIN4() asm volatile("" : "+{v[156:163]}"(wo[4][0][0]), "+{v[164:171]}"(wo[4][0][1]), "+{v[172:179]}"(wo[4][1][0]), "+{v[180:187]}"(wo[4][1][1]))
#define WOW_PIN5() asm volatile("" : "+{v[188:195]}"(wo[5][0][0]), "+{v[196:203]}"(wo[5][0][1]), "+{v[204:211]}"(wo[5][1][0]), "+{v[212:219]}"(wo[5][1][1]))
#define A_PIN(QW, i) do { switch (i) { case 0: if (QW) AQ_PIN0(); else AK_PIN0(); break; case 1: if (QW) AQ_PIN1(); else AK_PIN1(); break; case 2: if (QW) AQ_PIN2(); else AK_PIN2(); break; case 3: if (QW) AQ_PIN3(); else AK_PIN3(); break; case 4: if (QW) AQ_PIN4(); else AK_PIN4(); break; case 5: if (QW) AQ_PIN5(); else AK_PIN5(); break; case 6: if (QW) AQ_PIN6(); else AK_PIN6(); break; case 7: if (QW) AQ_PIN7(); else AK_PIN7(); break; case 8: if (QW) AQ_PIN8(); else AK_PIN8(); break; case 9: if (QW) AQ_PIN9(); else AK_PIN9(); break; case 10: if (QW) AQ_PIN10(); else AK_PIN10(); break; case 11: if (QW) AQ_PIN11(); else AK_PIN11(); break; } } while (0)
#define WO_PIN(VW, i) do { switch (i) { case 0: if (VW) WOV_PIN0(); else WOW_PIN0(); break; case 1: if (VW) WOV_PIN1(); else WOW_PIN1(); break; case 2: if (!VW) WOW_PIN2(); break; case 3: if (!VW) WOW_PIN3(); break; case 4: if (!VW) WOW_PIN4(); break; case 5: if (!VW) WOW_PIN5(); break; } } while (0)

__device__ __forceinline__ u32x4 ldsv(LAS unsigned char* p) { return *(volatile LAS u32x4*)p; }
#define DMA_A dman
#define DMA_V dman
#define DMA_O dmaw
#define TOUCH 0
#define TAUX 0
#define POISON4 ((u32x4){~0u, ~0u, ~0u, ~0u})
#define DLYM 0
#define DLYA 8
#define DLYK (DLYA + 8)
#define DLYO 8
#define FLA 0
#define FLB 0
#define CADM 1
#define CADO 1

#define RTC(id)
#define DCA 3
#define DCB 2
#define DL1 10
#define DL2 17
#define DLB 18
#define ADL 0
#define SN0 150
#define HELP 256
#define BORD 0
#define QT 0
__device__ __forceinline__ void track(int& est, int d, int up, int dn) { est += max(min(d - est, up), min(-dn, (d - est) >> 1)); }
__device__ __forceinline__ unsigned rfl(unsigned v) { unsigned r; asm volatile("s_nop 0\n\tv_readfirstlane_b32 %0, %1\n\ts_nop 4" : "=s"(r) : "v"(v)); return r; }
__device__ __forceinline__ void plan_wait(rsrc_t rs, unsigned fo, int s, int& lim, unsigned& tm, unsigned& d) {
  while ((d & 255u) != (unsigned)s && (lim -= DEC1) > 0) {
    __builtin_amdgcn_s_sleep(1);
    const u32x2 v = __builtin_amdgcn_raw_buffer_load_b64(rs, 0, fo, AUX_SC1);
    tm = rfl(v.x); d = rfl(v.y);
  }
  const unsigned now = (unsigned)__builtin_amdgcn_s_memrealtime() * 4u;
  tm = now + min((int)(tm - now), 2000);
  d = min(d >> 8, 1000u);
}
#define SPIN_BUDGET (1 << 21)

__device__ __forceinline__ unsigned rec_go(LAS unsigned char* w, const Ptrs& P, u32x2 f, int wid, int lane) {
  if (wid >= 6) {
    if (__any(f.y != P.tag)) f.x = flag_wait(P, wid == 6 ? F_LN0(lane) : F_CAL(lane), 0, lane);
    if (wid == 7) { const unsigned m = near_half(f.x); if (lane == 0) *(volatile LAS unsigned*)w = m; }
  }
  LBARRIER();
  return __builtin_amdgcn_readfirstlane(*(volatile LAS unsigned*)w);
}

#define SLOT(i) ((i) * 16384)
#define LA_MEM SLOT(8)
#define LA_Q SLOT(9)
#define LA_K (SLOT(9) + 2048)
#define LA_P (SLOT(9) + 4096)
#define LA_VM (SLOT(9) + 6144)
#define LA_KX (SLOT(9) + 8192)

template <bool QW>
__device__ __forceinline__ void a_wave(LAS unsigned char* lds, Ptrs& P, int h, int wid) {
  char* ws = P.ws;
  const int lane = opq(0), tid = wid * 64 + lane;
  const int j = wid & 3, g = lane >> 4, q4 = (lane & 15) >> 2, p4 = lane & 3;
  const rsrc_t rs = WS_RSRC(P);
  const rsrc_t rq = __builtin_amdgcn_make_buffer_rsrc((void*)P.Wq, 0, HD * DM * 2, RSRC_W3);
  const rsrc_t rk = __builtin_amdgcn_make_buffer_rsrc((void*)P.Wk, 0, HD * DM * 2, RSRC_W3);
  const unsigned lbase = (unsigned)(size_t)lds;
  int lim = SPIN_BUDGET;
  u32x8 wr[12][2];
  const int trow = 16 * (g >> 1) + 4 * (g & 1) + 8 * (q4 >> 1) + (q4 & 1);
  const int tsw = ((q4 & 1) | ((g & 1) << 1) | ((q4 >> 1) << 2)) << 1;
  const int toff = trow * 256 + (((4 * j + (p4 >> 1)) ^ tsw) << 4) + 8 * (p4 & 1);
  {
    unsigned dv[2];
    #pragma unroll
    for (int x = 0; x < 2; ++x) {
      const int row = 8 * wid + 4 * x + (lane >> 4);
      const int sw = ((row & 1) | (((row >> 2) & 1) << 1) | (((row >> 3) & 1) << 2)) << 1;
      dv[x] = (unsigned)(row * (HD * 2) + h * 256 + (((lane & 15) ^ sw) << 4));
    }
#define A_GRP(n) do { _Pragma("unroll") for (int i = 0; i < 8; ++i) \
      DMA_A(((n) & 1) ? rk : rq, dv[i & 1], (4 * (((n) & 1) ? ((n) >> 1) ^ ((n) >> 2) : (((n) >> 1) + 1) & 3) + (i >> 1)) * 64 * HD * 2, lbase + SLOT(4 * ((n) & 1) + (i >> 1)) + wid * 2048 + (i & 1) * 1024); } while (0)
    A_GRP(0); A_GRP(1);
    #pragma unroll
    for (int n = 0; n < 6; ++n) {
      VMW(8);
      BARRIER();
      if ((n & 1) == (QW ? 0 : 1)) {
        #pragma unroll
        for (int b = 0; b < 4; ++b) {
          LAS unsigned char* t = lds + SLOT(QW ? 0 : 4) + ((b ^ j) << 14);
          wr[4 * (n >> 1) + b][0] = rd_frag<8192, 512>(t + toff);
          wr[4 * (n >> 1) + b][1] = rd_frag<8192, 512>(t + (toff ^ 32));
        }
        #pragma unroll
        for (int b = 0; b < 4; ++b) A_PIN(QW, 4 * (n >> 1) + b);
      }
      LBARRIER();
      A_GRP(n + 2);
    }
  }
  const unsigned cb = comm_base(ws);
  u32x2 f0 = {0u, 0u};
  #pragma unroll
  for (int rd = 0; rd < 2; ++rd) {
    if (rd == 0) VMW(8);
    else {
      if (wid >= 6) f0 = ld8(rs, FLG_OFF + (wid == 6 ? F_LN0(lane) : F_CAL(lane)) * 128);
      const unsigned fv = wid == 3 ? FLG_OFF + F_PRG(min((lane >> 1) & 3, 2), h) * 128 + (lane & 1) * 4 : WS_QF + j * (16u << 20) + h * 8192 + lane * 128;
      if (QW) asm volatile("s_mov_b32 m0, %2\n\ts_nop 0\n\tbuffer_load_dword %0, %1, 0 offen sc1 lds" :: "v"(fv), "s"(rs), "s"(lbase + LA_P + (wid == 3 ? 640 : 1024)) : "memory");
      get_tag(P);
      VMWAIT();
    }
    BARRIER();
    u32x8 f[2][2];
    #pragma unroll
    for (int i = 0; i < 2; ++i) {
      f[i][0] = rd_frag<8192, 512>(lds + SLOT((QW ? 0 : 2) + 4 * rd + i) + toff);
      f[i][1] = rd_frag<8192, 512>(lds + SLOT((QW ? 0 : 2) + 4 * rd + i) + (toff ^ 32));
    }
    asm volatile("" : "+v"(f[0][0]), "+v"(f[0][1]), "+v"(f[1][0]), "+v"(f[1][1]));
    LBARRIER();
    #pragma unroll
    for (int i = 0; i < 2; ++i)
      #pragma unroll
      for (int t = 0; t < 2; ++t) {
        LAS unsigned char* p = lds + SLOT((QW ? 0 : 2) + 4 * rd + i) + j * 4096 + t * 2048 + lane * 16;
        *(LAS u32x4*)p = (u32x4){f[i][t][0], f[i][t][1], f[i][t][2], f[i][t][3]};
        *(LAS u32x4*)(p + 1024) = (u32x4){f[i][t][4], f[i][t][5], f[i][t][6], f[i][t][7]};
      }
  }
  const unsigned mask = rec_go(lds + LA_P + 128, P, f0, wid, lane);
  const int idx = (lane & 1) ? 0xEEEE : 0x4444;
  const unsigned ab_a = lbase + LA_MEM + lane * 16, am_a = ab_a + j * 1024;
  const unsigned t0_a = lbase + SLOT((QW ? 0 : 4) + j) + j * 4096 + lane * 16;
  const unsigned iw_a = lbase + (QW ? LA_Q : LA_K) + j * 512 + q4 * 128 + g * 32 + p4 * 4;
  const unsigned qr_a = lbase + LA_Q + g * 128 + (lane & 7) * 16;
  const unsigned kr_a = lbase + LA_K + g * 128 + (2 * q4 + (lane & 1)) * 16;
  const unsigned fr_a = lbase + LA_P + p4 * 4;
  if (tid < 12) *(LAS unsigned*)(lds + LA_P + tid * 4) = 0u;
  const unsigned vo_v = VB_AT(h) + j * 512 + lane * 4, md = lbase + LA_VM - VB_AT(h);
  const unsigned oo_v = OB_AT((lane >> 2) & 1, h) + (j * 16 + (g >> 1) * 8 + p4 * 2 + (g & 1)) * 16;
  const unsigned long long onej = 1ull << j;
  const unsigned lkx = lbase + LA_KX + j * 2048;
  u32x8 oxr = {};
  const unsigned xo_v = (lane >> 5) * 2048 + (lane & 31) * 16, vl_a = lbase + LA_VM + (j & 1) * 1024 + lane * 4;
  const int dq = 16 * (j >> 1) + (lane & 15) - 4 * g;
  const unsigned fb = step_base(cb, mask, 0);
  unsigned tgt = (unsigned)__builtin_amdgcn_s_memrealtime() * 4u;
  unsigned long long tp = 0;
  unsigned pt = 0, pd = 0;
  const unsigned long long fmask = __builtin_amdgcn_readfirstlane((16u >> wid) & 1u);
  int known = 0;
  __builtin_amdgcn_s_setprio(QW ? 2 : 1);
  LBARRIER();
  report(P, wid);
  TL(72);
  for (int s = 0; s < NSEG; ++s) {
    const unsigned sb = step_base(cb, mask, s);
    const unsigned sbm = sb - (lbase + LA_MEM);
    const unsigned fo = (unsigned)(s * NH + h) * 8192u + j * 2048 - (lbase + LA_MEM);
    GSDECL;
    RTC(h);
    lim = __builtin_amdgcn_readfirstlane(lim);
    if (QW) {
      if (wid == 3 && s > 0) {
        if (opq(0) == 0) __builtin_amdgcn_raw_buffer_store_b32((unsigned)tp * 4u - tgt, rs, 0, fb + FL_O(h), AUX_SC1);
        plan_wait(rs, fb + FL_T(s), s, lim, pt, pd);
        tgt = pt;
      }
      if (s >= known) {
        const u32x2 v = *(volatile LAS u32x2*)(lds + LA_P + 640 + (opq(0) & 3) * 8);
        if (!__any(v.y != P.tag)) known = rfl(min(min(__builtin_amdgcn_readlane(v.x, 0), __builtin_amdgcn_readlane(v.x, 1)), __builtin_amdgcn_readlane(v.x, 2)));
      }
      kv_wait(P, s, h, known, opq(0));
      const unsigned kxo = WS_KF + fo, vxo = WS_VF + fo;
      if (wid < 3) for (int i = 0; i < ADL; ++i) __builtin_amdgcn_s_sleep(1);
      dmas(rs, ab_a, kxo, lkx);
      dmas(rs, ab_a, kxo + 1024, lkx + 1024);
      dmas(rs, ab_a, WS_QF + fo, lbase + LA_P + 1024);
      dmas(rs, ab_a, WS_QF + fo + 1024, lbase + LA_P + 1024);
      if (wid == 2 && s + 1 >= known) {
        const int ln = opq(0);
        const unsigned fv = FLG_OFF + F_PRG(min((ln >> 1) & 3, 2), h) * 128 + (ln & 1) * 4;
        asm volatile("s_mov_b32 m0, %2\n\ts_nop 0\n\tbuffer_load_dword %0, %1, 0 offen sc1 lds" :: "v"(fv), "s"(rs), "s"(lbase + LA_P + 640) : "memory");
      }
      unsigned rg = 0x3c000000u;
      A_MAIN_Q();
      TL(s);
      GSFLUSH(0, 24 + wid); GSFLUSH(1, 4 + wid); GSFLUSH(2, 12 + j); GSFLUSH(3, 16 + j); GSFLUSH(4, 20 + j);
      if (s + PD < NSEG && !(opq(0) & 8)) __builtin_amdgcn_raw_buffer_store_b128(poison4(), rs, oo_v, step_base(cb, mask, s + PD), AUX_SC1);
    } else {
      const unsigned qxo = WS_QF + fo - (j & 1) * 2048, oxo = WS_OX + (unsigned)((s == 0 || s == NSEG - 1 ? s : s - 1) * NH + h) * 8192u + (j & 1) * 4096 + (j >> 1) * 1024;
      A_MAIN_K();
      if (s >= NSEG - 2) {
        __builtin_amdgcn_raw_buffer_store_b128((u32x4){oxr[0], oxr[1], oxr[2], oxr[3]}, rs, xo_v, oxo + (NSEG - 1 - s) * NH * 8192, AUX_SC1);
        __builtin_amdgcn_raw_buffer_store_b128((u32x4){oxr[4], oxr[5], oxr[6], oxr[7]}, rs, xo_v + 512, oxo + (NSEG - 1 - s) * NH * 8192, AUX_SC1);
      }
      if (wid == 7) GSFLUSH(0, 28);
      GSFLUSH(1, 4 + wid);
    }
  }
  VMWAIT();
  BARRIER();
  if (wid == 4) flag_set(P, F_PRG(4, h), NSEG, opq(0));
}

#define TSL 4
#define LB_MEM 0
#define LB_RED 16384
template <bool VW>
__device__ __forceinline__ void b_wave(LAS unsigned char* lds, Ptrs& P, int r, int c, int wid, int bs, int xg) {
  constexpr int NHD = VW ? 2 : 6;
  char* ws = P.ws;
  const int lane = opq(0), tid = wid * 64 + lane;
  const int j = wid & 3, g = lane >> 4, q4 = (lane & 15) >> 2, p4 = lane & 3;
  const int b = r * 32 + c;
  const rsrc_t rs = WS_RSRC(P);
  const rsrc_t rv = __builtin_amdgcn_make_buffer_rsrc((void*)P.Wv, 0, HD * DM * 2, RSRC_W3);
  const rsrc_t ro = __builtin_amdgcn_make_buffer_rsrc((void*)P.Wo, 0, HD * DM * 2, RSRC_W3);
  const unsigned lbase = (unsigned)(size_t)lds;
  int lim = SPIN_BUDGET;
  const int h0 = ((VW ? 2 * j : 8 + 6 * j) + 5 * c) & 31;
  u32x8 wv[16], wo[6][2][2];
  {
    if ((wid & 3) < 2 && xg < 2) {
      const unsigned nk = 8 + 2 * xg, M = (bs * 2 + (wid & 1)) * 64 + lane, row = M / nk, k = M - row * nk;
      if (row < HELP) touch(VW ? P.Wq : P.Wk, HD * DM * 2, (row < 256 ? row + (VW ? 512 : 256) : row + 512) * (HD * 2) + min(xg * 4096 + (k >> 1) * 1024 + 256, 7936u) + (k & 1) * 128, lds);
    }
    const int nl = xg == 2 ? 6 : 5;
    #pragma unroll
    for (int i = 0; i < 2; ++i) {
      const unsigned L = bs * 1024 + wid * 128 + i * 64 + lane, row = L / nl;
      touch(P.Wo, HD * DM * 2, row * (DM * 2) + (c - (bs >> 1)) * 64 + (L - row * nl) * 128, lds);
    }
    VMWAIT();
    BARRIER();
    const int pos = lane >> 2;
    const int prow = (pos & 9) | ((pos & 2) << 1) | ((pos & 4) >> 1);
    const unsigned dv0 = (unsigned)(prow * (DM * 2) + c * 64 + ((lane & 3) << 4));
    const unsigned dv1 = (unsigned)(prow * (DM * 2) + c * 64 + (((lane & 3) ^ 2) << 4));
    const int toff = wid * 16384 + (g * 16 + q4) * 64 + (((p4 >> 1) ^ ((g & 1) << 1)) << 4) + 8 * (p4 & 1);
    #pragma unroll
    for (int rd = 0; rd < NHD / 2; ++rd) {
      #pragma unroll
      for (int hh = 0; hh < 2; ++hh) {
        const int h = (h0 + 2 * rd + hh) & 31;
        #pragma unroll
        for (int i = 0; i < 8; ++i)
          DMA_O(ro, (i & 1) ? dv1 : dv0, (unsigned)(h * 128 + i * 16) * (DM * 2), lbase + wid * 16384 + hh * 8192 + i * 1024);
      }
      VMWAIT();
      #pragma unroll
      for (int hh = 0; hh < 2; ++hh)
        #pragma unroll
        for (int ks = 0; ks < 2; ++ks)
          #pragma unroll
          for (int t = 0; t < 2; ++t)
            wo[2 * rd + hh][ks][t] = rd_frag<4096, 256>(lds + (toff ^ (t * 32)) + hh * 8192 + ks * 512);
      #pragma unroll
      for (int hh = 0; hh < 2; ++hh)
        WO_PIN(VW, 2 * rd + hh);
      asm volatile("s_waitcnt lgkmcnt(0)" ::: "memory");
    }
    const int lr = lane >> 3;
    #pragma unroll
    for (int i = 0; i < 16; ++i) {
      const int sw = (((lr >> 2) & 1) | ((i & 1) << 1)) << 1;
      const unsigned dv = (unsigned)(lr * (HD * 2) + b * 128 + (((lane & 7) ^ sw) << 4));
      DMA_V(rv, dv, (unsigned)(wid * 128 + i * 8) * (HD * 2), lbase + wid * 16384 + i * 1024);
    }
    VMWAIT();
    BARRIER();
    if (VW) {
      const int trow = 16 * (g >> 1) + 4 * (g & 1) + 8 * (q4 >> 1) + (q4 & 1);
      const int tsw = ((g & 1) | ((q4 >> 1) << 1)) << 1;
      LAS unsigned char* tv = lds + trow * 128 + (((2 * j + (p4 >> 1)) ^ tsw) << 4) + 8 * (p4 & 1);
      #pragma unroll
      for (int ks = 0; ks < 16; ++ks) wv[ks] = rd_frag<4096, 256>(tv + ks * 8192);
      V_PIN();
    }
    LBARRIER();
  }
  const unsigned cb = comm_base(ws);
  get_tag(P);
  if (wid == 0) touch(P.x, SEQ * DM * 2, (SEQ - 64 + lane) * 2048 + c * 64, lds);
  const unsigned mask = rec_go(lds + LB_RED + 4096 + 64, P, (u32x2){0u, P.tag - 1u}, wid, lane);
  const int idx = (lane & 1) ? 0xEEEE : 0x4444;
  const unsigned am_a = lbase + LB_MEM + lane * 16;
  const unsigned fb = step_base(cb, mask, 0);
  volatile LAS unsigned* tsl = (volatile LAS unsigned*)(lds + LB_RED + 4096);
  if (tid < 8) tsl[tid] = tid ? 0u : (unsigned)__builtin_amdgcn_s_memrealtime() * 4u;
  unsigned dn = 600;
  int da = 570, gb = 360;
  if (VW) __builtin_amdgcn_s_setprio(2);
  LBARRIER();
  report(P, wid);
  TL(72);
  for (int s = 0; s < NSEG; ++s) {
    const unsigned sb = step_base(cb, mask, s);
    const int ln = opq(lane), gg = ln >> 4;
    GSDECL;
    RTC(32 + b);
    const int l16 = ln * 16;
    const int xc = 32 * (c & 1) + 2 * (ln & 15);
    const unsigned mo_v = MF_AT(c >> 1) + ((xc >> 4) * 16 + (4 * r + gg) * 2 + ((xc >> 3) & 1)) * 16 + (xc & 7) * 2;
    if (!VW) {
      const unsigned tm = __builtin_amdgcn_readfirstlane(*tsl), tgt = tm + DLB, la = lbase + LB_MEM + j * 2048 + l16, mo = sb + j * 2048;
      B_POLL();
      if (wid == 7) {
        const int late = (int)((unsigned)__builtin_amdgcn_s_memrealtime() * 4u - tgt) - SN0;
        const unsigned to = tm + dn + (late > 80 ? late : 0);
        if (ln == 0) tsl[1] = to;
        if (b == 0) {
          dn = da + DL1;
          if (ln == 0) { *tsl = to + gb + DL2; st8(rs, fb + FL_T(s + 1), (u32x2){to + gb + DL2, (dn << 8) | (s + 1)}); }
          const unsigned va = ld4(rs, fb + FL_O(ln & 31)), vb = ld4(rs, fb + FL_B(ln));
          if (s > 2) {
            track(da, rfl(wmax(va)), DCA, DCB);
            track(gb, rfl(wmax(vb)), DCA, DCB);
          }
        }
      }
      if (wid == 7 && s > 0 && s + 2 < NSEG) __builtin_amdgcn_raw_buffer_store_b32(~0u, rs, mo_v, step_base(cb, mask, s + 2), AUX_SC1);
    } else {
      GS(3);
      const unsigned vo = VB_AT(b >> 1) + ((b & 1) * 4 + j) * 256;
      V_PROJ();
      GS(0); GSFLUSH(0, 4 + j);
      if (s + PD < NSEG && ln < 16) st16(rs, step_base(cb, mask, s + PD) + vo + l16, poison4());
    }
    GSFLUSH(3, 24 + wid);
    if (s + 1 == NSEG) break;
    unsigned so[6];
    #pragma unroll
    for (int i = 0; i < NHD; ++i) { const int h = (h0 + i) & 31; so[i] = __builtin_amdgcn_readfirstlane(sb + OB_AT(r, h)); }
    const unsigned sn = __builtin_amdgcn_readfirstlane(step_base(cb, mask, s + 1));
    const unsigned rw_a = lbase + LB_RED + (wid * 128 + (gg & 1) * 64 + (gg >> 1) * 16 + (ln & 15)) * 4;
    const unsigned rr_a = lbase + LB_RED + ln * 8;
    if (wid == 7) {
      if (b != 0) {
        __builtin_amdgcn_s_sleep(4);
        unsigned tm = 0;
        dn = 0;
        plan_wait(rs, fb + FL_T(s + 1), s + 1, lim, tm, dn);
        if (ln == 0) *tsl = tm;
      }
      const unsigned tgt = rfl(tsl[1]);
      T_WAIT();
    }
    lim = __builtin_amdgcn_readfirstlane(lim);
    unsigned long long t1;
    if (VW) WO_V(); else WO_W();
    if (wid == 0) {
      if (ln == 0) st4(rs, fb + FL_B(b), (unsigned)t1 * 4u - tsl[1]);
    }
    if (wid == 0) {
      GSFLUSH(2, 16);
      TL(s);
    }
    GSFLUSH(1, 8 + wid);
    if (wid == 0 || wid == 4) { GSFLUSH(4, 17 + (wid >> 2)); GSFLUSH(0, 19 + (wid >> 2)); }
  }
  __builtin_amdgcn_s_setprio(0);
  const int ln = opq(0);
  TL(64);
  volatile LAS int* rdy = (volatile LAS int*)(lds + 98304 + 3 * 16384);
  if (tid == 0) *rdy = 61;
  LBARRIER();
#define T_SRC(sg, i) (WS_OX + (unsigned)((sg) * NH + ((h0 + (i)) & 31)) * 8192u + r * 1024)
#define T_MMA(i, k, f) do { const u32x8 w0 = wo[i][(k) & 1][0], w1 = wo[i][(k) & 1][1]; const int e = ((k) >> 1) * 4; \
    a0 = MFMA32(((u32x4){w0[e], w0[e + 1], w0[e + 2], w0[e + 3]}), f, a0); a1 = MFMA32(((u32x4){w1[e], w1[e + 1], w1[e + 2], w1[e + 3]}), f, a1); } while (0)
#define T_DMA(sg, i) do { _Pragma("unroll") for (int k = 0; k < 4; ++k) dmas(rs, ln * 16, T_SRC(sg, i) + k * 2048, lbase + j * 24576 + (i) * 4096 + k * 1024); } while (0)
  const int cb2 = 16 * ((ln >> 4) & 1) + 8 * (ln >> 5);
  const unsigned eo = (unsigned)((NSEG - 1 - j) * 32 + r * 16 + (ln & 15)) * 2048u + (unsigned)(c * 32 + cb2) * 2u;
  u32x4 gv, xv;
  if (VW && wid < NSEG - XS3) {
    xv = *(const u32x4*)((const char*)P.x + eo);
    cnt_wait(P, C_W4(7), 8, ln);
    gv = ld16(rs, WS_G + (unsigned)((NSEG - 1 - j) * 2 + r) * 32768u + c * 1024 + blk_in(ln & 15, cb2));
  }
  bool ahead = false;
  f32x4 k0, k1;
  #pragma unroll 1
  for (int sg = XS3; sg < NSEG; ++sg) {
    f32x4 a0 = zero4(), a1 = zero4();
    if (VW) {
      if (wid == 3) {
        for (int w = __builtin_amdgcn_readfirstlane(*rdy); w <= sg;) {
          w = max(w + 1, -wmax(-(int)flag_wait<TSL>(P, F_PRG(4, ln & 31), w + 1, ln)));
          if (ln == 0) *rdy = w;
        }
      } else while (__builtin_amdgcn_readfirstlane(*rdy) <= sg) __builtin_amdgcn_s_sleep(1);
      u32x4 of[8];
      #pragma unroll
      for (int i = 0; i < 8; ++i) of[i] = __builtin_amdgcn_raw_buffer_load_b128(rs, ln * 16 + (i & 3) * 2048, T_SRC(sg, i >> 2), AUX_SC1);
      #pragma unroll
      for (int i = 0; i < 8; ++i) T_MMA(i >> 2, i & 3, of[i]);
    } else {
      if (!ahead) {
        while (__builtin_amdgcn_readfirstlane(*rdy) <= sg) __builtin_amdgcn_s_sleep(1);
        #pragma unroll
        for (int i = 0; i < NHD; ++i) T_DMA(sg, i);
      }
      VMW(20);
      ahead = __builtin_amdgcn_readfirstlane(*rdy) > sg + 1;
      u32x4 of[2][4];
#define T_RD(i) do { _Pragma("unroll") for (int k = 0; k < 4; ++k) of[(i) & 1][k] = *(const LAS u32x4*)(lds + j * 24576 + (i) * 4096 + k * 1024 + ln * 16); } while (0)
      T_RD(0);
      #pragma unroll
      for (int i = 0; i < NHD; ++i) {
        asm volatile("s_waitcnt lgkmcnt(0)" : "+v"(of[i & 1][0]), "+v"(of[i & 1][1]), "+v"(of[i & 1][2]), "+v"(of[i & 1][3]) :: "memory");
        if (i + 1 < NHD) {
          if (ahead) VMW(16); else G_WAIT_V(16 - 4 * (i < 4 ? i : 4));
          T_RD(i + 1);
        }
        #pragma unroll
        for (int k = 0; k < 4; ++k) {
          if (ahead) dmas(rs, ln * 16, T_SRC(sg + 1, i) + k * 2048, lbase + j * 24576 + i * 4096 + k * 1024);
          T_MMA(i, k, of[i & 1][k]);
          __builtin_amdgcn_sched_barrier(0);
        }
      }
    }
    LAS f32x4* pw = (LAS f32x4*)(lds + 98304 + ((sg & 3) * 8 + wid) * 2048) + ln;
    if (wid == 0 && sg == NSEG - 1) { k0 = a0; k1 = a1; } else { pw[0] = a0; pw[64] = a1; }
  }
  if (VW && wid < NSEG - XS3) {
    LBARRIER();
    const LAS f32x4* pr = (const LAS f32x4*)(lds + 98304 + (3 - j) * 16384) + ln;
    f32x4 t[2];
    #pragma unroll
    for (int k = 0; k < 2; ++k)
      t[k] = (((wid ? pr[k * 64] : k ? k1 : k0) + pr[128 + k * 64]) + (pr[256 + k * 64] + pr[384 + k * 64])) + ((pr[512 + k * 64] + pr[640 + k * 64]) + (pr[768 + k * 64] + pr[896 + k * 64]));
    u32x4 v = epi_pack(t[0], t[1]);
    #pragma unroll
    for (int k = 0; k < 4; ++k) {
      v[k] = pack2(lo16(v[k]) * lo16(gv[k]), hi16(v[k]) * hi16(gv[k]));
      v[k] = pack2(lo16(xv[k]) + lo16(v[k]), hi16(xv[k]) + hi16(v[k]));
    }
    *(u32x4*)((char*)P.out + eo) = v;
  } else LBARRIER();
}

#define DMA_S dman
#define XDL 4
#define XPC 0
#define LS_P 131072
template <int MODE>
__device__ __forceinline__ void s_role(LAS unsigned char* lds, Ptrs& P, const u16* W, int h, unsigned obase, int nseg, int fl, int wid, int s0) {
  constexpr bool XM = MODE >= 2;
  constexpr unsigned RS = XM ? DM * 2 : HD * 2, SS = XM ? 262144u : 65536u;
  const int lane = opq(0);
  const int kh = wid >> 2, tp = wid & 3;
  const rsrc_t rs = WS_RSRC(P);
  const rsrc_t rw = __builtin_amdgcn_make_buffer_rsrc((void*)W, 0, HD * DM * 2, RSRC_W3);
  const unsigned lbase = (unsigned)(size_t)lds;
  u32x8 w[8][2];
  if (XM && XDL) {
    if (wid == 0) flag_wait(P, F_PRG(4, h), max(s0 - XDL, 0), lane);
    BARRIER();
  }
  {
    const int g = lane >> 4, q4 = (lane & 15) >> 2, p4 = lane & 3;
    const int trow = XM ? 16 * g + 4 * (q4 >> 1) + (q4 & 1) : 8 * g + q4;
#define S_SW(row) ((((row) & 1) | ((((row) >> (XM ? 2 : 1)) & 1) << 1) | ((((row) >> (XM ? 4 : 3)) & 1) << 2)) << 1)
    const int toff = trow * 256 + (((4 * tp + (p4 >> 1)) ^ S_SW(trow)) << 4) + 8 * (p4 & 1);
    unsigned dv[2];
    #pragma unroll
    for (int x = 0; x < 2; ++x) {
      const int row = 8 * wid + 4 * x + (lane >> 4);
      dv[x] = (unsigned)row * RS + (XM ? (h >> 3) * 1024 * RS + (h & 7) * 256 : h * 256) + (((lane & 15) ^ S_SW(row)) << 4);
    }
#define S_IMG(i) do { DMA_S(rw, dv[0], (i) * 64 * RS, lbase + ((i) & 7) * 16384 + wid * 2048); \
                      DMA_S(rw, dv[1], (i) * 64 * RS, lbase + ((i) & 7) * 16384 + wid * 2048 + 1024); if (XM && XPC) __builtin_amdgcn_s_sleep(XPC); } while (0)
    #pragma unroll
    for (int i = 0; i < 8; ++i) S_IMG(i);
    #pragma unroll
    for (int b = 0; b < 4; ++b) {
      if (b == 3 && !XM) get_tag(P);
      if (b < 3) VMW(8); else VMWAIT();
      BARRIER();
      if (kh == (b >> 1)) {
        #pragma unroll
        for (int i = 4 * (b & 1); i < 4 * (b & 1) + 4; ++i) {
          w[i][0] = rd_frag<XM ? 2048 : 8192, XM ? 512 : 1024>(lds + toff + i * 16384);
          w[i][1] = rd_frag<XM ? 2048 : 8192, XM ? 512 : 1024>(lds + (toff ^ 32) + i * 16384);
        }
      }
      LBARRIER();
      if (b < 2) {
        #pragma unroll
        for (int i = 4 * b + 8; i < 4 * b + 12; ++i) S_IMG(i);
      }
    }
  }
  TL(XM ? 74 : 72);
  const unsigned l16 = lane * 16;
  volatile LAS int* rd = (volatile LAS int*)(lds + LS_P + 20480);
#define X_READY(want) do { \
    if (wid == 0) { \
      const int n_ = -wmax(-(int)flag_wait(P, F_PRG(4, (h & 24) + (lane & 7)), s0 + (want), lane)) - s0; \
      if (lane == 0) *rd = max(n_, want); \
    } \
    LBARRIER(); } while (0)
  if (MODE == 2) X_READY(2);
  else if (!XM) {
    if (wid == 0) flag_wait(P, F_LN0(lane & 7), 0, lane);
    BARRIER();
  }
  unsigned sx = (XM ? WS_OX + s0 * SS + (h >> 3) * 65536 : WS_XN) + kh * 32768 + tp * 4096;
  const unsigned sd = lbase + kh * 65536 + tp * 4096;
  const unsigned xl = kh * 65536 + (XM ? l16 : blk_in(lane & 15, (lane >> 4) * 8));
  const unsigned xp = lbase + xl, pa = lbase + LS_P + tp * 4096 + l16;
#define S_MMA(a, xo, k0, k1) do { _Pragma("unroll") for (int ks = k0; ks < k1; ++ks) _Pragma("unroll") for (int i = 0; i < 4; ++i) { \
    const u32x4 x = *(const LAS u32x4*)(lds + (xo) + ks * 2048 + (i >> 1) * 1024); \
    const u32x8 wf = w[ks >> 1][i & 1]; \
    const u32x4 wh = {wf[(ks & 1) * 4], wf[(ks & 1) * 4 + 1], wf[(ks & 1) * 4 + 2], wf[(ks & 1) * 4 + 3]}; \
    a[i] = MODE == 1 ? MFMA32(x, wh, a[i]) : MFMA32(wh, x, a[i]); } } while (0)
  LAS f32x4* const pq = (LAS f32x4*)(lds + LS_P + tp * 4096 + l16);
  if (MODE == 3) {
#define X_GET(t) do { _Pragma("unroll") for (int i = 0; i < 16; ++i) \
      dmas(rs, l16, sx + (t) * SS + ((i >> 2) & 1) * 32768 + (i >> 3) * 16384 + (i & 3) * 1024, sd + ((i >> 2) & 1) * 65536 + ((t) & 1) * 32768 + (i >> 3) * 16384 + (i & 3) * 1024); } while (0)
    bool have = false;
    const unsigned pv = FLG_OFF + F_PRG(kh ? 7 : 4, kh ? h : (h & 24) + (lane & 7)) * 128;
    if (!kh) __builtin_amdgcn_s_setprio(1);
    #pragma unroll 1
    for (int t = 0; t < nseg; ++t) {
      if (!have && !kh) {
        flag_wait<2>(P, F_PRG(4, (h & 24) + (lane & 7)), s0 + t + 1, lane);
        X_GET(t);
        VMW(8);
      }
      BARRIER();
      const unsigned xp = lbase + xl + (t & 1) * 32768;
      f32x4 a[4];
      unsigned pg;
      X_ONE();
      have = t + 1 < nseg && !__any(pg < (unsigned)(s0 + t + 2));
      pq[kh * 128] = kh ? a[0] : a[2]; pq[kh * 128 + 64] = kh ? a[1] : a[3];
      if (have && !kh) X_GET(t + 1);
      LBARRIER();
      const unsigned oo = obase + (unsigned)(s0 + t) * 0x80000u + (unsigned)h * 16384u + tp * 4096 + kh * 2048 + l16;
      st16(rs, oo, __builtin_bit_cast(u32x4, (kh ? a[2] : a[0]) + pq[128 - kh * 128]));
      st16(rs, oo + 1024, __builtin_bit_cast(u32x4, (kh ? a[3] : a[1]) + pq[192 - kh * 128]));
      VMWAIT();
      BARRIER();
      if (wid == 1) flag_set(P, fl, t + 1, lane);
      TL(t);
    }
    __builtin_amdgcn_s_setprio(0);
    TL(65);
    return;
  }
  if (XM) {
    #pragma unroll
    for (int i = 0; i < 12; ++i) dmas(rs, l16, sx + (i >> 3) * SS + ((i >> 2) & 1) * 16384 + (i & 3) * 1024, sd + (i >> 2) * 16384 + (i & 3) * 1024);
    VMWAIT();
    BARRIER();
  } else {
    #pragma unroll
    for (int i = 0; i < 16; ++i) dmas(rs, l16, sx + (i >> 3) * SS + ((i >> 2) & 1) * 16384 + (i & 3) * 1024, sd + ((i >> 2) ^ 2) * 16384 + (i & 3) * 1024);
    VMW(8);
    BARRIER();
    f32x4 a[4] = {};
    S_MMA(a, xl + 32768, 0, 16);
    if (!kh) {
      #pragma unroll
      for (int i = 0; i < 4; ++i) pq[i * 64] = a[i];
    }
    LBARRIER();
    if (kh) {
      #pragma unroll
      for (int i = 0; i < 4; ++i) a[i] += pq[i * 64];
      #pragma unroll
      for (int i = 0; i < 2; ++i) {
        const f32x4 a0 = a[MODE ? i : 2 * i], a1 = a[MODE ? 2 + i : 2 * i + 1];
        if (MODE) st16(rs, obase + h * 8192 + (2 * tp + i) * 1024 + l16, (u32x4){pack2(a0[0], a0[1]), pack2(a0[2], a0[3]), pack2(a1[0], a1[1]), pack2(a1[2], a1[3])});
        else st16(rs, obase + h * 8192 + i * 4096 + tp * 1024 + l16, (u32x4){pack2(a0[0], a1[0]), pack2(a0[1], a1[1]), pack2(a0[2], a1[2]), pack2(a0[3], a1[3])});
      }
    }
    sx += SS;
    #pragma unroll
    for (int i = 0; i < 4; ++i) dmas(rs, l16, sx + SS + i * 1024, sd + 32768 + i * 1024);
    VMW(4);
    BARRIER();
    if (wid == 1) flag_set(P, fl, 1, lane);
    TL(62);
  }
  u32x8 xf[3], ac[2] = {};
  #pragma unroll
  for (int i = 0; i < 3; ++i) {
    const u32x4 a = *(const LAS u32x4*)(lds + xl + i * 2048), b = *(const LAS u32x4*)(lds + xl + i * 2048 + 1024);
    xf[i] = (u32x8){a[0], a[1], a[2], a[3], b[0], b[1], b[2], b[3]};
  }
  unsigned long long fm, pm;
  asm volatile("s_cmp_eq_u32 %2, 1\n\ts_cselect_b64 %0, 1, 0\n\ts_cmp_eq_u32 %3, 1\n\ts_cselect_b64 %1, -1, 0" : "=&s"(fm), "=&s"(pm) : "s"(wid), "s"(XM ? 0 : wid >> 1) : "scc");
  const unsigned fo = FLG_OFF + fl * 128, pd = lbase + LS_P + 16384;
  const unsigned po = 4 * 65536 + h * 2048 + (wid & 1) * 1024 - tp * 4096 - kh * 32768;
  unsigned oo = XM ? obase + (unsigned)h * 16384u + tp * 4096 + (s0 - 1) * 0x80000u : obase + (unsigned)h * 8192u + tp * (MODE ? 2048 : 1024), tmp, tmp2;
  unsigned long long em;
  int t = XM ? 0 : 1;
  if (MODE < 2) {
    #pragma unroll 1
    for (int part = 0; part < 2; ++part) {
      if (part == 1) { TL(63); if (wid == 0) setup_wait(P, lane); }
      if (MODE) S_LOOP_V(part ? nseg + 1 : 3, 0); else S_LOOP_K(part ? nseg + 1 : 3, 0);
    }
  } else {
    #pragma unroll 1
    while (t < nseg) {
      X_READY(min(t + 4, nseg));
      const int n = __builtin_amdgcn_readfirstlane(*rd), te = n >= nseg ? nseg : (n - 2) & ~1;
      TL(t);
      S_LOOP_X(te, nseg);
    }
  }
  TL(XM ? 65 : 64);
  VMWAIT();
  BARRIER();
  if (wid == 1) flag_set(P, fl, nseg, lane);
}

#define LND 1
#define CALN 6
__device__ __forceinline__ void setup_role(LAS unsigned char* lds, Ptrs& P, int cu, int wid) {
  const int lane = opq(0), tid = wid * 64 + lane;
  const rsrc_t rs = WS_RSRC(P);
  u32x4 v[8];
  const u32x4* px = (const u32x4*)(P.x + (size_t)(cu * 32 + wid * 4 + (lane >> 4)) * DM) + (lane & 15);
  #pragma unroll
  for (int i = 0; i < 8; ++i) v[i] = __builtin_nontemporal_load(px + i * 16);
  const unsigned epo = __hip_atomic_load((gu32*)(P.ws + EPO_OFF), RLX_AGENT);
  const unsigned cb = comm_base(P.ws);
  const u32x4 z = {0u, 0u, 0u, 0u};
  const int cr = 63 - cu;
  if (wid == 0 && cr < 16) st16(rs, CNT_OFF + cr * 1024 + lane * 16, z);
  if (cr < 4) st16(rs, cb + FL_B(0) + cr * 8192 + tid * 16, z);
  if (cr == 4 && tid < 256) {
    const int g4 = tid >> 6, k = (tid >> 5) & 1 & (g4 >> 1);
    st16(rs, WS_H1 + ((g4 & 1) ? 127u * 32768u : 0u) + (g4 * 8 + ((tid >> 2) & 7)) * 1024 + blk_in((g4 & 1) ? 15 - k : k, (tid & 3) * 8), z);
  }
  #pragma unroll
  for (int i = 0; i < 2; ++i) {
    const int f = i * 54 + cr;
    if (cr < PD * NCHUNK) {
      u32x4 m = {~0u, ~0u, ~0u, ~0u};
      if (f < 4) {
        const int ks = (f >> 1) * 8 + wid;
        m = *(const u32x4*)(P.mem0 + ((lane >> 1) & 7) * DM + 64 * ks + 16 * (lane >> 4) + 8 * (lane & 1));
        m.x = canon1(m.x); m.y = canon1(m.y); m.z = canon1(m.z); m.w = canon1(m.w);
      }
      st16(rs, cb + (unsigned)(f / (NCHUNK * 2)) * CB_STEP + (f % (NCHUNK * 2)) * 8192u + tid * 16, m);
    }
  }
  const unsigned bi = blk_in((wid & 3) * 4 + (lane >> 4), (lane & 3) * 8);
  const int c0 = (lane >> 2) & 3;
  ln_rows(v);
  TL(69);
  if (cu >= 8) for (int i = 0; i < LND; ++i) __builtin_amdgcn_s_sleep(32);
  #pragma unroll
  for (int i = 0; i < 8; ++i) st16(rs, WS_XN + xn_blk(cu * 2 + (wid >> 2), 4 * i + c0) + bi, v[i]);
  P.tag = __builtin_amdgcn_readfirstlane(epo) + 1u;
  ln_rows(v);
  if (cu < 8) {
    VMWAIT();
    BARRIER();
    if (wid == 0) flag_set(P, F_LN0(cu), 1, lane);
    TL(66);
  }
  asm volatile("" ::: "memory");
  #pragma unroll
  for (int i = 0; i < 8; ++i) st16(rs, WS_XN2 + (unsigned)(cu * 2 + (wid >> 2)) * 32768u + (4 * i + c0) * 1024 + bi, v[i]);
  touch(P.w1, DM * DM * 2, ((cu & 31) * 512 + tid) * 128, lds);
  VMW(9);
  BARRIER();
  if (wid == 0) flag_set(P, F_LN0(cu), 1, lane);
  TL(70);
  if (wid == 0) VMW(2); else VMW(1);
  BARRIER();
  if (wid == 0) flag_set(P, F_LN1(cu), 1, lane);
  TL(71);
  if (cu < 32 && (wid >> 1) == 1) {
    const int id = cu * 2 + (wid & 1);
    unsigned best[2] = {65535u, 65535u};
    #pragma unroll
    for (int it = 0; it < CALN; ++it) {
      const unsigned long long c0 = __builtin_amdgcn_s_memtime();
      asm volatile("s_waitcnt lgkmcnt(0)" ::: "memory");
      if (lane == 0) st4(rs, cb + (id & 3) * CB_STEP + (30 + ((id >> 2) & 1)) * 16384u + (it & 1) * 8192u + (id >> 3) * 1024u, 0u);
      VMWAIT();
      const unsigned d = (unsigned)(__builtin_amdgcn_s_memtime() - c0);
      if (it >= 2) best[it & 1] = min(best[it & 1], d);
    }
    flag_set(P, F_CAL(id), (best[0] << 16) | best[1], lane);
  }
}

#define CPF 1
__global__ __launch_bounds__(512, 2) void mega_kernel(const u16* x, const u16* mem0, const u16* Wq, const u16* Wk, const u16* Wv, const u16* Wo,
                                                     const u16* w1, const u16* w2, const u16* w3, const u16* w4, u16* out, char* ws) {
  __shared__ __attribute__((aligned(16))) unsigned char smem[163840];
  LAS unsigned char* lds = (LAS unsigned char*)smem;
  Ptrs P;
  P.x = x; P.mem0 = mem0; P.Wq = Wq; P.Wk = Wk; P.Wv = Wv; P.Wo = Wo; P.w1 = w1; P.w2 = w2; P.w3 = w3; P.w4 = w4; P.out = out; P.ws = ws;
  const int xcd = blockIdx.x & 7, slot = blockIdx.x >> 3;
  const int wid = __builtin_amdgcn_readfirstlane(threadIdx.x >> 6);
  TL(79);
  P.tag = 0;
  if (wid == 7) {
    unsigned long long pc;
    unsigned len;
    asm volatile("s_getpc_b64 %0\nMK_BEG:\n\ts_mov_b32 %1, MK_END-MK_BEG" : "=s"(pc), "=s"(len));
    touch((const void*)pc, len, (slot * 64 + opq(0)) * 128, lds);
  }
  const int na = xcd == 2 ? 8 : 12;
  const bool isa = xcd < 3 && slot < na;
  const int ah = xcd == 2 ? slot * 4 + 2 : xcd * 16 + (slot & 3) * 4 + (slot >> 2) + (slot >> 3), bs = slot - na, br = bs & 1, bc = (xcd == 1 ? 22 : xcd * 5) + (bs >> 1);
  if (isa) {
    if (wid < 4) a_wave<true>(lds, P, ah, wid); else a_wave<false>(lds, P, ah, wid);
  } else if (xcd < 3) {
    if (wid < 4) b_wave<true>(lds, P, br, bc, wid, bs, xcd); else b_wave<false>(lds, P, br, bc, wid, bs, xcd);
  } else {
    if ((xcd & 3) == 3) {
      setup_role(lds, P, (xcd >> 2) * 32 + slot, wid);
    } else if (xcd == 6) s_role<1>(lds, P, Wv, slot, WS_VF, NSEG, F_PRG(2, slot), wid, 0);
    else s_role<0>(lds, P, xcd == 4 ? Wk : Wq, slot, xcd == 4 ? WS_KF : WS_QF, NSEG, F_PRG(xcd - 4, slot), wid, 0);
    if ((xcd & 3) == 3) mlp_role(lds, P, xcd >> 2, slot, wid);
    else if (xcd == 6) s_role<3>(lds, P, Wo, slot, WS_PT, XS3 - XS2, F_PRG(7, slot), wid, XS2);
    else s_role<2>(lds, P, Wo, slot, WS_PT, xcd == 4 ? XS1 : XS2 - XS1, F_PRG(xcd + 1, slot), wid, xcd == 4 ? 0 : XS1);
    report(P, wid);
    fin_role(lds, P, wid);
    if (blockIdx.x == 3 && wid == 0) {
      cnt_wait(P, C_ARR, 256, threadIdx.x);
      if (threadIdx.x == 0) __hip_atomic_store((gu32*)(ws + EPO_OFF), P.tag, RLX_AGENT);
    }
  }
  asm volatile("MK_END:");
  TL(78);
}

extern "C" void kernel_launch(void* const* d_in, const int* in_sizes, int n_in, void* d_out, int out_size,
                              void* d_ws, size_t ws_size, hipStream_t stream) {
  mega_kernel<<<256, 512, 0, stream>>>((const u16*)d_in[0], (const u16*)d_in[1], (const u16*)d_in[2], (const u16*)d_in[3], (const u16*)d_in[4],
                                       (const u16*)d_in[5], (const u16*)d_in[8], (const u16*)d_in[9], (const u16*)d_in[10], (const u16*)d_in[11],
                                       (u16*)d_out, (char*)d_ws);
}
```

```cpp
#include <hip/hip_runtime.h>
#include <hip/hip_bf16.h>
#include <stdint.h>

#define SEQ 2048
#define DM 1024
#define NH 32
#define DH 128
#define HD (NH * DH)
#define MM 8
#define SEG 32
#define NSEG (SEQ / SEG)
#define FF 3584

typedef unsigned short u16;
typedef __attribute__((ext_vector_type(8))) short bf16x8;
typedef __attribute__((ext_vector_type(4))) short bf16x4;
typedef __attribute__((ext_vector_type(4))) float f32x4;
typedef __attribute__((ext_vector_type(4))) unsigned u32x4;
typedef __attribute__((ext_vector_type(2))) unsigned u32x2;
typedef __attribute__((ext_vector_type(2))) float f32x2;
typedef __attribute__((ext_vector_type(2))) __bf16 bf16v2;
typedef __attribute__((ext_vector_type(8))) unsigned u32x8;
#define LAS __attribute__((address_space(3)))
#define GAS __attribute__((address_space(1)))

__device__ __forceinline__ unsigned pack2(float lo, float hi) {
  return __builtin_bit_cast(unsigned, __builtin_convertvector((f32x2){lo, hi}, bf16v2));
}
__device__ __forceinline__ float rb1(float x) { return __uint_as_float(pack2(0.f, x)); }

typedef GAS unsigned gu32;
#define RLX_AGENT __ATOMIC_RELAXED, __HIP_MEMORY_SCOPE_AGENT
#define SPIN_CAP (1u << 20)
#define AUX_SC1 16
#define RSRC_W3 0x00020000
typedef __amdgpu_buffer_rsrc_t rsrc_t;

__device__ __forceinline__ u32x4 ld16(rsrc_t rs, unsigned off) { return __builtin_amdgcn_raw_buffer_load_b128(rs, off, 0, AUX_SC1); }
__device__ __forceinline__ u32x2 ld8(rsrc_t rs, unsigned off) { return __builtin_amdgcn_raw_buffer_load_b64(rs, off, 0, AUX_SC1); }
__device__ __forceinline__ unsigned ld4(rsrc_t rs, unsigned off) { return __builtin_amdgcn_raw_buffer_load_b32(rs, off, 0, AUX_SC1); }
__device__ __forceinline__ void st16(rsrc_t rs, unsigned off, u32x4 v) { __builtin_amdgcn_raw_buffer_store_b128(v, rs, off, 0, AUX_SC1); }
__device__ __forceinline__ void st8(rsrc_t rs, unsigned off, u32x2 v) { __builtin_amdgcn_raw_buffer_store_b64(v, rs, off, 0, AUX_SC1); }
__device__ __forceinline__ void st4(rsrc_t rs, unsigned off, unsigned v) { __builtin_amdgcn_raw_buffer_store_b32(v, rs, off, 0, AUX_SC1); }
__device__ __forceinline__ float lo16(unsigned w) { return __uint_as_float(w << 16); }
__device__ __forceinline__ float hi16(unsigned w) { return __uint_as_float(w & 0xffff0000u); }

__device__ __forceinline__ float xsum16_32(float v) {
  unsigned u = __float_as_uint(v);
  auto a = __builtin_amdgcn_permlane16_swap(u, u, false, false);
  v = __uint_as_float(a[0]) + __uint_as_float(a[1]);
  u = __float_as_uint(v);
  auto b = __builtin_amdgcn_permlane32_swap(u, u, false, false);
  return __uint_as_float(b[0]) + __uint_as_float(b[1]);
}

__device__ __forceinline__ unsigned canon1(unsigned w) { return min(w, 0xfffffffeu); }
__device__ __forceinline__ unsigned max4(u32x4 v) { return max(max(v.x, v.y), max(v.z, v.w)); }
__device__ __forceinline__ int opq(int) { int v; asm volatile("v_mbcnt_lo_u32_b32 %0, -1, 0\n\tv_mbcnt_hi_u32_b32 %0, -1, %0" : "=v"(v)); return v; }
__device__ __forceinline__ u32x4 poison4() { unsigned m = ~0u; asm volatile("" : "+v"(m)); return (u32x4){m, m, m, m}; }
__device__ __forceinline__ f32x4 zero4() { float m = 0.f; asm volatile("" : "+v"(m)); return (f32x4){m, m, m, m}; }
#define MFMA32(a, b, c) __builtin_amdgcn_mfma_f32_16x16x32_bf16(__builtin_bit_cast(bf16x8, a), __builtin_bit_cast(bf16x8, b), c, 0, 0, 0)
#define MFMA16(a, b, c) __builtin_amdgcn_mfma_f32_16x16x16bf16_1k(__builtin_bit_cast(bf16x4, a), __builtin_bit_cast(bf16x4, b), c, 0, 0, 0)
#define LBARRIER() do { asm volatile("s_waitcnt lgkmcnt(0)" ::: "memory"); __builtin_amdgcn_s_barrier(); asm volatile("" ::: "memory"); } while (0)
#define BARRIER() do { asm volatile("" ::: "memory"); __builtin_amdgcn_s_barrier(); asm volatile("" ::: "memory"); } while (0)
#define VMWAIT() asm volatile("s_waitcnt vmcnt(0)" ::: "memory")
#define VMW(n) asm volatile("s_waitcnt vmcnt(" #n ")" ::: "memory")
#define DELAY(n) do { if ((n) > 0) __builtin_amdgcn_s_sleep(n); } while (0)

#define EPO_OFF 0
#define TMO_OFF 128
#define FLG_OFF 256
#define F_LN0(i) (i)
#define F_LN1(i) (64 + (i))
#define F_CAL(i) (128 + (i))
#define F_PRG(t, h) (192 + (t) * 32 + (h))
#define CNT_OFF 65536
#define C_W1(p2) (p2)
#define C_W2(p2) (16 + (p2))
#define C_LN(p2) (32 + (p2))
#define C_W3(p2) (48 + (p2))
#define C_W4(p2) (64 + (p2))
#define C_HEAD 80
#define C_ARR 81
#define NCNT 128
#define WS_XN (1u << 20)
#define WS_XN2 (WS_XN + (4u << 20))
#define WS_QF (WS_XN2 + (4u << 20))
#define WS_KF (WS_QF + (16u << 20))
#define WS_VF (WS_KF + (16u << 20))
#define WS_OX (WS_VF + (16u << 20))
#define WS_H1 (WS_OX + (16u << 20) + (1u << 20))
#define WS_X1 (WS_H1 + (4u << 20) + 65536u)
#define WS_X1N (WS_X1 + (4u << 20))
#define WS_H3 (WS_X1N + (4u << 20))
#define WS_G (WS_H3 + (14u << 20))
#define WS_PT (WS_G + (4u << 20))
#define WS_COMM (WS_PT + (32u << 20))
#define WS_END (WS_COMM + (4u << 20))
#define CB_STEP 524288u
#define PD 3
__device__ __forceinline__ unsigned comm_base(const char* ws) { return WS_COMM + ((0u - ((unsigned)(size_t)ws + WS_COMM)) & 0x1fffffu); }
__device__ __forceinline__ unsigned step_base(unsigned cb, unsigned mask, int s) { return cb + (unsigned)(s & 3) * CB_STEP + mask; }
#define MF_AT(ks) (((ks) >> 3) * 16384u + ((ks) & 7) * 1024u)
#define OB_AT(r, h) ((2u + (r) * 4u + ((h) >> 3)) * 16384u + ((h) & 7) * 1024u)
#define VB_AT(h) ((10u + ((h) >> 2)) * 16384u + ((h) & 3) * 2048u)
#define XS1 22
#define XS2 44
#define XS3 62
#define CSL 8
#define FSL 6
#define SQUICK 3
#define NCHUNK 18
#define FL_O(h) (29u * 16384u + (h) * 128u)
#define FL_B(b) (28u * 16384u + (b) * 128u)
#define FL_T(s) FL_O(32 + ((s) & 3))

struct Ptrs { const u16 *x, *mem0, *Wq, *Wk, *Wv, *Wo, *w1, *w2, *w3, *w4; u16* out; char* ws; unsigned tag; };
#define WS_RSRC(P) __builtin_amdgcn_make_buffer_rsrc((void*)(P).ws, 0, WS_END, RSRC_W3)

__device__ __forceinline__ bool spin_fail(unsigned& spins, const Ptrs& P, int lane) {
  asm volatile("" ::: "memory");
  if (++spins > SPIN_CAP) {
    unsigned tv;
    asm volatile("v_mov_b32 %0, %1" : "=v"(tv) : "s"(P.tag));
    if (lane == 0) __builtin_amdgcn_raw_buffer_store_b32(tv, WS_RSRC(P), 0, TMO_OFF, AUX_SC1);
    return true;
  }
  return (spins & 255u) == 0u && (unsigned)__builtin_amdgcn_readfirstlane(__builtin_amdgcn_raw_buffer_load_b32(WS_RSRC(P), 0, TMO_OFF, AUX_SC1)) == P.tag;
}
__device__ __forceinline__ unsigned cnt_load(char* ws, int idx) {
  return __builtin_amdgcn_readfirstlane(__hip_atomic_load((gu32*)(ws + CNT_OFF + idx * 128), RLX_AGENT));
}
__device__ __forceinline__ void cnt_add(char* ws, int idx, int lane) {
  asm volatile("s_waitcnt vmcnt(0)" ::: "memory");
  if (lane == 0) __hip_atomic_fetch_add((gu32*)(ws + CNT_OFF + idx * 128), 1u, RLX_AGENT);
}
__device__ __forceinline__ void cnt_wait(const Ptrs& P, int idx, unsigned want, int lane) {
  for (unsigned spins = 0;;) {
    if (cnt_load(P.ws, idx) >= want) break;
    __builtin_amdgcn_s_sleep(CSL);
    if (spin_fail(spins, P, lane)) break;
  }
}
template <int SL = FSL>
__device__ __forceinline__ unsigned flag_wait(const Ptrs& P, int f, unsigned want, int lane) {
  const rsrc_t rs = WS_RSRC(P);
  u32x2 v;
  for (unsigned spins = 0;;) {
    v = ld8(rs, FLG_OFF + f * 128);
    if (!__any(v.y != P.tag || v.x < want)) break;
    __builtin_amdgcn_s_sleep(SL);
    if (spin_fail(spins, P, lane)) break;
  }
  asm volatile("" ::: "memory");
  return v.x;
}
__device__ __forceinline__ void flag_set(const Ptrs& P, int f, unsigned val, int lane) {
  if (lane == 0) st8(WS_RSRC(P), FLG_OFF + f * 128, (u32x2){val, P.tag});
}
__device__ __forceinline__ void get_tag(Ptrs& P) { P.tag = __builtin_amdgcn_readfirstlane(__hip_atomic_load((gu32*)(P.ws + EPO_OFF), RLX_AGENT)) + 1u; }
__device__ __forceinline__ void setup_wait(const Ptrs& P, int lane) { flag_wait(P, F_LN0(lane), 0, lane); }
__device__ __forceinline__ void clock_wait(unsigned tgt) {
  while ((int)(tgt - (unsigned)__builtin_amdgcn_s_memrealtime()) > 0) {}
}
__device__ __forceinline__ void report(const Ptrs& P, int wid) {
  if (wid == 0 && opq(0) == 0) __hip_atomic_fetch_add((gu32*)(P.ws + CNT_OFF + C_ARR * 128), 1u, RLX_AGENT);
}
__device__ __forceinline__ void kv_wait(const Ptrs& P, int s, int h, int& known, int lane) {
  if (s < known) return;
  const unsigned v = flag_wait(P, F_PRG(min(lane, 2), h), s + 1, lane);
  known = __builtin_amdgcn_readfirstlane(min(min(__builtin_amdgcn_readlane(v, 0), __builtin_amdgcn_readlane(v, 1)), __builtin_amdgcn_readlane(v, 2)));
}
__device__ __forceinline__ int wmax(int v) {
  v = max(v, __builtin_amdgcn_update_dpp(0, v, 0x128, 0xf, 0xf, false));
  v = max(v, __builtin_amdgcn_update_dpp(0, v, 0x124, 0xf, 0xf, false));
  v = max(v, __builtin_amdgcn_update_dpp(0, v, 0x122, 0xf, 0xf, false));
  v = max(v, __builtin_amdgcn_update_dpp(0, v, 0x121, 0xf, 0xf, false));
  return max(max(__builtin_amdgcn_readlane(v, 0), __builtin_amdgcn_readlane(v, 16)), max(__builtin_amdgcn_readlane(v, 32), __builtin_amdgcn_readlane(v, 48)));
}
__device__ __forceinline__ unsigned near_half(unsigned v) { return wmax(-(int)(v & 65535u)) > wmax(-(int)(v >> 16)) ? 8192u : 0u; }
__device__ __forceinline__ void dmaw(rsrc_t rs, unsigned voff, unsigned soff, unsigned ldsaddr) {
  asm volatile("s_mov_b32 m0, %3\n\ts_nop 0\n\tbuffer_load_dwordx4 %0, %1, %2 offen lds" :: "v"(voff), "s"(rs), "s"(soff), "s"(ldsaddr) : "memory");
}
__device__ __forceinline__ void dman(rsrc_t rs, unsigned voff, unsigned soff, unsigned ldsaddr) {
  asm volatile("s_mov_b32 m0, %3\n\ts_nop 0\n\tbuffer_load_dwordx4 %0, %1, %2 offen nt lds" :: "v"(voff), "s"(rs), "s"(soff), "s"(ldsaddr) : "memory");
}
__device__ __forceinline__ void dmas(rsrc_t rs, unsigned voff, unsigned soff, unsigned ldsaddr) {
  asm volatile("s_mov_b32 m0, %3\n\ts_nop 0\n\tbuffer_load_dwordx4 %0, %1, %2 offen sc1 lds" :: "v"(voff), "s"(rs), "s"(soff), "s"(ldsaddr) : "memory");
}
__device__ __forceinline__ void touch(const void* w, unsigned bytes, unsigned voff, LAS unsigned char* lds) {
  asm volatile("s_mov_b32 m0, %2\n\ts_nop 0\n\tbuffer_load_dword %0, %1, 0 offen lds"
               :: "v"(voff), "s"(__builtin_amdgcn_make_buffer_rsrc((void*)w, 0, bytes, RSRC_W3)), "s"((unsigned)(size_t)lds + 148480) : "memory");
}
__device__ __forceinline__ u32x2 trd(LAS unsigned char* p) {
  return __builtin_bit_cast(u32x2, __builtin_amdgcn_ds_read_tr16_b64_v4i16((LAS bf16x4*)p));
}
template <int BO, int UO>
__device__ __forceinline__ u32x8 rd_frag(LAS unsigned char* p) {
  const u32x2 a = trd(p), b = trd(p + UO), c = trd(p + BO), d = trd(p + BO + UO);
  return (u32x8){a.x, a.y, b.x, b.y, c.x, c.y, d.x, d.y};
}

#define TL(i) do {} while (0)
#define TLW(i) do {} while (0)
#define GSDECL
#define GS(i) do {} while (0)
#define GSFLUSH(k, i) do {} while (0)

#define QPRIO 0
#define VPOS 3
#define VLDI "buffer_load_dword v254, v17, %[rs], %[sb] offen sc1\n\tbuffer_load_dword v255, v17, %[rs], %[sb] offen offset:256 sc1\n\t"
#define VLD(p) VLD##p
#define VLD0
#define VLD1
#define VLD2
#define VLD3 VLDI
#define VLD4
#define DEC1 1
#define DEC2 1
#define DGA
#define DGB
#define DGC
#define DGO
#define DGP
#define DGQ
#define PACE "s_sleep 1\n\t"
#define STR_(x) #x
#define STR(x) STR_(x)
#define GSW
#define GSA(k)
#define GSO(k)
#define A_MAIN_Q() \
  asm volatile( \
    "v_mov_b32 v253, %[tag]\n\t" \
    "buffer_load_dwordx4 v[20:23], v12, %[rs], %[vx] offen sc1\n\t" \
    "buffer_load_dwordx4 v[24:27], v12, %[rs], %[vx] offen offset:1024 sc1\n\t" \
    "ds_read_b128 v[236:239], v9 offset:0\n\t" \
    "ds_read_b128 v[240:243], v9 offset:1024\n\t" \
    "ds_read_b128 v[244:247], v9 offset:2048\n\t" \
    "ds_read_b128 v[248:251], v9 offset:3072\n\t" \
    "v_mov_b32 v0, 0\n\t" \
    "v_mov_b32 v1, 0\n\t" \
    "v_mov_b32 v2, 0\n\t" \
    "v_mov_b32 v3, 0\n\t" \
    "v_mov_b32 v4, 0\n\t" \
    "v_mov_b32 v5, 0\n\t" \
    "v_mov_b32 v6, 0\n\t" \
    "v_mov_b32 v7, 0\n\t" \
    "s_cmp_lg_u32 %[sc], 3\n\t" \
    "s_cbranch_scc1 PB_%=\n\t" \
    "TWA_%=:\n\t" \
    "s_memrealtime s[88:89]\n\t" \
    "s_memtime s[90:91]\n\t" \
    "s_waitcnt lgkmcnt(0)\n\t" \
    "s_lshl_b32 s88, s88, 2\n\t" \
    "s_sub_u32 s89, %[tgt], s88\n\t" \
    "s_cmp_gt_i32 s89, 16\n\t" \
    "s_cbranch_scc1 TWA_%=\n\t" \
    "s_mul_i32 s89, s89, 11\n\t" \
    "s_ashr_i32 s89, s89, 1\n\t" \
    "s_add_u32 s89, s89, s90\n\t" \
    DGB \
    "TCA_%=:\n\t" \
    "s_memtime s[92:93]\n\t" \
    "s_waitcnt lgkmcnt(0)\n\t" \
    "s_sub_u32 s93, s89, s92\n\t" \
    "s_cmp_gt_i32 s93, 0\n\t" \
    "s_cbranch_scc1 TCA_%=\n\t" \
    "PB_%=:\n\t" \
    "s_waitcnt lgkmcnt(0)\n\t" \
    "s_barrier\n\t" \
    "buffer_load_dwordx4 v[220:223], v8, %[rs], %[m0] offen sc1\n\t" \
    "buffer_load_dwordx4 v[224:227], v8, %[rs], %[m1] offen sc1\n\t" \
    "PM_%=:\n\t" \
    "s_waitcnt vmcnt(1)\n\t" \
    "ds_write_b128 v8, v[220:223] offset:0\n\t" \
    "v_max3_u32 v252, v220, v221, v222\n\t" \
    "s_waitcnt vmcnt(0)\n\t" \
    "ds_write_b128 v8, v[224:227] offset:4096\n\t" \
    "v_max3_u32 v252, v252, v223, v224\n\t" \
    "v_max3_u32 v252, v252, v225, v226\n\t" \
    "v_max_u32 v252, v252, v227\n\t" \
    "v_cmp_eq_u32 vcc, -1, v252\n\t" \
    "s_cbranch_vccz PD_%=\n\t" \
    "s_sub_u32 %[lim], %[lim], 1\n\t" \
    "s_cmp_gt_i32 %[lim], 0\n\t" \
    "s_cbranch_scc0 PD_%=\n\t" \
    "s_sleep " STR(CADM) "\n\t" \
    "buffer_load_dwordx4 v[220:223], v8, %[rs], %[m0] offen sc1\n\t" \
    "buffer_load_dwordx4 v[224:227], v8, %[rs], %[m1] offen sc1\n\t" \
    "s_branch PM_%=\n\t" \
    "PD_%=:\n\t" \
    GSA(0) \
    "v_smfmac_f32_16x16x64_bf16 v[0:3], v[220:223], v[236:243], v10\n\t" \
    "v_xor_b32 v9, 16384, v9\n\t" \
    "ds_read_b128 v[236:239], v9 offset:0\n\t" \
    "ds_read_b128 v[240:243], v9 offset:1024\n\t" \
    "v_smfmac_f32_16x16x64_bf16 v[4:7], v[220:223], v[244:251], v10\n\t" \
    "ds_read_b128 v[244:247], v9 offset:2048\n\t" \
    "ds_read_b128 v[248:251], v9 offset:3072\n\t" \
    "s_waitcnt lgkmcnt(4)\n\t" \
    "s_barrier\n\t" \
    "ds_read_b128 v[228:231], v8 offset:8192\n\t" \
    "ds_read_b128 v[232:235], v8 offset:12288\n\t" \
    "v_xor_b32 v8, 1024, v8\n\t" \
    "ds_read_b128 v[220:223], v8 offset:0\n\t" \
    "v_smfmac_f32_16x16x64_bf16 v[0:3], v[224:227], v[28:35], v10\n\t" \
    "v_smfmac_f32_16x16x64_bf16 v[4:7], v[224:227], v[36:43], v10\n\t" \
    "ds_read_b128 v[224:227], v8 offset:4096\n\t" \
    "s_waitcnt lgkmcnt(3)\n\t" \
    "v_smfmac_f32_16x16x64_bf16 v[0:3], v[228:231], v[92:99], v10\n\t" \
    "v_smfmac_f32_16x16x64_bf16 v[4:7], v[228:231], v[100:107], v10\n\t" \
    "ds_read_b128 v[228:231], v8 offset:8192\n\t" \
    "s_waitcnt lgkmcnt(3)\n\t" \
    "v_smfmac_f32_16x16x64_bf16 v[0:3], v[232:235], v[156:163], v10\n\t" \
    "v_smfmac_f32_16x16x64_bf16 v[4:7], v[232:235], v[164:171], v10\n\t" \
    "ds_read_b128 v[232:235], v8 offset:12288\n\t" \
    "s_waitcnt lgkmcnt(3)\n\t" \
    "v_smfmac_f32_16x16x64_bf16 v[0:3], v[220:223], v[236:243], v10\n\t" \
    "v_xor_b32 v9, 49152, v9\n\t" \
    "ds_read_b128 v[236:239], v9 offset:0\n\t" \
    "v_smfmac_f32_16x16x64_bf16 v[4:7], v[220:223], v[244:251], v10\n\t" \
    "ds_read_b128 v[240:243], v9 offset:1024\n\t" \
    "s_waitcnt lgkmcnt(4)\n\t" \
    "v_smfmac_f32_16x16x64_bf16 v[0:3], v[224:227], v[44:51], v10\n\t" \
    "v_xor_b32 v8, 3072, v8\n\t" \
    "ds_read_b128 v[220:223], v8 offset:4096\n\t" \
    "v_smfmac_f32_16x16x64_bf16 v[4:7], v[224:227], v[52:59], v10\n\t" \
    "ds_read_b128 v[244:247], v9 offset:2048\n\t" \
    "s_waitcnt lgkmcnt(5)\n\t" \
    "v_smfmac_f32_16x16x64_bf16 v[0:3], v[228:231], v[108:115], v10\n\t" \
    "ds_read_b128 v[224:227], v8 offset:8192\n\t" \
    "v_smfmac_f32_16x16x64_bf16 v[4:7], v[228:231], v[116:123], v10\n\t" \
    "ds_read_b128 v[248:251], v9 offset:3072\n\t" \
    "s_waitcnt lgkmcnt(6)\n\t" \
    "v_smfmac_f32_16x16x64_bf16 v[0:3], v[232:235], v[172:179], v10\n\t" \
    "ds_read_b128 v[228:231], v8 offset:0\n\t" \
    "v_smfmac_f32_16x16x64_bf16 v[4:7], v[232:235], v[180:187], v10\n\t" \
    "ds_read_b128 v[232:235], v8 offset:12288\n\t" \
    "s_waitcnt lgkmcnt(5)\n\t" \
    "v_smfmac_f32_16x16x64_bf16 v[0:3], v[220:223], v[60:67], v10\n\t" \
    "v_smfmac_f32_16x16x64_bf16 v[4:7], v[220:223], v[68:75], v10\n\t" \
    "v_xor_b32 v8, 1024, v8\n\t" \
    "ds_read_b128 v[220:223], v8 offset:4096\n\t" \
    "s_waitcnt lgkmcnt(4)\n\t" \
    "v_smfmac_f32_16x16x64_bf16 v[0:3], v[224:227], v[124:131], v10\n\t" \
    "v_smfmac_f32_16x16x64_bf16 v[4:7], v[224:227], v[132:139], v10\n\t" \
    "ds_read_b128 v[224:227], v8 offset:8192\n\t" \
    "s_waitcnt lgkmcnt(3)\n\t" \
    "v_smfmac_f32_16x16x64_bf16 v[0:3], v[228:231], v[236:243], v10\n\t" \
    "v_xor_b32 v9, 16384, v9\n\t" \
    "ds_read_b128 v[236:239], v9 offset:0\n\t" \
    "v_smfmac_f32_16x16x64_bf16 v[4:7], v[228:231], v[244:251], v10\n\t" \
    "ds_read_b128 v[240:243], v9 offset:1024\n\t" \
    "s_waitcnt lgkmcnt(4)\n\t" \
    "v_smfmac_f32_16x16x64_bf16 v[0:3], v[232:235], v[188:195], v10\n\t" \
    "ds_read_b128 v[228:231], v8 offset:12288\n\t" \
    "v_smfmac_f32_16x16x64_bf16 v[4:7], v[232:235], v[196:203], v10\n\t" \
    "ds_read_b128 v[244:247], v9 offset:2048\n\t" \
    "s_waitcnt lgkmcnt(5)\n\t" \
    "v_smfmac_f32_16x16x64_bf16 v[0:3], v[220:223], v[76:83], v10\n\t" \
    "ds_read_b128 v[232:235], v8 offset:0\n\t" \
    "v_xor_b32 v8, 3072, v8\n\t" \
    "v_smfmac_f32_16x16x64_bf16 v[4:7], v[220:223], v[84:91], v10\n\t" \
    "ds_read_b128 v[248:251], v9 offset:3072\n\t" \
    "s_waitcnt lgkmcnt(6)\n\t" \
    "v_smfmac_f32_16x16x64_bf16 v[0:3], v[224:227], v[140:147], v10\n\t" \
    "ds_read_b128 v[220:223], v12 offset:24576\n\t" \
    "v_smfmac_f32_16x16x64_bf16 v[4:7], v[224:227], v[148:155], v10\n\t" \
    "ds_read_b128 v[224:227], v12 offset:28672\n\t" \
    "s_waitcnt lgkmcnt(5)\n\t" \
    "v_smfmac_f32_16x16x64_bf16 v[0:3], v[228:231], v[204:211], v10\n\t" \
    "v_smfmac_f32_16x16x64_bf16 v[4:7], v[228:231], v[212:219], v10\n\t" \
    "ds_read_b128 v[228:231], v12 offset:25600\n\t" \
    "s_waitcnt lgkmcnt(3)\n\t" \
    "v_smfmac_f32_16x16x64_bf16 v[0:3], v[232:235], v[236:243], v10\n\t" \
    "v_xor_b32 v9, 49152, v9\n\t" \
    "v_smfmac_f32_16x16x64_bf16 v[4:7], v[232:235], v[244:251], v10\n\t" \
    VLD(4) \
    "s_nop 5\n\t" \
    "v_add_f32 v0, v0, v1\n\t" \
    "v_add_f32 v2, v2, v3\n\t" \
    "v_add_f32 v4, v4, v5\n\t" \
    "v_add_f32 v6, v6, v7\n\t" \
    "v_mul_f32 v0, %[sl], v0\n\t" \
    "v_mul_f32 v2, %[sl], v2\n\t" \
    "v_mul_f32 v4, %[sl], v4\n\t" \
    "v_mul_f32 v6, %[sl], v6\n\t" \
    "v_cvt_pk_bf16_f32 v0, v0, v4\n\t" \
    "v_cvt_pk_bf16_f32 v2, v2, v6\n\t" \
    "ds_write2_b32 v11, v0, v2 offset0:0 offset1:4\n\t" \
    "s_mov_b64 exec, %[onej]\n\t" \
    "ds_write_b32 v16, v253\n\t" \
    "s_mov_b64 exec, -1\n\t" \
    GSA(1) \
    "s_setprio " STR(QPRIO) "\n\t" \
    "ds_read_b128 v[232:235], v12 offset:29696\n\t" \
    "s_nop 5\n\t" \
    "ds_read_b128 v[0:3], v12 offset:26624\n\t" \
    "s_nop 5\n\t" \
    "ds_read_b128 v[4:7], v12 offset:27648\n\t" \
    "s_nop 5\n\t" \
    "ds_read_b32 v252, v16\n\t" \
    "ds_read_b128 v[236:239], v14 offset:0\n\t" \
    "s_nop 5\n\t" \
    "ds_read_b128 v[240:243], v14 offset:512\n\t" \
    "s_nop 5\n\t" \
    "ds_read_b128 v[244:247], v14 offset:1024\n\t" \
    "s_nop 5\n\t" \
    VLD(3) \
    "ds_read_b128 v[248:251], v14 offset:1536\n\t" \
    "s_nop 5\n\t" \
    "s_setprio 0\n\t" \
    "s_waitcnt lgkmcnt(4)\n\t" \
    "v_cmp_ne_u32 vcc, v253, v252\n\t" \
    "s_cbranch_vccnz RP_%=\n\t" \
    "CP_%=:\n\t" \
    VLD(0) \
    "s_waitcnt lgkmcnt(2)\n\t" \
    "v_mfma_f32_16x16x32_bf16 v[220:223], v[220:223], v[236:239], 0\n\t" \
    "v_mfma_f32_16x16x32_bf16 v[224:227], v[224:227], v[236:239], 0\n\t" \
    "v_mfma_f32_16x16x32_bf16 v[220:223], v[228:231], v[240:243], v[220:223]\n\t" \
    "ds_read_b128 v[228:231], v12 offset:30720\n\t" \
    "v_mfma_f32_16x16x32_bf16 v[224:227], v[232:235], v[240:243], v[224:227]\n\t" \
    "ds_read_b128 v[232:235], v12 offset:31744\n\t" \
    "s_setprio " STR(QPRIO) "\n\t" \
    VLD(1) \
    "s_waitcnt lgkmcnt(2)\n\t" \
    "v_mfma_f32_16x16x32_bf16 v[220:223], v[0:3], v[244:247], v[220:223]\n\t" \
    "v_mfma_f32_16x16x32_bf16 v[220:223], v[4:7], v[248:251], v[220:223]\n\t" \
    "s_waitcnt lgkmcnt(1)\n\t" \
    "v_mfma_f32_16x16x32_bf16 v[224:227], v[228:231], v[244:247], v[224:227]\n\t" \
    "s_waitcnt lgkmcnt(0)\n\t" \
    "v_mfma_f32_16x16x32_bf16 v[224:227], v[232:235], v[248:251], v[224:227]\n\t" \
    GSA(2) \
    "s_setprio 2\n\t" \
    "ds_read_b32 v252, v16 offset:16\n\t" \
    "ds_read_b128 v[0:3], v15 offset:0\n\t" \
    "ds_read_b128 v[4:7], v15 offset:512\n\t" \
    "ds_read_b128 v[228:231], v15 offset:1024\n\t" \
    "ds_read_b128 v[232:235], v15 offset:1536\n\t" \
    VLD(2) \
    "v_exp_f32 v220, v220\n\t" \
    "v_exp_f32 v221, v221\n\t" \
    "v_exp_f32 v222, v222\n\t" \
    "v_exp_f32 v223, v223\n\t" \
    "v_exp_f32 v224, v224\n\t" \
    "v_exp_f32 v225, v225\n\t" \
    "v_exp_f32 v226, v226\n\t" \
    "v_exp_f32 v227, v227\n\t" \
    "v_cvt_pk_bf16_f32 v220, v220, v221\n\t" \
    "v_cvt_pk_bf16_f32 v221, v222, v223\n\t" \
    "s_waitcnt lgkmcnt(4)\n\t" \
    "v_cmp_ne_u32 vcc, %[tag], v252\n\t" \
    "s_cbranch_vccnz RK_%=\n\t" \
    "CK_%=:\n\t" \
    "s_waitcnt lgkmcnt(3)\n\t" \
    "v_mfma_f32_16x16x32_bf16 v[0:3], v[0:3], v[236:239], 0\n\t" \
    "v_cvt_pk_bf16_f32 v222, v224, v225\n\t" \
    "v_cvt_pk_bf16_f32 v223, v226, v227\n\t" \
    "s_waitcnt lgkmcnt(2)\n\t" \
    "v_mfma_f32_16x16x32_bf16 v[0:3], v[4:7], v[240:243], v[0:3]\n\t" \
    "v_mov_b32 v224, %[one]\n\t" \
    "v_mov_b32 v225, %[one]\n\t" \
    "s_waitcnt lgkmcnt(1)\n\t" \
    "v_mfma_f32_16x16x32_bf16 v[0:3], v[228:231], v[244:247], v[0:3]\n\t" \
    "v_mov_b32 v226, %[one]\n\t" \
    "v_mov_b32 v227, %[one]\n\t" \
    "s_waitcnt lgkmcnt(0)\n\t" \
    "v_mfma_f32_16x16x32_bf16 v[0:3], v[232:235], v[248:251], v[0:3]\n\t" \
    "v_mfma_f32_16x16x32_bf16 v[244:247], v[224:227], v[220:223], 0\n\t" \
    "v_mov_b32 v229, 0\n\t" \
    "v_mfma_f32_16x16x32_bf16 v[236:239], v[20:23], v[220:223], 0\n\t" \
    "v_mov_b32 v230, 0\n\t" \
    "v_mfma_f32_16x16x32_bf16 v[240:243], v[24:27], v[220:223], 0\n\t" \
    "s_nop 2\n\t" \
    "v_exp_f32 v0, v0\n\t" \
    "v_exp_f32 v1, v1\n\t" \
    "s_nop 0\n\t" \
    "v_cvt_pk_bf16_f32 v228, v0, v1\n\t" \
    "v_cvt_pk_bf16_f32 v231, v0, v1\n\t" \
    GSA(3) \
    "CM_%=:\n\t" \
    "s_nop 0\n\t" \
    "v_mfma_f32_16x16x16_bf16 v[244:247], v[224:225], v[228:229], v[244:247]\n\t" \
    "s_waitcnt vmcnt(0)\n\t" \
    "v_mfma_f32_16x16x16_bf16 v[236:239], v[254:255], v[228:229], v[236:239]\n\t" \
    "v_max_u32 v7, v254, v255\n\t" \
    "v_mfma_f32_16x16x16_bf16 v[240:243], v[254:255], v[230:231], v[240:243]\n\t" \
    "v_cmp_eq_u32 s[88:89], -1, v7\n\t" \
    "s_not_b64 exec, %[hi]\n\t" \
    "s_nop 1\n\t" \
    "v_rcp_f32 v2, v244\n\t" \
    "v_subrev_u32 v253, 0x21800000, v244\n\t" \
    "v_cmp_gt_u32 vcc, v253, %[rg]\n\t" \
    "v_pk_mul_f32 v[236:237], v[236:237], v[2:3] op_sel_hi:[1,0]\n\t" \
    "v_pk_mul_f32 v[238:239], v[238:239], v[2:3] op_sel_hi:[1,0]\n\t" \
    "v_pk_mul_f32 v[240:241], v[240:241], v[2:3] op_sel_hi:[1,0]\n\t" \
    "v_pk_mul_f32 v[242:243], v[242:243], v[2:3] op_sel_hi:[1,0]\n\t" \
    "s_or_b64 vcc, vcc, s[88:89]\n\t" \
    "v_cvt_pk_bf16_f32 v4, v236, v237\n\t" \
    "v_cvt_pk_bf16_f32 v5, v238, v239\n\t" \
    "v_cvt_pk_bf16_f32 v6, v240, v241\n\t" \
    "v_cvt_pk_bf16_f32 v7, v242, v243\n\t" \
    "s_cbranch_scc1 FX_%=\n\t" \
    "buffer_store_dwordx4 v[4:7], v18, %[rs], %[sb] offen sc1\n\t" \
    "s_memrealtime %[tp]\n\t" \
    DGA \
    GSA(4) \
    "v_max3_u32 v252, v4, v5, v6\n\t" \
    "v_max_u32 v252, v252, v7\n\t" \
    "v_cmp_eq_u32 vcc, -1, v252\n\t" \
    "s_cbranch_vccnz RC_%=\n\t" \
    "CC_%=:\n\t" \
    "s_mov_b64 exec, -1\n\t" \
    "v_add_u32 v252, %[vd], v12\n\t" \
    "v_add_u32 v253, %[md], v17\n\t" \
    "ds_write_b128 v252, v[20:23]\n\t" \
    "ds_write_b128 v252, v[24:27] offset:1024\n\t" \
    "ds_write2st64_b32 v253, v254, v255 offset0:0 offset1:1\n\t" \
    "s_waitcnt lgkmcnt(0)\n\t" \
    "s_cmp_lg_u32 %[sc], 3\n\t" \
    "s_cbranch_scc1 NH_%=\n\t" \
    "buffer_load_dwordx2 v[0:1], off, %[rs], %[fl] sc1\n\t" \
    "s_barrier\n\t" \
    "s_waitcnt vmcnt(0)\n\t" \
    "v_readfirstlane_b32 %[pt], v0\n\t" \
    "v_readfirstlane_b32 %[pd], v1\n\t" \
    "s_nop 3\n\t" \
    "s_branch E_%=\n\t" \
    "NH_%=:\n\t" \
    "s_barrier\n\t" \
    "s_branch E_%=\n\t" \
    "RP_%=:\n\t" \
    "s_waitcnt lgkmcnt(0)\n\t" \
    "ds_read_b32 v252, v16\n\t" \
    "ds_read_b128 v[236:239], v14 offset:0\n\t" \
    "ds_read_b128 v[240:243], v14 offset:512\n\t" \
    "ds_read_b128 v[244:247], v14 offset:1024\n\t" \
    "ds_read_b128 v[248:251], v14 offset:1536\n\t" \
    "s_sub_u32 %[lim], %[lim], " STR(DEC1) "\n\t" \
    "s_cmp_gt_i32 %[lim], 0\n\t" \
    "s_waitcnt lgkmcnt(4)\n\t" \
    "s_cbranch_scc0 CP_%=\n\t" \
    "v_cmp_ne_u32 vcc, %[tag], v252\n\t" \
    "s_cbranch_vccnz RP_%=\n\t" \
    "s_branch CP_%=\n\t" \
    "RK_%=:\n\t" \
    "s_waitcnt lgkmcnt(0)\n\t" \
    "ds_read_b32 v252, v16 offset:16\n\t" \
    "ds_read_b128 v[0:3], v15 offset:0\n\t" \
    "ds_read_b128 v[4:7], v15 offset:512\n\t" \
    "ds_read_b128 v[228:231], v15 offset:1024\n\t" \
    "ds_read_b128 v[232:235], v15 offset:1536\n\t" \
    "s_sub_u32 %[lim], %[lim], " STR(DEC1) "\n\t" \
    "s_cmp_gt_i32 %[lim], 0\n\t" \
    "s_waitcnt lgkmcnt(4)\n\t" \
    "s_cbranch_scc0 CK_%=\n\t" \
    "v_cmp_ne_u32 vcc, %[tag], v252\n\t" \
    "s_cbranch_vccnz RK_%=\n\t" \
    "s_branch CK_%=\n\t" \
    "RC_%=:\n\t" \
    "v_min_u32 v4, -2, v4\n\t" \
    "v_min_u32 v5, -2, v5\n\t" \
    "v_min_u32 v6, -2, v6\n\t" \
    "v_min_u32 v7, -2, v7\n\t" \
    "buffer_store_dwordx4 v[4:7], v18, %[rs], %[sb] offen sc1\n\t" \
    "s_branch CC_%=\n\t" \
    "FX_%=:\n\t" \
    "s_mov_b64 exec, -1\n\t" \
    "s_cmp_eq_u64 s[88:89], 0\n\t" \
    "s_cbranch_scc1 RM_%=\n\t" \
    "buffer_load_dword v254, v17, %[rs], %[sb] offen sc1\n\t" \
    "buffer_load_dword v255, v17, %[rs], %[sb] offen offset:256 sc1\n\t" \
    "v_mfma_f32_16x16x32_bf16 v[244:247], v[224:227], v[220:223], 0\n\t" \
    "v_mfma_f32_16x16x32_bf16 v[236:239], v[20:23], v[220:223], 0\n\t" \
    "v_mfma_f32_16x16x32_bf16 v[240:243], v[24:27], v[220:223], 0\n\t" \
    "s_sub_u32 %[lim], %[lim], " STR(DEC2) "\n\t" \
    "s_cmp_gt_i32 %[lim], 0\n\t" \
    "s_cbranch_scc1 CM_%=\n\t" \
    "s_waitcnt vmcnt(0)\n\t" \
    "v_mov_b32 v254, 0\n\t" \
    "v_mov_b32 v255, 0\n\t" \
    "s_branch CM_%=\n\t" \
    "RM_%=:\n\t" \
    "s_mov_b32 %[rg], -1\n\t" \
    "s_nop 7\n\t" \
    "ds_read_b128 v[236:239], v14 offset:0\n\t" \
    "ds_read_b128 v[240:243], v14 offset:512\n\t" \
    "ds_read_b128 v[244:247], v14 offset:1024\n\t" \
    "ds_read_b128 v[248:251], v14 offset:1536\n\t" \
    "ds_read_b128 v[232:235], v12 offset:24576\n\t" \
    "s_waitcnt lgkmcnt(0)\n\t" \
    "v_mfma_f32_16x16x32_bf16 v[220:223], v[232:235], v[236:239], 0\n\t" \
    "ds_read_b128 v[232:235], v12 offset:25600\n\t" \
    "s_waitcnt lgkmcnt(0)\n\t" \
    "v_mfma_f32_16x16x32_bf16 v[220:223], v[232:235], v[240:243], v[220:223]\n\t" \
    "ds_read_b128 v[232:235], v12 offset:26624\n\t" \
    "s_waitcnt lgkmcnt(0)\n\t" \
    "v_mfma_f32_16x16x32_bf16 v[220:223], v[232:235], v[244:247], v[220:223]\n\t" \
    "ds_read_b128 v[232:235], v12 offset:27648\n\t" \
    "s_waitcnt lgkmcnt(0)\n\t" \
    "v_mfma_f32_16x16x32_bf16 v[220:223], v[232:235], v[248:251], v[220:223]\n\t" \
    "ds_read_b128 v[232:235], v12 offset:28672\n\t" \
    "s_waitcnt lgkmcnt(0)\n\t" \
    "v_mfma_f32_16x16x32_bf16 v[224:227], v[232:235], v[236:239], 0\n\t" \
    "ds_read_b128 v[232:235], v12 offset:29696\n\t" \
    "s_waitcnt lgkmcnt(0)\n\t" \
    "v_mfma_f32_16x16x32_bf16 v[224:227], v[232:235], v[240:243], v[224:227]\n\t" \
    "ds_read_b128 v[232:235], v12 offset:30720\n\t" \
    "s_waitcnt lgkmcnt(0)\n\t" \
    "v_mfma_f32_16x16x32_bf16 v[224:227], v[232:235], v[244:247], v[224:227]\n\t" \
    "ds_read_b128 v[232:235], v12 offset:31744\n\t" \
    "s_waitcnt lgkmcnt(0)\n\t" \
    "v_mfma_f32_16x16x32_bf16 v[224:227], v[232:235], v[248:251], v[224:227]\n\t" \
    "ds_read_b128 v[232:235], v15 offset:0\n\t" \
    "s_waitcnt lgkmcnt(0)\n\t" \
    "v_mfma_f32_16x16x32_bf16 v[0:3], v[232:235], v[236:239], 0\n\t" \
    "ds_read_b128 v[232:235], v15 offset:512\n\t" \
    "s_waitcnt lgkmcnt(0)\n\t" \
    "v_mfma_f32_16x16x32_bf16 v[0:3], v[232:235], v[240:243], v[0:3]\n\t" \
    "ds_read_b128 v[232:235], v15 offset:1024\n\t" \
    "s_waitcnt lgkmcnt(0)\n\t" \
    "v_mfma_f32_16x16x32_bf16 v[0:3], v[232:235], v[244:247], v[0:3]\n\t" \
    "ds_read_b128 v[232:235], v15 offset:1536\n\t" \
    "s_waitcnt lgkmcnt(0)\n\t" \
    "v_mfma_f32_16x16x32_bf16 v[0:3], v[232:235], v[248:251], v[0:3]\n\t" \
    "v_max3_f32 v252, v220, v221, v222\n\t" \
    "v_max3_f32 v253, v223, v224, v225\n\t" \
    "v_max3_f32 v252, v252, v226, v227\n\t" \
    "s_nop 4\n\t" \
    "v_max3_f32 v253, v253, v0, v1\n\t" \
    "v_max_f32 v252, v252, v253\n\t" \
    "v_mov_b32 v253, v252\n\t" \
    "v_mov_b32 v2, v252\n\t" \
    "s_nop 1\n\t" \
    "v_permlane16_swap_b32 v253, v2\n\t" \
    "v_max_f32 v252, v253, v2\n\t" \
    "v_max_f32 v253, v253, v2\n\t" \
    "s_nop 1\n\t" \
    "v_permlane32_swap_b32 v252, v253\n\t" \
    "v_max_f32 v252, v252, v253\n\t" \
    "v_sub_f32 v220, v220, v252\n\t" \
    "v_sub_f32 v221, v221, v252\n\t" \
    "v_sub_f32 v222, v222, v252\n\t" \
    "v_sub_f32 v223, v223, v252\n\t" \
    "v_sub_f32 v224, v224, v252\n\t" \
    "v_sub_f32 v225, v225, v252\n\t" \
    "v_sub_f32 v226, v226, v252\n\t" \
    "v_sub_f32 v227, v227, v252\n\t" \
    "v_sub_f32 v0, v0, v252\n\t" \
    "v_sub_f32 v1, v1, v252\n\t" \
    "v_exp_f32 v220, v220\n\t" \
    "v_exp_f32 v221, v221\n\t" \
    "v_exp_f32 v222, v222\n\t" \
    "v_exp_f32 v223, v223\n\t" \
    "v_exp_f32 v224, v224\n\t" \
    "v_exp_f32 v225, v225\n\t" \
    "v_exp_f32 v226, v226\n\t" \
    "v_exp_f32 v227, v227\n\t" \
    "v_cvt_pk_bf16_f32 v220, v220, v221\n\t" \
    "v_cvt_pk_bf16_f32 v221, v222, v223\n\t" \
    "v_cvt_pk_bf16_f32 v222, v224, v225\n\t" \
    "v_cvt_pk_bf16_f32 v223, v226, v227\n\t" \
    "v_mov_b32 v224, %[one]\n\t" \
    "v_mov_b32 v225, %[one]\n\t" \
    "v_mov_b32 v226, %[one]\n\t" \
    "v_mov_b32 v227, %[one]\n\t" \
    "s_nop 1\n\t" \
    "v_mfma_f32_16x16x32_bf16 v[244:247], v[224:227], v[220:223], 0\n\t" \
    "v_mfma_f32_16x16x32_bf16 v[236:239], v[20:23], v[220:223], 0\n\t" \
    "v_mfma_f32_16x16x32_bf16 v[240:243], v[24:27], v[220:223], 0\n\t" \
    "v_exp_f32 v0, v0\n\t" \
    "v_exp_f32 v1, v1\n\t" \
    "s_nop 0\n\t" \
    "v_cvt_pk_bf16_f32 v228, v0, v1\n\t" \
    "v_cvt_pk_bf16_f32 v231, v0, v1\n\t" \
    "s_nop 1\n\t" \
    "s_branch CM_%=\n\t" \
    "E_%=:\n\t" \
    GSW \
    "s_nop 1\n\t" \
    : [lim] "+s"(lim), [rg] "+s"(rg), [tp] "=&s"(tp), [pt] "=&s"(pt), [pd] "=&s"(pd) DGO DGP GSO(0) GSO(1) GSO(2) GSO(3) GSO(4) \
    : "{v8}"(am_a), "{v9}"(t0_a), "{v10}"(idx), "{v11}"(iw_a), "{v12}"(ab_a), "{v14}"(qr_a), "{v15}"(kr_a), "{v16}"(fr_a), "{v17}"(vo_v), "{v18}"(oo_v), [rs] "s"(rs), [sb] "s"(sb), [tag] "s"(0xffffff81u + s), [hi] "s"(0xff00ff00ff00ff00ull), [tgt] "s"(tgt), [sc] "s"(wid), [onej] "s"(onej), [sl] "s"(0x3e0293ee), [one] "s"(0x3f803f80), [m0] "s"(sbm), [m1] "s"(sbm + 4096u), [vx] "s"(vxo), [fl] "s"(fb + FL_T(s + 1)), [vd] "s"(8192 + j * 2048), [md] "s"(md), "{v[28:35]}"(wr[0][0]), "{v[36:43]}"(wr[0][1]), "{v[44:51]}"(wr[1][0]), "{v[52:59]}"(wr[1][1]), "{v[60:67]}"(wr[2][0]), "{v[68:75]}"(wr[2][1]), "{v[76:83]}"(wr[3][0]), "{v[84:91]}"(wr[3][1]), "{v[92:99]}"(wr[4][0]), "{v[100:107]}"(wr[4][1]), "{v[108:115]}"(wr[5][0]), "{v[116:123]}"(wr[5][1]), "{v[124:131]}"(wr[6][0]), "{v[132:139]}"(wr[6][1]), "{v[140:147]}"(wr[7][0]), "{v[148:155]}"(wr[7][1]), "{v[156:163]}"(wr[8][0]), "{v[164:171]}"(wr[8][1]), "{v[172:179]}"(wr[9][0]), "{v[180:187]}"(wr[9][1]), "{v[188:195]}"(wr[10][0]), "{v[196:203]}"(wr[10][1]), "{v[204:211]}"(wr[11][0]), "{v[212:219]}"(wr[11][1]) \
    : "memory", "vcc", "scc", "s88", "s89", "s90", "s91", "s92", "s93", "v0", "v1", "v2", "v3", "v4", "v5", "v6", "v7", "v20", "v21", "v22", "v23", "v24", "v25", "v26", "v27", "v220", "v221", "v222", "v223", "v224", "v225", "v226", "v227", "v228", "v229", "v230", "v231", "v232", "v233", "v234", "v235", "v236", "v237", "v238", "v239", "v240", "v241", "v242", "v243", "v244", "v245", "v246", "v247", "v248", "v249", "v250", "v251", "v252", "v253", "v254", "v255")

#define A_MAIN_K() \
  asm volatile( \
    "ds_read_b128 v[236:239], v9 offset:0\n\t" \
    "ds_read_b128 v[240:243], v9 offset:1024\n\t" \
    "ds_read_b128 v[244:247], v9 offset:2048\n\t" \
    "ds_read_b128 v[248:251], v9 offset:3072\n\t" \
    "v_mov_b32 v0, 0\n\t" \
    "v_mov_b32 v1, 0\n\t" \
    "v_mov_b32 v2, 0\n\t" \
    "v_mov_b32 v3, 0\n\t" \
    "v_mov_b32 v4, 0\n\t" \
    "v_mov_b32 v5, 0\n\t" \
    "v_mov_b32 v6, 0\n\t" \
    "v_mov_b32 v7, 0\n\t" \
    "s_waitcnt lgkmcnt(0)\n\t" \
    "s_barrier\n\t" \
    "buffer_load_dwordx4 v[220:223], v8, %[rs], %[m0] offen sc1\n\t" \
    "buffer_load_dwordx4 v[224:227], v8, %[rs], %[m1] offen sc1\n\t" \
    GSA(3) \
    "v_mov_b32 v252, v252\n\t" \
    "s_nop 2\n\t" \
    "v_mov_b32 v252, v252\n\t" \
    "s_nop 2\n\t" \
    "v_mov_b32 v252, v252\n\t" \
    "s_nop 2\n\t" \
    "v_mov_b32 v252, v252\n\t" \
    "s_nop 2\n\t" \
    "v_mov_b32 v252, v252\n\t" \
    "s_nop 2\n\t" \
    "v_mov_b32 v252, v252\n\t" \
    "s_nop 2\n\t" \
    "v_mov_b32 v252, v252\n\t" \
    "s_nop 2\n\t" \
    "v_mov_b32 v252, v252\n\t" \
    "s_nop 2\n\t" \
    "v_mov_b32 v252, v252\n\t" \
    "s_nop 2\n\t" \
    "v_mov_b32 v252, v252\n\t" \
    "s_nop 2\n\t" \
    "v_mov_b32 v252, v252\n\t" \
    "s_nop 2\n\t" \
    "v_mov_b32 v252, v252\n\t" \
    "s_nop 2\n\t" \
    "v_mov_b32 v252, v252\n\t" \
    "s_nop 2\n\t" \
    "v_mov_b32 v252, v252\n\t" \
    "s_nop 2\n\t" \
    "v_mov_b32 v252, v252\n\t" \
    "s_nop 2\n\t" \
    "v_mov_b32 v252, v252\n\t" \
    "s_nop 2\n\t" \
    "v_mov_b32 v252, v252\n\t" \
    "s_nop 2\n\t" \
    "v_mov_b32 v252, v252\n\t" \
    "s_nop 2\n\t" \
    "v_mov_b32 v252, v252\n\t" \
    "s_nop 2\n\t" \
    "v_mov_b32 v252, v252\n\t" \
    "s_nop 2\n\t" \
    "v_mov_b32 v252, v252\n\t" \
    "s_nop 2\n\t" \
    "v_mov_b32 v252, v252\n\t" \
    "s_nop 2\n\t" \
    "v_mov_b32 v252, v252\n\t" \
    "s_nop 2\n\t" \
    "v_mov_b32 v252, v252\n\t" \
    "s_nop 2\n\t" \
    "v_mov_b32 v252, v252\n\t" \
    "s_nop 2\n\t" \
    "v_mov_b32 v252, v252\n\t" \
    "s_nop 2\n\t" \
    "v_mov_b32 v252, v252\n\t" \
    "s_nop 2\n\t" \
    "v_mov_b32 v252, v252\n\t" \
    "s_nop 2\n\t" \
    "v_mov_b32 v252, v252\n\t" \
    "s_nop 2\n\t" \
    "v_mov_b32 v252, v252\n\t" \
    "s_nop 2\n\t" \
    "PM_%=:\n\t" \
    "s_waitcnt vmcnt(1)\n\t" \
    "ds_write_b128 v8, v[220:223] offset:8192\n\t" \
    "v_max3_u32 v252, v220, v221, v222\n\t" \
    "s_waitcnt vmcnt(0)\n\t" \
    "ds_write_b128 v8, v[224:227] offset:12288\n\t" \
    "v_max3_u32 v252, v252, v223, v224\n\t" \
    "v_max3_u32 v252, v252, v225, v226\n\t" \
    "v_max_u32 v252, v252, v227\n\t" \
    "v_cmp_eq_u32 vcc, -1, v252\n\t" \
    "s_cbranch_vccz PD_%=\n\t" \
    "s_sub_u32 %[lim], %[lim], 1\n\t" \
    "s_cmp_gt_i32 %[lim], 0\n\t" \
    "s_cbranch_scc0 PD_%=\n\t" \
    "s_sleep " STR(CADM) "\n\t" \
    "buffer_load_dwordx4 v[220:223], v8, %[rs], %[m0] offen sc1\n\t" \
    "buffer_load_dwordx4 v[224:227], v8, %[rs], %[m1] offen sc1\n\t" \
    "s_branch PM_%=\n\t" \
    "PD_%=:\n\t" \
    GSA(0) \
    "v_smfmac_f32_16x16x64_bf16 v[0:3], v[220:223], v[236:243], v10\n\t" \
    "v_xor_b32 v9, 16384, v9\n\t" \
    "ds_read_b128 v[236:239], v9 offset:0\n\t" \
    "ds_read_b128 v[240:243], v9 offset:1024\n\t" \
    "v_smfmac_f32_16x16x64_bf16 v[4:7], v[220:223], v[244:251], v10\n\t" \
    "ds_read_b128 v[244:247], v9 offset:2048\n\t" \
    "ds_read_b128 v[248:251], v9 offset:3072\n\t" \
    "s_waitcnt lgkmcnt(4)\n\t" \
    "s_barrier\n\t" \
    DGA \
    "s_cmp_lg_u32 %[st], 63\n\t" \
    "s_cbranch_scc1 NL_%=\n\t" \
    "s_mov_b64 exec, %[fm]\n\t" \
    "v_mov_b32 v252, %[st]\n\t" \
    "v_mov_b32 v253, %[tag]\n\t" \
    "v_mov_b32 v254, 0\n\t" \
    "buffer_store_dwordx2 v[252:253], v254, %[rs], %[po] offen sc1\n\t" \
    "s_mov_b64 exec, -1\n\t" \
    "NL_%=:\n\t" \
    "ds_read_b128 v[228:231], v8 offset:0\n\t" \
    "ds_read_b128 v[232:235], v8 offset:4096\n\t" \
    "v_smfmac_f32_16x16x64_bf16 v[0:3], v[224:227], v[148:155], v10\n\t" \
    "v_xor_b32 v8, 1024, v8\n\t" \
    "ds_read_b128 v[220:223], v8 offset:8192\n\t" \
    "v_smfmac_f32_16x16x64_bf16 v[4:7], v[224:227], v[156:163], v10\n\t" \
    "ds_read_b128 v[224:227], v8 offset:12288\n\t" \
    "buffer_store_dwordx4 v[212:215], v14, %[rs], %[ox] offen offset:0 sc1\n\t" \
    "buffer_store_dwordx4 v[216:219], v14, %[rs], %[ox] offen offset:512 sc1\n\t" \
    "s_waitcnt lgkmcnt(3)\n\t" \
    "v_smfmac_f32_16x16x64_bf16 v[0:3], v[228:231], v[20:27], v10\n\t" \
    "v_smfmac_f32_16x16x64_bf16 v[4:7], v[228:231], v[28:35], v10\n\t" \
    "ds_read_b128 v[228:231], v8 offset:0\n\t" \
    "s_waitcnt lgkmcnt(3)\n\t" \
    "v_smfmac_f32_16x16x64_bf16 v[0:3], v[232:235], v[84:91], v10\n\t" \
    "v_smfmac_f32_16x16x64_bf16 v[4:7], v[232:235], v[92:99], v10\n\t" \
    "ds_read_b128 v[232:235], v8 offset:4096\n\t" \
    "s_waitcnt lgkmcnt(3)\n\t" \
    "v_smfmac_f32_16x16x64_bf16 v[0:3], v[220:223], v[236:243], v10\n\t" \
    "v_xor_b32 v9, 49152, v9\n\t" \
    "ds_read_b128 v[236:239], v9 offset:0\n\t" \
    "v_smfmac_f32_16x16x64_bf16 v[4:7], v[220:223], v[244:251], v10\n\t" \
    "ds_read_b128 v[240:243], v9 offset:1024\n\t" \
    "s_waitcnt lgkmcnt(4)\n\t" \
    "v_smfmac_f32_16x16x64_bf16 v[0:3], v[224:227], v[164:171], v10\n\t" \
    "v_xor_b32 v8, 3072, v8\n\t" \
    "ds_read_b128 v[220:223], v8 offset:12288\n\t" \
    "v_smfmac_f32_16x16x64_bf16 v[4:7], v[224:227], v[172:179], v10\n\t" \
    "ds_read_b128 v[244:247], v9 offset:2048\n\t" \
    "s_waitcnt lgkmcnt(5)\n\t" \
    "v_smfmac_f32_16x16x64_bf16 v[0:3], v[228:231], v[36:43], v10\n\t" \
    "ds_read_b128 v[224:227], v8 offset:0\n\t" \
    "v_smfmac_f32_16x16x64_bf16 v[4:7], v[228:231], v[44:51], v10\n\t" \
    "ds_read_b128 v[248:251], v9 offset:3072\n\t" \
    "s_waitcnt lgkmcnt(6)\n\t" \
    "v_smfmac_f32_16x16x64_bf16 v[0:3], v[232:235], v[100:107], v10\n\t" \
    "ds_read_b128 v[228:231], v8 offset:8192\n\t" \
    "v_smfmac_f32_16x16x64_bf16 v[4:7], v[232:235], v[108:115], v10\n\t" \
    "ds_read_b128 v[232:235], v8 offset:4096\n\t" \
    "s_waitcnt lgkmcnt(5)\n\t" \
    "v_smfmac_f32_16x16x64_bf16 v[0:3], v[220:223], v[180:187], v10\n\t" \
    "v_smfmac_f32_16x16x64_bf16 v[4:7], v[220:223], v[188:195], v10\n\t" \
    "v_xor_b32 v8, 1024, v8\n\t" \
    "ds_read_b128 v[220:223], v8 offset:12288\n\t" \
    "s_waitcnt lgkmcnt(4)\n\t" \
    "v_smfmac_f32_16x16x64_bf16 v[0:3], v[224:227], v[52:59], v10\n\t" \
    "v_smfmac_f32_16x16x64_bf16 v[4:7], v[224:227], v[60:67], v10\n\t" \
    "ds_read_b128 v[224:227], v8 offset:0\n\t" \
    "s_waitcnt lgkmcnt(3)\n\t" \
    "v_smfmac_f32_16x16x64_bf16 v[0:3], v[228:231], v[236:243], v10\n\t" \
    "v_xor_b32 v9, 16384, v9\n\t" \
    "ds_read_b128 v[236:239], v9 offset:0\n\t" \
    "v_smfmac_f32_16x16x64_bf16 v[4:7], v[228:231], v[244:251], v10\n\t" \
    "ds_read_b128 v[240:243], v9 offset:1024\n\t" \
    "s_waitcnt lgkmcnt(4)\n\t" \
    "v_smfmac_f32_16x16x64_bf16 v[0:3], v[232:235], v[116:123], v10\n\t" \
    "ds_read_b128 v[228:231], v8 offset:4096\n\t" \
    "v_smfmac_f32_16x16x64_bf16 v[4:7], v[232:235], v[124:131], v10\n\t" \
    "ds_read_b128 v[244:247], v9 offset:2048\n\t" \
    "s_waitcnt lgkmcnt(5)\n\t" \
    "v_smfmac_f32_16x16x64_bf16 v[0:3], v[220:223], v[196:203], v10\n\t" \
    "ds_read_b128 v[232:235], v8 offset:8192\n\t" \
    "v_xor_b32 v8, 3072, v8\n\t" \
    "v_smfmac_f32_16x16x64_bf16 v[4:7], v[220:223], v[204:211], v10\n\t" \
    "ds_read_b128 v[248:251], v9 offset:3072\n\t" \
    "s_waitcnt lgkmcnt(6)\n\t" \
    "v_smfmac_f32_16x16x64_bf16 v[0:3], v[224:227], v[68:75], v10\n\t" \
    "v_smfmac_f32_16x16x64_bf16 v[4:7], v[224:227], v[76:83], v10\n\t" \
    "s_waitcnt lgkmcnt(3)\n\t" \
    "v_smfmac_f32_16x16x64_bf16 v[0:3], v[228:231], v[132:139], v10\n\t" \
    "v_smfmac_f32_16x16x64_bf16 v[4:7], v[228:231], v[140:147], v10\n\t" \
    "s_waitcnt lgkmcnt(0)\n\t" \
    "v_smfmac_f32_16x16x64_bf16 v[0:3], v[232:235], v[236:243], v10\n\t" \
    "v_xor_b32 v9, 49152, v9\n\t" \
    "v_smfmac_f32_16x16x64_bf16 v[4:7], v[232:235], v[244:251], v10\n\t" \
    "v_mov_b32 v253, %[stag]\n\t" \
    "s_nop 4\n\t" \
    "v_add_f32 v0, v0, v1\n\t" \
    "v_add_f32 v2, v2, v3\n\t" \
    "v_add_f32 v4, v4, v5\n\t" \
    "v_add_f32 v6, v6, v7\n\t" \
    "v_cvt_pk_bf16_f32 v0, v0, v4\n\t" \
    "v_cvt_pk_bf16_f32 v2, v2, v6\n\t" \
    "ds_write2_b32 v11, v0, v2 offset0:0 offset1:4\n\t" \
    "s_mov_b64 exec, %[onej]\n\t" \
    "ds_write_b32 v18, v253 offset:16\n\t" \
    "s_mov_b64 exec, -1\n\t" \
    GSA(1) \
    "s_setprio 0\n\t" \
    "buffer_load_dwordx4 v[236:239], v12, %[rs], %[qx] offen offset:0 sc1\n\t" \
    "buffer_load_dwordx4 v[240:243], v12, %[rs], %[qx] offen offset:1024 sc1\n\t" \
    "buffer_load_dwordx4 v[244:247], v12, %[rs], %[qx] offen offset:2048 sc1\n\t" \
    "buffer_load_dwordx4 v[248:251], v12, %[rs], %[qx] offen offset:3072 sc1\n\t" \
    "v_cmp_le_i32 s[72:73], 0, v16\n\t" \
    "v_cmp_le_i32 s[74:75], 1, v16\n\t" \
    "v_cmp_le_i32 s[76:77], 2, v16\n\t" \
    "v_cmp_le_i32 s[78:79], 3, v16\n\t" \
    "v_cmp_le_i32 s[80:81], 16, v16\n\t" \
    "v_cmp_le_i32 s[82:83], 17, v16\n\t" \
    "v_cmp_le_i32 s[84:85], 18, v16\n\t" \
    "v_cmp_le_i32 s[86:87], 19, v16\n\t" \
    "v_mov_b32 v254, v254\n\t" \
    "s_nop 2\n\t" \
    "v_mov_b32 v254, v254\n\t" \
    "s_nop 2\n\t" \
    "v_mov_b32 v254, v254\n\t" \
    "s_nop 2\n\t" \
    "v_mov_b32 v254, v254\n\t" \
    "s_nop 2\n\t" \
    "v_mov_b32 v254, v254\n\t" \
    "s_nop 2\n\t" \
    "v_mov_b32 v254, v254\n\t" \
    "s_nop 2\n\t" \
    "v_mov_b32 v254, v254\n\t" \
    "s_nop 2\n\t" \
    "v_mov_b32 v254, v254\n\t" \
    "s_nop 2\n\t" \
    "v_mov_b32 v254, v254\n\t" \
    "s_nop 2\n\t" \
    "v_mov_b32 v254, v254\n\t" \
    "s_nop 2\n\t" \
    "v_mov_b32 v254, v254\n\t" \
    "s_nop 2\n\t" \
    "v_mov_b32 v254, v254\n\t" \
    "s_nop 2\n\t" \
    "v_mov_b32 v254, v254\n\t" \
    "s_nop 2\n\t" \
    "v_mov_b32 v254, v254\n\t" \
    "s_nop 2\n\t" \
    "v_mov_b32 v254, v254\n\t" \
    "s_nop 2\n\t" \
    "v_mov_b32 v254, v254\n\t" \
    "s_nop 2\n\t" \
    "v_mov_b32 v254, v254\n\t" \
    "s_nop 2\n\t" \
    "v_mov_b32 v254, v254\n\t" \
    "s_nop 2\n\t" \
    "v_mov_b32 v254, v254\n\t" \
    "s_nop 2\n\t" \
    "v_mov_b32 v254, v254\n\t" \
    "s_nop 2\n\t" \
    "LW_%=:\n\t" \
    "ds_read_b32 v0, v18 offset:16\n\t" \
    "s_sub_u32 %[lim], %[lim], " STR(DEC1) "\n\t" \
    "s_cmp_gt_i32 %[lim], 0\n\t" \
    "v_mov_b32 v254, v254\n\t" \
    "s_nop 2\n\t" \
    "v_mov_b32 v254, v254\n\t" \
    "s_nop 2\n\t" \
    "v_mov_b32 v254, v254\n\t" \
    "s_nop 2\n\t" \
    "v_mov_b32 v254, v254\n\t" \
    "s_nop 2\n\t" \
    "v_mov_b32 v254, v254\n\t" \
    "s_nop 2\n\t" \
    "s_waitcnt lgkmcnt(0)\n\t" \
    "s_cbranch_scc0 CW_%=\n\t" \
    "v_cmp_ne_u32 vcc, v0, v253\n\t" \
    "s_cbranch_vccnz LW_%=\n\t" \
    "CW_%=:\n\t" \
    "ds_read_b128 v[212:215], v12 offset:24576\n\t" \
    "ds_read_b128 v[216:219], v12 offset:28672\n\t" \
    "ds_read_b128 v[220:223], v15 offset:0\n\t" \
    "ds_read_b128 v[224:227], v12 offset:25600\n\t" \
    "ds_read_b128 v[228:231], v12 offset:29696\n\t" \
    "v_add_u32 v253, %[se], v12\n\t" \
    "v_mov_b32 v254, v254\n\t" \
    "s_nop 2\n\t" \
    "v_mov_b32 v254, v254\n\t" \
    "s_nop 2\n\t" \
    "v_mov_b32 v254, v254\n\t" \
    "s_nop 2\n\t" \
    "v_mov_b32 v254, v254\n\t" \
    "s_nop 2\n\t" \
    "s_waitcnt vmcnt(0)\n\t" \
    "s_waitcnt lgkmcnt(4)\n\t" \
    "v_mfma_f32_16x16x32_bf16 v[0:3], v[212:215], v[236:239], 0\n\t" \
    "ds_read_b128 v[212:215], v15 offset:512\n\t" \
    "s_waitcnt lgkmcnt(4)\n\t" \
    "v_mfma_f32_16x16x32_bf16 v[4:7], v[216:219], v[236:239], 0\n\t" \
    "ds_read_b128 v[216:219], v12 offset:26624\n\t" \
    "s_waitcnt lgkmcnt(4)\n\t" \
    "v_mfma_f32_16x16x32_bf16 v[232:235], v[220:223], v[236:239], 0\n\t" \
    "ds_read_b128 v[220:223], v12 offset:30720\n\t" \
    "s_waitcnt lgkmcnt(4)\n\t" \
    "v_mfma_f32_16x16x32_bf16 v[0:3], v[224:227], v[240:243], v[0:3]\n\t" \
    "ds_read_b128 v[224:227], v15 offset:1024\n\t" \
    "s_waitcnt lgkmcnt(4)\n\t" \
    "v_mfma_f32_16x16x32_bf16 v[4:7], v[228:231], v[240:243], v[4:7]\n\t" \
    "ds_read_b128 v[228:231], v12 offset:27648\n\t" \
    "s_waitcnt lgkmcnt(4)\n\t" \
    "v_mfma_f32_16x16x32_bf16 v[232:235], v[212:215], v[240:243], v[232:235]\n\t" \
    "ds_read_b128 v[212:215], v12 offset:31744\n\t" \
    "s_waitcnt lgkmcnt(4)\n\t" \
    "v_mfma_f32_16x16x32_bf16 v[0:3], v[216:219], v[244:247], v[0:3]\n\t" \
    "ds_read_b128 v[216:219], v15 offset:1536\n\t" \
    "s_waitcnt lgkmcnt(4)\n\t" \
    "v_mfma_f32_16x16x32_bf16 v[4:7], v[220:223], v[244:247], v[4:7]\n\t" \
    "s_waitcnt lgkmcnt(3)\n\t" \
    "v_mfma_f32_16x16x32_bf16 v[232:235], v[224:227], v[244:247], v[232:235]\n\t" \
    "s_waitcnt lgkmcnt(2)\n\t" \
    "v_mfma_f32_16x16x32_bf16 v[0:3], v[228:231], v[248:251], v[0:3]\n\t" \
    "s_waitcnt lgkmcnt(1)\n\t" \
    "v_mfma_f32_16x16x32_bf16 v[4:7], v[212:215], v[248:251], v[4:7]\n\t" \
    "s_waitcnt lgkmcnt(0)\n\t" \
    "v_mfma_f32_16x16x32_bf16 v[232:235], v[216:219], v[248:251], v[232:235]\n\t" \
    "s_barrier\n\t" \
    GSA(4) \
    "ds_read_b128 v[236:239], v253 offset:8192\n\t" \
    "ds_read_b128 v[240:243], v253 offset:9216\n\t" \
    "ds_read_b128 v[244:247], v253 offset:10240\n\t" \
    "ds_read_b128 v[248:251], v253 offset:11264\n\t" \
    "ds_read2st64_b32 v[228:229], v17 offset0:0 offset1:1\n\t" \
    "ds_read2st64_b32 v[230:231], v17 offset0:2 offset1:3\n\t" \
    "s_mov_b64 exec, %[fm]\n\t" \
    "v_mov_b32 v220, %[st]\n\t" \
    "v_mov_b32 v221, %[tag]\n\t" \
    "v_mov_b32 v222, 0\n\t" \
    "buffer_store_dwordx2 v[220:221], v222, %[rs], %[po] offen sc1\n\t" \
    "s_mov_b64 exec, -1\n\t" \
    "v_mov_b32 v254, 0xff800000\n\t" \
    "v_cvt_pk_bf16_f32 v0, 0, v0\n\t" \
    "v_cvt_pk_bf16_f32 v1, 0, v1\n\t" \
    "v_cvt_pk_bf16_f32 v2, 0, v2\n\t" \
    "v_cvt_pk_bf16_f32 v3, 0, v3\n\t" \
    "v_cvt_pk_bf16_f32 v4, 0, v4\n\t" \
    "v_cvt_pk_bf16_f32 v5, 0, v5\n\t" \
    "v_cvt_pk_bf16_f32 v6, 0, v6\n\t" \
    "v_cvt_pk_bf16_f32 v7, 0, v7\n\t" \
    "v_cvt_pk_bf16_f32 v232, 0, v232\n\t" \
    "v_cvt_pk_bf16_f32 v233, 0, v233\n\t" \
    "v_mul_f32 v0, 0x3db504f3, v0\n\t" \
    "v_mul_f32 v1, 0x3db504f3, v1\n\t" \
    "v_mul_f32 v2, 0x3db504f3, v2\n\t" \
    "v_mul_f32 v3, 0x3db504f3, v3\n\t" \
    "v_mul_f32 v4, 0x3db504f3, v4\n\t" \
    "v_mul_f32 v5, 0x3db504f3, v5\n\t" \
    "v_mul_f32 v6, 0x3db504f3, v6\n\t" \
    "v_mul_f32 v7, 0x3db504f3, v7\n\t" \
    "v_mul_f32 v232, 0x3db504f3, v232\n\t" \
    "v_mul_f32 v233, 0x3db504f3, v233\n\t" \
    "v_cvt_pk_bf16_f32 v0, 0, v0\n\t" \
    "v_cvt_pk_bf16_f32 v1, 0, v1\n\t" \
    "v_cvt_pk_bf16_f32 v2, 0, v2\n\t" \
    "v_cvt_pk_bf16_f32 v3, 0, v3\n\t" \
    "v_cvt_pk_bf16_f32 v4, 0, v4\n\t" \
    "v_cvt_pk_bf16_f32 v5, 0, v5\n\t" \
    "v_cvt_pk_bf16_f32 v6, 0, v6\n\t" \
    "v_cvt_pk_bf16_f32 v7, 0, v7\n\t" \
    "v_cvt_pk_bf16_f32 v232, 0, v232\n\t" \
    "v_cvt_pk_bf16_f32 v233, 0, v233\n\t" \
    "v_cndmask_b32 v0, v254, v0, s[72:73]\n\t" \
    "v_cndmask_b32 v1, v254, v1, s[74:75]\n\t" \
    "v_cndmask_b32 v2, v254, v2, s[76:77]\n\t" \
    "v_cndmask_b32 v3, v254, v3, s[78:79]\n\t" \
    "v_cndmask_b32 v4, v254, v4, s[80:81]\n\t" \
    "v_cndmask_b32 v5, v254, v5, s[82:83]\n\t" \
    "v_cndmask_b32 v6, v254, v6, s[84:85]\n\t" \
    "v_cndmask_b32 v7, v254, v7, s[86:87]\n\t" \
    "v_max3_f32 v252, v0, v1, v2\n\t" \
    "v_max3_f32 v255, v3, v4, v5\n\t" \
    "v_max3_f32 v254, v6, v7, v232\n\t" \
    "v_max3_f32 v252, v252, v255, v254\n\t" \
    "v_max_f32 v252, v252, v233\n\t" \
    "v_mov_b32 v255, v252\n\t" \
    "v_mov_b32 v254, v252\n\t" \
    "s_nop 1\n\t" \
    "v_permlane16_swap_b32 v255, v254\n\t" \
    "v_max_f32 v252, v255, v254\n\t" \
    "v_max_f32 v255, v255, v254\n\t" \
    "s_nop 1\n\t" \
    "v_permlane32_swap_b32 v252, v255\n\t" \
    "v_max_f32 v252, v252, v255\n\t" \
    "v_mul_f32 v252, 0xbfb8aa3b, v252\n\t" \
    "v_fma_f32 v0, v0, %[l2e], v252\n\t" \
    "v_fma_f32 v1, v1, %[l2e], v252\n\t" \
    "v_fma_f32 v2, v2, %[l2e], v252\n\t" \
    "v_fma_f32 v3, v3, %[l2e], v252\n\t" \
    "v_fma_f32 v4, v4, %[l2e], v252\n\t" \
    "v_fma_f32 v5, v5, %[l2e], v252\n\t" \
    "v_fma_f32 v6, v6, %[l2e], v252\n\t" \
    "v_fma_f32 v7, v7, %[l2e], v252\n\t" \
    "v_fma_f32 v232, v232, %[l2e], v252\n\t" \
    "v_fma_f32 v233, v233, %[l2e], v252\n\t" \
    "v_exp_f32 v0, v0\n\t" \
    "v_exp_f32 v1, v1\n\t" \
    "v_exp_f32 v2, v2\n\t" \
    "v_exp_f32 v3, v3\n\t" \
    "v_exp_f32 v4, v4\n\t" \
    "v_exp_f32 v5, v5\n\t" \
    "v_exp_f32 v6, v6\n\t" \
    "v_exp_f32 v7, v7\n\t" \
    "v_exp_f32 v232, v232\n\t" \
    "v_exp_f32 v233, v233\n\t" \
    "v_add_f32 v252, v0, v1\n\t" \
    "v_add_f32 v235, v2, v3\n\t" \
    "v_add_f32 v254, v4, v5\n\t" \
    "v_add_f32 v255, v6, v7\n\t" \
    "v_add_f32 v234, v232, v233\n\t" \
    "v_add_f32 v252, v252, v235\n\t" \
    "v_add_f32 v254, v254, v255\n\t" \
    "v_add_f32 v252, v252, v254\n\t" \
    "v_add_f32 v252, v252, v234\n\t" \
    "v_mov_b32 v255, v252\n\t" \
    "v_mov_b32 v254, v252\n\t" \
    "s_nop 1\n\t" \
    "v_permlane16_swap_b32 v255, v254\n\t" \
    "v_add_f32 v252, v255, v254\n\t" \
    "v_add_f32 v255, v255, v254\n\t" \
    "s_nop 1\n\t" \
    "v_permlane32_swap_b32 v252, v255\n\t" \
    "v_add_f32 v252, v252, v255\n\t" \
    "v_rcp_f32 v252, v252\n\t" \
    "s_nop 0\n\t" \
    "v_mul_f32 v0, v0, v252\n\t" \
    "v_mul_f32 v1, v1, v252\n\t" \
    "v_mul_f32 v2, v2, v252\n\t" \
    "v_mul_f32 v3, v3, v252\n\t" \
    "v_mul_f32 v4, v4, v252\n\t" \
    "v_mul_f32 v5, v5, v252\n\t" \
    "v_mul_f32 v6, v6, v252\n\t" \
    "v_mul_f32 v7, v7, v252\n\t" \
    "v_mul_f32 v232, v232, v252\n\t" \
    "v_mul_f32 v233, v233, v252\n\t" \
    "v_cvt_pk_bf16_f32 v0, v0, v1\n\t" \
    "v_cvt_pk_bf16_f32 v1, v2, v3\n\t" \
    "v_cvt_pk_bf16_f32 v2, v4, v5\n\t" \
    "v_cvt_pk_bf16_f32 v3, v6, v7\n\t" \
    "v_cvt_pk_bf16_f32 v4, v232, v233\n\t" \
    "v_mov_b32 v5, 0\n\t" \
    "v_mov_b32 v6, 0\n\t" \
    "v_cvt_pk_bf16_f32 v7, v232, v233\n\t" \
    "s_waitcnt lgkmcnt(0)\n\t" \
    "v_mfma_f32_16x16x32_bf16 v[236:239], v[236:239], v[0:3], 0\n\t" \
    "v_mfma_f32_16x16x32_bf16 v[240:243], v[240:243], v[0:3], 0\n\t" \
    "v_mfma_f32_16x16x32_bf16 v[244:247], v[244:247], v[0:3], 0\n\t" \
    "v_mfma_f32_16x16x32_bf16 v[248:251], v[248:251], v[0:3], 0\n\t" \
    "v_mfma_f32_16x16x16_bf16 v[236:239], v[228:229], v[4:5], v[236:239]\n\t" \
    "v_mfma_f32_16x16x16_bf16 v[240:243], v[228:229], v[6:7], v[240:243]\n\t" \
    "v_mfma_f32_16x16x16_bf16 v[244:247], v[230:231], v[4:5], v[244:247]\n\t" \
    "v_mfma_f32_16x16x16_bf16 v[248:251], v[230:231], v[6:7], v[248:251]\n\t" \
    "s_nop 4\n\t" \
    "v_cvt_pk_bf16_f32 v212, v236, v237\n\t" \
    "v_cvt_pk_bf16_f32 v213, v240, v241\n\t" \
    "v_cvt_pk_bf16_f32 v214, v238, v239\n\t" \
    "v_cvt_pk_bf16_f32 v215, v242, v243\n\t" \
    "v_permlane16_swap_b32 v212, v213\n\t" \
    "s_nop 0\n\t" \
    "v_permlane16_swap_b32 v214, v215\n\t" \
    "v_cvt_pk_bf16_f32 v216, v244, v245\n\t" \
    "v_cvt_pk_bf16_f32 v217, v248, v249\n\t" \
    "v_cvt_pk_bf16_f32 v218, v246, v247\n\t" \
    "v_cvt_pk_bf16_f32 v219, v250, v251\n\t" \
    "v_permlane16_swap_b32 v216, v217\n\t" \
    "s_nop 0\n\t" \
    "v_permlane16_swap_b32 v218, v219\n\t" \
    "s_setprio 1\n\t" \
    GSA(2) \
    GSW \
    "s_nop 1\n\t" \
    : [lim] "+s"(lim), "+{v[212:219]}"(oxr) DGO GSO(0) GSO(1) GSO(2) GSO(3) GSO(4) \
    : "{v8}"(am_a), "{v9}"(t0_a), "{v10}"(idx), "{v11}"(iw_a), "{v12}"(ab_a), "{v14}"(xo_v), "{v15}"(kr_a), "{v16}"(dq), "{v17}"(vl_a), "{v18}"(fr_a), [stag] "s"(0xffffff81u + s), [onej] "s"(onej), [rs] "s"(rs), [m0] "s"(sbm + 16384u), [m1] "s"(sbm + 20480u), [st] "s"(s), [tag] "s"(P.tag), [po] "s"(FLG_OFF + F_PRG(4, h) * 128), [fm] "s"(fmask), [qx] "s"(qxo), [qd] "s"(lbase + LA_VM), [qh] "s"((j & 1) * 2048), [ox] "s"(oxo), [se] "s"((j & 1) * 4096), [l2e] "s"(0x3fb8aa3b), "{v[20:27]}"(wr[0][0]), "{v[28:35]}"(wr[0][1]), "{v[36:43]}"(wr[1][0]), "{v[44:51]}"(wr[1][1]), "{v[52:59]}"(wr[2][0]), "{v[60:67]}"(wr[2][1]), "{v[68:75]}"(wr[3][0]), "{v[76:83]}"(wr[3][1]), "{v[84:91]}"(wr[4][0]), "{v[92:99]}"(wr[4][1]), "{v[100:107]}"(wr[5][0]), "{v[108:115]}"(wr[5][1]), "{v[116:123]}"(wr[6][0]), "{v[124:131]}"(wr[6][1]), "{v[132:139]}"(wr[7][0]), "{v[140:147]}"(wr[7][1]), "{v[148:155]}"(wr[8][0]), "{v[156:163]}"(wr[8][1]), "{v[164:171]}"(wr[9][0]), "{v[172:179]}"(wr[9][1]), "{v[180:187]}"(wr[10][0]), "{v[188:195]}"(wr[10][1]), "{v[196:203]}"(wr[11][0]), "{v[204:211]}"(wr[11][1]) \
    : "memory", "scc", "vcc", "s90", "s91", "s72", "s73", "s74", "s75", "s76", "s77", "s78", "s79", "s80", "s81", "s82", "s83", "s84", "s85", "s86", "s87", "v0", "v1", "v2", "v3", "v4", "v5", "v6", "v7", "v220", "v221", "v222", "v223", "v224", "v225", "v226", "v227", "v228", "v229", "v230", "v231", "v232", "v233", "v234", "v235", "v236", "v237", "v238", "v239", "v240", "v241", "v242", "v243", "v244", "v245", "v246", "v247", "v248", "v249", "v250", "v251", "v252", "v253", "v254", "v255")

#define B_POLL() \
  asm volatile( \
    "TWB_%=:\n\t" \
    "s_memrealtime s[88:89]\n\t" \
    "s_memtime s[90:91]\n\t" \
    "s_waitcnt lgkmcnt(0)\n\t" \
    "s_lshl_b32 s88, s88, 2\n\t" \
    "s_sub_u32 s89, %[tgt], s88\n\t" \
    "s_cmp_gt_i32 s89, 16\n\t" \
    "s_cbranch_scc1 TWB_%=\n\t" \
    "s_mul_i32 s89, s89, 11\n\t" \
    "s_ashr_i32 s89, s89, 1\n\t" \
    "s_add_u32 s89, s89, s90\n\t" \
    "TCB_%=:\n\t" \
    "s_memtime s[92:93]\n\t" \
    "s_waitcnt lgkmcnt(0)\n\t" \
    "s_sub_u32 s93, s89, s92\n\t" \
    "s_cmp_gt_i32 s93, 0\n\t" \
    "s_cbranch_scc1 TCB_%=\n\t" \
    "buffer_load_dwordx4 v[220:223], %[l16], %[rs], %[m0] offen offset:0 sc1\n\t" \
    "buffer_load_dwordx4 v[224:227], %[l16], %[rs], %[m0] offen offset:1024 sc1\n\t" \
    "buffer_load_dwordx4 v[228:231], %[l16], %[rs], %[m1] offen offset:0 sc1\n\t" \
    "buffer_load_dwordx4 v[232:235], %[l16], %[rs], %[m1] offen offset:1024 sc1\n\t" \
    "v_mov_b32 v252, v252\n\t" \
    "s_nop 2\n\t" \
    "v_mov_b32 v252, v252\n\t" \
    "s_nop 2\n\t" \
    "v_mov_b32 v252, v252\n\t" \
    "s_nop 2\n\t" \
    "v_mov_b32 v252, v252\n\t" \
    "s_nop 2\n\t" \
    "v_mov_b32 v252, v252\n\t" \
    "s_nop 2\n\t" \
    "v_mov_b32 v252, v252\n\t" \
    "s_nop 2\n\t" \
    "v_mov_b32 v252, v252\n\t" \
    "s_nop 2\n\t" \
    "v_mov_b32 v252, v252\n\t" \
    "s_nop 2\n\t" \
    "v_mov_b32 v252, v252\n\t" \
    "s_nop 2\n\t" \
    "v_mov_b32 v252, v252\n\t" \
    "s_nop 2\n\t" \
    "v_mov_b32 v252, v252\n\t" \
    "s_nop 2\n\t" \
    "v_mov_b32 v252, v252\n\t" \
    "s_nop 2\n\t" \
    "v_mov_b32 v252, v252\n\t" \
    "s_nop 2\n\t" \
    "v_mov_b32 v252, v252\n\t" \
    "s_nop 2\n\t" \
    "v_mov_b32 v252, v252\n\t" \
    "s_nop 2\n\t" \
    "v_mov_b32 v252, v252\n\t" \
    "s_nop 2\n\t" \
    "v_mov_b32 v252, v252\n\t" \
    "s_nop 2\n\t" \
    "v_mov_b32 v252, v252\n\t" \
    "s_nop 2\n\t" \
    "v_mov_b32 v252, v252\n\t" \
    "s_nop 2\n\t" \
    "v_mov_b32 v252, v252\n\t" \
    "s_nop 2\n\t" \
    "v_mov_b32 v252, v252\n\t" \
    "s_nop 2\n\t" \
    "v_mov_b32 v252, v252\n\t" \
    "s_nop 2\n\t" \
    "v_mov_b32 v252, v252\n\t" \
    "s_nop 2\n\t" \
    "v_mov_b32 v252, v252\n\t" \
    "s_nop 2\n\t" \
    "PM_%=:\n\t" \
    "s_waitcnt vmcnt(3)\n\t" \
    "ds_write_b128 %[la], v[220:223] offset:0\n\t" \
    "v_max3_u32 v252, v220, v220, v221\n\t" \
    "v_max3_u32 v252, v252, v222, v223\n\t" \
    "s_waitcnt vmcnt(2)\n\t" \
    "ds_write_b128 %[la], v[224:227] offset:1024\n\t" \
    "v_max3_u32 v252, v252, v224, v225\n\t" \
    "v_max3_u32 v252, v252, v226, v227\n\t" \
    "s_waitcnt vmcnt(1)\n\t" \
    "ds_write_b128 %[la], v[228:231] offset:8192\n\t" \
    "v_max3_u32 v252, v252, v228, v229\n\t" \
    "v_max3_u32 v252, v252, v230, v231\n\t" \
    "s_waitcnt vmcnt(0)\n\t" \
    "ds_write_b128 %[la], v[232:235] offset:9216\n\t" \
    "v_max3_u32 v252, v252, v232, v233\n\t" \
    "v_max3_u32 v252, v252, v234, v235\n\t" \
    "v_cmp_eq_u32 vcc, -1, v252\n\t" \
    "s_cbranch_vccz PD_%=\n\t" \
    "s_sub_u32 %[lim], %[lim], 1\n\t" \
    "s_cmp_gt_i32 %[lim], 0\n\t" \
    "s_cbranch_scc0 PD_%=\n\t" \
    "s_sleep " STR(CADM) "\n\t" \
    "buffer_load_dwordx4 v[220:223], %[l16], %[rs], %[m0] offen offset:0 sc1\n\t" \
    "buffer_load_dwordx4 v[224:227], %[l16], %[rs], %[m0] offen offset:1024 sc1\n\t" \
    "buffer_load_dwordx4 v[228:231], %[l16], %[rs], %[m1] offen offset:0 sc1\n\t" \
    "buffer_load_dwordx4 v[232:235], %[l16], %[rs], %[m1] offen offset:1024 sc1\n\t" \
    "s_branch PM_%=\n\t" \
    "PD_%=:\n\t" \
    GSA(3) \
    "s_waitcnt lgkmcnt(0)\n\t" \
    "s_barrier\n\t" \
    : [lim] "+s"(lim) GSO(3) \
    : [l16] "v"(l16), [la] "v"(la), [rs] "s"(rs), [m0] "s"(mo), [m1] "s"(mo + 16384u), [tgt] "s"(tgt) \
    : "memory", "vcc", "scc", "v252", "s88", "s89", "s90", "s91", "s92", "s93", "v220", "v221", "v222", "v223", "v224", "v225", "v226", "v227", "v228", "v229", "v230", "v231", "v232", "v233", "v234", "v235")

#define T_WAIT() \
  asm volatile( \
    "TWC_%=:\n\t" \
    "s_memrealtime s[88:89]\n\t" \
    "s_memtime s[90:91]\n\t" \
    "s_waitcnt lgkmcnt(0)\n\t" \
    "s_lshl_b32 s88, s88, 2\n\t" \
    "s_sub_u32 s89, %[tgt], s88\n\t" \
    "s_cmp_gt_i32 s89, 16\n\t" \
    "s_cbranch_scc1 TWC_%=\n\t" \
    "s_mul_i32 s89, s89, 11\n\t" \
    "s_ashr_i32 s89, s89, 1\n\t" \
    "s_add_u32 s89, s89, s90\n\t" \
    "TCC_%=:\n\t" \
    "s_memtime s[92:93]\n\t" \
    "s_waitcnt lgkmcnt(0)\n\t" \
    "s_sub_u32 s93, s89, s92\n\t" \
    "s_cmp_gt_i32 s93, 0\n\t" \
    "s_cbranch_scc1 TCC_%=\n\t" \
    :  \
    : [tgt] "s"(tgt) \
    : "s88", "s89", "s90", "s91", "s92", "s93", "memory", "scc")

#define V_PROJ() \
  asm volatile( \
    "v_mov_b32 v0, 0\n\t" \
    "v_mov_b32 v1, 0\n\t" \
    "v_mov_b32 v2, 0\n\t" \
    "v_mov_b32 v3, 0\n\t" \
    "v_mov_b32 v4, 0\n\t" \
    "v_mov_b32 v5, 0\n\t" \
    "v_mov_b32 v6, 0\n\t" \
    "v_mov_b32 v7, 0\n\t" \
    "s_barrier\n\t" \
    "ds_read_b128 v[220:223], %[am] offset:0\n\t" \
    "ds_read_b128 v[224:227], %[am] offset:1024\n\t" \
    "ds_read_b128 v[228:231], %[am] offset:2048\n\t" \
    "ds_read_b128 v[232:235], %[am] offset:3072\n\t" \
    "ds_read_b128 v[236:239], %[am] offset:4096\n\t" \
    "ds_read_b128 v[240:243], %[am] offset:5120\n\t" \
    "ds_read_b128 v[244:247], %[am] offset:6144\n\t" \
    "ds_read_b128 v[248:251], %[am] offset:7168\n\t" \
    "s_waitcnt lgkmcnt(7)\n\t" \
    "v_smfmac_f32_16x16x64_bf16 v[0:3], v[220:223], v[28:35], %[idx]\n\t" \
    "ds_read_b128 v[220:223], %[am] offset:8192\n\t" \
    "s_waitcnt lgkmcnt(7)\n\t" \
    "v_smfmac_f32_16x16x64_bf16 v[4:7], v[224:227], v[36:43], %[idx]\n\t" \
    "ds_read_b128 v[224:227], %[am] offset:9216\n\t" \
    "s_waitcnt lgkmcnt(7)\n\t" \
    "v_smfmac_f32_16x16x64_bf16 v[0:3], v[228:231], v[44:51], %[idx]\n\t" \
    "ds_read_b128 v[228:231], %[am] offset:10240\n\t" \
    "s_waitcnt lgkmcnt(7)\n\t" \
    "v_smfmac_f32_16x16x64_bf16 v[4:7], v[232:235], v[52:59], %[idx]\n\t" \
    "ds_read_b128 v[232:235], %[am] offset:11264\n\t" \
    "s_waitcnt lgkmcnt(7)\n\t" \
    "v_smfmac_f32_16x16x64_bf16 v[0:3], v[236:239], v[60:67], %[idx]\n\t" \
    "ds_read_b128 v[236:239], %[am] offset:12288\n\t" \
    "s_waitcnt lgkmcnt(7)\n\t" \
    "v_smfmac_f32_16x16x64_bf16 v[4:7], v[240:243], v[68:75], %[idx]\n\t" \
    "ds_read_b128 v[240:243], %[am] offset:13312\n\t" \
    "s_waitcnt lgkmcnt(7)\n\t" \
    "v_smfmac_f32_16x16x64_bf16 v[0:3], v[244:247], v[76:83], %[idx]\n\t" \
    "ds_read_b128 v[244:247], %[am] offset:14336\n\t" \
    "s_waitcnt lgkmcnt(7)\n\t" \
    "v_smfmac_f32_16x16x64_bf16 v[4:7], v[248:251], v[84:91], %[idx]\n\t" \
    "ds_read_b128 v[248:251], %[am] offset:15360\n\t" \
    "s_waitcnt lgkmcnt(7)\n\t" \
    "v_smfmac_f32_16x16x64_bf16 v[0:3], v[220:223], v[92:99], %[idx]\n\t" \
    "s_waitcnt lgkmcnt(6)\n\t" \
    "v_smfmac_f32_16x16x64_bf16 v[4:7], v[224:227], v[100:107], %[idx]\n\t" \
    "s_waitcnt lgkmcnt(5)\n\t" \
    "v_smfmac_f32_16x16x64_bf16 v[0:3], v[228:231], v[108:115], %[idx]\n\t" \
    "s_waitcnt lgkmcnt(4)\n\t" \
    "v_smfmac_f32_16x16x64_bf16 v[4:7], v[232:235], v[116:123], %[idx]\n\t" \
    "s_waitcnt lgkmcnt(3)\n\t" \
    "v_smfmac_f32_16x16x64_bf16 v[0:3], v[236:239], v[124:131], %[idx]\n\t" \
    "s_waitcnt lgkmcnt(2)\n\t" \
    "v_smfmac_f32_16x16x64_bf16 v[4:7], v[240:243], v[132:139], %[idx]\n\t" \
    "s_waitcnt lgkmcnt(1)\n\t" \
    "v_smfmac_f32_16x16x64_bf16 v[0:3], v[244:247], v[140:147], %[idx]\n\t" \
    "s_waitcnt lgkmcnt(0)\n\t" \
    "v_smfmac_f32_16x16x64_bf16 v[4:7], v[248:251], v[148:155], %[idx]\n\t" \
    "s_nop 5\n\t" \
    "v_add_f32 v0, v0, v1\n\t" \
    "v_add_f32 v2, v2, v3\n\t" \
    "v_add_f32 v4, v4, v5\n\t" \
    "v_add_f32 v6, v6, v7\n\t" \
    "v_add_f32 v0, v0, v4\n\t" \
    "v_add_f32 v2, v2, v6\n\t" \
    "v_cvt_pk_bf16_f32 v0, v0, v2\n\t" \
    "v_min_u32 v0, -2, v0\n\t" \
    "s_nop 1\n\t" \
    "v_mov_b32_dpp v1, v0 quad_perm:[1,1,1,1] row_mask:0xf bank_mask:0xf\n\t" \
    "v_mov_b32_dpp v2, v0 quad_perm:[2,2,2,2] row_mask:0xf bank_mask:0xf\n\t" \
    "v_mov_b32_dpp v3, v0 quad_perm:[3,3,3,3] row_mask:0xf bank_mask:0xf\n\t" \
    "s_mov_b64 exec, %[qm]\n\t" \
    "buffer_store_dwordx4 v[0:3], %[l4], %[rs], %[vo] offen sc1\n\t" \
    "s_mov_b64 exec, -1\n\t" \
    "s_nop 1\n\t" \
    :  \
    : [am] "v"(am_a), [idx] "v"(idx), [l4] "v"(ln * 4), [rs] "s"(rs), [vo] "s"(sb + vo), [qm] "s"(0x1111111111111111ull), "{v[28:35]}"(wv[0]), "{v[36:43]}"(wv[1]), "{v[44:51]}"(wv[2]), "{v[52:59]}"(wv[3]), "{v[60:67]}"(wv[4]), "{v[68:75]}"(wv[5]), "{v[76:83]}"(wv[6]), "{v[84:91]}"(wv[7]), "{v[92:99]}"(wv[8]), "{v[100:107]}"(wv[9]), "{v[108:115]}"(wv[10]), "{v[116:123]}"(wv[11]), "{v[124:131]}"(wv[12]), "{v[132:139]}"(wv[13]), "{v[140:147]}"(wv[14]), "{v[148:155]}"(wv[15]) \
    : "memory", "v0", "v1", "v2", "v3", "v4", "v5", "v6", "v7", "v220", "v221", "v222", "v223", "v224", "v225", "v226", "v227", "v228", "v229", "v230", "v231", "v232", "v233", "v234", "v235", "v236", "v237", "v238", "v239", "v240", "v241", "v242", "v243", "v244", "v245", "v246", "v247", "v248", "v249", "v250", "v251")

#define WO_V() \
  asm volatile( \
    "v_mov_b32 v0, 0\n\t" \
    "v_mov_b32 v1, 0\n\t" \
    "v_mov_b32 v2, 0\n\t" \
    "v_mov_b32 v3, 0\n\t" \
    "v_mov_b32 v4, 0\n\t" \
    "v_mov_b32 v5, 0\n\t" \
    "v_mov_b32 v6, 0\n\t" \
    "v_mov_b32 v7, 0\n\t" \
    "v_mov_b32 v8, 0\n\t" \
    "v_mov_b32 v9, 0\n\t" \
    "v_mov_b32 v10, 0\n\t" \
    "v_mov_b32 v11, 0\n\t" \
    "v_mov_b32 v12, 0\n\t" \
    "v_mov_b32 v13, 0\n\t" \
    "v_mov_b32 v14, 0\n\t" \
    "v_mov_b32 v15, 0\n\t" \
    "v_mov_b32 v252, 0\n\t" \
    "s_barrier\n\t" \
    GSA(4) \
    DGC \
    "buffer_load_dwordx4 v[220:223], %[l16], %[rs], %[s0] offen sc1\n\t" \
    "buffer_load_dwordx4 v[224:227], %[l16], %[rs], %[s1] offen sc1\n\t" \
    "A_%=:\n\t" \
    "s_waitcnt vmcnt(1)\n\t" \
    "v_smfmac_f32_16x16x64_bf16 v[0:3], v[220:223], v[156:163], %[idx]\n\t" \
    "v_max3_u32 v252, v252, v220, v221\n\t" \
    "v_smfmac_f32_16x16x64_bf16 v[4:7], v[220:223], v[164:171], %[idx]\n\t" \
    "v_max3_u32 v252, v252, v222, v223\n\t" \
    "v_smfmac_f32_16x16x64_bf16 v[8:11], v[220:223], v[172:179], %[idx]\n\t" \
    "v_smfmac_f32_16x16x64_bf16 v[12:15], v[220:223], v[180:187], %[idx]\n\t" \
    "s_waitcnt vmcnt(0)\n\t" \
    "v_smfmac_f32_16x16x64_bf16 v[0:3], v[224:227], v[188:195], %[idx]\n\t" \
    "v_max3_u32 v252, v252, v224, v225\n\t" \
    "v_smfmac_f32_16x16x64_bf16 v[4:7], v[224:227], v[196:203], %[idx]\n\t" \
    "v_max3_u32 v252, v252, v226, v227\n\t" \
    "v_smfmac_f32_16x16x64_bf16 v[8:11], v[224:227], v[204:211], %[idx]\n\t" \
    "v_smfmac_f32_16x16x64_bf16 v[12:15], v[224:227], v[212:219], %[idx]\n\t" \
    "v_cmp_eq_u32 vcc, -1, v252\n\t" \
    "s_cbranch_vccnz SL_%=\n\t" \
    "D_%=:\n\t" \
    GSA(1) \
    "s_nop 2\n\t" \
    "v_add_f32 v0, v0, v1\n\t" \
    "v_add_f32 v2, v2, v3\n\t" \
    "v_add_f32 v4, v4, v5\n\t" \
    "v_add_f32 v6, v6, v7\n\t" \
    "v_add_f32 v8, v8, v9\n\t" \
    "v_add_f32 v10, v10, v11\n\t" \
    "v_add_f32 v12, v12, v13\n\t" \
    "v_add_f32 v14, v14, v15\n\t" \
    "v_permlane32_swap_b32 v0, v4\n\t" \
    "v_permlane32_swap_b32 v2, v6\n\t" \
    "v_permlane32_swap_b32 v8, v12\n\t" \
    "v_permlane32_swap_b32 v10, v14\n\t" \
    "v_add_f32 v0, v0, v12\n\t" \
    "v_add_f32 v2, v2, v14\n\t" \
    "ds_write2_b32 %[rw], v0, v2 offset0:0 offset1:32\n\t" \
    "s_waitcnt lgkmcnt(0)\n\t" \
    "s_barrier\n\t" \
    "s_cmp_eq_u32 %[red], 0\n\t" \
    "s_cbranch_scc0 E_%=\n\t" \
    "ds_read2st64_b64 v[0:3], %[rr] offset0:0 offset1:1\n\t" \
    "ds_read2st64_b64 v[4:7], %[rr] offset0:2 offset1:3\n\t" \
    "ds_read2st64_b64 v[8:11], %[rr] offset0:4 offset1:5\n\t" \
    "ds_read2st64_b64 v[12:15], %[rr] offset0:6 offset1:7\n\t" \
    "s_waitcnt lgkmcnt(3)\n\t" \
    "v_pk_add_f32 v[0:1], v[0:1], v[2:3]\n\t" \
    "s_waitcnt lgkmcnt(2)\n\t" \
    "v_pk_add_f32 v[4:5], v[4:5], v[6:7]\n\t" \
    "s_waitcnt lgkmcnt(1)\n\t" \
    "v_pk_add_f32 v[8:9], v[8:9], v[10:11]\n\t" \
    "v_pk_add_f32 v[0:1], v[0:1], v[4:5]\n\t" \
    "s_waitcnt lgkmcnt(0)\n\t" \
    "v_pk_add_f32 v[12:13], v[12:13], v[14:15]\n\t" \
    "s_nop 0\n\t" \
    "v_pk_add_f32 v[8:9], v[8:9], v[12:13]\n\t" \
    "s_nop 0\n\t" \
    "v_pk_add_f32 v[0:1], v[0:1], v[8:9]\n\t" \
    "s_nop 0\n\t" \
    "v_cvt_pk_bf16_f32 v0, v0, v1\n\t" \
    "v_min_u32 v0, -2, v0\n\t" \
    "buffer_store_dword v0, %[mo], %[rs], %[sn] offen sc1\n\t" \
    "s_memrealtime %[t1]\n\t" \
    DGA \
    "s_waitcnt lgkmcnt(0)\n\t" \
    GSA(2) \
    "s_branch E_%=\n\t" \
    "SL_%=:\n\t" \
    "s_sleep 1\n\t" \
    "buffer_load_dwordx4 v[220:223], %[l16], %[rs], %[s0] offen sc1\n\t" \
    "buffer_load_dwordx4 v[224:227], %[l16], %[rs], %[s1] offen sc1\n\t" \
    "s_sub_u32 %[lim], %[lim], " STR(DEC2) "\n\t" \
    "s_cmp_gt_i32 %[lim], 0\n\t" \
    "s_cbranch_scc0 G_%=\n\t" \
    "s_nop 3\n\t" \
    "v_mov_b32 v0, 0\n\t" \
    "v_mov_b32 v1, 0\n\t" \
    "v_mov_b32 v2, 0\n\t" \
    "v_mov_b32 v3, 0\n\t" \
    "v_mov_b32 v4, 0\n\t" \
    "v_mov_b32 v5, 0\n\t" \
    "v_mov_b32 v6, 0\n\t" \
    "v_mov_b32 v7, 0\n\t" \
    "v_mov_b32 v8, 0\n\t" \
    "v_mov_b32 v9, 0\n\t" \
    "v_mov_b32 v10, 0\n\t" \
    "v_mov_b32 v11, 0\n\t" \
    "v_mov_b32 v12, 0\n\t" \
    "v_mov_b32 v13, 0\n\t" \
    "v_mov_b32 v14, 0\n\t" \
    "v_mov_b32 v15, 0\n\t" \
    "v_mov_b32 v252, 0\n\t" \
    "s_branch A_%=\n\t" \
    "G_%=:\n\t" \
    "s_waitcnt vmcnt(0)\n\t" \
    "s_branch D_%=\n\t" \
    "E_%=:\n\t" \
    GSW \
    : [lim] "+s"(lim) GSO(1) GSO(4), [t1] "=&s"(t1) DGO DGQ GSO(2) \
    : [l16] "v"(l16), [rs] "s"(rs), [idx] "v"(idx), [rw] "v"(rw_a), [s0] "s"(so[0]), [s1] "s"(so[1]), [rr] "v"(rr_a), [mo] "v"(mo_v), [sn] "s"(sn), [red] "s"(wid), "{v[156:163]}"(wo[0][0][0]), "{v[164:171]}"(wo[0][0][1]), "{v[172:179]}"(wo[0][1][0]), "{v[180:187]}"(wo[0][1][1]), "{v[188:195]}"(wo[1][0][0]), "{v[196:203]}"(wo[1][0][1]), "{v[204:211]}"(wo[1][1][0]), "{v[212:219]}"(wo[1][1][1]) \
    : "memory", "vcc", "scc", "v252", "v0", "v1", "v2", "v3", "v4", "v5", "v6", "v7", "v8", "v9", "v10", "v11", "v12", "v13", "v14", "v15", "v220", "v221", "v222", "v223", "v224", "v225", "v226", "v227")

#define WO_W() \
  asm volatile( \
    "v_mov_b32 v0, 0\n\t" \
    "v_mov_b32 v1, 0\n\t" \
    "v_mov_b32 v2, 0\n\t" \
    "v_mov_b32 v3, 0\n\t" \
    "v_mov_b32 v4, 0\n\t" \
    "v_mov_b32 v5, 0\n\t" \
    "v_mov_b32 v6, 0\n\t" \
    "v_mov_b32 v7, 0\n\t" \
    "v_mov_b32 v8, 0\n\t" \
    "v_mov_b32 v9, 0\n\t" \
    "v_mov_b32 v10, 0\n\t" \
    "v_mov_b32 v11, 0\n\t" \
    "v_mov_b32 v12, 0\n\t" \
    "v_mov_b32 v13, 0\n\t" \
    "v_mov_b32 v14, 0\n\t" \
    "v_mov_b32 v15, 0\n\t" \
    "v_mov_b32 v252, 0\n\t" \
    "s_barrier\n\t" \
    GSA(4) \
    "buffer_load_dwordx4 v[220:223], %[l16], %[rs], %[s0] offen sc1\n\t" \
    "s_nop 15\n\t" \
    "buffer_load_dwordx4 v[224:227], %[l16], %[rs], %[s1] offen sc1\n\t" \
    "s_nop 15\n\t" \
    "buffer_load_dwordx4 v[228:231], %[l16], %[rs], %[s2] offen sc1\n\t" \
    "s_nop 15\n\t" \
    "buffer_load_dwordx4 v[232:235], %[l16], %[rs], %[s3] offen sc1\n\t" \
    "s_nop 15\n\t" \
    "buffer_load_dwordx4 v[236:239], %[l16], %[rs], %[s4] offen sc1\n\t" \
    "s_nop 15\n\t" \
    "buffer_load_dwordx4 v[240:243], %[l16], %[rs], %[s5] offen sc1\n\t" \
    "A_%=:\n\t" \
    "s_waitcnt vmcnt(5)\n\t" \
    "v_smfmac_f32_16x16x64_bf16 v[0:3], v[220:223], v[28:35], %[idx]\n\t" \
    "v_max3_u32 v252, v252, v220, v221\n\t" \
    "v_smfmac_f32_16x16x64_bf16 v[4:7], v[220:223], v[36:43], %[idx]\n\t" \
    "v_max3_u32 v252, v252, v222, v223\n\t" \
    "v_smfmac_f32_16x16x64_bf16 v[8:11], v[220:223], v[44:51], %[idx]\n\t" \
    "v_smfmac_f32_16x16x64_bf16 v[12:15], v[220:223], v[52:59], %[idx]\n\t" \
    "s_waitcnt vmcnt(4)\n\t" \
    "v_smfmac_f32_16x16x64_bf16 v[0:3], v[224:227], v[60:67], %[idx]\n\t" \
    "v_max3_u32 v252, v252, v224, v225\n\t" \
    "v_smfmac_f32_16x16x64_bf16 v[4:7], v[224:227], v[68:75], %[idx]\n\t" \
    "v_max3_u32 v252, v252, v226, v227\n\t" \
    "v_smfmac_f32_16x16x64_bf16 v[8:11], v[224:227], v[76:83], %[idx]\n\t" \
    "v_smfmac_f32_16x16x64_bf16 v[12:15], v[224:227], v[84:91], %[idx]\n\t" \
    "s_waitcnt vmcnt(3)\n\t" \
    "v_smfmac_f32_16x16x64_bf16 v[0:3], v[228:231], v[92:99], %[idx]\n\t" \
    "v_max3_u32 v252, v252, v228, v229\n\t" \
    "v_smfmac_f32_16x16x64_bf16 v[4:7], v[228:231], v[100:107], %[idx]\n\t" \
    "v_max3_u32 v252, v252, v230, v231\n\t" \
    "v_smfmac_f32_16x16x64_bf16 v[8:11], v[228:231], v[108:115], %[idx]\n\t" \
    "v_smfmac_f32_16x16x64_bf16 v[12:15], v[228:231], v[116:123], %[idx]\n\t" \
    "s_waitcnt vmcnt(2)\n\t" \
    "v_smfmac_f32_16x16x64_bf16 v[0:3], v[232:235], v[124:131], %[idx]\n\t" \
    "v_max3_u32 v252, v252, v232, v233\n\t" \
    "v_smfmac_f32_16x16x64_bf16 v[4:7], v[232:235], v[132:139], %[idx]\n\t" \
    "v_max3_u32 v252, v252, v234, v235\n\t" \
    "v_smfmac_f32_16x16x64_bf16 v[8:11], v[232:235], v[140:147], %[idx]\n\t" \
    "v_smfmac_f32_16x16x64_bf16 v[12:15], v[232:235], v[148:155], %[idx]\n\t" \
    "s_waitcnt vmcnt(1)\n\t" \
    "v_smfmac_f32_16x16x64_bf16 v[0:3], v[236:239], v[156:163], %[idx]\n\t" \
    "v_max3_u32 v252, v252, v236, v237\n\t" \
    "v_smfmac_f32_16x16x64_bf16 v[4:7], v[236:239], v[164:171], %[idx]\n\t" \
    "v_max3_u32 v252, v252, v238, v239\n\t" \
    "v_smfmac_f32_16x16x64_bf16 v[8:11], v[236:239], v[172:179], %[idx]\n\t" \
    "v_smfmac_f32_16x16x64_bf16 v[12:15], v[236:239], v[180:187], %[idx]\n\t" \
    "s_waitcnt vmcnt(0)\n\t" \
    "v_smfmac_f32_16x16x64_bf16 v[0:3], v[240:243], v[188:195], %[idx]\n\t" \
    "v_max3_u32 v252, v252, v240, v241\n\t" \
    "v_smfmac_f32_16x16x64_bf16 v[4:7], v[240:243], v[196:203], %[idx]\n\t" \
    "v_max3_u32 v252, v252, v242, v243\n\t" \
    "v_smfmac_f32_16x16x64_bf16 v[8:11], v[240:243], v[204:211], %[idx]\n\t" \
    "v_smfmac_f32_16x16x64_bf16 v[12:15], v[240:243], v[212:219], %[idx]\n\t" \
    "v_cmp_eq_u32 vcc, -1, v252\n\t" \
    "s_cbranch_vccnz SL_%=\n\t" \
    "D_%=:\n\t" \
    GSA(1) \
    "s_nop 2\n\t" \
    "v_add_f32 v0, v0, v1\n\t" \
    "v_add_f32 v2, v2, v3\n\t" \
    "v_add_f32 v4, v4, v5\n\t" \
    "v_add_f32 v6, v6, v7\n\t" \
    "v_add_f32 v8, v8, v9\n\t" \
    "v_add_f32 v10, v10, v11\n\t" \
    "v_add_f32 v12, v12, v13\n\t" \
    "v_add_f32 v14, v14, v15\n\t" \
    "v_permlane32_swap_b32 v0, v4\n\t" \
    "v_permlane32_swap_b32 v2, v6\n\t" \
    "v_permlane32_swap_b32 v8, v12\n\t" \
    "v_permlane32_swap_b32 v10, v14\n\t" \
    "v_add_f32 v0, v0, v12\n\t" \
    "v_add_f32 v2, v2, v14\n\t" \
    "ds_write2_b32 %[rw], v0, v2 offset0:0 offset1:32\n\t" \
    "s_waitcnt lgkmcnt(0)\n\t" \
    "s_barrier\n\t" \
    "s_branch E_%=\n\t" \
    "SL_%=:\n\t" \
    "s_sleep 1\n\t" \
    "buffer_load_dwordx4 v[220:223], %[l16], %[rs], %[s0] offen sc1\n\t" \
    "buffer_load_dwordx4 v[224:227], %[l16], %[rs], %[s1] offen sc1\n\t" \
    "buffer_load_dwordx4 v[228:231], %[l16], %[rs], %[s2] offen sc1\n\t" \
    "buffer_load_dwordx4 v[232:235], %[l16], %[rs], %[s3] offen sc1\n\t" \
    "buffer_load_dwordx4 v[236:239], %[l16], %[rs], %[s4] offen sc1\n\t" \
    "buffer_load_dwordx4 v[240:243], %[l16], %[rs], %[s5] offen sc1\n\t" \
    "s_sub_u32 %[lim], %[lim], " STR(DEC2) "\n\t" \
    "s_cmp_gt_i32 %[lim], 0\n\t" \
    "s_cbranch_scc0 G_%=\n\t" \
    "s_nop 3\n\t" \
    "v_mov_b32 v0, 0\n\t" \
    "v_mov_b32 v1, 0\n\t" \
    "v_mov_b32 v2, 0\n\t" \
    "v_mov_b32 v3, 0\n\t" \
    "v_mov_b32 v4, 0\n\t" \
    "v_mov_b32 v5, 0\n\t" \
    "v_mov_b32 v6, 0\n\t" \
    "v_mov_b32 v7, 0\n\t" \
    "v_mov_b32 v8, 0\n\t" \
    "v_mov_b32 v9, 0\n\t" \
    "v_mov_b32 v10, 0\n\t" \
    "v_mov_b32 v11, 0\n\t" \
    "v_mov_b32 v12, 0\n\t" \
    "v_mov_b32 v13, 0\n\t" \
    "v_mov_b32 v14, 0\n\t" \
    "v_mov_b32 v15, 0\n\t" \
    "v_mov_b32 v252, 0\n\t" \
    "s_branch A_%=\n\t" \
    "G_%=:\n\t" \
    "s_waitcnt vmcnt(0)\n\t" \
    "s_branch D_%=\n\t" \
    "E_%=:\n\t" \
    GSW \
    : [lim] "+s"(lim) GSO(1) GSO(4) \
    : [l16] "v"(l16), [rs] "s"(rs), [idx] "v"(idx), [rw] "v"(rw_a), [s0] "s"(so[0]), [s1] "s"(so[1]), [s2] "s"(so[2]), [s3] "s"(so[3]), [s4] "s"(so[4]), [s5] "s"(so[5]), "{v[28:35]}"(wo[0][0][0]), "{v[36:43]}"(wo[0][0][1]), "{v[44:51]}"(wo[0][1][0]), "{v[52:59]}"(wo[0][1][1]), "{v[60:67]}"(wo[1][0][0]), "{v[68:75]}"(wo[1][0][1]), "{v[76:83]}"(wo[1][1][0]), "{v[84:91]}"(wo[1][1][1]), "{v[92:99]}"(wo[2][0][0]), "{v[100:107]}"(wo[2][0][1]), "{v[108:115]}"(wo[2][1][0]), "{v[116:123]}"(wo[2][1][1]), "{v[124:131]}"(wo[3][0][0]), "{v[132:139]}"(wo[3][0][1]), "{v[140:147]}"(wo[3][1][0]), "{v[148:155]}"(wo[3][1][1]), "{v[156:163]}"(wo[4][0][0]), "{v[164:171]}"(wo[4][0][1]), "{v[172:179]}"(wo[4][1][0]), "{v[180:187]}"(wo[4][1][1]), "{v[188:195]}"(wo[5][0][0]), "{v[196:203]}"(wo[5][0][1]), "{v[204:211]}"(wo[5][1][0]), "{v[212:219]}"(wo[5][1][1]) \
    : "memory", "vcc", "scc", "v252", "v0", "v1", "v2", "v3", "v4", "v5", "v6", "v7", "v8", "v9", "v10", "v11", "v12", "v13", "v14", "v15", "v220", "v221", "v222", "v223", "v224", "v225", "v226", "v227", "v228", "v229", "v230", "v231", "v232", "v233", "v234", "v235", "v236", "v237", "v238", "v239", "v240", "v241", "v242", "v243")

#define S_LOOP_K(TE_, FIN_) \
  asm volatile( \
    "s_cmp_eq_u32 %[kh], 0\n\t" \
    "s_cbranch_scc0 L1_%=\n\t" \
    "L0_%=:\n\t" \
    "s_setprio 0\n\t" \
    "s_nop 3\n\t" \
    "ds_write_b128 %[pa], v[196:199] offset:0\n\t" \
    "ds_write_b128 %[pa], v[200:203] offset:1024\n\t" \
    "ds_write_b128 %[pa], v[204:207] offset:2048\n\t" \
    "ds_write_b128 %[pa], v[208:211] offset:3072\n\t" \
    "s_sub_u32 %[a], %[t], 1\n\t" \
    "v_mov_b32 v53, %[tag]\n\t" \
    "v_mov_b32 v52, %[a]\n\t" \
    "s_sub_u32 %[b], %[t], 2\n\t" \
    "s_cmp_lt_u32 %[b], " STR(SQUICK) "\n\t" \
    "s_cselect_b64 exec, %[fm], 0\n\t" \
    "buffer_store_dwordx2 v[52:53], %[l16], %[rs], %[fo] offen sc1\n\t" \
    "s_mov_b64 exec, -1\n\t" \
    "s_add_u32 m0, %[sd], 49152\n\t" \
    "s_add_u32 %[a], %[sx], 81920\n\t" \
    "s_waitcnt lgkmcnt(0)\n\t" \
    "buffer_load_dwordx4 %[l16], %[rs], %[a] offen offset:0 sc1 lds\n\t" \
    "buffer_load_dwordx4 %[l16], %[rs], %[a] offen offset:1024 sc1 lds\n\t" \
    "buffer_load_dwordx4 %[l16], %[rs], %[a] offen offset:2048 sc1 lds\n\t" \
    "buffer_load_dwordx4 %[l16], %[rs], %[a] offen offset:3072 sc1 lds\n\t" \
    "v_mfma_f32_16x16x32_bf16 v[0:3], v[68:71], v[20:23], 0\n\t" \
    "ds_read_b128 v[44:47], %[xp] offset:6144\n\t" \
    "v_mfma_f32_16x16x32_bf16 v[4:7], v[76:79], v[20:23], 0\n\t" \
    "ds_read_b128 v[48:51], %[xp] offset:7168\n\t" \
    "v_mfma_f32_16x16x32_bf16 v[8:11], v[68:71], v[24:27], 0\n\t" \
    "v_mfma_f32_16x16x32_bf16 v[12:15], v[76:79], v[24:27], 0\n\t" \
    "v_mfma_f32_16x16x32_bf16 v[0:3], v[72:75], v[28:31], v[0:3]\n\t" \
    "ds_read_b128 v[20:23], %[xp] offset:8192\n\t" \
    "v_mfma_f32_16x16x32_bf16 v[4:7], v[80:83], v[28:31], v[4:7]\n\t" \
    "ds_read_b128 v[24:27], %[xp] offset:9216\n\t" \
    "v_mfma_f32_16x16x32_bf16 v[8:11], v[72:75], v[32:35], v[8:11]\n\t" \
    "v_mfma_f32_16x16x32_bf16 v[12:15], v[80:83], v[32:35], v[12:15]\n\t" \
    "v_mfma_f32_16x16x32_bf16 v[0:3], v[84:87], v[36:39], v[0:3]\n\t" \
    "ds_read_b128 v[28:31], %[xp] offset:10240\n\t" \
    "v_mfma_f32_16x16x32_bf16 v[4:7], v[92:95], v[36:39], v[4:7]\n\t" \
    "ds_read_b128 v[32:35], %[xp] offset:11264\n\t" \
    "v_mfma_f32_16x16x32_bf16 v[8:11], v[84:87], v[40:43], v[8:11]\n\t" \
    "s_waitcnt lgkmcnt(4)\n\t" \
    "v_mfma_f32_16x16x32_bf16 v[12:15], v[92:95], v[40:43], v[12:15]\n\t" \
    "v_mfma_f32_16x16x32_bf16 v[0:3], v[88:91], v[44:47], v[0:3]\n\t" \
    "ds_read_b128 v[36:39], %[xp] offset:12288\n\t" \
    "v_mfma_f32_16x16x32_bf16 v[4:7], v[96:99], v[44:47], v[4:7]\n\t" \
    "ds_read_b128 v[40:43], %[xp] offset:13312\n\t" \
    "v_mfma_f32_16x16x32_bf16 v[8:11], v[88:91], v[48:51], v[8:11]\n\t" \
    "s_waitcnt lgkmcnt(4)\n\t" \
    "v_mfma_f32_16x16x32_bf16 v[12:15], v[96:99], v[48:51], v[12:15]\n\t" \
    "v_mfma_f32_16x16x32_bf16 v[0:3], v[100:103], v[20:23], v[0:3]\n\t" \
    "ds_read_b128 v[44:47], %[xp] offset:14336\n\t" \
    "v_mfma_f32_16x16x32_bf16 v[4:7], v[108:111], v[20:23], v[4:7]\n\t" \
    "ds_read_b128 v[48:51], %[xp] offset:15360\n\t" \
    "v_mfma_f32_16x16x32_bf16 v[8:11], v[100:103], v[24:27], v[8:11]\n\t" \
    "s_waitcnt lgkmcnt(4)\n\t" \
    "v_mfma_f32_16x16x32_bf16 v[12:15], v[108:111], v[24:27], v[12:15]\n\t" \
    "v_mfma_f32_16x16x32_bf16 v[0:3], v[104:107], v[28:31], v[0:3]\n\t" \
    "ds_read_b128 v[20:23], %[xp] offset:16384\n\t" \
    "v_mfma_f32_16x16x32_bf16 v[4:7], v[112:115], v[28:31], v[4:7]\n\t" \
    "ds_read_b128 v[24:27], %[xp] offset:17408\n\t" \
    "v_mfma_f32_16x16x32_bf16 v[8:11], v[104:107], v[32:35], v[8:11]\n\t" \
    "s_waitcnt lgkmcnt(4)\n\t" \
    "v_mfma_f32_16x16x32_bf16 v[12:15], v[112:115], v[32:35], v[12:15]\n\t" \
    "v_mfma_f32_16x16x32_bf16 v[0:3], v[116:119], v[36:39], v[0:3]\n\t" \
    "ds_read_b128 v[28:31], %[xp] offset:18432\n\t" \
    "v_mfma_f32_16x16x32_bf16 v[4:7], v[124:127], v[36:39], v[4:7]\n\t" \
    "ds_read_b128 v[32:35], %[xp] offset:19456\n\t" \
    "v_mfma_f32_16x16x32_bf16 v[8:11], v[116:119], v[40:43], v[8:11]\n\t" \
    "s_waitcnt lgkmcnt(4)\n\t" \
    "v_mfma_f32_16x16x32_bf16 v[12:15], v[124:127], v[40:43], v[12:15]\n\t" \
    "v_mfma_f32_16x16x32_bf16 v[0:3], v[120:123], v[44:47], v[0:3]\n\t" \
    "ds_read_b128 v[36:39], %[xp] offset:20480\n\t" \
    "v_mfma_f32_16x16x32_bf16 v[4:7], v[128:131], v[44:47], v[4:7]\n\t" \
    "ds_read_b128 v[40:43], %[xp] offset:21504\n\t" \
    "v_mfma_f32_16x16x32_bf16 v[8:11], v[120:123], v[48:51], v[8:11]\n\t" \
    "s_waitcnt lgkmcnt(4)\n\t" \
    "v_mfma_f32_16x16x32_bf16 v[12:15], v[128:131], v[48:51], v[12:15]\n\t" \
    "s_waitcnt vmcnt(4)\n\t" \
    "s_barrier\n\t" \
    "s_setprio 0\n\t" \
    "s_sub_u32 %[a], %[t], 1\n\t" \
    "v_mov_b32 v53, %[tag]\n\t" \
    "v_mov_b32 v52, %[a]\n\t" \
    "s_cmp_gt_u32 %[t], 1\n\t" \
    "s_cselect_b64 exec, %[fm], 0\n\t" \
    "buffer_store_dwordx2 v[52:53], %[l16], %[rs], %[fo] offen sc1\n\t" \
    "s_mov_b64 exec, %[pm]\n\t" \
    "s_mov_b32 m0, %[pd]\n\t" \
    "s_add_u32 %[a], %[sx], %[po]\n\t" \
    "buffer_load_dwordx4 %[l16], %[rs], %[a] offen sc1 lds\n\t" \
    "s_mov_b64 exec, -1\n\t" \
    "s_add_u32 m0, %[sd], 0\n\t" \
    "s_add_u32 %[a], %[sx], 131072\n\t" \
    "buffer_load_dwordx4 %[l16], %[rs], %[a] offen offset:0 sc1 lds\n\t" \
    "buffer_load_dwordx4 %[l16], %[rs], %[a] offen offset:1024 sc1 lds\n\t" \
    "buffer_load_dwordx4 %[l16], %[rs], %[a] offen offset:2048 sc1 lds\n\t" \
    "buffer_load_dwordx4 %[l16], %[rs], %[a] offen offset:3072 sc1 lds\n\t" \
    "v_mfma_f32_16x16x32_bf16 v[0:3], v[132:135], v[20:23], v[0:3]\n\t" \
    "ds_read_b128 v[44:47], %[xp] offset:22528\n\t" \
    "v_mfma_f32_16x16x32_bf16 v[4:7], v[140:143], v[20:23], v[4:7]\n\t" \
    "ds_read_b128 v[48:51], %[xp] offset:23552\n\t" \
    "v_mfma_f32_16x16x32_bf16 v[8:11], v[132:135], v[24:27], v[8:11]\n\t" \
    "s_waitcnt lgkmcnt(4)\n\t" \
    "v_mfma_f32_16x16x32_bf16 v[12:15], v[140:143], v[24:27], v[12:15]\n\t" \
    "v_mfma_f32_16x16x32_bf16 v[0:3], v[136:139], v[28:31], v[0:3]\n\t" \
    "ds_read_b128 v[20:23], %[xp] offset:24576\n\t" \
    "v_mfma_f32_16x16x32_bf16 v[4:7], v[144:147], v[28:31], v[4:7]\n\t" \
    "ds_read_b128 v[24:27], %[xp] offset:25600\n\t" \
    "v_mfma_f32_16x16x32_bf16 v[8:11], v[136:139], v[32:35], v[8:11]\n\t" \
    "s_waitcnt lgkmcnt(4)\n\t" \
    "v_mfma_f32_16x16x32_bf16 v[12:15], v[144:147], v[32:35], v[12:15]\n\t" \
    "v_mfma_f32_16x16x32_bf16 v[0:3], v[148:151], v[36:39], v[0:3]\n\t" \
    "ds_read_b128 v[28:31], %[xp] offset:26624\n\t" \
    "v_mfma_f32_16x16x32_bf16 v[4:7], v[156:159], v[36:39], v[4:7]\n\t" \
    "ds_read_b128 v[32:35], %[xp] offset:27648\n\t" \
    "v_mfma_f32_16x16x32_bf16 v[8:11], v[148:151], v[40:43], v[8:11]\n\t" \
    "s_waitcnt lgkmcnt(4)\n\t" \
    "v_mfma_f32_16x16x32_bf16 v[12:15], v[156:159], v[40:43], v[12:15]\n\t" \
    "v_mfma_f32_16x16x32_bf16 v[0:3], v[152:155], v[44:47], v[0:3]\n\t" \
    "ds_read_b128 v[36:39], %[xp] offset:28672\n\t" \
    "v_mfma_f32_16x16x32_bf16 v[4:7], v[160:163], v[44:47], v[4:7]\n\t" \
    "ds_read_b128 v[40:43], %[xp] offset:29696\n\t" \
    "v_mfma_f32_16x16x32_bf16 v[8:11], v[152:155], v[48:51], v[8:11]\n\t" \
    "s_waitcnt lgkmcnt(4)\n\t" \
    "v_mfma_f32_16x16x32_bf16 v[12:15], v[160:163], v[48:51], v[12:15]\n\t" \
    "v_mfma_f32_16x16x32_bf16 v[0:3], v[164:167], v[20:23], v[0:3]\n\t" \
    "ds_read_b128 v[44:47], %[xp] offset:30720\n\t" \
    "v_mfma_f32_16x16x32_bf16 v[4:7], v[172:175], v[20:23], v[4:7]\n\t" \
    "ds_read_b128 v[48:51], %[xp] offset:31744\n\t" \
    "v_mfma_f32_16x16x32_bf16 v[8:11], v[164:167], v[24:27], v[8:11]\n\t" \
    "s_waitcnt lgkmcnt(4)\n\t" \
    "v_mfma_f32_16x16x32_bf16 v[12:15], v[172:175], v[24:27], v[12:15]\n\t" \
    "v_mfma_f32_16x16x32_bf16 v[0:3], v[168:171], v[28:31], v[0:3]\n\t" \
    "ds_read_b128 v[20:23], %[xp] offset:32768\n\t" \
    "v_mfma_f32_16x16x32_bf16 v[4:7], v[176:179], v[28:31], v[4:7]\n\t" \
    "ds_read_b128 v[24:27], %[xp] offset:33792\n\t" \
    "v_mfma_f32_16x16x32_bf16 v[8:11], v[168:171], v[32:35], v[8:11]\n\t" \
    "s_waitcnt lgkmcnt(4)\n\t" \
    "v_mfma_f32_16x16x32_bf16 v[12:15], v[176:179], v[32:35], v[12:15]\n\t" \
    "v_mfma_f32_16x16x32_bf16 v[0:3], v[180:183], v[36:39], v[0:3]\n\t" \
    "ds_read_b128 v[28:31], %[xp] offset:34816\n\t" \
    "v_mfma_f32_16x16x32_bf16 v[4:7], v[188:191], v[36:39], v[4:7]\n\t" \
    "ds_read_b128 v[32:35], %[xp] offset:35840\n\t" \
    "v_mfma_f32_16x16x32_bf16 v[8:11], v[180:183], v[40:43], v[8:11]\n\t" \
    "s_waitcnt lgkmcnt(4)\n\t" \
    "v_mfma_f32_16x16x32_bf16 v[12:15], v[188:191], v[40:43], v[12:15]\n\t" \
    "v_mfma_f32_16x16x32_bf16 v[0:3], v[184:187], v[44:47], v[0:3]\n\t" \
    "ds_read_b128 v[36:39], %[xp] offset:36864\n\t" \
    "v_mfma_f32_16x16x32_bf16 v[4:7], v[192:195], v[44:47], v[4:7]\n\t" \
    "ds_read_b128 v[40:43], %[xp] offset:37888\n\t" \
    "v_mfma_f32_16x16x32_bf16 v[8:11], v[184:187], v[48:51], v[8:11]\n\t" \
    "s_waitcnt lgkmcnt(4)\n\t" \
    "v_mfma_f32_16x16x32_bf16 v[12:15], v[192:195], v[48:51], v[12:15]\n\t" \
    "s_add_u32 %[sx], %[sx], 65536\n\t" \
    "s_add_u32 %[t], %[t], 1\n\t" \
    "s_waitcnt vmcnt(4)\n\t" \
    "s_barrier\n\t" \
    "s_setprio 0\n\t" \
    "s_nop 3\n\t" \
    "ds_write_b128 %[pa], v[0:3] offset:0\n\t" \
    "ds_write_b128 %[pa], v[4:7] offset:1024\n\t" \
    "ds_write_b128 %[pa], v[8:11] offset:2048\n\t" \
    "ds_write_b128 %[pa], v[12:15] offset:3072\n\t" \
    "s_sub_u32 %[a], %[t], 1\n\t" \
    "v_mov_b32 v53, %[tag]\n\t" \
    "v_mov_b32 v52, %[a]\n\t" \
    "s_sub_u32 %[b], %[t], 2\n\t" \
    "s_cmp_lt_u32 %[b], " STR(SQUICK) "\n\t" \
    "s_cselect_b64 exec, %[fm], 0\n\t" \
    "buffer_store_dwordx2 v[52:53], %[l16], %[rs], %[fo] offen sc1\n\t" \
    "s_mov_b64 exec, -1\n\t" \
    "s_add_u32 m0, %[sd], 16384\n\t" \
    "s_add_u32 %[a], %[sx], 81920\n\t" \
    "s_waitcnt lgkmcnt(0)\n\t" \
    "buffer_load_dwordx4 %[l16], %[rs], %[a] offen offset:0 sc1 lds\n\t" \
    "buffer_load_dwordx4 %[l16], %[rs], %[a] offen offset:1024 sc1 lds\n\t" \
    "buffer_load_dwordx4 %[l16], %[rs], %[a] offen offset:2048 sc1 lds\n\t" \
    "buffer_load_dwordx4 %[l16], %[rs], %[a] offen offset:3072 sc1 lds\n\t" \
    "v_mfma_f32_16x16x32_bf16 v[196:199], v[68:71], v[20:23], 0\n\t" \
    "ds_read_b128 v[44:47], %[xp] offset:38912\n\t" \
    "v_mfma_f32_16x16x32_bf16 v[200:203], v[76:79], v[20:23], 0\n\t" \
    "ds_read_b128 v[48:51], %[xp] offset:39936\n\t" \
    "v_mfma_f32_16x16x32_bf16 v[204:207], v[68:71], v[24:27], 0\n\t" \
    "v_mfma_f32_16x16x32_bf16 v[208:211], v[76:79], v[24:27], 0\n\t" \
    "v_mfma_f32_16x16x32_bf16 v[196:199], v[72:75], v[28:31], v[196:199]\n\t" \
    "ds_read_b128 v[20:23], %[xp] offset:40960\n\t" \
    "v_mfma_f32_16x16x32_bf16 v[200:203], v[80:83], v[28:31], v[200:203]\n\t" \
    "ds_read_b128 v[24:27], %[xp] offset:41984\n\t" \
    "v_mfma_f32_16x16x32_bf16 v[204:207], v[72:75], v[32:35], v[204:207]\n\t" \
    "v_mfma_f32_16x16x32_bf16 v[208:211], v[80:83], v[32:35], v[208:211]\n\t" \
    "v_mfma_f32_16x16x32_bf16 v[196:199], v[84:87], v[36:39], v[196:199]\n\t" \
    "ds_read_b128 v[28:31], %[xp] offset:43008\n\t" \
    "v_mfma_f32_16x16x32_bf16 v[200:203], v[92:95], v[36:39], v[200:203]\n\t" \
    "ds_read_b128 v[32:35], %[xp] offset:44032\n\t" \
    "v_mfma_f32_16x16x32_bf16 v[204:207], v[84:87], v[40:43], v[204:207]\n\t" \
    "s_waitcnt lgkmcnt(4)\n\t" \
    "v_mfma_f32_16x16x32_bf16 v[208:211], v[92:95], v[40:43], v[208:211]\n\t" \
    "v_mfma_f32_16x16x32_bf16 v[196:199], v[88:91], v[44:47], v[196:199]\n\t" \
    "ds_read_b128 v[36:39], %[xp] offset:45056\n\t" \
    "v_mfma_f32_16x16x32_bf16 v[200:203], v[96:99], v[44:47], v[200:203]\n\t" \
    "ds_read_b128 v[40:43], %[xp] offset:46080\n\t" \
    "v_mfma_f32_16x16x32_bf16 v[204:207], v[88:91], v[48:51], v[204:207]\n\t" \
    "s_waitcnt lgkmcnt(4)\n\t" \
    "v_mfma_f32_16x16x32_bf16 v[208:211], v[96:99], v[48:51], v[208:211]\n\t" \
    "v_mfma_f32_16x16x32_bf16 v[196:199], v[100:103], v[20:23], v[196:199]\n\t" \
    "ds_read_b128 v[44:47], %[xp] offset:47104\n\t" \
    "v_mfma_f32_16x16x32_bf16 v[200:203], v[108:111], v[20:23], v[200:203]\n\t" \
    "ds_read_b128 v[48:51], %[xp] offset:48128\n\t" \
    "v_mfma_f32_16x16x32_bf16 v[204:207], v[100:103], v[24:27], v[204:207]\n\t" \
    "s_waitcnt lgkmcnt(4)\n\t" \
    "v_mfma_f32_16x16x32_bf16 v[208:211], v[108:111], v[24:27], v[208:211]\n\t" \
    "v_mfma_f32_16x16x32_bf16 v[196:199], v[104:107], v[28:31], v[196:199]\n\t" \
    "ds_read_b128 v[20:23], %[xp] offset:49152\n\t" \
    "v_mfma_f32_16x16x32_bf16 v[200:203], v[112:115], v[28:31], v[200:203]\n\t" \
    "ds_read_b128 v[24:27], %[xp] offset:50176\n\t" \
    "v_mfma_f32_16x16x32_bf16 v[204:207], v[104:107], v[32:35], v[204:207]\n\t" \
    "s_waitcnt lgkmcnt(4)\n\t" \
    "v_mfma_f32_16x16x32_bf16 v[208:211], v[112:115], v[32:35], v[208:211]\n\t" \
    "v_mfma_f32_16x16x32_bf16 v[196:199], v[116:119], v[36:39], v[196:199]\n\t" \
    "ds_read_b128 v[28:31], %[xp] offset:51200\n\t" \
    "v_mfma_f32_16x16x32_bf16 v[200:203], v[124:127], v[36:39], v[200:203]\n\t" \
    "ds_read_b128 v[32:35], %[xp] offset:52224\n\t" \
    "v_mfma_f32_16x16x32_bf16 v[204:207], v[116:119], v[40:43], v[204:207]\n\t" \
    "s_waitcnt lgkmcnt(4)\n\t" \
    "v_mfma_f32_16x16x32_bf16 v[208:211], v[124:127], v[40:43], v[208:211]\n\t" \
    "v_mfma_f32_16x16x32_bf16 v[196:199], v[120:123], v[44:47], v[196:199]\n\t" \
    "ds_read_b128 v[36:39], %[xp] offset:53248\n\t" \
    "v_mfma_f32_16x16x32_bf16 v[200:203], v[128:131], v[44:47], v[200:203]\n\t" \
    "ds_read_b128 v[40:43], %[xp] offset:54272\n\t" \
    "v_mfma_f32_16x16x32_bf16 v[204:207], v[120:123], v[48:51], v[204:207]\n\t" \
    "s_waitcnt lgkmcnt(4)\n\t" \
    "v_mfma_f32_16x16x32_bf16 v[208:211], v[128:131], v[48:51], v[208:211]\n\t" \
    "s_waitcnt vmcnt(4)\n\t" \
    "s_barrier\n\t" \
    "s_setprio 0\n\t" \
    "s_sub_u32 %[a], %[t], 1\n\t" \
    "v_mov_b32 v53, %[tag]\n\t" \
    "v_mov_b32 v52, %[a]\n\t" \
    "s_cmp_gt_u32 %[t], 1\n\t" \
    "s_cselect_b64 exec, %[fm], 0\n\t" \
    "buffer_store_dwordx2 v[52:53], %[l16], %[rs], %[fo] offen sc1\n\t" \
    "s_mov_b64 exec, %[pm]\n\t" \
    "s_mov_b32 m0, %[pd]\n\t" \
    "s_add_u32 %[a], %[sx], %[po]\n\t" \
    "buffer_load_dwordx4 %[l16], %[rs], %[a] offen sc1 lds\n\t" \
    "s_mov_b64 exec, -1\n\t" \
    "s_add_u32 m0, %[sd], 32768\n\t" \
    "s_add_u32 %[a], %[sx], 131072\n\t" \
    "buffer_load_dwordx4 %[l16], %[rs], %[a] offen offset:0 sc1 lds\n\t" \
    "buffer_load_dwordx4 %[l16], %[rs], %[a] offen offset:1024 sc1 lds\n\t" \
    "buffer_load_dwordx4 %[l16], %[rs], %[a] offen offset:2048 sc1 lds\n\t" \
    "buffer_load_dwordx4 %[l16], %[rs], %[a] offen offset:3072 sc1 lds\n\t" \
    "v_mfma_f32_16x16x32_bf16 v[196:199], v[132:135], v[20:23], v[196:199]\n\t" \
    "ds_read_b128 v[44:47], %[xp] offset:55296\n\t" \
    "v_mfma_f32_16x16x32_bf16 v[200:203], v[140:143], v[20:23], v[200:203]\n\t" \
    "ds_read_b128 v[48:51], %[xp] offset:56320\n\t" \
    "v_mfma_f32_16x16x32_bf16 v[204:207], v[132:135], v[24:27], v[204:207]\n\t" \
    "s_waitcnt lgkmcnt(4)\n\t" \
    "v_mfma_f32_16x16x32_bf16 v[208:211], v[140:143], v[24:27], v[208:211]\n\t" \
    "v_mfma_f32_16x16x32_bf16 v[196:199], v[136:139], v[28:31], v[196:199]\n\t" \
    "ds_read_b128 v[20:23], %[xp] offset:57344\n\t" \
    "v_mfma_f32_16x16x32_bf16 v[200:203], v[144:147], v[28:31], v[200:203]\n\t" \
    "ds_read_b128 v[24:27], %[xp] offset:58368\n\t" \
    "v_mfma_f32_16x16x32_bf16 v[204:207], v[136:139], v[32:35], v[204:207]\n\t" \
    "s_waitcnt lgkmcnt(4)\n\t" \
    "v_mfma_f32_16x16x32_bf16 v[208:211], v[144:147], v[32:35], v[208:211]\n\t" \
    "v_mfma_f32_16x16x32_bf16 v[196:199], v[148:151], v[36:39], v[196:199]\n\t" \
    "ds_read_b128 v[28:31], %[xp] offset:59392\n\t" \
    "v_mfma_f32_16x16x32_bf16 v[200:203], v[156:159], v[36:39], v[200:203]\n\t" \
    "ds_read_b128 v[32:35], %[xp] offset:60416\n\t" \
    "v_mfma_f32_16x16x32_bf16 v[204:207], v[148:151], v[40:43], v[204:207]\n\t" \
    "s_waitcnt lgkmcnt(4)\n\t" \
    "v_mfma_f32_16x16x32_bf16 v[208:211], v[156:159], v[40:43], v[208:211]\n\t" \
    "v_mfma_f32_16x16x32_bf16 v[196:199], v[152:155], v[44:47], v[196:199]\n\t" \
    "ds_read_b128 v[36:39], %[xp] offset:61440\n\t" \
    "v_mfma_f32_16x16x32_bf16 v[200:203], v[160:163], v[44:47], v[200:203]\n\t" \
    "ds_read_b128 v[40:43], %[xp] offset:62464\n\t" \
    "v_mfma_f32_16x16x32_bf16 v[204:207], v[152:155], v[48:51], v[204:207]\n\t" \
    "s_waitcnt lgkmcnt(4)\n\t" \
    "v_mfma_f32_16x16x32_bf16 v[208:211], v[160:163], v[48:51], v[208:211]\n\t" \
    "v_mfma_f32_16x16x32_bf16 v[196:199], v[164:167], v[20:23], v[196:199]\n\t" \
    "ds_read_b128 v[44:47], %[xp] offset:63488\n\t" \
    "v_mfma_f32_16x16x32_bf16 v[200:203], v[172:175], v[20:23], v[200:203]\n\t" \
    "ds_read_b128 v[48:51], %[xp] offset:64512\n\t" \
    "v_mfma_f32_16x16x32_bf16 v[204:207], v[164:167], v[24:27], v[204:207]\n\t" \
    "s_waitcnt lgkmcnt(4)\n\t" \
    "v_mfma_f32_16x16x32_bf16 v[208:211], v[172:175], v[24:27], v[208:211]\n\t" \
    "v_mfma_f32_16x16x32_bf16 v[196:199], v[168:171], v[28:31], v[196:199]\n\t" \
    "ds_read_b128 v[20:23], %[xp] offset:0\n\t" \
    "v_mfma_f32_16x16x32_bf16 v[200:203], v[176:179], v[28:31], v[200:203]\n\t" \
    "ds_read_b128 v[24:27], %[xp] offset:1024\n\t" \
    "v_mfma_f32_16x16x32_bf16 v[204:207], v[168:171], v[32:35], v[204:207]\n\t" \
    "s_waitcnt lgkmcnt(4)\n\t" \
    "v_mfma_f32_16x16x32_bf16 v[208:211], v[176:179], v[32:35], v[208:211]\n\t" \
    "v_mfma_f32_16x16x32_bf16 v[196:199], v[180:183], v[36:39], v[196:199]\n\t" \
    "ds_read_b128 v[28:31], %[xp] offset:2048\n\t" \
    "v_mfma_f32_16x16x32_bf16 v[200:203], v[188:191], v[36:39], v[200:203]\n\t" \
    "ds_read_b128 v[32:35], %[xp] offset:3072\n\t" \
    "v_mfma_f32_16x16x32_bf16 v[204:207], v[180:183], v[40:43], v[204:207]\n\t" \
    "s_waitcnt lgkmcnt(4)\n\t" \
    "v_mfma_f32_16x16x32_bf16 v[208:211], v[188:191], v[40:43], v[208:211]\n\t" \
    "v_mfma_f32_16x16x32_bf16 v[196:199], v[184:187], v[44:47], v[196:199]\n\t" \
    "ds_read_b128 v[36:39], %[xp] offset:4096\n\t" \
    "v_mfma_f32_16x16x32_bf16 v[200:203], v[192:195], v[44:47], v[200:203]\n\t" \
    "ds_read_b128 v[40:43], %[xp] offset:5120\n\t" \
    "v_mfma_f32_16x16x32_bf16 v[204:207], v[184:187], v[48:51], v[204:207]\n\t" \
    "s_waitcnt lgkmcnt(4)\n\t" \
    "v_mfma_f32_16x16x32_bf16 v[208:211], v[192:195], v[48:51], v[208:211]\n\t" \
    "s_add_u32 %[sx], %[sx], 65536\n\t" \
    "s_add_u32 %[t], %[t], 1\n\t" \
    "s_waitcnt vmcnt(4)\n\t" \
    "s_barrier\n\t" \
    "s_cmp_lt_u32 %[t], %[te]\n\t" \
    "s_cbranch_scc1 L0_%=\n\t" \
    "s_cmp_eq_u32 %[fin], %[te]\n\t" \
    "s_cbranch_scc0 E_%=\n\t" \
    "s_nop 4\n\t" \
    "ds_write_b128 %[pa], v[196:199] offset:0\n\t" \
    "ds_write_b128 %[pa], v[200:203] offset:1024\n\t" \
    "ds_write_b128 %[pa], v[204:207] offset:2048\n\t" \
    "ds_write_b128 %[pa], v[208:211] offset:3072\n\t" \
    "s_waitcnt lgkmcnt(0)\n\t" \
    "s_barrier\n\t" \
    "s_branch E_%=\n\t" \
    "L1_%=:\n\t" \
    "s_setprio 1\n\t" \
    "s_add_u32 m0, %[sd], 49152\n\t" \
    "s_add_u32 %[a], %[sx], 81920\n\t" \
    "v_mfma_f32_16x16x32_bf16 v[0:3], v[68:71], v[20:23], 0\n\t" \
    "ds_read_b128 v[44:47], %[xp] offset:6144\n\t" \
    "v_mfma_f32_16x16x32_bf16 v[4:7], v[76:79], v[20:23], 0\n\t" \
    "ds_read_b128 v[48:51], %[xp] offset:7168\n\t" \
    "v_mfma_f32_16x16x32_bf16 v[8:11], v[68:71], v[24:27], 0\n\t" \
    "s_waitcnt lgkmcnt(4)\n\t" \
    "v_mfma_f32_16x16x32_bf16 v[12:15], v[76:79], v[24:27], 0\n\t" \
    "v_mfma_f32_16x16x32_bf16 v[0:3], v[72:75], v[28:31], v[0:3]\n\t" \
    "ds_read_b128 v[20:23], %[xp] offset:8192\n\t" \
    "v_mfma_f32_16x16x32_bf16 v[4:7], v[80:83], v[28:31], v[4:7]\n\t" \
    "ds_read_b128 v[24:27], %[xp] offset:9216\n\t" \
    "v_mfma_f32_16x16x32_bf16 v[8:11], v[72:75], v[32:35], v[8:11]\n\t" \
    "s_waitcnt lgkmcnt(4)\n\t" \
    "v_mfma_f32_16x16x32_bf16 v[12:15], v[80:83], v[32:35], v[12:15]\n\t" \
    "v_mfma_f32_16x16x32_bf16 v[0:3], v[84:87], v[36:39], v[0:3]\n\t" \
    "ds_read_b128 v[28:31], %[xp] offset:10240\n\t" \
    "v_mfma_f32_16x16x32_bf16 v[4:7], v[92:95], v[36:39], v[4:7]\n\t" \
    "ds_read_b128 v[32:35], %[xp] offset:11264\n\t" \
    "v_mfma_f32_16x16x32_bf16 v[8:11], v[84:87], v[40:43], v[8:11]\n\t" \
    "s_waitcnt lgkmcnt(4)\n\t" \
    "v_mfma_f32_16x16x32_bf16 v[12:15], v[92:95], v[40:43], v[12:15]\n\t" \
    "v_mfma_f32_16x16x32_bf16 v[0:3], v[88:91], v[44:47], v[0:3]\n\t" \
    "ds_read_b128 v[36:39], %[xp] offset:12288\n\t" \
    "v_mfma_f32_16x16x32_bf16 v[4:7], v[96:99], v[44:47], v[4:7]\n\t" \
    "ds_read_b128 v[40:43], %[xp] offset:13312\n\t" \
    "v_mfma_f32_16x16x32_bf16 v[8:11], v[88:91], v[48:51], v[8:11]\n\t" \
    "s_waitcnt lgkmcnt(4)\n\t" \
    "v_mfma_f32_16x16x32_bf16 v[12:15], v[96:99], v[48:51], v[12:15]\n\t" \
    "v_mfma_f32_16x16x32_bf16 v[0:3], v[100:103], v[20:23], v[0:3]\n\t" \
    "ds_read_b128 v[44:47], %[xp] offset:14336\n\t" \
    "v_mfma_f32_16x16x32_bf16 v[4:7], v[108:111], v[20:23], v[4:7]\n\t" \
    "ds_read_b128 v[48:51], %[xp] offset:15360\n\t" \
    "v_mfma_f32_16x16x32_bf16 v[8:11], v[100:103], v[24:27], v[8:11]\n\t" \
    "s_waitcnt lgkmcnt(4)\n\t" \
    "v_mfma_f32_16x16x32_bf16 v[12:15], v[108:111], v[24:27], v[12:15]\n\t" \
    "buffer_load_dwordx4 %[l16], %[rs], %[a] offen offset:0 sc1 lds\n\t" \
    "v_mfma_f32_16x16x32_bf16 v[0:3], v[104:107], v[28:31], v[0:3]\n\t" \
    "ds_read_b128 v[20:23], %[xp] offset:16384\n\t" \
    "v_mfma_f32_16x16x32_bf16 v[4:7], v[112:115], v[28:31], v[4:7]\n\t" \
    "ds_read_b128 v[24:27], %[xp] offset:17408\n\t" \
    "v_mfma_f32_16x16x32_bf16 v[8:11], v[104:107], v[32:35], v[8:11]\n\t" \
    "s_waitcnt lgkmcnt(4)\n\t" \
    "v_mfma_f32_16x16x32_bf16 v[12:15], v[112:115], v[32:35], v[12:15]\n\t" \
    "buffer_load_dwordx4 %[l16], %[rs], %[a] offen offset:1024 sc1 lds\n\t" \
    "v_mfma_f32_16x16x32_bf16 v[0:3], v[116:119], v[36:39], v[0:3]\n\t" \
    "ds_read_b128 v[28:31], %[xp] offset:18432\n\t" \
    "v_mfma_f32_16x16x32_bf16 v[4:7], v[124:127], v[36:39], v[4:7]\n\t" \
    "ds_read_b128 v[32:35], %[xp] offset:19456\n\t" \
    "v_mfma_f32_16x16x32_bf16 v[8:11], v[116:119], v[40:43], v[8:11]\n\t" \
    "s_waitcnt lgkmcnt(4)\n\t" \
    "v_mfma_f32_16x16x32_bf16 v[12:15], v[124:127], v[40:43], v[12:15]\n\t" \
    "buffer_load_dwordx4 %[l16], %[rs], %[a] offen offset:2048 sc1 lds\n\t" \
    "v_mfma_f32_16x16x32_bf16 v[0:3], v[120:123], v[44:47], v[0:3]\n\t" \
    "ds_read_b128 v[36:39], %[xp] offset:20480\n\t" \
    "v_mfma_f32_16x16x32_bf16 v[4:7], v[128:131], v[44:47], v[4:7]\n\t" \
    "ds_read_b128 v[40:43], %[xp] offset:21504\n\t" \
    "v_mfma_f32_16x16x32_bf16 v[8:11], v[120:123], v[48:51], v[8:11]\n\t" \
    "s_waitcnt lgkmcnt(4)\n\t" \
    "v_mfma_f32_16x16x32_bf16 v[12:15], v[128:131], v[48:51], v[12:15]\n\t" \
    "buffer_load_dwordx4 %[l16], %[rs], %[a] offen offset:3072 sc1 lds\n\t" \
    "s_waitcnt vmcnt(4)\n\t" \
    "s_barrier\n\t" \
    "s_setprio 1\n\t" \
    "s_add_u32 m0, %[sd], 0\n\t" \
    "s_add_u32 %[a], %[sx], 131072\n\t" \
    "s_add_u32 %[b], %[oo], 0x1000\n\t" \
    "s_cmp_lg_u32 %[t], 1\n\t" \
    "s_cselect_b64 %[em], -1, 0\n\t" \
    "v_mfma_f32_16x16x32_bf16 v[0:3], v[132:135], v[20:23], v[0:3]\n\t" \
    "ds_read_b128 v[44:47], %[xp] offset:22528\n\t" \
    "v_mfma_f32_16x16x32_bf16 v[4:7], v[140:143], v[20:23], v[4:7]\n\t" \
    "ds_read_b128 v[48:51], %[xp] offset:23552\n\t" \
    "v_mfma_f32_16x16x32_bf16 v[8:11], v[132:135], v[24:27], v[8:11]\n\t" \
    "ds_read_b128 v[52:55], %[pa] offset:0\n\t" \
    "s_waitcnt lgkmcnt(5)\n\t" \
    "v_mfma_f32_16x16x32_bf16 v[12:15], v[140:143], v[24:27], v[12:15]\n\t" \
    "buffer_load_dwordx4 %[l16], %[rs], %[a] offen offset:0 sc1 lds\n\t" \
    "v_mfma_f32_16x16x32_bf16 v[0:3], v[136:139], v[28:31], v[0:3]\n\t" \
    "ds_read_b128 v[20:23], %[xp] offset:24576\n\t" \
    "v_mfma_f32_16x16x32_bf16 v[4:7], v[144:147], v[28:31], v[4:7]\n\t" \
    "ds_read_b128 v[24:27], %[xp] offset:25600\n\t" \
    "v_mfma_f32_16x16x32_bf16 v[8:11], v[136:139], v[32:35], v[8:11]\n\t" \
    "ds_read_b128 v[56:59], %[pa] offset:1024\n\t" \
    "s_waitcnt lgkmcnt(6)\n\t" \
    "v_mfma_f32_16x16x32_bf16 v[12:15], v[144:147], v[32:35], v[12:15]\n\t" \
    "buffer_load_dwordx4 %[l16], %[rs], %[a] offen offset:1024 sc1 lds\n\t" \
    "v_mfma_f32_16x16x32_bf16 v[0:3], v[148:151], v[36:39], v[0:3]\n\t" \
    "ds_read_b128 v[28:31], %[xp] offset:26624\n\t" \
    "v_mfma_f32_16x16x32_bf16 v[4:7], v[156:159], v[36:39], v[4:7]\n\t" \
    "ds_read_b128 v[32:35], %[xp] offset:27648\n\t" \
    "v_mfma_f32_16x16x32_bf16 v[8:11], v[148:151], v[40:43], v[8:11]\n\t" \
    "ds_read_b128 v[60:63], %[pa] offset:2048\n\t" \
    "s_waitcnt lgkmcnt(7)\n\t" \
    "v_mfma_f32_16x16x32_bf16 v[12:15], v[156:159], v[40:43], v[12:15]\n\t" \
    "buffer_load_dwordx4 %[l16], %[rs], %[a] offen offset:2048 sc1 lds\n\t" \
    "v_mfma_f32_16x16x32_bf16 v[0:3], v[152:155], v[44:47], v[0:3]\n\t" \
    "ds_read_b128 v[36:39], %[xp] offset:28672\n\t" \
    "s_waitcnt lgkmcnt(7)\n\t" \
    "v_add_f32 v196, v196, v52\n\t" \
    "v_mfma_f32_16x16x32_bf16 v[4:7], v[160:163], v[44:47], v[4:7]\n\t" \
    "ds_read_b128 v[40:43], %[xp] offset:29696\n\t" \
    "v_add_f32 v197, v197, v53\n\t" \
    "v_mfma_f32_16x16x32_bf16 v[8:11], v[152:155], v[48:51], v[8:11]\n\t" \
    "ds_read_b128 v[64:67], %[pa] offset:3072\n\t" \
    "s_waitcnt lgkmcnt(7)\n\t" \
    "v_mfma_f32_16x16x32_bf16 v[12:15], v[160:163], v[48:51], v[12:15]\n\t" \
    "buffer_load_dwordx4 %[l16], %[rs], %[a] offen offset:3072 sc1 lds\n\t" \
    "v_add_f32 v198, v198, v54\n\t" \
    "v_add_f32 v199, v199, v55\n\t" \
    "v_mfma_f32_16x16x32_bf16 v[0:3], v[164:167], v[20:23], v[0:3]\n\t" \
    "ds_read_b128 v[44:47], %[xp] offset:30720\n\t" \
    "s_waitcnt lgkmcnt(7)\n\t" \
    "v_add_f32 v200, v200, v56\n\t" \
    "v_mfma_f32_16x16x32_bf16 v[4:7], v[172:175], v[20:23], v[4:7]\n\t" \
    "ds_read_b128 v[48:51], %[xp] offset:31744\n\t" \
    "v_add_f32 v201, v201, v57\n\t" \
    "v_mfma_f32_16x16x32_bf16 v[8:11], v[164:167], v[24:27], v[8:11]\n\t" \
    "v_add_f32 v202, v202, v58\n\t" \
    "s_waitcnt lgkmcnt(6)\n\t" \
    "v_mfma_f32_16x16x32_bf16 v[12:15], v[172:175], v[24:27], v[12:15]\n\t" \
    "v_add_f32 v203, v203, v59\n\t" \
    "s_waitcnt lgkmcnt(5)\n\t" \
    "v_add_f32 v204, v204, v60\n\t" \
    "v_add_f32 v205, v205, v61\n\t" \
    "v_mfma_f32_16x16x32_bf16 v[0:3], v[168:171], v[28:31], v[0:3]\n\t" \
    "ds_read_b128 v[20:23], %[xp] offset:32768\n\t" \
    "v_add_f32 v206, v206, v62\n\t" \
    "v_mfma_f32_16x16x32_bf16 v[4:7], v[176:179], v[28:31], v[4:7]\n\t" \
    "ds_read_b128 v[24:27], %[xp] offset:33792\n\t" \
    "v_add_f32 v207, v207, v63\n\t" \
    "v_mfma_f32_16x16x32_bf16 v[8:11], v[168:171], v[32:35], v[8:11]\n\t" \
    "s_waitcnt lgkmcnt(4)\n\t" \
    "v_add_f32 v208, v208, v64\n\t" \
    "v_mfma_f32_16x16x32_bf16 v[12:15], v[176:179], v[32:35], v[12:15]\n\t" \
    "v_add_f32 v209, v209, v65\n\t" \
    "v_add_f32 v210, v210, v66\n\t" \
    "v_add_f32 v211, v211, v67\n\t" \
    "v_mfma_f32_16x16x32_bf16 v[0:3], v[180:183], v[36:39], v[0:3]\n\t" \
    "ds_read_b128 v[28:31], %[xp] offset:34816\n\t" \
    "v_cvt_pk_bf16_f32 v52, v196, v200\n\t" \
    "v_mfma_f32_16x16x32_bf16 v[4:7], v[188:191], v[36:39], v[4:7]\n\t" \
    "ds_read_b128 v[32:35], %[xp] offset:35840\n\t" \
    "v_cvt_pk_bf16_f32 v53, v197, v201\n\t" \
    "v_mfma_f32_16x16x32_bf16 v[8:11], v[180:183], v[40:43], v[8:11]\n\t" \
    "v_cvt_pk_bf16_f32 v54, v198, v202\n\t" \
    "s_waitcnt lgkmcnt(4)\n\t" \
    "v_mfma_f32_16x16x32_bf16 v[12:15], v[188:191], v[40:43], v[12:15]\n\t" \
    "v_cvt_pk_bf16_f32 v55, v199, v203\n\t" \
    "v_cvt_pk_bf16_f32 v56, v204, v208\n\t" \
    "v_cvt_pk_bf16_f32 v57, v205, v209\n\t" \
    "v_mfma_f32_16x16x32_bf16 v[0:3], v[184:187], v[44:47], v[0:3]\n\t" \
    "ds_read_b128 v[36:39], %[xp] offset:36864\n\t" \
    "v_cvt_pk_bf16_f32 v58, v206, v210\n\t" \
    "v_mfma_f32_16x16x32_bf16 v[4:7], v[192:195], v[44:47], v[4:7]\n\t" \
    "ds_read_b128 v[40:43], %[xp] offset:37888\n\t" \
    "v_cvt_pk_bf16_f32 v59, v207, v211\n\t" \
    "v_mfma_f32_16x16x32_bf16 v[8:11], v[184:187], v[48:51], v[8:11]\n\t" \
    "s_waitcnt lgkmcnt(4)\n\t" \
    "v_mfma_f32_16x16x32_bf16 v[12:15], v[192:195], v[48:51], v[12:15]\n\t" \
    "s_mov_b64 exec, %[em]\n\t" \
    "buffer_store_dwordx4 v[52:55], %[l16], %[rs], %[oo] offen sc1\n\t" \
    "buffer_store_dwordx4 v[56:59], %[l16], %[rs], %[b] offen sc1\n\t" \
    "s_mov_b64 exec, -1\n\t" \
    "s_add_u32 %[sx], %[sx], 65536\n\t" \
    "s_add_u32 %[t], %[t], 1\n\t" \
    "s_add_u32 %[oo], %[oo], 262144\n\t" \
    "s_waitcnt vmcnt(6)\n\t" \
    "s_cmp_gt_u32 %[t], " STR(SQUICK) " + 1\n\t" \
    "s_cbranch_scc1 B0_%=\n\t" \
    "s_waitcnt vmcnt(0)\n\t" \
    "B0_%=:\n\t" \
    "s_barrier\n\t" \
    "s_setprio 1\n\t" \
    "s_add_u32 m0, %[sd], 16384\n\t" \
    "s_add_u32 %[a], %[sx], 81920\n\t" \
    "v_mfma_f32_16x16x32_bf16 v[196:199], v[68:71], v[20:23], 0\n\t" \
    "ds_read_b128 v[44:47], %[xp] offset:38912\n\t" \
    "v_mfma_f32_16x16x32_bf16 v[200:203], v[76:79], v[20:23], 0\n\t" \
    "ds_read_b128 v[48:51], %[xp] offset:39936\n\t" \
    "v_mfma_f32_16x16x32_bf16 v[204:207], v[68:71], v[24:27], 0\n\t" \
    "s_waitcnt lgkmcnt(4)\n\t" \
    "v_mfma_f32_16x16x32_bf16 v[208:211], v[76:79], v[24:27], 0\n\t" \
    "v_mfma_f32_16x16x32_bf16 v[196:199], v[72:75], v[28:31], v[196:199]\n\t" \
    "ds_read_b128 v[20:23], %[xp] offset:40960\n\t" \
    "v_mfma_f32_16x16x32_bf16 v[200:203], v[80:83], v[28:31], v[200:203]\n\t" \
    "ds_read_b128 v[24:27], %[xp] offset:41984\n\t" \
    "v_mfma_f32_16x16x32_bf16 v[204:207], v[72:75], v[32:35], v[204:207]\n\t" \
    "s_waitcnt lgkmcnt(4)\n\t" \
    "v_mfma_f32_16x16x32_bf16 v[208:211], v[80:83], v[32:35], v[208:211]\n\t" \
    "v_mfma_f32_16x16x32_bf16 v[196:199], v[84:87], v[36:39], v[196:199]\n\t" \
    "ds_read_b128 v[28:31], %[xp] offset:43008\n\t" \
    "v_mfma_f32_16x16x32_bf16 v[200:203], v[92:95], v[36:39], v[200:203]\n\t" \
    "ds_read_b128 v[32:35], %[xp] offset:44032\n\t" \
    "v_mfma_f32_16x16x32_bf16 v[204:207], v[84:87], v[40:43], v[204:207]\n\t" \
    "s_waitcnt lgkmcnt(4)\n\t" \
    "v_mfma_f32_16x16x32_bf16 v[208:211], v[92:95], v[40:43], v[208:211]\n\t" \
    "v_mfma_f32_16x16x32_bf16 v[196:199], v[88:91], v[44:47], v[196:199]\n\t" \
    "ds_read_b128 v[36:39], %[xp] offset:45056\n\t" \
    "v_mfma_f32_16x16x32_bf16 v[200:203], v[96:99], v[44:47], v[200:203]\n\t" \
    "ds_read_b128 v[40:43], %[xp] offset:46080\n\t" \
    "v_mfma_f32_16x16x32_bf16 v[204:207], v[88:91], v[48:51], v[204:207]\n\t" \
    "s_waitcnt lgkmcnt(4)\n\t" \
    "v_mfma_f32_16x16x32_bf16 v[208:211], v[96:99], v[48:51], v[208:211]\n\t" \
    "v_mfma_f32_16x16x32_bf16 v[196:199], v[100:103], v[20:23], v[196:199]\n\t" \
    "ds_read_b128 v[44:47], %[xp] offset:47104\n\t" \
    "v_mfma_f32_16x16x32_bf16 v[200:203], v[108:111], v[20:23], v[200:203]\n\t" \
    "ds_read_b128 v[48:51], %[xp] offset:48128\n\t" \
    "v_mfma_f32_16x16x32_bf16 v[204:207], v[100:103], v[24:27], v[204:207]\n\t" \
    "s_waitcnt lgkmcnt(4)\n\t" \
    "v_mfma_f32_16x16x32_bf16 v[208:211], v[108:111], v[24:27], v[208:211]\n\t" \
    "buffer_load_dwordx4 %[l16], %[rs], %[a] offen offset:0 sc1 lds\n\t" \
    "v_mfma_f32_16x16x32_bf16 v[196:199], v[104:107], v[28:31], v[196:199]\n\t" \
    "ds_read_b128 v[20:23], %[xp] offset:49152\n\t" \
    "v_mfma_f32_16x16x32_bf16 v[200:203], v[112:115], v[28:31], v[200:203]\n\t" \
    "ds_read_b128 v[24:27], %[xp] offset:50176\n\t" \
    "v_mfma_f32_16x16x32_bf16 v[204:207], v[104:107], v[32:35], v[204:207]\n\t" \
    "s_waitcnt lgkmcnt(4)\n\t" \
    "v_mfma_f32_16x16x32_bf16 v[208:211], v[112:115], v[32:35], v[208:211]\n\t" \
    "buffer_load_dwordx4 %[l16], %[rs], %[a] offen offset:1024 sc1 lds\n\t" \
    "v_mfma_f32_16x16x32_bf16 v[196:199], v[116:119], v[36:39], v[196:199]\n\t" \
    "ds_read_b128 v[28:31], %[xp] offset:51200\n\t" \
    "v_mfma_f32_16x16x32_bf16 v[200:203], v[124:127], v[36:39], v[200:203]\n\t" \
    "ds_read_b128 v[32:35], %[xp] offset:52224\n\t" \
    "v_mfma_f32_16x16x32_bf16 v[204:207], v[116:119], v[40:43], v[204:207]\n\t" \
    "s_waitcnt lgkmcnt(4)\n\t" \
    "v_mfma_f32_16x16x32_bf16 v[208:211], v[124:127], v[40:43], v[208:211]\n\t" \
    "buffer_load_dwordx4 %[l16], %[rs], %[a] offen offset:2048 sc1 lds\n\t" \
    "v_mfma_f32_16x16x32_bf16 v[196:199], v[120:123], v[44:47], v[196:199]\n\t" \
    "ds_read_b128 v[36:39], %[xp] offset:53248\n\t" \
    "v_mfma_f32_16x16x32_bf16 v[200:203], v[128:131], v[44:47], v[200:203]\n\t" \
    "ds_read_b128 v[40:43], %[xp] offset:54272\n\t" \
    "v_mfma_f32_16x16x32_bf16 v[204:207], v[120:123], v[48:51], v[204:207]\n\t" \
    "s_waitcnt lgkmcnt(4)\n\t" \
    "v_mfma_f32_16x16x32_bf16 v[208:211], v[128:131], v[48:51], v[208:211]\n\t" \
    "buffer_load_dwordx4 %[l16], %[rs], %[a] offen offset:3072 sc1 lds\n\t" \
    "s_waitcnt vmcnt(4)\n\t" \
    "s_barrier\n\t" \
    "s_setprio 1\n\t" \
    "s_add_u32 m0, %[sd], 32768\n\t" \
    "s_add_u32 %[a], %[sx], 131072\n\t" \
    "s_add_u32 %[b], %[oo], 0x1000\n\t" \
    "s_cmp_lg_u32 %[t], 1\n\t" \
    "s_cselect_b64 %[em], -1, 0\n\t" \
    "v_mfma_f32_16x16x32_bf16 v[196:199], v[132:135], v[20:23], v[196:199]\n\t" \
    "ds_read_b128 v[44:47], %[xp] offset:55296\n\t" \
    "v_mfma_f32_16x16x32_bf16 v[200:203], v[140:143], v[20:23], v[200:203]\n\t" \
    "ds_read_b128 v[48:51], %[xp] offset:56320\n\t" \
    "v_mfma_f32_16x16x32_bf16 v[204:207], v[132:135], v[24:27], v[204:207]\n\t" \
    "ds_read_b128 v[52:55], %[pa] offset:0\n\t" \
    "s_waitcnt lgkmcnt(5)\n\t" \
    "v_mfma_f32_16x16x32_bf16 v[208:211], v[140:143], v[24:27], v[208:211]\n\t" \
    "buffer_load_dwordx4 %[l16], %[rs], %[a] offen offset:0 sc1 lds\n\t" \
    "v_mfma_f32_16x16x32_bf16 v[196:199], v[136:139], v[28:31], v[196:199]\n\t" \
    "ds_read_b128 v[20:23], %[xp] offset:57344\n\t" \
    "v_mfma_f32_16x16x32_bf16 v[200:203], v[144:147], v[28:31], v[200:203]\n\t" \
    "ds_read_b128 v[24:27], %[xp] offset:58368\n\t" \
    "v_mfma_f32_16x16x32_bf16 v[204:207], v[136:139], v[32:35], v[204:207]\n\t" \
    "ds_read_b128 v[56:59], %[pa] offset:1024\n\t" \
    "s_waitcnt lgkmcnt(6)\n\t" \
    "v_mfma_f32_16x16x32_bf16 v[208:211], v[144:147], v[32:35], v[208:211]\n\t" \
    "buffer_load_dwordx4 %[l16], %[rs], %[a] offen offset:1024 sc1 lds\n\t" \
    "v_mfma_f32_16x16x32_bf16 v[196:199], v[148:151], v[36:39], v[196:199]\n\t" \
    "ds_read_b128 v[28:31], %[xp] offset:59392\n\t" \
    "v_mfma_f32_16x16x32_bf16 v[200:203], v[156:159], v[36:39], v[200:203]\n\t" \
    "ds_read_b128 v[32:35], %[xp] offset:60416\n\t" \
    "v_mfma_f32_16x16x32_bf16 v[204:207], v[148:151], v[40:43], v[204:207]\n\t" \
    "ds_read_b128 v[60:63], %[pa] offset:2048\n\t" \
    "s_waitcnt lgkmcnt(7)\n\t" \
    "v_mfma_f32_16x16x32_bf16 v[208:211], v[156:159], v[40:43], v[208:211]\n\t" \
    "buffer_load_dwordx4 %[l16], %[rs], %[a] offen offset:2048 sc1 lds\n\t" \
    "v_mfma_f32_16x16x32_bf16 v[196:199], v[152:155], v[44:47], v[196:199]\n\t" \
    "ds_read_b128 v[36:39], %[xp] offset:61440\n\t" \
    "s_waitcnt lgkmcnt(7)\n\t" \
    "v_add_f32 v0, v0, v52\n\t" \
    "v_mfma_f32_16x16x32_bf16 v[200:203], v[160:163], v[44:47], v[200:203]\n\t" \
    "ds_read_b128 v[40:43], %[xp] offset:62464\n\t" \
    "v_add_f32 v1, v1, v53\n\t" \
    "v_mfma_f32_16x16x32_bf16 v[204:207], v[152:155], v[48:51], v[204:207]\n\t" \
    "ds_read_b128 v[64:67], %[pa] offset:3072\n\t" \
    "s_waitcnt lgkmcnt(7)\n\t" \
    "v_mfma_f32_16x16x32_bf16 v[208:211], v[160:163], v[48:51], v[208:211]\n\t" \
    "buffer_load_dwordx4 %[l16], %[rs], %[a] offen offset:3072 sc1 lds\n\t" \
    "v_add_f32 v2, v2, v54\n\t" \
    "v_add_f32 v3, v3, v55\n\t" \
    "v_mfma_f32_16x16x32_bf16 v[196:199], v[164:167], v[20:23], v[196:199]\n\t" \
    "ds_read_b128 v[44:47], %[xp] offset:63488\n\t" \
    "s_waitcnt lgkmcnt(7)\n\t" \
    "v_add_f32 v4, v4, v56\n\t" \
    "v_mfma_f32_16x16x32_bf16 v[200:203], v[172:175], v[20:23], v[200:203]\n\t" \
    "ds_read_b128 v[48:51], %[xp] offset:64512\n\t" \
    "v_add_f32 v5, v5, v57\n\t" \
    "v_mfma_f32_16x16x32_bf16 v[204:207], v[164:167], v[24:27], v[204:207]\n\t" \
    "v_add_f32 v6, v6, v58\n\t" \
    "s_waitcnt lgkmcnt(6)\n\t" \
    "v_mfma_f32_16x16x32_bf16 v[208:211], v[172:175], v[24:27], v[208:211]\n\t" \
    "v_add_f32 v7, v7, v59\n\t" \
    "s_waitcnt lgkmcnt(5)\n\t" \
    "v_add_f32 v8, v8, v60\n\t" \
    "v_add_f32 v9, v9, v61\n\t" \
    "v_mfma_f32_16x16x32_bf16 v[196:199], v[168:171], v[28:31], v[196:199]\n\t" \
    "ds_read_b128 v[20:23], %[xp] offset:0\n\t" \
    "v_add_f32 v10, v10, v62\n\t" \
    "v_mfma_f32_16x16x32_bf16 v[200:203], v[176:179], v[28:31], v[200:203]\n\t" \
    "ds_read_b128 v[24:27], %[xp] offset:1024\n\t" \
    "v_add_f32 v11, v11, v63\n\t" \
    "v_mfma_f32_16x16x32_bf16 v[204:207], v[168:171], v[32:35], v[204:207]\n\t" \
    "s_waitcnt lgkmcnt(4)\n\t" \
    "v_add_f32 v12, v12, v64\n\t" \
    "v_mfma_f32_16x16x32_bf16 v[208:211], v[176:179], v[32:35], v[208:211]\n\t" \
    "v_add_f32 v13, v13, v65\n\t" \
    "v_add_f32 v14, v14, v66\n\t" \
    "v_add_f32 v15, v15, v67\n\t" \
    "v_mfma_f32_16x16x32_bf16 v[196:199], v[180:183], v[36:39], v[196:199]\n\t" \
    "ds_read_b128 v[28:31], %[xp] offset:2048\n\t" \
    "v_cvt_pk_bf16_f32 v52, v0, v4\n\t" \
    "v_mfma_f32_16x16x32_bf16 v[200:203], v[188:191], v[36:39], v[200:203]\n\t" \
    "ds_read_b128 v[32:35], %[xp] offset:3072\n\t" \
    "v_cvt_pk_bf16_f32 v53, v1, v5\n\t" \
    "v_mfma_f32_16x16x32_bf16 v[204:207], v[180:183], v[40:43], v[204:207]\n\t" \
    "v_cvt_pk_bf16_f32 v54, v2, v6\n\t" \
    "s_waitcnt lgkmcnt(4)\n\t" \
    "v_mfma_f32_16x16x32_bf16 v[208:211], v[188:191], v[40:43], v[208:211]\n\t" \
    "v_cvt_pk_bf16_f32 v55, v3, v7\n\t" \
    "v_cvt_pk_bf16_f32 v56, v8, v12\n\t" \
    "v_cvt_pk_bf16_f32 v57, v9, v13\n\t" \
    "v_mfma_f32_16x16x32_bf16 v[196:199], v[184:187], v[44:47], v[196:199]\n\t" \
    "ds_read_b128 v[36:39], %[xp] offset:4096\n\t" \
    "v_cvt_pk_bf16_f32 v58, v10, v14\n\t" \
    "v_mfma_f32_16x16x32_bf16 v[200:203], v[192:195], v[44:47], v[200:203]\n\t" \
    "ds_read_b128 v[40:43], %[xp] offset:5120\n\t" \
    "v_cvt_pk_bf16_f32 v59, v11, v15\n\t" \
    "v_mfma_f32_16x16x32_bf16 v[204:207], v[184:187], v[48:51], v[204:207]\n\t" \
    "s_waitcnt lgkmcnt(4)\n\t" \
    "v_mfma_f32_16x16x32_bf16 v[208:211], v[192:195], v[48:51], v[208:211]\n\t" \
    "s_mov_b64 exec, %[em]\n\t" \
    "buffer_store_dwordx4 v[52:55], %[l16], %[rs], %[oo] offen sc1\n\t" \
    "buffer_store_dwordx4 v[56:59], %[l16], %[rs], %[b] offen sc1\n\t" \
    "s_mov_b64 exec, -1\n\t" \
    "s_add_u32 %[sx], %[sx], 65536\n\t" \
    "s_add_u32 %[t], %[t], 1\n\t" \
    "s_add_u32 %[oo], %[oo], 262144\n\t" \
    "s_waitcnt vmcnt(6)\n\t" \
    "s_cmp_gt_u32 %[t], " STR(SQUICK) " + 1\n\t" \
    "s_cbranch_scc1 B1_%=\n\t" \
    "s_waitcnt vmcnt(0)\n\t" \
    "B1_%=:\n\t" \
    "s_barrier\n\t" \
    "s_cmp_lt_u32 %[t], %[te]\n\t" \
    "s_cbranch_scc1 L1_%=\n\t" \
    "s_cmp_eq_u32 %[fin], %[te]\n\t" \
    "s_cbranch_scc0 E_%=\n\t" \
    "s_nop 4\n\t" \
    "s_barrier\n\t" \
    "ds_read_b128 v[52:55], %[pa] offset:0\n\t" \
    "ds_read_b128 v[56:59], %[pa] offset:1024\n\t" \
    "ds_read_b128 v[60:63], %[pa] offset:2048\n\t" \
    "ds_read_b128 v[64:67], %[pa] offset:3072\n\t" \
    "s_add_u32 %[b], %[oo], 0x1000\n\t" \
    "s_waitcnt lgkmcnt(0)\n\t" \
    "v_add_f32 v196, v196, v52\n\t" \
    "v_add_f32 v197, v197, v53\n\t" \
    "v_add_f32 v198, v198, v54\n\t" \
    "v_add_f32 v199, v199, v55\n\t" \
    "v_add_f32 v200, v200, v56\n\t" \
    "v_add_f32 v201, v201, v57\n\t" \
    "v_add_f32 v202, v202, v58\n\t" \
    "v_add_f32 v203, v203, v59\n\t" \
    "v_add_f32 v204, v204, v60\n\t" \
    "v_add_f32 v205, v205, v61\n\t" \
    "v_add_f32 v206, v206, v62\n\t" \
    "v_add_f32 v207, v207, v63\n\t" \
    "v_add_f32 v208, v208, v64\n\t" \
    "v_add_f32 v209, v209, v65\n\t" \
    "v_add_f32 v210, v210, v66\n\t" \
    "v_add_f32 v211, v211, v67\n\t" \
    "v_cvt_pk_bf16_f32 v52, v196, v200\n\t" \
    "v_cvt_pk_bf16_f32 v53, v197, v201\n\t" \
    "v_cvt_pk_bf16_f32 v54, v198, v202\n\t" \
    "v_cvt_pk_bf16_f32 v55, v199, v203\n\t" \
    "v_cvt_pk_bf16_f32 v56, v204, v208\n\t" \
    "v_cvt_pk_bf16_f32 v57, v205, v209\n\t" \
    "v_cvt_pk_bf16_f32 v58, v206, v210\n\t" \
    "v_cvt_pk_bf16_f32 v59, v207, v211\n\t" \
    "buffer_store_dwordx4 v[52:55], %[l16], %[rs], %[oo] offen sc1\n\t" \
    "buffer_store_dwordx4 v[56:59], %[l16], %[rs], %[b] offen sc1\n\t" \
    "E_%=:\n\t" \
    "s_setprio 0\n\t" \
    "s_waitcnt lgkmcnt(0)\n\t" \
    "s_nop 7\n\t" \
    : [t] "+s"(t), [sx] "+s"(sx), [oo] "+s"(oo), [a] "=&s"(tmp), [b] "=&s"(tmp2), [em] "=&s"(em), "+{v[20:27]}"(xf[0]), "+{v[28:35]}"(xf[1]), "+{v[36:43]}"(xf[2]), "+{v[196:203]}"(ac[0]), "+{v[204:211]}"(ac[1]) \
    : [te] "s"(TE_), [fin] "s"(FIN_), [kh] "s"(kh), [sd] "s"(sd), [rs] "s"(rs), [l16] "v"(l16), [xp] "v"(xp), [pa] "v"(pa), [fm] "s"(fm), [pm] "s"(pm), [fo] "s"(fo), [po] "s"(po), [pd] "s"(pd), [tag] "s"(P.tag), "{v[68:75]}"(w[0][0]), "{v[76:83]}"(w[0][1]), "{v[84:91]}"(w[1][0]), "{v[92:99]}"(w[1][1]), "{v[100:107]}"(w[2][0]), "{v[108:115]}"(w[2][1]), "{v[116:123]}"(w[3][0]), "{v[124:131]}"(w[3][1]), "{v[132:139]}"(w[4][0]), "{v[140:147]}"(w[4][1]), "{v[148:155]}"(w[5][0]), "{v[156:163]}"(w[5][1]), "{v[164:171]}"(w[6][0]), "{v[172:179]}"(w[6][1]), "{v[180:187]}"(w[7][0]), "{v[188:195]}"(w[7][1]) \
    : "memory", "scc", "v0", "v1", "v2", "v3", "v4", "v5", "v6", "v7", "v8", "v9", "v10", "v11", "v12", "v13", "v14", "v15", "v44", "v45", "v46", "v47", "v48", "v49", "v50", "v51", "v52", "v53", "v54", "v55", "v56", "v57", "v58", "v59", "v60", "v61", "v62", "v63", "v64", "v65", "v66", "v67")

#define S_LOOP_V(TE_, FIN_) \
  asm volatile( \
    "s_cmp_eq_u32 %[kh], 0\n\t" \
    "s_cbranch_scc0 L1_%=\n\t" \
    "L0_%=:\n\t" \
    "s_setprio 0\n\t" \
    "s_nop 3\n\t" \
    "ds_write_b128 %[pa], v[196:199] offset:0\n\t" \
    "ds_write_b128 %[pa], v[200:203] offset:1024\n\t" \
    "ds_write_b128 %[pa], v[204:207] offset:2048\n\t" \
    "ds_write_b128 %[pa], v[208:211] offset:3072\n\t" \
    "s_sub_u32 %[a], %[t], 1\n\t" \
    "v_mov_b32 v53, %[tag]\n\t" \
    "v_mov_b32 v52, %[a]\n\t" \
    "s_sub_u32 %[b], %[t], 2\n\t" \
    "s_cmp_lt_u32 %[b], " STR(SQUICK) "\n\t" \
    "s_cselect_b64 exec, %[fm], 0\n\t" \
    "buffer_store_dwordx2 v[52:53], %[l16], %[rs], %[fo] offen sc1\n\t" \
    "s_mov_b64 exec, -1\n\t" \
    "s_add_u32 m0, %[sd], 49152\n\t" \
    "s_add_u32 %[a], %[sx], 81920\n\t" \
    "s_waitcnt lgkmcnt(0)\n\t" \
    "buffer_load_dwordx4 %[l16], %[rs], %[a] offen offset:0 sc1 lds\n\t" \
    "buffer_load_dwordx4 %[l16], %[rs], %[a] offen offset:1024 sc1 lds\n\t" \
    "buffer_load_dwordx4 %[l16], %[rs], %[a] offen offset:2048 sc1 lds\n\t" \
    "buffer_load_dwordx4 %[l16], %[rs], %[a] offen offset:3072 sc1 lds\n\t" \
    "v_mfma_f32_16x16x32_bf16 v[0:3], v[20:23], v[68:71], 0\n\t" \
    "ds_read_b128 v[44:47], %[xp] offset:6144\n\t" \
    "v_mfma_f32_16x16x32_bf16 v[4:7], v[20:23], v[76:79], 0\n\t" \
    "ds_read_b128 v[48:51], %[xp] offset:7168\n\t" \
    "v_mfma_f32_16x16x32_bf16 v[8:11], v[24:27], v[68:71], 0\n\t" \
    "v_mfma_f32_16x16x32_bf16 v[12:15], v[24:27], v[76:79], 0\n\t" \
    "v_mfma_f32_16x16x32_bf16 v[0:3], v[28:31], v[72:75], v[0:3]\n\t" \
    "ds_read_b128 v[20:23], %[xp] offset:8192\n\t" \
    "v_mfma_f32_16x16x32_bf16 v[4:7], v[28:31], v[80:83], v[4:7]\n\t" \
    "ds_read_b128 v[24:27], %[xp] offset:9216\n\t" \
    "v_mfma_f32_16x16x32_bf16 v[8:11], v[32:35], v[72:75], v[8:11]\n\t" \
    "v_mfma_f32_16x16x32_bf16 v[12:15], v[32:35], v[80:83], v[12:15]\n\t" \
    "v_mfma_f32_16x16x32_bf16 v[0:3], v[36:39], v[84:87], v[0:3]\n\t" \
    "ds_read_b128 v[28:31], %[xp] offset:10240\n\t" \
    "v_mfma_f32_16x16x32_bf16 v[4:7], v[36:39], v[92:95], v[4:7]\n\t" \
    "ds_read_b128 v[32:35], %[xp] offset:11264\n\t" \
    "v_mfma_f32_16x16x32_bf16 v[8:11], v[40:43], v[84:87], v[8:11]\n\t" \
    "s_waitcnt lgkmcnt(4)\n\t" \
    "v_mfma_f32_16x16x32_bf16 v[12:15], v[40:43], v[92:95], v[12:15]\n\t" \
    "v_mfma_f32_16x16x32_bf16 v[0:3], v[44:47], v[88:91], v[0:3]\n\t" \
    "ds_read_b128 v[36:39], %[xp] offset:12288\n\t" \
    "v_mfma_f32_16x16x32_bf16 v[4:7], v[44:47], v[96:99], v[4:7]\n\t" \
    "ds_read_b128 v[40:43], %[xp] offset:13312\n\t" \
    "v_mfma_f32_16x16x32_bf16 v[8:11], v[48:51], v[88:91], v[8:11]\n\t" \
    "s_waitcnt lgkmcnt(4)\n\t" \
    "v_mfma_f32_16x16x32_bf16 v[12:15], v[48:51], v[96:99], v[12:15]\n\t" \
    "v_mfma_f32_16x16x32_bf16 v[0:3], v[20:23], v[100:103], v[0:3]\n\t" \
    "ds_read_b128 v[44:47], %[xp] offset:14336\n\t" \
    "v_mfma_f32_16x16x32_bf16 v[4:7], v[20:23], v[108:111], v[4:7]\n\t" \
    "ds_read_b128 v[48:51], %[xp] offset:15360\n\t" \
    "v_mfma_f32_16x16x32_bf16 v[8:11], v[24:27], v[100:103], v[8:11]\n\t" \
    "s_waitcnt lgkmcnt(4)\n\t" \
    "v_mfma_f32_16x16x32_bf16 v[12:15], v[24:27], v[108:111], v[12:15]\n\t" \
    "v_mfma_f32_16x16x32_bf16 v[0:3], v[28:31], v[104:107], v[0:3]\n\t" \
    "ds_read_b128 v[20:23], %[xp] offset:16384\n\t" \
    "v_mfma_f32_16x16x32_bf16 v[4:7], v[28:31], v[112:115], v[4:7]\n\t" \
    "ds_read_b128 v[24:27], %[xp] offset:17408\n\t" \
    "v_mfma_f32_16x16x32_bf16 v[8:11], v[32:35], v[104:107], v[8:11]\n\t" \
    "s_waitcnt lgkmcnt(4)\n\t" \
    "v_mfma_f32_16x16x32_bf16 v[12:15], v[32:35], v[112:115], v[12:15]\n\t" \
    "v_mfma_f32_16x16x32_bf16 v[0:3], v[36:39], v[116:119], v[0:3]\n\t" \
    "ds_read_b128 v[28:31], %[xp] offset:18432\n\t" \
    "v_mfma_f32_16x16x32_bf16 v[4:7], v[36:39], v[124:127], v[4:7]\n\t" \
    "ds_read_b128 v[32:35], %[xp] offset:19456\n\t" \
    "v_mfma_f32_16x16x32_bf16 v[8:11], v[40:43], v[116:119], v[8:11]\n\t" \
    "s_waitcnt lgkmcnt(4)\n\t" \
    "v_mfma_f32_16x16x32_bf16 v[12:15], v[40:43], v[124:127], v[12:15]\n\t" \
    "v_mfma_f32_16x16x32_bf16 v[0:3], v[44:47], v[120:123], v[0:3]\n\t" \
    "ds_read_b128 v[36:39], %[xp] offset:20480\n\t" \
    "v_mfma_f32_16x16x32_bf16 v[4:7], v[44:47], v[128:131], v[4:7]\n\t" \
    "ds_read_b128 v[40:43], %[xp] offset:21504\n\t" \
    "v_mfma_f32_16x16x32_bf16 v[8:11], v[48:51], v[120:123], v[8:11]\n\t" \
    "s_waitcnt lgkmcnt(4)\n\t" \
    "v_mfma_f32_16x16x32_bf16 v[12:15], v[48:51], v[128:131], v[12:15]\n\t" \
    "s_waitcnt vmcnt(4)\n\t" \
    "s_barrier\n\t" \
    "s_setprio 0\n\t" \
    "s_sub_u32 %[a], %[t], 1\n\t" \
    "v_mov_b32 v53, %[tag]\n\t" \
    "v_mov_b32 v52, %[a]\n\t" \
    "s_cmp_gt_u32 %[t], 1\n\t" \
    "s_cselect_b64 exec, %[fm], 0\n\t" \
    "buffer_store_dwordx2 v[52:53], %[l16], %[rs], %[fo] offen sc1\n\t" \
    "s_mov_b64 exec, %[pm]\n\t" \
    "s_mov_b32 m0, %[pd]\n\t" \
    "s_add_u32 %[a], %[sx], %[po]\n\t" \
    "buffer_load_dwordx4 %[l16], %[rs], %[a] offen sc1 lds\n\t" \
    "s_mov_b64 exec, -1\n\t" \
    "s_add_u32 m0, %[sd], 0\n\t" \
    "s_add_u32 %[a], %[sx], 131072\n\t" \
    "buffer_load_dwordx4 %[l16], %[rs], %[a] offen offset:0 sc1 lds\n\t" \
    "buffer_load_dwordx4 %[l16], %[rs], %[a] offen offset:1024 sc1 lds\n\t" \
    "buffer_load_dwordx4 %[l16], %[rs], %[a] offen offset:2048 sc1 lds\n\t" \
    "buffer_load_dwordx4 %[l16], %[rs], %[a] offen offset:3072 sc1 lds\n\t" \
    "v_mfma_f32_16x16x32_bf16 v[0:3], v[20:23], v[132:135], v[0:3]\n\t" \
    "ds_read_b128 v[44:47], %[xp] offset:22528\n\t" \
    "v_mfma_f32_16x16x32_bf16 v[4:7], v[20:23], v[140:143], v[4:7]\n\t" \
    "ds_read_b128 v[48:51], %[xp] offset:23552\n\t" \
    "v_mfma_f32_16x16x32_bf16 v[8:11], v[24:27], v[132:135], v[8:11]\n\t" \
    "s_waitcnt lgkmcnt(4)\n\t" \
    "v_mfma_f32_16x16x32_bf16 v[12:15], v[24:27], v[140:143], v[12:15]\n\t" \
    "v_mfma_f32_16x16x32_bf16 v[0:3], v[28:31], v[136:139], v[0:3]\n\t" \
    "ds_read_b128 v[20:23], %[xp] offset:24576\n\t" \
    "v_mfma_f32_16x16x32_bf16 v[4:7], v[28:31], v[144:147], v[4:7]\n\t" \
    "ds_read_b128 v[24:27], %[xp] offset:25600\n\t" \
    "v_mfma_f32_16x16x32_bf16 v[8:11], v[32:35], v[136:139], v[8:11]\n\t" \
    "s_waitcnt lgkmcnt(4)\n\t" \
    "v_mfma_f32_16x16x32_bf16 v[12:15], v[32:35], v[144:147], v[12:15]\n\t" \
    "v_mfma_f32_16x16x32_bf16 v[0:3], v[36:39], v[148:151], v[0:3]\n\t" \
    "ds_read_b128 v[28:31], %[xp] offset:26624\n\t" \
    "v_mfma_f32_16x16x32_bf16 v[4:7], v[36:39], v[156:159], v[4:7]\n\t" \
    "ds_read_b128 v[32:35], %[xp] offset:27648\n\t" \
    "v_mfma_f32_16x16x32_bf16 v[8:11], v[40:43], v[148:151], v[8:11]\n\t" \
    "s_waitcnt lgkmcnt(4)\n\t" \
    "v_mfma_f32_16x16x32_bf16 v[12:15], v[40:43], v[156:159], v[12:15]\n\t" \
    "v_mfma_f32_16x16x32_bf16 v[0:3], v[44:47], v[152:155], v[0:3]\n\t" \
    "ds_read_b128 v[36:39], %[xp] offset:28672\n\t" \
    "v_mfma_f32_16x16x32_bf16 v[4:7], v[44:47], v[160:163], v[4:7]\n\t" \
    "ds_read_b128 v[40:43], %[xp] offset:29696\n\t" \
    "v_mfma_f32_16x16x32_bf16 v[8:11], v[48:51], v[152:155], v[8:11]\n\t" \
    "s_waitcnt lgkmcnt(4)\n\t" \
    "v_mfma_f32_16x16x32_bf16 v[12:15], v[48:51], v[160:163], v[12:15]\n\t" \
    "v_mfma_f32_16x16x32_bf16 v[0:3], v[20:23], v[164:167], v[0:3]\n\t" \
    "ds_read_b128 v[44:47], %[xp] offset:30720\n\t" \
    "v_mfma_f32_16x16x32_bf16 v[4:7], v[20:23], v[172:175], v[4:7]\n\t" \
    "ds_read_b128 v[48:51], %[xp] offset:31744\n\t" \
    "v_mfma_f32_16x16x32_bf16 v[8:11], v[24:27], v[164:167], v[8:11]\n\t" \
    "s_waitcnt lgkmcnt(4)\n\t" \
    "v_mfma_f32_16x16x32_bf16 v[12:15], v[24:27], v[172:175], v[12:15]\n\t" \
    "v_mfma_f32_16x16x32_bf16 v[0:3], v[28:31], v[168:171], v[0:3]\n\t" \
    "ds_read_b128 v[20:23], %[xp] offset:32768\n\t" \
    "v_mfma_f32_16x16x32_bf16 v[4:7], v[28:31], v[176:179], v[4:7]\n\t" \
    "ds_read_b128 v[24:27], %[xp] offset:33792\n\t" \
    "v_mfma_f32_16x16x32_bf16 v[8:11], v[32:35], v[168:171], v[8:11]\n\t" \
    "s_waitcnt lgkmcnt(4)\n\t" \
    "v_mfma_f32_16x16x32_bf16 v[12:15], v[32:35], v[176:179], v[12:15]\n\t" \
    "v_mfma_f32_16x16x32_bf16 v[0:3], v[36:39], v[180:183], v[0:3]\n\t" \
    "ds_read_b128 v[28:31], %[xp] offset:34816\n\t" \
    "v_mfma_f32_16x16x32_bf16 v[4:7], v[36:39], v[188:191], v[4:7]\n\t" \
    "ds_read_b128 v[32:35], %[xp] offset:35840\n\t" \
    "v_mfma_f32_16x16x32_bf16 v[8:11], v[40:43], v[180:183], v[8:11]\n\t" \
    "s_waitcnt lgkmcnt(4)\n\t" \
    "v_mfma_f32_16x16x32_bf16 v[12:15], v[40:43], v[188:191], v[12:15]\n\t" \
    "v_mfma_f32_16x16x32_bf16 v[0:3], v[44:47], v[184:187], v[0:3]\n\t" \
    "ds_read_b128 v[36:39], %[xp] offset:36864\n\t" \
    "v_mfma_f32_16x16x32_bf16 v[4:7], v[44:47], v[192:195], v[4:7]\n\t" \
    "ds_read_b128 v[40:43], %[xp] offset:37888\n\t" \
    "v_mfma_f32_16x16x32_bf16 v[8:11], v[48:51], v[184:187], v[8:11]\n\t" \
    "s_waitcnt lgkmcnt(4)\n\t" \
    "v_mfma_f32_16x16x32_bf16 v[12:15], v[48:51], v[192:195], v[12:15]\n\t" \
    "s_add_u32 %[sx], %[sx], 65536\n\t" \
    "s_add_u32 %[t], %[t], 1\n\t" \
    "s_waitcnt vmcnt(4)\n\t" \
    "s_barrier\n\t" \
    "s_setprio 0\n\t" \
    "s_nop 3\n\t" \
    "ds_write_b128 %[pa], v[0:3] offset:0\n\t" \
    "ds_write_b128 %[pa], v[4:7] offset:1024\n\t" \
    "ds_write_b128 %[pa], v[8:11] offset:2048\n\t" \
    "ds_write_b128 %[pa], v[12:15] offset:3072\n\t" \
    "s_sub_u32 %[a], %[t], 1\n\t" \
    "v_mov_b32 v53, %[tag]\n\t" \
    "v_mov_b32 v52, %[a]\n\t" \
    "s_sub_u32 %[b], %[t], 2\n\t" \
    "s_cmp_lt_u32 %[b], " STR(SQUICK) "\n\t" \
    "s_cselect_b64 exec, %[fm], 0\n\t" \
    "buffer_store_dwordx2 v[52:53], %[l16], %[rs], %[fo] offen sc1\n\t" \
    "s_mov_b64 exec, -1\n\t" \
    "s_add_u32 m0, %[sd], 16384\n\t" \
    "s_add_u32 %[a], %[sx], 81920\n\t" \
    "s_waitcnt lgkmcnt(0)\n\t" \
    "buffer_load_dwordx4 %[l16], %[rs], %[a] offen offset:0 sc1 lds\n\t" \
    "buffer_load_dwordx4 %[l16], %[rs], %[a] offen offset:1024 sc1 lds\n\t" \
    "buffer_load_dwordx4 %[l16], %[rs], %[a] offen offset:2048 sc1 lds\n\t" \
    "buffer_load_dwordx4 %[l16], %[rs], %[a] offen offset:3072 sc1 lds\n\t" \
    "v_mfma_f32_16x16x32_bf16 v[196:199], v[20:23], v[68:71], 0\n\t" \
    "ds_read_b128 v[44:47], %[xp] offset:38912\n\t" \
    "v_mfma_f32_16x16x32_bf16 v[200:203], v[20:23], v[76:79], 0\n\t" \
    "ds_read_b128 v[48:51], %[xp] offset:39936\n\t" \
    "v_mfma_f32_16x16x32_bf16 v[204:207], v[24:27], v[68:71], 0\n\t" \
    "v_mfma_f32_16x16x32_bf16 v[208:211], v[24:27], v[76:79], 0\n\t" \
    "v_mfma_f32_16x16x32_bf16 v[196:199], v[28:31], v[72:75], v[196:199]\n\t" \
    "ds_read_b128 v[20:23], %[xp] offset:40960\n\t" \
    "v_mfma_f32_16x16x32_bf16 v[200:203], v[28:31], v[80:83], v[200:203]\n\t" \
    "ds_read_b128 v[24:27], %[xp] offset:41984\n\t" \
    "v_mfma_f32_16x16x32_bf16 v[204:207], v[32:35], v[72:75], v[204:207]\n\t" \
    "v_mfma_f32_16x16x32_bf16 v[208:211], v[32:35], v[80:83], v[208:211]\n\t" \
    "v_mfma_f32_16x16x32_bf16 v[196:199], v[36:39], v[84:87], v[196:199]\n\t" \
    "ds_read_b128 v[28:31], %[xp] offset:43008\n\t" \
    "v_mfma_f32_16x16x32_bf16 v[200:203], v[36:39], v[92:95], v[200:203]\n\t" \
    "ds_read_b128 v[32:35], %[xp] offset:44032\n\t" \
    "v_mfma_f32_16x16x32_bf16 v[204:207], v[40:43], v[84:87], v[204:207]\n\t" \
    "s_waitcnt lgkmcnt(4)\n\t" \
    "v_mfma_f32_16x16x32_bf16 v[208:211], v[40:43], v[92:95], v[208:211]\n\t" \
    "v_mfma_f32_16x16x32_bf16 v[196:199], v[44:47], v[88:91], v[196:199]\n\t" \
    "ds_read_b128 v[36:39], %[xp] offset:45056\n\t" \
    "v_mfma_f32_16x16x32_bf16 v[200:203], v[44:47], v[96:99], v[200:203]\n\t" \
    "ds_read_b128 v[40:43], %[xp] offset:46080\n\t" \
    "v_mfma_f32_16x16x32_bf16 v[204:207], v[48:51], v[88:91], v[204:207]\n\t" \
    "s_waitcnt lgkmcnt(4)\n\t" \
    "v_mfma_f32_16x16x32_bf16 v[208:211], v[48:51], v[96:99], v[208:211]\n\t" \
    "v_mfma_f32_16x16x32_bf16 v[196:199], v[20:23], v[100:103], v[196:199]\n\t" \
    "ds_read_b128 v[44:47], %[xp] offset:47104\n\t" \
    "v_mfma_f32_16x16x32_bf16 v[200:203], v[20:23], v[108:111], v[200:203]\n\t" \
    "ds_read_b128 v[48:51], %[xp] offset:48128\n\t" \
    "v_mfma_f32_16x16x32_bf16 v[204:207], v[24:27], v[100:103], v[204:207]\n\t" \
    "s_waitcnt lgkmcnt(4)\n\t" \
    "v_mfma_f32_16x16x32_bf16 v[208:211], v[24:27], v[108:111], v[208:211]\n\t" \
    "v_mfma_f32_16x16x32_bf16 v[196:199], v[28:31], v[104:107], v[196:199]\n\t" \
    "ds_read_b128 v[20:23], %[xp] offset:49152\n\t" \
    "v_mfma_f32_16x16x32_bf16 v[200:203], v[28:31], v[112:115], v[200:203]\n\t" \
    "ds_read_b128 v[24:27], %[xp] offset:50176\n\t" \
    "v_mfma_f32_16x16x32_bf16 v[204:207], v[32:35], v[104:107], v[204:207]\n\t" \
    "s_waitcnt lgkmcnt(4)\n\t" \
    "v_mfma_f32_16x16x32_bf16 v[208:211], v[32:35], v[112:115], v[208:211]\n\t" \
    "v_mfma_f32_16x16x32_bf16 v[196:199], v[36:39], v[116:119], v[196:199]\n\t" \
    "ds_read_b128 v[28:31], %[xp] offset:51200\n\t" \
    "v_mfma_f32_16x16x32_bf16 v[200:203], v[36:39], v[124:127], v[200:203]\n\t" \
    "ds_read_b128 v[32:35], %[xp] offset:52224\n\t" \
    "v_mfma_f32_16x16x32_bf16 v[204:207], v[40:43], v[116:119], v[204:207]\n\t" \
    "s_waitcnt lgkmcnt(4)\n\t" \
    "v_mfma_f32_16x16x32_bf16 v[208:211], v[40:43], v[124:127], v[208:211]\n\t" \
    "v_mfma_f32_16x16x32_bf16 v[196:199], v[44:47], v[120:123], v[196:199]\n\t" \
    "ds_read_b128 v[36:39], %[xp] offset:53248\n\t" \
    "v_mfma_f32_16x16x32_bf16 v[200:203], v[44:47], v[128:131], v[200:203]\n\t" \
    "ds_read_b128 v[40:43], %[xp] offset:54272\n\t" \
    "v_mfma_f32_16x16x32_bf16 v[204:207], v[48:51], v[120:123], v[204:207]\n\t" \
    "s_waitcnt lgkmcnt(4)\n\t" \
    "v_mfma_f32_16x16x32_bf16 v[208:211], v[48:51], v[128:131], v[208:211]\n\t" \
    "s_waitcnt vmcnt(4)\n\t" \
    "s_barrier\n\t" \
    "s_setprio 0\n\t" \
    "s_sub_u32 %[a], %[t], 1\n\t" \
    "v_mov_b32 v53, %[tag]\n\t" \
    "v_mov_b32 v52, %[a]\n\t" \
    "s_cmp_gt_u32 %[t], 1\n\t" \
    "s_cselect_b64 exec, %[fm], 0\n\t" \
    "buffer_store_dwordx2 v[52:53], %[l16], %[rs], %[fo] offen sc1\n\t" \
    "s_mov_b64 exec, %[pm]\n\t" \
    "s_mov_b32 m0, %[pd]\n\t" \
    "s_add_u32 %[a], %[sx], %[po]\n\t" \
    "buffer_load_dwordx4 %[l16], %[rs], %[a] offen sc1 lds\n\t" \
    "s_mov_b64 exec, -1\n\t" \
    "s_add_u32 m0, %[sd], 32768\n\t" \
    "s_add_u32 %[a], %[sx], 131072\n\t" \
    "buffer_load_dwordx4 %[l16], %[rs], %[a] offen offset:0 sc1 lds\n\t" \
    "buffer_load_dwordx4 %[l16], %[rs], %[a] offen offset:1024 sc1 lds\n\t" \
    "buffer_load_dwordx4 %[l16], %[rs], %[a] offen offset:2048 sc1 lds\n\t" \
    "buffer_load_dwordx4 %[l16], %[rs], %[a] offen offset:3072 sc1 lds\n\t" \
    "v_mfma_f32_16x16x32_bf16 v[196:199], v[20:23], v[132:135], v[196:199]\n\t" \
    "ds_read_b128 v[44:47], %[xp] offset:55296\n\t" \
    "v_mfma_f32_16x16x32_bf16 v[200:203], v[20:23], v[140:143], v[200:203]\n\t" \
    "ds_read_b128 v[48:51], %[xp] offset:56320\n\t" \
    "v_mfma_f32_16x16x32_bf16 v[204:207], v[24:27], v[132:135], v[204:207]\n\t" \
    "s_waitcnt lgkmcnt(4)\n\t" \
    "v_mfma_f32_16x16x32_bf16 v[208:211], v[24:27], v[140:143], v[208:211]\n\t" \
    "v_mfma_f32_16x16x32_bf16 v[196:199], v[28:31], v[136:139], v[196:199]\n\t" \
    "ds_read_b128 v[20:23], %[xp] offset:57344\n\t" \
    "v_mfma_f32_16x16x32_bf16 v[200:203], v[28:31], v[144:147], v[200:203]\n\t" \
    "ds_read_b128 v[24:27], %[xp] offset:58368\n\t" \
    "v_mfma_f32_16x16x32_bf16 v[204:207], v[32:35], v[136:139], v[204:207]\n\t" \
    "s_waitcnt lgkmcnt(4)\n\t" \
    "v_mfma_f32_16x16x32_bf16 v[208:211], v[32:35], v[144:147], v[208:211]\n\t" \
    "v_mfma_f32_16x16x32_bf16 v[196:199], v[36:39], v[148:151], v[196:199]\n\t" \
    "ds_read_b128 v[28:31], %[xp] offset:59392\n\t" \
    "v_mfma_f32_16x16x32_bf16 v[200:203], v[36:39], v[156:159], v[200:203]\n\t" \
    "ds_read_b128 v[32:35], %[xp] offset:60416\n\t" \
    "v_mfma_f32_16x16x32_bf16 v[204:207], v[40:43], v[148:151], v[204:207]\n\t" \
    "s_waitcnt lgkmcnt(4)\n\t" \
    "v_mfma_f32_16x16x32_bf16 v[208:211], v[40:43], v[156:159], v[208:211]\n\t" \
    "v_mfma_f32_16x16x32_bf16 v[196:199], v[44:47], v[152:155], v[196:199]\n\t" \
    "ds_read_b128 v[36:39], %[xp] offset:61440\n\t" \
    "v_mfma_f32_16x16x32_bf16 v[200:203], v[44:47], v[160:163], v[200:203]\n\t" \
    "ds_read_b128 v[40:43], %[xp] offset:62464\n\t" \
    "v_mfma_f32_16x16x32_bf16 v[204:207], v[48:51], v[152:155], v[204:207]\n\t" \
    "s_waitcnt lgkmcnt(4)\n\t" \
    "v_mfma_f32_16x16x32_bf16 v[208:211], v[48:51], v[160:163], v[208:211]\n\t" \
    "v_mfma_f32_16x16x32_bf16 v[196:199], v[20:23], v[164:167], v[196:199]\n\t" \
    "ds_read_b128 v[44:47], %[xp] offset:63488\n\t" \
    "v_mfma_f32_16x16x32_bf16 v[200:203], v[20:23], v[172:175], v[200:203]\n\t" \
    "ds_read_b128 v[48:51], %[xp] offset:64512\n\t" \
    "v_mfma_f32_16x16x32_bf16 v[204:207], v[24:27], v[164:167], v[204:207]\n\t" \
    "s_waitcnt lgkmcnt(4)\n\t" \
    "v_mfma_f32_16x16x32_bf16 v[208:211], v[24:27], v[172:175], v[208:211]\n\t" \
    "v_mfma_f32_16x16x32_bf16 v[196:199], v[28:31], v[168:171], v[196:199]\n\t" \
    "ds_read_b128 v[20:23], %[xp] offset:0\n\t" \
    "v_mfma_f32_16x16x32_bf16 v[200:203], v[28:31], v[176:179], v[200:203]\n\t" \
    "ds_read_b128 v[24:27], %[xp] offset:1024\n\t" \
    "v_mfma_f32_16x16x32_bf16 v[204:207], v[32:35], v[168:171], v[204:207]\n\t" \
    "s_waitcnt lgkmcnt(4)\n\t" \
    "v_mfma_f32_16x16x32_bf16 v[208:211], v[32:35], v[176:179], v[208:211]\n\t" \
    "v_mfma_f32_16x16x32_bf16 v[196:199], v[36:39], v[180:183], v[196:199]\n\t" \
    "ds_read_b128 v[28:31], %[xp] offset:2048\n\t" \
    "v_mfma_f32_16x16x32_bf16 v[200:203], v[36:39], v[188:191], v[200:203]\n\t" \
    "ds_read_b128 v[32:35], %[xp] offset:3072\n\t" \
    "v_mfma_f32_16x16x32_bf16 v[204:207], v[40:43], v[180:183], v[204:207]\n\t" \
    "s_waitcnt lgkmcnt(4)\n\t" \
    "v_mfma_f32_16x16x32_bf16 v[208:211], v[40:43], v[188:191], v[208:211]\n\t" \
    "v_mfma_f32_16x16x32_bf16 v[196:199], v[44:47], v[184:187], v[196:199]\n\t" \
    "ds_read_b128 v[36:39], %[xp] offset:4096\n\t" \
    "v_mfma_f32_16x16x32_bf16 v[200:203], v[44:47], v[192:195], v[200:203]\n\t" \
    "ds_read_b128 v[40:43], %[xp] offset:5120\n\t" \
    "v_mfma_f32_16x16x32_bf16 v[204:207], v[48:51], v[184:187], v[204:207]\n\t" \
    "s_waitcnt lgkmcnt(4)\n\t" \
    "v_mfma_f32_16x16x32_bf16 v[208:211], v[48:51], v[192:195], v[208:211]\n\t" \
    "s_add_u32 %[sx], %[sx], 65536\n\t" \
    "s_add_u32 %[t], %[t], 1\n\t" \
    "s_waitcnt vmcnt(4)\n\t" \
    "s_barrier\n\t" \
    "s_cmp_lt_u32 %[t], %[te]\n\t" \
    "s_cbranch_scc1 L0_%=\n\t" \
    "s_cmp_eq_u32 %[fin], %[te]\n\t" \
    "s_cbranch_scc0 E_%=\n\t" \
    "s_nop 4\n\t" \
    "ds_write_b128 %[pa], v[196:199] offset:0\n\t" \
    "ds_write_b128 %[pa], v[200:203] offset:1024\n\t" \
    "ds_write_b128 %[pa], v[204:207] offset:2048\n\t" \
    "ds_write_b128 %[pa], v[208:211] offset:3072\n\t" \
    "s_waitcnt lgkmcnt(0)\n\t" \
    "s_barrier\n\t" \
    "s_branch E_%=\n\t" \
    "L1_%=:\n\t" \
    "s_setprio 1\n\t" \
    "s_add_u32 m0, %[sd], 49152\n\t" \
    "s_add_u32 %[a], %[sx], 81920\n\t" \
    "v_mfma_f32_16x16x32_bf16 v[0:3], v[20:23], v[68:71], 0\n\t" \
    "ds_read_b128 v[44:47], %[xp] offset:6144\n\t" \
    "v_mfma_f32_16x16x32_bf16 v[4:7], v[20:23], v[76:79], 0\n\t" \
    "ds_read_b128 v[48:51], %[xp] offset:7168\n\t" \
    "v_mfma_f32_16x16x32_bf16 v[8:11], v[24:27], v[68:71], 0\n\t" \
    "s_waitcnt lgkmcnt(4)\n\t" \
    "v_mfma_f32_16x16x32_bf16 v[12:15], v[24:27], v[76:79], 0\n\t" \
    "v_mfma_f32_16x16x32_bf16 v[0:3], v[28:31], v[72:75], v[0:3]\n\t" \
    "ds_read_b128 v[20:23], %[xp] offset:8192\n\t" \
    "v_mfma_f32_16x16x32_bf16 v[4:7], v[28:31], v[80:83], v[4:7]\n\t" \
    "ds_read_b128 v[24:27], %[xp] offset:9216\n\t" \
    "v_mfma_f32_16x16x32_bf16 v[8:11], v[32:35], v[72:75], v[8:11]\n\t" \
    "s_waitcnt lgkmcnt(4)\n\t" \
    "v_mfma_f32_16x16x32_bf16 v[12:15], v[32:35], v[80:83], v[12:15]\n\t" \
    "v_mfma_f32_16x16x32_bf16 v[0:3], v[36:39], v[84:87], v[0:3]\n\t" \
    "ds_read_b128 v[28:31], %[xp] offset:10240\n\t" \
    "v_mfma_f32_16x16x32_bf16 v[4:7], v[36:39], v[92:95], v[4:7]\n\t" \
    "ds_read_b128 v[32:35], %[xp] offset:11264\n\t" \
    "v_mfma_f32_16x16x32_bf16 v[8:11], v[40:43], v[84:87], v[8:11]\n\t" \
    "s_waitcnt lgkmcnt(4)\n\t" \
    "v_mfma_f32_16x16x32_bf16 v[12:15], v[40:43], v[92:95], v[12:15]\n\t" \
    "v_mfma_f32_16x16x32_bf16 v[0:3], v[44:47], v[88:91], v[0:3]\n\t" \
    "ds_read_b128 v[36:39], %[xp] offset:12288\n\t" \
    "v_mfma_f32_16x16x32_bf16 v[4:7], v[44:47], v[96:99], v[4:7]\n\t" \
    "ds_read_b128 v[40:43], %[xp] offset:13312\n\t" \
    "v_mfma_f32_16x16x32_bf16 v[8:11], v[48:51], v[88:91], v[8:11]\n\t" \
    "s_waitcnt lgkmcnt(4)\n\t" \
    "v_mfma_f32_16x16x32_bf16 v[12:15], v[48:51], v[96:99], v[12:15]\n\t" \
    "v_mfma_f32_16x16x32_bf16 v[0:3], v[20:23], v[100:103], v[0:3]\n\t" \
    "ds_read_b128 v[44:47], %[xp] offset:14336\n\t" \
    "v_mfma_f32_16x16x32_bf16 v[4:7], v[20:23], v[108:111], v[4:7]\n\t" \
    "ds_read_b128 v[48:51], %[xp] offset:15360\n\t" \
    "v_mfma_f32_16x16x32_bf16 v[8:11], v[24:27], v[100:103], v[8:11]\n\t" \
    "s_waitcnt lgkmcnt(4)\n\t" \
    "v_mfma_f32_16x16x32_bf16 v[12:15], v[24:27], v[108:111], v[12:15]\n\t" \
    "buffer_load_dwordx4 %[l16], %[rs], %[a] offen offset:0 sc1 lds\n\t" \
    "v_mfma_f32_16x16x32_bf16 v[0:3], v[28:31], v[104:107], v[0:3]\n\t" \
    "ds_read_b128 v[20:23], %[xp] offset:16384\n\t" \
    "v_mfma_f32_16x16x32_bf16 v[4:7], v[28:31], v[112:115], v[4:7]\n\t" \
    "ds_read_b128 v[24:27], %[xp] offset:17408\n\t" \
    "v_mfma_f32_16x16x32_bf16 v[8:11], v[32:35], v[104:107], v[8:11]\n\t" \
    "s_waitcnt lgkmcnt(4)\n\t" \
    "v_mfma_f32_16x16x32_bf16 v[12:15], v[32:35], v[112:115], v[12:15]\n\t" \
    "buffer_load_dwordx4 %[l16], %[rs], %[a] offen offset:1024 sc1 lds\n\t" \
    "v_mfma_f32_16x16x32_bf16 v[0:3], v[36:39], v[116:119], v[0:3]\n\t" \
    "ds_read_b128 v[28:31], %[xp] offset:18432\n\t" \
    "v_mfma_f32_16x16x32_bf16 v[4:7], v[36:39], v[124:127], v[4:7]\n\t" \
    "ds_read_b128 v[32:35], %[xp] offset:19456\n\t" \
    "v_mfma_f32_16x16x32_bf16 v[8:11], v[40:43], v[116:119], v[8:11]\n\t" \
    "s_waitcnt lgkmcnt(4)\n\t" \
    "v_mfma_f32_16x16x32_bf16 v[12:15], v[40:43], v[124:127], v[12:15]\n\t" \
    "buffer_load_dwordx4 %[l16], %[rs], %[a] offen offset:2048 sc1 lds\n\t" \
    "v_mfma_f32_16x16x32_bf16 v[0:3], v[44:47], v[120:123], v[0:3]\n\t" \
    "ds_read_b128 v[36:39], %[xp] offset:20480\n\t" \
    "v_mfma_f32_16x16x32_bf16 v[4:7], v[44:47], v[128:131], v[4:7]\n\t" \
    "ds_read_b128 v[40:43], %[xp] offset:21504\n\t" \
    "v_mfma_f32_16x16x32_bf16 v[8:11], v[48:51], v[120:123], v[8:11]\n\t" \
    "s_waitcnt lgkmcnt(4)\n\t" \
    "v_mfma_f32_16x16x32_bf16 v[12:15], v[48:51], v[128:131], v[12:15]\n\t" \
    "buffer_load_dwordx4 %[l16], %[rs], %[a] offen offset:3072 sc1 lds\n\t" \
    "s_waitcnt vmcnt(4)\n\t" \
    "s_barrier\n\t" \
    "s_setprio 1\n\t" \
    "s_add_u32 m0, %[sd], 0\n\t" \
    "s_add_u32 %[a], %[sx], 131072\n\t" \
    "s_add_u32 %[b], %[oo], 0x1000\n\t" \
    "s_cmp_lg_u32 %[t], 1\n\t" \
    "s_cselect_b64 %[em], -1, 0\n\t" \
    "v_mfma_f32_16x16x32_bf16 v[0:3], v[20:23], v[132:135], v[0:3]\n\t" \
    "ds_read_b128 v[44:47], %[xp] offset:22528\n\t" \
    "v_mfma_f32_16x16x32_bf16 v[4:7], v[20:23], v[140:143], v[4:7]\n\t" \
    "ds_read_b128 v[48:51], %[xp] offset:23552\n\t" \
    "v_mfma_f32_16x16x32_bf16 v[8:11], v[24:27], v[132:135], v[8:11]\n\t" \
    "ds_read_b128 v[52:55], %[pa] offset:0\n\t" \
    "s_waitcnt lgkmcnt(5)\n\t" \
    "v_mfma_f32_16x16x32_bf16 v[12:15], v[24:27], v[140:143], v[12:15]\n\t" \
    "buffer_load_dwordx4 %[l16], %[rs], %[a] offen offset:0 sc1 lds\n\t" \
    "v_mfma_f32_16x16x32_bf16 v[0:3], v[28:31], v[136:139], v[0:3]\n\t" \
    "ds_read_b128 v[20:23], %[xp] offset:24576\n\t" \
    "v_mfma_f32_16x16x32_bf16 v[4:7], v[28:31], v[144:147], v[4:7]\n\t" \
    "ds_read_b128 v[24:27], %[xp] offset:25600\n\t" \
    "v_mfma_f32_16x16x32_bf16 v[8:11], v[32:35], v[136:139], v[8:11]\n\t" \
    "ds_read_b128 v[56:59], %[pa] offset:1024\n\t" \
    "s_waitcnt lgkmcnt(6)\n\t" \
    "v_mfma_f32_16x16x32_bf16 v[12:15], v[32:35], v[144:147], v[12:15]\n\t" \
    "buffer_load_dwordx4 %[l16], %[rs], %[a] offen offset:1024 sc1 lds\n\t" \
    "v_mfma_f32_16x16x32_bf16 v[0:3], v[36:39], v[148:151], v[0:3]\n\t" \
    "ds_read_b128 v[28:31], %[xp] offset:26624\n\t" \
    "v_mfma_f32_16x16x32_bf16 v[4:7], v[36:39], v[156:159], v[4:7]\n\t" \
    "ds_read_b128 v[32:35], %[xp] offset:27648\n\t" \
    "v_mfma_f32_16x16x32_bf16 v[8:11], v[40:43], v[148:151], v[8:11]\n\t" \
    "ds_read_b128 v[60:63], %[pa] offset:2048\n\t" \
    "s_waitcnt lgkmcnt(7)\n\t" \
    "v_mfma_f32_16x16x32_bf16 v[12:15], v[40:43], v[156:159], v[12:15]\n\t" \
    "buffer_load_dwordx4 %[l16], %[rs], %[a] offen offset:2048 sc1 lds\n\t" \
    "v_mfma_f32_16x16x32_bf16 v[0:3], v[44:47], v[152:155], v[0:3]\n\t" \
    "ds_read_b128 v[36:39], %[xp] offset:28672\n\t" \
    "s_waitcnt lgkmcnt(7)\n\t" \
    "v_add_f32 v196, v196, v52\n\t" \
    "v_mfma_f32_16x16x32_bf16 v[4:7], v[44:47], v[160:163], v[4:7]\n\t" \
    "ds_read_b128 v[40:43], %[xp] offset:29696\n\t" \
    "v_add_f32 v197, v197, v53\n\t" \
    "v_mfma_f32_16x16x32_bf16 v[8:11], v[48:51], v[152:155], v[8:11]\n\t" \
    "ds_read_b128 v[64:67], %[pa] offset:3072\n\t" \
    "s_waitcnt lgkmcnt(7)\n\t" \
    "v_mfma_f32_16x16x32_bf16 v[12:15], v[48:51], v[160:163], v[12:15]\n\t" \
    "buffer_load_dwordx4 %[l16], %[rs], %[a] offen offset:3072 sc1 lds\n\t" \
    "v_add_f32 v198, v198, v54\n\t" \
    "v_add_f32 v199, v199, v55\n\t" \
    "v_mfma_f32_16x16x32_bf16 v[0:3], v[20:23], v[164:167], v[0:3]\n\t" \
    "ds_read_b128 v[44:47], %[xp] offset:30720\n\t" \
    "s_waitcnt lgkmcnt(7)\n\t" \
    "v_add_f32 v200, v200, v56\n\t" \
    "v_mfma_f32_16x16x32_bf16 v[4:7], v[20:23], v[172:175], v[4:7]\n\t" \
    "ds_read_b128 v[48:51], %[xp] offset:31744\n\t" \
    "v_add_f32 v201, v201, v57\n\t" \
    "v_mfma_f32_16x16x32_bf16 v[8:11], v[24:27], v[164:167], v[8:11]\n\t" \
    "v_add_f32 v202, v202, v58\n\t" \
    "s_waitcnt lgkmcnt(6)\n\t" \
    "v_mfma_f32_16x16x32_bf16 v[12:15], v[24:27], v[172:175], v[12:15]\n\t" \
    "v_add_f32 v203, v203, v59\n\t" \
    "s_waitcnt lgkmcnt(5)\n\t" \
    "v_add_f32 v204, v204, v60\n\t" \
    "v_add_f32 v205, v205, v61\n\t" \
    "v_mfma_f32_16x16x32_bf16 v[0:3], v[28:31], v[168:171], v[0:3]\n\t" \
    "ds_read_b128 v[20:23], %[xp] offset:32768\n\t" \
    "v_add_f32 v206, v206, v62\n\t" \
    "v_mfma_f32_16x16x32_bf16 v[4:7], v[28:31], v[176:179], v[4:7]\n\t" \
    "ds_read_b128 v[24:27], %[xp] offset:33792\n\t" \
    "v_add_f32 v207, v207, v63\n\t" \
    "v_mfma_f32_16x16x32_bf16 v[8:11], v[32:35], v[168:171], v[8:11]\n\t" \
    "s_waitcnt lgkmcnt(4)\n\t" \
    "v_add_f32 v208, v208, v64\n\t" \
    "v_mfma_f32_16x16x32_bf16 v[12:15], v[32:35], v[176:179], v[12:15]\n\t" \
    "v_add_f32 v209, v209, v65\n\t" \
    "v_add_f32 v210, v210, v66\n\t" \
    "v_add_f32 v211, v211, v67\n\t" \
    "v_mfma_f32_16x16x32_bf16 v[0:3], v[36:39], v[180:183], v[0:3]\n\t" \
    "ds_read_b128 v[28:31], %[xp] offset:34816\n\t" \
    "v_cvt_pk_bf16_f32 v52, v196, v197\n\t" \
    "v_mfma_f32_16x16x32_bf16 v[4:7], v[36:39], v[188:191], v[4:7]\n\t" \
    "ds_read_b128 v[32:35], %[xp] offset:35840\n\t" \
    "v_cvt_pk_bf16_f32 v53, v198, v199\n\t" \
    "v_mfma_f32_16x16x32_bf16 v[8:11], v[40:43], v[180:183], v[8:11]\n\t" \
    "v_cvt_pk_bf16_f32 v54, v204, v205\n\t" \
    "s_waitcnt lgkmcnt(4)\n\t" \
    "v_mfma_f32_16x16x32_bf16 v[12:15], v[40:43], v[188:191], v[12:15]\n\t" \
    "v_cvt_pk_bf16_f32 v55, v206, v207\n\t" \
    "v_cvt_pk_bf16_f32 v56, v200, v201\n\t" \
    "v_cvt_pk_bf16_f32 v57, v202, v203\n\t" \
    "v_mfma_f32_16x16x32_bf16 v[0:3], v[44:47], v[184:187], v[0:3]\n\t" \
    "ds_read_b128 v[36:39], %[xp] offset:36864\n\t" \
    "v_cvt_pk_bf16_f32 v58, v208, v209\n\t" \
    "v_mfma_f32_16x16x32_bf16 v[4:7], v[44:47], v[192:195], v[4:7]\n\t" \
    "ds_read_b128 v[40:43], %[xp] offset:37888\n\t" \
    "v_cvt_pk_bf16_f32 v59, v210, v211\n\t" \
    "v_mfma_f32_16x16x32_bf16 v[8:11], v[48:51], v[184:187], v[8:11]\n\t" \
    "s_waitcnt lgkmcnt(4)\n\t" \
    "v_mfma_f32_16x16x32_bf16 v[12:15], v[48:51], v[192:195], v[12:15]\n\t" \
    "s_mov_b64 exec, %[em]\n\t" \
    "buffer_store_dwordx4 v[52:55], %[l16], %[rs], %[oo] offen sc1\n\t" \
    "buffer_store_dwordx4 v[56:59], %[l16], %[rs], %[oo] offen offset:1024 sc1\n\t" \
    "s_mov_b64 exec, -1\n\t" \
    "s_add_u32 %[sx], %[sx], 65536\n\t" \
    "s_add_u32 %[t], %[t], 1\n\t" \
    "s_add_u32 %[oo], %[oo], 262144\n\t" \
    "s_waitcnt vmcnt(6)\n\t" \
    "s_cmp_gt_u32 %[t], " STR(SQUICK) " + 1\n\t" \
    "s_cbranch_scc1 B0_%=\n\t" \
    "s_waitcnt vmcnt(0)\n\t" \
    "B0_%=:\n\t" \
    "s_barrier\n\t" \
    "s_setprio 1\n\t" \
    "s_add_u32 m0, %[sd], 16384\n\t" \
    "s_add_u32 %[a], %[sx], 81920\n\t" \
    "v_mfma_f32_16x16x32_bf16 v[196:199], v[20:23], v[68:71], 0\n\t" \
    "ds_read_b128 v[44:47], %[xp] offset:38912\n\t" \
    "v_mfma_f32_16x16x32_bf16 v[200:203], v[20:23], v[76:79], 0\n\t" \
    "ds_read_b128 v[48:51], %[xp] offset:39936\n\t" \
    "v_mfma_f32_16x16x32_bf16 v[204:207], v[24:27], v[68:71], 0\n\t" \
    "s_waitcnt lgkmcnt(4)\n\t" \
    "v_mfma_f32_16x16x32_bf16 v[208:211], v[24:27], v[76:79], 0\n\t" \
    "v_mfma_f32_16x16x32_bf16 v[196:199], v[28:31], v[72:75], v[196:199]\n\t" \
    "ds_read_b128 v[20:23], %[xp] offset:40960\n\t" \
    "v_mfma_f32_16x16x32_bf16 v[200:203], v[28:31], v[80:83], v[200:203]\n\t" \
    "ds_read_b128 v[24:27], %[xp] offset:41984\n\t" \
    "v_mfma_f32_16x16x32_bf16 v[204:207], v[32:35], v[72:75], v[204:207]\n\t" \
    "s_waitcnt lgkmcnt(4)\n\t" \
    "v_mfma_f32_16x16x32_bf16 v[208:211], v[32:35], v[80:83], v[208:211]\n\t" \
    "v_mfma_f32_16x16x32_bf16 v[196:199], v[36:39], v[84:87], v[196:199]\n\t" \
    "ds_read_b128 v[28:31], %[xp] offset:43008\n\t" \
    "v_mfma_f32_16x16x32_bf16 v[200:203], v[36:39], v[92:95], v[200:203]\n\t" \
    "ds_read_b128 v[32:35], %[xp] offset:44032\n\t" \
    "v_mfma_f32_16x16x32_bf16 v[204:207], v[40:43], v[84:87], v[204:207]\n\t" \
    "s_waitcnt lgkmcnt(4)\n\t" \
    "v_mfma_f32_16x16x32_bf16 v[208:211], v[40:43], v[92:95], v[208:211]\n\t" \
    "v_mfma_f32_16x16x32_bf16 v[196:199], v[44:47], v[88:91], v[196:199]\n\t" \
    "ds_read_b128 v[36:39], %[xp] offset:45056\n\t" \
    "v_mfma_f32_16x16x32_bf16 v[200:203], v[44:47], v[96:99], v[200:203]\n\t" \
    "ds_read_b128 v[40:43], %[xp] offset:46080\n\t" \
    "v_mfma_f32_16x16x32_bf16 v[204:207], v[48:51], v[88:91], v[204:207]\n\t" \
    "s_waitcnt lgkmcnt(4)\n\t" \
    "v_mfma_f32_16x16x32_bf16 v[208:211], v[48:51], v[96:99], v[208:211]\n\t" \
    "v_mfma_f32_16x16x32_bf16 v[196:199], v[20:23], v[100:103], v[196:199]\n\t" \
    "ds_read_b128 v[44:47], %[xp] offset:47104\n\t" \
    "v_mfma_f32_16x16x32_bf16 v[200:203], v[20:23], v[108:111], v[200:203]\n\t" \
    "ds_read_b128 v[48:51], %[xp] offset:48128\n\t" \
    "v_mfma_f32_16x16x32_bf16 v[204:207], v[24:27], v[100:103], v[204:207]\n\t" \
    "s_waitcnt lgkmcnt(4)\n\t" \
    "v_mfma_f32_16x16x32_bf16 v[208:211], v[24:27], v[108:111], v[208:211]\n\t" \
    "buffer_load_dwordx4 %[l16], %[rs], %[a] offen offset:0 sc1 lds\n\t" \
    "v_mfma_f32_16x16x32_bf16 v[196:199], v[28:31], v[104:107], v[196:199]\n\t" \
    "ds_read_b128 v[20:23], %[xp] offset:49152\n\t" \
    "v_mfma_f32_16x16x32_bf16 v[200:203], v[28:31], v[112:115], v[200:203]\n\t" \
    "ds_read_b128 v[24:27], %[xp] offset:50176\n\t" \
    "v_mfma_f32_16x16x32_bf16 v[204:207], v[32:35], v[104:107], v[204:207]\n\t" \
    "s_waitcnt lgkmcnt(4)\n\t" \
    "v_mfma_f32_16x16x32_bf16 v[208:211], v[32:35], v[112:115], v[208:211]\n\t" \
    "buffer_load_dwordx4 %[l16], %[rs], %[a] offen offset:1024 sc1 lds\n\t" \
    "v_mfma_f32_16x16x32_bf16 v[196:199], v[36:39], v[116:119], v[196:199]\n\t" \
    "ds_read_b128 v[28:31], %[xp] offset:51200\n\t" \
    "v_mfma_f32_16x16x32_bf16 v[200:203], v[36:39], v[124:127], v[200:203]\n\t" \
    "ds_read_b128 v[32:35], %[xp] offset:52224\n\t" \
    "v_mfma_f32_16x16x32_bf16 v[204:207], v[40:43], v[116:119], v[204:207]\n\t" \
    "s_waitcnt lgkmcnt(4)\n\t" \
    "v_mfma_f32_16x16x32_bf16 v[208:211], v[40:43], v[124:127], v[208:211]\n\t" \
    "buffer_load_dwordx4 %[l16], %[rs], %[a] offen offset:2048 sc1 lds\n\t" \
    "v_mfma_f32_16x16x32_bf16 v[196:199], v[44:47], v[120:123], v[196:199]\n\t" \
    "ds_read_b128 v[36:39], %[xp] offset:53248\n\t" \
    "v_mfma_f32_16x16x32_bf16 v[200:203], v[44:47], v[128:131], v[200:203]\n\t" \
    "ds_read_b128 v[40:43], %[xp] offset:54272\n\t" \
    "v_mfma_f32_16x16x32_bf16 v[204:207], v[48:51], v[120:123], v[204:207]\n\t" \
    "s_waitcnt lgkmcnt(4)\n\t" \
    "v_mfma_f32_16x16x32_bf16 v[208:211], v[48:51], v[128:131], v[208:211]\n\t" \
    "buffer_load_dwordx4 %[l16], %[rs], %[a] offen offset:3072 sc1 lds\n\t" \
    "s_waitcnt vmcnt(4)\n\t" \
    "s_barrier\n\t" \
    "s_setprio 1\n\t" \
    "s_add_u32 m0, %[sd], 32768\n\t" \
    "s_add_u32 %[a], %[sx], 131072\n\t" \
    "s_add_u32 %[b], %[oo], 0x1000\n\t" \
    "s_cmp_lg_u32 %[t], 1\n\t" \
    "s_cselect_b64 %[em], -1, 0\n\t" \
    "v_mfma_f32_16x16x32_bf16 v[196:199], v[20:23], v[132:135], v[196:199]\n\t" \
    "ds_read_b128 v[44:47], %[xp] offset:55296\n\t" \
    "v_mfma_f32_16x16x32_bf16 v[200:203], v[20:23], v[140:143], v[200:203]\n\t" \
    "ds_read_b128 v[48:51], %[xp] offset:56320\n\t" \
    "v_mfma_f32_16x16x32_bf16 v[204:207], v[24:27], v[132:135], v[204:207]\n\t" \
    "ds_read_b128 v[52:55], %[pa] offset:0\n\t" \
    "s_waitcnt lgkmcnt(5)\n\t" \
    "v_mfma_f32_16x16x32_bf16 v[208:211], v[24:27], v[140:143], v[208:211]\n\t" \
    "buffer_load_dwordx4 %[l16], %[rs], %[a] offen offset:0 sc1 lds\n\t" \
    "v_mfma_f32_16x16x32_bf16 v[196:199], v[28:31], v[136:139], v[196:199]\n\t" \
    "ds_read_b128 v[20:23], %[xp] offset:57344\n\t" \
    "v_mfma_f32_16x16x32_bf16 v[200:203], v[28:31], v[144:147], v[200:203]\n\t" \
    "ds_read_b128 v[24:27], %[xp] offset:58368\n\t" \
    "v_mfma_f32_16x16x32_bf16 v[204:207], v[32:35], v[136:139], v[204:207]\n\t" \
    "ds_read_b128 v[56:59], %[pa] offset:1024\n\t" \
    "s_waitcnt lgkmcnt(6)\n\t" \
    "v_mfma_f32_16x16x32_bf16 v[208:211], v[32:35], v[144:147], v[208:211]\n\t" \
    "buffer_load_dwordx4 %[l16], %[rs], %[a] offen offset:1024 sc1 lds\n\t" \
    "v_mfma_f32_16x16x32_bf16 v[196:199], v[36:39], v[148:151], v[196:199]\n\t" \
    "ds_read_b128 v[28:31], %[xp] offset:59392\n\t" \
    "v_mfma_f32_16x16x32_bf16 v[200:203], v[36:39], v[156:159], v[200:203]\n\t" \
    "ds_read_b128 v[32:35], %[xp] offset:60416\n\t" \
    "v_mfma_f32_16x16x32_bf16 v[204:207], v[40:43], v[148:151], v[204:207]\n\t" \
    "ds_read_b128 v[60:63], %[pa] offset:2048\n\t" \
    "s_waitcnt lgkmcnt(7)\n\t" \
    "v_mfma_f32_16x16x32_bf16 v[208:211], v[40:43], v[156:159], v[208:211]\n\t" \
    "buffer_load_dwordx4 %[l16], %[rs], %[a] offen offset:2048 sc1 lds\n\t" \
    "v_mfma_f32_16x16x32_bf16 v[196:199], v[44:47], v[152:155], v[196:199]\n\t" \
    "ds_read_b128 v[36:39], %[xp] offset:61440\n\t" \
    "s_waitcnt lgkmcnt(7)\n\t" \
    "v_add_f32 v0, v0, v52\n\t" \
    "v_mfma_f32_16x16x32_bf16 v[200:203], v[44:47], v[160:163], v[200:203]\n\t" \
    "ds_read_b128 v[40:43], %[xp] offset:62464\n\t" \
    "v_add_f32 v1, v1, v53\n\t" \
    "v_mfma_f32_16x16x32_bf16 v[204:207], v[48:51], v[152:155], v[204:207]\n\t" \
    "ds_read_b128 v[64:67], %[pa] offset:3072\n\t" \
    "s_waitcnt lgkmcnt(7)\n\t" \
    "v_mfma_f32_16x16x32_bf16 v[208:211], v[48:51], v[160:163], v[208:211]\n\t" \
    "buffer_load_dwordx4 %[l16], %[rs], %[a] offen offset:3072 sc1 lds\n\t" \
    "v_add_f32 v2, v2, v54\n\t" \
    "v_add_f32 v3, v3, v55\n\t" \
    "v_mfma_f32_16x16x32_bf16 v[196:199], v[20:23], v[164:167], v[196:199]\n\t" \
    "ds_read_b128 v[44:47], %[xp] offset:63488\n\t" \
    "s_waitcnt lgkmcnt(7)\n\t" \
    "v_add_f32 v4, v4, v56\n\t" \
    "v_mfma_f32_16x16x32_bf16 v[200:203], v[20:23], v[172:175], v[200:203]\n\t" \
    "ds_read_b128 v[48:51], %[xp] offset:64512\n\t" \
    "v_add_f32 v5, v5, v57\n\t" \
    "v_mfma_f32_16x16x32_bf16 v[204:207], v[24:27], v[164:167], v[204:207]\n\t" \
    "v_add_f32 v6, v6, v58\n\t" \
    "s_waitcnt lgkmcnt(6)\n\t" \
    "v_mfma_f32_16x16x32_bf16 v[208:211], v[24:27], v[172:175], v[208:211]\n\t" \
    "v_add_f32 v7, v7, v59\n\t" \
    "s_waitcnt lgkmcnt(5)\n\t" \
    "v_add_f32 v8, v8, v60\n\t" \
    "v_add_f32 v9, v9, v61\n\t" \
    "v_mfma_f32_16x16x32_bf16 v[196:199], v[28:31], v[168:171], v[196:199]\n\t" \
    "ds_read_b128 v[20:23], %[xp] offset:0\n\t" \
    "v_add_f32 v10, v10, v62\n\t" \
    "v_mfma_f32_16x16x32_bf16 v[200:203], v[28:31], v[176:179], v[200:203]\n\t" \
    "ds_read_b128 v[24:27], %[xp] offset:1024\n\t" \
    "v_add_f32 v11, v11, v63\n\t" \
    "v_mfma_f32_16x16x32_bf16 v[204:207], v[32:35], v[168:171], v[204:207]\n\t" \
    "s_waitcnt lgkmcnt(4)\n\t" \
    "v_add_f32 v12, v12, v64\n\t" \
    "v_mfma_f32_16x16x32_bf16 v[208:211], v[32:35], v[176:179], v[208:211]\n\t" \
    "v_add_f32 v13, v13, v65\n\t" \
    "v_add_f32 v14, v14, v66\n\t" \
    "v_add_f32 v15, v15, v67\n\t" \
    "v_mfma_f32_16x16x32_bf16 v[196:199], v[36:39], v[180:183], v[196:199]\n\t" \
    "ds_read_b128 v[28:31], %[xp] offset:2048\n\t" \
    "v_cvt_pk_bf16_f32 v52, v0, v1\n\t" \
    "v_mfma_f32_16x16x32_bf16 v[200:203], v[36:39], v[188:191], v[200:203]\n\t" \
    "ds_read_b128 v[32:35], %[xp] offset:3072\n\t" \
    "v_cvt_pk_bf16_f32 v53, v2, v3\n\t" \
    "v_mfma_f32_16x16x32_bf16 v[204:207], v[40:43], v[180:183], v[204:207]\n\t" \
    "v_cvt_pk_bf16_f32 v54, v8, v9\n\t" \
    "s_waitcnt lgkmcnt(4)\n\t" \
    "v_mfma_f32_16x16x32_bf16 v[208:211], v[40:43], v[188:191], v[208:211]\n\t" \
    "v_cvt_pk_bf16_f32 v55, v10, v11\n\t" \
    "v_cvt_pk_bf16_f32 v56, v4, v5\n\t" \
    "v_cvt_pk_bf16_f32 v57, v6, v7\n\t" \
    "v_mfma_f32_16x16x32_bf16 v[196:199], v[44:47], v[184:187], v[196:199]\n\t" \
    "ds_read_b128 v[36:39], %[xp] offset:4096\n\t" \
    "v_cvt_pk_bf16_f32 v58, v12, v13\n\t" \
    "v_mfma_f32_16x16x32_bf16 v[200:203], v[44:47], v[192:195], v[200:203]\n\t" \
    "ds_read_b128 v[40:43], %[xp] offset:5120\n\t" \
    "v_cvt_pk_bf16_f32 v59, v14, v15\n\t" \
    "v_mfma_f32_16x16x32_bf16 v[204:207], v[48:51], v[184:187], v[204:207]\n\t" \
    "s_waitcnt lgkmcnt(4)\n\t" \
    "v_mfma_f32_16x16x32_bf16 v[208:211], v[48:51], v[192:195], v[208:211]\n\t" \
    "s_mov_b64 exec, %[em]\n\t" \
    "buffer_store_dwordx4 v[52:55], %[l16], %[rs], %[oo] offen sc1\n\t" \
    "buffer_store_dwordx4 v[56:59], %[l16], %[rs], %[oo] offen offset:1024 sc1\n\t" \
    "s_mov_b64 exec, -1\n\t" \
    "s_add_u32 %[sx], %[sx], 65536\n\t" \
    "s_add_u32 %[t], %[t], 1\n\t" \
    "s_add_u32 %[oo], %[oo], 262144\n\t" \
    "s_waitcnt vmcnt(6)\n\t" \
    "s_cmp_gt_u32 %[t], " STR(SQUICK) " + 1\n\t" \
    "s_cbranch_scc1 B1_%=\n\t" \
    "s_waitcnt vmcnt(0)\n\t" \
    "B1_%=:\n\t" \
    "s_barrier\n\t" \
    "s_cmp_lt_u32 %[t], %[te]\n\t" \
    "s_cbranch_scc1 L1_%=\n\t" \
    "s_cmp_eq_u32 %[fin], %[te]\n\t" \
    "s_cbranch_scc0 E_%=\n\t" \
    "s_nop 4\n\t" \
    "s_barrier\n\t" \
    "ds_read_b128 v[52:55], %[pa] offset:0\n\t" \
    "ds_read_b128 v[56:59], %[pa] offset:1024\n\t" \
    "ds_read_b128 v[60:63], %[pa] offset:2048\n\t" \
    "ds_read_b128 v[64:67], %[pa] offset:3072\n\t" \
    "s_add_u32 %[b], %[oo], 0x1000\n\t" \
    "s_waitcnt lgkmcnt(0)\n\t" \
    "v_add_f32 v196, v196, v52\n\t" \
    "v_add_f32 v197, v197, v53\n\t" \
    "v_add_f32 v198, v198, v54\n\t" \
    "v_add_f32 v199, v199, v55\n\t" \
    "v_add_f32 v200, v200, v56\n\t" \
    "v_add_f32 v201, v201, v57\n\t" \
    "v_add_f32 v202, v202, v58\n\t" \
    "v_add_f32 v203, v203, v59\n\t" \
    "v_add_f32 v204, v204, v60\n\t" \
    "v_add_f32 v205, v205, v61\n\t" \
    "v_add_f32 v206, v206, v62\n\t" \
    "v_add_f32 v207, v207, v63\n\t" \
    "v_add_f32 v208, v208, v64\n\t" \
    "v_add_f32 v209, v209, v65\n\t" \
    "v_add_f32 v210, v210, v66\n\t" \
    "v_add_f32 v211, v211, v67\n\t" \
    "v_cvt_pk_bf16_f32 v52, v196, v197\n\t" \
    "v_cvt_pk_bf16_f32 v53, v198, v199\n\t" \
    "v_cvt_pk_bf16_f32 v54, v204, v205\n\t" \
    "v_cvt_pk_bf16_f32 v55, v206, v207\n\t" \
    "v_cvt_pk_bf16_f32 v56, v200, v201\n\t" \
    "v_cvt_pk_bf16_f32 v57, v202, v203\n\t" \
    "v_cvt_pk_bf16_f32 v58, v208, v209\n\t" \
    "v_cvt_pk_bf16_f32 v59, v210, v211\n\t" \
    "buffer_store_dwordx4 v[52:55], %[l16], %[rs], %[oo] offen sc1\n\t" \
    "buffer_store_dwordx4 v[56:59], %[l16], %[rs], %[oo] offen offset:1024 sc1\n\t" \
    "E_%=:\n\t" \
    "s_setprio 0\n\t" \
    "s_waitcnt lgkmcnt(0)\n\t" \
    "s_nop 7\n\t" \
    : [t] "+s"(t), [sx] "+s"(sx), [oo] "+s"(oo), [a] "=&s"(tmp), [b] "=&s"(tmp2), [em] "=&s"(em), "+{v[20:27]}"(xf[0]), "+{v[28:35]}"(xf[1]), "+{v[36:43]}"(xf[2]), "+{v[196:203]}"(ac[0]), "+{v[204:211]}"(ac[1]) \
    : [te] "s"(TE_), [fin] "s"(FIN_), [kh] "s"(kh), [sd] "s"(sd), [rs] "s"(rs), [l16] "v"(l16), [xp] "v"(xp), [pa] "v"(pa), [fm] "s"(fm), [pm] "s"(pm), [fo] "s"(fo), [po] "s"(po), [pd] "s"(pd), [tag] "s"(P.tag), "{v[68:75]}"(w[0][0]), "{v[76:83]}"(w[0][1]), "{v[84:91]}"(w[1][0]), "{v[92:99]}"(w[1][1]), "{v[100:107]}"(w[2][0]), "{v[108:115]}"(w[2][1]), "{v[116:123]}"(w[3][0]), "{v[124:131]}"(w[3][1]), "{v[132:139]}"(w[4][0]), "{v[140:147]}"(w[4][1]), "{v[148:155]}"(w[5][0]), "{v[156:163]}"(w[5][1]), "{v[164:171]}"(w[6][0]), "{v[172:179]}"(w[6][1]), "{v[180:187]}"(w[7][0]), "{v[188:195]}"(w[7][1]) \
    : "memory", "scc", "v0", "v1", "v2", "v3", "v4", "v5", "v6", "v7", "v8", "v9", "v10", "v11", "v12", "v13", "v14", "v15", "v44", "v45", "v46", "v47", "v48", "v49", "v50", "v51", "v52", "v53", "v54", "v55", "v56", "v57", "v58", "v59", "v60", "v61", "v62", "v63", "v64", "v65", "v66", "v67")

#define S_LOOP_X(TE_, FIN_) \
  asm volatile( \
    "s_cmp_eq_u32 %[kh], 0\n\t" \
    "s_cbranch_scc0 L1_%=\n\t" \
    "L0_%=:\n\t" \
    "s_setprio 0\n\t" \
    "s_nop 3\n\t" \
    "ds_write_b128 %[pa], v[196:199] offset:0\n\t" \
    "ds_write_b128 %[pa], v[200:203] offset:1024\n\t" \
    "ds_write_b128 %[pa], v[204:207] offset:2048\n\t" \
    "ds_write_b128 %[pa], v[208:211] offset:3072\n\t" \
    "s_sub_u32 %[a], %[t], 1\n\t" \
    "v_mov_b32 v53, %[tag]\n\t" \
    "v_mov_b32 v52, %[a]\n\t" \
    "s_sub_u32 %[b], %[t], 2\n\t" \
    "s_cmp_lt_u32 %[b], " STR(SQUICK) "\n\t" \
    "s_cselect_b64 exec, %[fm], 0\n\t" \
    "buffer_store_dwordx2 v[52:53], %[l16], %[rs], %[fo] offen sc1\n\t" \
    "s_mov_b64 exec, -1\n\t" \
    "s_add_u32 m0, %[sd], 49152\n\t" \
    "s_add_u32 %[a], %[sx], 278528\n\t" \
    "s_waitcnt lgkmcnt(0)\n\t" \
    "buffer_load_dwordx4 %[l16], %[rs], %[a] offen offset:0 sc1 lds\n\t" \
    "buffer_load_dwordx4 %[l16], %[rs], %[a] offen offset:1024 sc1 lds\n\t" \
    "buffer_load_dwordx4 %[l16], %[rs], %[a] offen offset:2048 sc1 lds\n\t" \
    "buffer_load_dwordx4 %[l16], %[rs], %[a] offen offset:3072 sc1 lds\n\t" \
    "v_mfma_f32_16x16x32_bf16 v[0:3], v[68:71], v[20:23], 0\n\t" \
    "ds_read_b128 v[44:47], %[xp] offset:6144\n\t" \
    "v_mfma_f32_16x16x32_bf16 v[4:7], v[76:79], v[20:23], 0\n\t" \
    "ds_read_b128 v[48:51], %[xp] offset:7168\n\t" \
    "v_mfma_f32_16x16x32_bf16 v[8:11], v[68:71], v[24:27], 0\n\t" \
    "v_mfma_f32_16x16x32_bf16 v[12:15], v[76:79], v[24:27], 0\n\t" \
    "v_mfma_f32_16x16x32_bf16 v[0:3], v[72:75], v[28:31], v[0:3]\n\t" \
    "ds_read_b128 v[20:23], %[xp] offset:8192\n\t" \
    "v_mfma_f32_16x16x32_bf16 v[4:7], v[80:83], v[28:31], v[4:7]\n\t" \
    "ds_read_b128 v[24:27], %[xp] offset:9216\n\t" \
    "v_mfma_f32_16x16x32_bf16 v[8:11], v[72:75], v[32:35], v[8:11]\n\t" \
    "v_mfma_f32_16x16x32_bf16 v[12:15], v[80:83], v[32:35], v[12:15]\n\t" \
    "v_mfma_f32_16x16x32_bf16 v[0:3], v[84:87], v[36:39], v[0:3]\n\t" \
    "ds_read_b128 v[28:31], %[xp] offset:10240\n\t" \
    "v_mfma_f32_16x16x32_bf16 v[4:7], v[92:95], v[36:39], v[4:7]\n\t" \
    "ds_read_b128 v[32:35], %[xp] offset:11264\n\t" \
    "v_mfma_f32_16x16x32_bf16 v[8:11], v[84:87], v[40:43], v[8:11]\n\t" \
    "s_waitcnt lgkmcnt(4)\n\t" \
    "v_mfma_f32_16x16x32_bf16 v[12:15], v[92:95], v[40:43], v[12:15]\n\t" \
    "v_mfma_f32_16x16x32_bf16 v[0:3], v[88:91], v[44:47], v[0:3]\n\t" \
    "ds_read_b128 v[36:39], %[xp] offset:12288\n\t" \
    "v_mfma_f32_16x16x32_bf16 v[4:7], v[96:99], v[44:47], v[4:7]\n\t" \
    "ds_read_b128 v[40:43], %[xp] offset:13312\n\t" \
    "v_mfma_f32_16x16x32_bf16 v[8:11], v[88:91], v[48:51], v[8:11]\n\t" \
    "s_waitcnt lgkmcnt(4)\n\t" \
    "v_mfma_f32_16x16x32_bf16 v[12:15], v[96:99], v[48:51], v[12:15]\n\t" \
    "v_mfma_f32_16x16x32_bf16 v[0:3], v[100:103], v[20:23], v[0:3]\n\t" \
    "ds_read_b128 v[44:47], %[xp] offset:14336\n\t" \
    "v_mfma_f32_16x16x32_bf16 v[4:7], v[108:111], v[20:23], v[4:7]\n\t" \
    "ds_read_b128 v[48:51], %[xp] offset:15360\n\t" \
    "v_mfma_f32_16x16x32_bf16 v[8:11], v[100:103], v[24:27], v[8:11]\n\t" \
    "s_waitcnt lgkmcnt(4)\n\t" \
    "v_mfma_f32_16x16x32_bf16 v[12:15], v[108:111], v[24:27], v[12:15]\n\t" \
    "v_mfma_f32_16x16x32_bf16 v[0:3], v[104:107], v[28:31], v[0:3]\n\t" \
    "ds_read_b128 v[20:23], %[xp] offset:16384\n\t" \
    "v_mfma_f32_16x16x32_bf16 v[4:7], v[112:115], v[28:31], v[4:7]\n\t" \
    "ds_read_b128 v[24:27], %[xp] offset:17408\n\t" \
    "v_mfma_f32_16x16x32_bf16 v[8:11], v[104:107], v[32:35], v[8:11]\n\t" \
    "s_waitcnt lgkmcnt(4)\n\t" \
    "v_mfma_f32_16x16x32_bf16 v[12:15], v[112:115], v[32:35], v[12:15]\n\t" \
    "v_mfma_f32_16x16x32_bf16 v[0:3], v[116:119], v[36:39], v[0:3]\n\t" \
    "ds_read_b128 v[28:31], %[xp] offset:18432\n\t" \
    "v_mfma_f32_16x16x32_bf16 v[4:7], v[124:127], v[36:39], v[4:7]\n\t" \
    "ds_read_b128 v[32:35], %[xp] offset:19456\n\t" \
    "v_mfma_f32_16x16x32_bf16 v[8:11], v[116:119], v[40:43], v[8:11]\n\t" \
    "s_waitcnt lgkmcnt(4)\n\t" \
    "v_mfma_f32_16x16x32_bf16 v[12:15], v[124:127], v[40:43], v[12:15]\n\t" \
    "v_mfma_f32_16x16x32_bf16 v[0:3], v[120:123], v[44:47], v[0:3]\n\t" \
    "ds_read_b128 v[36:39], %[xp] offset:20480\n\t" \
    "v_mfma_f32_16x16x32_bf16 v[4:7], v[128:131], v[44:47], v[4:7]\n\t" \
    "ds_read_b128 v[40:43], %[xp] offset:21504\n\t" \
    "v_mfma_f32_16x16x32_bf16 v[8:11], v[120:123], v[48:51], v[8:11]\n\t" \
    "s_waitcnt lgkmcnt(4)\n\t" \
    "v_mfma_f32_16x16x32_bf16 v[12:15], v[128:131], v[48:51], v[12:15]\n\t" \
    "s_waitcnt vmcnt(4)\n\t" \
    "s_barrier\n\t" \
    "s_setprio 0\n\t" \
    "s_sub_u32 %[a], %[t], 1\n\t" \
    "v_mov_b32 v53, %[tag]\n\t" \
    "v_mov_b32 v52, %[a]\n\t" \
    "s_cmp_gt_u32 %[t], 1\n\t" \
    "s_cselect_b64 exec, %[fm], 0\n\t" \
    "buffer_store_dwordx2 v[52:53], %[l16], %[rs], %[fo] offen sc1\n\t" \
    "s_mov_b64 exec, %[pm]\n\t" \
    "s_mov_b32 m0, %[pd]\n\t" \
    "s_add_u32 %[a], %[sx], %[po]\n\t" \
    "buffer_load_dwordx4 %[l16], %[rs], %[a] offen sc1 lds\n\t" \
    "s_mov_b64 exec, -1\n\t" \
    "s_add_u32 m0, %[sd], 0\n\t" \
    "s_add_u32 %[a], %[sx], 524288\n\t" \
    "buffer_load_dwordx4 %[l16], %[rs], %[a] offen offset:0 sc1 lds\n\t" \
    "buffer_load_dwordx4 %[l16], %[rs], %[a] offen offset:1024 sc1 lds\n\t" \
    "buffer_load_dwordx4 %[l16], %[rs], %[a] offen offset:2048 sc1 lds\n\t" \
    "buffer_load_dwordx4 %[l16], %[rs], %[a] offen offset:3072 sc1 lds\n\t" \
    "v_mfma_f32_16x16x32_bf16 v[0:3], v[132:135], v[20:23], v[0:3]\n\t" \
    "ds_read_b128 v[44:47], %[xp] offset:22528\n\t" \
    "v_mfma_f32_16x16x32_bf16 v[4:7], v[140:143], v[20:23], v[4:7]\n\t" \
    "ds_read_b128 v[48:51], %[xp] offset:23552\n\t" \
    "v_mfma_f32_16x16x32_bf16 v[8:11], v[132:135], v[24:27], v[8:11]\n\t" \
    "s_waitcnt lgkmcnt(4)\n\t" \
    "v_mfma_f32_16x16x32_bf16 v[12:15], v[140:143], v[24:27], v[12:15]\n\t" \
    "v_mfma_f32_16x16x32_bf16 v[0:3], v[136:139], v[28:31], v[0:3]\n\t" \
    "ds_read_b128 v[20:23], %[xp] offset:24576\n\t" \
    "v_mfma_f32_16x16x32_bf16 v[4:7], v[144:147], v[28:31], v[4:7]\n\t" \
    "ds_read_b128 v[24:27], %[xp] offset:25600\n\t" \
    "v_mfma_f32_16x16x32_bf16 v[8:11], v[136:139], v[32:35], v[8:11]\n\t" \
    "s_waitcnt lgkmcnt(4)\n\t" \
    "v_mfma_f32_16x16x32_bf16 v[12:15], v[144:147], v[32:35], v[12:15]\n\t" \
    "v_mfma_f32_16x16x32_bf16 v[0:3], v[148:151], v[36:39], v[0:3]\n\t" \
    "ds_read_b128 v[28:31], %[xp] offset:26624\n\t" \
    "v_mfma_f32_16x16x32_bf16 v[4:7], v[156:159], v[36:39], v[4:7]\n\t" \
    "ds_read_b128 v[32:35], %[xp] offset:27648\n\t" \
    "v_mfma_f32_16x16x32_bf16 v[8:11], v[148:151], v[40:43], v[8:11]\n\t" \
    "s_waitcnt lgkmcnt(4)\n\t" \
    "v_mfma_f32_16x16x32_bf16 v[12:15], v[156:159], v[40:43], v[12:15]\n\t" \
    "v_mfma_f32_16x16x32_bf16 v[0:3], v[152:155], v[44:47], v[0:3]\n\t" \
    "ds_read_b128 v[36:39], %[xp] offset:28672\n\t" \
    "v_mfma_f32_16x16x32_bf16 v[4:7], v[160:163], v[44:47], v[4:7]\n\t" \
    "ds_read_b128 v[40:43], %[xp] offset:29696\n\t" \
    "v_mfma_f32_16x16x32_bf16 v[8:11], v[152:155], v[48:51], v[8:11]\n\t" \
    "s_waitcnt lgkmcnt(4)\n\t" \
    "v_mfma_f32_16x16x32_bf16 v[12:15], v[160:163], v[48:51], v[12:15]\n\t" \
    "v_mfma_f32_16x16x32_bf16 v[0:3], v[164:167], v[20:23], v[0:3]\n\t" \
    "ds_read_b128 v[44:47], %[xp] offset:30720\n\t" \
    "v_mfma_f32_16x16x32_bf16 v[4:7], v[172:175], v[20:23], v[4:7]\n\t" \
    "ds_read_b128 v[48:51], %[xp] offset:31744\n\t" \
    "v_mfma_f32_16x16x32_bf16 v[8:11], v[164:167], v[24:27], v[8:11]\n\t" \
    "s_waitcnt lgkmcnt(4)\n\t" \
    "v_mfma_f32_16x16x32_bf16 v[12:15], v[172:175], v[24:27], v[12:15]\n\t" \
    "v_mfma_f32_16x16x32_bf16 v[0:3], v[168:171], v[28:31], v[0:3]\n\t" \
    "ds_read_b128 v[20:23], %[xp] offset:32768\n\t" \
    "v_mfma_f32_16x16x32_bf16 v[4:7], v[176:179], v[28:31], v[4:7]\n\t" \
    "ds_read_b128 v[24:27], %[xp] offset:33792\n\t" \
    "v_mfma_f32_16x16x32_bf16 v[8:11], v[168:171], v[32:35], v[8:11]\n\t" \
    "s_waitcnt lgkmcnt(4)\n\t" \
    "v_mfma_f32_16x16x32_bf16 v[12:15], v[176:179], v[32:35], v[12:15]\n\t" \
    "v_mfma_f32_16x16x32_bf16 v[0:3], v[180:183], v[36:39], v[0:3]\n\t" \
    "ds_read_b128 v[28:31], %[xp] offset:34816\n\t" \
    "v_mfma_f32_16x16x32_bf16 v[4:7], v[188:191], v[36:39], v[4:7]\n\t" \
    "ds_read_b128 v[32:35], %[xp] offset:35840\n\t" \
    "v_mfma_f32_16x16x32_bf16 v[8:11], v[180:183], v[40:43], v[8:11]\n\t" \
    "s_waitcnt lgkmcnt(4)\n\t" \
    "v_mfma_f32_16x16x32_bf16 v[12:15], v[188:191], v[40:43], v[12:15]\n\t" \
    "v_mfma_f32_16x16x32_bf16 v[0:3], v[184:187], v[44:47], v[0:3]\n\t" \
    "ds_read_b128 v[36:39], %[xp] offset:36864\n\t" \
    "v_mfma_f32_16x16x32_bf16 v[4:7], v[192:195], v[44:47], v[4:7]\n\t" \
    "ds_read_b128 v[40:43], %[xp] offset:37888\n\t" \
    "v_mfma_f32_16x16x32_bf16 v[8:11], v[184:187], v[48:51], v[8:11]\n\t" \
    "s_waitcnt lgkmcnt(4)\n\t" \
    "v_mfma_f32_16x16x32_bf16 v[12:15], v[192:195], v[48:51], v[12:15]\n\t" \
    "s_add_u32 %[sx], %[sx], 262144\n\t" \
    "s_add_u32 %[t], %[t], 1\n\t" \
    "s_waitcnt vmcnt(4)\n\t" \
    "s_barrier\n\t" \
    "s_setprio 0\n\t" \
    "s_nop 3\n\t" \
    "ds_write_b128 %[pa], v[0:3] offset:0\n\t" \
    "ds_write_b128 %[pa], v[4:7] offset:1024\n\t" \
    "ds_write_b128 %[pa], v[8:11] offset:2048\n\t" \
    "ds_write_b128 %[pa], v[12:15] offset:3072\n\t" \
    "s_sub_u32 %[a], %[t], 1\n\t" \
    "v_mov_b32 v53, %[tag]\n\t" \
    "v_mov_b32 v52, %[a]\n\t" \
    "s_sub_u32 %[b], %[t], 2\n\t" \
    "s_cmp_lt_u32 %[b], " STR(SQUICK) "\n\t" \
    "s_cselect_b64 exec, %[fm], 0\n\t" \
    "buffer_store_dwordx2 v[52:53], %[l16], %[rs], %[fo] offen sc1\n\t" \
    "s_mov_b64 exec, -1\n\t" \
    "s_add_u32 m0, %[sd], 16384\n\t" \
    "s_add_u32 %[a], %[sx], 278528\n\t" \
    "s_waitcnt lgkmcnt(0)\n\t" \
    "buffer_load_dwordx4 %[l16], %[rs], %[a] offen offset:0 sc1 lds\n\t" \
    "buffer_load_dwordx4 %[l16], %[rs], %[a] offen offset:1024 sc1 lds\n\t" \
    "buffer_load_dwordx4 %[l16], %[rs], %[a] offen offset:2048 sc1 lds\n\t" \
    "buffer_load_dwordx4 %[l16], %[rs], %[a] offen offset:3072 sc1 lds\n\t" \
    "v_mfma_f32_16x16x32_bf16 v[196:199], v[68:71], v[20:23], 0\n\t" \
    "ds_read_b128 v[44:47], %[xp] offset:38912\n\t" \
    "v_mfma_f32_16x16x32_bf16 v[200:203], v[76:79], v[20:23], 0\n\t" \
    "ds_read_b128 v[48:51], %[xp] offset:39936\n\t" \
    "v_mfma_f32_16x16x32_bf16 v[204:207], v[68:71], v[24:27], 0\n\t" \
    "v_mfma_f32_16x16x32_bf16 v[208:211], v[76:79], v[24:27], 0\n\t" \
    "v_mfma_f32_16x16x32_bf16 v[196:199], v[72:75], v[28:31], v[196:199]\n\t" \
    "ds_read_b128 v[20:23], %[xp] offset:40960\n\t" \
    "v_mfma_f32_16x16x32_bf16 v[200:203], v[80:83], v[28:31], v[200:203]\n\t" \
    "ds_read_b128 v[24:27], %[xp] offset:41984\n\t" \
    "v_mfma_f32_16x16x32_bf16 v[204:207], v[72:75], v[32:35], v[204:207]\n\t" \
    "v_mfma_f32_16x16x32_bf16 v[208:211], v[80:83], v[32:35], v[208:211]\n\t" \
    "v_mfma_f32_16x16x32_bf16 v[196:199], v[84:87], v[36:39], v[196:199]\n\t" \
    "ds_read_b128 v[28:31], %[xp] offset:43008\n\t" \
    "v_mfma_f32_16x16x32_bf16 v[200:203], v[92:95], v[36:39], v[200:203]\n\t" \
    "ds_read_b128 v[32:35], %[xp] offset:44032\n\t" \
    "v_mfma_f32_16x16x32_bf16 v[204:207], v[84:87], v[40:43], v[204:207]\n\t" \
    "s_waitcnt lgkmcnt(4)\n\t" \
    "v_mfma_f32_16x16x32_bf16 v[208:211], v[92:95], v[40:43], v[208:211]\n\t" \
    "v_mfma_f32_16x16x32_bf16 v[196:199], v[88:91], v[44:47], v[196:199]\n\t" \
    "ds_read_b128 v[36:39], %[xp] offset:45056\n\t" \
    "v_mfma_f32_16x16x32_bf16 v[200:203], v[96:99], v[44:47], v[200:203]\n\t" \
    "ds_read_b128 v[40:43], %[xp] offset:46080\n\t" \
    "v_mfma_f32_16x16x32_bf16 v[204:207], v[88:91], v[48:51], v[204:207]\n\t" \
    "s_waitcnt lgkmcnt(4)\n\t" \
    "v_mfma_f32_16x16x32_bf16 v[208:211], v[96:99], v[48:51], v[208:211]\n\t" \
    "v_mfma_f32_16x16x32_bf16 v[196:199], v[100:103], v[20:23], v[196:199]\n\t" \
    "ds_read_b128 v[44:47], %[xp] offset:47104\n\t" \
    "v_mfma_f32_16x16x32_bf16 v[200:203], v[108:111], v[20:23], v[200:203]\n\t" \
    "ds_read_b128 v[48:51], %[xp] offset:48128\n\t" \
    "v_mfma_f32_16x16x32_bf16 v[204:207], v[100:103], v[24:27], v[204:207]\n\t" \
    "s_waitcnt lgkmcnt(4)\n\t" \
    "v_mfma_f32_16x16x32_bf16 v[208:211], v[108:111], v[24:27], v[208:211]\n\t" \
    "v_mfma_f32_16x16x32_bf16 v[196:199], v[104:107], v[28:31], v[196:199]\n\t" \
    "ds_read_b128 v[20:23], %[xp] offset:49152\n\t" \
    "v_mfma_f32_16x16x32_bf16 v[200:203], v[112:115], v[28:31], v[200:203]\n\t" \
    "ds_read_b128 v[24:27], %[xp] offset:50176\n\t" \
    "v_mfma_f32_16x16x32_bf16 v[204:207], v[104:107], v[32:35], v[204:207]\n\t" \
    "s_waitcnt lgkmcnt(4)\n\t" \
    "v_mfma_f32_16x16x32_bf16 v[208:211], v[112:115], v[32:35], v[208:211]\n\t" \
    "v_mfma_f32_16x16x32_bf16 v[196:199], v[116:119], v[36:39], v[196:199]\n\t" \
    "ds_read_b128 v[28:31], %[xp] offset:51200\n\t" \
    "v_mfma_f32_16x16x32_bf16 v[200:203], v[124:127], v[36:39], v[200:203]\n\t" \
    "ds_read_b128 v[32:35], %[xp] offset:52224\n\t" \
    "v_mfma_f32_16x16x32_bf16 v[204:207], v[116:119], v[40:43], v[204:207]\n\t" \
    "s_waitcnt lgkmcnt(4)\n\t" \
    "v_mfma_f32_16x16x32_bf16 v[208:211], v[124:127], v[40:43], v[208:211]\n\t" \
    "v_mfma_f32_16x16x32_bf16 v[196:199], v[120:123], v[44:47], v[196:199]\n\t" \
    "ds_read_b128 v[36:39], %[xp] offset:53248\n\t" \
    "v_mfma_f32_16x16x32_bf16 v[200:203], v[128:131], v[44:47], v[200:203]\n\t" \
    "ds_read_b128 v[40:43], %[xp] offset:54272\n\t" \
    "v_mfma_f32_16x16x32_bf16 v[204:207], v[120:123], v[48:51], v[204:207]\n\t" \
    "s_waitcnt lgkmcnt(4)\n\t" \
    "v_mfma_f32_16x16x32_bf16 v[208:211], v[128:131], v[48:51], v[208:211]\n\t" \
    "s_waitcnt vmcnt(4)\n\t" \
    "s_barrier\n\t" \
    "s_setprio 0\n\t" \
    "s_sub_u32 %[a], %[t], 1\n\t" \
    "v_mov_b32 v53, %[tag]\n\t" \
    "v_mov_b32 v52, %[a]\n\t" \
    "s_cmp_gt_u32 %[t], 1\n\t" \
    "s_cselect_b64 exec, %[fm], 0\n\t" \
    "buffer_store_dwordx2 v[52:53], %[l16], %[rs], %[fo] offen sc1\n\t" \
    "s_mov_b64 exec, %[pm]\n\t" \
    "s_mov_b32 m0, %[pd]\n\t" \
    "s_add_u32 %[a], %[sx], %[po]\n\t" \
    "buffer_load_dwordx4 %[l16], %[rs], %[a] offen sc1 lds\n\t" \
    "s_mov_b64 exec, -1\n\t" \
    "s_add_u32 m0, %[sd], 32768\n\t" \
    "s_add_u32 %[a], %[sx], 524288\n\t" \
    "buffer_load_dwordx4 %[l16], %[rs], %[a] offen offset:0 sc1 lds\n\t" \
    "buffer_load_dwordx4 %[l16], %[rs], %[a] offen offset:1024 sc1 lds\n\t" \
    "buffer_load_dwordx4 %[l16], %[rs], %[a] offen offset:2048 sc1 lds\n\t" \
    "buffer_load_dwordx4 %[l16], %[rs], %[a] offen offset:3072 sc1 lds\n\t" \
    "v_mfma_f32_16x16x32_bf16 v[196:199], v[132:135], v[20:23], v[196:199]\n\t" \
    "ds_read_b128 v[44:47], %[xp] offset:55296\n\t" \
    "v_mfma_f32_16x16x32_bf16 v[200:203], v[140:143], v[20:23], v[200:203]\n\t" \
    "ds_read_b128 v[48:51], %[xp] offset:56320\n\t" \
    "v_mfma_f32_16x16x32_bf16 v[204:207], v[132:135], v[24:27], v[204:207]\n\t" \
    "s_waitcnt lgkmcnt(4)\n\t" \
    "v_mfma_f32_16x16x32_bf16 v[208:211], v[140:143], v[24:27], v[208:211]\n\t" \
    "v_mfma_f32_16x16x32_bf16 v[196:199], v[136:139], v[28:31], v[196:199]\n\t" \
    "ds_read_b128 v[20:23], %[xp] offset:57344\n\t" \
    "v_mfma_f32_16x16x32_bf16 v[200:203], v[144:147], v[28:31], v[200:203]\n\t" \
    "ds_read_b128 v[24:27], %[xp] offset:58368\n\t" \
    "v_mfma_f32_16x16x32_bf16 v[204:207], v[136:139], v[32:35], v[204:207]\n\t" \
    "s_waitcnt lgkmcnt(4)\n\t" \
    "v_mfma_f32_16x16x32_bf16 v[208:211], v[144:147], v[32:35], v[208:211]\n\t" \
    "v_mfma_f32_16x16x32_bf16 v[196:199], v[148:151], v[36:39], v[196:199]\n\t" \
    "ds_read_b128 v[28:31], %[xp] offset:59392\n\t" \
    "v_mfma_f32_16x16x32_bf16 v[200:203], v[156:159], v[36:39], v[200:203]\n\t" \
    "ds_read_b128 v[32:35], %[xp] offset:60416\n\t" \
    "v_mfma_f32_16x16x32_bf16 v[204:207], v[148:151], v[40:43], v[204:207]\n\t" \
    "s_waitcnt lgkmcnt(4)\n\t" \
    "v_mfma_f32_16x16x32_bf16 v[208:211], v[156:159], v[40:43], v[208:211]\n\t" \
    "v_mfma_f32_16x16x32_bf16 v[196:199], v[152:155], v[44:47], v[196:199]\n\t" \
    "ds_read_b128 v[36:39], %[xp] offset:61440\n\t" \
    "v_mfma_f32_16x16x32_bf16 v[200:203], v[160:163], v[44:47], v[200:203]\n\t" \
    "ds_read_b128 v[40:43], %[xp] offset:62464\n\t" \
    "v_mfma_f32_16x16x32_bf16 v[204:207], v[152:155], v[48:51], v[204:207]\n\t" \
    "s_waitcnt lgkmcnt(4)\n\t" \
    "v_mfma_f32_16x16x32_bf16 v[208:211], v[160:163], v[48:51], v[208:211]\n\t" \
    "v_mfma_f32_16x16x32_bf16 v[196:199], v[164:167], v[20:23], v[196:199]\n\t" \
    "ds_read_b128 v[44:47], %[xp] offset:63488\n\t" \
    "v_mfma_f32_16x16x32_bf16 v[200:203], v[172:175], v[20:23], v[200:203]\n\t" \
    "ds_read_b128 v[48:51], %[xp] offset:64512\n\t" \
    "v_mfma_f32_16x16x32_bf16 v[204:207], v[164:167], v[24:27], v[204:207]\n\t" \
    "s_waitcnt lgkmcnt(4)\n\t" \
    "v_mfma_f32_16x16x32_bf16 v[208:211], v[172:175], v[24:27], v[208:211]\n\t" \
    "v_mfma_f32_16x16x32_bf16 v[196:199], v[168:171], v[28:31], v[196:199]\n\t" \
    "ds_read_b128 v[20:23], %[xp] offset:0\n\t" \
    "v_mfma_f32_16x16x32_bf16 v[200:203], v[176:179], v[28:31], v[200:203]\n\t" \
    "ds_read_b128 v[24:27], %[xp] offset:1024\n\t" \
    "v_mfma_f32_16x16x32_bf16 v[204:207], v[168:171], v[32:35], v[204:207]\n\t" \
    "s_waitcnt lgkmcnt(4)\n\t" \
    "v_mfma_f32_16x16x32_bf16 v[208:211], v[176:179], v[32:35], v[208:211]\n\t" \
    "v_mfma_f32_16x16x32_bf16 v[196:199], v[180:183], v[36:39], v[196:199]\n\t" \
    "ds_read_b128 v[28:31], %[xp] offset:2048\n\t" \
    "v_mfma_f32_16x16x32_bf16 v[200:203], v[188:191], v[36:39], v[200:203]\n\t" \
    "ds_read_b128 v[32:35], %[xp] offset:3072\n\t" \
    "v_mfma_f32_16x16x32_bf16 v[204:207], v[180:183], v[40:43], v[204:207]\n\t" \
    "s_waitcnt lgkmcnt(4)\n\t" \
    "v_mfma_f32_16x16x32_bf16 v[208:211], v[188:191], v[40:43], v[208:211]\n\t" \
    "v_mfma_f32_16x16x32_bf16 v[196:199], v[184:187], v[44:47], v[196:199]\n\t" \
    "ds_read_b128 v[36:39], %[xp] offset:4096\n\t" \
    "v_mfma_f32_16x16x32_bf16 v[200:203], v[192:195], v[44:47], v[200:203]\n\t" \
    "ds_read_b128 v[40:43], %[xp] offset:5120\n\t" \
    "v_mfma_f32_16x16x32_bf16 v[204:207], v[184:187], v[48:51], v[204:207]\n\t" \
    "s_waitcnt lgkmcnt(4)\n\t" \
    "v_mfma_f32_16x16x32_bf16 v[208:211], v[192:195], v[48:51], v[208:211]\n\t" \
    "s_add_u32 %[sx], %[sx], 262144\n\t" \
    "s_add_u32 %[t], %[t], 1\n\t" \
    "s_waitcnt vmcnt(4)\n\t" \
    "s_barrier\n\t" \
    "s_cmp_lt_u32 %[t], %[te]\n\t" \
    "s_cbranch_scc1 L0_%=\n\t" \
    "s_cmp_eq_u32 %[fin], %[te]\n\t" \
    "s_cbranch_scc0 E_%=\n\t" \
    "s_nop 4\n\t" \
    "ds_write_b128 %[pa], v[196:199] offset:0\n\t" \
    "ds_write_b128 %[pa], v[200:203] offset:1024\n\t" \
    "ds_write_b128 %[pa], v[204:207] offset:2048\n\t" \
    "ds_write_b128 %[pa], v[208:211] offset:3072\n\t" \
    "s_waitcnt lgkmcnt(0)\n\t" \
    "s_barrier\n\t" \
    "s_branch E_%=\n\t" \
    "L1_%=:\n\t" \
    "s_setprio 1\n\t" \
    "s_add_u32 m0, %[sd], 49152\n\t" \
    "s_add_u32 %[a], %[sx], 278528\n\t" \
    "v_mfma_f32_16x16x32_bf16 v[0:3], v[68:71], v[20:23], 0\n\t" \
    "ds_read_b128 v[44:47], %[xp] offset:6144\n\t" \
    "v_mfma_f32_16x16x32_bf16 v[4:7], v[76:79], v[20:23], 0\n\t" \
    "ds_read_b128 v[48:51], %[xp] offset:7168\n\t" \
    "v_mfma_f32_16x16x32_bf16 v[8:11], v[68:71], v[24:27], 0\n\t" \
    "s_waitcnt lgkmcnt(4)\n\t" \
    "v_mfma_f32_16x16x32_bf16 v[12:15], v[76:79], v[24:27], 0\n\t" \
    "v_mfma_f32_16x16x32_bf16 v[0:3], v[72:75], v[28:31], v[0:3]\n\t" \
    "ds_read_b128 v[20:23], %[xp] offset:8192\n\t" \
    "v_mfma_f32_16x16x32_bf16 v[4:7], v[80:83], v[28:31], v[4:7]\n\t" \
    "ds_read_b128 v[24:27], %[xp] offset:9216\n\t" \
    "v_mfma_f32_16x16x32_bf16 v[8:11], v[72:75], v[32:35], v[8:11]\n\t" \
    "s_waitcnt lgkmcnt(4)\n\t" \
    "v_mfma_f32_16x16x32_bf16 v[12:15], v[80:83], v[32:35], v[12:15]\n\t" \
    "v_mfma_f32_16x16x32_bf16 v[0:3], v[84:87], v[36:39], v[0:3]\n\t" \
    "ds_read_b128 v[28:31], %[xp] offset:10240\n\t" \
    "v_mfma_f32_16x16x32_bf16 v[4:7], v[92:95], v[36:39], v[4:7]\n\t" \
    "ds_read_b128 v[32:35], %[xp] offset:11264\n\t" \
    "v_mfma_f32_16x16x32_bf16 v[8:11], v[84:87], v[40:43], v[8:11]\n\t" \
    "s_waitcnt lgkmcnt(4)\n\t" \
    "v_mfma_f32_16x16x32_bf16 v[12:15], v[92:95], v[40:43], v[12:15]\n\t" \
    "v_mfma_f32_16x16x32_bf16 v[0:3], v[88:91], v[44:47], v[0:3]\n\t" \
    "ds_read_b128 v[36:39], %[xp] offset:12288\n\t" \
    "v_mfma_f32_16x16x32_bf16 v[4:7], v[96:99], v[44:47], v[4:7]\n\t" \
    "ds_read_b128 v[40:43], %[xp] offset:13312\n\t" \
    "v_mfma_f32_16x16x32_bf16 v[8:11], v[88:91], v[48:51], v[8:11]\n\t" \
    "s_waitcnt lgkmcnt(4)\n\t" \
    "v_mfma_f32_16x16x32_bf16 v[12:15], v[96:99], v[48:51], v[12:15]\n\t" \
    "v_mfma_f32_16x16x32_bf16 v[0:3], v[100:103], v[20:23], v[0:3]\n\t" \
    "ds_read_b128 v[44:47], %[xp] offset:14336\n\t" \
    "v_mfma_f32_16x16x32_bf16 v[4:7], v[108:111], v[20:23], v[4:7]\n\t" \
    "ds_read_b128 v[48:51], %[xp] offset:15360\n\t" \
    "v_mfma_f32_16x16x32_bf16 v[8:11], v[100:103], v[24:27], v[8:11]\n\t" \
    "s_waitcnt lgkmcnt(4)\n\t" \
    "v_mfma_f32_16x16x32_bf16 v[12:15], v[108:111], v[24:27], v[12:15]\n\t" \
    "buffer_load_dwordx4 %[l16], %[rs], %[a] offen offset:0 sc1 lds\n\t" \
    "v_mfma_f32_16x16x32_bf16 v[0:3], v[104:107], v[28:31], v[0:3]\n\t" \
    "ds_read_b128 v[20:23], %[xp] offset:16384\n\t" \
    "v_mfma_f32_16x16x32_bf16 v[4:7], v[112:115], v[28:31], v[4:7]\n\t" \
    "ds_read_b128 v[24:27], %[xp] offset:17408\n\t" \
    "v_mfma_f32_16x16x32_bf16 v[8:11], v[104:107], v[32:35], v[8:11]\n\t" \
    "s_waitcnt lgkmcnt(4)\n\t" \
    "v_mfma_f32_16x16x32_bf16 v[12:15], v[112:115], v[32:35], v[12:15]\n\t" \
    "buffer_load_dwordx4 %[l16], %[rs], %[a] offen offset:1024 sc1 lds\n\t" \
    "v_mfma_f32_16x16x32_bf16 v[0:3], v[116:119], v[36:39], v[0:3]\n\t" \
    "ds_read_b128 v[28:31], %[xp] offset:18432\n\t" \
    "v_mfma_f32_16x16x32_bf16 v[4:7], v[124:127], v[36:39], v[4:7]\n\t" \
    "ds_read_b128 v[32:35], %[xp] offset:19456\n\t" \
    "v_mfma_f32_16x16x32_bf16 v[8:11], v[116:119], v[40:43], v[8:11]\n\t" \
    "s_waitcnt lgkmcnt(4)\n\t" \
    "v_mfma_f32_16x16x32_bf16 v[12:15], v[124:127], v[40:43], v[12:15]\n\t" \
    "buffer_load_dwordx4 %[l16], %[rs], %[a] offen offset:2048 sc1 lds\n\t" \
    "v_mfma_f32_16x16x32_bf16 v[0:3], v[120:123], v[44:47], v[0:3]\n\t" \
    "ds_read_b128 v[36:39], %[xp] offset:20480\n\t" \
    "v_mfma_f32_16x16x32_bf16 v[4:7], v[128:131], v[44:47], v[4:7]\n\t" \
    "ds_read_b128 v[40:43], %[xp] offset:21504\n\t" \
    "v_mfma_f32_16x16x32_bf16 v[8:11], v[120:123], v[48:51], v[8:11]\n\t" \
    "s_waitcnt lgkmcnt(4)\n\t" \
    "v_mfma_f32_16x16x32_bf16 v[12:15], v[128:131], v[48:51], v[12:15]\n\t" \
    "buffer_load_dwordx4 %[l16], %[rs], %[a] offen offset:3072 sc1 lds\n\t" \
    "s_waitcnt vmcnt(4)\n\t" \
    "s_barrier\n\t" \
    "s_setprio 1\n\t" \
    "s_add_u32 m0, %[sd], 0\n\t" \
    "s_add_u32 %[a], %[sx], 524288\n\t" \
    "s_add_u32 %[b], %[oo], 0x1000\n\t" \
    "s_cmp_lg_u32 %[t], 0\n\t" \
    "s_cselect_b64 %[em], -1, 0\n\t" \
    "v_mfma_f32_16x16x32_bf16 v[0:3], v[132:135], v[20:23], v[0:3]\n\t" \
    "ds_read_b128 v[44:47], %[xp] offset:22528\n\t" \
    "v_mfma_f32_16x16x32_bf16 v[4:7], v[140:143], v[20:23], v[4:7]\n\t" \
    "ds_read_b128 v[48:51], %[xp] offset:23552\n\t" \
    "v_mfma_f32_16x16x32_bf16 v[8:11], v[132:135], v[24:27], v[8:11]\n\t" \
    "ds_read_b128 v[52:55], %[pa] offset:0\n\t" \
    "s_waitcnt lgkmcnt(5)\n\t" \
    "v_mfma_f32_16x16x32_bf16 v[12:15], v[140:143], v[24:27], v[12:15]\n\t" \
    "buffer_load_dwordx4 %[l16], %[rs], %[a] offen offset:0 sc1 lds\n\t" \
    "v_mfma_f32_16x16x32_bf16 v[0:3], v[136:139], v[28:31], v[0:3]\n\t" \
    "ds_read_b128 v[20:23], %[xp] offset:24576\n\t" \
    "v_mfma_f32_16x16x32_bf16 v[4:7], v[144:147], v[28:31], v[4:7]\n\t" \
    "ds_read_b128 v[24:27], %[xp] offset:25600\n\t" \
    "v_mfma_f32_16x16x32_bf16 v[8:11], v[136:139], v[32:35], v[8:11]\n\t" \
    "ds_read_b128 v[56:59], %[pa] offset:1024\n\t" \
    "s_waitcnt lgkmcnt(6)\n\t" \
    "v_mfma_f32_16x16x32_bf16 v[12:15], v[144:147], v[32:35], v[12:15]\n\t" \
    "buffer_load_dwordx4 %[l16], %[rs], %[a] offen offset:1024 sc1 lds\n\t" \
    "v_mfma_f32_16x16x32_bf16 v[0:3], v[148:151], v[36:39], v[0:3]\n\t" \
    "ds_read_b128 v[28:31], %[xp] offset:26624\n\t" \
    "v_mfma_f32_16x16x32_bf16 v[4:7], v[156:159], v[36:39], v[4:7]\n\t" \
    "ds_read_b128 v[32:35], %[xp] offset:27648\n\t" \
    "v_mfma_f32_16x16x32_bf16 v[8:11], v[148:151], v[40:43], v[8:11]\n\t" \
    "ds_read_b128 v[60:63], %[pa] offset:2048\n\t" \
    "s_waitcnt lgkmcnt(7)\n\t" \
    "v_mfma_f32_16x16x32_bf16 v[12:15], v[156:159], v[40:43], v[12:15]\n\t" \
    "buffer_load_dwordx4 %[l16], %[rs], %[a] offen offset:2048 sc1 lds\n\t" \
    "v_mfma_f32_16x16x32_bf16 v[0:3], v[152:155], v[44:47], v[0:3]\n\t" \
    "ds_read_b128 v[36:39], %[xp] offset:28672\n\t" \
    "s_waitcnt lgkmcnt(7)\n\t" \
    "v_add_f32 v196, v196, v52\n\t" \
    "v_mfma_f32_16x16x32_bf16 v[4:7], v[160:163], v[44:47], v[4:7]\n\t" \
    "ds_read_b128 v[40:43], %[xp] offset:29696\n\t" \
    "v_add_f32 v197, v197, v53\n\t" \
    "v_mfma_f32_16x16x32_bf16 v[8:11], v[152:155], v[48:51], v[8:11]\n\t" \
    "ds_read_b128 v[64:67], %[pa] offset:3072\n\t" \
    "s_waitcnt lgkmcnt(7)\n\t" \
    "v_mfma_f32_16x16x32_bf16 v[12:15], v[160:163], v[48:51], v[12:15]\n\t" \
    "buffer_load_dwordx4 %[l16], %[rs], %[a] offen offset:3072 sc1 lds\n\t" \
    "v_add_f32 v198, v198, v54\n\t" \
    "v_add_f32 v199, v199, v55\n\t" \
    "v_mfma_f32_16x16x32_bf16 v[0:3], v[164:167], v[20:23], v[0:3]\n\t" \
    "ds_read_b128 v[44:47], %[xp] offset:30720\n\t" \
    "s_waitcnt lgkmcnt(7)\n\t" \
    "v_add_f32 v200, v200, v56\n\t" \
    "v_mfma_f32_16x16x32_bf16 v[4:7], v[172:175], v[20:23], v[4:7]\n\t" \
    "ds_read_b128 v[48:51], %[xp] offset:31744\n\t" \
    "v_add_f32 v201, v201, v57\n\t" \
    "v_mfma_f32_16x16x32_bf16 v[8:11], v[164:167], v[24:27], v[8:11]\n\t" \
    "v_add_f32 v202, v202, v58\n\t" \
    "s_waitcnt lgkmcnt(6)\n\t" \
    "v_mfma_f32_16x16x32_bf16 v[12:15], v[172:175], v[24:27], v[12:15]\n\t" \
    "v_add_f32 v203, v203, v59\n\t" \
    "s_waitcnt lgkmcnt(5)\n\t" \
    "v_add_f32 v204, v204, v60\n\t" \
    "v_add_f32 v205, v205, v61\n\t" \
    "v_mfma_f32_16x16x32_bf16 v[0:3], v[168:171], v[28:31], v[0:3]\n\t" \
    "ds_read_b128 v[20:23], %[xp] offset:32768\n\t" \
    "v_add_f32 v206, v206, v62\n\t" \
    "v_mfma_f32_16x16x32_bf16 v[4:7], v[176:179], v[28:31], v[4:7]\n\t" \
    "ds_read_b128 v[24:27], %[xp] offset:33792\n\t" \
    "v_add_f32 v207, v207, v63\n\t" \
    "v_mfma_f32_16x16x32_bf16 v[8:11], v[168:171], v[32:35], v[8:11]\n\t" \
    "s_waitcnt lgkmcnt(4)\n\t" \
    "v_add_f32 v208, v208, v64\n\t" \
    "v_mfma_f32_16x16x32_bf16 v[12:15], v[176:179], v[32:35], v[12:15]\n\t" \
    "v_add_f32 v209, v209, v65\n\t" \
    "v_add_f32 v210, v210, v66\n\t" \
    "v_add_f32 v211, v211, v67\n\t" \
    "v_mfma_f32_16x16x32_bf16 v[0:3], v[180:183], v[36:39], v[0:3]\n\t" \
    "ds_read_b128 v[28:31], %[xp] offset:34816\n\t" \
    "v_mfma_f32_16x16x32_bf16 v[4:7], v[188:191], v[36:39], v[4:7]\n\t" \
    "ds_read_b128 v[32:35], %[xp] offset:35840\n\t" \
    "s_mov_b64 exec, %[em]\n\t" \
    "buffer_store_dwordx4 v[196:199], %[l16], %[rs], %[oo] offen offset:0 sc1\n\t" \
    "s_mov_b64 exec, -1\n\t" \
    "v_mfma_f32_16x16x32_bf16 v[8:11], v[180:183], v[40:43], v[8:11]\n\t" \
    "s_waitcnt lgkmcnt(4)\n\t" \
    "v_mfma_f32_16x16x32_bf16 v[12:15], v[188:191], v[40:43], v[12:15]\n\t" \
    "s_mov_b64 exec, %[em]\n\t" \
    "buffer_store_dwordx4 v[200:203], %[l16], %[rs], %[oo] offen offset:1024 sc1\n\t" \
    "s_mov_b64 exec, -1\n\t" \
    "v_mfma_f32_16x16x32_bf16 v[0:3], v[184:187], v[44:47], v[0:3]\n\t" \
    "ds_read_b128 v[36:39], %[xp] offset:36864\n\t" \
    "v_mfma_f32_16x16x32_bf16 v[4:7], v[192:195], v[44:47], v[4:7]\n\t" \
    "ds_read_b128 v[40:43], %[xp] offset:37888\n\t" \
    "s_mov_b64 exec, %[em]\n\t" \
    "buffer_store_dwordx4 v[204:207], %[l16], %[rs], %[oo] offen offset:2048 sc1\n\t" \
    "s_mov_b64 exec, -1\n\t" \
    "v_mfma_f32_16x16x32_bf16 v[8:11], v[184:187], v[48:51], v[8:11]\n\t" \
    "s_waitcnt lgkmcnt(4)\n\t" \
    "v_mfma_f32_16x16x32_bf16 v[12:15], v[192:195], v[48:51], v[12:15]\n\t" \
    "s_mov_b64 exec, %[em]\n\t" \
    "buffer_store_dwordx4 v[208:211], %[l16], %[rs], %[oo] offen offset:3072 sc1\n\t" \
    "s_mov_b64 exec, -1\n\t" \
    "s_add_u32 %[sx], %[sx], 262144\n\t" \
    "s_add_u32 %[t], %[t], 1\n\t" \
    "s_add_u32 %[oo], %[oo], 524288\n\t" \
    "s_waitcnt vmcnt(8)\n\t" \
    "s_cmp_gt_u32 %[t], " STR(SQUICK) " + 1\n\t" \
    "s_cbranch_scc1 B0_%=\n\t" \
    "s_waitcnt vmcnt(0)\n\t" \
    "B0_%=:\n\t" \
    "s_barrier\n\t" \
    "s_setprio 1\n\t" \
    "s_add_u32 m0, %[sd], 16384\n\t" \
    "s_add_u32 %[a], %[sx], 278528\n\t" \
    "v_mfma_f32_16x16x32_bf16 v[196:199], v[68:71], v[20:23], 0\n\t" \
    "ds_read_b128 v[44:47], %[xp] offset:38912\n\t" \
    "v_mfma_f32_16x16x32_bf16 v[200:203], v[76:79], v[20:23], 0\n\t" \
    "ds_read_b128 v[48:51], %[xp] offset:39936\n\t" \
    "v_mfma_f32_16x16x32_bf16 v[204:207], v[68:71], v[24:27], 0\n\t" \
    "s_waitcnt lgkmcnt(4)\n\t" \
    "v_mfma_f32_16x16x32_bf16 v[208:211], v[76:79], v[24:27], 0\n\t" \
    "v_mfma_f32_16x16x32_bf16 v[196:199], v[72:75], v[28:31], v[196:199]\n\t" \
    "ds_read_b128 v[20:23], %[xp] offset:40960\n\t" \
    "v_mfma_f32_16x16x32_bf16 v[200:203], v[80:83], v[28:31], v[200:203]\n\t" \
    "ds_read_b128 v[24:27], %[xp] offset:41984\n\t" \
    "v_mfma_f32_16x16x32_bf16 v[204:207], v[72:75], v[32:35], v[204:207]\n\t" \
    "s_waitcnt lgkmcnt(4)\n\t" \
    "v_mfma_f32_16x16x32_bf16 v[208:211], v[80:83], v[32:35], v[208:211]\n\t" \
    "v_mfma_f32_16x16x32_bf16 v[196:199], v[84:87], v[36:39], v[196:199]\n\t" \
    "ds_read_b128 v[28:31], %[xp] offset:43008\n\t" \
    "v_mfma_f32_16x16x32_bf16 v[200:203], v[92:95], v[36:39], v[200:203]\n\t" \
    "ds_read_b128 v[32:35], %[xp] offset:44032\n\t" \
    "v_mfma_f32_16x16x32_bf16 v[204:207], v[84:87], v[40:43], v[204:207]\n\t" \
    "s_waitcnt lgkmcnt(4)\n\t" \
    "v_mfma_f32_16x16x32_bf16 v[208:211], v[92:95], v[40:43], v[208:211]\n\t" \
    "v_mfma_f32_16x16x32_bf16 v[196:199], v[88:91], v[44:47], v[196:199]\n\t" \
    "ds_read_b128 v[36:39], %[xp] offset:45056\n\t" \
    "v_mfma_f32_16x16x32_bf16 v[200:203], v[96:99], v[44:47], v[200:203]\n\t" \
    "ds_read_b128 v[40:43], %[xp] offset:46080\n\t" \
    "v_mfma_f32_16x16x32_bf16 v[204:207], v[88:91], v[48:51], v[204:207]\n\t" \
    "s_waitcnt lgkmcnt(4)\n\t" \
    "v_mfma_f32_16x16x32_bf16 v[208:211], v[96:99], v[48:51], v[208:211]\n\t" \
    "v_mfma_f32_16x16x32_bf16 v[196:199], v[100:103], v[20:23], v[196:199]\n\t" \
    "ds_read_b128 v[44:47], %[xp] offset:47104\n\t" \
    "v_mfma_f32_16x16x32_bf16 v[200:203], v[108:111], v[20:23], v[200:203]\n\t" \
    "ds_read_b128 v[48:51], %[xp] offset:48128\n\t" \
    "v_mfma_f32_16x16x32_bf16 v[204:207], v[100:103], v[24:27], v[204:207]\n\t" \
    "s_waitcnt lgkmcnt(4)\n\t" \
    "v_mfma_f32_16x16x32_bf16 v[208:211], v[108:111], v[24:27], v[208:211]\n\t" \
    "buffer_load_dwordx4 %[l16], %[rs], %[a] offen offset:0 sc1 lds\n\t" \
    "v_mfma_f32_16x16x32_bf16 v[196:199], v[104:107], v[28:31], v[196:199]\n\t" \
    "ds_read_b128 v[20:23], %[xp] offset:49152\n\t" \
    "v_mfma_f32_16x16x32_bf16 v[200:203], v[112:115], v[28:31], v[200:203]\n\t" \
    "ds_read_b128 v[24:27], %[xp] offset:50176\n\t" \
    "v_mfma_f32_16x16x32_bf16 v[204:207], v[104:107], v[32:35], v[204:207]\n\t" \
    "s_waitcnt lgkmcnt(4)\n\t" \
    "v_mfma_f32_16x16x32_bf16 v[208:211], v[112:115], v[32:35], v[208:211]\n\t" \
    "buffer_load_dwordx4 %[l16], %[rs], %[a] offen offset:1024 sc1 lds\n\t" \
    "v_mfma_f32_16x16x32_bf16 v[196:199], v[116:119], v[36:39], v[196:199]\n\t" \
    "ds_read_b128 v[28:31], %[xp] offset:51200\n\t" \
    "v_mfma_f32_16x16x32_bf16 v[200:203], v[124:127], v[36:39], v[200:203]\n\t" \
    "ds_read_b128 v[32:35], %[xp] offset:52224\n\t" \
    "v_mfma_f32_16x16x32_bf16 v[204:207], v[116:119], v[40:43], v[204:207]\n\t" \
    "s_waitcnt lgkmcnt(4)\n\t" \
    "v_mfma_f32_16x16x32_bf16 v[208:211], v[124:127], v[40:43], v[208:211]\n\t" \
    "buffer_load_dwordx4 %[l16], %[rs], %[a] offen offset:2048 sc1 lds\n\t" \
    "v_mfma_f32_16x16x32_bf16 v[196:199], v[120:123], v[44:47], v[196:199]\n\t" \
    "ds_read_b128 v[36:39], %[xp] offset:53248\n\t" \
    "v_mfma_f32_16x16x32_bf16 v[200:203], v[128:131], v[44:47], v[200:203]\n\t" \
    "ds_read_b128 v[40:43], %[xp] offset:54272\n\t" \
    "v_mfma_f32_16x16x32_bf16 v[204:207], v[120:123], v[48:51], v[204:207]\n\t" \
    "s_waitcnt lgkmcnt(4)\n\t" \
    "v_mfma_f32_16x16x32_bf16 v[208:211], v[128:131], v[48:51], v[208:211]\n\t" \
    "buffer_load_dwordx4 %[l16], %[rs], %[a] offen offset:3072 sc1 lds\n\t" \
    "s_waitcnt vmcnt(4)\n\t" \
    "s_barrier\n\t" \
    "s_setprio 1\n\t" \
    "s_add_u32 m0, %[sd], 32768\n\t" \
    "s_add_u32 %[a], %[sx], 524288\n\t" \
    "s_add_u32 %[b], %[oo], 0x1000\n\t" \
    "s_cmp_lg_u32 %[t], 0\n\t" \
    "s_cselect_b64 %[em], -1, 0\n\t" \
    "v_mfma_f32_16x16x32_bf16 v[196:199], v[132:135], v[20:23], v[196:199]\n\t" \
    "ds_read_b128 v[44:47], %[xp] offset:55296\n\t" \
    "v_mfma_f32_16x16x32_bf16 v[200:203], v[140:143], v[20:23], v[200:203]\n\t" \
    "ds_read_b128 v[48:51], %[xp] offset:56320\n\t" \
    "v_mfma_f32_16x16x32_bf16 v[204:207], v[132:135], v[24:27], v[204:207]\n\t" \
    "ds_read_b128 v[52:55], %[pa] offset:0\n\t" \
    "s_waitcnt lgkmcnt(5)\n\t" \
    "v_mfma_f32_16x16x32_bf16 v[208:211], v[140:143], v[24:27], v[208:211]\n\t" \
    "buffer_load_dwordx4 %[l16], %[rs], %[a] offen offset:0 sc1 lds\n\t" \
    "v_mfma_f32_16x16x32_bf16 v[196:199], v[136:139], v[28:31], v[196:199]\n\t" \
    "ds_read_b128 v[20:23], %[xp] offset:57344\n\t" \
    "v_mfma_f32_16x16x32_bf16 v[200:203], v[144:147], v[28:31], v[200:203]\n\t" \
    "ds_read_b128 v[24:27], %[xp] offset:58368\n\t" \
    "v_mfma_f32_16x16x32_bf16 v[204:207], v[136:139], v[32:35], v[204:207]\n\t" \
    "ds_read_b128 v[56:59], %[pa] offset:1024\n\t" \
    "s_waitcnt lgkmcnt(6)\n\t" \
    "v_mfma_f32_16x16x32_bf16 v[208:211], v[144:147], v[32:35], v[208:211]\n\t" \
    "buffer_load_dwordx4 %[l16], %[rs], %[a] offen offset:1024 sc1 lds\n\t" \
    "v_mfma_f32_16x16x32_bf16 v[196:199], v[148:151], v[36:39], v[196:199]\n\t" \
    "ds_read_b128 v[28:31], %[xp] offset:59392\n\t" \
    "v_mfma_f32_16x16x32_bf16 v[200:203], v[156:159], v[36:39], v[200:203]\n\t" \
    "ds_read_b128 v[32:35], %[xp] offset:60416\n\t" \
    "v_mfma_f32_16x16x32_bf16 v[204:207], v[148:151], v[40:43], v[204:207]\n\t" \
    "ds_read_b128 v[60:63], %[pa] offset:2048\n\t" \
    "s_waitcnt lgkmcnt(7)\n\t" \
    "v_mfma_f32_16x16x32_bf16 v[208:211], v[156:159], v[40:43], v[208:211]\n\t" \
    "buffer_load_dwordx4 %[l16], %[rs], %[a] offen offset:2048 sc1 lds\n\t" \
    "v_mfma_f32_16x16x32_bf16 v[196:199], v[152:155], v[44:47], v[196:199]\n\t" \
    "ds_read_b128 v[36:39], %[xp] offset:61440\n\t" \
    "s_waitcnt lgkmcnt(7)\n\t" \
    "v_add_f32 v0, v0, v52\n\t" \
    "v_mfma_f32_16x16x32_bf16 v[200:203], v[160:163], v[44:47], v[200:203]\n\t" \
    "ds_read_b128 v[40:43], %[xp] offset:62464\n\t" \
    "v_add_f32 v1, v1, v53\n\t" \
    "v_mfma_f32_16x16x32_bf16 v[204:207], v[152:155], v[48:51], v[204:207]\n\t" \
    "ds_read_b128 v[64:67], %[pa] offset:3072\n\t" \
    "s_waitcnt lgkmcnt(7)\n\t" \
    "v_mfma_f32_16x16x32_bf16 v[208:211], v[160:163], v[48:51], v[208:211]\n\t" \
    "buffer_load_dwordx4 %[l16], %[rs], %[a] offen offset:3072 sc1 lds\n\t" \
    "v_add_f32 v2, v2, v54\n\t" \
    "v_add_f32 v3, v3, v55\n\t" \
    "v_mfma_f32_16x16x32_bf16 v[196:199], v[164:167], v[20:23], v[196:199]\n\t" \
    "ds_read_b128 v[44:47], %[xp] offset:63488\n\t" \
    "s_waitcnt lgkmcnt(7)\n\t" \
    "v_add_f32 v4, v4, v56\n\t" \
    "v_mfma_f32_16x16x32_bf16 v[200:203], v[172:175], v[20:23], v[200:203]\n\t" \
    "ds_read_b128 v[48:51], %[xp] offset:64512\n\t" \
    "v_add_f32 v5, v5, v57\n\t" \
    "v_mfma_f32_16x16x32_bf16 v[204:207], v[164:167], v[24:27], v[204:207]\n\t" \
    "v_add_f32 v6, v6, v58\n\t" \
    "s_waitcnt lgkmcnt(6)\n\t" \
    "v_mfma_f32_16x16x32_bf16 v[208:211], v[172:175], v[24:27], v[208:211]\n\t" \
    "v_add_f32 v7, v7, v59\n\t" \
    "s_waitcnt lgkmcnt(5)\n\t" \
    "v_add_f32 v8, v8, v60\n\t" \
    "v_add_f32 v9, v9, v61\n\t" \
    "v_mfma_f32_16x16x32_bf16 v[196:199], v[168:171], v[28:31], v[196:199]\n\t" \
    "ds_read_b128 v[20:23], %[xp] offset:0\n\t" \
    "v_add_f32 v10, v10, v62\n\t" \
    "v_mfma_f32_16x16x32_bf16 v[200:203], v[176:179], v[28:31], v[200:203]\n\t" \
    "ds_read_b128 v[24:27], %[xp] offset:1024\n\t" \
    "v_add_f32 v11, v11, v63\n\t" \
    "v_mfma_f32_16x16x32_bf16 v[204:207], v[168:171], v[32:35], v[204:207]\n\t" \
    "s_waitcnt lgkmcnt(4)\n\t" \
    "v_add_f32 v12, v12, v64\n\t" \
    "v_mfma_f32_16x16x32_bf16 v[208:211], v[176:179], v[32:35], v[208:211]\n\t" \
    "v_add_f32 v13, v13, v65\n\t" \
    "v_add_f32 v14, v14, v66\n\t" \
    "v_add_f32 v15, v15, v67\n\t" \
    "v_mfma_f32_16x16x32_bf16 v[196:199], v[180:183], v[36:39], v[196:199]\n\t" \
    "ds_read_b128 v[28:31], %[xp] offset:2048\n\t" \
    "v_mfma_f32_16x16x32_bf16 v[200:203], v[188:191], v[36:39], v[200:203]\n\t" \
    "ds_read_b128 v[32:35], %[xp] offset:3072\n\t" \
    "s_mov_b64 exec, %[em]\n\t" \
    "buffer_store_dwordx4 v[0:3], %[l16], %[rs], %[oo] offen offset:0 sc1\n\t" \
    "s_mov_b64 exec, -1\n\t" \
    "v_mfma_f32_16x16x32_bf16 v[204:207], v[180:183], v[40:43], v[204:207]\n\t" \
    "s_waitcnt lgkmcnt(4)\n\t" \
    "v_mfma_f32_16x16x32_bf16 v[208:211], v[188:191], v[40:43], v[208:211]\n\t" \
    "s_mov_b64 exec, %[em]\n\t" \
    "buffer_store_dwordx4 v[4:7], %[l16], %[rs], %[oo] offen offset:1024 sc1\n\t" \
    "s_mov_b64 exec, -1\n\t" \
    "v_mfma_f32_16x16x32_bf16 v[196:199], v[184:187], v[44:47], v[196:199]\n\t" \
    "ds_read_b128 v[36:39], %[xp] offset:4096\n\t" \
    "v_mfma_f32_16x16x32_bf16 v[200:203], v[192:195], v[44:47], v[200:203]\n\t" \
    "ds_read_b128 v[40:43], %[xp] offset:5120\n\t" \
    "s_mov_b64 exec, %[em]\n\t" \
    "buffer_store_dwordx4 v[8:11], %[l16], %[rs], %[oo] offen offset:2048 sc1\n\t" \
    "s_mov_b64 exec, -1\n\t" \
    "v_mfma_f32_16x16x32_bf16 v[204:207], v[184:187], v[48:51], v[204:207]\n\t" \
    "s_waitcnt lgkmcnt(4)\n\t" \
    "v_mfma_f32_16x16x32_bf16 v[208:211], v[192:195], v[48:51], v[208:211]\n\t" \
    "s_mov_b64 exec, %[em]\n\t" \
    "buffer_store_dwordx4 v[12:15], %[l16], %[rs], %[oo] offen offset:3072 sc1\n\t" \
    "s_mov_b64 exec, -1\n\t" \
    "s_add_u32 %[sx], %[sx], 262144\n\t" \
    "s_add_u32 %[t], %[t], 1\n\t" \
    "s_add_u32 %[oo], %[oo], 524288\n\t" \
    "s_waitcnt vmcnt(8)\n\t" \
    "s_cmp_gt_u32 %[t], " STR(SQUICK) " + 1\n\t" \
    "s_cbranch_scc1 B1_%=\n\t" \
    "s_waitcnt vmcnt(0)\n\t" \
    "B1_%=:\n\t" \
    "s_barrier\n\t" \
    "s_cmp_lt_u32 %[t], %[te]\n\t" \
    "s_cbranch_scc1 L1_%=\n\t" \
    "s_cmp_eq_u32 %[fin], %[te]\n\t" \
    "s_cbranch_scc0 E_%=\n\t" \
    "s_nop 4\n\t" \
    "s_barrier\n\t" \
    "ds_read_b128 v[52:55], %[pa] offset:0\n\t" \
    "ds_read_b128 v[56:59], %[pa] offset:1024\n\t" \
    "ds_read_b128 v[60:63], %[pa] offset:2048\n\t" \
    "ds_read_b128 v[64:67], %[pa] offset:3072\n\t" \
    "s_add_u32 %[b], %[oo], 0x1000\n\t" \
    "s_waitcnt lgkmcnt(0)\n\t" \
    "v_add_f32 v196, v196, v52\n\t" \
    "v_add_f32 v197, v197, v53\n\t" \
    "v_add_f32 v198, v198, v54\n\t" \
    "v_add_f32 v199, v199, v55\n\t" \
    "v_add_f32 v200, v200, v56\n\t" \
    "v_add_f32 v201, v201, v57\n\t" \
    "v_add_f32 v202, v202, v58\n\t" \
    "v_add_f32 v203, v203, v59\n\t" \
    "v_add_f32 v204, v204, v60\n\t" \
    "v_add_f32 v205, v205, v61\n\t" \
    "v_add_f32 v206, v206, v62\n\t" \
    "v_add_f32 v207, v207, v63\n\t" \
    "v_add_f32 v208, v208, v64\n\t" \
    "v_add_f32 v209, v209, v65\n\t" \
    "v_add_f32 v210, v210, v66\n\t" \
    "v_add_f32 v211, v211, v67\n\t" \
    "buffer_store_dwordx4 v[196:199], %[l16], %[rs], %[oo] offen offset:0 sc1\n\t" \
    "buffer_store_dwordx4 v[200:203], %[l16], %[rs], %[oo] offen offset:1024 sc1\n\t" \
    "buffer_store_dwordx4 v[204:207], %[l16], %[rs], %[oo] offen offset:2048 sc1\n\t" \
    "buffer_store_dwordx4 v[208:211], %[l16], %[rs], %[oo] offen offset:3072 sc1\n\t" \
    "E_%=:\n\t" \
    "s_setprio 0\n\t" \
    "s_waitcnt lgkmcnt(0)\n\t" \
    "s_nop 7\n\t" \
    : [t] "+s"(t), [sx] "+s"(sx), [oo] "+s"(oo), [a] "=&s"(tmp), [b] "=&s"(tmp2), [em] "=&s"(em), "+{v[20:27]}"(xf[0]), "+{v[28:35]}"(xf[1]), "+{v[36:43]}"(xf[2]), "+{v[196:203]}"(ac[0]), "+{v[204:211]}"(ac[1]) \
    : [te] "s"(TE_), [fin] "s"(FIN_), [kh] "s"(kh), [sd] "s"(sd), [rs] "s"(rs), [l16] "v"(l16), [xp] "v"(xp), [pa] "v"(pa), [fm] "s"(fm), [pm] "s"(pm), [fo] "s"(fo), [po] "s"(po), [pd] "s"(pd), [tag] "s"(P.tag), "{v[68:75]}"(w[0][0]), "{v[76:83]}"(w[0][1]), "{v[84:91]}"(w[1][0]), "{v[92:99]}"(w[1][1]), "{v[100:107]}"(w[2][0]), "{v[108:115]}"(w[2][1]), "{v[116:123]}"(w[3][0]), "{v[124:131]}"(w[3][1]), "{v[132:139]}"(w[4][0]), "{v[140:147]}"(w[4][1]), "{v[148:155]}"(w[5][0]), "{v[156:163]}"(w[5][1]), "{v[164:171]}"(w[6][0]), "{v[172:179]}"(w[6][1]), "{v[180:187]}"(w[7][0]), "{v[188:195]}"(w[7][1]) \
    : "memory", "scc", "v0", "v1", "v2", "v3", "v4", "v5", "v6", "v7", "v8", "v9", "v10", "v11", "v12", "v13", "v14", "v15", "v44", "v45", "v46", "v47", "v48", "v49", "v50", "v51", "v52", "v53", "v54", "v55", "v56", "v57", "v58", "v59", "v60", "v61", "v62", "v63", "v64", "v65", "v66", "v67")

#define X_ONE() \
  asm volatile( \
    "ds_read_b128 v[20:23], %[xp] offset:0\n\t" \
    "ds_read_b128 v[24:27], %[xp] offset:1024\n\t" \
    "ds_read_b128 v[28:31], %[xp] offset:2048\n\t" \
    "ds_read_b128 v[32:35], %[xp] offset:3072\n\t" \
    "ds_read_b128 v[36:39], %[xp] offset:4096\n\t" \
    "ds_read_b128 v[40:43], %[xp] offset:5120\n\t" \
    "s_waitcnt lgkmcnt(4)\n\t" \
    "v_mfma_f32_16x16x32_bf16 v[0:3], v[68:71], v[20:23], 0\n\t" \
    "ds_read_b128 v[44:47], %[xp] offset:6144\n\t" \
    "v_mfma_f32_16x16x32_bf16 v[4:7], v[76:79], v[20:23], 0\n\t" \
    "ds_read_b128 v[48:51], %[xp] offset:7168\n\t" \
    "v_mfma_f32_16x16x32_bf16 v[8:11], v[68:71], v[24:27], 0\n\t" \
    "s_waitcnt lgkmcnt(4)\n\t" \
    "v_mfma_f32_16x16x32_bf16 v[12:15], v[76:79], v[24:27], 0\n\t" \
    "v_mfma_f32_16x16x32_bf16 v[0:3], v[72:75], v[28:31], v[0:3]\n\t" \
    "ds_read_b128 v[20:23], %[xp] offset:8192\n\t" \
    "v_mfma_f32_16x16x32_bf16 v[4:7], v[80:83], v[28:31], v[4:7]\n\t" \
    "ds_read_b128 v[24:27], %[xp] offset:9216\n\t" \
    "v_mfma_f32_16x16x32_bf16 v[8:11], v[72:75], v[32:35], v[8:11]\n\t" \
    "s_waitcnt lgkmcnt(4)\n\t" \
    "v_mfma_f32_16x16x32_bf16 v[12:15], v[80:83], v[32:35], v[12:15]\n\t" \
    "v_mfma_f32_16x16x32_bf16 v[0:3], v[84:87], v[36:39], v[0:3]\n\t" \
    "ds_read_b128 v[28:31], %[xp] offset:10240\n\t" \
    "v_mfma_f32_16x16x32_bf16 v[4:7], v[92:95], v[36:39], v[4:7]\n\t" \
    "ds_read_b128 v[32:35], %[xp] offset:11264\n\t" \
    "v_mfma_f32_16x16x32_bf16 v[8:11], v[84:87], v[40:43], v[8:11]\n\t" \
    "s_waitcnt lgkmcnt(4)\n\t" \
    "v_mfma_f32_16x16x32_bf16 v[12:15], v[92:95], v[40:43], v[12:15]\n\t" \
    "v_mfma_f32_16x16x32_bf16 v[0:3], v[88:91], v[44:47], v[0:3]\n\t" \
    "ds_read_b128 v[36:39], %[xp] offset:12288\n\t" \
    "v_mfma_f32_16x16x32_bf16 v[4:7], v[96:99], v[44:47], v[4:7]\n\t" \
    "ds_read_b128 v[40:43], %[xp] offset:13312\n\t" \
    "v_mfma_f32_16x16x32_bf16 v[8:11], v[88:91], v[48:51], v[8:11]\n\t" \
    "s_waitcnt lgkmcnt(4)\n\t" \
    "v_mfma_f32_16x16x32_bf16 v[12:15], v[96:99], v[48:51], v[12:15]\n\t" \
    "v_mfma_f32_16x16x32_bf16 v[0:3], v[100:103], v[20:23], v[0:3]\n\t" \
    "ds_read_b128 v[44:47], %[xp] offset:14336\n\t" \
    "v_mfma_f32_16x16x32_bf16 v[4:7], v[108:111], v[20:23], v[4:7]\n\t" \
    "ds_read_b128 v[48:51], %[xp] offset:15360\n\t" \
    "v_mfma_f32_16x16x32_bf16 v[8:11], v[100:103], v[24:27], v[8:11]\n\t" \
    "s_waitcnt lgkmcnt(4)\n\t" \
    "v_mfma_f32_16x16x32_bf16 v[12:15], v[108:111], v[24:27], v[12:15]\n\t" \
    "s_waitcnt vmcnt(0)\n\t" \
    "s_barrier\n\t" \
    "v_mfma_f32_16x16x32_bf16 v[0:3], v[104:107], v[28:31], v[0:3]\n\t" \
    "ds_read_b128 v[20:23], %[xp] offset:16384\n\t" \
    "v_mfma_f32_16x16x32_bf16 v[4:7], v[112:115], v[28:31], v[4:7]\n\t" \
    "ds_read_b128 v[24:27], %[xp] offset:17408\n\t" \
    "v_mfma_f32_16x16x32_bf16 v[8:11], v[104:107], v[32:35], v[8:11]\n\t" \
    "s_waitcnt lgkmcnt(4)\n\t" \
    "v_mfma_f32_16x16x32_bf16 v[12:15], v[112:115], v[32:35], v[12:15]\n\t" \
    "buffer_load_dword v52, %[pv], %[rs], 0 offen sc1\n\t" \
    "v_mfma_f32_16x16x32_bf16 v[0:3], v[116:119], v[36:39], v[0:3]\n\t" \
    "ds_read_b128 v[28:31], %[xp] offset:18432\n\t" \
    "v_mfma_f32_16x16x32_bf16 v[4:7], v[124:127], v[36:39], v[4:7]\n\t" \
    "ds_read_b128 v[32:35], %[xp] offset:19456\n\t" \
    "v_mfma_f32_16x16x32_bf16 v[8:11], v[116:119], v[40:43], v[8:11]\n\t" \
    "s_waitcnt lgkmcnt(4)\n\t" \
    "v_mfma_f32_16x16x32_bf16 v[12:15], v[124:127], v[40:43], v[12:15]\n\t" \
    "v_mfma_f32_16x16x32_bf16 v[0:3], v[120:123], v[44:47], v[0:3]\n\t" \
    "ds_read_b128 v[36:39], %[xp] offset:20480\n\t" \
    "v_mfma_f32_16x16x32_bf16 v[4:7], v[128:131], v[44:47], v[4:7]\n\t" \
    "ds_read_b128 v[40:43], %[xp] offset:21504\n\t" \
    "v_mfma_f32_16x16x32_bf16 v[8:11], v[120:123], v[48:51], v[8:11]\n\t" \
    "s_waitcnt lgkmcnt(4)\n\t" \
    "v_mfma_f32_16x16x32_bf16 v[12:15], v[128:131], v[48:51], v[12:15]\n\t" \
    "v_mfma_f32_16x16x32_bf16 v[0:3], v[132:135], v[20:23], v[0:3]\n\t" \
    "ds_read_b128 v[44:47], %[xp] offset:22528\n\t" \
    "v_mfma_f32_16x16x32_bf16 v[4:7], v[140:143], v[20:23], v[4:7]\n\t" \
    "ds_read_b128 v[48:51], %[xp] offset:23552\n\t" \
    "v_mfma_f32_16x16x32_bf16 v[8:11], v[132:135], v[24:27], v[8:11]\n\t" \
    "s_waitcnt lgkmcnt(4)\n\t" \
    "v_mfma_f32_16x16x32_bf16 v[12:15], v[140:143], v[24:27], v[12:15]\n\t" \
    "v_mfma_f32_16x16x32_bf16 v[0:3], v[136:139], v[28:31], v[0:3]\n\t" \
    "ds_read_b128 v[20:23], %[xp] offset:24576\n\t" \
    "v_mfma_f32_16x16x32_bf16 v[4:7], v[144:147], v[28:31], v[4:7]\n\t" \
    "ds_read_b128 v[24:27], %[xp] offset:25600\n\t" \
    "v_mfma_f32_16x16x32_bf16 v[8:11], v[136:139], v[32:35], v[8:11]\n\t" \
    "s_waitcnt lgkmcnt(4)\n\t" \
    "v_mfma_f32_16x16x32_bf16 v[12:15], v[144:147], v[32:35], v[12:15]\n\t" \
    "v_mfma_f32_16x16x32_bf16 v[0:3], v[148:151], v[36:39], v[0:3]\n\t" \
    "ds_read_b128 v[28:31], %[xp] offset:26624\n\t" \
    "v_mfma_f32_16x16x32_bf16 v[4:7], v[156:159], v[36:39], v[4:7]\n\t" \
    "ds_read_b128 v[32:35], %[xp] offset:27648\n\t" \
    "v_mfma_f32_16x16x32_bf16 v[8:11], v[148:151], v[40:43], v[8:11]\n\t" \
    "s_waitcnt lgkmcnt(4)\n\t" \
    "v_mfma_f32_16x16x32_bf16 v[12:15], v[156:159], v[40:43], v[12:15]\n\t" \
    "v_mfma_f32_16x16x32_bf16 v[0:3], v[152:155], v[44:47], v[0:3]\n\t" \
    "ds_read_b128 v[36:39], %[xp] offset:28672\n\t" \
    "v_mfma_f32_16x16x32_bf16 v[4:7], v[160:163], v[44:47], v[4:7]\n\t" \
    "ds_read_b128 v[40:43], %[xp] offset:29696\n\t" \
    "v_mfma_f32_16x16x32_bf16 v[8:11], v[152:155], v[48:51], v[8:11]\n\t" \
    "s_waitcnt lgkmcnt(4)\n\t" \
    "v_mfma_f32_16x16x32_bf16 v[12:15], v[160:163], v[48:51], v[12:15]\n\t" \
    "v_mfma_f32_16x16x32_bf16 v[0:3], v[164:167], v[20:23], v[0:3]\n\t" \
    "ds_read_b128 v[44:47], %[xp] offset:30720\n\t" \
    "v_mfma_f32_16x16x32_bf16 v[4:7], v[172:175], v[20:23], v[4:7]\n\t" \
    "ds_read_b128 v[48:51], %[xp] offset:31744\n\t" \
    "v_mfma_f32_16x16x32_bf16 v[8:11], v[164:167], v[24:27], v[8:11]\n\t" \
    "s_waitcnt lgkmcnt(4)\n\t" \
    "v_mfma_f32_16x16x32_bf16 v[12:15], v[172:175], v[24:27], v[12:15]\n\t" \
    "v_mfma_f32_16x16x32_bf16 v[0:3], v[168:171], v[28:31], v[0:3]\n\t" \
    "v_mfma_f32_16x16x32_bf16 v[4:7], v[176:179], v[28:31], v[4:7]\n\t" \
    "v_mfma_f32_16x16x32_bf16 v[8:11], v[168:171], v[32:35], v[8:11]\n\t" \
    "s_waitcnt lgkmcnt(2)\n\t" \
    "v_mfma_f32_16x16x32_bf16 v[12:15], v[176:179], v[32:35], v[12:15]\n\t" \
    "v_mfma_f32_16x16x32_bf16 v[0:3], v[180:183], v[36:39], v[0:3]\n\t" \
    "v_mfma_f32_16x16x32_bf16 v[4:7], v[188:191], v[36:39], v[4:7]\n\t" \
    "v_mfma_f32_16x16x32_bf16 v[8:11], v[180:183], v[40:43], v[8:11]\n\t" \
    "s_waitcnt lgkmcnt(0)\n\t" \
    "v_mfma_f32_16x16x32_bf16 v[12:15], v[188:191], v[40:43], v[12:15]\n\t" \
    "v_mfma_f32_16x16x32_bf16 v[0:3], v[184:187], v[44:47], v[0:3]\n\t" \
    "v_mfma_f32_16x16x32_bf16 v[4:7], v[192:195], v[44:47], v[4:7]\n\t" \
    "v_mfma_f32_16x16x32_bf16 v[8:11], v[184:187], v[48:51], v[8:11]\n\t" \
    "v_mfma_f32_16x16x32_bf16 v[12:15], v[192:195], v[48:51], v[12:15]\n\t" \
    "s_waitcnt vmcnt(0)\n\t" \
    "s_nop 7\n\t" \
    : "=&{v52}"(pg), "=&{v[0:3]}"(a[0]), "=&{v[4:7]}"(a[1]), "=&{v[8:11]}"(a[2]), "=&{v[12:15]}"(a[3]) \
    : [xp] "v"(xp), [pv] "v"(pv), [rs] "s"(rs), "{v[68:75]}"(w[0][0]), "{v[76:83]}"(w[0][1]), "{v[84:91]}"(w[1][0]), "{v[92:99]}"(w[1][1]), "{v[100:107]}"(w[2][0]), "{v[108:115]}"(w[2][1]), "{v[116:123]}"(w[3][0]), "{v[124:131]}"(w[3][1]), "{v[132:139]}"(w[4][0]), "{v[140:147]}"(w[4][1]), "{v[148:155]}"(w[5][0]), "{v[156:163]}"(w[5][1]), "{v[164:171]}"(w[6][0]), "{v[172:179]}"(w[6][1]), "{v[180:187]}"(w[7][0]), "{v[188:195]}"(w[7][1]) \
    : "memory", "v20", "v21", "v22", "v23", "v24", "v25", "v26", "v27", "v28", "v29", "v30", "v31", "v32", "v33", "v34", "v35", "v36", "v37", "v38", "v39", "v40", "v41", "v42", "v43", "v44", "v45", "v46", "v47", "v48", "v49", "v50", "v51")

#define H_LOOP1() \
  asm volatile( \
    "v_mov_b32 v20, 0\n\t" \
    "v_mov_b32 v21, 0\n\t" \
    "v_mov_b32 v22, 0\n\t" \
    "v_mov_b32 v23, 0\n\t" \
    "v_mov_b32 v24, 0\n\t" \
    "v_mov_b32 v25, 0\n\t" \
    "v_mov_b32 v26, 0\n\t" \
    "v_mov_b32 v27, 0\n\t" \
    "v_mov_b32 v28, 0\n\t" \
    "v_mov_b32 v29, 0\n\t" \
    "v_mov_b32 v30, 0\n\t" \
    "v_mov_b32 v31, 0\n\t" \
    "v_mov_b32 v32, 0\n\t" \
    "v_mov_b32 v33, 0\n\t" \
    "v_mov_b32 v34, 0\n\t" \
    "v_mov_b32 v35, 0\n\t" \
    "v_mov_b32 v36, 0\n\t" \
    "v_mov_b32 v37, 0\n\t" \
    "v_mov_b32 v38, 0\n\t" \
    "v_mov_b32 v39, 0\n\t" \
    "v_mov_b32 v40, 0\n\t" \
    "v_mov_b32 v41, 0\n\t" \
    "v_mov_b32 v42, 0\n\t" \
    "v_mov_b32 v43, 0\n\t" \
    "v_mov_b32 v44, 0\n\t" \
    "v_mov_b32 v45, 0\n\t" \
    "v_mov_b32 v46, 0\n\t" \
    "v_mov_b32 v47, 0\n\t" \
    "v_mov_b32 v48, 0\n\t" \
    "v_mov_b32 v49, 0\n\t" \
    "v_mov_b32 v50, 0\n\t" \
    "v_mov_b32 v51, 0\n\t" \
    "v_mov_b32 v52, 0\n\t" \
    "v_mov_b32 v53, 0\n\t" \
    "v_mov_b32 v54, 0\n\t" \
    "v_mov_b32 v55, 0\n\t" \
    "v_mov_b32 v56, 0\n\t" \
    "v_mov_b32 v57, 0\n\t" \
    "v_mov_b32 v58, 0\n\t" \
    "v_mov_b32 v59, 0\n\t" \
    "v_mov_b32 v60, 0\n\t" \
    "v_mov_b32 v61, 0\n\t" \
    "v_mov_b32 v62, 0\n\t" \
    "v_mov_b32 v63, 0\n\t" \
    "v_mov_b32 v64, 0\n\t" \
    "v_mov_b32 v65, 0\n\t" \
    "v_mov_b32 v66, 0\n\t" \
    "v_mov_b32 v67, 0\n\t" \
    "v_mov_b32 v68, 0\n\t" \
    "v_mov_b32 v69, 0\n\t" \
    "v_mov_b32 v70, 0\n\t" \
    "v_mov_b32 v71, 0\n\t" \
    "v_mov_b32 v72, 0\n\t" \
    "v_mov_b32 v73, 0\n\t" \
    "v_mov_b32 v74, 0\n\t" \
    "v_mov_b32 v75, 0\n\t" \
    "v_mov_b32 v76, 0\n\t" \
    "v_mov_b32 v77, 0\n\t" \
    "v_mov_b32 v78, 0\n\t" \
    "v_mov_b32 v79, 0\n\t" \
    "v_mov_b32 v80, 0\n\t" \
    "v_mov_b32 v81, 0\n\t" \
    "v_mov_b32 v82, 0\n\t" \
    "v_mov_b32 v83, 0\n\t" \
    "s_waitcnt vmcnt(8)\n\t" \
    "s_barrier\n\t" \
    "ds_read_b64_tr_b16 v[116:117], %[b0] offset:0\n\t" \
    "ds_read_b64_tr_b16 v[118:119], %[b0] offset:1024\n\t" \
    "ds_read_b64_tr_b16 v[124:125], %[b1] offset:0\n\t" \
    "ds_read_b64_tr_b16 v[126:127], %[b1] offset:1024\n\t" \
    "ds_read_b64_tr_b16 v[120:121], %[b0] offset:8192\n\t" \
    "ds_read_b64_tr_b16 v[122:123], %[b0] offset:9216\n\t" \
    "ds_read_b64_tr_b16 v[128:129], %[b1] offset:8192\n\t" \
    "ds_read_b64_tr_b16 v[130:131], %[b1] offset:9216\n\t" \
    "ds_read_b128 v[84:87], %[a] offset:0\n\t" \
    "ds_read_b128 v[88:91], %[a] offset:1024\n\t" \
    "ds_read_b128 v[92:95], %[a] offset:2048\n\t" \
    "ds_read_b128 v[96:99], %[a] offset:3072\n\t" \
    "ds_read_b128 v[100:103], %[a] offset:4096\n\t" \
    "ds_read_b128 v[104:107], %[a] offset:5120\n\t" \
    "ds_read_b128 v[108:111], %[a] offset:6144\n\t" \
    "ds_read_b128 v[112:115], %[a] offset:7168\n\t" \
    "L_%=:\n\t" \
    "s_add_u32 %[d], %[lw], %[c]\n\t" \
    "s_add_u32 %[y], %[c], 49152\n\t" \
    "s_cmp_eq_u32 %[y], 147456\n\t" \
    "s_cselect_b32 %[y], 0, %[y]\n\t" \
    "s_sub_u32 %[x], %[y], %[c]\n\t" \
    "s_mov_b32 %[c], %[y]\n\t" \
    "v_add_u32 %[a], %[x], %[a]\n\t" \
    "v_add_u32 %[b0], %[x], %[b0]\n\t" \
    "v_add_u32 %[b1], %[x], %[b1]\n\t" \
    "s_add_u32 %[x], %[t], 3\n\t" \
    "s_cmp_lt_u32 %[x], %[nt]\n\t" \
    "s_cselect_b32 %[x], %[x], 0\n\t" \
    "s_mul_i32 %[y], %[x], %[kb]\n\t" \
    "s_add_u32 %[y], %[y], %[cb]\n\t" \
    "s_lshl_b32 %[x], %[x], 11\n\t" \
    "s_add_u32 %[x], %[x], %[ca]\n\t" \
    "s_waitcnt vmcnt(4) lgkmcnt(0)\n\t" \
    "s_barrier\n\t" \
    "v_mfma_f32_16x16x32_bf16 v[20:23], v[116:119], v[84:87], v[20:23]\n\t" \
    "ds_read_b64_tr_b16 v[180:181], %[b0] offset:0\n\t" \
    "ds_read_b64_tr_b16 v[182:183], %[b0] offset:1024\n\t" \
    "s_add_u32 m0, %[d], 0\n\t" \
    "v_mfma_f32_16x16x32_bf16 v[24:27], v[124:127], v[84:87], v[24:27]\n\t" \
    "buffer_load_dwordx4 %[vb0], %[rb], %[y] offen lds\n\t" \
    "v_mfma_f32_16x16x32_bf16 v[20:23], v[120:123], v[88:91], v[20:23]\n\t" \
    "ds_read_b64_tr_b16 v[188:189], %[b1] offset:0\n\t" \
    "ds_read_b64_tr_b16 v[190:191], %[b1] offset:1024\n\t" \
    "v_mfma_f32_16x16x32_bf16 v[24:27], v[128:131], v[88:91], v[24:27]\n\t" \
    "ds_read_b64_tr_b16 v[184:185], %[b0] offset:8192\n\t" \
    "ds_read_b64_tr_b16 v[186:187], %[b0] offset:9216\n\t" \
    "v_mfma_f32_16x16x32_bf16 v[28:31], v[116:119], v[92:95], v[28:31]\n\t" \
    "ds_read_b64_tr_b16 v[192:193], %[b1] offset:8192\n\t" \
    "ds_read_b64_tr_b16 v[194:195], %[b1] offset:9216\n\t" \
    "s_add_u32 m0, %[d], 8192\n\t" \
    "v_mfma_f32_16x16x32_bf16 v[32:35], v[124:127], v[92:95], v[32:35]\n\t" \
    "buffer_load_dwordx4 %[vb1], %[rb], %[y] offen lds\n\t" \
    "v_mfma_f32_16x16x32_bf16 v[28:31], v[120:123], v[96:99], v[28:31]\n\t" \
    "ds_read_b128 v[148:151], %[a] offset:0\n\t" \
    "v_mfma_f32_16x16x32_bf16 v[32:35], v[128:131], v[96:99], v[32:35]\n\t" \
    "ds_read_b128 v[152:155], %[a] offset:1024\n\t" \
    "v_mfma_f32_16x16x32_bf16 v[36:39], v[116:119], v[100:103], v[36:39]\n\t" \
    "ds_read_b128 v[156:159], %[a] offset:2048\n\t" \
    "s_add_u32 m0, %[d], 32768\n\t" \
    "v_mfma_f32_16x16x32_bf16 v[40:43], v[124:127], v[100:103], v[40:43]\n\t" \
    "buffer_load_dwordx4 %[va0], %[ra], %[x] offen sc1 lds\n\t" \
    "v_mfma_f32_16x16x32_bf16 v[36:39], v[120:123], v[104:107], v[36:39]\n\t" \
    "ds_read_b128 v[160:163], %[a] offset:3072\n\t" \
    "v_mfma_f32_16x16x32_bf16 v[40:43], v[128:131], v[104:107], v[40:43]\n\t" \
    "ds_read_b128 v[164:167], %[a] offset:4096\n\t" \
    "v_mfma_f32_16x16x32_bf16 v[44:47], v[116:119], v[108:111], v[44:47]\n\t" \
    "ds_read_b128 v[168:171], %[a] offset:5120\n\t" \
    "s_add_u32 m0, %[d], 40960\n\t" \
    "v_mfma_f32_16x16x32_bf16 v[48:51], v[124:127], v[108:111], v[48:51]\n\t" \
    "buffer_load_dwordx4 %[va1], %[ra], %[x] offen sc1 lds\n\t" \
    "v_mfma_f32_16x16x32_bf16 v[44:47], v[120:123], v[112:115], v[44:47]\n\t" \
    "ds_read_b128 v[172:175], %[a] offset:6144\n\t" \
    "v_mfma_f32_16x16x32_bf16 v[48:51], v[128:131], v[112:115], v[48:51]\n\t" \
    "ds_read_b128 v[176:179], %[a] offset:7168\n\t" \
    "s_mov_b32 m0, %[pd]\n\t" \
    "s_cmp_lt_u32 %[ps], %[pe]\n\t" \
    "s_cselect_b64 exec, -1, 0\n\t" \
    "buffer_load_dword %[pv], %[rp], %[ps] offen sc1 lds\n\t" \
    "s_mov_b64 exec, -1\n\t" \
    "s_add_u32 %[ps], %[ps], %[pk]\n\t" \
    "s_add_u32 %[t], %[t], 1\n\t" \
    "s_add_u32 %[d], %[lw], %[c]\n\t" \
    "s_add_u32 %[y], %[c], 49152\n\t" \
    "s_cmp_eq_u32 %[y], 147456\n\t" \
    "s_cselect_b32 %[y], 0, %[y]\n\t" \
    "s_sub_u32 %[x], %[y], %[c]\n\t" \
    "s_mov_b32 %[c], %[y]\n\t" \
    "v_add_u32 %[a], %[x], %[a]\n\t" \
    "v_add_u32 %[b0], %[x], %[b0]\n\t" \
    "v_add_u32 %[b1], %[x], %[b1]\n\t" \
    "s_add_u32 %[x], %[t], 3\n\t" \
    "s_cmp_lt_u32 %[x], %[nt]\n\t" \
    "s_cselect_b32 %[x], %[x], 0\n\t" \
    "s_mul_i32 %[y], %[x], %[kb]\n\t" \
    "s_add_u32 %[y], %[y], %[cb]\n\t" \
    "s_lshl_b32 %[x], %[x], 11\n\t" \
    "s_add_u32 %[x], %[x], %[ca]\n\t" \
    "s_waitcnt vmcnt(4) lgkmcnt(0)\n\t" \
    "s_barrier\n\t" \
    "v_mfma_f32_16x16x32_bf16 v[20:23], v[180:183], v[148:151], v[20:23]\n\t" \
    "ds_read_b64_tr_b16 v[116:117], %[b0] offset:0\n\t" \
    "ds_read_b64_tr_b16 v[118:119], %[b0] offset:1024\n\t" \
    "s_add_u32 m0, %[d], 0\n\t" \
    "v_mfma_f32_16x16x32_bf16 v[24:27], v[188:191], v[148:151], v[24:27]\n\t" \
    "buffer_load_dwordx4 %[vb0], %[rb], %[y] offen lds\n\t" \
    "v_mfma_f32_16x16x32_bf16 v[20:23], v[184:187], v[152:155], v[20:23]\n\t" \
    "ds_read_b64_tr_b16 v[124:125], %[b1] offset:0\n\t" \
    "ds_read_b64_tr_b16 v[126:127], %[b1] offset:1024\n\t" \
    "v_mfma_f32_16x16x32_bf16 v[24:27], v[192:195], v[152:155], v[24:27]\n\t" \
    "ds_read_b64_tr_b16 v[120:121], %[b0] offset:8192\n\t" \
    "ds_read_b64_tr_b16 v[122:123], %[b0] offset:9216\n\t" \
    "v_mfma_f32_16x16x32_bf16 v[28:31], v[180:183], v[156:159], v[28:31]\n\t" \
    "ds_read_b64_tr_b16 v[128:129], %[b1] offset:8192\n\t" \
    "ds_read_b64_tr_b16 v[130:131], %[b1] offset:9216\n\t" \
    "s_add_u32 m0, %[d], 8192\n\t" \
    "v_mfma_f32_16x16x32_bf16 v[32:35], v[188:191], v[156:159], v[32:35]\n\t" \
    "buffer_load_dwordx4 %[vb1], %[rb], %[y] offen lds\n\t" \
    "v_mfma_f32_16x16x32_bf16 v[28:31], v[184:187], v[160:163], v[28:31]\n\t" \
    "ds_read_b128 v[84:87], %[a] offset:0\n\t" \
    "v_mfma_f32_16x16x32_bf16 v[32:35], v[192:195], v[160:163], v[32:35]\n\t" \
    "ds_read_b128 v[88:91], %[a] offset:1024\n\t" \
    "v_mfma_f32_16x16x32_bf16 v[36:39], v[180:183], v[164:167], v[36:39]\n\t" \
    "ds_read_b128 v[92:95], %[a] offset:2048\n\t" \
    "s_add_u32 m0, %[d], 32768\n\t" \
    "v_mfma_f32_16x16x32_bf16 v[40:43], v[188:191], v[164:167], v[40:43]\n\t" \
    "buffer_load_dwordx4 %[va0], %[ra], %[x] offen sc1 lds\n\t" \
    "v_mfma_f32_16x16x32_bf16 v[36:39], v[184:187], v[168:171], v[36:39]\n\t" \
    "ds_read_b128 v[96:99], %[a] offset:3072\n\t" \
    "v_mfma_f32_16x16x32_bf16 v[40:43], v[192:195], v[168:171], v[40:43]\n\t" \
    "ds_read_b128 v[100:103], %[a] offset:4096\n\t" \
    "v_mfma_f32_16x16x32_bf16 v[44:47], v[180:183], v[172:175], v[44:47]\n\t" \
    "ds_read_b128 v[104:107], %[a] offset:5120\n\t" \
    "s_add_u32 m0, %[d], 40960\n\t" \
    "v_mfma_f32_16x16x32_bf16 v[48:51], v[188:191], v[172:175], v[48:51]\n\t" \
    "buffer_load_dwordx4 %[va1], %[ra], %[x] offen sc1 lds\n\t" \
    "v_mfma_f32_16x16x32_bf16 v[44:47], v[184:187], v[176:179], v[44:47]\n\t" \
    "ds_read_b128 v[108:111], %[a] offset:6144\n\t" \
    "v_mfma_f32_16x16x32_bf16 v[48:51], v[192:195], v[176:179], v[48:51]\n\t" \
    "ds_read_b128 v[112:115], %[a] offset:7168\n\t" \
    "s_mov_b32 m0, %[pd]\n\t" \
    "s_cmp_lt_u32 %[ps], %[pe]\n\t" \
    "s_cselect_b64 exec, -1, 0\n\t" \
    "buffer_load_dword %[pv], %[rp], %[ps] offen sc1 lds\n\t" \
    "s_mov_b64 exec, -1\n\t" \
    "s_add_u32 %[ps], %[ps], %[pk]\n\t" \
    "s_add_u32 %[t], %[t], 1\n\t" \
    "s_cmp_lt_u32 %[t], %[nt]\n\t" \
    "s_cbranch_scc1 L_%=\n\t" \
    "s_waitcnt lgkmcnt(0)\n\t" \
    "s_nop 7\n\t" \
    : "=&{v[20:23]}"(acc[0][0][0][0]), "=&{v[24:27]}"(acc[0][0][0][1]), "=&{v[28:31]}"(acc[0][0][1][0]), "=&{v[32:35]}"(acc[0][0][1][1]), "=&{v[36:39]}"(acc[0][0][2][0]), "=&{v[40:43]}"(acc[0][0][2][1]), "=&{v[44:47]}"(acc[0][0][3][0]), "=&{v[48:51]}"(acc[0][0][3][1]), "=&{v[52:55]}"(acc[0][1][0][0]), "=&{v[56:59]}"(acc[0][1][0][1]), "=&{v[60:63]}"(acc[0][1][1][0]), "=&{v[64:67]}"(acc[0][1][1][1]), "=&{v[68:71]}"(acc[0][1][2][0]), "=&{v[72:75]}"(acc[0][1][2][1]), "=&{v[76:79]}"(acc[0][1][3][0]), "=&{v[80:83]}"(acc[0][1][3][1]), [a] "+v"(aoff), [b0] "+v"(boff0), [b1] "+v"(boff1), [c] "+s"(cur), [t] "+s"(t), [ps] "+s"(ps), [d] "=&s"(sd), [x] "=&s"(sx), [y] "=&s"(sy) \
    : [va0] "v"(voffA[0]), [va1] "v"(voffA[1]), [vb0] "v"(voffB[0]), [vb1] "v"(voffB[1]), [pv] "v"(pf.v), [ra] "s"(ra), [rb] "s"(rb), [rp] "s"(rp), [lw] "s"(ldsw), [nt] "s"(nt), [kb] "s"(kB), [ca] "s"(cA), [cb] "s"(cB), [pd] "s"(pd), [pe] "s"(pe), [pk] "s"(pf.step) \
    : "memory", "scc", "v84", "v85", "v86", "v87", "v88", "v89", "v90", "v91", "v92", "v93", "v94", "v95", "v96", "v97", "v98", "v99", "v100", "v101", "v102", "v103", "v104", "v105", "v106", "v107", "v108", "v109", "v110", "v111", "v112", "v113", "v114", "v115", "v116", "v117", "v118", "v119", "v120", "v121", "v122", "v123", "v124", "v125", "v126", "v127", "v128", "v129", "v130", "v131", "v132", "v133", "v134", "v135", "v136", "v137", "v138", "v139", "v140", "v141", "v142", "v143", "v144", "v145", "v146", "v147", "v148", "v149", "v150", "v151", "v152", "v153", "v154", "v155", "v156", "v157", "v158", "v159", "v160", "v161", "v162", "v163", "v164", "v165", "v166", "v167", "v168", "v169", "v170", "v171", "v172", "v173", "v174", "v175", "v176", "v177", "v178", "v179", "v180", "v181", "v182", "v183", "v184", "v185", "v186", "v187", "v188", "v189", "v190", "v191", "v192", "v193", "v194", "v195", "v196", "v197", "v198", "v199", "v200", "v201", "v202", "v203", "v204", "v205", "v206", "v207", "v208", "v209", "v210", "v211")

#define H_LOOP2() \
  asm volatile( \
    "v_mov_b32 v20, 0\n\t" \
    "v_mov_b32 v21, 0\n\t" \
    "v_mov_b32 v22, 0\n\t" \
    "v_mov_b32 v23, 0\n\t" \
    "v_mov_b32 v24, 0\n\t" \
    "v_mov_b32 v25, 0\n\t" \
    "v_mov_b32 v26, 0\n\t" \
    "v_mov_b32 v27, 0\n\t" \
    "v_mov_b32 v28, 0\n\t" \
    "v_mov_b32 v29, 0\n\t" \
    "v_mov_b32 v30, 0\n\t" \
    "v_mov_b32 v31, 0\n\t" \
    "v_mov_b32 v32, 0\n\t" \
    "v_mov_b32 v33, 0\n\t" \
    "v_mov_b32 v34, 0\n\t" \
    "v_mov_b32 v35, 0\n\t" \
    "v_mov_b32 v36, 0\n\t" \
    "v_mov_b32 v37, 0\n\t" \
    "v_mov_b32 v38, 0\n\t" \
    "v_mov_b32 v39, 0\n\t" \
    "v_mov_b32 v40, 0\n\t" \
    "v_mov_b32 v41, 0\n\t" \
    "v_mov_b32 v42, 0\n\t" \
    "v_mov_b32 v43, 0\n\t" \
    "v_mov_b32 v44, 0\n\t" \
    "v_mov_b32 v45, 0\n\t" \
    "v_mov_b32 v46, 0\n\t" \
    "v_mov_b32 v47, 0\n\t" \
    "v_mov_b32 v48, 0\n\t" \
    "v_mov_b32 v49, 0\n\t" \
    "v_mov_b32 v50, 0\n\t" \
    "v_mov_b32 v51, 0\n\t" \
    "v_mov_b32 v52, 0\n\t" \
    "v_mov_b32 v53, 0\n\t" \
    "v_mov_b32 v54, 0\n\t" \
    "v_mov_b32 v55, 0\n\t" \
    "v_mov_b32 v56, 0\n\t" \
    "v_mov_b32 v57, 0\n\t" \
    "v_mov_b32 v58, 0\n\t" \
    "v_mov_b32 v59, 0\n\t" \
    "v_mov_b32 v60, 0\n\t" \
    "v_mov_b32 v61, 0\n\t" \
    "v_mov_b32 v62, 0\n\t" \
    "v_mov_b32 v63, 0\n\t" \
    "v_mov_b32 v64, 0\n\t" \
    "v_mov_b32 v65, 0\n\t" \
    "v_mov_b32 v66, 0\n\t" \
    "v_mov_b32 v67, 0\n\t" \
    "v_mov_b32 v68, 0\n\t" \
    "v_mov_b32 v69, 0\n\t" \
    "v_mov_b32 v70, 0\n\t" \
    "v_mov_b32 v71, 0\n\t" \
    "v_mov_b32 v72, 0\n\t" \
    "v_mov_b32 v73, 0\n\t" \
    "v_mov_b32 v74, 0\n\t" \
    "v_mov_b32 v75, 0\n\t" \
    "v_mov_b32 v76, 0\n\t" \
    "v_mov_b32 v77, 0\n\t" \
    "v_mov_b32 v78, 0\n\t" \
    "v_mov_b32 v79, 0\n\t" \
    "v_mov_b32 v80, 0\n\t" \
    "v_mov_b32 v81, 0\n\t" \
    "v_mov_b32 v82, 0\n\t" \
    "v_mov_b32 v83, 0\n\t" \
    "s_waitcnt vmcnt(12)\n\t" \
    "s_barrier\n\t" \
    "ds_read_b64_tr_b16 v[116:117], %[b0] offset:0\n\t" \
    "ds_read_b64_tr_b16 v[118:119], %[b0] offset:1024\n\t" \
    "ds_read_b64_tr_b16 v[124:125], %[b1] offset:0\n\t" \
    "ds_read_b64_tr_b16 v[126:127], %[b1] offset:1024\n\t" \
    "ds_read_b64_tr_b16 v[120:121], %[b0] offset:8192\n\t" \
    "ds_read_b64_tr_b16 v[122:123], %[b0] offset:9216\n\t" \
    "ds_read_b64_tr_b16 v[128:129], %[b1] offset:8192\n\t" \
    "ds_read_b64_tr_b16 v[130:131], %[b1] offset:9216\n\t" \
    "ds_read_b128 v[84:87], %[a] offset:0\n\t" \
    "ds_read_b128 v[88:91], %[a] offset:1024\n\t" \
    "ds_read_b128 v[92:95], %[a] offset:2048\n\t" \
    "ds_read_b128 v[96:99], %[a] offset:3072\n\t" \
    "ds_read_b128 v[100:103], %[a] offset:4096\n\t" \
    "ds_read_b128 v[104:107], %[a] offset:5120\n\t" \
    "ds_read_b128 v[108:111], %[a] offset:6144\n\t" \
    "ds_read_b128 v[112:115], %[a] offset:7168\n\t" \
    "ds_read_b64_tr_b16 v[132:133], %[b0] offset:16384\n\t" \
    "ds_read_b64_tr_b16 v[134:135], %[b0] offset:17408\n\t" \
    "ds_read_b64_tr_b16 v[140:141], %[b1] offset:16384\n\t" \
    "ds_read_b64_tr_b16 v[142:143], %[b1] offset:17408\n\t" \
    "ds_read_b64_tr_b16 v[136:137], %[b0] offset:24576\n\t" \
    "ds_read_b64_tr_b16 v[138:139], %[b0] offset:25600\n\t" \
    "ds_read_b64_tr_b16 v[144:145], %[b1] offset:24576\n\t" \
    "ds_read_b64_tr_b16 v[146:147], %[b1] offset:25600\n\t" \
    "L_%=:\n\t" \
    "s_add_u32 %[d], %[lw], %[c]\n\t" \
    "s_add_u32 %[y], %[c], 49152\n\t" \
    "s_cmp_eq_u32 %[y], 147456\n\t" \
    "s_cselect_b32 %[y], 0, %[y]\n\t" \
    "s_sub_u32 %[x], %[y], %[c]\n\t" \
    "s_mov_b32 %[c], %[y]\n\t" \
    "v_add_u32 %[a], %[x], %[a]\n\t" \
    "v_add_u32 %[b0], %[x], %[b0]\n\t" \
    "v_add_u32 %[b1], %[x], %[b1]\n\t" \
    "s_add_u32 %[x], %[t], 3\n\t" \
    "s_cmp_lt_u32 %[x], %[nt]\n\t" \
    "s_cselect_b32 %[x], %[x], 0\n\t" \
    "s_mul_i32 %[y], %[x], %[kb]\n\t" \
    "s_add_u32 %[y], %[y], %[cb]\n\t" \
    "s_lshl_b32 %[x], %[x], 11\n\t" \
    "s_add_u32 %[x], %[x], %[ca]\n\t" \
    "s_waitcnt vmcnt(6) lgkmcnt(0)\n\t" \
    "s_barrier\n\t" \
    "v_mfma_f32_16x16x32_bf16 v[20:23], v[116:119], v[84:87], v[20:23]\n\t" \
    "ds_read_b64_tr_b16 v[180:181], %[b0] offset:0\n\t" \
    "s_add_u32 m0, %[d], 0\n\t" \
    "v_mfma_f32_16x16x32_bf16 v[24:27], v[124:127], v[84:87], v[24:27]\n\t" \
    "buffer_load_dwordx4 %[vb0], %[rb], %[y] offen lds\n\t" \
    "v_mfma_f32_16x16x32_bf16 v[20:23], v[120:123], v[88:91], v[20:23]\n\t" \
    "ds_read_b64_tr_b16 v[182:183], %[b0] offset:1024\n\t" \
    "v_mfma_f32_16x16x32_bf16 v[24:27], v[128:131], v[88:91], v[24:27]\n\t" \
    "ds_read_b64_tr_b16 v[188:189], %[b1] offset:0\n\t" \
    "v_mfma_f32_16x16x32_bf16 v[28:31], v[116:119], v[92:95], v[28:31]\n\t" \
    "ds_read_b64_tr_b16 v[190:191], %[b1] offset:1024\n\t" \
    "v_mfma_f32_16x16x32_bf16 v[32:35], v[124:127], v[92:95], v[32:35]\n\t" \
    "ds_read_b64_tr_b16 v[184:185], %[b0] offset:8192\n\t" \
    "s_add_u32 m0, %[d], 8192\n\t" \
    "v_mfma_f32_16x16x32_bf16 v[28:31], v[120:123], v[96:99], v[28:31]\n\t" \
    "buffer_load_dwordx4 %[vb1], %[rb], %[y] offen lds\n\t" \
    "v_mfma_f32_16x16x32_bf16 v[32:35], v[128:131], v[96:99], v[32:35]\n\t" \
    "ds_read_b64_tr_b16 v[186:187], %[b0] offset:9216\n\t" \
    "v_mfma_f32_16x16x32_bf16 v[36:39], v[116:119], v[100:103], v[36:39]\n\t" \
    "ds_read_b64_tr_b16 v[192:193], %[b1] offset:8192\n\t" \
    "v_mfma_f32_16x16x32_bf16 v[40:43], v[124:127], v[100:103], v[40:43]\n\t" \
    "ds_read_b64_tr_b16 v[194:195], %[b1] offset:9216\n\t" \
    "v_mfma_f32_16x16x32_bf16 v[36:39], v[120:123], v[104:107], v[36:39]\n\t" \
    "ds_read_b128 v[148:151], %[a] offset:0\n\t" \
    "s_add_u32 m0, %[d], 32768\n\t" \
    "v_mfma_f32_16x16x32_bf16 v[40:43], v[128:131], v[104:107], v[40:43]\n\t" \
    "buffer_load_dwordx4 %[va0], %[ra], %[x] offen sc1 lds\n\t" \
    "v_mfma_f32_16x16x32_bf16 v[44:47], v[116:119], v[108:111], v[44:47]\n\t" \
    "ds_read_b128 v[152:155], %[a] offset:1024\n\t" \
    "v_mfma_f32_16x16x32_bf16 v[48:51], v[124:127], v[108:111], v[48:51]\n\t" \
    "ds_read_b128 v[156:159], %[a] offset:2048\n\t" \
    "v_mfma_f32_16x16x32_bf16 v[44:47], v[120:123], v[112:115], v[44:47]\n\t" \
    "ds_read_b128 v[160:163], %[a] offset:3072\n\t" \
    "v_mfma_f32_16x16x32_bf16 v[48:51], v[128:131], v[112:115], v[48:51]\n\t" \
    "ds_read_b128 v[164:167], %[a] offset:4096\n\t" \
    "s_add_u32 m0, %[d], 40960\n\t" \
    "v_mfma_f32_16x16x32_bf16 v[52:55], v[132:135], v[84:87], v[52:55]\n\t" \
    "buffer_load_dwordx4 %[va1], %[ra], %[x] offen sc1 lds\n\t" \
    "v_mfma_f32_16x16x32_bf16 v[56:59], v[140:143], v[84:87], v[56:59]\n\t" \
    "ds_read_b128 v[168:171], %[a] offset:5120\n\t" \
    "v_mfma_f32_16x16x32_bf16 v[52:55], v[136:139], v[88:91], v[52:55]\n\t" \
    "ds_read_b128 v[172:175], %[a] offset:6144\n\t" \
    "v_mfma_f32_16x16x32_bf16 v[56:59], v[144:147], v[88:91], v[56:59]\n\t" \
    "ds_read_b128 v[176:179], %[a] offset:7168\n\t" \
    "v_mfma_f32_16x16x32_bf16 v[60:63], v[132:135], v[92:95], v[60:63]\n\t" \
    "ds_read_b64_tr_b16 v[196:197], %[b0] offset:16384\n\t" \
    "s_add_u32 m0, %[d], 16128\n\t" \
    "v_mfma_f32_16x16x32_bf16 v[64:67], v[140:143], v[92:95], v[64:67]\n\t" \
    "buffer_load_dwordx4 %[vb0], %[rb], %[y] offen offset:256 lds\n\t" \
    "v_mfma_f32_16x16x32_bf16 v[60:63], v[136:139], v[96:99], v[60:63]\n\t" \
    "ds_read_b64_tr_b16 v[198:199], %[b0] offset:17408\n\t" \
    "v_mfma_f32_16x16x32_bf16 v[64:67], v[144:147], v[96:99], v[64:67]\n\t" \
    "ds_read_b64_tr_b16 v[204:205], %[b1] offset:16384\n\t" \
    "v_mfma_f32_16x16x32_bf16 v[68:71], v[132:135], v[100:103], v[68:71]\n\t" \
    "ds_read_b64_tr_b16 v[206:207], %[b1] offset:17408\n\t" \
    "v_mfma_f32_16x16x32_bf16 v[72:75], v[140:143], v[100:103], v[72:75]\n\t" \
    "ds_read_b64_tr_b16 v[200:201], %[b0] offset:24576\n\t" \
    "s_add_u32 m0, %[d], 24320\n\t" \
    "v_mfma_f32_16x16x32_bf16 v[68:71], v[136:139], v[104:107], v[68:71]\n\t" \
    "buffer_load_dwordx4 %[vb1], %[rb], %[y] offen offset:256 lds\n\t" \
    "v_mfma_f32_16x16x32_bf16 v[72:75], v[144:147], v[104:107], v[72:75]\n\t" \
    "ds_read_b64_tr_b16 v[202:203], %[b0] offset:25600\n\t" \
    "v_mfma_f32_16x16x32_bf16 v[76:79], v[132:135], v[108:111], v[76:79]\n\t" \
    "ds_read_b64_tr_b16 v[208:209], %[b1] offset:24576\n\t" \
    "v_mfma_f32_16x16x32_bf16 v[80:83], v[140:143], v[108:111], v[80:83]\n\t" \
    "ds_read_b64_tr_b16 v[210:211], %[b1] offset:25600\n\t" \
    "v_mfma_f32_16x16x32_bf16 v[76:79], v[136:139], v[112:115], v[76:79]\n\t" \
    "v_mfma_f32_16x16x32_bf16 v[80:83], v[144:147], v[112:115], v[80:83]\n\t" \
    "s_mov_b32 m0, %[pd]\n\t" \
    "s_cmp_lt_u32 %[ps], %[pe]\n\t" \
    "s_cselect_b64 exec, -1, 0\n\t" \
    "buffer_load_dword %[pv], %[rp], %[ps] offen sc1 lds\n\t" \
    "s_mov_b64 exec, -1\n\t" \
    "s_add_u32 %[ps], %[ps], %[pk]\n\t" \
    "s_add_u32 %[t], %[t], 1\n\t" \
    "s_add_u32 %[d], %[lw], %[c]\n\t" \
    "s_add_u32 %[y], %[c], 49152\n\t" \
    "s_cmp_eq_u32 %[y], 147456\n\t" \
    "s_cselect_b32 %[y], 0, %[y]\n\t" \
    "s_sub_u32 %[x], %[y], %[c]\n\t" \
    "s_mov_b32 %[c], %[y]\n\t" \
    "v_add_u32 %[a], %[x], %[a]\n\t" \
    "v_add_u32 %[b0], %[x], %[b0]\n\t" \
    "v_add_u32 %[b1], %[x], %[b1]\n\t" \
    "s_add_u32 %[x], %[t], 3\n\t" \
    "s_cmp_lt_u32 %[x], %[nt]\n\t" \
    "s_cselect_b32 %[x], %[x], 0\n\t" \
    "s_mul_i32 %[y], %[x], %[kb]\n\t" \
    "s_add_u32 %[y], %[y], %[cb]\n\t" \
    "s_lshl_b32 %[x], %[x], 11\n\t" \
    "s_add_u32 %[x], %[x], %[ca]\n\t" \
    "s_waitcnt vmcnt(6) lgkmcnt(0)\n\t" \
    "s_barrier\n\t" \
    "v_mfma_f32_16x16x32_bf16 v[20:23], v[180:183], v[148:151], v[20:23]\n\t" \
    "ds_read_b64_tr_b16 v[116:117], %[b0] offset:0\n\t" \
    "s_add_u32 m0, %[d], 0\n\t" \
    "v_mfma_f32_16x16x32_bf16 v[24:27], v[188:191], v[148:151], v[24:27]\n\t" \
    "buffer_load_dwordx4 %[vb0], %[rb], %[y] offen lds\n\t" \
    "v_mfma_f32_16x16x32_bf16 v[20:23], v[184:187], v[152:155], v[20:23]\n\t" \
    "ds_read_b64_tr_b16 v[118:119], %[b0] offset:1024\n\t" \
    "v_mfma_f32_16x16x32_bf16 v[24:27], v[192:195], v[152:155], v[24:27]\n\t" \
    "ds_read_b64_tr_b16 v[124:125], %[b1] offset:0\n\t" \
    "v_mfma_f32_16x16x32_bf16 v[28:31], v[180:183], v[156:159], v[28:31]\n\t" \
    "ds_read_b64_tr_b16 v[126:127], %[b1] offset:1024\n\t" \
    "v_mfma_f32_16x16x32_bf16 v[32:35], v[188:191], v[156:159], v[32:35]\n\t" \
    "ds_read_b64_tr_b16 v[120:121], %[b0] offset:8192\n\t" \
    "s_add_u32 m0, %[d], 8192\n\t" \
    "v_mfma_f32_16x16x32_bf16 v[28:31], v[184:187], v[160:163], v[28:31]\n\t" \
    "buffer_load_dwordx4 %[vb1], %[rb], %[y] offen lds\n\t" \
    "v_mfma_f32_16x16x32_bf16 v[32:35], v[192:195], v[160:163], v[32:35]\n\t" \
    "ds_read_b64_tr_b16 v[122:123], %[b0] offset:9216\n\t" \
    "v_mfma_f32_16x16x32_bf16 v[36:39], v[180:183], v[164:167], v[36:39]\n\t" \
    "ds_read_b64_tr_b16 v[128:129], %[b1] offset:8192\n\t" \
    "v_mfma_f32_16x16x32_bf16 v[40:43], v[188:191], v[164:167], v[40:43]\n\t" \
    "ds_read_b64_tr_b16 v[130:131], %[b1] offset:9216\n\t" \
    "v_mfma_f32_16x16x32_bf16 v[36:39], v[184:187], v[168:171], v[36:39]\n\t" \
    "ds_read_b128 v[84:87], %[a] offset:0\n\t" \
    "s_add_u32 m0, %[d], 32768\n\t" \
    "v_mfma_f32_16x16x32_bf16 v[40:43], v[192:195], v[168:171], v[40:43]\n\t" \
    "buffer_load_dwordx4 %[va0], %[ra], %[x] offen sc1 lds\n\t" \
    "v_mfma_f32_16x16x32_bf16 v[44:47], v[180:183], v[172:175], v[44:47]\n\t" \
    "ds_read_b128 v[88:91], %[a] offset:1024\n\t" \
    "v_mfma_f32_16x16x32_bf16 v[48:51], v[188:191], v[172:175], v[48:51]\n\t" \
    "ds_read_b128 v[92:95], %[a] offset:2048\n\t" \
    "v_mfma_f32_16x16x32_bf16 v[44:47], v[184:187], v[176:179], v[44:47]\n\t" \
    "ds_read_b128 v[96:99], %[a] offset:3072\n\t" \
    "v_mfma_f32_16x16x32_bf16 v[48:51], v[192:195], v[176:179], v[48:51]\n\t" \
    "ds_read_b128 v[100:103], %[a] offset:4096\n\t" \
    "s_add_u32 m0, %[d], 40960\n\t" \
    "v_mfma_f32_16x16x32_bf16 v[52:55], v[196:199], v[148:151], v[52:55]\n\t" \
    "buffer_load_dwordx4 %[va1], %[ra], %[x] offen sc1 lds\n\t" \
    "v_mfma_f32_16x16x32_bf16 v[56:59], v[204:207], v[148:151], v[56:59]\n\t" \
    "ds_read_b128 v[104:107], %[a] offset:5120\n\t" \
    "v_mfma_f32_16x16x32_bf16 v[52:55], v[200:203], v[152:155], v[52:55]\n\t" \
    "ds_read_b128 v[108:111], %[a] offset:6144\n\t" \
    "v_mfma_f32_16x16x32_bf16 v[56:59], v[208:211], v[152:155], v[56:59]\n\t" \
    "ds_read_b128 v[112:115], %[a] offset:7168\n\t" \
    "v_mfma_f32_16x16x32_bf16 v[60:63], v[196:199], v[156:159], v[60:63]\n\t" \
    "ds_read_b64_tr_b16 v[132:133], %[b0] offset:16384\n\t" \
    "s_add_u32 m0, %[d], 16128\n\t" \
    "v_mfma_f32_16x16x32_bf16 v[64:67], v[204:207], v[156:159], v[64:67]\n\t" \
    "buffer_load_dwordx4 %[vb0], %[rb], %[y] offen offset:256 lds\n\t" \
    "v_mfma_f32_16x16x32_bf16 v[60:63], v[200:203], v[160:163], v[60:63]\n\t" \
    "ds_read_b64_tr_b16 v[134:135], %[b0] offset:17408\n\t" \
    "v_mfma_f32_16x16x32_bf16 v[64:67], v[208:211], v[160:163], v[64:67]\n\t" \
    "ds_read_b64_tr_b16 v[140:141], %[b1] offset:16384\n\t" \
    "v_mfma_f32_16x16x32_bf16 v[68:71], v[196:199], v[164:167], v[68:71]\n\t" \
    "ds_read_b64_tr_b16 v[142:143], %[b1] offset:17408\n\t" \
    "v_mfma_f32_16x16x32_bf16 v[72:75], v[204:207], v[164:167], v[72:75]\n\t" \
    "ds_read_b64_tr_b16 v[136:137], %[b0] offset:24576\n\t" \
    "s_add_u32 m0, %[d], 24320\n\t" \
    "v_mfma_f32_16x16x32_bf16 v[68:71], v[200:203], v[168:171], v[68:71]\n\t" \
    "buffer_load_dwordx4 %[vb1], %[rb], %[y] offen offset:256 lds\n\t" \
    "v_mfma_f32_16x16x32_bf16 v[72:75], v[208:211], v[168:171], v[72:75]\n\t" \
    "ds_read_b64_tr_b16 v[138:139], %[b0] offset:25600\n\t" \
    "v_mfma_f32_16x16x32_bf16 v[76:79], v[196:199], v[172:175], v[76:79]\n\t" \
    "ds_read_b64_tr_b16 v[144:145], %[b1] offset:24576\n\t" \
    "v_mfma_f32_16x16x32_bf16 v[80:83], v[204:207], v[172:175], v[80:83]\n\t" \
    "ds_read_b64_tr_b16 v[146:147], %[b1] offset:25600\n\t" \
    "v_mfma_f32_16x16x32_bf16 v[76:79], v[200:203], v[176:179], v[76:79]\n\t" \
    "v_mfma_f32_16x16x32_bf16 v[80:83], v[208:211], v[176:179], v[80:83]\n\t" \
    "s_mov_b32 m0, %[pd]\n\t" \
    "s_cmp_lt_u32 %[ps], %[pe]\n\t" \
    "s_cselect_b64 exec, -1, 0\n\t" \
    "buffer_load_dword %[pv], %[rp], %[ps] offen sc1 lds\n\t" \
    "s_mov_b64 exec, -1\n\t" \
    "s_add_u32 %[ps], %[ps], %[pk]\n\t" \
    "s_add_u32 %[t], %[t], 1\n\t" \
    "s_cmp_lt_u32 %[t], %[nt]\n\t" \
    "s_cbranch_scc1 L_%=\n\t" \
    "s_waitcnt lgkmcnt(0)\n\t" \
    "s_nop 7\n\t" \
    : "=&{v[20:23]}"(acc[0][0][0][0]), "=&{v[24:27]}"(acc[0][0][0][1]), "=&{v[28:31]}"(acc[0][0][1][0]), "=&{v[32:35]}"(acc[0][0][1][1]), "=&{v[36:39]}"(acc[0][0][2][0]), "=&{v[40:43]}"(acc[0][0][2][1]), "=&{v[44:47]}"(acc[0][0][3][0]), "=&{v[48:51]}"(acc[0][0][3][1]), "=&{v[52:55]}"(acc[0][1][0][0]), "=&{v[56:59]}"(acc[0][1][0][1]), "=&{v[60:63]}"(acc[0][1][1][0]), "=&{v[64:67]}"(acc[0][1][1][1]), "=&{v[68:71]}"(acc[0][1][2][0]), "=&{v[72:75]}"(acc[0][1][2][1]), "=&{v[76:79]}"(acc[0][1][3][0]), "=&{v[80:83]}"(acc[0][1][3][1]), [a] "+v"(aoff), [b0] "+v"(boff0), [b1] "+v"(boff1), [c] "+s"(cur), [t] "+s"(t), [ps] "+s"(ps), [d] "=&s"(sd), [x] "=&s"(sx), [y] "=&s"(sy) \
    : [va0] "v"(voffA[0]), [va1] "v"(voffA[1]), [vb0] "v"(voffB[0]), [vb1] "v"(voffB[1]), [pv] "v"(pf.v), [ra] "s"(ra), [rb] "s"(rb), [rp] "s"(rp), [lw] "s"(ldsw), [nt] "s"(nt), [kb] "s"(kB), [ca] "s"(cA), [cb] "s"(cB), [pd] "s"(pd), [pe] "s"(pe), [pk] "s"(pf.step) \
    : "memory", "scc", "v84", "v85", "v86", "v87", "v88", "v89", "v90", "v91", "v92", "v93", "v94", "v95", "v96", "v97", "v98", "v99", "v100", "v101", "v102", "v103", "v104", "v105", "v106", "v107", "v108", "v109", "v110", "v111", "v112", "v113", "v114", "v115", "v116", "v117", "v118", "v119", "v120", "v121", "v122", "v123", "v124", "v125", "v126", "v127", "v128", "v129", "v130", "v131", "v132", "v133", "v134", "v135", "v136", "v137", "v138", "v139", "v140", "v141", "v142", "v143", "v144", "v145", "v146", "v147", "v148", "v149", "v150", "v151", "v152", "v153", "v154", "v155", "v156", "v157", "v158", "v159", "v160", "v161", "v162", "v163", "v164", "v165", "v166", "v167", "v168", "v169", "v170", "v171", "v172", "v173", "v174", "v175", "v176", "v177", "v178", "v179", "v180", "v181", "v182", "v183", "v184", "v185", "v186", "v187", "v188", "v189", "v190", "v191", "v192", "v193", "v194", "v195", "v196", "v197", "v198", "v199", "v200", "v201", "v202", "v203", "v204", "v205", "v206", "v207", "v208", "v209", "v210", "v211")
#define AQ_PIN0() asm volatile("" : "+{v[28:35]}"(wr[0][0]), "+{v[36:43]}"(wr[0][1]))
#define AQ_PIN1() asm volatile("" : "+{v[44:51]}"(wr[1][0]), "+{v[52:59]}"(wr[1][1]))
#define AQ_PIN2() asm volatile("" : "+{v[60:67]}"(wr[2][0]), "+{v[68:75]}"(wr[2][1]))
#define AQ_PIN3() asm volatile("" : "+{v[76:83]}"(wr[3][0]), "+{v[84:91]}"(wr[3][1]))
#define AQ_PIN4() asm volatile("" : "+{v[92:99]}"(wr[4][0]), "+{v[100:107]}"(wr[4][1]))
#define AQ_PIN5() asm volatile("" : "+{v[108:115]}"(wr[5][0]), "+{v[116:123]}"(wr[5][1]))
#define AQ_PIN6() asm volatile("" : "+{v[124:131]}"(wr[6][0]), "+{v[132:139]}"(wr[6][1]))
#define AQ_PIN7() asm volatile("" : "+{v[140:147]}"(wr[7][0]), "+{v[148:155]}"(wr[7][1]))
#define AQ_PIN8() asm volatile("" : "+{v[156:163]}"(wr[8][0]), "+{v[164:171]}"(wr[8][1]))
#define AQ_PIN9() asm volatile("" : "+{v[172:179]}"(wr[9][0]), "+{v[180:187]}"(wr[9][1]))
#define AQ_PIN10() asm volatile("" : "+{v[188:195]}"(wr[10][0]), "+{v[196:203]}"(wr[10][1]))
#define AQ_PIN11() asm volatile("" : "+{v[204:211]}"(wr[11][0]), "+{v[212:219]}"(wr[11][1]))
#define AK_PIN0() asm volatile("" : "+{v[20:27]}"(wr[0][0]), "+{v[28:35]}"(wr[0][1]))
#define AK_PIN1() asm volatile("" : "+{v[36:43]}"(wr[1][0]), "+{v[44:51]}"(wr[1][1]))
#define AK_PIN2() asm volatile("" : "+{v[52:59]}"(wr[2][0]), "+{v[60:67]}"(wr[2][1]))
#define AK_PIN3() asm volatile("" : "+{v[68:75]}"(wr[3][0]), "+{v[76:83]}"(wr[3][1]))
#define AK_PIN4() asm volatile("" : "+{v[84:91]}"(wr[4][0]), "+{v[92:99]}"(wr[4][1]))
#define AK_PIN5() asm volatile("" : "+{v[100:107]}"(wr[5][0]), "+{v[108:115]}"(wr[5][1]))
#define AK_PIN6() asm volatile("" : "+{v[116:123]}"(wr[6][0]), "+{v[124:131]}"(wr[6][1]))
#define AK_PIN7() asm volatile("" : "+{v[132:139]}"(wr[7][0]), "+{v[140:147]}"(wr[7][1]))
#define AK_PIN8() asm volatile("" : "+{v[148:155]}"(wr[8][0]), "+{v[156:163]}"(wr[8][1]))
#define AK_PIN9() asm volatile("" : "+{v[164:171]}"(wr[9][0]), "+{v[172:179]}"(wr[9][1]))
#define AK_PIN10() asm volatile("" : "+{v[180:187]}"(wr[10][0]), "+{v[188:195]}"(wr[10][1]))
#define AK_PIN11() asm volatile("" : "+{v[196:203]}"(wr[11][0]), "+{v[204:211]}"(wr[11][1]))
#define V_PIN() asm volatile("" : "+{v[28:35]}"(wv[0]), "+{v[36:43]}"(wv[1]), "+{v[44:51]}"(wv[2]), "+{v[52:59]}"(wv[3]), "+{v[60:67]}"(wv[4]), "+{v[68:75]}"(wv[5]), "+{v[76:83]}"(wv[6]), "+{v[84:91]}"(wv[7]), "+{v[92:99]}"(wv[8]), "+{v[100:107]}"(wv[9]), "+{v[108:115]}"(wv[10]), "+{v[116:123]}"(wv[11]), "+{v[124:131]}"(wv[12]), "+{v[132:139]}"(wv[13]), "+{v[140:147]}"(wv[14]), "+{v[148:155]}"(wv[15]))
#define WOV_PIN0() asm volatile("" : "+{v[156:163]}"(wo[0][0][0]), "+{v[164:171]}"(wo[0][0][1]), "+{v[172:179]}"(wo[0][1][0]), "+{v[180:187]}"(wo[0][1][1]))
#define WOV_PIN1() asm volatile("" : "+{v[188:195]}"(wo[1][0][0]), "+{v[196:203]}"(wo[1][0][1]), "+{v[204:211]}"(wo[1][1][0]), "+{v[212:219]}"(wo[1][1][1]))
#define WOW_PIN0() asm volatile("" : "+{v[28:35]}"(wo[0][0][0]), "+{v[36:43]}"(wo[0][0][1]), "+{v[44:51]}"(wo[0][1][0]), "+{v[52:59]}"(wo[0][1][1]))
#define WOW_PIN1() asm volatile("" : "+{v[60:67]}"(wo[1][0][0]), "+{v[68:75]}"(wo[1][0][1]), "+{v[76:83]}"(wo[1][1][0]), "+{v[84:91]}"(wo[1][1][1]))
#define WOW_PIN2() asm volatile("" : "+{v[92:99]}"(wo[2][0][0]), "+{v[100:107]}"(wo[2][0][1]), "+{v[108:115]}"(wo[2][1][0]), "+{v[116:123]}"(wo[2][1][1]))
#define WOW_PIN3() asm volatile("" : "+{v[124:131]}"(wo[3][0][0]), "+{v[132:139]}"(wo[3][0][1]), "+{v[140:147]}"(wo[3][1][0]), "+{v[148:155]}"(wo[3][1][1]))
#define WOW_PIN4() asm volatile("" : "+{v[156:163]}"(wo[4][0][0]), "+{v[164:171]}"(wo[4][0][1]), "+{v[172:179]}"(wo[4][1][0]), "+{v[180:187]}"(wo[4][1][1]))
#define WOW_PIN5() asm volatile("" : "+{v[188:195]}"(wo[5][0][0]), "+{v[196:203]}"(wo[5][0][1]), "+{v[204:211]}"(wo[5][1][0]), "+{v[212:219]}"(wo[5][1][1]))
#define A_PIN(QW, i) do { switch (i) { case 0: if (QW) AQ_PIN0(); else AK_PIN0(); break; case 1: if (QW) AQ_PIN1(); else AK_PIN1(); break; case 2: if (QW) AQ_PIN2(); else AK_PIN2(); break; case 3: if (QW) AQ_PIN3(); else AK_PIN3(); break; case 4: if (QW) AQ_PIN4(); else AK_PIN4(); break; case 5: if (QW) AQ_PIN5(); else AK_PIN5(); break; case 6: if (QW) AQ_PIN6(); else AK_PIN6(); break; case 7: if (QW) AQ_PIN7(); else AK_PIN7(); break; case 8: if (QW) AQ_PIN8(); else AK_PIN8(); break; case 9: if (QW) AQ_PIN9(); else AK_PIN9(); break; case 10: if (QW) AQ_PIN10(); else AK_PIN10(); break; case 11: if (QW) AQ_PIN11(); else AK_PIN11(); break; } } while (0)
#define WO_PIN(VW, i) do { switch (i) { case 0: if (VW) WOV_PIN0(); else WOW_PIN0(); break; case 1: if (VW) WOV_PIN1(); else WOW_PIN1(); break; case 2: if (!VW) WOW_PIN2(); break; case 3: if (!VW) WOW_PIN3(); break; case 4: if (!VW) WOW_PIN4(); break; case 5: if (!VW) WOW_PIN5(); break; } } while (0)

__device__ __forceinline__ int lds_byte(int r, int c) {
  const int st = (r >> 4) * 2 + (c >> 5), rr = r & 15, cc = c & 31, ob = rr * 64 + cc * 2;
  return st * 1024 + (ob ^ (((ob >> 9) & 1) << 5));
}
#define PFNT ""
#define G_DvoffB dmaw
#define PFH 5
struct Pf { unsigned v, step; bool on, a; };
#define G_PF(t) do { if (pf.on && (t) < nt) asm volatile("s_mov_b32 m0, %3\n\ts_nop 0\n\tbuffer_load_dword %0, %1, %2 offen sc1 " PFNT "lds" \
    :: "v"(pf.v), "s"(pf.a ? ra : rb), "s"((t) * pf.step), "s"((unsigned)(size_t)lds + 148480) : "memory"); } while (0)

#define G_ST1(bufoff, r, so, voff, i) G_D##voff(r, (voff)[i], so, ldsw + (bufoff) + (i) * 8192)
#define G_DvoffA dmas
#define G_STAGE(bufoff, so, voff) do { G_ST1(bufoff, G_R##voff, so, voff, 0); G_ST1(bufoff, G_R##voff, so, voff, 1); } while (0)
#define G_RvoffA ra
#define G_RvoffB rb
#define G_WAIT_V(n) asm volatile("s_waitcnt vmcnt(%0)" :: "n"(n) : "memory")
__device__ __forceinline__ void gemm_h(LAS unsigned char* lds, rsrc_t ra, rsrc_t rb, unsigned cA, unsigned cB, int lda, int ldb, int nt,
                                       const Pf& pf, bool wide, f32x4 (&acc)[1][2][4][2]) {
  const int wid = __builtin_amdgcn_readfirstlane(threadIdx.x >> 6), lane = opq(0), tid = wid * 64 + lane;
  const int wr = wid >> 2, wc = wid & 3, fr = lane & 15, fq = lane >> 4;
  unsigned voffA[2], voffB[2];
  #pragma unroll
  for (int i = 0; i < 2; ++i) {
    voffA[i] = (unsigned)(((wid >> 1) + 4 * i) * 16 * lda + (wid & 1) * 1024 + lane * 16);
    const int b = tid * 16 + i * 8192, r = b >> 8, pos = (b >> 4) & 15;
    const int c = pos ^ (((r & 3) << 1) | (((r >> 3) & 1) << 3));
    voffB[i] = (unsigned)(r * ldb + c * 16);
  }
  const unsigned kB = 64 * ldb;
  const unsigned ldsw = (unsigned)(size_t)lds + (unsigned)wid * 1024u;
  const int q = (lane & 15) >> 2, p = lane & 3;
  unsigned aoff = (unsigned)(size_t)lds + 32768 + lds_byte(wr * 64 + fr, fq * 8);
  unsigned boff0 = (unsigned)(size_t)lds + (8 * fq + q) * 256 + (((wc * 4 + (p >> 1)) ^ ((q << 1) | ((fq & 1) << 3))) << 4) + 8 * (p & 1), boff1 = boff0 ^ 32;
#define H_STAGE(so, t) do { G_STAGE(so, cB + (t) * kB, voffB); G_STAGE((so) + 32768, cA + (t) * 2048, voffA); if (wide) G_STAGE((so) + 16384, cB + (t) * kB + 256, voffB); } while (0)
  #pragma unroll
  for (int t = 3; t < PFH; ++t) G_PF(t);
  H_STAGE(0, 0); H_STAGE(49152, 1); H_STAGE(98304, 2);
  const rsrc_t rp = pf.a ? ra : rb;
  const unsigned pd = (unsigned)(size_t)lds + 148480, pe = pf.on ? nt * pf.step : 0u;
  unsigned cur = 0, t = 0, ps = PFH * pf.step, sd, sx, sy;
  if (wide) H_LOOP2(); else H_LOOP1();
}

__device__ __forceinline__ u32x4 epi_pack(const f32x4 a0, const f32x4 a1) {
  const unsigned x0 = pack2(a0[0], a0[1]), x1 = pack2(a0[2], a0[3]);
  const unsigned y0 = pack2(a1[0], a1[1]), y1 = pack2(a1[2], a1[3]);
  auto s0 = __builtin_amdgcn_permlane16_swap(x0, y0, false, false);
  auto s1 = __builtin_amdgcn_permlane16_swap(x1, y1, false, false);
  return (u32x4){s0[0], s1[0], s0[1], s1[1]};
}
__device__ __forceinline__ unsigned gelu2(unsigned w) {
  const f32x2 x = {lo16(w), hi16(w)};
  const float c0 = -2.f * 1.4426950408889634f * 0.7978845608028654f, c1 = c0 * 0.044715f;
  const f32x2 a = x * (x * x * c1 + c0);
  const f32x2 d = (f32x2){__builtin_amdgcn_exp2f(a[0]), __builtin_amdgcn_exp2f(a[1])} + 1.f;
  const f32x2 y = x * (f32x2){__builtin_amdgcn_rcpf(d[0]), __builtin_amdgcn_rcpf(d[1])};
  return pack2(y[0], y[1]);
}
__device__ __forceinline__ float silu_f(float b) { return b * __builtin_amdgcn_rcpf(1.f + __expf(-b)); }

__device__ __forceinline__ unsigned blk_in(int r, int c) { const int ob = r * 64 + c * 2; return ob ^ (((ob >> 9) & 1) << 5); }
__device__ __forceinline__ unsigned xn_blk(int rb, int cb) { return (unsigned)(((rb >> 1) * 32 + cb) * 2 + (rb & 1)) * 1024u; }

template <int EPI>
__device__ __forceinline__ void epi_rows(const f32x4 (&acc)[1][2][4][2], rsrc_t rs, unsigned obase, int ldc, int pm2, int pn2, int sh, bool wide, int wr, int wc, int ln) {
  const int fq = ln >> 4, cb = 16 * (fq & 1) + 8 * (fq >> 1), fr = (ln & 15) + sh;
  const unsigned lo = (unsigned)((fr >> 4) * 16 * ldc) + blk_in(fr & 15, cb);
  const unsigned ob = (unsigned)(pm2 * 128 + 64 * wr) * (unsigned)ldc + (unsigned)(pn2 * 4 + wc) * 1024u + lo;
  u32x4 r[8];
  if (EPI >= 2) {
    #pragma unroll
    for (int i = 0; i < 8; ++i)
      r[i] = ld16(rs, EPI == 2 ? WS_XN + xn_blk(pm2 * 8 + 4 * wr + (i & 3), pn2 * 4 + 4 * (i >> 2) + wc) + lo : WS_X1 + ob + (i & 3) * 32768 + (i >> 2) * 4096);
  }
  #pragma unroll
  for (int i = 0; i < 8; ++i) {
    if (i == 4 && !wide) break;
    u32x4 w = epi_pack(acc[0][i >> 2][i & 3][0], acc[0][i >> 2][i & 3][1]);
    #pragma unroll
    for (int k = 0; k < 4; ++k) {
      w[k] = EPI < 2 ? gelu2(w[k]) : pack2(lo16(r[i][k]) + lo16(w[k]), hi16(r[i][k]) + hi16(w[k]));
      if (EPI == 3) w[k] = pack2(silu_f(lo16(w[k])), silu_f(hi16(w[k])));
    }
    st16(rs, obase + ob + (unsigned)(16 * (i & 3) * ldc) + (i >> 2) * 4096, w);
  }
}

__device__ __forceinline__ float row_sum16(float v) {
  v += __uint_as_float(__builtin_amdgcn_update_dpp(0, (int)__float_as_uint(v), 0x128, 0xf, 0xf, false));
  v += __uint_as_float(__builtin_amdgcn_update_dpp(0, (int)__float_as_uint(v), 0x124, 0xf, 0xf, false));
  v += __uint_as_float(__builtin_amdgcn_update_dpp(0, (int)__float_as_uint(v), 0x122, 0xf, 0xf, false));
  v += __uint_as_float(__builtin_amdgcn_update_dpp(0, (int)__float_as_uint(v), 0x121, 0xf, 0xf, false));
  return v;
}
__device__ __forceinline__ void ln_rows(u32x4 (&a)[8]) {
  f32x2 v[32];
  #pragma unroll
  for (int i = 0; i < 32; ++i) v[i] = (f32x2){lo16(a[i >> 2][i & 3]), hi16(a[i >> 2][i & 3])};
  f32x2 s[4] = {v[0], v[1], v[2], v[3]};
  #pragma unroll
  for (int i = 4; i < 32; ++i) s[i & 3] += v[i];
  s[0] = (s[0] + s[1]) + (s[2] + s[3]);
  const float mean = row_sum16(s[0][0] + s[0][1]) * (1.f / DM);
  #pragma unroll
  for (int i = 0; i < 32; ++i) { const f32x2 d = v[i] - mean; s[i & 3] = i < 4 ? d * d : s[i & 3] + d * d; }
  s[0] = (s[0] + s[1]) + (s[2] + s[3]);
  const float var = row_sum16(s[0][0] + s[0][1]) * (1.f / DM);
  const float mb = rb1(mean), vb = rb1(var);
  const float rden = __builtin_amdgcn_rcpf(rb1(__builtin_amdgcn_sqrtf(rb1(vb + 1e-5f))));
  #pragma unroll
  for (int i = 0; i < 32; ++i) {
    const f32x2 d = v[i] - mb;
    const unsigned w = pack2(d[0], d[1]);
    const f32x2 y = (f32x2){lo16(w), hi16(w)} * rden;
    a[i >> 2][i & 3] = pack2(y[0], y[1]);
  }
}

template <int N>
__device__ __forceinline__ void fin_out(rsrc_t rs, const Ptrs& P, unsigned po, unsigned go, unsigned eo, int fw = -1, int want = 0) {
  u32x4 pv[N][8], gv[N], xv[N];
  #pragma unroll
  for (int c = 0; c < N; ++c) {
    gv[c] = ld16(rs, go + c * 4096);
    xv[c] = __builtin_nontemporal_load((const u32x4*)((const char*)P.x + eo + c * 256));
    if (N == 1) {
      if (fw >= 0) flag_wait<3>(P, fw, want, opq(0));
      BARRIER();
    }
    #pragma unroll
    for (int i = 0; i < 8; ++i) pv[c][i] = ld16(rs, po + c * 16384 + (i >> 1) * 131072 + (i & 1) * 1024);
  }
  #pragma unroll
  for (int c = 0; c < N; ++c) {
#define PF4(i) __builtin_bit_cast(f32x4, pv[c][i])
    u32x4 v = epi_pack((PF4(0) + PF4(2)) + (PF4(4) + PF4(6)), (PF4(1) + PF4(3)) + (PF4(5) + PF4(7)));
    #pragma unroll
    for (int k = 0; k < 4; ++k) {
      v[k] = pack2(lo16(v[k]) * lo16(gv[c][k]), hi16(v[k]) * hi16(gv[c][k]));
      v[k] = pack2(lo16(xv[c][k]) + lo16(v[k]), hi16(xv[c][k]) + hi16(v[k]));
    }
    *(u32x4*)((char*)P.out + eo + c * 256) = v;
  }
}

__device__ __forceinline__ void mlp_role(LAS unsigned char* lds, const Ptrs& P, int hf, int slot, int wid) {
  const int wr = wid >> 2, wc = wid & 3;
  const int p2 = hf * 8 + (slot >> 2), pn = slot & 3;
  char* ws = P.ws;
  const rsrc_t rs = WS_RSRC(P);
  const u16* const p1 = P.w1; const u16* const p2w = P.w2; const u16* const p3 = P.w3; const u16* const p4 = P.w4;
#define M_DONE(c) do { VMWAIT(); BARRIER(); if (wid == 0) cnt_add(ws, c, opq(0)); } while (0)
  #pragma unroll 1
  for (int code = 0; code < 7; ++code) {
    int ln = opq(0);
    if (wid == 0) {
      if (code == 0) flag_wait(P, F_LN1(p2 * 4 + (ln & 3)), 0, ln);
      else if (code < 3 || code == 6) {
        const unsigned co = CNT_OFF + (code == 1 ? C_W1(min(max(p2 - 1 + min(ln, 2), 0), 15)) : code == 2 ? C_LN(p2) : C_W3(p2)) * 128;
        for (unsigned spins = 0; __any(ld4(rs, co) < 4u);) {
          __builtin_amdgcn_s_sleep(2);
          if (spin_fail(spins, P, ln)) break;
        }
      }
    }
    BARRIER();
    TLW(3 * code);
    const bool w3 = code >= 2 && code < 6, wide = code != 5;
    const int lda = code == 6 ? 7168 : 2048, ldb = w3 ? 7168 : 2048, c2 = w3 ? (code - 2) * 8 + (wide ? 2 * pn : pn) : 2 * pn;
    const rsrc_t rb = __builtin_amdgcn_make_buffer_rsrc((void*)(code == 0 ? p1 : code == 1 ? p2w : w3 ? p3 : p4), 0, code < 2 ? DM * DM * 2 : DM * FF * 2, RSRC_W3);
    const unsigned cA = (code == 0 ? WS_XN2 : code == 1 ? WS_H1 : w3 ? WS_X1N : WS_H3) + p2 * 128 * lda, cB = c2 * 256;
    Pf pf;
    pf.on = wc == 0 && (!wr || code < 3 || code == 6); pf.a = wr;
    ln &= 31;
    pf.v = wr ? cA + (pn * 2 + (ln >> 4)) * 16 * lda + (ln & 15) * 128 : cB + ((slot >> 2) * 8 + (ln >> 2)) * ldb + (ln & (wide ? 3 : 1)) * 128;
    pf.step = wr ? 2048 : 64 * ldb;
    f32x4 acq[1][2][4][2];
    gemm_h(lds, rs, rb, cA, cB, lda, ldb, code == 6 ? 56 : 16, pf, wide, acq);
    TLW(3 * code + 1);
    ln = opq(0);
    if (code == 1) {
      epi_rows<2>(acq, rs, WS_X1, 2048, p2, c2, 0, true, wr, wc, ln);
      M_DONE(C_W2(p2));
      if (wid == 0) cnt_wait(P, C_W2(p2), 4, ln);
      BARRIER();
      const unsigned ro = (unsigned)(p2 * 8 + pn * 2 + wr) * 32768u + ((ln >> 2) & 3) * 1024 + blk_in(wc * 4 + (ln >> 4), (ln & 3) * 8);
      u32x4 v[8];
      #pragma unroll
      for (int i = 0; i < 8; ++i) v[i] = ld16(rs, WS_X1 + ro + i * 4096);
      ln_rows(v);
      #pragma unroll
      for (int i = 0; i < 8; ++i) st16(rs, WS_X1N + ro + i * 4096, v[i]);
      M_DONE(C_LN(p2));
    } else if (code == 6) {
      epi_rows<3>(acq, rs, WS_G, 2048, p2, c2, 0, true, wr, wc, ln);
      M_DONE(C_W4(p2));
    } else {
      VMWAIT();
      if (code == 0 && wid < 4) touch(p2w, DM * DM * 2, ((hf * 32 + slot) * 256 + wid * 64 + ln) * 128, lds);
      epi_rows<1>(acq, rs, code ? WS_H3 : WS_H1, ldb, p2, c2, code ? 0 : (pn & 1) ? -1 - (pn >> 1) : 1 + (pn >> 1), wide, wr, wc, ln);
      if (code == 0 && wid == 0) setup_wait(P, ln);
      if (code == 0 || code == 5) M_DONE(code ? C_W3(p2) : C_W1(p2));
    }
    TLW(3 * code + 2);
  }
}

#define FINE 52
__device__ __forceinline__ void fin_role(LAS unsigned char* lds, const Ptrs& P, int wid) {
  const int wr = wid >> 2, wc = wid & 3;
  const rsrc_t rs = WS_RSRC(P);
  volatile LAS int* tk = (volatile LAS int*)(lds + 147456);
  unsigned tks = 0;
#define POP() do { if (wid == 0 && opq(0) == 0) tks = __hip_atomic_fetch_add((gu32*)(P.ws + CNT_OFF + C_HEAD * 128), 1u, RLX_AGENT); } while (0)
  POP();
  for (int round = 0;; ++round) {
    if (wid == 0 && opq(0) == 0) tk[round & 1] = tks;
    LBARRIER();
    const int u = __builtin_amdgcn_readfirstlane(tk[round & 1]);
    if (u >= 2 * FINE + 8 * (XS3 - FINE)) break;
    const bool big = u < 2 * FINE;
    const int sg = big ? u >> 1 : FINE + ((u - 2 * FINE) >> 3), cc0 = big ? (u & 1) * 4 : (u - 2 * FINE) & 7;
    const int ln = opq(0), fq = ln >> 4, cb = 16 * (fq & 1) + 8 * (fq >> 1);
    const unsigned eo = (unsigned)(sg * 32 + wr * 16 + (ln & 15)) * 2048u + (unsigned)(cc0 * 128 + wc * 32 + cb) * 2u;
    const unsigned go = WS_G + (unsigned)(sg * 2 + wr) * 32768u + (cc0 * 4 + wc) * 1024 + blk_in(ln & 15, cb);
    const unsigned po = WS_PT + (unsigned)sg * 524288u + cc0 * 16384 + wc * 4096 + wr * 2048 + ln * 16;
    if (wid == 1) cnt_wait(P, C_W4(sg >> 2), 4, ln);
    const int xg = (sg >= XS1) + (sg >= XS2), want = sg + 1 - (xg == 2 ? XS2 : xg * XS1);
    if (wid == 2 && big) flag_wait(P, F_PRG(5 + xg, ln & 31), want, ln);
    BARRIER();
    if (!big) fin_out<1>(rs, P, po, go, eo, wid == 2 ? F_PRG(5 + xg, (ln & 3) * 8 + cc0) : -1, want);
    else { fin_out<2>(rs, P, po, go, eo); fin_out<2>(rs, P, po + 32768, go + 8192, eo + 512); }
    POP();
  }
}

__device__ __forceinline__ u32x4 ldsv(LAS unsigned char* p) { return *(volatile LAS u32x4*)p; }
#define DMA_A dman
#define DMA_V dman
#define DMA_O dmaw
#define TOUCH 0
#define TAUX 0
#define POISON4 ((u32x4){~0u, ~0u, ~0u, ~0u})
#define DLYM 0
#define DLYA 8
#define DLYK (DLYA + 8)
#define DLYO 8
#define FLA 0
#define FLB 0
#define CADM 1
#define CADO 1

#define RTC(id)
#define DCA 3
#define DCB 2
#define DL1 10
#define DL2 17
#define DLB 18
#define ADL 0
#define SN0 150
#define HELP 256
#define BORD 0
#define QT 0
__device__ __forceinline__ void track(int& est, int d, int up, int dn) { est += max(min(d - est, up), min(-dn, (d - est) >> 1)); }
__device__ __forceinline__ unsigned rfl(unsigned v) { unsigned r; asm volatile("s_nop 0\n\tv_readfirstlane_b32 %0, %1\n\ts_nop 4" : "=s"(r) : "v"(v)); return r; }
__device__ __forceinline__ void plan_wait(rsrc_t rs, unsigned fo, int s, int& lim, unsigned& tm, unsigned& d) {
  while ((d & 255u) != (unsigned)s && (lim -= DEC1) > 0) {
    __builtin_amdgcn_s_sleep(1);
    const u32x2 v = __builtin_amdgcn_raw_buffer_load_b64(rs, 0, fo, AUX_SC1);
    tm = rfl(v.x); d = rfl(v.y);
  }
  const unsigned now = (unsigned)__builtin_amdgcn_s_memrealtime() * 4u;
  tm = now + min((int)(tm - now), 2000);
  d = min(d >> 8, 1000u);
}
#define SPIN_BUDGET (1 << 21)

__device__ __forceinline__ unsigned rec_go(LAS unsigned char* w, const Ptrs& P, u32x2 f, int wid, int lane) {
  if (wid >= 6) {
    if (__any(f.y != P.tag)) f.x = flag_wait(P, wid == 6 ? F_LN0(lane) : F_CAL(lane), 0, lane);
    if (wid == 7) { const unsigned m = near_half(f.x); if (lane == 0) *(volatile LAS unsigned*)w = m; }
  }
  LBARRIER();
  return __builtin_amdgcn_readfirstlane(*(volatile LAS unsigned*)w);
}

#define SLOT(i) ((i) * 16384)
#define LA_MEM SLOT(8)
#define LA_Q SLOT(9)
#define LA_K (SLOT(9) + 2048)
#define LA_P (SLOT(9) + 4096)
#define LA_VM (SLOT(9) + 6144)
#define LA_KX (SLOT(9) + 8192)

template <bool QW>
__device__ __forceinline__ void a_wave(LAS unsigned char* lds, Ptrs& P, int h, int wid) {
  char* ws = P.ws;
  const int lane = opq(0), tid = wid * 64 + lane;
  const int j = wid & 3, g = lane >> 4, q4 = (lane & 15) >> 2, p4 = lane & 3;
  const rsrc_t rs = WS_RSRC(P);
  const rsrc_t rq = __builtin_amdgcn_make_buffer_rsrc((void*)P.Wq, 0, HD * DM * 2, RSRC_W3);
  const rsrc_t rk = __builtin_amdgcn_make_buffer_rsrc((void*)P.Wk, 0, HD * DM * 2, RSRC_W3);
  const unsigned lbase = (unsigned)(size_t)lds;
  int lim = SPIN_BUDGET;
  u32x8 wr[12][2];
  const int trow = 16 * (g >> 1) + 4 * (g & 1) + 8 * (q4 >> 1) + (q4 & 1);
  const int tsw = ((q4 & 1) | ((g & 1) << 1) | ((q4 >> 1) << 2)) << 1;
  const int toff = trow * 256 + (((4 * j + (p4 >> 1)) ^ tsw) << 4) + 8 * (p4 & 1);
  {
    unsigned dv[2];
    #pragma unroll
    for (int x = 0; x < 2; ++x) {
      const int row = 8 * wid + 4 * x + (lane >> 4);
      const int sw = ((row & 1) | (((row >> 2) & 1) << 1) | (((row >> 3) & 1) << 2)) << 1;
      dv[x] = (unsigned)(row * (HD * 2) + h * 256 + (((lane & 15) ^ sw) << 4));
    }
#define A_GRP(n) do { _Pragma("unroll") for (int i = 0; i < 8; ++i) \
      DMA_A(((n) & 1) ? rk : rq, dv[i & 1], (4 * (((n) & 1) ? ((n) >> 1) ^ ((n) >> 2) : (((n) >> 1) + 1) & 3) + (i >> 1)) * 64 * HD * 2, lbase + SLOT(4 * ((n) & 1) + (i >> 1)) + wid * 2048 + (i & 1) * 1024); } while (0)
    A_GRP(0); A_GRP(1);
    #pragma unroll
    for (int n = 0; n < 6; ++n) {
      VMW(8);
      BARRIER();
      if ((n & 1) == (QW ? 0 : 1)) {
        #pragma unroll
        for (int b = 0; b < 4; ++b) {
          LAS unsigned char* t = lds + SLOT(QW ? 0 : 4) + ((b ^ j) << 14);
          wr[4 * (n >> 1) + b][0] = rd_frag<8192, 512>(t + toff);
          wr[4 * (n >> 1) + b][1] = rd_frag<8192, 512>(t + (toff ^ 32));
        }
        #pragma unroll
        for (int b = 0; b < 4; ++b) A_PIN(QW, 4 * (n >> 1) + b);
      }
      LBARRIER();
      A_GRP(n + 2);
    }
  }
  const unsigned cb = comm_base(ws);
  u32x2 f0 = {0u, 0u};
  #pragma unroll
  for (int rd = 0; rd < 2; ++rd) {
    if (rd == 0) VMW(8);
    else {
      if (wid >= 6) f0 = ld8(rs, FLG_OFF + (wid == 6 ? F_LN0(lane) : F_CAL(lane)) * 128);
      const unsigned fv = wid == 3 ? FLG_OFF + F_PRG(min((lane >> 1) & 3, 2), h) * 128 + (lane & 1) * 4 : WS_QF + j * (16u << 20) + h * 8192 + lane * 128;
      if (QW) asm volatile("s_mov_b32 m0, %2\n\ts_nop 0\n\tbuffer_load_dword %0, %1, 0 offen sc1 lds" :: "v"(fv), "s"(rs), "s"(lbase + LA_P + (wid == 3 ? 640 : 1024)) : "memory");
      get_tag(P);
      VMWAIT();
    }
    BARRIER();
    u32x8 f[2][2];
    #pragma unroll
    for (int i = 0; i < 2; ++i) {
      f[i][0] = rd_frag<8192, 512>(lds + SLOT((QW ? 0 : 2) + 4 * rd + i) + toff);
      f[i][1] = rd_frag<8192, 512>(lds + SLOT((QW ? 0 : 2) + 4 * rd + i) + (toff ^ 32));
    }
    asm volatile("" : "+v"(f[0][0]), "+v"(f[0][1]), "+v"(f[1][0]), "+v"(f[1][1]));
    LBARRIER();
    #pragma unroll
    for (int i = 0; i < 2; ++i)
      #pragma unroll
      for (int t = 0; t < 2; ++t) {
        LAS unsigned char* p = lds + SLOT((QW ? 0 : 2) + 4 * rd + i) + j * 4096 + t * 2048 + lane * 16;
        *(LAS u32x4*)p = (u32x4){f[i][t][0], f[i][t][1], f[i][t][2], f[i][t][3]};
        *(LAS u32x4*)(p + 1024) = (u32x4){f[i][t][4], f[i][t][5], f[i][t][6], f[i][t][7]};
      }
  }
  const unsigned mask = rec_go(lds + LA_P + 128, P, f0, wid, lane);
  const int idx = (lane & 1) ? 0xEEEE : 0x4444;
  const unsigned ab_a = lbase + LA_MEM + lane * 16, am_a = ab_a + j * 1024;
  const unsigned t0_a = lbase + SLOT((QW ? 0 : 4) + j) + j * 4096 + lane * 16;
  const unsigned iw_a = lbase + (QW ? LA_Q : LA_K) + j * 512 + q4 * 128 + g * 32 + p4 * 4;
  const unsigned qr_a = lbase + LA_Q + g * 128 + (lane & 7) * 16;
  const unsigned kr_a = lbase + LA_K + g * 128 + (2 * q4 + (lane & 1)) * 16;
  const unsigned fr_a = lbase + LA_P + p4 * 4;
  if (tid < 12) *(LAS unsigned*)(lds + LA_P + tid * 4) = 0u;
  const unsigned vo_v = VB_AT(h) + j * 512 + lane * 4, md = lbase + LA_VM - VB_AT(h);
  const unsigned oo_v = OB_AT((lane >> 2) & 1, h) + (j * 16 + (g >> 1) * 8 + p4 * 2 + (g & 1)) * 16;
  const unsigned long long onej = 1ull << j;
  const unsigned lkx = lbase + LA_KX + j * 2048;
  u32x8 oxr = {};
  const unsigned xo_v = (lane >> 5) * 2048 + (lane & 31) * 16, vl_a = lbase + LA_VM + (j & 1) * 1024 + lane * 4;
  const int dq = 16 * (j >> 1) + (lane & 15) - 4 * g;
  const unsigned fb = step_base(cb, mask, 0);
  unsigned tgt = (unsigned)__builtin_amdgcn_s_memrealtime() * 4u;
  unsigned long long tp = 0;
  unsigned pt = 0, pd = 0;
  const unsigned long long fmask = __builtin_amdgcn_readfirstlane((16u >> wid) & 1u);
  int known = 0;
  __builtin_amdgcn_s_setprio(QW ? 2 : 1);
  LBARRIER();
  report(P, wid);
  TL(72);
  for (int s = 0; s < NSEG; ++s) {
    const unsigned sb = step_base(cb, mask, s);
    const unsigned sbm = sb - (lbase + LA_MEM);
    const unsigned fo = (unsigned)(s * NH + h) * 8192u + j * 2048 - (lbase + LA_MEM);
    GSDECL;
    RTC(h);
    lim = __builtin_amdgcn_readfirstlane(lim);
    if (QW) {
      if (wid == 3 && s > 0) {
        if (opq(0) == 0) __builtin_amdgcn_raw_buffer_store_b32((unsigned)tp * 4u - tgt, rs, 0, fb + FL_O(h), AUX_SC1);
        plan_wait(rs, fb + FL_T(s), s, lim, pt, pd);
        tgt = pt;
      }
      if (s >= known) {
        const u32x2 v = *(volatile LAS u32x2*)(lds + LA_P + 640 + (opq(0) & 3) * 8);
        if (!__any(v.y != P.tag)) known = rfl(min(min(__builtin_amdgcn_readlane(v.x, 0), __builtin_amdgcn_readlane(v.x, 1)), __builtin_amdgcn_readlane(v.x, 2)));
      }
      kv_wait(P, s, h, known, opq(0));
      const unsigned kxo = WS_KF + fo, vxo = WS_VF + fo;
      if (wid < 3) for (int i = 0; i < ADL; ++i) __builtin_amdgcn_s_sleep(1);
      dmas(rs, ab_a, kxo, lkx);
      dmas(rs, ab_a, kxo + 1024, lkx + 1024);
      dmas(rs, ab_a, WS_QF + fo, lbase + LA_P + 1024);
      dmas(rs, ab_a, WS_QF + fo + 1024, lbase + LA_P + 1024);
      if (wid == 2 && s + 1 >= known) {
        const int ln = opq(0);
        const unsigned fv = FLG_OFF + F_PRG(min((ln >> 1) & 3, 2), h) * 128 + (ln & 1) * 4;
        asm volatile("s_mov_b32 m0, %2\n\ts_nop 0\n\tbuffer_load_dword %0, %1, 0 offen sc1 lds" :: "v"(fv), "s"(rs), "s"(lbase + LA_P + 640) : "memory");
      }
      unsigned rg = 0x3c000000u;
      A_MAIN_Q();
      TL(s);
      GSFLUSH(0, 24 + wid); GSFLUSH(1, 4 + wid); GSFLUSH(2, 12 + j); GSFLUSH(3, 16 + j); GSFLUSH(4, 20 + j);
      if (s + PD < NSEG && !(opq(0) & 8)) __builtin_amdgcn_raw_buffer_store_b128(poison4(), rs, oo_v, step_base(cb, mask, s + PD), AUX_SC1);
    } else {
      const unsigned qxo = WS_QF + fo - (j & 1) * 2048, oxo = WS_OX + (unsigned)((s == 0 || s == NSEG - 1 ? s : s - 1) * NH + h) * 8192u + (j & 1) * 4096 + (j >> 1) * 1024;
      A_MAIN_K();
      if (s >= NSEG - 2) {
        __builtin_amdgcn_raw_buffer_store_b128((u32x4){oxr[0], oxr[1], oxr[2], oxr[3]}, rs, xo_v, oxo + (NSEG - 1 - s) * NH * 8192, AUX_SC1);
        __builtin_amdgcn_raw_buffer_store_b128((u32x4){oxr[4], oxr[5], oxr[6], oxr[7]}, rs, xo_v + 512, oxo + (NSEG - 1 - s) * NH * 8192, AUX_SC1);
      }
      if (wid == 7) GSFLUSH(0, 28);
      GSFLUSH(1, 4 + wid);
    }
  }
  VMWAIT();
  BARRIER();
  if (wid == 4) flag_set(P, F_PRG(4, h), NSEG, opq(0));
}

#define TSL 4
#define LB_MEM 0
#define LB_RED 16384
template <bool VW>
__device__ __forceinline__ void b_wave(LAS unsigned char* lds, Ptrs& P, int r, int c, int wid, int bs, int xg) {
  constexpr int NHD = VW ? 2 : 6;
  char* ws = P.ws;
  const int lane = opq(0), tid = wid * 64 + lane;
  const int j = wid & 3, g = lane >> 4, q4 = (lane & 15) >> 2, p4 = lane & 3;
  const int b = r * 32 + c;
  const rsrc_t rs = WS_RSRC(P);
  const rsrc_t rv = __builtin_amdgcn_make_buffer_rsrc((void*)P.Wv, 0, HD * DM * 2, RSRC_W3);
  const rsrc_t ro = __builtin_amdgcn_make_buffer_rsrc((void*)P.Wo, 0, HD * DM * 2, RSRC_W3);
  const unsigned lbase = (unsigned)(size_t)lds;
  int lim = SPIN_BUDGET;
  const int h0 = ((VW ? 2 * j : 8 + 6 * j) + 5 * c) & 31;
  u32x8 wv[16], wo[6][2][2];
  {
    if ((wid & 3) < 2 && xg < 2) {
      const unsigned nk = 8 + 2 * xg, M = (bs * 2 + (wid & 1)) * 64 + lane, row = M / nk, k = M - row * nk;
      if (row < HELP) touch(VW ? P.Wq : P.Wk, HD * DM * 2, (row < 256 ? row + (VW ? 512 : 256) : row + 512) * (HD * 2) + min(xg * 4096 + (k >> 1) * 1024 + 256, 7936u) + (k & 1) * 128, lds);
    }
    const int nl = xg == 2 ? 6 : 5;
    #pragma unroll
    for (int i = 0; i < 2; ++i) {
      const unsigned L = bs * 1024 + wid * 128 + i * 64 + lane, row = L / nl;
      touch(P.Wo, HD * DM * 2, row * (DM * 2) + (c - (bs >> 1)) * 64 + (L - row * nl) * 128, lds);
    }
    VMWAIT();
    BARRIER();
    const int pos = lane >> 2;
    const int prow = (pos & 9) | ((pos & 2) << 1) | ((pos & 4) >> 1);
    const unsigned dv0 = (unsigned)(prow * (DM * 2) + c * 64 + ((lane & 3) << 4));
    const unsigned dv1 = (unsigned)(prow * (DM * 2) + c * 64 + (((lane & 3) ^ 2) << 4));
    const int toff = wid * 16384 + (g * 16 + q4) * 64 + (((p4 >> 1) ^ ((g & 1) << 1)) << 4) + 8 * (p4 & 1);
    #pragma unroll
    for (int rd = 0; rd < NHD / 2; ++rd) {
      #pragma unroll
      for (int hh = 0; hh < 2; ++hh) {
        const int h = (h0 + 2 * rd + hh) & 31;
        #pragma unroll
        for (int i = 0; i < 8; ++i)
          DMA_O(ro, (i & 1) ? dv1 : dv0, (unsigned)(h * 128 + i * 16) * (DM * 2), lbase + wid * 16384 + hh * 8192 + i * 1024);
      }
      VMWAIT();
      #pragma unroll
      for (int hh = 0; hh < 2; ++hh)
        #pragma unroll
        for (int ks = 0; ks < 2; ++ks)
          #pragma unroll
          for (int t = 0; t < 2; ++t)
            wo[2 * rd + hh][ks][t] = rd_frag<4096, 256>(lds + (toff ^ (t * 32)) + hh * 8192 + ks * 512);
      #pragma unroll
      for (int hh = 0; hh < 2; ++hh)
        WO_PIN(VW, 2 * rd + hh);
      asm volatile("s_waitcnt lgkmcnt(0)" ::: "memory");
    }
    const int lr = lane >> 3;
    #pragma unroll
    for (int i = 0; i < 16; ++i) {
      const int sw = (((lr >> 2) & 1) | ((i & 1) << 1)) << 1;
      const unsigned dv = (unsigned)(lr * (HD * 2) + b * 128 + (((lane & 7) ^ sw) << 4));
      DMA_V(rv, dv, (unsigned)(wid * 128 + i * 8) * (HD * 2), lbase + wid * 16384 + i * 1024);
    }
    VMWAIT();
    BARRIER();
    if (VW) {
      const int trow = 16 * (g >> 1) + 4 * (g & 1) + 8 * (q4 >> 1) + (q4 & 1);
      const int tsw = ((g & 1) | ((q4 >> 1) << 1)) << 1;
      LAS unsigned char* tv = lds + trow * 128 + (((2 * j + (p4 >> 1)) ^ tsw) << 4) + 8 * (p4 & 1);
      #pragma unroll
      for (int ks = 0; ks < 16; ++ks) wv[ks] = rd_frag<4096, 256>(tv + ks * 8192);
      V_PIN();
    }
    LBARRIER();
  }
  const unsigned cb = comm_base(ws);
  get_tag(P);
  if (wid == 0) touch(P.x, SEQ * DM * 2, (SEQ - 64 + lane) * 2048 + c * 64, lds);
  const unsigned mask = rec_go(lds + LB_RED + 4096 + 64, P, (u32x2){0u, P.tag - 1u}, wid, lane);
  const int idx = (lane & 1) ? 0xEEEE : 0x4444;
  const unsigned am_a = lbase + LB_MEM + lane * 16;
  const unsigned fb = step_base(cb, mask, 0);
  volatile LAS unsigned* tsl = (volatile LAS unsigned*)(lds + LB_RED + 4096);
  if (tid < 8) tsl[tid] = tid ? 0u : (unsigned)__builtin_amdgcn_s_memrealtime() * 4u;
  unsigned dn = 600;
  int da = 570, gb = 360;
  if (VW) __builtin_amdgcn_s_setprio(2);
  LBARRIER();
  report(P, wid);
  TL(72);
  for (int s = 0; s < NSEG; ++s) {
    const unsigned sb = step_base(cb, mask, s);
    const int ln = opq(lane), gg = ln >> 4;
    GSDECL;
    RTC(32 + b);
    const int l16 = ln * 16;
    const int xc = 32 * (c & 1) + 2 * (ln & 15);
    const unsigned mo_v = MF_AT(c >> 1) + ((xc >> 4) * 16 + (4 * r + gg) * 2 + ((xc >> 3) & 1)) * 16 + (xc & 7) * 2;
    if (!VW) {
      const unsigned tm = __builtin_amdgcn_readfirstlane(*tsl), tgt = tm + DLB, la = lbase + LB_MEM + j * 2048 + l16, mo = sb + j * 2048;
      B_POLL();
      if (wid == 7) {
        const int late = (int)((unsigned)__builtin_amdgcn_s_memrealtime() * 4u - tgt) - SN0;
        const unsigned to = tm + dn + (late > 80 ? late : 0);
        if (ln == 0) tsl[1] = to;
        if (b == 0) {
          dn = da + DL1;
          if (ln == 0) { *tsl = to + gb + DL2; st8(rs, fb + FL_T(s + 1), (u32x2){to + gb + DL2, (dn << 8) | (s + 1)}); }
          const unsigned va = ld4(rs, fb + FL_O(ln & 31)), vb = ld4(rs, fb + FL_B(ln));
          if (s > 2) {
            track(da, rfl(wmax(va)), DCA, DCB);
            track(gb, rfl(wmax(vb)), DCA, DCB);
          }
        }
      }
      if (wid == 7 && s > 0 && s + 2 < NSEG) __builtin_amdgcn_raw_buffer_store_b32(~0u, rs, mo_v, step_base(cb, mask, s + 2), AUX_SC1);
    } else {
      GS(3);
      const unsigned vo = VB_AT(b >> 1) + ((b & 1) * 4 + j) * 256;
      V_PROJ();
      GS(0); GSFLUSH(0, 4 + j);
      if (s + PD < NSEG && ln < 16) st16(rs, step_base(cb, mask, s + PD) + vo + l16, poison4());
    }
    GSFLUSH(3, 24 + wid);
    if (s + 1 == NSEG) break;
    unsigned so[6];
    #pragma unroll
    for (int i = 0; i < NHD; ++i) { const int h = (h0 + i) & 31; so[i] = __builtin_amdgcn_readfirstlane(sb + OB_AT(r, h)); }
    const unsigned sn = __builtin_amdgcn_readfirstlane(step_base(cb, mask, s + 1));
    const unsigned rw_a = lbase + LB_RED + (wid * 128 + (gg & 1) * 64 + (gg >> 1) * 16 + (ln & 15)) * 4;
    const unsigned rr_a = lbase + LB_RED + ln * 8;
    if (wid == 7) {
      if (b != 0) {
        __builtin_amdgcn_s_sleep(4);
        unsigned tm = 0;
        dn = 0;
        plan_wait(rs, fb + FL_T(s + 1), s + 1, lim, tm, dn);
        if (ln == 0) *tsl = tm;
      }
      const unsigned tgt = rfl(tsl[1]);
      T_WAIT();
    }
    lim = __builtin_amdgcn_readfirstlane(lim);
    unsigned long long t1;
    if (VW) WO_V(); else WO_W();
    if (wid == 0) {
      if (ln == 0) st4(rs, fb + FL_B(b), (unsigned)t1 * 4u - tsl[1]);
    }
    if (wid == 0) {
      GSFLUSH(2, 16);
      TL(s);
    }
    GSFLUSH(1, 8 + wid);
    if (wid == 0 || wid == 4) { GSFLUSH(4, 17 + (wid >> 2)); GSFLUSH(0, 19 + (wid >> 2)); }
  }
  __builtin_amdgcn_s_setprio(0);
  const int ln = opq(0);
  TL(64);
  volatile LAS int* rdy = (volatile LAS int*)(lds + 98304 + 3 * 16384);
  if (tid == 0) *rdy = 61;
  LBARRIER();
#define T_SRC(sg, i) (WS_OX + (unsigned)((sg) * NH + ((h0 + (i)) & 31)) * 8192u + r * 1024)
#define T_MMA(i, k, f) do { const u32x8 w0 = wo[i][(k) & 1][0], w1 = wo[i][(k) & 1][1]; const int e = ((k) >> 1) * 4; \
    a0 = MFMA32(((u32x4){w0[e], w0[e + 1], w0[e + 2], w0[e + 3]}), f, a0); a1 = MFMA32(((u32x4){w1[e], w1[e + 1], w1[e + 2], w1[e + 3]}), f, a1); } while (0)
#define T_DMA(sg, i) do { _Pragma("unroll") for (int k = 0; k < 4; ++k) dmas(rs, ln * 16, T_SRC(sg, i) + k * 2048, lbase + j * 24576 + (i) * 4096 + k * 1024); } while (0)
  const int cb2 = 16 * ((ln >> 4) & 1) + 8 * (ln >> 5);
  const unsigned eo = (unsigned)((NSEG - 1 - j) * 32 + r * 16 + (ln & 15)) * 2048u + (unsigned)(c * 32 + cb2) * 2u;
  u32x4 gv, xv;
  if (VW && wid < NSEG - XS3) {
    xv = *(const u32x4*)((const char*)P.x + eo);
    cnt_wait(P, C_W4(15), 4, ln);
    gv = ld16(rs, WS_G + (unsigned)((NSEG - 1 - j) * 2 + r) * 32768u + c * 1024 + blk_in(ln & 15, cb2));
  }
  bool ahead = false;
  f32x4 k0, k1;
  #pragma unroll 1
  for (int sg = XS3; sg < NSEG; ++sg) {
    f32x4 a0 = zero4(), a1 = zero4();
    if (VW) {
      if (wid == 3) {
        for (int w = __builtin_amdgcn_readfirstlane(*rdy); w <= sg;) {
          w = max(w + 1, -wmax(-(int)flag_wait<TSL>(P, F_PRG(4, ln & 31), w + 1, ln)));
          if (ln == 0) *rdy = w;
        }
      } else while (__builtin_amdgcn_readfirstlane(*rdy) <= sg) __builtin_amdgcn_s_sleep(1);
      u32x4 of[8];
      #pragma unroll
      for (int i = 0; i < 8; ++i) of[i] = __builtin_amdgcn_raw_buffer_load_b128(rs, ln * 16 + (i & 3) * 2048, T_SRC(sg, i >> 2), AUX_SC1);
      #pragma unroll
      for (int i = 0; i < 8; ++i) T_MMA(i >> 2, i & 3, of[i]);
    } else {
      if (!ahead) {
        while (__builtin_amdgcn_readfirstlane(*rdy) <= sg) __builtin_amdgcn_s_sleep(1);
        #pragma unroll
        for (int i = 0; i < NHD; ++i) T_DMA(sg, i);
      }
      VMW(20);
      ahead = __builtin_amdgcn_readfirstlane(*rdy) > sg + 1;
      u32x4 of[2][4];
#define T_RD(i) do { _Pragma("unroll") for (int k = 0; k < 4; ++k) of[(i) & 1][k] = *(const LAS u32x4*)(lds + j * 24576 + (i) * 4096 + k * 1024 + ln * 16); } while (0)
      T_RD(0);
      #pragma unroll
      for (int i = 0; i < NHD; ++i) {
        asm volatile("s_waitcnt lgkmcnt(0)" : "+v"(of[i & 1][0]), "+v"(of[i & 1][1]), "+v"(of[i & 1][2]), "+v"(of[i & 1][3]) :: "memory");
        if (i + 1 < NHD) {
          if (ahead) VMW(16); else G_WAIT_V(16 - 4 * (i < 4 ? i : 4));
          T_RD(i + 1);
        }
        #pragma unroll
        for (int k = 0; k < 4; ++k) {
          if (ahead) dmas(rs, ln * 16, T_SRC(sg + 1, i) + k * 2048, lbase + j * 24576 + i * 4096 + k * 1024);
          T_MMA(i, k, of[i & 1][k]);
          __builtin_amdgcn_sched_barrier(0);
        }
      }
    }
    LAS f32x4* pw = (LAS f32x4*)(lds + 98304 + ((sg & 3) * 8 + wid) * 2048) + ln;
    if (wid == 0 && sg == NSEG - 1) { k0 = a0; k1 = a1; } else { pw[0] = a0; pw[64] = a1; }
  }
  if (VW && wid < NSEG - XS3) {
    LBARRIER();
    const LAS f32x4* pr = (const LAS f32x4*)(lds + 98304 + (3 - j) * 16384) + ln;
    f32x4 t[2];
    #pragma unroll
    for (int k = 0; k < 2; ++k)
      t[k] = (((wid ? pr[k * 64] : k ? k1 : k0) + pr[128 + k * 64]) + (pr[256 + k * 64] + pr[384 + k * 64])) + ((pr[512 + k * 64] + pr[640 + k * 64]) + (pr[768 + k * 64] + pr[896 + k * 64]));
    u32x4 v = epi_pack(t[0], t[1]);
    #pragma unroll
    for (int k = 0; k < 4; ++k) {
      v[k] = pack2(lo16(v[k]) * lo16(gv[k]), hi16(v[k]) * hi16(gv[k]));
      v[k] = pack2(lo16(xv[k]) + lo16(v[k]), hi16(xv[k]) + hi16(v[k]));
    }
    *(u32x4*)((char*)P.out + eo) = v;
  } else LBARRIER();
}

#define DMA_S dman
#define XDL 4
#define XPC 0
#define PW3 11
#define PW4 25
#define LS_P 131072
template <int MODE>
__device__ __forceinline__ void s_role(LAS unsigned char* lds, Ptrs& P, const u16* W, int h, unsigned obase, int nseg, int fl, int wid, int s0) {
  constexpr bool XM = MODE >= 2;
  constexpr unsigned RS = XM ? DM * 2 : HD * 2, SS = XM ? 262144u : 65536u;
  const int lane = opq(0);
  const int kh = wid >> 2, tp = wid & 3;
  const rsrc_t rs = WS_RSRC(P);
  const rsrc_t rw = __builtin_amdgcn_make_buffer_rsrc((void*)W, 0, HD * DM * 2, RSRC_W3);
  const unsigned lbase = (unsigned)(size_t)lds;
  u32x8 w[8][2];
  if (XM && XDL) {
    if (wid == 0) flag_wait(P, F_PRG(4, h), max(s0 - XDL, 0), lane);
    BARRIER();
  }
  {
    const int g = lane >> 4, q4 = (lane & 15) >> 2, p4 = lane & 3;
    const int trow = XM ? 16 * g + 4 * (q4 >> 1) + (q4 & 1) : 8 * g + q4;
#define S_SW(row) ((((row) & 1) | ((((row) >> (XM ? 2 : 1)) & 1) << 1) | ((((row) >> (XM ? 4 : 3)) & 1) << 2)) << 1)
    const int toff = trow * 256 + (((4 * tp + (p4 >> 1)) ^ S_SW(trow)) << 4) + 8 * (p4 & 1);
    unsigned dv[2];
    #pragma unroll
    for (int x = 0; x < 2; ++x) {
      const int row = 8 * wid + 4 * x + (lane >> 4);
      dv[x] = (unsigned)row * RS + (XM ? (h >> 3) * 1024 * RS + (h & 7) * 256 : h * 256) + (((lane & 15) ^ S_SW(row)) << 4);
    }
#define S_IMG(i) do { DMA_S(rw, dv[0], (i) * 64 * RS, lbase + ((i) & 7) * 16384 + wid * 2048); \
                      DMA_S(rw, dv[1], (i) * 64 * RS, lbase + ((i) & 7) * 16384 + wid * 2048 + 1024); if (XM && XPC) __builtin_amdgcn_s_sleep(XPC); } while (0)
    #pragma unroll
    for (int i = 0; i < 8; ++i) S_IMG(i);
    #pragma unroll
    for (int b = 0; b < 4; ++b) {
      if (b == 3 && !XM) get_tag(P);
      if (b < 3) VMW(8); else VMWAIT();
      BARRIER();
      if (kh == (b >> 1)) {
        #pragma unroll
        for (int i = 4 * (b & 1); i < 4 * (b & 1) + 4; ++i) {
          w[i][0] = rd_frag<XM ? 2048 : 8192, XM ? 512 : 1024>(lds + toff + i * 16384);
          w[i][1] = rd_frag<XM ? 2048 : 8192, XM ? 512 : 1024>(lds + (toff ^ 32) + i * 16384);
        }
      }
      LBARRIER();
      if (b < 2) {
        #pragma unroll
        for (int i = 4 * b + 8; i < 4 * b + 12; ++i) S_IMG(i);
      }
    }
  }
  TL(XM ? 74 : 72);
  const unsigned l16 = lane * 16;
  volatile LAS int* rd = (volatile LAS int*)(lds + LS_P + 20480);
#define X_READY(want) do { \
    if (wid == 0) { \
      const int n_ = -wmax(-(int)flag_wait(P, F_PRG(4, (h & 24) + (lane & 7)), s0 + (want), lane)) - s0; \
      if (lane == 0) *rd = max(n_, want); \
    } \
    LBARRIER(); } while (0)
  if (MODE == 2) X_READY(2);
  else if (!XM) {
    if (wid == 0) flag_wait(P, F_LN0(lane & 7), 0, lane);
    BARRIER();
  }
  unsigned sx = (XM ? WS_OX + s0 * SS + (h >> 3) * 65536 : WS_XN) + kh * 32768 + tp * 4096;
  const unsigned sd = lbase + kh * 65536 + tp * 4096;
  const unsigned xl = kh * 65536 + (XM ? l16 : blk_in(lane & 15, (lane >> 4) * 8));
  const unsigned xp = lbase + xl, pa = lbase + LS_P + tp * 4096 + l16;
#define S_MMA(a, xo, k0, k1) do { _Pragma("unroll") for (int ks = k0; ks < k1; ++ks) _Pragma("unroll") for (int i = 0; i < 4; ++i) { \
    const u32x4 x = *(const LAS u32x4*)(lds + (xo) + ks * 2048 + (i >> 1) * 1024); \
    const u32x8 wf = w[ks >> 1][i & 1]; \
    const u32x4 wh = {wf[(ks & 1) * 4], wf[(ks & 1) * 4 + 1], wf[(ks & 1) * 4 + 2], wf[(ks & 1) * 4 + 3]}; \
    a[i] = MODE == 1 ? MFMA32(x, wh, a[i]) : MFMA32(wh, x, a[i]); } } while (0)
  LAS f32x4* const pq = (LAS f32x4*)(lds + LS_P + tp * 4096 + l16);
  if (MODE == 3) {
#define X_GET(t) do { _Pragma("unroll") for (int i = 0; i < 16; ++i) \
      dmas(rs, l16, sx + (t) * SS + ((i >> 2) & 1) * 32768 + (i >> 3) * 16384 + (i & 3) * 1024, sd + ((i >> 2) & 1) * 65536 + ((t) & 1) * 32768 + (i >> 3) * 16384 + (i & 3) * 1024); } while (0)
    bool have = false;
    const unsigned pv = FLG_OFF + F_PRG(kh ? 7 : 4, kh ? h : (h & 24) + (lane & 7)) * 128;
    if (!kh) __builtin_amdgcn_s_setprio(1);
    #pragma unroll 1
    for (int t = 0; t < nseg; ++t) {
      if (!have && !kh) {
        flag_wait<2>(P, F_PRG(4, (h & 24) + (lane & 7)), s0 + t + 1, lane);
        X_GET(t);
        VMW(8);
      }
      BARRIER();
      const unsigned xp = lbase + xl + (t & 1) * 32768;
      f32x4 a[4];
      unsigned pg;
      X_ONE();
      have = t + 1 < nseg && !__any(pg < (unsigned)(s0 + t + 2));
      pq[kh * 128] = kh ? a[0] : a[2]; pq[kh * 128 + 64] = kh ? a[1] : a[3];
      if (have && !kh) X_GET(t + 1);
      LBARRIER();
      const unsigned oo = obase + (unsigned)(s0 + t) * 0x80000u + (unsigned)h * 16384u + tp * 4096 + kh * 2048 + l16;
      st16(rs, oo, __builtin_bit_cast(u32x4, (kh ? a[2] : a[0]) + pq[128 - kh * 128]));
      st16(rs, oo + 1024, __builtin_bit_cast(u32x4, (kh ? a[3] : a[1]) + pq[192 - kh * 128]));
      VMWAIT();
      BARRIER();
      if (wid == 1) flag_set(P, fl, t + 1, lane);
      TL(t);
    }
    __builtin_amdgcn_s_setprio(0);
    TL(65);
    return;
  }
  if (XM) {
    #pragma unroll
    for (int i = 0; i < 12; ++i) dmas(rs, l16, sx + (i >> 3) * SS + ((i >> 2) & 1) * 16384 + (i & 3) * 1024, sd + (i >> 2) * 16384 + (i & 3) * 1024);
    VMWAIT();
    BARRIER();
  } else {
    #pragma unroll
    for (int i = 0; i < 16; ++i) dmas(rs, l16, sx + (i >> 3) * SS + ((i >> 2) & 1) * 16384 + (i & 3) * 1024, sd + ((i >> 2) ^ 2) * 16384 + (i & 3) * 1024);
    VMW(8);
    BARRIER();
    f32x4 a[4] = {};
    S_MMA(a, xl + 32768, 0, 16);
    if (!kh) {
      #pragma unroll
      for (int i = 0; i < 4; ++i) pq[i * 64] = a[i];
    }
    LBARRIER();
    if (kh) {
      #pragma unroll
      for (int i = 0; i < 4; ++i) a[i] += pq[i * 64];
      #pragma unroll
      for (int i = 0; i < 2; ++i) {
        const f32x4 a0 = a[MODE ? i : 2 * i], a1 = a[MODE ? 2 + i : 2 * i + 1];
        if (MODE) st16(rs, obase + h * 8192 + (2 * tp + i) * 1024 + l16, (u32x4){pack2(a0[0], a0[1]), pack2(a0[2], a0[3]), pack2(a1[0], a1[1]), pack2(a1[2], a1[3])});
        else st16(rs, obase + h * 8192 + i * 4096 + tp * 1024 + l16, (u32x4){pack2(a0[0], a1[0]), pack2(a0[1], a1[1]), pack2(a0[2], a1[2]), pack2(a0[3], a1[3])});
      }
    }
    sx += SS;
    #pragma unroll
    for (int i = 0; i < 4; ++i) dmas(rs, l16, sx + SS + i * 1024, sd + 32768 + i * 1024);
    VMW(4);
    BARRIER();
    if (wid == 1) flag_set(P, fl, 1, lane);
    TL(62);
  }
  u32x8 xf[3], ac[2] = {};
  #pragma unroll
  for (int i = 0; i < 3; ++i) {
    const u32x4 a = *(const LAS u32x4*)(lds + xl + i * 2048), b = *(const LAS u32x4*)(lds + xl + i * 2048 + 1024);
    xf[i] = (u32x8){a[0], a[1], a[2], a[3], b[0], b[1], b[2], b[3]};
  }
  unsigned long long fm, pm;
  asm volatile("s_cmp_eq_u32 %2, 1\n\ts_cselect_b64 %0, 1, 0\n\ts_cmp_eq_u32 %3, 1\n\ts_cselect_b64 %1, -1, 0" : "=&s"(fm), "=&s"(pm) : "s"(wid), "s"(XM ? 0 : wid >> 1) : "scc");
  const unsigned fo = FLG_OFF + fl * 128, pd = lbase + LS_P + 16384;
  const unsigned po = 4 * 65536 + h * 2048 + (wid & 1) * 1024 - tp * 4096 - kh * 32768;
  unsigned oo = XM ? obase + (unsigned)h * 16384u + tp * 4096 + (s0 - 1) * 0x80000u : obase + (unsigned)h * 8192u + tp * (MODE ? 2048 : 1024), tmp, tmp2;
  unsigned long long em;
  int t = XM ? 0 : 1;
  if (MODE < 2) {
    #pragma unroll 1
    for (int part = 0; part < 4; ++part) {
      if (part == 1) { TL(63); if (wid == 0) setup_wait(P, lane); }
      if (MODE == 1 && part >= 2) {
        #pragma unroll
        for (int i = 0; i < 4; ++i) {
          const unsigned to = ((i * 32 + h) * 512 + wid * 64 + opq(0)) * 128;
          if (part == 2) touch(P.w3, DM * FF * 2, to, lds); else touch(P.w4, DM * FF * 2, to, lds);
        }
      }
      const int te = part == 0 ? 3 : part == 1 ? PW3 : part == 2 ? PW4 : nseg + 1;
      if (MODE) S_LOOP_V(te, 0); else S_LOOP_K(te, 0);
    }
  } else {
    #pragma unroll 1
    while (t < nseg) {
      X_READY(min(t + 4, nseg));
      const int n = __builtin_amdgcn_readfirstlane(*rd), te = n >= nseg ? nseg : (n - 2) & ~1;
      TL(t);
      S_LOOP_X(te, nseg);
    }
  }
  TL(XM ? 65 : 64);
  VMWAIT();
  BARRIER();
  if (wid == 1) flag_set(P, fl, nseg, lane);
}

#define LND 1
#define CALN 6
__device__ __forceinline__ void setup_role(LAS unsigned char* lds, Ptrs& P, int cu, int wid) {
  const int lane = opq(0), tid = wid * 64 + lane;
  const rsrc_t rs = WS_RSRC(P);
  u32x4 v[8];
  const u32x4* px = (const u32x4*)(P.x + (size_t)(cu * 32 + wid * 4 + (lane >> 4)) * DM) + (lane & 15);
  #pragma unroll
  for (int i = 0; i < 8; ++i) v[i] = __builtin_nontemporal_load(px + i * 16);
  const unsigned epo = __hip_atomic_load((gu32*)(P.ws + EPO_OFF), RLX_AGENT);
  const unsigned cb = comm_base(P.ws);
  const u32x4 z = {0u, 0u, 0u, 0u};
  const int cr = 63 - cu;
  if (wid == 0 && cr < 16) st16(rs, CNT_OFF + cr * 1024 + lane * 16, z);
  if (cr < 4) st16(rs, cb + FL_B(0) + cr * 8192 + tid * 16, z);
  if (cr == 4 && tid < 256) {
    const int g4 = tid >> 6, k = (tid >> 5) & 1 & (g4 >> 1);
    st16(rs, WS_H1 + ((g4 & 1) ? 127u * 32768u : 0u) + (g4 * 8 + ((tid >> 2) & 7)) * 1024 + blk_in((g4 & 1) ? 15 - k : k, (tid & 3) * 8), z);
  }
  #pragma unroll
  for (int i = 0; i < 2; ++i) {
    const int f = i * 54 + cr;
    if (cr < PD * NCHUNK) {
      u32x4 m = {~0u, ~0u, ~0u, ~0u};
      if (f < 4) {
        const int ks = (f >> 1) * 8 + wid;
        m = *(const u32x4*)(P.mem0 + ((lane >> 1) & 7) * DM + 64 * ks + 16 * (lane >> 4) + 8 * (lane & 1));
        m.x = canon1(m.x); m.y = canon1(m.y); m.z = canon1(m.z); m.w = canon1(m.w);
      }
      st16(rs, cb + (unsigned)(f / (NCHUNK * 2)) * CB_STEP + (f % (NCHUNK * 2)) * 8192u + tid * 16, m);
    }
  }
  const unsigned bi = blk_in((wid & 3) * 4 + (lane >> 4), (lane & 3) * 8);
  const int c0 = (lane >> 2) & 3;
  ln_rows(v);
  TL(69);
  if (cu >= 8) for (int i = 0; i < LND; ++i) __builtin_amdgcn_s_sleep(32);
  #pragma unroll
  for (int i = 0; i < 8; ++i) st16(rs, WS_XN + xn_blk(cu * 2 + (wid >> 2), 4 * i + c0) + bi, v[i]);
  P.tag = __builtin_amdgcn_readfirstlane(epo) + 1u;
  ln_rows(v);
  if (cu < 8) {
    VMWAIT();
    BARRIER();
    if (wid == 0) flag_set(P, F_LN0(cu), 1, lane);
    TL(66);
  }
  asm volatile("" ::: "memory");
  #pragma unroll
  for (int i = 0; i < 8; ++i) st16(rs, WS_XN2 + (unsigned)(cu * 2 + (wid >> 2)) * 32768u + (4 * i + c0) * 1024 + bi, v[i]);
  touch(P.w1, DM * DM * 2, ((cu & 31) * 512 + tid) * 128, lds);
  VMW(9);
  BARRIER();
  if (wid == 0) flag_set(P, F_LN0(cu), 1, lane);
  TL(70);
  if (wid == 0) VMW(2); else VMW(1);
  BARRIER();
  if (wid == 0) flag_set(P, F_LN1(cu), 1, lane);
  TL(71);
  if (cu < 32 && (wid >> 1) == 1) {
    const int id = cu * 2 + (wid & 1);
    unsigned best[2] = {65535u, 65535u};
    #pragma unroll
    for (int it = 0; it < CALN; ++it) {
      const unsigned long long c0 = __builtin_amdgcn_s_memtime();
      asm volatile("s_waitcnt lgkmcnt(0)" ::: "memory");
      if (lane == 0) st4(rs, cb + (id & 3) * CB_STEP + (30 + ((id >> 2) & 1)) * 16384u + (it & 1) * 8192u + (id >> 3) * 1024u, 0u);
      VMWAIT();
      const unsigned d = (unsigned)(__builtin_amdgcn_s_memtime() - c0);
      if (it >= 2) best[it & 1] = min(best[it & 1], d);
    }
    flag_set(P, F_CAL(id), (best[0] << 16) | best[1], lane);
  }
}

#define CPF 1
__global__ __launch_bounds__(512, 2) void mega_kernel(const u16* x, const u16* mem0, const u16* Wq, const u16* Wk, const u16* Wv, const u16* Wo,
                                                     const u16* w1, const u16* w2, const u16* w3, const u16* w4, u16* out, char* ws) {
  __shared__ __attribute__((aligned(16))) unsigned char smem[163840];
  LAS unsigned char* lds = (LAS unsigned char*)smem;
  Ptrs P;
  P.x = x; P.mem0 = mem0; P.Wq = Wq; P.Wk = Wk; P.Wv = Wv; P.Wo = Wo; P.w1 = w1; P.w2 = w2; P.w3 = w3; P.w4 = w4; P.out = out; P.ws = ws;
  const int xcd = blockIdx.x & 7, slot = blockIdx.x >> 3;
  const int wid = __builtin_amdgcn_readfirstlane(threadIdx.x >> 6);
  TL(79);
  P.tag = 0;
  if (wid == 7) {
    unsigned long long pc;
    unsigned len;
    asm volatile("s_getpc_b64 %0\nMK_BEG:\n\ts_mov_b32 %1, MK_END-MK_BEG" : "=s"(pc), "=s"(len));
    touch((const void*)pc, len, (slot * 64 + opq(0)) * 128, lds);
  }
  const int na = xcd == 2 ? 8 : 12;
  const bool isa = xcd < 3 && slot < na;
  const int ah = xcd == 2 ? slot * 4 + 2 : xcd * 16 + (slot & 3) * 4 + (slot >> 2) + (slot >> 3), bs = slot - na, br = bs & 1, bc = (xcd == 1 ? 22 : xcd * 5) + (bs >> 1);
  if (isa) {
    if (wid < 4) a_wave<true>(lds, P, ah, wid); else a_wave<false>(lds, P, ah, wid);
  } else if (xcd < 3) {
    if (wid < 4) b_wave<true>(lds, P, br, bc, wid, bs, xcd); else b_wave<false>(lds, P, br, bc, wid, bs, xcd);
  } else {
    if ((xcd & 3) == 3) {
      setup_role(lds, P, (xcd >> 2) * 32 + slot, wid);
    } else if (xcd == 6) s_role<1>(lds, P, Wv, slot, WS_VF, NSEG, F_PRG(2, slot), wid, 0);
    else s_role<0>(lds, P, xcd == 4 ? Wk : Wq, slot, xcd == 4 ? WS_KF : WS_QF, NSEG, F_PRG(xcd - 4, slot), wid, 0);
    if ((xcd & 3) == 3) mlp_role(lds, P, xcd >> 2, slot, wid);
    else if (xcd == 6) s_role<3>(lds, P, Wo, slot, WS_PT, XS3 - XS2, F_PRG(7, slot), wid, XS2);
    else s_role<2>(lds, P, Wo, slot, WS_PT, xcd == 4 ? XS1 : XS2 - XS1, F_PRG(xcd + 1, slot), wid, xcd == 4 ? 0 : XS1);
    report(P, wid);
    fin_role(lds, P, wid);
    if (blockIdx.x == 3 && wid == 0) {
      cnt_wait(P, C_ARR, 256, threadIdx.x);
      if (threadIdx.x == 0) __hip_atomic_store((gu32*)(ws + EPO_OFF), P.tag, RLX_AGENT);
    }
  }
  asm volatile("MK_END:");
  TL(78);
}

extern "C" void kernel_launch(void* const* d_in, const int* in_sizes, int n_in, void* d_out, int out_size,
                              void* d_ws, size_t ws_size, hipStream_t stream) {
  mega_kernel<<<256, 512, 0, stream>>>((const u16*)d_in[0], (const u16*)d_in[1], (const u16*)d_in[2], (const u16*)d_in[3], (const u16*)d_in[4],
                                       (const u16*)d_in[5], (const u16*)d_in[8], (const u16*)d_in[9], (const u16*)d_in[10], (const u16*)d_in[11],
                                       (u16*)d_out, (char*)d_ws);
}
```

```cpp
#include <hip/hip_runtime.h>
#include <hip/hip_bf16.h>
#include <stdint.h>

#define SEQ 2048
#define DM 1024
#define NH 32
#define DH 128
#define HD (NH * DH)
#define MM 8
#define SEG 32
#define NSEG (SEQ / SEG)
#define FF 3584

typedef unsigned short u16;
typedef __attribute__((ext_vector_type(8))) short bf16x8;
typedef __attribute__((ext_vector_type(4))) short bf16x4;
typedef __attribute__((ext_vector_type(4))) float f32x4;
typedef __attribute__((ext_vector_type(4))) unsigned u32x4;
typedef __attribute__((ext_vector_type(2))) unsigned u32x2;
typedef __attribute__((ext_vector_type(2))) float f32x2;
typedef __attribute__((ext_vector_type(2))) __bf16 bf16v2;
typedef __attribute__((ext_vector_type(8))) unsigned u32x8;
#define LAS __attribute__((address_space(3)))
#define GAS __attribute__((address_space(1)))

__device__ __forceinline__ unsigned pack2(float lo, float hi) {
  return __builtin_bit_cast(unsigned, __builtin_convertvector((f32x2){lo, hi}, bf16v2));
}
__device__ __forceinline__ float rb1(float x) { return __uint_as_float(pack2(0.f, x)); }

typedef GAS unsigned gu32;
#define RLX_AGENT __ATOMIC_RELAXED, __HIP_MEMORY_SCOPE_AGENT
#define SPIN_CAP (1u << 20)
#define AUX_SC1 16
#define RSRC_W3 0x00020000
typedef __amdgpu_buffer_rsrc_t rsrc_t;

__device__ __forceinline__ u32x4 ld16(rsrc_t rs, unsigned off) { return __builtin_amdgcn_raw_buffer_load_b128(rs, off, 0, AUX_SC1); }
__device__ __forceinline__ u32x2 ld8(rsrc_t rs, unsigned off) { return __builtin_amdgcn_raw_buffer_load_b64(rs, off, 0, AUX_SC1); }
__device__ __forceinline__ unsigned ld4(rsrc_t rs, unsigned off) { return __builtin_amdgcn_raw_buffer_load_b32(rs, off, 0, AUX_SC1); }
__device__ __forceinline__ void st16(rsrc_t rs, unsigned off, u32x4 v) { __builtin_amdgcn_raw_buffer_store_b128(v, rs, off, 0, AUX_SC1); }
__device__ __forceinline__ void st8(rsrc_t rs, unsigned off, u32x2 v) { __builtin_amdgcn_raw_buffer_store_b64(v, rs, off, 0, AUX_SC1); }
__device__ __forceinline__ void st4(rsrc_t rs, unsigned off, unsigned v) { __builtin_amdgcn_raw_buffer_store_b32(v, rs, off, 0, AUX_SC1); }
__device__ __forceinline__ float lo16(unsigned w) { return __uint_as_float(w << 16); }
__device__ __forceinline__ float hi16(unsigned w) { return __uint_as_float(w & 0xffff0000u); }

__device__ __forceinline__ float xsum16_32(float v) {
  unsigned u = __float_as_uint(v);
  auto a = __builtin_amdgcn_permlane16_swap(u, u, false, false);
  v = __uint_as_float(a[0]) + __uint_as_float(a[1]);
  u = __float_as_uint(v);
  auto b = __builtin_amdgcn_permlane32_swap(u, u, false, false);
  return __uint_as_float(b[0]) + __uint_as_float(b[1]);
}

__device__ __forceinline__ unsigned canon1(unsigned w) { return min(w, 0xfffffffeu); }
__device__ __forceinline__ unsigned max4(u32x4 v) { return max(max(v.x, v.y), max(v.z, v.w)); }
__device__ __forceinline__ int opq(int) { int v; asm volatile("v_mbcnt_lo_u32_b32 %0, -1, 0\n\tv_mbcnt_hi_u32_b32 %0, -1, %0" : "=v"(v)); return v; }
__device__ __forceinline__ u32x4 poison4() { unsigned m = ~0u; asm volatile("" : "+v"(m)); return (u32x4){m, m, m, m}; }
__device__ __forceinline__ f32x4 zero4() { float m = 0.f; asm volatile("" : "+v"(m)); return (f32x4){m, m, m, m}; }
#define MFMA32(a, b, c) __builtin_amdgcn_mfma_f32_16x16x32_bf16(__builtin_bit_cast(bf16x8, a), __builtin_bit_cast(bf16x8, b), c, 0, 0, 0)
#define MFMA16(a, b, c) __builtin_amdgcn_mfma_f32_16x16x16bf16_1k(__builtin_bit_cast(bf16x4, a), __builtin_bit_cast(bf16x4, b), c, 0, 0, 0)
#define LBARRIER() do { asm volatile("s_waitcnt lgkmcnt(0)" ::: "memory"); __builtin_amdgcn_s_barrier(); asm volatile("" ::: "memory"); } while (0)
#define BARRIER() do { asm volatile("" ::: "memory"); __builtin_amdgcn_s_barrier(); asm volatile("" ::: "memory"); } while (0)
#define VMWAIT() asm volatile("s_waitcnt vmcnt(0)" ::: "memory")
#define VMW(n) asm volatile("s_waitcnt vmcnt(" #n ")" ::: "memory")
#define DELAY(n) do { if ((n) > 0) __builtin_amdgcn_s_sleep(n); } while (0)

#define EPO_OFF 0
#define TMO_OFF 128
#define FLG_OFF 256
#define F_LN0(i) (i)
#define F_LN1(i) (64 + (i))
#define F_CAL(i) (128 + (i))
#define F_PRG(t, h) (192 + (t) * 32 + (h))
#define CNT_OFF 65536
#define C_W1(p2) (p2)
#define C_W2(p2) (16 + (p2))
#define C_LN(p2) (32 + (p2))
#define C_W3(p2) (48 + (p2))
#define C_W4(p2) (64 + (p2))
#define C_HEAD 80
#define C_ARR 81
#define NCNT 128
#define WS_XN (1u << 20)
#define WS_XN2 (WS_XN + (4u << 20))
#define WS_QF (WS_XN2 + (4u << 20))
#define WS_KF (WS_QF + (16u << 20))
#define WS_VF (WS_KF + (16u << 20))
#define WS_OX (WS_VF + (16u << 20))
#define WS_H1 (WS_OX + (16u << 20) + (1u << 20))
#define WS_X1 (WS_H1 + (4u << 20) + 65536u)
#define WS_X1N (WS_X1 + (4u << 20))
#define WS_H3 (WS_X1N + (4u << 20))
#define WS_G (WS_H3 + (14u << 20))
#define WS_PT (WS_G + (4u << 20))
#define WS_COMM (WS_PT + (32u << 20))
#define WS_END (WS_COMM + (4u << 20))
#define CB_STEP 524288u
#define PD 3
__device__ __forceinline__ unsigned comm_base(const char* ws) { return WS_COMM + ((0u - ((unsigned)(size_t)ws + WS_COMM)) & 0x1fffffu); }
__device__ __forceinline__ unsigned step_base(unsigned cb, unsigned mask, int s) { return cb + (unsigned)(s & 3) * CB_STEP + mask; }
#define MF_AT(ks) (((ks) >> 3) * 16384u + ((ks) & 7) * 1024u)
#define OB_AT(r, h) ((2u + (r) * 4u + ((h) >> 3)) * 16384u + ((h) & 7) * 1024u)
#define VB_AT(h) ((10u + ((h) >> 2)) * 16384u + ((h) & 3) * 2048u)
#define XS1 22
#define XS2 42
#define XS3 62
#define CSL 8
#define FSL 6
#define SQUICK 3
#define NCHUNK 18
#define FL_O(h) (29u * 16384u + (h) * 128u)
#define FL_B(b) (28u * 16384u + (b) * 128u)
#define FL_T(s) FL_O(32 + ((s) & 3))

struct Ptrs { const u16 *x, *mem0, *Wq, *Wk, *Wv, *Wo, *w1, *w2, *w3, *w4; u16* out; char* ws; unsigned tag; };
#define WS_RSRC(P) __builtin_amdgcn_make_buffer_rsrc((void*)(P).ws, 0, WS_END, RSRC_W3)

__device__ __forceinline__ bool spin_fail(unsigned& spins, const Ptrs& P, int lane) {
  asm volatile("" ::: "memory");
  if (++spins > SPIN_CAP) {
    unsigned tv;
    asm volatile("v_mov_b32 %0, %1" : "=v"(tv) : "s"(P.tag));
    if (lane == 0) __builtin_amdgcn_raw_buffer_store_b32(tv, WS_RSRC(P), 0, TMO_OFF, AUX_SC1);
    return true;
  }
  return (spins & 255u) == 0u && (unsigned)__builtin_amdgcn_readfirstlane(__builtin_amdgcn_raw_buffer_load_b32(WS_RSRC(P), 0, TMO_OFF, AUX_SC1)) == P.tag;
}
__device__ __forceinline__ unsigned cnt_load(char* ws, int idx) {
  return __builtin_amdgcn_readfirstlane(__hip_atomic_load((gu32*)(ws + CNT_OFF + idx * 128), RLX_AGENT));
}
__device__ __forceinline__ void cnt_add(char* ws, int idx, int lane) {
  asm volatile("s_waitcnt vmcnt(0)" ::: "memory");
  if (lane == 0) __hip_atomic_fetch_add((gu32*)(ws + CNT_OFF + idx * 128), 1u, RLX_AGENT);
}
__device__ __forceinline__ void cnt_wait(const Ptrs& P, int idx, unsigned want, int lane) {
  for (unsigned spins = 0;;) {
    if (cnt_load(P.ws, idx) >= want) break;
    __builtin_amdgcn_s_sleep(CSL);
    if (spin_fail(spins, P, lane)) break;
  }
}
template <int SL = FSL>
__device__ __forceinline__ unsigned flag_wait(const Ptrs& P, int f, unsigned want, int lane) {
  const rsrc_t rs = WS_RSRC(P);
  u32x2 v;
  for (unsigned spins = 0;;) {
    v = ld8(rs, FLG_OFF + f * 128);
    if (!__any(v.y != P.tag || v.x < want)) break;
    __builtin_amdgcn_s_sleep(SL);
    if (spin_fail(spins, P, lane)) break;
  }
  asm volatile("" ::: "memory");
  return v.x;
}
__device__ __forceinline__ void flag_set(const Ptrs& P, int f, unsigned val, int lane) {
  if (lane == 0) st8(WS_RSRC(P), FLG_OFF + f * 128, (u32x2){val, P.tag});
}
__device__ __forceinline__ void get_tag(Ptrs& P) { P.tag = __builtin_amdgcn_readfirstlane(__hip_atomic_load((gu32*)(P.ws + EPO_OFF), RLX_AGENT)) + 1u; }
__device__ __forceinline__ void setup_wait(const Ptrs& P, int lane) { flag_wait(P, F_LN0(lane), 0, lane); }
__device__ __forceinline__ void clock_wait(unsigned tgt) {
  while ((int)(tgt - (unsigned)__builtin_amdgcn_s_memrealtime()) > 0) {}
}
__device__ __forceinline__ void report(const Ptrs& P, int wid) {
  if (wid == 0 && opq(0) == 0) __hip_atomic_fetch_add((gu32*)(P.ws + CNT_OFF + C_ARR * 128), 1u, RLX_AGENT);
}
__device__ __forceinline__ void kv_wait(const Ptrs& P, int s, int h, int& known, int lane) {
  if (s < known) return;
  const unsigned v = flag_wait(P, F_PRG(min(lane, 2), h), s + 1, lane);
  known = __builtin_amdgcn_readfirstlane(min(min(__builtin_amdgcn_readlane(v, 0), __builtin_amdgcn_readlane(v, 1)), __builtin_amdgcn_readlane(v, 2)));
}
__device__ __forceinline__ int wmax(int v) {
  v = max(v, __builtin_amdgcn_update_dpp(0, v, 0x128, 0xf, 0xf, false));
  v = max(v, __builtin_amdgcn_update_dpp(0, v, 0x124, 0xf, 0xf, false));
  v = max(v, __builtin_amdgcn_update_dpp(0, v, 0x122, 0xf, 0xf, false));
  v = max(v, __builtin_amdgcn_update_dpp(0, v, 0x121, 0xf, 0xf, false));
  return max(max(__builtin_amdgcn_readlane(v, 0), __builtin_amdgcn_readlane(v, 16)), max(__builtin_amdgcn_readlane(v, 32), __builtin_amdgcn_readlane(v, 48)));
}
__device__ __forceinline__ unsigned near_half(unsigned v) { return wmax(-(int)(v & 65535u)) > wmax(-(int)(v >> 16)) ? 8192u : 0u; }
__device__ __forceinline__ void dmaw(rsrc_t rs, unsigned voff, unsigned soff, unsigned ldsaddr) {
  asm volatile("s_mov_b32 m0, %3\n\ts_nop 0\n\tbuffer_load_dwordx4 %0, %1, %2 offen lds" :: "v"(voff), "s"(rs), "s"(soff), "s"(ldsaddr) : "memory");
}
__device__ __forceinline__ void dman(rsrc_t rs, unsigned voff, unsigned soff, unsigned ldsaddr) {
  asm volatile("s_mov_b32 m0, %3\n\ts_nop 0\n\tbuffer_load_dwordx4 %0, %1, %2 offen nt lds" :: "v"(voff), "s"(rs), "s"(soff), "s"(ldsaddr) : "memory");
}
__device__ __forceinline__ void dmas(rsrc_t rs, unsigned voff, unsigned soff, unsigned ldsaddr) {
  asm volatile("s_mov_b32 m0, %3\n\ts_nop 0\n\tbuffer_load_dwordx4 %0, %1, %2 offen sc1 lds" :: "v"(voff), "s"(rs), "s"(soff), "s"(ldsaddr) : "memory");
}
__device__ __forceinline__ void touch(const void* w, unsigned bytes, unsigned voff, LAS unsigned char* lds) {
  asm volatile("s_mov_b32 m0, %2\n\ts_nop 0\n\tbuffer_load_dword %0, %1, 0 offen lds"
               :: "v"(voff), "s"(__builtin_amdgcn_make_buffer_rsrc((void*)w, 0, bytes, RSRC_W3)), "s"((unsigned)(size_t)lds + 148480) : "memory");
}
__device__ __forceinline__ u32x2 trd(LAS unsigned char* p) {
  return __builtin_bit_cast(u32x2, __builtin_amdgcn_ds_read_tr16_b64_v4i16((LAS bf16x4*)p));
}
template <int BO, int UO>
__device__ __forceinline__ u32x8 rd_frag(LAS unsigned char* p) {
  const u32x2 a = trd(p), b = trd(p + UO), c = trd(p + BO), d = trd(p + BO + UO);
  return (u32x8){a.x, a.y, b.x, b.y, c.x, c.y, d.x, d.y};
}

#define TL(i) do {} while (0)
#define TLW(i) do {} while (0)
#define GSDECL
#define GS(i) do {} while (0)
#define GSFLUSH(k, i) do {} while (0)

#define QPRIO 0
#define VPOS 3
#define VLDI "buffer_load_dword v254, v17, %[rs], %[sb] offen sc1\n\tbuffer_load_dword v255, v17, %[rs], %[sb] offen offset:256 sc1\n\t"
#define VLD(p) VLD##p
#define VLD0
#define VLD1
#define VLD2
#define VLD3 VLDI
#define VLD4
#define DEC1 1
#define DEC2 1
#define DGA
#define DGB
#define DGC
#define DGO
#define DGP
#define DGQ
#define PACE "s_sleep 1\n\t"
#define STR_(x) #x
#define STR(x) STR_(x)
#define GSW
#define GSA(k)
#define GSO(k)
#define A_MAIN_Q() \
  asm volatile( \
    "v_mov_b32 v253, %[tag]\n\t" \
    "buffer_load_dwordx4 v[20:23], v12, %[rs], %[vx] offen sc1\n\t" \
    "buffer_load_dwordx4 v[24:27], v12, %[rs], %[vx] offen offset:1024 sc1\n\t" \
    "ds_read_b128 v[236:239], v9 offset:0\n\t" \
    "ds_read_b128 v[240:243], v9 offset:1024\n\t" \
    "ds_read_b128 v[244:247], v9 offset:2048\n\t" \
    "ds_read_b128 v[248:251], v9 offset:3072\n\t" \
    "v_mov_b32 v0, 0\n\t" \
    "v_mov_b32 v1, 0\n\t" \
    "v_mov_b32 v2, 0\n\t" \
    "v_mov_b32 v3, 0\n\t" \
    "v_mov_b32 v4, 0\n\t" \
    "v_mov_b32 v5, 0\n\t" \
    "v_mov_b32 v6, 0\n\t" \
    "v_mov_b32 v7, 0\n\t" \
    "s_cmp_lg_u32 %[sc], 3\n\t" \
    "s_cbranch_scc1 PB_%=\n\t" \
    "TWA_%=:\n\t" \
    "s_memrealtime s[88:89]\n\t" \
    "s_memtime s[90:91]\n\t" \
    "s_waitcnt lgkmcnt(0)\n\t" \
    "s_lshl_b32 s88, s88, 2\n\t" \
    "s_sub_u32 s89, %[tgt], s88\n\t" \
    "s_cmp_gt_i32 s89, 16\n\t" \
    "s_cbranch_scc1 TWA_%=\n\t" \
    "s_mul_i32 s89, s89, 11\n\t" \
    "s_ashr_i32 s89, s89, 1\n\t" \
    "s_add_u32 s89, s89, s90\n\t" \
    DGB \
    "TCA_%=:\n\t" \
    "s_memtime s[92:93]\n\t" \
    "s_waitcnt lgkmcnt(0)\n\t" \
    "s_sub_u32 s93, s89, s92\n\t" \
    "s_cmp_gt_i32 s93, 0\n\t" \
    "s_cbranch_scc1 TCA_%=\n\t" \
    "PB_%=:\n\t" \
    "s_waitcnt lgkmcnt(0)\n\t" \
    "s_barrier\n\t" \
    "buffer_load_dwordx4 v[220:223], v8, %[rs], %[m0] offen sc1\n\t" \
    "buffer_load_dwordx4 v[224:227], v8, %[rs], %[m1] offen sc1\n\t" \
    "PM_%=:\n\t" \
    "s_waitcnt vmcnt(1)\n\t" \
    "ds_write_b128 v8, v[220:223] offset:0\n\t" \
    "v_max3_u32 v252, v220, v221, v222\n\t" \
    "s_waitcnt vmcnt(0)\n\t" \
    "ds_write_b128 v8, v[224:227] offset:4096\n\t" \
    "v_max3_u32 v252, v252, v223, v224\n\t" \
    "v_max3_u32 v252, v252, v225, v226\n\t" \
    "v_max_u32 v252, v252, v227\n\t" \
    "v_cmp_eq_u32 vcc, -1, v252\n\t" \
    "s_cbranch_vccz PD_%=\n\t" \
    "s_sub_u32 %[lim], %[lim], 1\n\t" \
    "s_cmp_gt_i32 %[lim], 0\n\t" \
    "s_cbranch_scc0 PD_%=\n\t" \
    "s_sleep " STR(CADM) "\n\t" \
    "buffer_load_dwordx4 v[220:223], v8, %[rs], %[m0] offen sc1\n\t" \
    "buffer_load_dwordx4 v[224:227], v8, %[rs], %[m1] offen sc1\n\t" \
    "s_branch PM_%=\n\t" \
    "PD_%=:\n\t" \
    GSA(0) \
    "v_smfmac_f32_16x16x64_bf16 v[0:3], v[220:223], v[236:243], v10\n\t" \
    "v_xor_b32 v9, 16384, v9\n\t" \
    "ds_read_b128 v[236:239], v9 offset:0\n\t" \
    "ds_read_b128 v[240:243], v9 offset:1024\n\t" \
    "v_smfmac_f32_16x16x64_bf16 v[4:7], v[220:223], v[244:251], v10\n\t" \
    "ds_read_b128 v[244:247], v9 offset:2048\n\t" \
    "ds_read_b128 v[248:251], v9 offset:3072\n\t" \
    "s_waitcnt lgkmcnt(4)\n\t" \
    "s_barrier\n\t" \
    "ds_read_b128 v[228:231], v8 offset:8192\n\t" \
    "ds_read_b128 v[232:235], v8 offset:12288\n\t" \
    "v_xor_b32 v8, 1024, v8\n\t" \
    "ds_read_b128 v[220:223], v8 offset:0\n\t" \
    "v_smfmac_f32_16x16x64_bf16 v[0:3], v[224:227], v[28:35], v10\n\t" \
    "v_smfmac_f32_16x16x64_bf16 v[4:7], v[224:227], v[36:43], v10\n\t" \
    "ds_read_b128 v[224:227], v8 offset:4096\n\t" \
    "s_waitcnt lgkmcnt(3)\n\t" \
    "v_smfmac_f32_16x16x64_bf16 v[0:3], v[228:231], v[92:99], v10\n\t" \
    "v_smfmac_f32_16x16x64_bf16 v[4:7], v[228:231], v[100:107], v10\n\t" \
    "ds_read_b128 v[228:231], v8 offset:8192\n\t" \
    "s_waitcnt lgkmcnt(3)\n\t" \
    "v_smfmac_f32_16x16x64_bf16 v[0:3], v[232:235], v[156:163], v10\n\t" \
    "v_smfmac_f32_16x16x64_bf16 v[4:7], v[232:235], v[164:171], v10\n\t" \
    "ds_read_b128 v[232:235], v8 offset:12288\n\t" \
    "s_waitcnt lgkmcnt(3)\n\t" \
    "v_smfmac_f32_16x16x64_bf16 v[0:3], v[220:223], v[236:243], v10\n\t" \
    "v_xor_b32 v9, 49152, v9\n\t" \
    "ds_read_b128 v[236:239], v9 offset:0\n\t" \
    "v_smfmac_f32_16x16x64_bf16 v[4:7], v[220:223], v[244:251], v10\n\t" \
    "ds_read_b128 v[240:243], v9 offset:1024\n\t" \
    "s_waitcnt lgkmcnt(4)\n\t" \
    "v_smfmac_f32_16x16x64_bf16 v[0:3], v[224:227], v[44:51], v10\n\t" \
    "v_xor_b32 v8, 3072, v8\n\t" \
    "ds_read_b128 v[220:223], v8 offset:4096\n\t" \
    "v_smfmac_f32_16x16x64_bf16 v[4:7], v[224:227], v[52:59], v10\n\t" \
    "ds_read_b128 v[244:247], v9 offset:2048\n\t" \
    "s_waitcnt lgkmcnt(5)\n\t" \
    "v_smfmac_f32_16x16x64_bf16 v[0:3], v[228:231], v[108:115], v10\n\t" \
    "ds_read_b128 v[224:227], v8 offset:8192\n\t" \
    "v_smfmac_f32_16x16x64_bf16 v[4:7], v[228:231], v[116:123], v10\n\t" \
    "ds_read_b128 v[248:251], v9 offset:3072\n\t" \
    "s_waitcnt lgkmcnt(6)\n\t" \
    "v_smfmac_f32_16x16x64_bf16 v[0:3], v[232:235], v[172:179], v10\n\t" \
    "ds_read_b128 v[228:231], v8 offset:0\n\t" \
    "v_smfmac_f32_16x16x64_bf16 v[4:7], v[232:235], v[180:187], v10\n\t" \
    "ds_read_b128 v[232:235], v8 offset:12288\n\t" \
    "s_waitcnt lgkmcnt(5)\n\t" \
    "v_smfmac_f32_16x16x64_bf16 v[0:3], v[220:223], v[60:67], v10\n\t" \
    "v_smfmac_f32_16x16x64_bf16 v[4:7], v[220:223], v[68:75], v10\n\t" \
    "v_xor_b32 v8, 1024, v8\n\t" \
    "ds_read_b128 v[220:223], v8 offset:4096\n\t" \
    "s_waitcnt lgkmcnt(4)\n\t" \
    "v_smfmac_f32_16x16x64_bf16 v[0:3], v[224:227], v[124:131], v10\n\t" \
    "v_smfmac_f32_16x16x64_bf16 v[4:7], v[224:227], v[132:139], v10\n\t" \
    "ds_read_b128 v[224:227], v8 offset:8192\n\t" \
    "s_waitcnt lgkmcnt(3)\n\t" \
    "v_smfmac_f32_16x16x64_bf16 v[0:3], v[228:231], v[236:243], v10\n\t" \
    "v_xor_b32 v9, 16384, v9\n\t" \
    "ds_read_b128 v[236:239], v9 offset:0\n\t" \
    "v_smfmac_f32_16x16x64_bf16 v[4:7], v[228:231], v[244:251], v10\n\t" \
    "ds_read_b128 v[240:243], v9 offset:1024\n\t" \
    "s_waitcnt lgkmcnt(4)\n\t" \
    "v_smfmac_f32_16x16x64_bf16 v[0:3], v[232:235], v[188:195], v10\n\t" \
    "ds_read_b128 v[228:231], v8 offset:12288\n\t" \
    "v_smfmac_f32_16x16x64_bf16 v[4:7], v[232:235], v[196:203], v10\n\t" \
    "ds_read_b128 v[244:247], v9 offset:2048\n\t" \
    "s_waitcnt lgkmcnt(5)\n\t" \
    "v_smfmac_f32_16x16x64_bf16 v[0:3], v[220:223], v[76:83], v10\n\t" \
    "ds_read_b128 v[232:235], v8 offset:0\n\t" \
    "v_xor_b32 v8, 3072, v8\n\t" \
    "v_smfmac_f32_16x16x64_bf16 v[4:7], v[220:223], v[84:91], v10\n\t" \
    "ds_read_b128 v[248:251], v9 offset:3072\n\t" \
    "s_waitcnt lgkmcnt(6)\n\t" \
    "v_smfmac_f32_16x16x64_bf16 v[0:3], v[224:227], v[140:147], v10\n\t" \
    "ds_read_b128 v[220:223], v12 offset:24576\n\t" \
    "v_smfmac_f32_16x16x64_bf16 v[4:7], v[224:227], v[148:155], v10\n\t" \
    "ds_read_b128 v[224:227], v12 offset:28672\n\t" \
    "s_waitcnt lgkmcnt(5)\n\t" \
    "v_smfmac_f32_16x16x64_bf16 v[0:3], v[228:231], v[204:211], v10\n\t" \
    "v_smfmac_f32_16x16x64_bf16 v[4:7], v[228:231], v[212:219], v10\n\t" \
    "ds_read_b128 v[228:231], v12 offset:25600\n\t" \
    "s_waitcnt lgkmcnt(3)\n\t" \
    "v_smfmac_f32_16x16x64_bf16 v[0:3], v[232:235], v[236:243], v10\n\t" \
    "v_xor_b32 v9, 49152, v9\n\t" \
    "v_smfmac_f32_16x16x64_bf16 v[4:7], v[232:235], v[244:251], v10\n\t" \
    VLD(4) \
    "s_nop 5\n\t" \
    "v_add_f32 v0, v0, v1\n\t" \
    "v_add_f32 v2, v2, v3\n\t" \
    "v_add_f32 v4, v4, v5\n\t" \
    "v_add_f32 v6, v6, v7\n\t" \
    "v_mul_f32 v0, %[sl], v0\n\t" \
    "v_mul_f32 v2, %[sl], v2\n\t" \
    "v_mul_f32 v4, %[sl], v4\n\t" \
    "v_mul_f32 v6, %[sl], v6\n\t" \
    "v_cvt_pk_bf16_f32 v0, v0, v4\n\t" \
    "v_cvt_pk_bf16_f32 v2, v2, v6\n\t" \
    "ds_write2_b32 v11, v0, v2 offset0:0 offset1:4\n\t" \
    "s_mov_b64 exec, %[onej]\n\t" \
    "ds_write_b32 v16, v253\n\t" \
    "s_mov_b64 exec, -1\n\t" \
    GSA(1) \
    "s_setprio " STR(QPRIO) "\n\t" \
    "ds_read_b128 v[232:235], v12 offset:29696\n\t" \
    "s_nop 5\n\t" \
    "ds_read_b128 v[0:3], v12 offset:26624\n\t" \
    "s_nop 5\n\t" \
    "ds_read_b128 v[4:7], v12 offset:27648\n\t" \
    "s_nop 5\n\t" \
    "ds_read_b32 v252, v16\n\t" \
    "ds_read_b128 v[236:239], v14 offset:0\n\t" \
    "s_nop 5\n\t" \
    "ds_read_b128 v[240:243], v14 offset:512\n\t" \
    "s_nop 5\n\t" \
    "ds_read_b128 v[244:247], v14 offset:1024\n\t" \
    "s_nop 5\n\t" \
    VLD(3) \
    "ds_read_b128 v[248:251], v14 offset:1536\n\t" \
    "s_nop 5\n\t" \
    "s_setprio 0\n\t" \
    "s_waitcnt lgkmcnt(4)\n\t" \
    "v_cmp_ne_u32 vcc, v253, v252\n\t" \
    "s_cbranch_vccnz RP_%=\n\t" \
    "CP_%=:\n\t" \
    VLD(0) \
    "s_waitcnt lgkmcnt(2)\n\t" \
    "v_mfma_f32_16x16x32_bf16 v[220:223], v[220:223], v[236:239], 0\n\t" \
    "v_mfma_f32_16x16x32_bf16 v[224:227], v[224:227], v[236:239], 0\n\t" \
    "v_mfma_f32_16x16x32_bf16 v[220:223], v[228:231], v[240:243], v[220:223]\n\t" \
    "ds_read_b128 v[228:231], v12 offset:30720\n\t" \
    "v_mfma_f32_16x16x32_bf16 v[224:227], v[232:235], v[240:243], v[224:227]\n\t" \
    "ds_read_b128 v[232:235], v12 offset:31744\n\t" \
    "s_setprio " STR(QPRIO) "\n\t" \
    VLD(1) \
    "s_waitcnt lgkmcnt(2)\n\t" \
    "v_mfma_f32_16x16x32_bf16 v[220:223], v[0:3], v[244:247], v[220:223]\n\t" \
    "v_mfma_f32_16x16x32_bf16 v[220:223], v[4:7], v[248:251], v[220:223]\n\t" \
    "s_waitcnt lgkmcnt(1)\n\t" \
    "v_mfma_f32_16x16x32_bf16 v[224:227], v[228:231], v[244:247], v[224:227]\n\t" \
    "s_waitcnt lgkmcnt(0)\n\t" \
    "v_mfma_f32_16x16x32_bf16 v[224:227], v[232:235], v[248:251], v[224:227]\n\t" \
    GSA(2) \
    "s_setprio 2\n\t" \
    "ds_read_b32 v252, v16 offset:16\n\t" \
    "ds_read_b128 v[0:3], v15 offset:0\n\t" \
    "ds_read_b128 v[4:7], v15 offset:512\n\t" \
    "ds_read_b128 v[228:231], v15 offset:1024\n\t" \
    "ds_read_b128 v[232:235], v15 offset:1536\n\t" \
    VLD(2) \
    "v_exp_f32 v220, v220\n\t" \
    "v_exp_f32 v221, v221\n\t" \
    "v_exp_f32 v222, v222\n\t" \
    "v_exp_f32 v223, v223\n\t" \
    "v_exp_f32 v224, v224\n\t" \
    "v_exp_f32 v225, v225\n\t" \
    "v_exp_f32 v226, v226\n\t" \
    "v_exp_f32 v227, v227\n\t" \
    "v_cvt_pk_bf16_f32 v220, v220, v221\n\t" \
    "v_cvt_pk_bf16_f32 v221, v222, v223\n\t" \
    "s_waitcnt lgkmcnt(4)\n\t" \
    "v_cmp_ne_u32 vcc, %[tag], v252\n\t" \
    "s_cbranch_vccnz RK_%=\n\t" \
    "CK_%=:\n\t" \
    "s_waitcnt lgkmcnt(3)\n\t" \
    "v_mfma_f32_16x16x32_bf16 v[0:3], v[0:3], v[236:239], 0\n\t" \
    "v_cvt_pk_bf16_f32 v222, v224, v225\n\t" \
    "v_cvt_pk_bf16_f32 v223, v226, v227\n\t" \
    "s_waitcnt lgkmcnt(2)\n\t" \
    "v_mfma_f32_16x16x32_bf16 v[0:3], v[4:7], v[240:243], v[0:3]\n\t" \
    "v_mov_b32 v224, %[one]\n\t" \
    "v_mov_b32 v225, %[one]\n\t" \
    "s_waitcnt lgkmcnt(1)\n\t" \
    "v_mfma_f32_16x16x32_bf16 v[0:3], v[228:231], v[244:247], v[0:3]\n\t" \
    "v_mov_b32 v226, %[one]\n\t" \
    "v_mov_b32 v227, %[one]\n\t" \
    "s_waitcnt lgkmcnt(0)\n\t" \
    "v_mfma_f32_16x16x32_bf16 v[0:3], v[232:235], v[248:251], v[0:3]\n\t" \
    "v_mfma_f32_16x16x32_bf16 v[244:247], v[224:227], v[220:223], 0\n\t" \
    "v_mov_b32 v229, 0\n\t" \
    "v_mfma_f32_16x16x32_bf16 v[236:239], v[20:23], v[220:223], 0\n\t" \
    "v_mov_b32 v230, 0\n\t" \
    "v_mfma_f32_16x16x32_bf16 v[240:243], v[24:27], v[220:223], 0\n\t" \
    "s_nop 2\n\t" \
    "v_exp_f32 v0, v0\n\t" \
    "v_exp_f32 v1, v1\n\t" \
    "s_nop 0\n\t" \
    "v_cvt_pk_bf16_f32 v228, v0, v1\n\t" \
    "v_cvt_pk_bf16_f32 v231, v0, v1\n\t" \
    GSA(3) \
    "CM_%=:\n\t" \
    "s_nop 0\n\t" \
    "v_mfma_f32_16x16x16_bf16 v[244:247], v[224:225], v[228:229], v[244:247]\n\t" \
    "s_waitcnt vmcnt(0)\n\t" \
    "v_mfma_f32_16x16x16_bf16 v[236:239], v[254:255], v[228:229], v[236:239]\n\t" \
    "v_max_u32 v7, v254, v255\n\t" \
    "v_mfma_f32_16x16x16_bf16 v[240:243], v[254:255], v[230:231], v[240:243]\n\t" \
    "v_cmp_eq_u32 s[88:89], -1, v7\n\t" \
    "s_not_b64 exec, %[hi]\n\t" \
    "s_nop 1\n\t" \
    "v_rcp_f32 v2, v244\n\t" \
    "v_subrev_u32 v253, 0x21800000, v244\n\t" \
    "v_cmp_gt_u32 vcc, v253, %[rg]\n\t" \
    "v_pk_mul_f32 v[236:237], v[236:237], v[2:3] op_sel_hi:[1,0]\n\t" \
    "v_pk_mul_f32 v[238:239], v[238:239], v[2:3] op_sel_hi:[1,0]\n\t" \
    "v_pk_mul_f32 v[240:241], v[240:241], v[2:3] op_sel_hi:[1,0]\n\t" \
    "v_pk_mul_f32 v[242:243], v[242:243], v[2:3] op_sel_hi:[1,0]\n\t" \
    "s_or_b64 vcc, vcc, s[88:89]\n\t" \
    "v_cvt_pk_bf16_f32 v4, v236, v237\n\t" \
    "v_cvt_pk_bf16_f32 v5, v238, v239\n\t" \
    "v_cvt_pk_bf16_f32 v6, v240, v241\n\t" \
    "v_cvt_pk_bf16_f32 v7, v242, v243\n\t" \
    "s_cbranch_scc1 FX_%=\n\t" \
    "buffer_store_dwordx4 v[4:7], v18, %[rs], %[sb] offen sc1\n\t" \
    "s_memrealtime %[tp]\n\t" \
    DGA \
    GSA(4) \
    "v_max3_u32 v252, v4, v5, v6\n\t" \
    "v_max_u32 v252, v252, v7\n\t" \
    "v_cmp_eq_u32 vcc, -1, v252\n\t" \
    "s_cbranch_vccnz RC_%=\n\t" \
    "CC_%=:\n\t" \
    "s_mov_b64 exec, -1\n\t" \
    "v_add_u32 v252, %[vd], v12\n\t" \
    "v_add_u32 v253, %[md], v17\n\t" \
    "ds_write_b128 v252, v[20:23]\n\t" \
    "ds_write_b128 v252, v[24:27] offset:1024\n\t" \
    "ds_write2st64_b32 v253, v254, v255 offset0:0 offset1:1\n\t" \
    "s_waitcnt lgkmcnt(0)\n\t" \
    "s_cmp_lg_u32 %[sc], 3\n\t" \
    "s_cbranch_scc1 NH_%=\n\t" \
    "buffer_load_dwordx2 v[0:1], off, %[rs], %[fl] sc1\n\t" \
    "s_barrier\n\t" \
    "s_waitcnt vmcnt(0)\n\t" \
    "v_readfirstlane_b32 %[pt], v0\n\t" \
    "v_readfirstlane_b32 %[pd], v1\n\t" \
    "s_nop 3\n\t" \
    "s_branch E_%=\n\t" \
    "NH_%=:\n\t" \
    "s_barrier\n\t" \
    "s_branch E_%=\n\t" \
    "RP_%=:\n\t" \
    "s_waitcnt lgkmcnt(0)\n\t" \
    "ds_read_b32 v252, v16\n\t" \
    "ds_read_b128 v[236:239], v14 offset:0\n\t" \
    "ds_read_b128 v[240:243], v14 offset:512\n\t" \
    "ds_read_b128 v[244:247], v14 offset:1024\n\t" \
    "ds_read_b128 v[248:251], v14 offset:1536\n\t" \
    "s_sub_u32 %[lim], %[lim], " STR(DEC1) "\n\t" \
    "s_cmp_gt_i32 %[lim], 0\n\t" \
    "s_waitcnt lgkmcnt(4)\n\t" \
    "s_cbranch_scc0 CP_%=\n\t" \
    "v_cmp_ne_u32 vcc, %[tag], v252\n\t" \
    "s_cbranch_vccnz RP_%=\n\t" \
    "s_branch CP_%=\n\t" \
    "RK_%=:\n\t" \
    "s_waitcnt lgkmcnt(0)\n\t" \
    "ds_read_b32 v252, v16 offset:16\n\t" \
    "ds_read_b128 v[0:3], v15 offset:0\n\t" \
    "ds_read_b128 v[4:7], v15 offset:512\n\t" \
    "ds_read_b128 v[228:231], v15 offset:1024\n\t" \
    "ds_read_b128 v[232:235], v15 offset:1536\n\t" \
    "s_sub_u32 %[lim], %[lim], " STR(DEC1) "\n\t" \
    "s_cmp_gt_i32 %[lim], 0\n\t" \
    "s_waitcnt lgkmcnt(4)\n\t" \
    "s_cbranch_scc0 CK_%=\n\t" \
    "v_cmp_ne_u32 vcc, %[tag], v252\n\t" \
    "s_cbranch_vccnz RK_%=\n\t" \
    "s_branch CK_%=\n\t" \
    "RC_%=:\n\t" \
    "v_min_u32 v4, -2, v4\n\t" \
    "v_min_u32 v5, -2, v5\n\t" \
    "v_min_u32 v6, -2, v6\n\t" \
    "v_min_u32 v7, -2, v7\n\t" \
    "buffer_store_dwordx4 v[4:7], v18, %[rs], %[sb] offen sc1\n\t" \
    "s_branch CC_%=\n\t" \
    "FX_%=:\n\t" \
    "s_mov_b64 exec, -1\n\t" \
    "s_cmp_eq_u64 s[88:89], 0\n\t" \
    "s_cbranch_scc1 RM_%=\n\t" \
    "buffer_load_dword v254, v17, %[rs], %[sb] offen sc1\n\t" \
    "buffer_load_dword v255, v17, %[rs], %[sb] offen offset:256 sc1\n\t" \
    "v_mfma_f32_16x16x32_bf16 v[244:247], v[224:227], v[220:223], 0\n\t" \
    "v_mfma_f32_16x16x32_bf16 v[236:239], v[20:23], v[220:223], 0\n\t" \
    "v_mfma_f32_16x16x32_bf16 v[240:243], v[24:27], v[220:223], 0\n\t" \
    "s_sub_u32 %[lim], %[lim], " STR(DEC2) "\n\t" \
    "s_cmp_gt_i32 %[lim], 0\n\t" \
    "s_cbranch_scc1 CM_%=\n\t" \
    "s_waitcnt vmcnt(0)\n\t" \
    "v_mov_b32 v254, 0\n\t" \
    "v_mov_b32 v255, 0\n\t" \
    "s_branch CM_%=\n\t" \
    "RM_%=:\n\t" \
    "s_mov_b32 %[rg], -1\n\t" \
    "s_nop 7\n\t" \
    "ds_read_b128 v[236:239], v14 offset:0\n\t" \
    "ds_read_b128 v[240:243], v14 offset:512\n\t" \
    "ds_read_b128 v[244:247], v14 offset:1024\n\t" \
    "ds_read_b128 v[248:251], v14 offset:1536\n\t" \
    "ds_read_b128 v[232:235], v12 offset:24576\n\t" \
    "s_waitcnt lgkmcnt(0)\n\t" \
    "v_mfma_f32_16x16x32_bf16 v[220:223], v[232:235], v[236:239], 0\n\t" \
    "ds_read_b128 v[232:235], v12 offset:25600\n\t" \
    "s_waitcnt lgkmcnt(0)\n\t" \
    "v_mfma_f32_16x16x32_bf16 v[220:223], v[232:235], v[240:243], v[220:223]\n\t" \
    "ds_read_b128 v[232:235], v12 offset:26624\n\t" \
    "s_waitcnt lgkmcnt(0)\n\t" \
    "v_mfma_f32_16x16x32_bf16 v[220:223], v[232:235], v[244:247], v[220:223]\n\t" \
    "ds_read_b128 v[232:235], v12 offset:27648\n\t" \
    "s_waitcnt lgkmcnt(0)\n\t" \
    "v_mfma_f32_16x16x32_bf16 v[220:223], v[232:235], v[248:251], v[220:223]\n\t" \
    "ds_read_b128 v[232:235], v12 offset:28672\n\t" \
    "s_waitcnt lgkmcnt(0)\n\t" \
    "v_mfma_f32_16x16x32_bf16 v[224:227], v[232:235], v[236:239], 0\n\t" \
    "ds_read_b128 v[232:235], v12 offset:29696\n\t" \
    "s_waitcnt lgkmcnt(0)\n\t" \
    "v_mfma_f32_16x16x32_bf16 v[224:227], v[232:235], v[240:243], v[224:227]\n\t" \
    "ds_read_b128 v[232:235], v12 offset:30720\n\t" \
    "s_waitcnt lgkmcnt(0)\n\t" \
    "v_mfma_f32_16x16x32_bf16 v[224:227], v[232:235], v[244:247], v[224:227]\n\t" \
    "ds_read_b128 v[232:235], v12 offset:31744\n\t" \
    "s_waitcnt lgkmcnt(0)\n\t" \
    "v_mfma_f32_16x16x32_bf16 v[224:227], v[232:235], v[248:251], v[224:227]\n\t" \
    "ds_read_b128 v[232:235], v15 offset:0\n\t" \
    "s_waitcnt lgkmcnt(0)\n\t" \
    "v_mfma_f32_16x16x32_bf16 v[0:3], v[232:235], v[236:239], 0\n\t" \
    "ds_read_b128 v[232:235], v15 offset:512\n\t" \
    "s_waitcnt lgkmcnt(0)\n\t" \
    "v_mfma_f32_16x16x32_bf16 v[0:3], v[232:235], v[240:243], v[0:3]\n\t" \
    "ds_read_b128 v[232:235], v15 offset:1024\n\t" \
    "s_waitcnt lgkmcnt(0)\n\t" \
    "v_mfma_f32_16x16x32_bf16 v[0:3], v[232:235], v[244:247], v[0:3]\n\t" \
    "ds_read_b128 v[232:235], v15 offset:1536\n\t" \
    "s_waitcnt lgkmcnt(0)\n\t" \
    "v_mfma_f32_16x16x32_bf16 v[0:3], v[232:235], v[248:251], v[0:3]\n\t" \
    "v_max3_f32 v252, v220, v221, v222\n\t" \
    "v_max3_f32 v253, v223, v224, v225\n\t" \
    "v_max3_f32 v252, v252, v226, v227\n\t" \
    "s_nop 4\n\t" \
    "v_max3_f32 v253, v253, v0, v1\n\t" \
    "v_max_f32 v252, v252, v253\n\t" \
    "v_mov_b32 v253, v252\n\t" \
    "v_mov_b32 v2, v252\n\t" \
    "s_nop 1\n\t" \
    "v_permlane16_swap_b32 v253, v2\n\t" \
    "v_max_f32 v252, v253, v2\n\t" \
    "v_max_f32 v253, v253, v2\n\t" \
    "s_nop 1\n\t" \
    "v_permlane32_swap_b32 v252, v253\n\t" \
    "v_max_f32 v252, v252, v253\n\t" \
    "v_sub_f32 v220, v220, v252\n\t" \
    "v_sub_f32 v221, v221, v252\n\t" \
    "v_sub_f32 v222, v222, v252\n\t" \
    "v_sub_f32 v223, v223, v252\n\t" \
    "v_sub_f32 v224, v224, v252\n\t" \
    "v_sub_f32 v225, v225, v252\n\t" \
    "v_sub_f32 v226, v226, v252\n\t" \
    "v_sub_f32 v227, v227, v252\n\t" \
    "v_sub_f32 v0, v0, v252\n\t" \
    "v_sub_f32 v1, v1, v252\n\t" \
    "v_exp_f32 v220, v220\n\t" \
    "v_exp_f32 v221, v221\n\t" \
    "v_exp_f32 v222, v222\n\t" \
    "v_exp_f32 v223, v223\n\t" \
    "v_exp_f32 v224, v224\n\t" \
    "v_exp_f32 v225, v225\n\t" \
    "v_exp_f32 v226, v226\n\t" \
    "v_exp_f32 v227, v227\n\t" \
    "v_cvt_pk_bf16_f32 v220, v220, v221\n\t" \
    "v_cvt_pk_bf16_f32 v221, v222, v223\n\t" \
    "v_cvt_pk_bf16_f32 v222, v224, v225\n\t" \
    "v_cvt_pk_bf16_f32 v223, v226, v227\n\t" \
    "v_mov_b32 v224, %[one]\n\t" \
    "v_mov_b32 v225, %[one]\n\t" \
    "v_mov_b32 v226, %[one]\n\t" \
    "v_mov_b32 v227, %[one]\n\t" \
    "s_nop 1\n\t" \
    "v_mfma_f32_16x16x32_bf16 v[244:247], v[224:227], v[220:223], 0\n\t" \
    "v_mfma_f32_16x16x32_bf16 v[236:239], v[20:23], v[220:223], 0\n\t" \
    "v_mfma_f32_16x16x32_bf16 v[240:243], v[24:27], v[220:223], 0\n\t" \
    "v_exp_f32 v0, v0\n\t" \
    "v_exp_f32 v1, v1\n\t" \
    "s_nop 0\n\t" \
    "v_cvt_pk_bf16_f32 v228, v0, v1\n\t" \
    "v_cvt_pk_bf16_f32 v231, v0, v1\n\t" \
    "s_nop 1\n\t" \
    "s_branch CM_%=\n\t" \
    "E_%=:\n\t" \
    GSW \
    "s_nop 1\n\t" \
    : [lim] "+s"(lim), [rg] "+s"(rg), [tp] "=&s"(tp), [pt] "=&s"(pt), [pd] "=&s"(pd) DGO DGP GSO(0) GSO(1) GSO(2) GSO(3) GSO(4) \
    : "{v8}"(am_a), "{v9}"(t0_a), "{v10}"(idx), "{v11}"(iw_a), "{v12}"(ab_a), "{v14}"(qr_a), "{v15}"(kr_a), "{v16}"(fr_a), "{v17}"(vo_v), "{v18}"(oo_v), [rs] "s"(rs), [sb] "s"(sb), [tag] "s"(0xffffff81u + s), [hi] "s"(0xff00ff00ff00ff00ull), [tgt] "s"(tgt), [sc] "s"(wid), [onej] "s"(onej), [sl] "s"(0x3e0293ee), [one] "s"(0x3f803f80), [m0] "s"(sbm), [m1] "s"(sbm + 4096u), [vx] "s"(vxo), [fl] "s"(fb + FL_T(s + 1)), [vd] "s"(8192 + j * 2048), [md] "s"(md), "{v[28:35]}"(wr[0][0]), "{v[36:43]}"(wr[0][1]), "{v[44:51]}"(wr[1][0]), "{v[52:59]}"(wr[1][1]), "{v[60:67]}"(wr[2][0]), "{v[68:75]}"(wr[2][1]), "{v[76:83]}"(wr[3][0]), "{v[84:91]}"(wr[3][1]), "{v[92:99]}"(wr[4][0]), "{v[100:107]}"(wr[4][1]), "{v[108:115]}"(wr[5][0]), "{v[116:123]}"(wr[5][1]), "{v[124:131]}"(wr[6][0]), "{v[132:139]}"(wr[6][1]), "{v[140:147]}"(wr[7][0]), "{v[148:155]}"(wr[7][1]), "{v[156:163]}"(wr[8][0]), "{v[164:171]}"(wr[8][1]), "{v[172:179]}"(wr[9][0]), "{v[180:187]}"(wr[9][1]), "{v[188:195]}"(wr[10][0]), "{v[196:203]}"(wr[10][1]), "{v[204:211]}"(wr[11][0]), "{v[212:219]}"(wr[11][1]) \
    : "memory", "vcc", "scc", "s88", "s89", "s90", "s91", "s92", "s93", "v0", "v1", "v2", "v3", "v4", "v5", "v6", "v7", "v20", "v21", "v22", "v23", "v24", "v25", "v26", "v27", "v220", "v221", "v222", "v223", "v224", "v225", "v226", "v227", "v228", "v229", "v230", "v231", "v232", "v233", "v234", "v235", "v236", "v237", "v238", "v239", "v240", "v241", "v242", "v243", "v244", "v245", "v246", "v247", "v248", "v249", "v250", "v251", "v252", "v253", "v254", "v255")

#define A_MAIN_K() \
  asm volatile( \
    "ds_read_b128 v[236:239], v9 offset:0\n\t" \
    "ds_read_b128 v[240:243], v9 offset:1024\n\t" \
    "ds_read_b128 v[244:247], v9 offset:2048\n\t" \
    "ds_read_b128 v[248:251], v9 offset:3072\n\t" \
    "v_mov_b32 v0, 0\n\t" \
    "v_mov_b32 v1, 0\n\t" \
    "v_mov_b32 v2, 0\n\t" \
    "v_mov_b32 v3, 0\n\t" \
    "v_mov_b32 v4, 0\n\t" \
    "v_mov_b32 v5, 0\n\t" \
    "v_mov_b32 v6, 0\n\t" \
    "v_mov_b32 v7, 0\n\t" \
    "s_waitcnt lgkmcnt(0)\n\t" \
    "s_barrier\n\t" \
    "buffer_load_dwordx4 v[220:223], v8, %[rs], %[m0] offen sc1\n\t" \
    "buffer_load_dwordx4 v[224:227], v8, %[rs], %[m1] offen sc1\n\t" \
    GSA(3) \
    "v_mov_b32 v252, v252\n\t" \
    "s_nop 2\n\t" \
    "v_mov_b32 v252, v252\n\t" \
    "s_nop 2\n\t" \
    "v_mov_b32 v252, v252\n\t" \
    "s_nop 2\n\t" \
    "v_mov_b32 v252, v252\n\t" \
    "s_nop 2\n\t" \
    "v_mov_b32 v252, v252\n\t" \
    "s_nop 2\n\t" \
    "v_mov_b32 v252, v252\n\t" \
    "s_nop 2\n\t" \
    "v_mov_b32 v252, v252\n\t" \
    "s_nop 2\n\t" \
    "v_mov_b32 v252, v252\n\t" \
    "s_nop 2\n\t" \
    "v_mov_b32 v252, v252\n\t" \
    "s_nop 2\n\t" \
    "v_mov_b32 v252, v252\n\t" \
    "s_nop 2\n\t" \
    "v_mov_b32 v252, v252\n\t" \
    "s_nop 2\n\t" \
    "v_mov_b32 v252, v252\n\t" \
    "s_nop 2\n\t" \
    "v_mov_b32 v252, v252\n\t" \
    "s_nop 2\n\t" \
    "v_mov_b32 v252, v252\n\t" \
    "s_nop 2\n\t" \
    "v_mov_b32 v252, v252\n\t" \
    "s_nop 2\n\t" \
    "v_mov_b32 v252, v252\n\t" \
    "s_nop 2\n\t" \
    "v_mov_b32 v252, v252\n\t" \
    "s_nop 2\n\t" \
    "v_mov_b32 v252, v252\n\t" \
    "s_nop 2\n\t" \
    "v_mov_b32 v252, v252\n\t" \
    "s_nop 2\n\t" \
    "v_mov_b32 v252, v252\n\t" \
    "s_nop 2\n\t" \
    "v_mov_b32 v252, v252\n\t" \
    "s_nop 2\n\t" \
    "v_mov_b32 v252, v252\n\t" \
    "s_nop 2\n\t" \
    "v_mov_b32 v252, v252\n\t" \
    "s_nop 2\n\t" \
    "v_mov_b32 v252, v252\n\t" \
    "s_nop 2\n\t" \
    "v_mov_b32 v252, v252\n\t" \
    "s_nop 2\n\t" \
    "v_mov_b32 v252, v252\n\t" \
    "s_nop 2\n\t" \
    "v_mov_b32 v252, v252\n\t" \
    "s_nop 2\n\t" \
    "v_mov_b32 v252, v252\n\t" \
    "s_nop 2\n\t" \
    "v_mov_b32 v252, v252\n\t" \
    "s_nop 2\n\t" \
    "v_mov_b32 v252, v252\n\t" \
    "s_nop 2\n\t" \
    "PM_%=:\n\t" \
    "s_waitcnt vmcnt(1)\n\t" \
    "ds_write_b128 v8, v[220:223] offset:8192\n\t" \
    "v_max3_u32 v252, v220, v221, v222\n\t" \
    "s_waitcnt vmcnt(0)\n\t" \
    "ds_write_b128 v8, v[224:227] offset:12288\n\t" \
    "v_max3_u32 v252, v252, v223, v224\n\t" \
    "v_max3_u32 v252, v252, v225, v226\n\t" \
    "v_max_u32 v252, v252, v227\n\t" \
    "v_cmp_eq_u32 vcc, -1, v252\n\t" \
    "s_cbranch_vccz PD_%=\n\t" \
    "s_sub_u32 %[lim], %[lim], 1\n\t" \
    "s_cmp_gt_i32 %[lim], 0\n\t" \
    "s_cbranch_scc0 PD_%=\n\t" \
    "s_sleep " STR(CADM) "\n\t" \
    "buffer_load_dwordx4 v[220:223], v8, %[rs], %[m0] offen sc1\n\t" \
    "buffer_load_dwordx4 v[224:227], v8, %[rs], %[m1] offen sc1\n\t" \
    "s_branch PM_%=\n\t" \
    "PD_%=:\n\t" \
    GSA(0) \
    "v_smfmac_f32_16x16x64_bf16 v[0:3], v[220:223], v[236:243], v10\n\t" \
    "v_xor_b32 v9, 16384, v9\n\t" \
    "ds_read_b128 v[236:239], v9 offset:0\n\t" \
    "ds_read_b128 v[240:243], v9 offset:1024\n\t" \
    "v_smfmac_f32_16x16x64_bf16 v[4:7], v[220:223], v[244:251], v10\n\t" \
    "ds_read_b128 v[244:247], v9 offset:2048\n\t" \
    "ds_read_b128 v[248:251], v9 offset:3072\n\t" \
    "s_waitcnt lgkmcnt(4)\n\t" \
    "s_barrier\n\t" \
    DGA \
    "s_cmp_lg_u32 %[st], 63\n\t" \
    "s_cbranch_scc1 NL_%=\n\t" \
    "s_mov_b64 exec, %[fm]\n\t" \
    "v_mov_b32 v252, %[st]\n\t" \
    "v_mov_b32 v253, %[tag]\n\t" \
    "v_mov_b32 v254, 0\n\t" \
    "buffer_store_dwordx2 v[252:253], v254, %[rs], %[po] offen sc1\n\t" \
    "s_mov_b64 exec, -1\n\t" \
    "NL_%=:\n\t" \
    "ds_read_b128 v[228:231], v8 offset:0\n\t" \
    "ds_read_b128 v[232:235], v8 offset:4096\n\t" \
    "v_smfmac_f32_16x16x64_bf16 v[0:3], v[224:227], v[148:155], v10\n\t" \
    "v_xor_b32 v8, 1024, v8\n\t" \
    "ds_read_b128 v[220:223], v8 offset:8192\n\t" \
    "v_smfmac_f32_16x16x64_bf16 v[4:7], v[224:227], v[156:163], v10\n\t" \
    "ds_read_b128 v[224:227], v8 offset:12288\n\t" \
    "buffer_store_dwordx4 v[212:215], v14, %[rs], %[ox] offen offset:0 sc1\n\t" \
    "buffer_store_dwordx4 v[216:219], v14, %[rs], %[ox] offen offset:512 sc1\n\t" \
    "s_waitcnt lgkmcnt(3)\n\t" \
    "v_smfmac_f32_16x16x64_bf16 v[0:3], v[228:231], v[20:27], v10\n\t" \
    "v_smfmac_f32_16x16x64_bf16 v[4:7], v[228:231], v[28:35], v10\n\t" \
    "ds_read_b128 v[228:231], v8 offset:0\n\t" \
    "s_waitcnt lgkmcnt(3)\n\t" \
    "v_smfmac_f32_16x16x64_bf16 v[0:3], v[232:235], v[84:91], v10\n\t" \
    "v_smfmac_f32_16x16x64_bf16 v[4:7], v[232:235], v[92:99], v10\n\t" \
    "ds_read_b128 v[232:235], v8 offset:4096\n\t" \
    "s_waitcnt lgkmcnt(3)\n\t" \
    "v_smfmac_f32_16x16x64_bf16 v[0:3], v[220:223], v[236:243], v10\n\t" \
    "v_xor_b32 v9, 49152, v9\n\t" \
    "ds_read_b128 v[236:239], v9 offset:0\n\t" \
    "v_smfmac_f32_16x16x64_bf16 v[4:7], v[220:223], v[244:251], v10\n\t" \
    "ds_read_b128 v[240:243], v9 offset:1024\n\t" \
    "s_waitcnt lgkmcnt(4)\n\t" \
    "v_smfmac_f32_16x16x64_bf16 v[0:3], v[224:227], v[164:171], v10\n\t" \
    "v_xor_b32 v8, 3072, v8\n\t" \
    "ds_read_b128 v[220:223], v8 offset:12288\n\t" \
    "v_smfmac_f32_16x16x64_bf16 v[4:7], v[224:227], v[172:179], v10\n\t" \
    "ds_read_b128 v[244:247], v9 offset:2048\n\t" \
    "s_waitcnt lgkmcnt(5)\n\t" \
    "v_smfmac_f32_16x16x64_bf16 v[0:3], v[228:231], v[36:43], v10\n\t" \
    "ds_read_b128 v[224:227], v8 offset:0\n\t" \
    "v_smfmac_f32_16x16x64_bf16 v[4:7], v[228:231], v[44:51], v10\n\t" \
    "ds_read_b128 v[248:251], v9 offset:3072\n\t" \
    "s_waitcnt lgkmcnt(6)\n\t" \
    "v_smfmac_f32_16x16x64_bf16 v[0:3], v[232:235], v[100:107], v10\n\t" \
    "ds_read_b128 v[228:231], v8 offset:8192\n\t" \
    "v_smfmac_f32_16x16x64_bf16 v[4:7], v[232:235], v[108:115], v10\n\t" \
    "ds_read_b128 v[232:235], v8 offset:4096\n\t" \
    "s_waitcnt lgkmcnt(5)\n\t" \
    "v_smfmac_f32_16x16x64_bf16 v[0:3], v[220:223], v[180:187], v10\n\t" \
    "v_smfmac_f32_16x16x64_bf16 v[4:7], v[220:223], v[188:195], v10\n\t" \
    "v_xor_b32 v8, 1024, v8\n\t" \
    "ds_read_b128 v[220:223], v8 offset:12288\n\t" \
    "s_waitcnt lgkmcnt(4)\n\t" \
    "v_smfmac_f32_16x16x64_bf16 v[0:3], v[224:227], v[52:59], v10\n\t" \
    "v_smfmac_f32_16x16x64_bf16 v[4:7], v[224:227], v[60:67], v10\n\t" \
    "ds_read_b128 v[224:227], v8 offset:0\n\t" \
    "s_waitcnt lgkmcnt(3)\n\t" \
    "v_smfmac_f32_16x16x64_bf16 v[0:3], v[228:231], v[236:243], v10\n\t" \
    "v_xor_b32 v9, 16384, v9\n\t" \
    "ds_read_b128 v[236:239], v9 offset:0\n\t" \
    "v_smfmac_f32_16x16x64_bf16 v[4:7], v[228:231], v[244:251], v10\n\t" \
    "ds_read_b128 v[240:243], v9 offset:1024\n\t" \
    "s_waitcnt lgkmcnt(4)\n\t" \
    "v_smfmac_f32_16x16x64_bf16 v[0:3], v[232:235], v[116:123], v10\n\t" \
    "ds_read_b128 v[228:231], v8 offset:4096\n\t" \
    "v_smfmac_f32_16x16x64_bf16 v[4:7], v[232:235], v[124:131], v10\n\t" \
    "ds_read_b128 v[244:247], v9 offset:2048\n\t" \
    "s_waitcnt lgkmcnt(5)\n\t" \
    "v_smfmac_f32_16x16x64_bf16 v[0:3], v[220:223], v[196:203], v10\n\t" \
    "ds_read_b128 v[232:235], v8 offset:8192\n\t" \
    "v_xor_b32 v8, 3072, v8\n\t" \
    "v_smfmac_f32_16x16x64_bf16 v[4:7], v[220:223], v[204:211], v10\n\t" \
    "ds_read_b128 v[248:251], v9 offset:3072\n\t" \
    "s_waitcnt lgkmcnt(6)\n\t" \
    "v_smfmac_f32_16x16x64_bf16 v[0:3], v[224:227], v[68:75], v10\n\t" \
    "v_smfmac_f32_16x16x64_bf16 v[4:7], v[224:227], v[76:83], v10\n\t" \
    "s_waitcnt lgkmcnt(3)\n\t" \
    "v_smfmac_f32_16x16x64_bf16 v[0:3], v[228:231], v[132:139], v10\n\t" \
    "v_smfmac_f32_16x16x64_bf16 v[4:7], v[228:231], v[140:147], v10\n\t" \
    "s_waitcnt lgkmcnt(0)\n\t" \
    "v_smfmac_f32_16x16x64_bf16 v[0:3], v[232:235], v[236:243], v10\n\t" \
    "v_xor_b32 v9, 49152, v9\n\t" \
    "v_smfmac_f32_16x16x64_bf16 v[4:7], v[232:235], v[244:251], v10\n\t" \
    "v_mov_b32 v253, %[stag]\n\t" \
    "s_nop 4\n\t" \
    "v_add_f32 v0, v0, v1\n\t" \
    "v_add_f32 v2, v2, v3\n\t" \
    "v_add_f32 v4, v4, v5\n\t" \
    "v_add_f32 v6, v6, v7\n\t" \
    "v_cvt_pk_bf16_f32 v0, v0, v4\n\t" \
    "v_cvt_pk_bf16_f32 v2, v2, v6\n\t" \
    "ds_write2_b32 v11, v0, v2 offset0:0 offset1:4\n\t" \
    "s_mov_b64 exec, %[onej]\n\t" \
    "ds_write_b32 v18, v253 offset:16\n\t" \
    "s_mov_b64 exec, -1\n\t" \
    GSA(1) \
    "s_setprio 0\n\t" \
    "buffer_load_dwordx4 v[236:239], v12, %[rs], %[qx] offen offset:0 sc1\n\t" \
    "buffer_load_dwordx4 v[240:243], v12, %[rs], %[qx] offen offset:1024 sc1\n\t" \
    "buffer_load_dwordx4 v[244:247], v12, %[rs], %[qx] offen offset:2048 sc1\n\t" \
    "buffer_load_dwordx4 v[248:251], v12, %[rs], %[qx] offen offset:3072 sc1\n\t" \
    "v_cmp_le_i32 s[72:73], 0, v16\n\t" \
    "v_cmp_le_i32 s[74:75], 1, v16\n\t" \
    "v_cmp_le_i32 s[76:77], 2, v16\n\t" \
    "v_cmp_le_i32 s[78:79], 3, v16\n\t" \
    "v_cmp_le_i32 s[80:81], 16, v16\n\t" \
    "v_cmp_le_i32 s[82:83], 17, v16\n\t" \
    "v_cmp_le_i32 s[84:85], 18, v16\n\t" \
    "v_cmp_le_i32 s[86:87], 19, v16\n\t" \
    "v_mov_b32 v254, v254\n\t" \
    "s_nop 2\n\t" \
    "v_mov_b32 v254, v254\n\t" \
    "s_nop 2\n\t" \
    "v_mov_b32 v254, v254\n\t" \
    "s_nop 2\n\t" \
    "v_mov_b32 v254, v254\n\t" \
    "s_nop 2\n\t" \
    "v_mov_b32 v254, v254\n\t" \
    "s_nop 2\n\t" \
    "v_mov_b32 v254, v254\n\t" \
    "s_nop 2\n\t" \
    "v_mov_b32 v254, v254\n\t" \
    "s_nop 2\n\t" \
    "v_mov_b32 v254, v254\n\t" \
    "s_nop 2\n\t" \
    "v_mov_b32 v254, v254\n\t" \
    "s_nop 2\n\t" \
    "v_mov_b32 v254, v254\n\t" \
    "s_nop 2\n\t" \
    "v_mov_b32 v254, v254\n\t" \
    "s_nop 2\n\t" \
    "v_mov_b32 v254, v254\n\t" \
    "s_nop 2\n\t" \
    "v_mov_b32 v254, v254\n\t" \
    "s_nop 2\n\t" \
    "v_mov_b32 v254, v254\n\t" \
    "s_nop 2\n\t" \
    "v_mov_b32 v254, v254\n\t" \
    "s_nop 2\n\t" \
    "v_mov_b32 v254, v254\n\t" \
    "s_nop 2\n\t" \
    "v_mov_b32 v254, v254\n\t" \
    "s_nop 2\n\t" \
    "v_mov_b32 v254, v254\n\t" \
    "s_nop 2\n\t" \
    "v_mov_b32 v254, v254\n\t" \
    "s_nop 2\n\t" \
    "v_mov_b32 v254, v254\n\t" \
    "s_nop 2\n\t" \
    "LW_%=:\n\t" \
    "ds_read_b32 v0, v18 offset:16\n\t" \
    "s_sub_u32 %[lim], %[lim], " STR(DEC1) "\n\t" \
    "s_cmp_gt_i32 %[lim], 0\n\t" \
    "v_mov_b32 v254, v254\n\t" \
    "s_nop 2\n\t" \
    "v_mov_b32 v254, v254\n\t" \
    "s_nop 2\n\t" \
    "v_mov_b32 v254, v254\n\t" \
    "s_nop 2\n\t" \
    "v_mov_b32 v254, v254\n\t" \
    "s_nop 2\n\t" \
    "v_mov_b32 v254, v254\n\t" \
    "s_nop 2\n\t" \
    "s_waitcnt lgkmcnt(0)\n\t" \
    "s_cbranch_scc0 CW_%=\n\t" \
    "v_cmp_ne_u32 vcc, v0, v253\n\t" \
    "s_cbranch_vccnz LW_%=\n\t" \
    "CW_%=:\n\t" \
    "ds_read_b128 v[212:215], v12 offset:24576\n\t" \
    "ds_read_b128 v[216:219], v12 offset:28672\n\t" \
    "ds_read_b128 v[220:223], v15 offset:0\n\t" \
    "ds_read_b128 v[224:227], v12 offset:25600\n\t" \
    "ds_read_b128 v[228:231], v12 offset:29696\n\t" \
    "v_add_u32 v253, %[se], v12\n\t" \
    "v_mov_b32 v254, v254\n\t" \
    "s_nop 2\n\t" \
    "v_mov_b32 v254, v254\n\t" \
    "s_nop 2\n\t" \
    "v_mov_b32 v254, v254\n\t" \
    "s_nop 2\n\t" \
    "v_mov_b32 v254, v254\n\t" \
    "s_nop 2\n\t" \
    "s_waitcnt vmcnt(0)\n\t" \
    "s_waitcnt lgkmcnt(4)\n\t" \
    "v_mfma_f32_16x16x32_bf16 v[0:3], v[212:215], v[236:239], 0\n\t" \
    "ds_read_b128 v[212:215], v15 offset:512\n\t" \
    "s_waitcnt lgkmcnt(4)\n\t" \
    "v_mfma_f32_16x16x32_bf16 v[4:7], v[216:219], v[236:239], 0\n\t" \
    "ds_read_b128 v[216:219], v12 offset:26624\n\t" \
    "s_waitcnt lgkmcnt(4)\n\t" \
    "v_mfma_f32_16x16x32_bf16 v[232:235], v[220:223], v[236:239], 0\n\t" \
    "ds_read_b128 v[220:223], v12 offset:30720\n\t" \
    "s_waitcnt lgkmcnt(4)\n\t" \
    "v_mfma_f32_16x16x32_bf16 v[0:3], v[224:227], v[240:243], v[0:3]\n\t" \
    "ds_read_b128 v[224:227], v15 offset:1024\n\t" \
    "s_waitcnt lgkmcnt(4)\n\t" \
    "v_mfma_f32_16x16x32_bf16 v[4:7], v[228:231], v[240:243], v[4:7]\n\t" \
    "ds_read_b128 v[228:231], v12 offset:27648\n\t" \
    "s_waitcnt lgkmcnt(4)\n\t" \
    "v_mfma_f32_16x16x32_bf16 v[232:235], v[212:215], v[240:243], v[232:235]\n\t" \
    "ds_read_b128 v[212:215], v12 offset:31744\n\t" \
    "s_waitcnt lgkmcnt(4)\n\t" \
    "v_mfma_f32_16x16x32_bf16 v[0:3], v[216:219], v[244:247], v[0:3]\n\t" \
    "ds_read_b128 v[216:219], v15 offset:1536\n\t" \
    "s_waitcnt lgkmcnt(4)\n\t" \
    "v_mfma_f32_16x16x32_bf16 v[4:7], v[220:223], v[244:247], v[4:7]\n\t" \
    "s_waitcnt lgkmcnt(3)\n\t" \
    "v_mfma_f32_16x16x32_bf16 v[232:235], v[224:227], v[244:247], v[232:235]\n\t" \
    "s_waitcnt lgkmcnt(2)\n\t" \
    "v_mfma_f32_16x16x32_bf16 v[0:3], v[228:231], v[248:251], v[0:3]\n\t" \
    "s_waitcnt lgkmcnt(1)\n\t" \
    "v_mfma_f32_16x16x32_bf16 v[4:7], v[212:215], v[248:251], v[4:7]\n\t" \
    "s_waitcnt lgkmcnt(0)\n\t" \
    "v_mfma_f32_16x16x32_bf16 v[232:235], v[216:219], v[248:251], v[232:235]\n\t" \
    "s_barrier\n\t" \
    GSA(4) \
    "ds_read_b128 v[236:239], v253 offset:8192\n\t" \
    "ds_read_b128 v[240:243], v253 offset:9216\n\t" \
    "ds_read_b128 v[244:247], v253 offset:10240\n\t" \
    "ds_read_b128 v[248:251], v253 offset:11264\n\t" \
    "ds_read2st64_b32 v[228:229], v17 offset0:0 offset1:1\n\t" \
    "ds_read2st64_b32 v[230:231], v17 offset0:2 offset1:3\n\t" \
    "s_mov_b64 exec, %[fm]\n\t" \
    "v_mov_b32 v220, %[st]\n\t" \
    "v_mov_b32 v221, %[tag]\n\t" \
    "v_mov_b32 v222, 0\n\t" \
    "buffer_store_dwordx2 v[220:221], v222, %[rs], %[po] offen sc1\n\t" \
    "s_mov_b64 exec, -1\n\t" \
    "v_mov_b32 v254, 0xff800000\n\t" \
    "v_cvt_pk_bf16_f32 v0, 0, v0\n\t" \
    "v_cvt_pk_bf16_f32 v1, 0, v1\n\t" \
    "v_cvt_pk_bf16_f32 v2, 0, v2\n\t" \
    "v_cvt_pk_bf16_f32 v3, 0, v3\n\t" \
    "v_cvt_pk_bf16_f32 v4, 0, v4\n\t" \
    "v_cvt_pk_bf16_f32 v5, 0, v5\n\t" \
    "v_cvt_pk_bf16_f32 v6, 0, v6\n\t" \
    "v_cvt_pk_bf16_f32 v7, 0, v7\n\t" \
    "v_cvt_pk_bf16_f32 v232, 0, v232\n\t" \
    "v_cvt_pk_bf16_f32 v233, 0, v233\n\t" \
    "v_mul_f32 v0, 0x3db504f3, v0\n\t" \
    "v_mul_f32 v1, 0x3db504f3, v1\n\t" \
    "v_mul_f32 v2, 0x3db504f3, v2\n\t" \
    "v_mul_f32 v3, 0x3db504f3, v3\n\t" \
    "v_mul_f32 v4, 0x3db504f3, v4\n\t" \
    "v_mul_f32 v5, 0x3db504f3, v5\n\t" \
    "v_mul_f32 v6, 0x3db504f3, v6\n\t" \
    "v_mul_f32 v7, 0x3db504f3, v7\n\t" \
    "v_mul_f32 v232, 0x3db504f3, v232\n\t" \
    "v_mul_f32 v233, 0x3db504f3, v233\n\t" \
    "v_cvt_pk_bf16_f32 v0, 0, v0\n\t" \
    "v_cvt_pk_bf16_f32 v1, 0, v1\n\t" \
    "v_cvt_pk_bf16_f32 v2, 0, v2\n\t" \
    "v_cvt_pk_bf16_f32 v3, 0, v3\n\t" \
    "v_cvt_pk_bf16_f32 v4, 0, v4\n\t" \
    "v_cvt_pk_bf16_f32 v5, 0, v5\n\t" \
    "v_cvt_pk_bf16_f32 v6, 0, v6\n\t" \
    "v_cvt_pk_bf16_f32 v7, 0, v7\n\t" \
    "v_cvt_pk_bf16_f32 v232, 0, v232\n\t" \
    "v_cvt_pk_bf16_f32 v233, 0, v233\n\t" \
    "v_cndmask_b32 v0, v254, v0, s[72:73]\n\t" \
    "v_cndmask_b32 v1, v254, v1, s[74:75]\n\t" \
    "v_cndmask_b32 v2, v254, v2, s[76:77]\n\t" \
    "v_cndmask_b32 v3, v254, v3, s[78:79]\n\t" \
    "v_cndmask_b32 v4, v254, v4, s[80:81]\n\t" \
    "v_cndmask_b32 v5, v254, v5, s[82:83]\n\t" \
    "v_cndmask_b32 v6, v254, v6, s[84:85]\n\t" \
    "v_cndmask_b32 v7, v254, v7, s[86:87]\n\t" \
    "v_max3_f32 v252, v0, v1, v2\n\t" \
    "v_max3_f32 v255, v3, v4, v5\n\t" \
    "v_max3_f32 v254, v6, v7, v232\n\t" \
    "v_max3_f32 v252, v252, v255, v254\n\t" \
    "v_max_f32 v252, v252, v233\n\t" \
    "v_mov_b32 v255, v252\n\t" \
    "v_mov_b32 v254, v252\n\t" \
    "s_nop 1\n\t" \
    "v_permlane16_swap_b32 v255, v254\n\t" \
    "v_max_f32 v252, v255, v254\n\t" \
    "v_max_f32 v255, v255, v254\n\t" \
    "s_nop 1\n\t" \
    "v_permlane32_swap_b32 v252, v255\n\t" \
    "v_max_f32 v252, v252, v255\n\t" \
    "v_mul_f32 v252, 0xbfb8aa3b, v252\n\t" \
    "v_fma_f32 v0, v0, %[l2e], v252\n\t" \
    "v_fma_f32 v1, v1, %[l2e], v252\n\t" \
    "v_fma_f32 v2, v2, %[l2e], v252\n\t" \
    "v_fma_f32 v3, v3, %[l2e], v252\n\t" \
    "v_fma_f32 v4, v4, %[l2e], v252\n\t" \
    "v_fma_f32 v5, v5, %[l2e], v252\n\t" \
    "v_fma_f32 v6, v6, %[l2e], v252\n\t" \
    "v_fma_f32 v7, v7, %[l2e], v252\n\t" \
    "v_fma_f32 v232, v232, %[l2e], v252\n\t" \
    "v_fma_f32 v233, v233, %[l2e], v252\n\t" \
    "v_exp_f32 v0, v0\n\t" \
    "v_exp_f32 v1, v1\n\t" \
    "v_exp_f32 v2, v2\n\t" \
    "v_exp_f32 v3, v3\n\t" \
    "v_exp_f32 v4, v4\n\t" \
    "v_exp_f32 v5, v5\n\t" \
    "v_exp_f32 v6, v6\n\t" \
    "v_exp_f32 v7, v7\n\t" \
    "v_exp_f32 v232, v232\n\t" \
    "v_exp_f32 v233, v233\n\t" \
    "v_add_f32 v252, v0, v1\n\t" \
    "v_add_f32 v235, v2, v3\n\t" \
    "v_add_f32 v254, v4, v5\n\t" \
    "v_add_f32 v255, v6, v7\n\t" \
    "v_add_f32 v234, v232, v233\n\t" \
    "v_add_f32 v252, v252, v235\n\t" \
    "v_add_f32 v254, v254, v255\n\t" \
    "v_add_f32 v252, v252, v254\n\t" \
    "v_add_f32 v252, v252, v234\n\t" \
    "v_mov_b32 v255, v252\n\t" \
    "v_mov_b32 v254, v252\n\t" \
    "s_nop 1\n\t" \
    "v_permlane16_swap_b32 v255, v254\n\t" \
    "v_add_f32 v252, v255, v254\n\t" \
    "v_add_f32 v255, v255, v254\n\t" \
    "s_nop 1\n\t" \
    "v_permlane32_swap_b32 v252, v255\n\t" \
    "v_add_f32 v252, v252, v255\n\t" \
    "v_rcp_f32 v252, v252\n\t" \
    "s_nop 0\n\t" \
    "v_mul_f32 v0, v0, v252\n\t" \
    "v_mul_f32 v1, v1, v252\n\t" \
    "v_mul_f32 v2, v2, v252\n\t" \
    "v_mul_f32 v3, v3, v252\n\t" \
    "v_mul_f32 v4, v4, v252\n\t" \
    "v_mul_f32 v5, v5, v252\n\t" \
    "v_mul_f32 v6, v6, v252\n\t" \
    "v_mul_f32 v7, v7, v252\n\t" \
    "v_mul_f32 v232, v232, v252\n\t" \
    "v_mul_f32 v233, v233, v252\n\t" \
    "v_cvt_pk_bf16_f32 v0, v0, v1\n\t" \
    "v_cvt_pk_bf16_f32 v1, v2, v3\n\t" \
    "v_cvt_pk_bf16_f32 v2, v4, v5\n\t" \
    "v_cvt_pk_bf16_f32 v3, v6, v7\n\t" \
    "v_cvt_pk_bf16_f32 v4, v232, v233\n\t" \
    "v_mov_b32 v5, 0\n\t" \
    "v_mov_b32 v6, 0\n\t" \
    "v_cvt_pk_bf16_f32 v7, v232, v233\n\t" \
    "s_waitcnt lgkmcnt(0)\n\t" \
    "v_mfma_f32_16x16x32_bf16 v[236:239], v[236:239], v[0:3], 0\n\t" \
    "v_mfma_f32_16x16x32_bf16 v[240:243], v[240:243], v[0:3], 0\n\t" \
    "v_mfma_f32_16x16x32_bf16 v[244:247], v[244:247], v[0:3], 0\n\t" \
    "v_mfma_f32_16x16x32_bf16 v[248:251], v[248:251], v[0:3], 0\n\t" \
    "v_mfma_f32_16x16x16_bf16 v[236:239], v[228:229], v[4:5], v[236:239]\n\t" \
    "v_mfma_f32_16x16x16_bf16 v[240:243], v[228:229], v[6:7], v[240:243]\n\t" \
    "v_mfma_f32_16x16x16_bf16 v[244:247], v[230:231], v[4:5], v[244:247]\n\t" \
    "v_mfma_f32_16x16x16_bf16 v[248:251], v[230:231], v[6:7], v[248:251]\n\t" \
    "s_nop 4\n\t" \
    "v_cvt_pk_bf16_f32 v212, v236, v237\n\t" \
    "v_cvt_pk_bf16_f32 v213, v240, v241\n\t" \
    "v_cvt_pk_bf16_f32 v214, v238, v239\n\t" \
    "v_cvt_pk_bf16_f32 v215, v242, v243\n\t" \
    "v_permlane16_swap_b32 v212, v213\n\t" \
    "s_nop 0\n\t" \
    "v_permlane16_swap_b32 v214, v215\n\t" \
    "v_cvt_pk_bf16_f32 v216, v244, v245\n\t" \
    "v_cvt_pk_bf16_f32 v217, v248, v249\n\t" \
    "v_cvt_pk_bf16_f32 v218, v246, v247\n\t" \
    "v_cvt_pk_bf16_f32 v219, v250, v251\n\t" \
    "v_permlane16_swap_b32 v216, v217\n\t" \
    "s_nop 0\n\t" \
    "v_permlane16_swap_b32 v218, v219\n\t" \
    "s_setprio 1\n\t" \
    GSA(2) \
    GSW \
    "s_nop 1\n\t" \
    : [lim] "+s"(lim), "+{v[212:219]}"(oxr) DGO GSO(0) GSO(1) GSO(2) GSO(3) GSO(4) \
    : "{v8}"(am_a), "{v9}"(t0_a), "{v10}"(idx), "{v11}"(iw_a), "{v12}"(ab_a), "{v14}"(xo_v), "{v15}"(kr_a), "{v16}"(dq), "{v17}"(vl_a), "{v18}"(fr_a), [stag] "s"(0xffffff81u + s), [onej] "s"(onej), [rs] "s"(rs), [m0] "s"(sbm + 16384u), [m1] "s"(sbm + 20480u), [st] "s"(s), [tag] "s"(P.tag), [po] "s"(FLG_OFF + F_PRG(4, h) * 128), [fm] "s"(fmask), [qx] "s"(qxo), [qd] "s"(lbase + LA_VM), [qh] "s"((j & 1) * 2048), [ox] "s"(oxo), [se] "s"((j & 1) * 4096), [l2e] "s"(0x3fb8aa3b), "{v[20:27]}"(wr[0][0]), "{v[28:35]}"(wr[0][1]), "{v[36:43]}"(wr[1][0]), "{v[44:51]}"(wr[1][1]), "{v[52:59]}"(wr[2][0]), "{v[60:67]}"(wr[2][1]), "{v[68:75]}"(wr[3][0]), "{v[76:83]}"(wr[3][1]), "{v[84:91]}"(wr[4][0]), "{v[92:99]}"(wr[4][1]), "{v[100:107]}"(wr[5][0]), "{v[108:115]}"(wr[5][1]), "{v[116:123]}"(wr[6][0]), "{v[124:131]}"(wr[6][1]), "{v[132:139]}"(wr[7][0]), "{v[140:147]}"(wr[7][1]), "{v[148:155]}"(wr[8][0]), "{v[156:163]}"(wr[8][1]), "{v[164:171]}"(wr[9][0]), "{v[172:179]}"(wr[9][1]), "{v[180:187]}"(wr[10][0]), "{v[188:195]}"(wr[10][1]), "{v[196:203]}"(wr[11][0]), "{v[204:211]}"(wr[11][1]) \
    : "memory", "scc", "vcc", "s90", "s91", "s72", "s73", "s74", "s75", "s76", "s77", "s78", "s79", "s80", "s81", "s82", "s83", "s84", "s85", "s86", "s87", "v0", "v1", "v2", "v3", "v4", "v5", "v6", "v7", "v220", "v221", "v222", "v223", "v224", "v225", "v226", "v227", "v228", "v229", "v230", "v231", "v232", "v233", "v234", "v235", "v236", "v237", "v238", "v239", "v240", "v241", "v242", "v243", "v244", "v245", "v246", "v247", "v248", "v249", "v250", "v251", "v252", "v253", "v254", "v255")

#define B_POLL() \
  asm volatile( \
    "TWB_%=:\n\t" \
    "s_memrealtime s[88:89]\n\t" \
    "s_memtime s[90:91]\n\t" \
    "s_waitcnt lgkmcnt(0)\n\t" \
    "s_lshl_b32 s88, s88, 2\n\t" \
    "s_sub_u32 s89, %[tgt], s88\n\t" \
    "s_cmp_gt_i32 s89, 16\n\t" \
    "s_cbranch_scc1 TWB_%=\n\t" \
    "s_mul_i32 s89, s89, 11\n\t" \
    "s_ashr_i32 s89, s89, 1\n\t" \
    "s_add_u32 s89, s89, s90\n\t" \
    "TCB_%=:\n\t" \
    "s_memtime s[92:93]\n\t" \
    "s_waitcnt lgkmcnt(0)\n\t" \
    "s_sub_u32 s93, s89, s92\n\t" \
    "s_cmp_gt_i32 s93, 0\n\t" \
    "s_cbranch_scc1 TCB_%=\n\t" \
    "buffer_load_dwordx4 v[220:223], %[l16], %[rs], %[m0] offen offset:0 sc1\n\t" \
    "buffer_load_dwordx4 v[224:227], %[l16], %[rs], %[m0] offen offset:1024 sc1\n\t" \
    "buffer_load_dwordx4 v[228:231], %[l16], %[rs], %[m1] offen offset:0 sc1\n\t" \
    "buffer_load_dwordx4 v[232:235], %[l16], %[rs], %[m1] offen offset:1024 sc1\n\t" \
    "v_mov_b32 v252, v252\n\t" \
    "s_nop 2\n\t" \
    "v_mov_b32 v252, v252\n\t" \
    "s_nop 2\n\t" \
    "v_mov_b32 v252, v252\n\t" \
    "s_nop 2\n\t" \
    "v_mov_b32 v252, v252\n\t" \
    "s_nop 2\n\t" \
    "v_mov_b32 v252, v252\n\t" \
    "s_nop 2\n\t" \
    "v_mov_b32 v252, v252\n\t" \
    "s_nop 2\n\t" \
    "v_mov_b32 v252, v252\n\t" \
    "s_nop 2\n\t" \
    "v_mov_b32 v252, v252\n\t" \
    "s_nop 2\n\t" \
    "v_mov_b32 v252, v252\n\t" \
    "s_nop 2\n\t" \
    "v_mov_b32 v252, v252\n\t" \
    "s_nop 2\n\t" \
    "v_mov_b32 v252, v252\n\t" \
    "s_nop 2\n\t" \
    "v_mov_b32 v252, v252\n\t" \
    "s_nop 2\n\t" \
    "v_mov_b32 v252, v252\n\t" \
    "s_nop 2\n\t" \
    "v_mov_b32 v252, v252\n\t" \
    "s_nop 2\n\t" \
    "v_mov_b32 v252, v252\n\t" \
    "s_nop 2\n\t" \
    "v_mov_b32 v252, v252\n\t" \
    "s_nop 2\n\t" \
    "v_mov_b32 v252, v252\n\t" \
    "s_nop 2\n\t" \
    "v_mov_b32 v252, v252\n\t" \
    "s_nop 2\n\t" \
    "v_mov_b32 v252, v252\n\t" \
    "s_nop 2\n\t" \
    "v_mov_b32 v252, v252\n\t" \
    "s_nop 2\n\t" \
    "v_mov_b32 v252, v252\n\t" \
    "s_nop 2\n\t" \
    "v_mov_b32 v252, v252\n\t" \
    "s_nop 2\n\t" \
    "v_mov_b32 v252, v252\n\t" \
    "s_nop 2\n\t" \
    "v_mov_b32 v252, v252\n\t" \
    "s_nop 2\n\t" \
    "PM_%=:\n\t" \
    "s_waitcnt vmcnt(3)\n\t" \
    "ds_write_b128 %[la], v[220:223] offset:0\n\t" \
    "v_max3_u32 v252, v220, v220, v221\n\t" \
    "v_max3_u32 v252, v252, v222, v223\n\t" \
    "s_waitcnt vmcnt(2)\n\t" \
    "ds_write_b128 %[la], v[224:227] offset:1024\n\t" \
    "v_max3_u32 v252, v252, v224, v225\n\t" \
    "v_max3_u32 v252, v252, v226, v227\n\t" \
    "s_waitcnt vmcnt(1)\n\t" \
    "ds_write_b128 %[la], v[228:231] offset:8192\n\t" \
    "v_max3_u32 v252, v252, v228, v229\n\t" \
    "v_max3_u32 v252, v252, v230, v231\n\t" \
    "s_waitcnt vmcnt(0)\n\t" \
    "ds_write_b128 %[la], v[232:235] offset:9216\n\t" \
    "v_max3_u32 v252, v252, v232, v233\n\t" \
    "v_max3_u32 v252, v252, v234, v235\n\t" \
    "v_cmp_eq_u32 vcc, -1, v252\n\t" \
    "s_cbranch_vccz PD_%=\n\t" \
    "s_sub_u32 %[lim], %[lim], 1\n\t" \
    "s_cmp_gt_i32 %[lim], 0\n\t" \
    "s_cbranch_scc0 PD_%=\n\t" \
    "s_sleep " STR(CADM) "\n\t" \
    "buffer_load_dwordx4 v[220:223], %[l16], %[rs], %[m0] offen offset:0 sc1\n\t" \
    "buffer_load_dwordx4 v[224:227], %[l16], %[rs], %[m0] offen offset:1024 sc1\n\t" \
    "buffer_load_dwordx4 v[228:231], %[l16], %[rs], %[m1] offen offset:0 sc1\n\t" \
    "buffer_load_dwordx4 v[232:235], %[l16], %[rs], %[m1] offen offset:1024 sc1\n\t" \
    "s_branch PM_%=\n\t" \
    "PD_%=:\n\t" \
    GSA(3) \
    "s_waitcnt lgkmcnt(0)\n\t" \
    "s_barrier\n\t" \
    : [lim] "+s"(lim) GSO(3) \
    : [l16] "v"(l16), [la] "v"(la), [rs] "s"(rs), [m0] "s"(mo), [m1] "s"(mo + 16384u), [tgt] "s"(tgt) \
    : "memory", "vcc", "scc", "v252", "s88", "s89", "s90", "s91", "s92", "s93", "v220", "v221", "v222", "v223", "v224", "v225", "v226", "v227", "v228", "v229", "v230", "v231", "v232", "v233", "v234", "v235")

#define T_WAIT() \
  asm volatile( \
    "TWC_%=:\n\t" \
    "s_memrealtime s[88:89]\n\t" \
    "s_memtime s[90:91]\n\t" \
    "s_waitcnt lgkmcnt(0)\n\t" \
    "s_lshl_b32 s88, s88, 2\n\t" \
    "s_sub_u32 s89, %[tgt], s88\n\t" \
    "s_cmp_gt_i32 s89, 16\n\t" \
    "s_cbranch_scc1 TWC_%=\n\t" \
    "s_mul_i32 s89, s89, 11\n\t" \
    "s_ashr_i32 s89, s89, 1\n\t" \
    "s_add_u32 s89, s89, s90\n\t" \
    "TCC_%=:\n\t" \
    "s_memtime s[92:93]\n\t" \
    "s_waitcnt lgkmcnt(0)\n\t" \
    "s_sub_u32 s93, s89, s92\n\t" \
    "s_cmp_gt_i32 s93, 0\n\t" \
    "s_cbranch_scc1 TCC_%=\n\t" \
    :  \
    : [tgt] "s"(tgt) \
    : "s88", "s89", "s90", "s91", "s92", "s93", "memory", "scc")

#define V_PROJ() \
  asm volatile( \
    "v_mov_b32 v0, 0\n\t" \
    "v_mov_b32 v1, 0\n\t" \
    "v_mov_b32 v2, 0\n\t" \
    "v_mov_b32 v3, 0\n\t" \
    "v_mov_b32 v4, 0\n\t" \
    "v_mov_b32 v5, 0\n\t" \
    "v_mov_b32 v6, 0\n\t" \
    "v_mov_b32 v7, 0\n\t" \
    "s_barrier\n\t" \
    "ds_read_b128 v[220:223], %[am] offset:0\n\t" \
    "ds_read_b128 v[224:227], %[am] offset:1024\n\t" \
    "ds_read_b128 v[228:231], %[am] offset:2048\n\t" \
    "ds_read_b128 v[232:235], %[am] offset:3072\n\t" \
    "ds_read_b128 v[236:239], %[am] offset:4096\n\t" \
    "ds_read_b128 v[240:243], %[am] offset:5120\n\t" \
    "ds_read_b128 v[244:247], %[am] offset:6144\n\t" \
    "ds_read_b128 v[248:251], %[am] offset:7168\n\t" \
    "s_waitcnt lgkmcnt(7)\n\t" \
    "v_smfmac_f32_16x16x64_bf16 v[0:3], v[220:223], v[28:35], %[idx]\n\t" \
    "ds_read_b128 v[220:223], %[am] offset:8192\n\t" \
    "s_waitcnt lgkmcnt(7)\n\t" \
    "v_smfmac_f32_16x16x64_bf16 v[4:7], v[224:227], v[36:43], %[idx]\n\t" \
    "ds_read_b128 v[224:227], %[am] offset:9216\n\t" \
    "s_waitcnt lgkmcnt(7)\n\t" \
    "v_smfmac_f32_16x16x64_bf16 v[0:3], v[228:231], v[44:51], %[idx]\n\t" \
    "ds_read_b128 v[228:231], %[am] offset:10240\n\t" \
    "s_waitcnt lgkmcnt(7)\n\t" \
    "v_smfmac_f32_16x16x64_bf16 v[4:7], v[232:235], v[52:59], %[idx]\n\t" \
    "ds_read_b128 v[232:235], %[am] offset:11264\n\t" \
    "s_waitcnt lgkmcnt(7)\n\t" \
    "v_smfmac_f32_16x16x64_bf16 v[0:3], v[236:239], v[60:67], %[idx]\n\t" \
    "ds_read_b128 v[236:239], %[am] offset:12288\n\t" \
    "s_waitcnt lgkmcnt(7)\n\t" \
    "v_smfmac_f32_16x16x64_bf16 v[4:7], v[240:243], v[68:75], %[idx]\n\t" \
    "ds_read_b128 v[240:243], %[am] offset:13312\n\t" \
    "s_waitcnt lgkmcnt(7)\n\t" \
    "v_smfmac_f32_16x16x64_bf16 v[0:3], v[244:247], v[76:83], %[idx]\n\t" \
    "ds_read_b128 v[244:247], %[am] offset:14336\n\t" \
    "s_waitcnt lgkmcnt(7)\n\t" \
    "v_smfmac_f32_16x16x64_bf16 v[4:7], v[248:251], v[84:91], %[idx]\n\t" \
    "ds_read_b128 v[248:251], %[am] offset:15360\n\t" \
    "s_waitcnt lgkmcnt(7)\n\t" \
    "v_smfmac_f32_16x16x64_bf16 v[0:3], v[220:223], v[92:99], %[idx]\n\t" \
    "s_waitcnt lgkmcnt(6)\n\t" \
    "v_smfmac_f32_16x16x64_bf16 v[4:7], v[224:227], v[100:107], %[idx]\n\t" \
    "s_waitcnt lgkmcnt(5)\n\t" \
    "v_smfmac_f32_16x16x64_bf16 v[0:3], v[228:231], v[108:115], %[idx]\n\t" \
    "s_waitcnt lgkmcnt(4)\n\t" \
    "v_smfmac_f32_16x16x64_bf16 v[4:7], v[232:235], v[116:123], %[idx]\n\t" \
    "s_waitcnt lgkmcnt(3)\n\t" \
    "v_smfmac_f32_16x16x64_bf16 v[0:3], v[236:239], v[124:131], %[idx]\n\t" \
    "s_waitcnt lgkmcnt(2)\n\t" \
    "v_smfmac_f32_16x16x64_bf16 v[4:7], v[240:243], v[132:139], %[idx]\n\t" \
    "s_waitcnt lgkmcnt(1)\n\t" \
    "v_smfmac_f32_16x16x64_bf16 v[0:3], v[244:247], v[140:147], %[idx]\n\t" \
    "s_waitcnt lgkmcnt(0)\n\t" \
    "v_smfmac_f32_16x16x64_bf16 v[4:7], v[248:251], v[148:155], %[idx]\n\t" \
    "s_nop 5\n\t" \
    "v_add_f32 v0, v0, v1\n\t" \
    "v_add_f32 v2, v2, v3\n\t" \
    "v_add_f32 v4, v4, v5\n\t" \
    "v_add_f32 v6, v6, v7\n\t" \
    "v_add_f32 v0, v0, v4\n\t" \
    "v_add_f32 v2, v2, v6\n\t" \
    "v_cvt_pk_bf16_f32 v0, v0, v2\n\t" \
    "v_min_u32 v0, -2, v0\n\t" \
    "s_nop 1\n\t" \
    "v_mov_b32_dpp v1, v0 quad_perm:[1,1,1,1] row_mask:0xf bank_mask:0xf\n\t" \
    "v_mov_b32_dpp v2, v0 quad_perm:[2,2,2,2] row_mask:0xf bank_mask:0xf\n\t" \
    "v_mov_b32_dpp v3, v0 quad_perm:[3,3,3,3] row_mask:0xf bank_mask:0xf\n\t" \
    "s_mov_b64 exec, %[qm]\n\t" \
    "buffer_store_dwordx4 v[0:3], %[l4], %[rs], %[vo] offen sc1\n\t" \
    "s_mov_b64 exec, -1\n\t" \
    "s_nop 1\n\t" \
    :  \
    : [am] "v"(am_a), [idx] "v"(idx), [l4] "v"(ln * 4), [rs] "s"(rs), [vo] "s"(sb + vo), [qm] "s"(0x1111111111111111ull), "{v[28:35]}"(wv[0]), "{v[36:43]}"(wv[1]), "{v[44:51]}"(wv[2]), "{v[52:59]}"(wv[3]), "{v[60:67]}"(wv[4]), "{v[68:75]}"(wv[5]), "{v[76:83]}"(wv[6]), "{v[84:91]}"(wv[7]), "{v[92:99]}"(wv[8]), "{v[100:107]}"(wv[9]), "{v[108:115]}"(wv[10]), "{v[116:123]}"(wv[11]), "{v[124:131]}"(wv[12]), "{v[132:139]}"(wv[13]), "{v[140:147]}"(wv[14]), "{v[148:155]}"(wv[15]) \
    : "memory", "v0", "v1", "v2", "v3", "v4", "v5", "v6", "v7", "v220", "v221", "v222", "v223", "v224", "v225", "v226", "v227", "v228", "v229", "v230", "v231", "v232", "v233", "v234", "v235", "v236", "v237", "v238", "v239", "v240", "v241", "v242", "v243", "v244", "v245", "v246", "v247", "v248", "v249", "v250", "v251")

#define WO_V() \
  asm volatile( \
    "v_mov_b32 v0, 0\n\t" \
    "v_mov_b32 v1, 0\n\t" \
    "v_mov_b32 v2, 0\n\t" \
    "v_mov_b32 v3, 0\n\t" \
    "v_mov_b32 v4, 0\n\t" \
    "v_mov_b32 v5, 0\n\t" \
    "v_mov_b32 v6, 0\n\t" \
    "v_mov_b32 v7, 0\n\t" \
    "v_mov_b32 v8, 0\n\t" \
    "v_mov_b32 v9, 0\n\t" \
    "v_mov_b32 v10, 0\n\t" \
    "v_mov_b32 v11, 0\n\t" \
    "v_mov_b32 v12, 0\n\t" \
    "v_mov_b32 v13, 0\n\t" \
    "v_mov_b32 v14, 0\n\t" \
    "v_mov_b32 v15, 0\n\t" \
    "v_mov_b32 v252, 0\n\t" \
    "s_barrier\n\t" \
    GSA(4) \
    DGC \
    "buffer_load_dwordx4 v[220:223], %[l16], %[rs], %[s0] offen sc1\n\t" \
    "buffer_load_dwordx4 v[224:227], %[l16], %[rs], %[s1] offen sc1\n\t" \
    "A_%=:\n\t" \
    "s_waitcnt vmcnt(1)\n\t" \
    "v_smfmac_f32_16x16x64_bf16 v[0:3], v[220:223], v[156:163], %[idx]\n\t" \
    "v_max3_u32 v252, v252, v220, v221\n\t" \
    "v_smfmac_f32_16x16x64_bf16 v[4:7], v[220:223], v[164:171], %[idx]\n\t" \
    "v_max3_u32 v252, v252, v222, v223\n\t" \
    "v_smfmac_f32_16x16x64_bf16 v[8:11], v[220:223], v[172:179], %[idx]\n\t" \
    "v_smfmac_f32_16x16x64_bf16 v[12:15], v[220:223], v[180:187], %[idx]\n\t" \
    "s_waitcnt vmcnt(0)\n\t" \
    "v_smfmac_f32_16x16x64_bf16 v[0:3], v[224:227], v[188:195], %[idx]\n\t" \
    "v_max3_u32 v252, v252, v224, v225\n\t" \
    "v_smfmac_f32_16x16x64_bf16 v[4:7], v[224:227], v[196:203], %[idx]\n\t" \
    "v_max3_u32 v252, v252, v226, v227\n\t" \
    "v_smfmac_f32_16x16x64_bf16 v[8:11], v[224:227], v[204:211], %[idx]\n\t" \
    "v_smfmac_f32_16x16x64_bf16 v[12:15], v[224:227], v[212:219], %[idx]\n\t" \
    "v_cmp_eq_u32 vcc, -1, v252\n\t" \
    "s_cbranch_vccnz SL_%=\n\t" \
    "D_%=:\n\t" \
    GSA(1) \
    "s_nop 2\n\t" \
    "v_add_f32 v0, v0, v1\n\t" \
    "v_add_f32 v2, v2, v3\n\t" \
    "v_add_f32 v4, v4, v5\n\t" \
    "v_add_f32 v6, v6, v7\n\t" \
    "v_add_f32 v8, v8, v9\n\t" \
    "v_add_f32 v10, v10, v11\n\t" \
    "v_add_f32 v12, v12, v13\n\t" \
    "v_add_f32 v14, v14, v15\n\t" \
    "v_permlane32_swap_b32 v0, v4\n\t" \
    "v_permlane32_swap_b32 v2, v6\n\t" \
    "v_permlane32_swap_b32 v8, v12\n\t" \
    "v_permlane32_swap_b32 v10, v14\n\t" \
    "v_add_f32 v0, v0, v12\n\t" \
    "v_add_f32 v2, v2, v14\n\t" \
    "ds_write2_b32 %[rw], v0, v2 offset0:0 offset1:32\n\t" \
    "s_waitcnt lgkmcnt(0)\n\t" \
    "s_barrier\n\t" \
    "s_cmp_eq_u32 %[red], 0\n\t" \
    "s_cbranch_scc0 E_%=\n\t" \
    "ds_read2st64_b64 v[0:3], %[rr] offset0:0 offset1:1\n\t" \
    "ds_read2st64_b64 v[4:7], %[rr] offset0:2 offset1:3\n\t" \
    "ds_read2st64_b64 v[8:11], %[rr] offset0:4 offset1:5\n\t" \
    "ds_read2st64_b64 v[12:15], %[rr] offset0:6 offset1:7\n\t" \
    "s_waitcnt lgkmcnt(3)\n\t" \
    "v_pk_add_f32 v[0:1], v[0:1], v[2:3]\n\t" \
    "s_waitcnt lgkmcnt(2)\n\t" \
    "v_pk_add_f32 v[4:5], v[4:5], v[6:7]\n\t" \
    "s_waitcnt lgkmcnt(1)\n\t" \
    "v_pk_add_f32 v[8:9], v[8:9], v[10:11]\n\t" \
    "v_pk_add_f32 v[0:1], v[0:1], v[4:5]\n\t" \
    "s_waitcnt lgkmcnt(0)\n\t" \
    "v_pk_add_f32 v[12:13], v[12:13], v[14:15]\n\t" \
    "s_nop 0\n\t" \
    "v_pk_add_f32 v[8:9], v[8:9], v[12:13]\n\t" \
    "s_nop 0\n\t" \
    "v_pk_add_f32 v[0:1], v[0:1], v[8:9]\n\t" \
    "s_nop 0\n\t" \
    "v_cvt_pk_bf16_f32 v0, v0, v1\n\t" \
    "v_min_u32 v0, -2, v0\n\t" \
    "buffer_store_dword v0, %[mo], %[rs], %[sn] offen sc1\n\t" \
    "s_memrealtime %[t1]\n\t" \
    DGA \
    "s_waitcnt lgkmcnt(0)\n\t" \
    GSA(2) \
    "s_branch E_%=\n\t" \
    "SL_%=:\n\t" \
    "s_sleep 1\n\t" \
    "buffer_load_dwordx4 v[220:223], %[l16], %[rs], %[s0] offen sc1\n\t" \
    "buffer_load_dwordx4 v[224:227], %[l16], %[rs], %[s1] offen sc1\n\t" \
    "s_sub_u32 %[lim], %[lim], " STR(DEC2) "\n\t" \
    "s_cmp_gt_i32 %[lim], 0\n\t" \
    "s_cbranch_scc0 G_%=\n\t" \
    "s_nop 3\n\t" \
    "v_mov_b32 v0, 0\n\t" \
    "v_mov_b32 v1, 0\n\t" \
    "v_mov_b32 v2, 0\n\t" \
    "v_mov_b32 v3, 0\n\t" \
    "v_mov_b32 v4, 0\n\t" \
    "v_mov_b32 v5, 0\n\t" \
    "v_mov_b32 v6, 0\n\t" \
    "v_mov_b32 v7, 0\n\t" \
    "v_mov_b32 v8, 0\n\t" \
    "v_mov_b32 v9, 0\n\t" \
    "v_mov_b32 v10, 0\n\t" \
    "v_mov_b32 v11, 0\n\t" \
    "v_mov_b32 v12, 0\n\t" \
    "v_mov_b32 v13, 0\n\t" \
    "v_mov_b32 v14, 0\n\t" \
    "v_mov_b32 v15, 0\n\t" \
    "v_mov_b32 v252, 0\n\t" \
    "s_branch A_%=\n\t" \
    "G_%=:\n\t" \
    "s_waitcnt vmcnt(0)\n\t" \
    "s_branch D_%=\n\t" \
    "E_%=:\n\t" \
    GSW \
    : [lim] "+s"(lim) GSO(1) GSO(4), [t1] "=&s"(t1) DGO DGQ GSO(2) \
    : [l16] "v"(l16), [rs] "s"(rs), [idx] "v"(idx), [rw] "v"(rw_a), [s0] "s"(so[0]), [s1] "s"(so[1]), [rr] "v"(rr_a), [mo] "v"(mo_v), [sn] "s"(sn), [red] "s"(wid), "{v[156:163]}"(wo[0][0][0]), "{v[164:171]}"(wo[0][0][1]), "{v[172:179]}"(wo[0][1][0]), "{v[180:187]}"(wo[0][1][1]), "{v[188:195]}"(wo[1][0][0]), "{v[196:203]}"(wo[1][0][1]), "{v[204:211]}"(wo[1][1][0]), "{v[212:219]}"(wo[1][1][1]) \
    : "memory", "vcc", "scc", "v252", "v0", "v1", "v2", "v3", "v4", "v5", "v6", "v7", "v8", "v9", "v10", "v11", "v12", "v13", "v14", "v15", "v220", "v221", "v222", "v223", "v224", "v225", "v226", "v227")

#define WO_W() \
  asm volatile( \
    "v_mov_b32 v0, 0\n\t" \
    "v_mov_b32 v1, 0\n\t" \
    "v_mov_b32 v2, 0\n\t" \
    "v_mov_b32 v3, 0\n\t" \
    "v_mov_b32 v4, 0\n\t" \
    "v_mov_b32 v5, 0\n\t" \
    "v_mov_b32 v6, 0\n\t" \
    "v_mov_b32 v7, 0\n\t" \
    "v_mov_b32 v8, 0\n\t" \
    "v_mov_b32 v9, 0\n\t" \
    "v_mov_b32 v10, 0\n\t" \
    "v_mov_b32 v11, 0\n\t" \
    "v_mov_b32 v12, 0\n\t" \
    "v_mov_b32 v13, 0\n\t" \
    "v_mov_b32 v14, 0\n\t" \
    "v_mov_b32 v15, 0\n\t" \
    "v_mov_b32 v252, 0\n\t" \
    "s_barrier\n\t" \
    GSA(4) \
    "buffer_load_dwordx4 v[220:223], %[l16], %[rs], %[s0] offen sc1\n\t" \
    "s_nop 15\n\t" \
    "buffer_load_dwordx4 v[224:227], %[l16], %[rs], %[s1] offen sc1\n\t" \
    "s_nop 15\n\t" \
    "buffer_load_dwordx4 v[228:231], %[l16], %[rs], %[s2] offen sc1\n\t" \
    "s_nop 15\n\t" \
    "buffer_load_dwordx4 v[232:235], %[l16], %[rs], %[s3] offen sc1\n\t" \
    "s_nop 15\n\t" \
    "buffer_load_dwordx4 v[236:239], %[l16], %[rs], %[s4] offen sc1\n\t" \
    "s_nop 15\n\t" \
    "buffer_load_dwordx4 v[240:243], %[l16], %[rs], %[s5] offen sc1\n\t" \
    "A_%=:\n\t" \
    "s_waitcnt vmcnt(5)\n\t" \
    "v_smfmac_f32_16x16x64_bf16 v[0:3], v[220:223], v[28:35], %[idx]\n\t" \
    "v_max3_u32 v252, v252, v220, v221\n\t" \
    "v_smfmac_f32_16x16x64_bf16 v[4:7], v[220:223], v[36:43], %[idx]\n\t" \
    "v_max3_u32 v252, v252, v222, v223\n\t" \
    "v_smfmac_f32_16x16x64_bf16 v[8:11], v[220:223], v[44:51], %[idx]\n\t" \
    "v_smfmac_f32_16x16x64_bf16 v[12:15], v[220:223], v[52:59], %[idx]\n\t" \
    "s_waitcnt vmcnt(4)\n\t" \
    "v_smfmac_f32_16x16x64_bf16 v[0:3], v[224:227], v[60:67], %[idx]\n\t" \
    "v_max3_u32 v252, v252, v224, v225\n\t" \
    "v_smfmac_f32_16x16x64_bf16 v[4:7], v[224:227], v[68:75], %[idx]\n\t" \
    "v_max3_u32 v252, v252, v226, v227\n\t" \
    "v_smfmac_f32_16x16x64_bf16 v[8:11], v[224:227], v[76:83], %[idx]\n\t" \
    "v_smfmac_f32_16x16x64_bf16 v[12:15], v[224:227], v[84:91], %[idx]\n\t" \
    "s_waitcnt vmcnt(3)\n\t" \
    "v_smfmac_f32_16x16x64_bf16 v[0:3], v[228:231], v[92:99], %[idx]\n\t" \
    "v_max3_u32 v252, v252, v228, v229\n\t" \
    "v_smfmac_f32_16x16x64_bf16 v[4:7], v[228:231], v[100:107], %[idx]\n\t" \
    "v_max3_u32 v252, v252, v230, v231\n\t" \
    "v_smfmac_f32_16x16x64_bf16 v[8:11], v[228:231], v[108:115], %[idx]\n\t" \
    "v_smfmac_f32_16x16x64_bf16 v[12:15], v[228:231], v[116:123], %[idx]\n\t" \
    "s_waitcnt vmcnt(2)\n\t" \
    "v_smfmac_f32_16x16x64_bf16 v[0:3], v[232:235], v[124:131], %[idx]\n\t" \
    "v_max3_u32 v252, v252, v232, v233\n\t" \
    "v_smfmac_f32_16x16x64_bf16 v[4:7], v[232:235], v[132:139], %[idx]\n\t" \
    "v_max3_u32 v252, v252, v234, v235\n\t" \
    "v_smfmac_f32_16x16x64_bf16 v[8:11], v[232:235], v[140:147], %[idx]\n\t" \
    "v_smfmac_f32_16x16x64_bf16 v[12:15], v[232:235], v[148:155], %[idx]\n\t" \
    "s_waitcnt vmcnt(1)\n\t" \
    "v_smfmac_f32_16x16x64_bf16 v[0:3], v[236:239], v[156:163], %[idx]\n\t" \
    "v_max3_u32 v252, v252, v236, v237\n\t" \
    "v_smfmac_f32_16x16x64_bf16 v[4:7], v[236:239], v[164:171], %[idx]\n\t" \
    "v_max3_u32 v252, v252, v238, v239\n\t" \
    "v_smfmac_f32_16x16x64_bf16 v[8:11], v[236:239], v[172:179], %[idx]\n\t" \
    "v_smfmac_f32_16x16x64_bf16 v[12:15], v[236:239], v[180:187], %[idx]\n\t" \
    "s_waitcnt vmcnt(0)\n\t" \
    "v_smfmac_f32_16x16x64_bf16 v[0:3], v[240:243], v[188:195], %[idx]\n\t" \
    "v_max3_u32 v252, v252, v240, v241\n\t" \
    "v_smfmac_f32_16x16x64_bf16 v[4:7], v[240:243], v[196:203], %[idx]\n\t" \
    "v_max3_u32 v252, v252, v242, v243\n\t" \
    "v_smfmac_f32_16x16x64_bf16 v[8:11], v[240:243], v[204:211], %[idx]\n\t" \
    "v_smfmac_f32_16x16x64_bf16 v[12:15], v[240:243], v[212:219], %[idx]\n\t" \
    "v_cmp_eq_u32 vcc, -1, v252\n\t" \
    "s_cbranch_vccnz SL_%=\n\t" \
    "D_%=:\n\t" \
    GSA(1) \
    "s_nop 2\n\t" \
    "v_add_f32 v0, v0, v1\n\t" \
    "v_add_f32 v2, v2, v3\n\t" \
    "v_add_f32 v4, v4, v5\n\t" \
    "v_add_f32 v6, v6, v7\n\t" \
    "v_add_f32 v8, v8, v9\n\t" \
    "v_add_f32 v10, v10, v11\n\t" \
    "v_add_f32 v12, v12, v13\n\t" \
    "v_add_f32 v14, v14, v15\n\t" \
    "v_permlane32_swap_b32 v0, v4\n\t" \
    "v_permlane32_swap_b32 v2, v6\n\t" \
    "v_permlane32_swap_b32 v8, v12\n\t" \
    "v_permlane32_swap_b32 v10, v14\n\t" \
    "v_add_f32 v0, v0, v12\n\t" \
    "v_add_f32 v2, v2, v14\n\t" \
    "ds_write2_b32 %[rw], v0, v2 offset0:0 offset1:32\n\t" \
    "s_waitcnt lgkmcnt(0)\n\t" \
    "s_barrier\n\t" \
    "s_branch E_%=\n\t" \
    "SL_%=:\n\t" \
    "s_sleep 1\n\t" \
    "buffer_load_dwordx4 v[220:223], %[l16], %[rs], %[s0] offen sc1\n\t" \
    "buffer_load_dwordx4 v[224:227], %[l16], %[rs], %[s1] offen sc1\n\t" \
    "buffer_load_dwordx4 v[228:231], %[l16], %[rs], %[s2] offen sc1\n\t" \
    "buffer_load_dwordx4 v[232:235], %[l16], %[rs], %[s3] offen sc1\n\t" \
    "buffer_load_dwordx4 v[236:239], %[l16], %[rs], %[s4] offen sc1\n\t" \
    "buffer_load_dwordx4 v[240:243], %[l16], %[rs], %[s5] offen sc1\n\t" \
    "s_sub_u32 %[lim], %[lim], " STR(DEC2) "\n\t" \
    "s_cmp_gt_i32 %[lim], 0\n\t" \
    "s_cbranch_scc0 G_%=\n\t" \
    "s_nop 3\n\t" \
    "v_mov_b32 v0, 0\n\t" \
    "v_mov_b32 v1, 0\n\t" \
    "v_mov_b32 v2, 0\n\t" \
    "v_mov_b32 v3, 0\n\t" \
    "v_mov_b32 v4, 0\n\t" \
    "v_mov_b32 v5, 0\n\t" \
    "v_mov_b32 v6, 0\n\t" \
    "v_mov_b32 v7, 0\n\t" \
    "v_mov_b32 v8, 0\n\t" \
    "v_mov_b32 v9, 0\n\t" \
    "v_mov_b32 v10, 0\n\t" \
    "v_mov_b32 v11, 0\n\t" \
    "v_mov_b32 v12, 0\n\t" \
    "v_mov_b32 v13, 0\n\t" \
    "v_mov_b32 v14, 0\n\t" \
    "v_mov_b32 v15, 0\n\t" \
    "v_mov_b32 v252, 0\n\t" \
    "s_branch A_%=\n\t" \
    "G_%=:\n\t" \
    "s_waitcnt vmcnt(0)\n\t" \
    "s_branch D_%=\n\t" \
    "E_%=:\n\t" \
    GSW \
    : [lim] "+s"(lim) GSO(1) GSO(4) \
    : [l16] "v"(l16), [rs] "s"(rs), [idx] "v"(idx), [rw] "v"(rw_a), [s0] "s"(so[0]), [s1] "s"(so[1]), [s2] "s"(so[2]), [s3] "s"(so[3]), [s4] "s"(so[4]), [s5] "s"(so[5]), "{v[28:35]}"(wo[0][0][0]), "{v[36:43]}"(wo[0][0][1]), "{v[44:51]}"(wo[0][1][0]), "{v[52:59]}"(wo[0][1][1]), "{v[60:67]}"(wo[1][0][0]), "{v[68:75]}"(wo[1][0][1]), "{v[76:83]}"(wo[1][1][0]), "{v[84:91]}"(wo[1][1][1]), "{v[92:99]}"(wo[2][0][0]), "{v[100:107]}"(wo[2][0][1]), "{v[108:115]}"(wo[2][1][0]), "{v[116:123]}"(wo[2][1][1]), "{v[124:131]}"(wo[3][0][0]), "{v[132:139]}"(wo[3][0][1]), "{v[140:147]}"(wo[3][1][0]), "{v[148:155]}"(wo[3][1][1]), "{v[156:163]}"(wo[4][0][0]), "{v[164:171]}"(wo[4][0][1]), "{v[172:179]}"(wo[4][1][0]), "{v[180:187]}"(wo[4][1][1]), "{v[188:195]}"(wo[5][0][0]), "{v[196:203]}"(wo[5][0][1]), "{v[204:211]}"(wo[5][1][0]), "{v[212:219]}"(wo[5][1][1]) \
    : "memory", "vcc", "scc", "v252", "v0", "v1", "v2", "v3", "v4", "v5", "v6", "v7", "v8", "v9", "v10", "v11", "v12", "v13", "v14", "v15", "v220", "v221", "v222", "v223", "v224", "v225", "v226", "v227", "v228", "v229", "v230", "v231", "v232", "v233", "v234", "v235", "v236", "v237", "v238", "v239", "v240", "v241", "v242", "v243")

#define S_LOOP_K(TE_, FIN_) \
  asm volatile( \
    "s_cmp_eq_u32 %[kh], 0\n\t" \
    "s_cbranch_scc0 L1_%=\n\t" \
    "L0_%=:\n\t" \
    "s_setprio 0\n\t" \
    "s_nop 3\n\t" \
    "ds_write_b128 %[pa], v[196:199] offset:0\n\t" \
    "ds_write_b128 %[pa], v[200:203] offset:1024\n\t" \
    "ds_write_b128 %[pa], v[204:207] offset:2048\n\t" \
    "ds_write_b128 %[pa], v[208:211] offset:3072\n\t" \
    "s_sub_u32 %[a], %[t], 1\n\t" \
    "v_mov_b32 v53, %[tag]\n\t" \
    "v_mov_b32 v52, %[a]\n\t" \
    "s_sub_u32 %[b], %[t], 2\n\t" \
    "s_cmp_lt_u32 %[b], " STR(SQUICK) "\n\t" \
    "s_cselect_b64 exec, %[fm], 0\n\t" \
    "buffer_store_dwordx2 v[52:53], %[l16], %[rs], %[fo] offen sc1\n\t" \
    "s_mov_b64 exec, -1\n\t" \
    "s_add_u32 m0, %[sd], 49152\n\t" \
    "s_add_u32 %[a], %[sx], 81920\n\t" \
    "s_waitcnt lgkmcnt(0)\n\t" \
    "buffer_load_dwordx4 %[l16], %[rs], %[a] offen offset:0 sc1 lds\n\t" \
    "buffer_load_dwordx4 %[l16], %[rs], %[a] offen offset:1024 sc1 lds\n\t" \
    "buffer_load_dwordx4 %[l16], %[rs], %[a] offen offset:2048 sc1 lds\n\t" \
    "buffer_load_dwordx4 %[l16], %[rs], %[a] offen offset:3072 sc1 lds\n\t" \
    "v_mfma_f32_16x16x32_bf16 v[0:3], v[68:71], v[20:23], 0\n\t" \
    "ds_read_b128 v[44:47], %[xp] offset:6144\n\t" \
    "v_mfma_f32_16x16x32_bf16 v[4:7], v[76:79], v[20:23], 0\n\t" \
    "ds_read_b128 v[48:51], %[xp] offset:7168\n\t" \
    "v_mfma_f32_16x16x32_bf16 v[8:11], v[68:71], v[24:27], 0\n\t" \
    "v_mfma_f32_16x16x32_bf16 v[12:15], v[76:79], v[24:27], 0\n\t" \
    "v_mfma_f32_16x16x32_bf16 v[0:3], v[72:75], v[28:31], v[0:3]\n\t" \
    "ds_read_b128 v[20:23], %[xp] offset:8192\n\t" \
    "v_mfma_f32_16x16x32_bf16 v[4:7], v[80:83], v[28:31], v[4:7]\n\t" \
    "ds_read_b128 v[24:27], %[xp] offset:9216\n\t" \
    "v_mfma_f32_16x16x32_bf16 v[8:11], v[72:75], v[32:35], v[8:11]\n\t" \
    "v_mfma_f32_16x16x32_bf16 v[12:15], v[80:83], v[32:35], v[12:15]\n\t" \
    "v_mfma_f32_16x16x32_bf16 v[0:3], v[84:87], v[36:39], v[0:3]\n\t" \
    "ds_read_b128 v[28:31], %[xp] offset:10240\n\t" \
    "v_mfma_f32_16x16x32_bf16 v[4:7], v[92:95], v[36:39], v[4:7]\n\t" \
    "ds_read_b128 v[32:35], %[xp] offset:11264\n\t" \
    "v_mfma_f32_16x16x32_bf16 v[8:11], v[84:87], v[40:43], v[8:11]\n\t" \
    "s_waitcnt lgkmcnt(4)\n\t" \
    "v_mfma_f32_16x16x32_bf16 v[12:15], v[92:95], v[40:43], v[12:15]\n\t" \
    "v_mfma_f32_16x16x32_bf16 v[0:3], v[88:91], v[44:47], v[0:3]\n\t" \
    "ds_read_b128 v[36:39], %[xp] offset:12288\n\t" \
    "v_mfma_f32_16x16x32_bf16 v[4:7], v[96:99], v[44:47], v[4:7]\n\t" \
    "ds_read_b128 v[40:43], %[xp] offset:13312\n\t" \
    "v_mfma_f32_16x16x32_bf16 v[8:11], v[88:91], v[48:51], v[8:11]\n\t" \
    "s_waitcnt lgkmcnt(4)\n\t" \
    "v_mfma_f32_16x16x32_bf16 v[12:15], v[96:99], v[48:51], v[12:15]\n\t" \
    "v_mfma_f32_16x16x32_bf16 v[0:3], v[100:103], v[20:23], v[0:3]\n\t" \
    "ds_read_b128 v[44:47], %[xp] offset:14336\n\t" \
    "v_mfma_f32_16x16x32_bf16 v[4:7], v[108:111], v[20:23], v[4:7]\n\t" \
    "ds_read_b128 v[48:51], %[xp] offset:15360\n\t" \
    "v_mfma_f32_16x16x32_bf16 v[8:11], v[100:103], v[24:27], v[8:11]\n\t" \
    "s_waitcnt lgkmcnt(4)\n\t" \
    "v_mfma_f32_16x16x32_bf16 v[12:15], v[108:111], v[24:27], v[12:15]\n\t" \
    "v_mfma_f32_16x16x32_bf16 v[0:3], v[104:107], v[28:31], v[0:3]\n\t" \
    "ds_read_b128 v[20:23], %[xp] offset:16384\n\t" \
    "v_mfma_f32_16x16x32_bf16 v[4:7], v[112:115], v[28:31], v[4:7]\n\t" \
    "ds_read_b128 v[24:27], %[xp] offset:17408\n\t" \
    "v_mfma_f32_16x16x32_bf16 v[8:11], v[104:107], v[32:35], v[8:11]\n\t" \
    "s_waitcnt lgkmcnt(4)\n\t" \
    "v_mfma_f32_16x16x32_bf16 v[12:15], v[112:115], v[32:35], v[12:15]\n\t" \
    "v_mfma_f32_16x16x32_bf16 v[0:3], v[116:119], v[36:39], v[0:3]\n\t" \
    "ds_read_b128 v[28:31], %[xp] offset:18432\n\t" \
    "v_mfma_f32_16x16x32_bf16 v[4:7], v[124:127], v[36:39], v[4:7]\n\t" \
    "ds_read_b128 v[32:35], %[xp] offset:19456\n\t" \
    "v_mfma_f32_16x16x32_bf16 v[8:11], v[116:119], v[40:43], v[8:11]\n\t" \
    "s_waitcnt lgkmcnt(4)\n\t" \
    "v_mfma_f32_16x16x32_bf16 v[12:15], v[124:127], v[40:43], v[12:15]\n\t" \
    "v_mfma_f32_16x16x32_bf16 v[0:3], v[120:123], v[44:47], v[0:3]\n\t" \
    "ds_read_b128 v[36:39], %[xp] offset:20480\n\t" \
    "v_mfma_f32_16x16x32_bf16 v[4:7], v[128:131], v[44:47], v[4:7]\n\t" \
    "ds_read_b128 v[40:43], %[xp] offset:21504\n\t" \
    "v_mfma_f32_16x16x32_bf16 v[8:11], v[120:123], v[48:51], v[8:11]\n\t" \
    "s_waitcnt lgkmcnt(4)\n\t" \
    "v_mfma_f32_16x16x32_bf16 v[12:15], v[128:131], v[48:51], v[12:15]\n\t" \
    "s_waitcnt vmcnt(4)\n\t" \
    "s_barrier\n\t" \
    "s_setprio 0\n\t" \
    "s_sub_u32 %[a], %[t], 1\n\t" \
    "v_mov_b32 v53, %[tag]\n\t" \
    "v_mov_b32 v52, %[a]\n\t" \
    "s_cmp_gt_u32 %[t], 1\n\t" \
    "s_cselect_b64 exec, %[fm], 0\n\t" \
    "buffer_store_dwordx2 v[52:53], %[l16], %[rs], %[fo] offen sc1\n\t" \
    "s_mov_b64 exec, %[pm]\n\t" \
    "s_mov_b32 m0, %[pd]\n\t" \
    "s_add_u32 %[a], %[sx], %[po]\n\t" \
    "buffer_load_dwordx4 %[l16], %[rs], %[a] offen sc1 lds\n\t" \
    "s_mov_b64 exec, -1\n\t" \
    "s_add_u32 m0, %[sd], 0\n\t" \
    "s_add_u32 %[a], %[sx], 131072\n\t" \
    "buffer_load_dwordx4 %[l16], %[rs], %[a] offen offset:0 sc1 lds\n\t" \
    "buffer_load_dwordx4 %[l16], %[rs], %[a] offen offset:1024 sc1 lds\n\t" \
    "buffer_load_dwordx4 %[l16], %[rs], %[a] offen offset:2048 sc1 lds\n\t" \
    "buffer_load_dwordx4 %[l16], %[rs], %[a] offen offset:3072 sc1 lds\n\t" \
    "v_mfma_f32_16x16x32_bf16 v[0:3], v[132:135], v[20:23], v[0:3]\n\t" \
    "ds_read_b128 v[44:47], %[xp] offset:22528\n\t" \
    "v_mfma_f32_16x16x32_bf16 v[4:7], v[140:143], v[20:23], v[4:7]\n\t" \
    "ds_read_b128 v[48:51], %[xp] offset:23552\n\t" \
    "v_mfma_f32_16x16x32_bf16 v[8:11], v[132:135], v[24:27], v[8:11]\n\t" \
    "s_waitcnt lgkmcnt(4)\n\t" \
    "v_mfma_f32_16x16x32_bf16 v[12:15], v[140:143], v[24:27], v[12:15]\n\t" \
    "v_mfma_f32_16x16x32_bf16 v[0:3], v[136:139], v[28:31], v[0:3]\n\t" \
    "ds_read_b128 v[20:23], %[xp] offset:24576\n\t" \
    "v_mfma_f32_16x16x32_bf16 v[4:7], v[144:147], v[28:31], v[4:7]\n\t" \
    "ds_read_b128 v[24:27], %[xp] offset:25600\n\t" \
    "v_mfma_f32_16x16x32_bf16 v[8:11], v[136:139], v[32:35], v[8:11]\n\t" \
    "s_waitcnt lgkmcnt(4)\n\t" \
    "v_mfma_f32_16x16x32_bf16 v[12:15], v[144:147], v[32:35], v[12:15]\n\t" \
    "v_mfma_f32_16x16x32_bf16 v[0:3], v[148:151], v[36:39], v[0:3]\n\t" \
    "ds_read_b128 v[28:31], %[xp] offset:26624\n\t" \
    "v_mfma_f32_16x16x32_bf16 v[4:7], v[156:159], v[36:39], v[4:7]\n\t" \
    "ds_read_b128 v[32:35], %[xp] offset:27648\n\t" \
    "v_mfma_f32_16x16x32_bf16 v[8:11], v[148:151], v[40:43], v[8:11]\n\t" \
    "s_waitcnt lgkmcnt(4)\n\t" \
    "v_mfma_f32_16x16x32_bf16 v[12:15], v[156:159], v[40:43], v[12:15]\n\t" \
    "v_mfma_f32_16x16x32_bf16 v[0:3], v[152:155], v[44:47], v[0:3]\n\t" \
    "ds_read_b128 v[36:39], %[xp] offset:28672\n\t" \
    "v_mfma_f32_16x16x32_bf16 v[4:7], v[160:163], v[44:47], v[4:7]\n\t" \
    "ds_read_b128 v[40:43], %[xp] offset:29696\n\t" \
    "v_mfma_f32_16x16x32_bf16 v[8:11], v[152:155], v[48:51], v[8:11]\n\t" \
    "s_waitcnt lgkmcnt(4)\n\t" \
    "v_mfma_f32_16x16x32_bf16 v[12:15], v[160:163], v[48:51], v[12:15]\n\t" \
    "v_mfma_f32_16x16x32_bf16 v[0:3], v[164:167], v[20:23], v[0:3]\n\t" \
    "ds_read_b128 v[44:47], %[xp] offset:30720\n\t" \
    "v_mfma_f32_16x16x32_bf16 v[4:7], v[172:175], v[20:23], v[4:7]\n\t" \
    "ds_read_b128 v[48:51], %[xp] offset:31744\n\t" \
    "v_mfma_f32_16x16x32_bf16 v[8:11], v[164:167], v[24:27], v[8:11]\n\t" \
    "s_waitcnt lgkmcnt(4)\n\t" \
    "v_mfma_f32_16x16x32_bf16 v[12:15], v[172:175], v[24:27], v[12:15]\n\t" \
    "v_mfma_f32_16x16x32_bf16 v[0:3], v[168:171], v[28:31], v[0:3]\n\t" \
    "ds_read_b128 v[20:23], %[xp] offset:32768\n\t" \
    "v_mfma_f32_16x16x32_bf16 v[4:7], v[176:179], v[28:31], v[4:7]\n\t" \
    "ds_read_b128 v[24:27], %[xp] offset:33792\n\t" \
    "v_mfma_f32_16x16x32_bf16 v[8:11], v[168:171], v[32:35], v[8:11]\n\t" \
    "s_waitcnt lgkmcnt(4)\n\t" \
    "v_mfma_f32_16x16x32_bf16 v[12:15], v[176:179], v[32:35], v[12:15]\n\t" \
    "v_mfma_f32_16x16x32_bf16 v[0:3], v[180:183], v[36:39], v[0:3]\n\t" \
    "ds_read_b128 v[28:31], %[xp] offset:34816\n\t" \
    "v_mfma_f32_16x16x32_bf16 v[4:7], v[188:191], v[36:39], v[4:7]\n\t" \
    "ds_read_b128 v[32:35], %[xp] offset:35840\n\t" \
    "v_mfma_f32_16x16x32_bf16 v[8:11], v[180:183], v[40:43], v[8:11]\n\t" \
    "s_waitcnt lgkmcnt(4)\n\t" \
    "v_mfma_f32_16x16x32_bf16 v[12:15], v[188:191], v[40:43], v[12:15]\n\t" \
    "v_mfma_f32_16x16x32_bf16 v[0:3], v[184:187], v[44:47], v[0:3]\n\t" \
    "ds_read_b128 v[36:39], %[xp] offset:36864\n\t" \
    "v_mfma_f32_16x16x32_bf16 v[4:7], v[192:195], v[44:47], v[4:7]\n\t" \
    "ds_read_b128 v[40:43], %[xp] offset:37888\n\t" \
    "v_mfma_f32_16x16x32_bf16 v[8:11], v[184:187], v[48:51], v[8:11]\n\t" \
    "s_waitcnt lgkmcnt(4)\n\t" \
    "v_mfma_f32_16x16x32_bf16 v[12:15], v[192:195], v[48:51], v[12:15]\n\t" \
    "s_add_u32 %[sx], %[sx], 65536\n\t" \
    "s_add_u32 %[t], %[t], 1\n\t" \
    "s_waitcnt vmcnt(4)\n\t" \
    "s_barrier\n\t" \
    "s_setprio 0\n\t" \
    "s_nop 3\n\t" \
    "ds_write_b128 %[pa], v[0:3] offset:0\n\t" \
    "ds_write_b128 %[pa], v[4:7] offset:1024\n\t" \
    "ds_write_b128 %[pa], v[8:11] offset:2048\n\t" \
    "ds_write_b128 %[pa], v[12:15] offset:3072\n\t" \
    "s_sub_u32 %[a], %[t], 1\n\t" \
    "v_mov_b32 v53, %[tag]\n\t" \
    "v_mov_b32 v52, %[a]\n\t" \
    "s_sub_u32 %[b], %[t], 2\n\t" \
    "s_cmp_lt_u32 %[b], " STR(SQUICK) "\n\t" \
    "s_cselect_b64 exec, %[fm], 0\n\t" \
    "buffer_store_dwordx2 v[52:53], %[l16], %[rs], %[fo] offen sc1\n\t" \
    "s_mov_b64 exec, -1\n\t" \
    "s_add_u32 m0, %[sd], 16384\n\t" \
    "s_add_u32 %[a], %[sx], 81920\n\t" \
    "s_waitcnt lgkmcnt(0)\n\t" \
    "buffer_load_dwordx4 %[l16], %[rs], %[a] offen offset:0 sc1 lds\n\t" \
    "buffer_load_dwordx4 %[l16], %[rs], %[a] offen offset:1024 sc1 lds\n\t" \
    "buffer_load_dwordx4 %[l16], %[rs], %[a] offen offset:2048 sc1 lds\n\t" \
    "buffer_load_dwordx4 %[l16], %[rs], %[a] offen offset:3072 sc1 lds\n\t" \
    "v_mfma_f32_16x16x32_bf16 v[196:199], v[68:71], v[20:23], 0\n\t" \
    "ds_read_b128 v[44:47], %[xp] offset:38912\n\t" \
    "v_mfma_f32_16x16x32_bf16 v[200:203], v[76:79], v[20:23], 0\n\t" \
    "ds_read_b128 v[48:51], %[xp] offset:39936\n\t" \
    "v_mfma_f32_16x16x32_bf16 v[204:207], v[68:71], v[24:27], 0\n\t" \
    "v_mfma_f32_16x16x32_bf16 v[208:211], v[76:79], v[24:27], 0\n\t" \
    "v_mfma_f32_16x16x32_bf16 v[196:199], v[72:75], v[28:31], v[196:199]\n\t" \
    "ds_read_b128 v[20:23], %[xp] offset:40960\n\t" \
    "v_mfma_f32_16x16x32_bf16 v[200:203], v[80:83], v[28:31], v[200:203]\n\t" \
    "ds_read_b128 v[24:27], %[xp] offset:41984\n\t" \
    "v_mfma_f32_16x16x32_bf16 v[204:207], v[72:75], v[32:35], v[204:207]\n\t" \
    "v_mfma_f32_16x16x32_bf16 v[208:211], v[80:83], v[32:35], v[208:211]\n\t" \
    "v_mfma_f32_16x16x32_bf16 v[196:199], v[84:87], v[36:39], v[196:199]\n\t" \
    "ds_read_b128 v[28:31], %[xp] offset:43008\n\t" \
    "v_mfma_f32_16x16x32_bf16 v[200:203], v[92:95], v[36:39], v[200:203]\n\t" \
    "ds_read_b128 v[32:35], %[xp] offset:44032\n\t" \
    "v_mfma_f32_16x16x32_bf16 v[204:207], v[84:87], v[40:43], v[204:207]\n\t" \
    "s_waitcnt lgkmcnt(4)\n\t" \
    "v_mfma_f32_16x16x32_bf16 v[208:211], v[92:95], v[40:43], v[208:211]\n\t" \
    "v_mfma_f32_16x16x32_bf16 v[196:199], v[88:91], v[44:47], v[196:199]\n\t" \
    "ds_read_b128 v[36:39], %[xp] offset:45056\n\t" \
    "v_mfma_f32_16x16x32_bf16 v[200:203], v[96:99], v[44:47], v[200:203]\n\t" \
    "ds_read_b128 v[40:43], %[xp] offset:46080\n\t" \
    "v_mfma_f32_16x16x32_bf16 v[204:207], v[88:91], v[48:51], v[204:207]\n\t" \
    "s_waitcnt lgkmcnt(4)\n\t" \
    "v_mfma_f32_16x16x32_bf16 v[208:211], v[96:99], v[48:51], v[208:211]\n\t" \
    "v_mfma_f32_16x16x32_bf16 v[196:199], v[100:103], v[20:23], v[196:199]\n\t" \
    "ds_read_b128 v[44:47], %[xp] offset:47104\n\t" \
    "v_mfma_f32_16x16x32_bf16 v[200:203], v[108:111], v[20:23], v[200:203]\n\t" \
    "ds_read_b128 v[48:51], %[xp] offset:48128\n\t" \
    "v_mfma_f32_16x16x32_bf16 v[204:207], v[100:103], v[24:27], v[204:207]\n\t" \
    "s_waitcnt lgkmcnt(4)\n\t" \
    "v_mfma_f32_16x16x32_bf16 v[208:211], v[108:111], v[24:27], v[208:211]\n\t" \
    "v_mfma_f32_16x16x32_bf16 v[196:199], v[104:107], v[28:31], v[196:199]\n\t" \
    "ds_read_b128 v[20:23], %[xp] offset:49152\n\t" \
    "v_mfma_f32_16x16x32_bf16 v[200:203], v[112:115], v[28:31], v[200:203]\n\t" \
    "ds_read_b128 v[24:27], %[xp] offset:50176\n\t" \
    "v_mfma_f32_16x16x32_bf16 v[204:207], v[104:107], v[32:35], v[204:207]\n\t" \
    "s_waitcnt lgkmcnt(4)\n\t" \
    "v_mfma_f32_16x16x32_bf16 v[208:211], v[112:115], v[32:35], v[208:211]\n\t" \
    "v_mfma_f32_16x16x32_bf16 v[196:199], v[116:119], v[36:39], v[196:199]\n\t" \
    "ds_read_b128 v[28:31], %[xp] offset:51200\n\t" \
    "v_mfma_f32_16x16x32_bf16 v[200:203], v[124:127], v[36:39], v[200:203]\n\t" \
    "ds_read_b128 v[32:35], %[xp] offset:52224\n\t" \
    "v_mfma_f32_16x16x32_bf16 v[204:207], v[116:119], v[40:43], v[204:207]\n\t" \
    "s_waitcnt lgkmcnt(4)\n\t" \
    "v_mfma_f32_16x16x32_bf16 v[208:211], v[124:127], v[40:43], v[208:211]\n\t" \
    "v_mfma_f32_16x16x32_bf16 v[196:199], v[120:123], v[44:47], v[196:199]\n\t" \
    "ds_read_b128 v[36:39], %[xp] offset:53248\n\t" \
    "v_mfma_f32_16x16x32_bf16 v[200:203], v[128:131], v[44:47], v[200:203]\n\t" \
    "ds_read_b128 v[40:43], %[xp] offset:54272\n\t" \
    "v_mfma_f32_16x16x32_bf16 v[204:207], v[120:123], v[48:51], v[204:207]\n\t" \
    "s_waitcnt lgkmcnt(4)\n\t" \
    "v_mfma_f32_16x16x32_bf16 v[208:211], v[128:131], v[48:51], v[208:211]\n\t" \
    "s_waitcnt vmcnt(4)\n\t" \
    "s_barrier\n\t" \
    "s_setprio 0\n\t" \
    "s_sub_u32 %[a], %[t], 1\n\t" \
    "v_mov_b32 v53, %[tag]\n\t" \
    "v_mov_b32 v52, %[a]\n\t" \
    "s_cmp_gt_u32 %[t], 1\n\t" \
    "s_cselect_b64 exec, %[fm], 0\n\t" \
    "buffer_store_dwordx2 v[52:53], %[l16], %[rs], %[fo] offen sc1\n\t" \
    "s_mov_b64 exec, %[pm]\n\t" \
    "s_mov_b32 m0, %[pd]\n\t" \
    "s_add_u32 %[a], %[sx], %[po]\n\t" \
    "buffer_load_dwordx4 %[l16], %[rs], %[a] offen sc1 lds\n\t" \
    "s_mov_b64 exec, -1\n\t" \
    "s_add_u32 m0, %[sd], 32768\n\t" \
    "s_add_u32 %[a], %[sx], 131072\n\t" \
    "buffer_load_dwordx4 %[l16], %[rs], %[a] offen offset:0 sc1 lds\n\t" \
    "buffer_load_dwordx4 %[l16], %[rs], %[a] offen offset:1024 sc1 lds\n\t" \
    "buffer_load_dwordx4 %[l16], %[rs], %[a] offen offset:2048 sc1 lds\n\t" \
    "buffer_load_dwordx4 %[l16], %[rs], %[a] offen offset:3072 sc1 lds\n\t" \
    "v_mfma_f32_16x16x32_bf16 v[196:199], v[132:135], v[20:23], v[196:199]\n\t" \
    "ds_read_b128 v[44:47], %[xp] offset:55296\n\t" \
    "v_mfma_f32_16x16x32_bf16 v[200:203], v[140:143], v[20:23], v[200:203]\n\t" \
    "ds_read_b128 v[48:51], %[xp] offset:56320\n\t" \
    "v_mfma_f32_16x16x32_bf16 v[204:207], v[132:135], v[24:27], v[204:207]\n\t" \
    "s_waitcnt lgkmcnt(4)\n\t" \
    "v_mfma_f32_16x16x32_bf16 v[208:211], v[140:143], v[24:27], v[208:211]\n\t" \
    "v_mfma_f32_16x16x32_bf16 v[196:199], v[136:139], v[28:31], v[196:199]\n\t" \
    "ds_read_b128 v[20:23], %[xp] offset:57344\n\t" \
    "v_mfma_f32_16x16x32_bf16 v[200:203], v[144:147], v[28:31], v[200:203]\n\t" \
    "ds_read_b128 v[24:27], %[xp] offset:58368\n\t" \
    "v_mfma_f32_16x16x32_bf16 v[204:207], v[136:139], v[32:35], v[204:207]\n\t" \
    "s_waitcnt lgkmcnt(4)\n\t" \
    "v_mfma_f32_16x16x32_bf16 v[208:211], v[144:147], v[32:35], v[208:211]\n\t" \
    "v_mfma_f32_16x16x32_bf16 v[196:199], v[148:151], v[36:39], v[196:199]\n\t" \
    "ds_read_b128 v[28:31], %[xp] offset:59392\n\t" \
    "v_mfma_f32_16x16x32_bf16 v[200:203], v[156:159], v[36:39], v[200:203]\n\t" \
    "ds_read_b128 v[32:35], %[xp] offset:60416\n\t" \
    "v_mfma_f32_16x16x32_bf16 v[204:207], v[148:151], v[40:43], v[204:207]\n\t" \
    "s_waitcnt lgkmcnt(4)\n\t" \
    "v_mfma_f32_16x16x32_bf16 v[208:211], v[156:159], v[40:43], v[208:211]\n\t" \
    "v_mfma_f32_16x16x32_bf16 v[196:199], v[152:155], v[44:47], v[196:199]\n\t" \
    "ds_read_b128 v[36:39], %[xp] offset:61440\n\t" \
    "v_mfma_f32_16x16x32_bf16 v[200:203], v[160:163], v[44:47], v[200:203]\n\t" \
    "ds_read_b128 v[40:43], %[xp] offset:62464\n\t" \
    "v_mfma_f32_16x16x32_bf16 v[204:207], v[152:155], v[48:51], v[204:207]\n\t" \
    "s_waitcnt lgkmcnt(4)\n\t" \
    "v_mfma_f32_16x16x32_bf16 v[208:211], v[160:163], v[48:51], v[208:211]\n\t" \
    "v_mfma_f32_16x16x32_bf16 v[196:199], v[164:167], v[20:23], v[196:199]\n\t" \
    "ds_read_b128 v[44:47], %[xp] offset:63488\n\t" \
    "v_mfma_f32_16x16x32_bf16 v[200:203], v[172:175], v[20:23], v[200:203]\n\t" \
    "ds_read_b128 v[48:51], %[xp] offset:64512\n\t" \
    "v_mfma_f32_16x16x32_bf16 v[204:207], v[164:167], v[24:27], v[204:207]\n\t" \
    "s_waitcnt lgkmcnt(4)\n\t" \
    "v_mfma_f32_16x16x32_bf16 v[208:211], v[172:175], v[24:27], v[208:211]\n\t" \
    "v_mfma_f32_16x16x32_bf16 v[196:199], v[168:171], v[28:31], v[196:199]\n\t" \
    "ds_read_b128 v[20:23], %[xp] offset:0\n\t" \
    "v_mfma_f32_16x16x32_bf16 v[200:203], v[176:179], v[28:31], v[200:203]\n\t" \
    "ds_read_b128 v[24:27], %[xp] offset:1024\n\t" \
    "v_mfma_f32_16x16x32_bf16 v[204:207], v[168:171], v[32:35], v[204:207]\n\t" \
    "s_waitcnt lgkmcnt(4)\n\t" \
    "v_mfma_f32_16x16x32_bf16 v[208:211], v[176:179], v[32:35], v[208:211]\n\t" \
    "v_mfma_f32_16x16x32_bf16 v[196:199], v[180:183], v[36:39], v[196:199]\n\t" \
    "ds_read_b128 v[28:31], %[xp] offset:2048\n\t" \
    "v_mfma_f32_16x16x32_bf16 v[200:203], v[188:191], v[36:39], v[200:203]\n\t" \
    "ds_read_b128 v[32:35], %[xp] offset:3072\n\t" \
    "v_mfma_f32_16x16x32_bf16 v[204:207], v[180:183], v[40:43], v[204:207]\n\t" \
    "s_waitcnt lgkmcnt(4)\n\t" \
    "v_mfma_f32_16x16x32_bf16 v[208:211], v[188:191], v[40:43], v[208:211]\n\t" \
    "v_mfma_f32_16x16x32_bf16 v[196:199], v[184:187], v[44:47], v[196:199]\n\t" \
    "ds_read_b128 v[36:39], %[xp] offset:4096\n\t" \
    "v_mfma_f32_16x16x32_bf16 v[200:203], v[192:195], v[44:47], v[200:203]\n\t" \
    "ds_read_b128 v[40:43], %[xp] offset:5120\n\t" \
    "v_mfma_f32_16x16x32_bf16 v[204:207], v[184:187], v[48:51], v[204:207]\n\t" \
    "s_waitcnt lgkmcnt(4)\n\t" \
    "v_mfma_f32_16x16x32_bf16 v[208:211], v[192:195], v[48:51], v[208:211]\n\t" \
    "s_add_u32 %[sx], %[sx], 65536\n\t" \
    "s_add_u32 %[t], %[t], 1\n\t" \
    "s_waitcnt vmcnt(4)\n\t" \
    "s_barrier\n\t" \
    "s_cmp_lt_u32 %[t], %[te]\n\t" \
    "s_cbranch_scc1 L0_%=\n\t" \
    "s_cmp_eq_u32 %[fin], %[te]\n\t" \
    "s_cbranch_scc0 E_%=\n\t" \
    "s_nop 4\n\t" \
    "ds_write_b128 %[pa], v[196:199] offset:0\n\t" \
    "ds_write_b128 %[pa], v[200:203] offset:1024\n\t" \
    "ds_write_b128 %[pa], v[204:207] offset:2048\n\t" \
    "ds_write_b128 %[pa], v[208:211] offset:3072\n\t" \
    "s_waitcnt lgkmcnt(0)\n\t" \
    "s_barrier\n\t" \
    "s_branch E_%=\n\t" \
    "L1_%=:\n\t" \
    "s_setprio 1\n\t" \
    "s_add_u32 m0, %[sd], 49152\n\t" \
    "s_add_u32 %[a], %[sx], 81920\n\t" \
    "v_mfma_f32_16x16x32_bf16 v[0:3], v[68:71], v[20:23], 0\n\t" \
    "ds_read_b128 v[44:47], %[xp] offset:6144\n\t" \
    "v_mfma_f32_16x16x32_bf16 v[4:7], v[76:79], v[20:23], 0\n\t" \
    "ds_read_b128 v[48:51], %[xp] offset:7168\n\t" \
    "v_mfma_f32_16x16x32_bf16 v[8:11], v[68:71], v[24:27], 0\n\t" \
    "s_waitcnt lgkmcnt(4)\n\t" \
    "v_mfma_f32_16x16x32_bf16 v[12:15], v[76:79], v[24:27], 0\n\t" \
    "v_mfma_f32_16x16x32_bf16 v[0:3], v[72:75], v[28:31], v[0:3]\n\t" \
    "ds_read_b128 v[20:23], %[xp] offset:8192\n\t" \
    "v_mfma_f32_16x16x32_bf16 v[4:7], v[80:83], v[28:31], v[4:7]\n\t" \
    "ds_read_b128 v[24:27], %[xp] offset:9216\n\t" \
    "v_mfma_f32_16x16x32_bf16 v[8:11], v[72:75], v[32:35], v[8:11]\n\t" \
    "s_waitcnt lgkmcnt(4)\n\t" \
    "v_mfma_f32_16x16x32_bf16 v[12:15], v[80:83], v[32:35], v[12:15]\n\t" \
    "v_mfma_f32_16x16x32_bf16 v[0:3], v[84:87], v[36:39], v[0:3]\n\t" \
    "ds_read_b128 v[28:31], %[xp] offset:10240\n\t" \
    "v_mfma_f32_16x16x32_bf16 v[4:7], v[92:95], v[36:39], v[4:7]\n\t" \
    "ds_read_b128 v[32:35], %[xp] offset:11264\n\t" \
    "v_mfma_f32_16x16x32_bf16 v[8:11], v[84:87], v[40:43], v[8:11]\n\t" \
    "s_waitcnt lgkmcnt(4)\n\t" \
    "v_mfma_f32_16x16x32_bf16 v[12:15], v[92:95], v[40:43], v[12:15]\n\t" \
    "v_mfma_f32_16x16x32_bf16 v[0:3], v[88:91], v[44:47], v[0:3]\n\t" \
    "ds_read_b128 v[36:39], %[xp] offset:12288\n\t" \
    "v_mfma_f32_16x16x32_bf16 v[4:7], v[96:99], v[44:47], v[4:7]\n\t" \
    "ds_read_b128 v[40:43], %[xp] offset:13312\n\t" \
    "v_mfma_f32_16x16x32_bf16 v[8:11], v[88:91], v[48:51], v[8:11]\n\t" \
    "s_waitcnt lgkmcnt(4)\n\t" \
    "v_mfma_f32_16x16x32_bf16 v[12:15], v[96:99], v[48:51], v[12:15]\n\t" \
    "v_mfma_f32_16x16x32_bf16 v[0:3], v[100:103], v[20:23], v[0:3]\n\t" \
    "ds_read_b128 v[44:47], %[xp] offset:14336\n\t" \
    "v_mfma_f32_16x16x32_bf16 v[4:7], v[108:111], v[20:23], v[4:7]\n\t" \
    "ds_read_b128 v[48:51], %[xp] offset:15360\n\t" \
    "v_mfma_f32_16x16x32_bf16 v[8:11], v[100:103], v[24:27], v[8:11]\n\t" \
    "s_waitcnt lgkmcnt(4)\n\t" \
    "v_mfma_f32_16x16x32_bf16 v[12:15], v[108:111], v[24:27], v[12:15]\n\t" \
    "buffer_load_dwordx4 %[l16], %[rs], %[a] offen offset:0 sc1 lds\n\t" \
    "v_mfma_f32_16x16x32_bf16 v[0:3], v[104:107], v[28:31], v[0:3]\n\t" \
    "ds_read_b128 v[20:23], %[xp] offset:16384\n\t" \
    "v_mfma_f32_16x16x32_bf16 v[4:7], v[112:115], v[28:31], v[4:7]\n\t" \
    "ds_read_b128 v[24:27], %[xp] offset:17408\n\t" \
    "v_mfma_f32_16x16x32_bf16 v[8:11], v[104:107], v[32:35], v[8:11]\n\t" \
    "s_waitcnt lgkmcnt(4)\n\t" \
    "v_mfma_f32_16x16x32_bf16 v[12:15], v[112:115], v[32:35], v[12:15]\n\t" \
    "buffer_load_dwordx4 %[l16], %[rs], %[a] offen offset:1024 sc1 lds\n\t" \
    "v_mfma_f32_16x16x32_bf16 v[0:3], v[116:119], v[36:39], v[0:3]\n\t" \
    "ds_read_b128 v[28:31], %[xp] offset:18432\n\t" \
    "v_mfma_f32_16x16x32_bf16 v[4:7], v[124:127], v[36:39], v[4:7]\n\t" \
    "ds_read_b128 v[32:35], %[xp] offset:19456\n\t" \
    "v_mfma_f32_16x16x32_bf16 v[8:11], v[116:119], v[40:43], v[8:11]\n\t" \
    "s_waitcnt lgkmcnt(4)\n\t" \
    "v_mfma_f32_16x16x32_bf16 v[12:15], v[124:127], v[40:43], v[12:15]\n\t" \
    "buffer_load_dwordx4 %[l16], %[rs], %[a] offen offset:2048 sc1 lds\n\t" \
    "v_mfma_f32_16x16x32_bf16 v[0:3], v[120:123], v[44:47], v[0:3]\n\t" \
    "ds_read_b128 v[36:39], %[xp] offset:20480\n\t" \
    "v_mfma_f32_16x16x32_bf16 v[4:7], v[128:131], v[44:47], v[4:7]\n\t" \
    "ds_read_b128 v[40:43], %[xp] offset:21504\n\t" \
    "v_mfma_f32_16x16x32_bf16 v[8:11], v[120:123], v[48:51], v[8:11]\n\t" \
    "s_waitcnt lgkmcnt(4)\n\t" \
    "v_mfma_f32_16x16x32_bf16 v[12:15], v[128:131], v[48:51], v[12:15]\n\t" \
    "buffer_load_dwordx4 %[l16], %[rs], %[a] offen offset:3072 sc1 lds\n\t" \
    "s_waitcnt vmcnt(4)\n\t" \
    "s_barrier\n\t" \
    "s_setprio 1\n\t" \
    "s_add_u32 m0, %[sd], 0\n\t" \
    "s_add_u32 %[a], %[sx], 131072\n\t" \
    "s_add_u32 %[b], %[oo], 0x1000\n\t" \
    "s_cmp_lg_u32 %[t], 1\n\t" \
    "s_cselect_b64 %[em], -1, 0\n\t" \
    "v_mfma_f32_16x16x32_bf16 v[0:3], v[132:135], v[20:23], v[0:3]\n\t" \
    "ds_read_b128 v[44:47], %[xp] offset:22528\n\t" \
    "v_mfma_f32_16x16x32_bf16 v[4:7], v[140:143], v[20:23], v[4:7]\n\t" \
    "ds_read_b128 v[48:51], %[xp] offset:23552\n\t" \
    "v_mfma_f32_16x16x32_bf16 v[8:11], v[132:135], v[24:27], v[8:11]\n\t" \
    "ds_read_b128 v[52:55], %[pa] offset:0\n\t" \
    "s_waitcnt lgkmcnt(5)\n\t" \
    "v_mfma_f32_16x16x32_bf16 v[12:15], v[140:143], v[24:27], v[12:15]\n\t" \
    "buffer_load_dwordx4 %[l16], %[rs], %[a] offen offset:0 sc1 lds\n\t" \
    "v_mfma_f32_16x16x32_bf16 v[0:3], v[136:139], v[28:31], v[0:3]\n\t" \
    "ds_read_b128 v[20:23], %[xp] offset:24576\n\t" \
    "v_mfma_f32_16x16x32_bf16 v[4:7], v[144:147], v[28:31], v[4:7]\n\t" \
    "ds_read_b128 v[24:27], %[xp] offset:25600\n\t" \
    "v_mfma_f32_16x16x32_bf16 v[8:11], v[136:139], v[32:35], v[8:11]\n\t" \
    "ds_read_b128 v[56:59], %[pa] offset:1024\n\t" \
    "s_waitcnt lgkmcnt(6)\n\t" \
    "v_mfma_f32_16x16x32_bf16 v[12:15], v[144:147], v[32:35], v[12:15]\n\t" \
    "buffer_load_dwordx4 %[l16], %[rs], %[a] offen offset:1024 sc1 lds\n\t" \
    "v_mfma_f32_16x16x32_bf16 v[0:3], v[148:151], v[36:39], v[0:3]\n\t" \
    "ds_read_b128 v[28:31], %[xp] offset:26624\n\t" \
    "v_mfma_f32_16x16x32_bf16 v[4:7], v[156:159], v[36:39], v[4:7]\n\t" \
    "ds_read_b128 v[32:35], %[xp] offset:27648\n\t" \
    "v_mfma_f32_16x16x32_bf16 v[8:11], v[148:151], v[40:43], v[8:11]\n\t" \
    "ds_read_b128 v[60:63], %[pa] offset:2048\n\t" \
    "s_waitcnt lgkmcnt(7)\n\t" \
    "v_mfma_f32_16x16x32_bf16 v[12:15], v[156:159], v[40:43], v[12:15]\n\t" \
    "buffer_load_dwordx4 %[l16], %[rs], %[a] offen offset:2048 sc1 lds\n\t" \
    "v_mfma_f32_16x16x32_bf16 v[0:3], v[152:155], v[44:47], v[0:3]\n\t" \
    "ds_read_b128 v[36:39], %[xp] offset:28672\n\t" \
    "s_waitcnt lgkmcnt(7)\n\t" \
    "v_add_f32 v196, v196, v52\n\t" \
    "v_mfma_f32_16x16x32_bf16 v[4:7], v[160:163], v[44:47], v[4:7]\n\t" \
    "ds_read_b128 v[40:43], %[xp] offset:29696\n\t" \
    "v_add_f32 v197, v197, v53\n\t" \
    "v_mfma_f32_16x16x32_bf16 v[8:11], v[152:155], v[48:51], v[8:11]\n\t" \
    "ds_read_b128 v[64:67], %[pa] offset:3072\n\t" \
    "s_waitcnt lgkmcnt(7)\n\t" \
    "v_mfma_f32_16x16x32_bf16 v[12:15], v[160:163], v[48:51], v[12:15]\n\t" \
    "buffer_load_dwordx4 %[l16], %[rs], %[a] offen offset:3072 sc1 lds\n\t" \
    "v_add_f32 v198, v198, v54\n\t" \
    "v_add_f32 v199, v199, v55\n\t" \
    "v_mfma_f32_16x16x32_bf16 v[0:3], v[164:167], v[20:23], v[0:3]\n\t" \
    "ds_read_b128 v[44:47], %[xp] offset:30720\n\t" \
    "s_waitcnt lgkmcnt(7)\n\t" \
    "v_add_f32 v200, v200, v56\n\t" \
    "v_mfma_f32_16x16x32_bf16 v[4:7], v[172:175], v[20:23], v[4:7]\n\t" \
    "ds_read_b128 v[48:51], %[xp] offset:31744\n\t" \
    "v_add_f32 v201, v201, v57\n\t" \
    "v_mfma_f32_16x16x32_bf16 v[8:11], v[164:167], v[24:27], v[8:11]\n\t" \
    "v_add_f32 v202, v202, v58\n\t" \
    "s_waitcnt lgkmcnt(6)\n\t" \
    "v_mfma_f32_16x16x32_bf16 v[12:15], v[172:175], v[24:27], v[12:15]\n\t" \
    "v_add_f32 v203, v203, v59\n\t" \
    "s_waitcnt lgkmcnt(5)\n\t" \
    "v_add_f32 v204, v204, v60\n\t" \
    "v_add_f32 v205, v205, v61\n\t" \
    "v_mfma_f32_16x16x32_bf16 v[0:3], v[168:171], v[28:31], v[0:3]\n\t" \
    "ds_read_b128 v[20:23], %[xp] offset:32768\n\t" \
    "v_add_f32 v206, v206, v62\n\t" \
    "v_mfma_f32_16x16x32_bf16 v[4:7], v[176:179], v[28:31], v[4:7]\n\t" \
    "ds_read_b128 v[24:27], %[xp] offset:33792\n\t" \
    "v_add_f32 v207, v207, v63\n\t" \
    "v_mfma_f32_16x16x32_bf16 v[8:11], v[168:171], v[32:35], v[8:11]\n\t" \
    "s_waitcnt lgkmcnt(4)\n\t" \
    "v_add_f32 v208, v208, v64\n\t" \
    "v_mfma_f32_16x16x32_bf16 v[12:15], v[176:179], v[32:35], v[12:15]\n\t" \
    "v_add_f32 v209, v209, v65\n\t" \
    "v_add_f32 v210, v210, v66\n\t" \
    "v_add_f32 v211, v211, v67\n\t" \
    "v_mfma_f32_16x16x32_bf16 v[0:3], v[180:183], v[36:39], v[0:3]\n\t" \
    "ds_read_b128 v[28:31], %[xp] offset:34816\n\t" \
    "v_cvt_pk_bf16_f32 v52, v196, v200\n\t" \
    "v_mfma_f32_16x16x32_bf16 v[4:7], v[188:191], v[36:39], v[4:7]\n\t" \
    "ds_read_b128 v[32:35], %[xp] offset:35840\n\t" \
    "v_cvt_pk_bf16_f32 v53, v197, v201\n\t" \
    "v_mfma_f32_16x16x32_bf16 v[8:11], v[180:183], v[40:43], v[8:11]\n\t" \
    "v_cvt_pk_bf16_f32 v54, v198, v202\n\t" \
    "s_waitcnt lgkmcnt(4)\n\t" \
    "v_mfma_f32_16x16x32_bf16 v[12:15], v[188:191], v[40:43], v[12:15]\n\t" \
    "v_cvt_pk_bf16_f32 v55, v199, v203\n\t" \
    "v_cvt_pk_bf16_f32 v56, v204, v208\n\t" \
    "v_cvt_pk_bf16_f32 v57, v205, v209\n\t" \
    "v_mfma_f32_16x16x32_bf16 v[0:3], v[184:187], v[44:47], v[0:3]\n\t" \
    "ds_read_b128 v[36:39], %[xp] offset:36864\n\t" \
    "v_cvt_pk_bf16_f32 v58, v206, v210\n\t" \
    "v_mfma_f32_16x16x32_bf16 v[4:7], v[192:195], v[44:47], v[4:7]\n\t" \
    "ds_read_b128 v[40:43], %[xp] offset:37888\n\t" \
    "v_cvt_pk_bf16_f32 v59, v207, v211\n\t" \
    "v_mfma_f32_16x16x32_bf16 v[8:11], v[184:187], v[48:51], v[8:11]\n\t" \
    "s_waitcnt lgkmcnt(4)\n\t" \
    "v_mfma_f32_16x16x32_bf16 v[12:15], v[192:195], v[48:51], v[12:15]\n\t" \
    "s_mov_b64 exec, %[em]\n\t" \
    "buffer_store_dwordx4 v[52:55], %[l16], %[rs], %[oo] offen sc1\n\t" \
    "buffer_store_dwordx4 v[56:59], %[l16], %[rs], %[b] offen sc1\n\t" \
    "s_mov_b64 exec, -1\n\t" \
    "s_add_u32 %[sx], %[sx], 65536\n\t" \
    "s_add_u32 %[t], %[t], 1\n\t" \
    "s_add_u32 %[oo], %[oo], 262144\n\t" \
    "s_waitcnt vmcnt(6)\n\t" \
    "s_cmp_gt_u32 %[t], " STR(SQUICK) " + 1\n\t" \
    "s_cbranch_scc1 B0_%=\n\t" \
    "s_waitcnt vmcnt(0)\n\t" \
    "B0_%=:\n\t" \
    "s_barrier\n\t" \
    "s_setprio 1\n\t" \
    "s_add_u32 m0, %[sd], 16384\n\t" \
    "s_add_u32 %[a], %[sx], 81920\n\t" \
    "v_mfma_f32_16x16x32_bf16 v[196:199], v[68:71], v[20:23], 0\n\t" \
    "ds_read_b128 v[44:47], %[xp] offset:38912\n\t" \
    "v_mfma_f32_16x16x32_bf16 v[200:203], v[76:79], v[20:23], 0\n\t" \
    "ds_read_b128 v[48:51], %[xp] offset:39936\n\t" \
    "v_mfma_f32_16x16x32_bf16 v[204:207], v[68:71], v[24:27], 0\n\t" \
    "s_waitcnt lgkmcnt(4)\n\t" \
    "v_mfma_f32_16x16x32_bf16 v[208:211], v[76:79], v[24:27], 0\n\t" \
    "v_mfma_f32_16x16x32_bf16 v[196:199], v[72:75], v[28:31], v[196:199]\n\t" \
    "ds_read_b128 v[20:23], %[xp] offset:40960\n\t" \
    "v_mfma_f32_16x16x32_bf16 v[200:203], v[80:83], v[28:31], v[200:203]\n\t" \
    "ds_read_b128 v[24:27], %[xp] offset:41984\n\t" \
    "v_mfma_f32_16x16x32_bf16 v[204:207], v[72:75], v[32:35], v[204:207]\n\t" \
    "s_waitcnt lgkmcnt(4)\n\t" \
    "v_mfma_f32_16x16x32_bf16 v[208:211], v[80:83], v[32:35], v[208:211]\n\t" \
    "v_mfma_f32_16x16x32_bf16 v[196:199], v[84:87], v[36:39], v[196:199]\n\t" \
    "ds_read_b128 v[28:31], %[xp] offset:43008\n\t" \
    "v_mfma_f32_16x16x32_bf16 v[200:203], v[92:95], v[36:39], v[200:203]\n\t" \
    "ds_read_b128 v[32:35], %[xp] offset:44032\n\t" \
    "v_mfma_f32_16x16x32_bf16 v[204:207], v[84:87], v[40:43], v[204:207]\n\t" \
    "s_waitcnt lgkmcnt(4)\n\t" \
    "v_mfma_f32_16x16x32_bf16 v[208:211], v[92:95], v[40:43], v[208:211]\n\t" \
    "v_mfma_f32_16x16x32_bf16 v[196:199], v[88:91], v[44:47], v[196:199]\n\t" \
    "ds_read_b128 v[36:39], %[xp] offset:45056\n\t" \
    "v_mfma_f32_16x16x32_bf16 v[200:203], v[96:99], v[44:47], v[200:203]\n\t" \
    "ds_read_b128 v[40:43], %[xp] offset:46080\n\t" \
    "v_mfma_f32_16x16x32_bf16 v[204:207], v[88:91], v[48:51], v[204:207]\n\t" \
    "s_waitcnt lgkmcnt(4)\n\t" \
    "v_mfma_f32_16x16x32_bf16 v[208:211], v[96:99], v[48:51], v[208:211]\n\t" \
    "v_mfma_f32_16x16x32_bf16 v[196:199], v[100:103], v[20:23], v[196:199]\n\t" \
    "ds_read_b128 v[44:47], %[xp] offset:47104\n\t" \
    "v_mfma_f32_16x16x32_bf16 v[200:203], v[108:111], v[20:23], v[200:203]\n\t" \
    "ds_read_b128 v[48:51], %[xp] offset:48128\n\t" \
    "v_mfma_f32_16x16x32_bf16 v[204:207], v[100:103], v[24:27], v[204:207]\n\t" \
    "s_waitcnt lgkmcnt(4)\n\t" \
    "v_mfma_f32_16x16x32_bf16 v[208:211], v[108:111], v[24:27], v[208:211]\n\t" \
    "buffer_load_dwordx4 %[l16], %[rs], %[a] offen offset:0 sc1 lds\n\t" \
    "v_mfma_f32_16x16x32_bf16 v[196:199], v[104:107], v[28:31], v[196:199]\n\t" \
    "ds_read_b128 v[20:23], %[xp] offset:49152\n\t" \
    "v_mfma_f32_16x16x32_bf16 v[200:203], v[112:115], v[28:31], v[200:203]\n\t" \
    "ds_read_b128 v[24:27], %[xp] offset:50176\n\t" \
    "v_mfma_f32_16x16x32_bf16 v[204:207], v[104:107], v[32:35], v[204:207]\n\t" \
    "s_waitcnt lgkmcnt(4)\n\t" \
    "v_mfma_f32_16x16x32_bf16 v[208:211], v[112:115], v[32:35], v[208:211]\n\t" \
    "buffer_load_dwordx4 %[l16], %[rs], %[a] offen offset:1024 sc1 lds\n\t" \
    "v_mfma_f32_16x16x32_bf16 v[196:199], v[116:119], v[36:39], v[196:199]\n\t" \
    "ds_read_b128 v[28:31], %[xp] offset:51200\n\t" \
    "v_mfma_f32_16x16x32_bf16 v[200:203], v[124:127], v[36:39], v[200:203]\n\t" \
    "ds_read_b128 v[32:35], %[xp] offset:52224\n\t" \
    "v_mfma_f32_16x16x32_bf16 v[204:207], v[116:119], v[40:43], v[204:207]\n\t" \
    "s_waitcnt lgkmcnt(4)\n\t" \
    "v_mfma_f32_16x16x32_bf16 v[208:211], v[124:127], v[40:43], v[208:211]\n\t" \
    "buffer_load_dwordx4 %[l16], %[rs], %[a] offen offset:2048 sc1 lds\n\t" \
    "v_mfma_f32_16x16x32_bf16 v[196:199], v[120:123], v[44:47], v[196:199]\n\t" \
    "ds_read_b128 v[36:39], %[xp] offset:53248\n\t" \
    "v_mfma_f32_16x16x32_bf16 v[200:203], v[128:131], v[44:47], v[200:203]\n\t" \
    "ds_read_b128 v[40:43], %[xp] offset:54272\n\t" \
    "v_mfma_f32_16x16x32_bf16 v[204:207], v[120:123], v[48:51], v[204:207]\n\t" \
    "s_waitcnt lgkmcnt(4)\n\t" \
    "v_mfma_f32_16x16x32_bf16 v[208:211], v[128:131], v[48:51], v[208:211]\n\t" \
    "buffer_load_dwordx4 %[l16], %[rs], %[a] offen offset:3072 sc1 lds\n\t" \
    "s_waitcnt vmcnt(4)\n\t" \
    "s_barrier\n\t" \
    "s_setprio 1\n\t" \
    "s_add_u32 m0, %[sd], 32768\n\t" \
    "s_add_u32 %[a], %[sx], 131072\n\t" \
    "s_add_u32 %[b], %[oo], 0x1000\n\t" \
    "s_cmp_lg_u32 %[t], 1\n\t" \
    "s_cselect_b64 %[em], -1, 0\n\t" \
    "v_mfma_f32_16x16x32_bf16 v[196:199], v[132:135], v[20:23], v[196:199]\n\t" \
    "ds_read_b128 v[44:47], %[xp] offset:55296\n\t" \
    "v_mfma_f32_16x16x32_bf16 v[200:203], v[140:143], v[20:23], v[200:203]\n\t" \
    "ds_read_b128 v[48:51], %[xp] offset:56320\n\t" \
    "v_mfma_f32_16x16x32_bf16 v[204:207], v[132:135], v[24:27], v[204:207]\n\t" \
    "ds_read_b128 v[52:55], %[pa] offset:0\n\t" \
    "s_waitcnt lgkmcnt(5)\n\t" \
    "v_mfma_f32_16x16x32_bf16 v[208:211], v[140:143], v[24:27], v[208:211]\n\t" \
    "buffer_load_dwordx4 %[l16], %[rs], %[a] offen offset:0 sc1 lds\n\t" \
    "v_mfma_f32_16x16x32_bf16 v[196:199], v[136:139], v[28:31], v[196:199]\n\t" \
    "ds_read_b128 v[20:23], %[xp] offset:57344\n\t" \
    "v_mfma_f32_16x16x32_bf16 v[200:203], v[144:147], v[28:31], v[200:203]\n\t" \
    "ds_read_b128 v[24:27], %[xp] offset:58368\n\t" \
    "v_mfma_f32_16x16x32_bf16 v[204:207], v[136:139], v[32:35], v[204:207]\n\t" \
    "ds_read_b128 v[56:59], %[pa] offset:1024\n\t" \
    "s_waitcnt lgkmcnt(6)\n\t" \
    "v_mfma_f32_16x16x32_bf16 v[208:211], v[144:147], v[32:35], v[208:211]\n\t" \
    "buffer_load_dwordx4 %[l16], %[rs], %[a] offen offset:1024 sc1 lds\n\t" \
    "v_mfma_f32_16x16x32_bf16 v[196:199], v[148:151], v[36:39], v[196:199]\n\t" \
    "ds_read_b128 v[28:31], %[xp] offset:59392\n\t" \
    "v_mfma_f32_16x16x32_bf16 v[200:203], v[156:159], v[36:39], v[200:203]\n\t" \
    "ds_read_b128 v[32:35], %[xp] offset:60416\n\t" \
    "v_mfma_f32_16x16x32_bf16 v[204:207], v[148:151], v[40:43], v[204:207]\n\t" \
    "ds_read_b128 v[60:63], %[pa] offset:2048\n\t" \
    "s_waitcnt lgkmcnt(7)\n\t" \
    "v_mfma_f32_16x16x32_bf16 v[208:211], v[156:159], v[40:43], v[208:211]\n\t" \
    "buffer_load_dwordx4 %[l16], %[rs], %[a] offen offset:2048 sc1 lds\n\t" \
    "v_mfma_f32_16x16x32_bf16 v[196:199], v[152:155], v[44:47], v[196:199]\n\t" \
    "ds_read_b128 v[36:39], %[xp] offset:61440\n\t" \
    "s_waitcnt lgkmcnt(7)\n\t" \
    "v_add_f32 v0, v0, v52\n\t" \
    "v_mfma_f32_16x16x32_bf16 v[200:203], v[160:163], v[44:47], v[200:203]\n\t" \
    "ds_read_b128 v[40:43], %[xp] offset:62464\n\t" \
    "v_add_f32 v1, v1, v53\n\t" \
    "v_mfma_f32_16x16x32_bf16 v[204:207], v[152:155], v[48:51], v[204:207]\n\t" \
    "ds_read_b128 v[64:67], %[pa] offset:3072\n\t" \
    "s_waitcnt lgkmcnt(7)\n\t" \
    "v_mfma_f32_16x16x32_bf16 v[208:211], v[160:163], v[48:51], v[208:211]\n\t" \
    "buffer_load_dwordx4 %[l16], %[rs], %[a] offen offset:3072 sc1 lds\n\t" \
    "v_add_f32 v2, v2, v54\n\t" \
    "v_add_f32 v3, v3, v55\n\t" \
    "v_mfma_f32_16x16x32_bf16 v[196:199], v[164:167], v[20:23], v[196:199]\n\t" \
    "ds_read_b128 v[44:47], %[xp] offset:63488\n\t" \
    "s_waitcnt lgkmcnt(7)\n\t" \
    "v_add_f32 v4, v4, v56\n\t" \
    "v_mfma_f32_16x16x32_bf16 v[200:203], v[172:175], v[20:23], v[200:203]\n\t" \
    "ds_read_b128 v[48:51], %[xp] offset:64512\n\t" \
    "v_add_f32 v5, v5, v57\n\t" \
    "v_mfma_f32_16x16x32_bf16 v[204:207], v[164:167], v[24:27], v[204:207]\n\t" \
    "v_add_f32 v6, v6, v58\n\t" \
    "s_waitcnt lgkmcnt(6)\n\t" \
    "v_mfma_f32_16x16x32_bf16 v[208:211], v[172:175], v[24:27], v[208:211]\n\t" \
    "v_add_f32 v7, v7, v59\n\t" \
    "s_waitcnt lgkmcnt(5)\n\t" \
    "v_add_f32 v8, v8, v60\n\t" \
    "v_add_f32 v9, v9, v61\n\t" \
    "v_mfma_f32_16x16x32_bf16 v[196:199], v[168:171], v[28:31], v[196:199]\n\t" \
    "ds_read_b128 v[20:23], %[xp] offset:0\n\t" \
    "v_add_f32 v10, v10, v62\n\t" \
    "v_mfma_f32_16x16x32_bf16 v[200:203], v[176:179], v[28:31], v[200:203]\n\t" \
    "ds_read_b128 v[24:27], %[xp] offset:1024\n\t" \
    "v_add_f32 v11, v11, v63\n\t" \
    "v_mfma_f32_16x16x32_bf16 v[204:207], v[168:171], v[32:35], v[204:207]\n\t" \
    "s_waitcnt lgkmcnt(4)\n\t" \
    "v_add_f32 v12, v12, v64\n\t" \
    "v_mfma_f32_16x16x32_bf16 v[208:211], v[176:179], v[32:35], v[208:211]\n\t" \
    "v_add_f32 v13, v13, v65\n\t" \
    "v_add_f32 v14, v14, v66\n\t" \
    "v_add_f32 v15, v15, v67\n\t" \
    "v_mfma_f32_16x16x32_bf16 v[196:199], v[180:183], v[36:39], v[196:199]\n\t" \
    "ds_read_b128 v[28:31], %[xp] offset:2048\n\t" \
    "v_cvt_pk_bf16_f32 v52, v0, v4\n\t" \
    "v_mfma_f32_16x16x32_bf16 v[200:203], v[188:191], v[36:39], v[200:203]\n\t" \
    "ds_read_b128 v[32:35], %[xp] offset:3072\n\t" \
    "v_cvt_pk_bf16_f32 v53, v1, v5\n\t" \
    "v_mfma_f32_16x16x32_bf16 v[204:207], v[180:183], v[40:43], v[204:207]\n\t" \
    "v_cvt_pk_bf16_f32 v54, v2, v6\n\t" \
    "s_waitcnt lgkmcnt(4)\n\t" \
    "v_mfma_f32_16x16x32_bf16 v[208:211], v[188:191], v[40:43], v[208:211]\n\t" \
    "v_cvt_pk_bf16_f32 v55, v3, v7\n\t" \
    "v_cvt_pk_bf16_f32 v56, v8, v12\n\t" \
    "v_cvt_pk_bf16_f32 v57, v9, v13\n\t" \
    "v_mfma_f32_16x16x32_bf16 v[196:199], v[184:187], v[44:47], v[196:199]\n\t" \
    "ds_read_b128 v[36:39], %[xp] offset:4096\n\t" \
    "v_cvt_pk_bf16_f32 v58, v10, v14\n\t" \
    "v_mfma_f32_16x16x32_bf16 v[200:203], v[192:195], v[44:47], v[200:203]\n\t" \
    "ds_read_b128 v[40:43], %[xp] offset:5120\n\t" \
    "v_cvt_pk_bf16_f32 v59, v11, v15\n\t" \
    "v_mfma_f32_16x16x32_bf16 v[204:207], v[184:187], v[48:51], v[204:207]\n\t" \
    "s_waitcnt lgkmcnt(4)\n\t" \
    "v_mfma_f32_16x16x32_bf16 v[208:211], v[192:195], v[48:51], v[208:211]\n\t" \
    "s_mov_b64 exec, %[em]\n\t" \
    "buffer_store_dwordx4 v[52:55], %[l16], %[rs], %[oo] offen sc1\n\t" \
    "buffer_store_dwordx4 v[56:59], %[l16], %[rs], %[b] offen sc1\n\t" \
    "s_mov_b64 exec, -1\n\t" \
    "s_add_u32 %[sx], %[sx], 65536\n\t" \
    "s_add_u32 %[t], %[t], 1\n\t" \
    "s_add_u32 %[oo], %[oo], 262144\n\t" \
    "s_waitcnt vmcnt(6)\n\t" \
    "s_cmp_gt_u32 %[t], " STR(SQUICK) " + 1\n\t" \
    "s_cbranch_scc1 B1_%=\n\t" \
    "s_waitcnt vmcnt(0)\n\t" \
    "B1_%=:\n\t" \
    "s_barrier\n\t" \
    "s_cmp_lt_u32 %[t], %[te]\n\t" \
    "s_cbranch_scc1 L1_%=\n\t" \
    "s_cmp_eq_u32 %[fin], %[te]\n\t" \
    "s_cbranch_scc0 E_%=\n\t" \
    "s_nop 4\n\t" \
    "s_barrier\n\t" \
    "ds_read_b128 v[52:55], %[pa] offset:0\n\t" \
    "ds_read_b128 v[56:59], %[pa] offset:1024\n\t" \
    "ds_read_b128 v[60:63], %[pa] offset:2048\n\t" \
    "ds_read_b128 v[64:67], %[pa] offset:3072\n\t" \
    "s_add_u32 %[b], %[oo], 0x1000\n\t" \
    "s_waitcnt lgkmcnt(0)\n\t" \
    "v_add_f32 v196, v196, v52\n\t" \
    "v_add_f32 v197, v197, v53\n\t" \
    "v_add_f32 v198, v198, v54\n\t" \
    "v_add_f32 v199, v199, v55\n\t" \
    "v_add_f32 v200, v200, v56\n\t" \
    "v_add_f32 v201, v201, v57\n\t" \
    "v_add_f32 v202, v202, v58\n\t" \
    "v_add_f32 v203, v203, v59\n\t" \
    "v_add_f32 v204, v204, v60\n\t" \
    "v_add_f32 v205, v205, v61\n\t" \
    "v_add_f32 v206, v206, v62\n\t" \
    "v_add_f32 v207, v207, v63\n\t" \
    "v_add_f32 v208, v208, v64\n\t" \
    "v_add_f32 v209, v209, v65\n\t" \
    "v_add_f32 v210, v210, v66\n\t" \
    "v_add_f32 v211, v211, v67\n\t" \
    "v_cvt_pk_bf16_f32 v52, v196, v200\n\t" \
    "v_cvt_pk_bf16_f32 v53, v197, v201\n\t" \
    "v_cvt_pk_bf16_f32 v54, v198, v202\n\t" \
    "v_cvt_pk_bf16_f32 v55, v199, v203\n\t" \
    "v_cvt_pk_bf16_f32 v56, v204, v208\n\t" \
    "v_cvt_pk_bf16_f32 v57, v205, v209\n\t" \
    "v_cvt_pk_bf16_f32 v58, v206, v210\n\t" \
    "v_cvt_pk_bf16_f32 v59, v207, v211\n\t" \
    "buffer_store_dwordx4 v[52:55], %[l16], %[rs], %[oo] offen sc1\n\t" \
    "buffer_store_dwordx4 v[56:59], %[l16], %[rs], %[b] offen sc1\n\t" \
    "E_%=:\n\t" \
    "s_setprio 0\n\t" \
    "s_waitcnt lgkmcnt(0)\n\t" \
    "s_nop 7\n\t" \
    : [t] "+s"(t), [sx] "+s"(sx), [oo] "+s"(oo), [a] "=&s"(tmp), [b] "=&s"(tmp2), [em] "=&s"(em), "+{v[20:27]}"(xf[0]), "+{v[28:35]}"(xf[1]), "+{v[36:43]}"(xf[2]), "+{v[196:203]}"(ac[0]), "+{v[204:211]}"(ac[1]) \
    : [te] "s"(TE_), [fin] "s"(FIN_), [kh] "s"(kh), [sd] "s"(sd), [rs] "s"(rs), [l16] "v"(l16), [xp] "v"(xp), [pa] "v"(pa), [fm] "s"(fm), [pm] "s"(pm), [fo] "s"(fo), [po] "s"(po), [pd] "s"(pd), [tag] "s"(P.tag), "{v[68:75]}"(w[0][0]), "{v[76:83]}"(w[0][1]), "{v[84:91]}"(w[1][0]), "{v[92:99]}"(w[1][1]), "{v[100:107]}"(w[2][0]), "{v[108:115]}"(w[2][1]), "{v[116:123]}"(w[3][0]), "{v[124:131]}"(w[3][1]), "{v[132:139]}"(w[4][0]), "{v[140:147]}"(w[4][1]), "{v[148:155]}"(w[5][0]), "{v[156:163]}"(w[5][1]), "{v[164:171]}"(w[6][0]), "{v[172:179]}"(w[6][1]), "{v[180:187]}"(w[7][0]), "{v[188:195]}"(w[7][1]) \
    : "memory", "scc", "v0", "v1", "v2", "v3", "v4", "v5", "v6", "v7", "v8", "v9", "v10", "v11", "v12", "v13", "v14", "v15", "v44", "v45", "v46", "v47", "v48", "v49", "v50", "v51", "v52", "v53", "v54", "v55", "v56", "v57", "v58", "v59", "v60", "v61", "v62", "v63", "v64", "v65", "v66", "v67")

#define S_LOOP_V(TE_, FIN_) \
  asm volatile( \
    "s_cmp_eq_u32 %[kh], 0\n\t" \
    "s_cbranch_scc0 L1_%=\n\t" \
    "L0_%=:\n\t" \
    "s_setprio 0\n\t" \
    "s_nop 3\n\t" \
    "ds_write_b128 %[pa], v[196:199] offset:0\n\t" \
    "ds_write_b128 %[pa], v[200:203] offset:1024\n\t" \
    "ds_write_b128 %[pa], v[204:207] offset:2048\n\t" \
    "ds_write_b128 %[pa], v[208:211] offset:3072\n\t" \
    "s_sub_u32 %[a], %[t], 1\n\t" \
    "v_mov_b32 v53, %[tag]\n\t" \
    "v_mov_b32 v52, %[a]\n\t" \
    "s_sub_u32 %[b], %[t], 2\n\t" \
    "s_cmp_lt_u32 %[b], " STR(SQUICK) "\n\t" \
    "s_cselect_b64 exec, %[fm], 0\n\t" \
    "buffer_store_dwordx2 v[52:53], %[l16], %[rs], %[fo] offen sc1\n\t" \
    "s_mov_b64 exec, -1\n\t" \
    "s_add_u32 m0, %[sd], 49152\n\t" \
    "s_add_u32 %[a], %[sx], 81920\n\t" \
    "s_waitcnt lgkmcnt(0)\n\t" \
    "buffer_load_dwordx4 %[l16], %[rs], %[a] offen offset:0 sc1 lds\n\t" \
    "buffer_load_dwordx4 %[l16], %[rs], %[a] offen offset:1024 sc1 lds\n\t" \
    "buffer_load_dwordx4 %[l16], %[rs], %[a] offen offset:2048 sc1 lds\n\t" \
    "buffer_load_dwordx4 %[l16], %[rs], %[a] offen offset:3072 sc1 lds\n\t" \
    "v_mfma_f32_16x16x32_bf16 v[0:3], v[20:23], v[68:71], 0\n\t" \
    "ds_read_b128 v[44:47], %[xp] offset:6144\n\t" \
    "v_mfma_f32_16x16x32_bf16 v[4:7], v[20:23], v[76:79], 0\n\t" \
    "ds_read_b128 v[48:51], %[xp] offset:7168\n\t" \
    "v_mfma_f32_16x16x32_bf16 v[8:11], v[24:27], v[68:71], 0\n\t" \
    "v_mfma_f32_16x16x32_bf16 v[12:15], v[24:27], v[76:79], 0\n\t" \
    "v_mfma_f32_16x16x32_bf16 v[0:3], v[28:31], v[72:75], v[0:3]\n\t" \
    "ds_read_b128 v[20:23], %[xp] offset:8192\n\t" \
    "v_mfma_f32_16x16x32_bf16 v[4:7], v[28:31], v[80:83], v[4:7]\n\t" \
    "ds_read_b128 v[24:27], %[xp] offset:9216\n\t" \
    "v_mfma_f32_16x16x32_bf16 v[8:11], v[32:35], v[72:75], v[8:11]\n\t" \
    "v_mfma_f32_16x16x32_bf16 v[12:15], v[32:35], v[80:83], v[12:15]\n\t" \
    "v_mfma_f32_16x16x32_bf16 v[0:3], v[36:39], v[84:87], v[0:3]\n\t" \
    "ds_read_b128 v[28:31], %[xp] offset:10240\n\t" \
    "v_mfma_f32_16x16x32_bf16 v[4:7], v[36:39], v[92:95], v[4:7]\n\t" \
    "ds_read_b128 v[32:35], %[xp] offset:11264\n\t" \
    "v_mfma_f32_16x16x32_bf16 v[8:11], v[40:43], v[84:87], v[8:11]\n\t" \
    "s_waitcnt lgkmcnt(4)\n\t" \
    "v_mfma_f32_16x16x32_bf16 v[12:15], v[40:43], v[92:95], v[12:15]\n\t" \
    "v_mfma_f32_16x16x32_bf16 v[0:3], v[44:47], v[88:91], v[0:3]\n\t" \
    "ds_read_b128 v[36:39], %[xp] offset:12288\n\t" \
    "v_mfma_f32_16x16x32_bf16 v[4:7], v[44:47], v[96:99], v[4:7]\n\t" \
    "ds_read_b128 v[40:43], %[xp] offset:13312\n\t" \
    "v_mfma_f32_16x16x32_bf16 v[8:11], v[48:51], v[88:91], v[8:11]\n\t" \
    "s_waitcnt lgkmcnt(4)\n\t" \
    "v_mfma_f32_16x16x32_bf16 v[12:15], v[48:51], v[96:99], v[12:15]\n\t" \
    "v_mfma_f32_16x16x32_bf16 v[0:3], v[20:23], v[100:103], v[0:3]\n\t" \
    "ds_read_b128 v[44:47], %[xp] offset:14336\n\t" \
    "v_mfma_f32_16x16x32_bf16 v[4:7], v[20:23], v[108:111], v[4:7]\n\t" \
    "ds_read_b128 v[48:51], %[xp] offset:15360\n\t" \
    "v_mfma_f32_16x16x32_bf16 v[8:11], v[24:27], v[100:103], v[8:11]\n\t" \
    "s_waitcnt lgkmcnt(4)\n\t" \
    "v_mfma_f32_16x16x32_bf16 v[12:15], v[24:27], v[108:111], v[12:15]\n\t" \
    "v_mfma_f32_16x16x32_bf16 v[0:3], v[28:31], v[104:107], v[0:3]\n\t" \
    "ds_read_b128 v[20:23], %[xp] offset:16384\n\t" \
    "v_mfma_f32_16x16x32_bf16 v[4:7], v[28:31], v[112:115], v[4:7]\n\t" \
    "ds_read_b128 v[24:27], %[xp] offset:17408\n\t" \
    "v_mfma_f32_16x16x32_bf16 v[8:11], v[32:35], v[104:107], v[8:11]\n\t" \
    "s_waitcnt lgkmcnt(4)\n\t" \
    "v_mfma_f32_16x16x32_bf16 v[12:15], v[32:35], v[112:115], v[12:15]\n\t" \
    "v_mfma_f32_16x16x32_bf16 v[0:3], v[36:39], v[116:119], v[0:3]\n\t" \
    "ds_read_b128 v[28:31], %[xp] offset:18432\n\t" \
    "v_mfma_f32_16x16x32_bf16 v[4:7], v[36:39], v[124:127], v[4:7]\n\t" \
    "ds_read_b128 v[32:35], %[xp] offset:19456\n\t" \
    "v_mfma_f32_16x16x32_bf16 v[8:11], v[40:43], v[116:119], v[8:11]\n\t" \
    "s_waitcnt lgkmcnt(4)\n\t" \
    "v_mfma_f32_16x16x32_bf16 v[12:15], v[40:43], v[124:127], v[12:15]\n\t" \
    "v_mfma_f32_16x16x32_bf16 v[0:3], v[44:47], v[120:123], v[0:3]\n\t" \
    "ds_read_b128 v[36:39], %[xp] offset:20480\n\t" \
    "v_mfma_f32_16x16x32_bf16 v[4:7], v[44:47], v[128:131], v[4:7]\n\t" \
    "ds_read_b128 v[40:43], %[xp] offset:21504\n\t" \
    "v_mfma_f32_16x16x32_bf16 v[8:11], v[48:51], v[120:123], v[8:11]\n\t" \
    "s_waitcnt lgkmcnt(4)\n\t" \
    "v_mfma_f32_16x16x32_bf16 v[12:15], v[48:51], v[128:131], v[12:15]\n\t" \
    "s_waitcnt vmcnt(4)\n\t" \
    "s_barrier\n\t" \
    "s_setprio 0\n\t" \
    "s_sub_u32 %[a], %[t], 1\n\t" \
    "v_mov_b32 v53, %[tag]\n\t" \
    "v_mov_b32 v52, %[a]\n\t" \
    "s_cmp_gt_u32 %[t], 1\n\t" \
    "s_cselect_b64 exec, %[fm], 0\n\t" \
    "buffer_store_dwordx2 v[52:53], %[l16], %[rs], %[fo] offen sc1\n\t" \
    "s_mov_b64 exec, %[pm]\n\t" \
    "s_mov_b32 m0, %[pd]\n\t" \
    "s_add_u32 %[a], %[sx], %[po]\n\t" \
    "buffer_load_dwordx4 %[l16], %[rs], %[a] offen sc1 lds\n\t" \
    "s_mov_b64 exec, -1\n\t" \
    "s_add_u32 m0, %[sd], 0\n\t" \
    "s_add_u32 %[a], %[sx], 131072\n\t" \
    "buffer_load_dwordx4 %[l16], %[rs], %[a] offen offset:0 sc1 lds\n\t" \
    "buffer_load_dwordx4 %[l16], %[rs], %[a] offen offset:1024 sc1 lds\n\t" \
    "buffer_load_dwordx4 %[l16], %[rs], %[a] offen offset:2048 sc1 lds\n\t" \
    "buffer_load_dwordx4 %[l16], %[rs], %[a] offen offset:3072 sc1 lds\n\t" \
    "v_mfma_f32_16x16x32_bf16 v[0:3], v[20:23], v[132:135], v[0:3]\n\t" \
    "ds_read_b128 v[44:47], %[xp] offset:22528\n\t" \
    "v_mfma_f32_16x16x32_bf16 v[4:7], v[20:23], v[140:143], v[4:7]\n\t" \
    "ds_read_b128 v[48:51], %[xp] offset:23552\n\t" \
    "v_mfma_f32_16x16x32_bf16 v[8:11], v[24:27], v[132:135], v[8:11]\n\t" \
    "s_waitcnt lgkmcnt(4)\n\t" \
    "v_mfma_f32_16x16x32_bf16 v[12:15], v[24:27], v[140:143], v[12:15]\n\t" \
    "v_mfma_f32_16x16x32_bf16 v[0:3], v[28:31], v[136:139], v[0:3]\n\t" \
    "ds_read_b128 v[20:23], %[xp] offset:24576\n\t" \
    "v_mfma_f32_16x16x32_bf16 v[4:7], v[28:31], v[144:147], v[4:7]\n\t" \
    "ds_read_b128 v[24:27], %[xp] offset:25600\n\t" \
    "v_mfma_f32_16x16x32_bf16 v[8:11], v[32:35], v[136:139], v[8:11]\n\t" \
    "s_waitcnt lgkmcnt(4)\n\t" \
    "v_mfma_f32_16x16x32_bf16 v[12:15], v[32:35], v[144:147], v[12:15]\n\t" \
    "v_mfma_f32_16x16x32_bf16 v[0:3], v[36:39], v[148:151], v[0:3]\n\t" \
    "ds_read_b128 v[28:31], %[xp] offset:26624\n\t" \
    "v_mfma_f32_16x16x32_bf16 v[4:7], v[36:39], v[156:159], v[4:7]\n\t" \
    "ds_read_b128 v[32:35], %[xp] offset:27648\n\t" \
    "v_mfma_f32_16x16x32_bf16 v[8:11], v[40:43], v[148:151], v[8:11]\n\t" \
    "s_waitcnt lgkmcnt(4)\n\t" \
    "v_mfma_f32_16x16x32_bf16 v[12:15], v[40:43], v[156:159], v[12:15]\n\t" \
    "v_mfma_f32_16x16x32_bf16 v[0:3], v[44:47], v[152:155], v[0:3]\n\t" \
    "ds_read_b128 v[36:39], %[xp] offset:28672\n\t" \
    "v_mfma_f32_16x16x32_bf16 v[4:7], v[44:47], v[160:163], v[4:7]\n\t" \
    "ds_read_b128 v[40:43], %[xp] offset:29696\n\t" \
    "v_mfma_f32_16x16x32_bf16 v[8:11], v[48:51], v[152:155], v[8:11]\n\t" \
    "s_waitcnt lgkmcnt(4)\n\t" \
    "v_mfma_f32_16x16x32_bf16 v[12:15], v[48:51], v[160:163], v[12:15]\n\t" \
    "v_mfma_f32_16x16x32_bf16 v[0:3], v[20:23], v[164:167], v[0:3]\n\t" \
    "ds_read_b128 v[44:47], %[xp] offset:30720\n\t" \
    "v_mfma_f32_16x16x32_bf16 v[4:7], v[20:23], v[172:175], v[4:7]\n\t" \
    "ds_read_b128 v[48:51], %[xp] offset:31744\n\t" \
    "v_mfma_f32_16x16x32_bf16 v[8:11], v[24:27], v[164:167], v[8:11]\n\t" \
    "s_waitcnt lgkmcnt(4)\n\t" \
    "v_mfma_f32_16x16x32_bf16 v[12:15], v[24:27], v[172:175], v[12:15]\n\t" \
    "v_mfma_f32_16x16x32_bf16 v[0:3], v[28:31], v[168:171], v[0:3]\n\t" \
    "ds_read_b128 v[20:23], %[xp] offset:32768\n\t" \
    "v_mfma_f32_16x16x32_bf16 v[4:7], v[28:31], v[176:179], v[4:7]\n\t" \
    "ds_read_b128 v[24:27], %[xp] offset:33792\n\t" \
    "v_mfma_f32_16x16x32_bf16 v[8:11], v[32:35], v[168:171], v[8:11]\n\t" \
    "s_waitcnt lgkmcnt(4)\n\t" \
    "v_mfma_f32_16x16x32_bf16 v[12:15], v[32:35], v[176:179], v[12:15]\n\t" \
    "v_mfma_f32_16x16x32_bf16 v[0:3], v[36:39], v[180:183], v[0:3]\n\t" \
    "ds_read_b128 v[28:31], %[xp] offset:34816\n\t" \
    "v_mfma_f32_16x16x32_bf16 v[4:7], v[36:39], v[188:191], v[4:7]\n\t" \
    "ds_read_b128 v[32:35], %[xp] offset:35840\n\t" \
    "v_mfma_f32_16x16x32_bf16 v[8:11], v[40:43], v[180:183], v[8:11]\n\t" \
    "s_waitcnt lgkmcnt(4)\n\t" \
    "v_mfma_f32_16x16x32_bf16 v[12:15], v[40:43], v[188:191], v[12:15]\n\t" \
    "v_mfma_f32_16x16x32_bf16 v[0:3], v[44:47], v[184:187], v[0:3]\n\t" \
    "ds_read_b128 v[36:39], %[xp] offset:36864\n\t" \
    "v_mfma_f32_16x16x32_bf16 v[4:7], v[44:47], v[192:195], v[4:7]\n\t" \
    "ds_read_b128 v[40:43], %[xp] offset:37888\n\t" \
    "v_mfma_f32_16x16x32_bf16 v[8:11], v[48:51], v[184:187], v[8:11]\n\t" \
    "s_waitcnt lgkmcnt(4)\n\t" \
    "v_mfma_f32_16x16x32_bf16 v[12:15], v[48:51], v[192:195], v[12:15]\n\t" \
    "s_add_u32 %[sx], %[sx], 65536\n\t" \
    "s_add_u32 %[t], %[t], 1\n\t" \
    "s_waitcnt vmcnt(4)\n\t" \
    "s_barrier\n\t" \
    "s_setprio 0\n\t" \
    "s_nop 3\n\t" \
    "ds_write_b128 %[pa], v[0:3] offset:0\n\t" \
    "ds_write_b128 %[pa], v[4:7] offset:1024\n\t" \
    "ds_write_b128 %[pa], v[8:11] offset:2048\n\t" \
    "ds_write_b128 %[pa], v[12:15] offset:3072\n\t" \
    "s_sub_u32 %[a], %[t], 1\n\t" \
    "v_mov_b32 v53, %[tag]\n\t" \
    "v_mov_b32 v52, %[a]\n\t" \
    "s_sub_u32 %[b], %[t], 2\n\t" \
    "s_cmp_lt_u32 %[b], " STR(SQUICK) "\n\t" \
    "s_cselect_b64 exec, %[fm], 0\n\t" \
    "buffer_store_dwordx2 v[52:53], %[l16], %[rs], %[fo] offen sc1\n\t" \
    "s_mov_b64 exec, -1\n\t" \
    "s_add_u32 m0, %[sd], 16384\n\t" \
    "s_add_u32 %[a], %[sx], 81920\n\t" \
    "s_waitcnt lgkmcnt(0)\n\t" \
    "buffer_load_dwordx4 %[l16], %[rs], %[a] offen offset:0 sc1 lds\n\t" \
    "buffer_load_dwordx4 %[l16], %[rs], %[a] offen offset:1024 sc1 lds\n\t" \
    "buffer_load_dwordx4 %[l16], %[rs], %[a] offen offset:2048 sc1 lds\n\t" \
    "buffer_load_dwordx4 %[l16], %[rs], %[a] offen offset:3072 sc1 lds\n\t" \
    "v_mfma_f32_16x16x32_bf16 v[196:199], v[20:23], v[68:71], 0\n\t" \
    "ds_read_b128 v[44:47], %[xp] offset:38912\n\t" \
    "v_mfma_f32_16x16x32_bf16 v[200:203], v[20:23], v[76:79], 0\n\t" \
    "ds_read_b128 v[48:51], %[xp] offset:39936\n\t" \
    "v_mfma_f32_16x16x32_bf16 v[204:207], v[24:27], v[68:71], 0\n\t" \
    "v_mfma_f32_16x16x32_bf16 v[208:211], v[24:27], v[76:79], 0\n\t" \
    "v_mfma_f32_16x16x32_bf16 v[196:199], v[28:31], v[72:75], v[196:199]\n\t" \
    "ds_read_b128 v[20:23], %[xp] offset:40960\n\t" \
    "v_mfma_f32_16x16x32_bf16 v[200:203], v[28:31], v[80:83], v[200:203]\n\t" \
    "ds_read_b128 v[24:27], %[xp] offset:41984\n\t" \
    "v_mfma_f32_16x16x32_bf16 v[204:207], v[32:35], v[72:75], v[204:207]\n\t" \
    "v_mfma_f32_16x16x32_bf16 v[208:211], v[32:35], v[80:83], v[208:211]\n\t" \
    "v_mfma_f32_16x16x32_bf16 v[196:199], v[36:39], v[84:87], v[196:199]\n\t" \
    "ds_read_b128 v[28:31], %[xp] offset:43008\n\t" \
    "v_mfma_f32_16x16x32_bf16 v[200:203], v[36:39], v[92:95], v[200:203]\n\t" \
    "ds_read_b128 v[32:35], %[xp] offset:44032\n\t" \
    "v_mfma_f32_16x16x32_bf16 v[204:207], v[40:43], v[84:87], v[204:207]\n\t" \
    "s_waitcnt lgkmcnt(4)\n\t" \
    "v_mfma_f32_16x16x32_bf16 v[208:211], v[40:43], v[92:95], v[208:211]\n\t" \
    "v_mfma_f32_16x16x32_bf16 v[196:199], v[44:47], v[88:91], v[196:199]\n\t" \
    "ds_read_b128 v[36:39], %[xp] offset:45056\n\t" \
    "v_mfma_f32_16x16x32_bf16 v[200:203], v[44:47], v[96:99], v[200:203]\n\t" \
    "ds_read_b128 v[40:43], %[xp] offset:46080\n\t" \
    "v_mfma_f32_16x16x32_bf16 v[204:207], v[48:51], v[88:91], v[204:207]\n\t" \
    "s_waitcnt lgkmcnt(4)\n\t" \
    "v_mfma_f32_16x16x32_bf16 v[208:211], v[48:51], v[96:99], v[208:211]\n\t" \
    "v_mfma_f32_16x16x32_bf16 v[196:199], v[20:23], v[100:103], v[196:199]\n\t" \
    "ds_read_b128 v[44:47], %[xp] offset:47104\n\t" \
    "v_mfma_f32_16x16x32_bf16 v[200:203], v[20:23], v[108:111], v[200:203]\n\t" \
    "ds_read_b128 v[48:51], %[xp] offset:48128\n\t" \
    "v_mfma_f32_16x16x32_bf16 v[204:207], v[24:27], v[100:103], v[204:207]\n\t" \
    "s_waitcnt lgkmcnt(4)\n\t" \
    "v_mfma_f32_16x16x32_bf16 v[208:211], v[24:27], v[108:111], v[208:211]\n\t" \
    "v_mfma_f32_16x16x32_bf16 v[196:199], v[28:31], v[104:107], v[196:199]\n\t" \
    "ds_read_b128 v[20:23], %[xp] offset:49152\n\t" \
    "v_mfma_f32_16x16x32_bf16 v[200:203], v[28:31], v[112:115], v[200:203]\n\t" \
    "ds_read_b128 v[24:27], %[xp] offset:50176\n\t" \
    "v_mfma_f32_16x16x32_bf16 v[204:207], v[32:35], v[104:107], v[204:207]\n\t" \
    "s_waitcnt lgkmcnt(4)\n\t" \
    "v_mfma_f32_16x16x32_bf16 v[208:211], v[32:35], v[112:115], v[208:211]\n\t" \
    "v_mfma_f32_16x16x32_bf16 v[196:199], v[36:39], v[116:119], v[196:199]\n\t" \
    "ds_read_b128 v[28:31], %[xp] offset:51200\n\t" \
    "v_mfma_f32_16x16x32_bf16 v[200:203], v[36:39], v[124:127], v[200:203]\n\t" \
    "ds_read_b128 v[32:35], %[xp] offset:52224\n\t" \
    "v_mfma_f32_16x16x32_bf16 v[204:207], v[40:43], v[116:119], v[204:207]\n\t" \
    "s_waitcnt lgkmcnt(4)\n\t" \
    "v_mfma_f32_16x16x32_bf16 v[208:211], v[40:43], v[124:127], v[208:211]\n\t" \
    "v_mfma_f32_16x16x32_bf16 v[196:199], v[44:47], v[120:123], v[196:199]\n\t" \
    "ds_read_b128 v[36:39], %[xp] offset:53248\n\t" \
    "v_mfma_f32_16x16x32_bf16 v[200:203], v[44:47], v[128:131], v[200:203]\n\t" \
    "ds_read_b128 v[40:43], %[xp] offset:54272\n\t" \
    "v_mfma_f32_16x16x32_bf16 v[204:207], v[48:51], v[120:123], v[204:207]\n\t" \
    "s_waitcnt lgkmcnt(4)\n\t" \
    "v_mfma_f32_16x16x32_bf16 v[208:211], v[48:51], v[128:131], v[208:211]\n\t" \
    "s_waitcnt vmcnt(4)\n\t" \
    "s_barrier\n\t" \
    "s_setprio 0\n\t" \
    "s_sub_u32 %[a], %[t], 1\n\t" \
    "v_mov_b32 v53, %[tag]\n\t" \
    "v_mov_b32 v52, %[a]\n\t" \
    "s_cmp_gt_u32 %[t], 1\n\t" \
    "s_cselect_b64 exec, %[fm], 0\n\t" \
    "buffer_store_dwordx2 v[52:53], %[l16], %[rs], %[fo] offen sc1\n\t" \
    "s_mov_b64 exec, %[pm]\n\t" \
    "s_mov_b32 m0, %[pd]\n\t" \
    "s_add_u32 %[a], %[sx], %[po]\n\t" \
    "buffer_load_dwordx4 %[l16], %[rs], %[a] offen sc1 lds\n\t" \
    "s_mov_b64 exec, -1\n\t" \
    "s_add_u32 m0, %[sd], 32768\n\t" \
    "s_add_u32 %[a], %[sx], 131072\n\t" \
    "buffer_load_dwordx4 %[l16], %[rs], %[a] offen offset:0 sc1 lds\n\t" \
    "buffer_load_dwordx4 %[l16], %[rs], %[a] offen offset:1024 sc1 lds\n\t" \
    "buffer_load_dwordx4 %[l16], %[rs], %[a] offen offset:2048 sc1 lds\n\t" \
    "buffer_load_dwordx4 %[l16], %[rs], %[a] offen offset:3072 sc1 lds\n\t" \
    "v_mfma_f32_16x16x32_bf16 v[196:199], v[20:23], v[132:135], v[196:199]\n\t" \
    "ds_read_b128 v[44:47], %[xp] offset:55296\n\t" \
    "v_mfma_f32_16x16x32_bf16 v[200:203], v[20:23], v[140:143], v[200:203]\n\t" \
    "ds_read_b128 v[48:51], %[xp] offset:56320\n\t" \
    "v_mfma_f32_16x16x32_bf16 v[204:207], v[24:27], v[132:135], v[204:207]\n\t" \
    "s_waitcnt lgkmcnt(4)\n\t" \
    "v_mfma_f32_16x16x32_bf16 v[208:211], v[24:27], v[140:143], v[208:211]\n\t" \
    "v_mfma_f32_16x16x32_bf16 v[196:199], v[28:31], v[136:139], v[196:199]\n\t" \
    "ds_read_b128 v[20:23], %[xp] offset:57344\n\t" \
    "v_mfma_f32_16x16x32_bf16 v[200:203], v[28:31], v[144:147], v[200:203]\n\t" \
    "ds_read_b128 v[24:27], %[xp] offset:58368\n\t" \
    "v_mfma_f32_16x16x32_bf16 v[204:207], v[32:35], v[136:139], v[204:207]\n\t" \
    "s_waitcnt lgkmcnt(4)\n\t" \
    "v_mfma_f32_16x16x32_bf16 v[208:211], v[32:35], v[144:147], v[208:211]\n\t" \
    "v_mfma_f32_16x16x32_bf16 v[196:199], v[36:39], v[148:151], v[196:199]\n\t" \
    "ds_read_b128 v[28:31], %[xp] offset:59392\n\t" \
    "v_mfma_f32_16x16x32_bf16 v[200:203], v[36:39], v[156:159], v[200:203]\n\t" \
    "ds_read_b128 v[32:35], %[xp] offset:60416\n\t" \
    "v_mfma_f32_16x16x32_bf16 v[204:207], v[40:43], v[148:151], v[204:207]\n\t" \
    "s_waitcnt lgkmcnt(4)\n\t" \
    "v_mfma_f32_16x16x32_bf16 v[208:211], v[40:43], v[156:159], v[208:211]\n\t" \
    "v_mfma_f32_16x16x32_bf16 v[196:199], v[44:47], v[152:155], v[196:199]\n\t" \
    "ds_read_b128 v[36:39], %[xp] offset:61440\n\t" \
    "v_mfma_f32_16x16x32_bf16 v[200:203], v[44:47], v[160:163], v[200:203]\n\t" \
    "ds_read_b128 v[40:43], %[xp] offset:62464\n\t" \
    "v_mfma_f32_16x16x32_bf16 v[204:207], v[48:51], v[152:155], v[204:207]\n\t" \
    "s_waitcnt lgkmcnt(4)\n\t" \
    "v_mfma_f32_16x16x32_bf16 v[208:211], v[48:51], v[160:163], v[208:211]\n\t" \
    "v_mfma_f32_16x16x32_bf16 v[196:199], v[20:23], v[164:167], v[196:199]\n\t" \
    "ds_read_b128 v[44:47], %[xp] offset:63488\n\t" \
    "v_mfma_f32_16x16x32_bf16 v[200:203], v[20:23], v[172:175], v[200:203]\n\t" \
    "ds_read_b128 v[48:51], %[xp] offset:64512\n\t" \
    "v_mfma_f32_16x16x32_bf16 v[204:207], v[24:27], v[164:167], v[204:207]\n\t" \
    "s_waitcnt lgkmcnt(4)\n\t" \
    "v_mfma_f32_16x16x32_bf16 v[208:211], v[24:27], v[172:175], v[208:211]\n\t" \
    "v_mfma_f32_16x16x32_bf16 v[196:199], v[28:31], v[168:171], v[196:199]\n\t" \
    "ds_read_b128 v[20:23], %[xp] offset:0\n\t" \
    "v_mfma_f32_16x16x32_bf16 v[200:203], v[28:31], v[176:179], v[200:203]\n\t" \
    "ds_read_b128 v[24:27], %[xp] offset:1024\n\t" \
    "v_mfma_f32_16x16x32_bf16 v[204:207], v[32:35], v[168:171], v[204:207]\n\t" \
    "s_waitcnt lgkmcnt(4)\n\t" \
    "v_mfma_f32_16x16x32_bf16 v[208:211], v[32:35], v[176:179], v[208:211]\n\t" \
    "v_mfma_f32_16x16x32_bf16 v[196:199], v[36:39], v[180:183], v[196:199]\n\t" \
    "ds_read_b128 v[28:31], %[xp] offset:2048\n\t" \
    "v_mfma_f32_16x16x32_bf16 v[200:203], v[36:39], v[188:191], v[200:203]\n\t" \
    "ds_read_b128 v[32:35], %[xp] offset:3072\n\t" \
    "v_mfma_f32_16x16x32_bf16 v[204:207], v[40:43], v[180:183], v[204:207]\n\t" \
    "s_waitcnt lgkmcnt(4)\n\t" \
    "v_mfma_f32_16x16x32_bf16 v[208:211], v[40:43], v[188:191], v[208:211]\n\t" \
    "v_mfma_f32_16x16x32_bf16 v[196:199], v[44:47], v[184:187], v[196:199]\n\t" \
    "ds_read_b128 v[36:39], %[xp] offset:4096\n\t" \
    "v_mfma_f32_16x16x32_bf16 v[200:203], v[44:47], v[192:195], v[200:203]\n\t" \
    "ds_read_b128 v[40:43], %[xp] offset:5120\n\t" \
    "v_mfma_f32_16x16x32_bf16 v[204:207], v[48:51], v[184:187], v[204:207]\n\t" \
    "s_waitcnt lgkmcnt(4)\n\t" \
    "v_mfma_f32_16x16x32_bf16 v[208:211], v[48:51], v[192:195], v[208:211]\n\t" \
    "s_add_u32 %[sx], %[sx], 65536\n\t" \
    "s_add_u32 %[t], %[t], 1\n\t" \
    "s_waitcnt vmcnt(4)\n\t" \
    "s_barrier\n\t" \
    "s_cmp_lt_u32 %[t], %[te]\n\t" \
    "s_cbranch_scc1 L0_%=\n\t" \
    "s_cmp_eq_u32 %[fin], %[te]\n\t" \
    "s_cbranch_scc0 E_%=\n\t" \
    "s_nop 4\n\t" \
    "ds_write_b128 %[pa], v[196:199] offset:0\n\t" \
    "ds_write_b128 %[pa], v[200:203] offset:1024\n\t" \
    "ds_write_b128 %[pa], v[204:207] offset:2048\n\t" \
    "ds_write_b128 %[pa], v[208:211] offset:3072\n\t" \
    "s_waitcnt lgkmcnt(0)\n\t" \
    "s_barrier\n\t" \
    "s_branch E_%=\n\t" \
    "L1_%=:\n\t" \
    "s_setprio 1\n\t" \
    "s_add_u32 m0, %[sd], 49152\n\t" \
    "s_add_u32 %[a], %[sx], 81920\n\t" \
    "v_mfma_f32_16x16x32_bf16 v[0:3], v[20:23], v[68:71], 0\n\t" \
    "ds_read_b128 v[44:47], %[xp] offset:6144\n\t" \
    "v_mfma_f32_16x16x32_bf16 v[4:7], v[20:23], v[76:79], 0\n\t" \
    "ds_read_b128 v[48:51], %[xp] offset:7168\n\t" \
    "v_mfma_f32_16x16x32_bf16 v[8:11], v[24:27], v[68:71], 0\n\t" \
    "s_waitcnt lgkmcnt(4)\n\t" \
    "v_mfma_f32_16x16x32_bf16 v[12:15], v[24:27], v[76:79], 0\n\t" \
    "v_mfma_f32_16x16x32_bf16 v[0:3], v[28:31], v[72:75], v[0:3]\n\t" \
    "ds_read_b128 v[20:23], %[xp] offset:8192\n\t" \
    "v_mfma_f32_16x16x32_bf16 v[4:7], v[28:31], v[80:83], v[4:7]\n\t" \
    "ds_read_b128 v[24:27], %[xp] offset:9216\n\t" \
    "v_mfma_f32_16x16x32_bf16 v[8:11], v[32:35], v[72:75], v[8:11]\n\t" \
    "s_waitcnt lgkmcnt(4)\n\t" \
    "v_mfma_f32_16x16x32_bf16 v[12:15], v[32:35], v[80:83], v[12:15]\n\t" \
    "v_mfma_f32_16x16x32_bf16 v[0:3], v[36:39], v[84:87], v[0:3]\n\t" \
    "ds_read_b128 v[28:31], %[xp] offset:10240\n\t" \
    "v_mfma_f32_16x16x32_bf16 v[4:7], v[36:39], v[92:95], v[4:7]\n\t" \
    "ds_read_b128 v[32:35], %[xp] offset:11264\n\t" \
    "v_mfma_f32_16x16x32_bf16 v[8:11], v[40:43], v[84:87], v[8:11]\n\t" \
    "s_waitcnt lgkmcnt(4)\n\t" \
    "v_mfma_f32_16x16x32_bf16 v[12:15], v[40:43], v[92:95], v[12:15]\n\t" \
    "v_mfma_f32_16x16x32_bf16 v[0:3], v[44:47], v[88:91], v[0:3]\n\t" \
    "ds_read_b128 v[36:39], %[xp] offset:12288\n\t" \
    "v_mfma_f32_16x16x32_bf16 v[4:7], v[44:47], v[96:99], v[4:7]\n\t" \
    "ds_read_b128 v[40:43], %[xp] offset:13312\n\t" \
    "v_mfma_f32_16x16x32_bf16 v[8:11], v[48:51], v[88:91], v[8:11]\n\t" \
    "s_waitcnt lgkmcnt(4)\n\t" \
    "v_mfma_f32_16x16x32_bf16 v[12:15], v[48:51], v[96:99], v[12:15]\n\t" \
    "v_mfma_f32_16x16x32_bf16 v[0:3], v[20:23], v[100:103], v[0:3]\n\t" \
    "ds_read_b128 v[44:47], %[xp] offset:14336\n\t" \
    "v_mfma_f32_16x16x32_bf16 v[4:7], v[20:23], v[108:111], v[4:7]\n\t" \
    "ds_read_b128 v[48:51], %[xp] offset:15360\n\t" \
    "v_mfma_f32_16x16x32_bf16 v[8:11], v[24:27], v[100:103], v[8:11]\n\t" \
    "s_waitcnt lgkmcnt(4)\n\t" \
    "v_mfma_f32_16x16x32_bf16 v[12:15], v[24:27], v[108:111], v[12:15]\n\t" \
    "buffer_load_dwordx4 %[l16], %[rs], %[a] offen offset:0 sc1 lds\n\t" \
    "v_mfma_f32_16x16x32_bf16 v[0:3], v[28:31], v[104:107], v[0:3]\n\t" \
    "ds_read_b128 v[20:23], %[xp] offset:16384\n\t" \
    "v_mfma_f32_16x16x32_bf16 v[4:7], v[28:31], v[112:115], v[4:7]\n\t" \
    "ds_read_b128 v[24:27], %[xp] offset:17408\n\t" \
    "v_mfma_f32_16x16x32_bf16 v[8:11], v[32:35], v[104:107], v[8:11]\n\t" \
    "s_waitcnt lgkmcnt(4)\n\t" \
    "v_mfma_f32_16x16x32_bf16 v[12:15], v[32:35], v[112:115], v[12:15]\n\t" \
    "buffer_load_dwordx4 %[l16], %[rs], %[a] offen offset:1024 sc1 lds\n\t" \
    "v_mfma_f32_16x16x32_bf16 v[0:3], v[36:39], v[116:119], v[0:3]\n\t" \
    "ds_read_b128 v[28:31], %[xp] offset:18432\n\t" \
    "v_mfma_f32_16x16x32_bf16 v[4:7], v[36:39], v[124:127], v[4:7]\n\t" \
    "ds_read_b128 v[32:35], %[xp] offset:19456\n\t" \
    "v_mfma_f32_16x16x32_bf16 v[8:11], v[40:43], v[116:119], v[8:11]\n\t" \
    "s_waitcnt lgkmcnt(4)\n\t" \
    "v_mfma_f32_16x16x32_bf16 v[12:15], v[40:43], v[124:127], v[12:15]\n\t" \
    "buffer_load_dwordx4 %[l16], %[rs], %[a] offen offset:2048 sc1 lds\n\t" \
    "v_mfma_f32_16x16x32_bf16 v[0:3], v[44:47], v[120:123], v[0:3]\n\t" \
    "ds_read_b128 v[36:39], %[xp] offset:20480\n\t" \
    "v_mfma_f32_16x16x32_bf16 v[4:7], v[44:47], v[128:131], v[4:7]\n\t" \
    "ds_read_b128 v[40:43], %[xp] offset:21504\n\t" \
    "v_mfma_f32_16x16x32_bf16 v[8:11], v[48:51], v[120:123], v[8:11]\n\t" \
    "s_waitcnt lgkmcnt(4)\n\t" \
    "v_mfma_f32_16x16x32_bf16 v[12:15], v[48:51], v[128:131], v[12:15]\n\t" \
    "buffer_load_dwordx4 %[l16], %[rs], %[a] offen offset:3072 sc1 lds\n\t" \
    "s_waitcnt vmcnt(4)\n\t" \
    "s_barrier\n\t" \
    "s_setprio 1\n\t" \
    "s_add_u32 m0, %[sd], 0\n\t" \
    "s_add_u32 %[a], %[sx], 131072\n\t" \
    "s_add_u32 %[b], %[oo], 0x1000\n\t" \
    "s_cmp_lg_u32 %[t], 1\n\t" \
    "s_cselect_b64 %[em], -1, 0\n\t" \
    "v_mfma_f32_16x16x32_bf16 v[0:3], v[20:23], v[132:135], v[0:3]\n\t" \
    "ds_read_b128 v[44:47], %[xp] offset:22528\n\t" \
    "v_mfma_f32_16x16x32_bf16 v[4:7], v[20:23], v[140:143], v[4:7]\n\t" \
    "ds_read_b128 v[48:51], %[xp] offset:23552\n\t" \
    "v_mfma_f32_16x16x32_bf16 v[8:11], v[24:27], v[132:135], v[8:11]\n\t" \
    "ds_read_b128 v[52:55], %[pa] offset:0\n\t" \
    "s_waitcnt lgkmcnt(5)\n\t" \
    "v_mfma_f32_16x16x32_bf16 v[12:15], v[24:27], v[140:143], v[12:15]\n\t" \
    "buffer_load_dwordx4 %[l16], %[rs], %[a] offen offset:0 sc1 lds\n\t" \
    "v_mfma_f32_16x16x32_bf16 v[0:3], v[28:31], v[136:139], v[0:3]\n\t" \
    "ds_read_b128 v[20:23], %[xp] offset:24576\n\t" \
    "v_mfma_f32_16x16x32_bf16 v[4:7], v[28:31], v[144:147], v[4:7]\n\t" \
    "ds_read_b128 v[24:27], %[xp] offset:25600\n\t" \
    "v_mfma_f32_16x16x32_bf16 v[8:11], v[32:35], v[136:139], v[8:11]\n\t" \
    "ds_read_b128 v[56:59], %[pa] offset:1024\n\t" \
    "s_waitcnt lgkmcnt(6)\n\t" \
    "v_mfma_f32_16x16x32_bf16 v[12:15], v[32:35], v[144:147], v[12:15]\n\t" \
    "buffer_load_dwordx4 %[l16], %[rs], %[a] offen offset:1024 sc1 lds\n\t" \
    "v_mfma_f32_16x16x32_bf16 v[0:3], v[36:39], v[148:151], v[0:3]\n\t" \
    "ds_read_b128 v[28:31], %[xp] offset:26624\n\t" \
    "v_mfma_f32_16x16x32_bf16 v[4:7], v[36:39], v[156:159], v[4:7]\n\t" \
    "ds_read_b128 v[32:35], %[xp] offset:27648\n\t" \
    "v_mfma_f32_16x16x32_bf16 v[8:11], v[40:43], v[148:151], v[8:11]\n\t" \
    "ds_read_b128 v[60:63], %[pa] offset:2048\n\t" \
    "s_waitcnt lgkmcnt(7)\n\t" \
    "v_mfma_f32_16x16x32_bf16 v[12:15], v[40:43], v[156:159], v[12:15]\n\t" \
    "buffer_load_dwordx4 %[l16], %[rs], %[a] offen offset:2048 sc1 lds\n\t" \
    "v_mfma_f32_16x16x32_bf16 v[0:3], v[44:47], v[152:155], v[0:3]\n\t" \
    "ds_read_b128 v[36:39], %[xp] offset:28672\n\t" \
    "s_waitcnt lgkmcnt(7)\n\t" \
    "v_add_f32 v196, v196, v52\n\t" \
    "v_mfma_f32_16x16x32_bf16 v[4:7], v[44:47], v[160:163], v[4:7]\n\t" \
    "ds_read_b128 v[40:43], %[xp] offset:29696\n\t" \
    "v_add_f32 v197, v197, v53\n\t" \
    "v_mfma_f32_16x16x32_bf16 v[8:11], v[48:51], v[152:155], v[8:11]\n\t" \
    "ds_read_b128 v[64:67], %[pa] offset:3072\n\t" \
    "s_waitcnt lgkmcnt(7)\n\t" \
    "v_mfma_f32_16x16x32_bf16 v[12:15], v[48:51], v[160:163], v[12:15]\n\t" \
    "buffer_load_dwordx4 %[l16], %[rs], %[a] offen offset:3072 sc1 lds\n\t" \
    "v_add_f32 v198, v198, v54\n\t" \
    "v_add_f32 v199, v199, v55\n\t" \
    "v_mfma_f32_16x16x32_bf16 v[0:3], v[20:23], v[164:167], v[0:3]\n\t" \
    "ds_read_b128 v[44:47], %[xp] offset:30720\n\t" \
    "s_waitcnt lgkmcnt(7)\n\t" \
    "v_add_f32 v200, v200, v56\n\t" \
    "v_mfma_f32_16x16x32_bf16 v[4:7], v[20:23], v[172:175], v[4:7]\n\t" \
    "ds_read_b128 v[48:51], %[xp] offset:31744\n\t" \
    "v_add_f32 v201, v201, v57\n\t" \
    "v_mfma_f32_16x16x32_bf16 v[8:11], v[24:27], v[164:167], v[8:11]\n\t" \
    "v_add_f32 v202, v202, v58\n\t" \
    "s_waitcnt lgkmcnt(6)\n\t" \
    "v_mfma_f32_16x16x32_bf16 v[12:15], v[24:27], v[172:175], v[12:15]\n\t" \
    "v_add_f32 v203, v203, v59\n\t" \
    "s_waitcnt lgkmcnt(5)\n\t" \
    "v_add_f32 v204, v204, v60\n\t" \
    "v_add_f32 v205, v205, v61\n\t" \
    "v_mfma_f32_16x16x32_bf16 v[0:3], v[28:31], v[168:171], v[0:3]\n\t" \
    "ds_read_b128 v[20:23], %[xp] offset:32768\n\t" \
    "v_add_f32 v206, v206, v62\n\t" \
    "v_mfma_f32_16x16x32_bf16 v[4:7], v[28:31], v[176:179], v[4:7]\n\t" \
    "ds_read_b128 v[24:27], %[xp] offset:33792\n\t" \
    "v_add_f32 v207, v207, v63\n\t" \
    "v_mfma_f32_16x16x32_bf16 v[8:11], v[32:35], v[168:171], v[8:11]\n\t" \
    "s_waitcnt lgkmcnt(4)\n\t" \
    "v_add_f32 v208, v208, v64\n\t" \
    "v_mfma_f32_16x16x32_bf16 v[12:15], v[32:35], v[176:179], v[12:15]\n\t" \
    "v_add_f32 v209, v209, v65\n\t" \
    "v_add_f32 v210, v210, v66\n\t" \
    "v_add_f32 v211, v211, v67\n\t" \
    "v_mfma_f32_16x16x32_bf16 v[0:3], v[36:39], v[180:183], v[0:3]\n\t" \
    "ds_read_b128 v[28:31], %[xp] offset:34816\n\t" \
    "v_cvt_pk_bf16_f32 v52, v196, v197\n\t" \
    "v_mfma_f32_16x16x32_bf16 v[4:7], v[36:39], v[188:191], v[4:7]\n\t" \
    "ds_read_b128 v[32:35], %[xp] offset:35840\n\t" \
    "v_cvt_pk_bf16_f32 v53, v198, v199\n\t" \
    "v_mfma_f32_16x16x32_bf16 v[8:11], v[40:43], v[180:183], v[8:11]\n\t" \
    "v_cvt_pk_bf16_f32 v54, v204, v205\n\t" \
    "s_waitcnt lgkmcnt(4)\n\t" \
    "v_mfma_f32_16x16x32_bf16 v[12:15], v[40:43], v[188:191], v[12:15]\n\t" \
    "v_cvt_pk_bf16_f32 v55, v206, v207\n\t" \
    "v_cvt_pk_bf16_f32 v56, v200, v201\n\t" \
    "v_cvt_pk_bf16_f32 v57, v202, v203\n\t" \
    "v_mfma_f32_16x16x32_bf16 v[0:3], v[44:47], v[184:187], v[0:3]\n\t" \
    "ds_read_b128 v[36:39], %[xp] offset:36864\n\t" \
    "v_cvt_pk_bf16_f32 v58, v208, v209\n\t" \
    "v_mfma_f32_16x16x32_bf16 v[4:7], v[44:47], v[192:195], v[4:7]\n\t" \
    "ds_read_b128 v[40:43], %[xp] offset:37888\n\t" \
    "v_cvt_pk_bf16_f32 v59, v210, v211\n\t" \
    "v_mfma_f32_16x16x32_bf16 v[8:11], v[48:51], v[184:187], v[8:11]\n\t" \
    "s_waitcnt lgkmcnt(4)\n\t" \
    "v_mfma_f32_16x16x32_bf16 v[12:15], v[48:51], v[192:195], v[12:15]\n\t" \
    "s_mov_b64 exec, %[em]\n\t" \
    "buffer_store_dwordx4 v[52:55], %[l16], %[rs], %[oo] offen sc1\n\t" \
    "buffer_store_dwordx4 v[56:59], %[l16], %[rs], %[oo] offen offset:1024 sc1\n\t" \
    "s_mov_b64 exec, -1\n\t" \
    "s_add_u32 %[sx], %[sx], 65536\n\t" \
    "s_add_u32 %[t], %[t], 1\n\t" \
    "s_add_u32 %[oo], %[oo], 262144\n\t" \
    "s_waitcnt vmcnt(6)\n\t" \
    "s_cmp_gt_u32 %[t], " STR(SQUICK) " + 1\n\t" \
    "s_cbranch_scc1 B0_%=\n\t" \
    "s_waitcnt vmcnt(0)\n\t" \
    "B0_%=:\n\t" \
    "s_barrier\n\t" \
    "s_setprio 1\n\t" \
    "s_add_u32 m0, %[sd], 16384\n\t" \
    "s_add_u32 %[a], %[sx], 81920\n\t" \
    "v_mfma_f32_16x16x32_bf16 v[196:199], v[20:23], v[68:71], 0\n\t" \
    "ds_read_b128 v[44:47], %[xp] offset:38912\n\t" \
    "v_mfma_f32_16x16x32_bf16 v[200:203], v[20:23], v[76:79], 0\n\t" \
    "ds_read_b128 v[48:51], %[xp] offset:39936\n\t" \
    "v_mfma_f32_16x16x32_bf16 v[204:207], v[24:27], v[68:71], 0\n\t" \
    "s_waitcnt lgkmcnt(4)\n\t" \
    "v_mfma_f32_16x16x32_bf16 v[208:211], v[24:27], v[76:79], 0\n\t" \
    "v_mfma_f32_16x16x32_bf16 v[196:199], v[28:31], v[72:75], v[196:199]\n\t" \
    "ds_read_b128 v[20:23], %[xp] offset:40960\n\t" \
    "v_mfma_f32_16x16x32_bf16 v[200:203], v[28:31], v[80:83], v[200:203]\n\t" \
    "ds_read_b128 v[24:27], %[xp] offset:41984\n\t" \
    "v_mfma_f32_16x16x32_bf16 v[204:207], v[32:35], v[72:75], v[204:207]\n\t" \
    "s_waitcnt lgkmcnt(4)\n\t" \
    "v_mfma_f32_16x16x32_bf16 v[208:211], v[32:35], v[80:83], v[208:211]\n\t" \
    "v_mfma_f32_16x16x32_bf16 v[196:199], v[36:39], v[84:87], v[196:199]\n\t" \
    "ds_read_b128 v[28:31], %[xp] offset:43008\n\t" \
    "v_mfma_f32_16x16x32_bf16 v[200:203], v[36:39], v[92:95], v[200:203]\n\t" \
    "ds_read_b128 v[32:35], %[xp] offset:44032\n\t" \
    "v_mfma_f32_16x16x32_bf16 v[204:207], v[40:43], v[84:87], v[204:207]\n\t" \
    "s_waitcnt lgkmcnt(4)\n\t" \
    "v_mfma_f32_16x16x32_bf16 v[208:211], v[40:43], v[92:95], v[208:211]\n\t" \
    "v_mfma_f32_16x16x32_bf16 v[196:199], v[44:47], v[88:91], v[196:199]\n\t" \
    "ds_read_b128 v[36:39], %[xp] offset:45056\n\t" \
    "v_mfma_f32_16x16x32_bf16 v[200:203], v[44:47], v[96:99], v[200:203]\n\t" \
    "ds_read_b128 v[40:43], %[xp] offset:46080\n\t" \
    "v_mfma_f32_16x16x32_bf16 v[204:207], v[48:51], v[88:91], v[204:207]\n\t" \
    "s_waitcnt lgkmcnt(4)\n\t" \
    "v_mfma_f32_16x16x32_bf16 v[208:211], v[48:51], v[96:99], v[208:211]\n\t" \
    "v_mfma_f32_16x16x32_bf16 v[196:199], v[20:23], v[100:103], v[196:199]\n\t" \
    "ds_read_b128 v[44:47], %[xp] offset:47104\n\t" \
    "v_mfma_f32_16x16x32_bf16 v[200:203], v[20:23], v[108:111], v[200:203]\n\t" \
    "ds_read_b128 v[48:51], %[xp] offset:48128\n\t" \
    "v_mfma_f32_16x16x32_bf16 v[204:207], v[24:27], v[100:103], v[204:207]\n\t" \
    "s_waitcnt lgkmcnt(4)\n\t" \
    "v_mfma_f32_16x16x32_bf16 v[208:211], v[24:27], v[108:111], v[208:211]\n\t" \
    "buffer_load_dwordx4 %[l16], %[rs], %[a] offen offset:0 sc1 lds\n\t" \
    "v_mfma_f32_16x16x32_bf16 v[196:199], v[28:31], v[104:107], v[196:199]\n\t" \
    "ds_read_b128 v[20:23], %[xp] offset:49152\n\t" \
    "v_mfma_f32_16x16x32_bf16 v[200:203], v[28:31], v[112:115], v[200:203]\n\t" \
    "ds_read_b128 v[24:27], %[xp] offset:50176\n\t" \
    "v_mfma_f32_16x16x32_bf16 v[204:207], v[32:35], v[104:107], v[204:207]\n\t" \
    "s_waitcnt lgkmcnt(4)\n\t" \
    "v_mfma_f32_16x16x32_bf16 v[208:211], v[32:35], v[112:115], v[208:211]\n\t" \
    "buffer_load_dwordx4 %[l16], %[rs], %[a] offen offset:1024 sc1 lds\n\t" \
    "v_mfma_f32_16x16x32_bf16 v[196:199], v[36:39], v[116:119], v[196:199]\n\t" \
    "ds_read_b128 v[28:31], %[xp] offset:51200\n\t" \
    "v_mfma_f32_16x16x32_bf16 v[200:203], v[36:39], v[124:127], v[200:203]\n\t" \
    "ds_read_b128 v[32:35], %[xp] offset:52224\n\t" \
    "v_mfma_f32_16x16x32_bf16 v[204:207], v[40:43], v[116:119], v[204:207]\n\t" \
    "s_waitcnt lgkmcnt(4)\n\t" \
    "v_mfma_f32_16x16x32_bf16 v[208:211], v[40:43], v[124:127], v[208:211]\n\t" \
    "buffer_load_dwordx4 %[l16], %[rs], %[a] offen offset:2048 sc1 lds\n\t" \
    "v_mfma_f32_16x16x32_bf16 v[196:199], v[44:47], v[120:123], v[196:199]\n\t" \
    "ds_read_b128 v[36:39], %[xp] offset:53248\n\t" \
    "v_mfma_f32_16x16x32_bf16 v[200:203], v[44:47], v[128:131], v[200:203]\n\t" \
    "ds_read_b128 v[40:43], %[xp] offset:54272\n\t" \
    "v_mfma_f32_16x16x32_bf16 v[204:207], v[48:51], v[120:123], v[204:207]\n\t" \
    "s_waitcnt lgkmcnt(4)\n\t" \
    "v_mfma_f32_16x16x32_bf16 v[208:211], v[48:51], v[128:131], v[208:211]\n\t" \
    "buffer_load_dwordx4 %[l16], %[rs], %[a] offen offset:3072 sc1 lds\n\t" \
    "s_waitcnt vmcnt(4)\n\t" \
    "s_barrier\n\t" \
    "s_setprio 1\n\t" \
    "s_add_u32 m0, %[sd], 32768\n\t" \
    "s_add_u32 %[a], %[sx], 131072\n\t" \
    "s_add_u32 %[b], %[oo], 0x1000\n\t" \
    "s_cmp_lg_u32 %[t], 1\n\t" \
    "s_cselect_b64 %[em], -1, 0\n\t" \
    "v_mfma_f32_16x16x32_bf16 v[196:199], v[20:23], v[132:135], v[196:199]\n\t" \
    "ds_read_b128 v[44:47], %[xp] offset:55296\n\t" \
    "v_mfma_f32_16x16x32_bf16 v[200:203], v[20:23], v[140:143], v[200:203]\n\t" \
    "ds_read_b128 v[48:51], %[xp] offset:56320\n\t" \
    "v_mfma_f32_16x16x32_bf16 v[204:207], v[24:27], v[132:135], v[204:207]\n\t" \
    "ds_read_b128 v[52:55], %[pa] offset:0\n\t" \
    "s_waitcnt lgkmcnt(5)\n\t" \
    "v_mfma_f32_16x16x32_bf16 v[208:211], v[24:27], v[140:143], v[208:211]\n\t" \
    "buffer_load_dwordx4 %[l16], %[rs], %[a] offen offset:0 sc1 lds\n\t" \
    "v_mfma_f32_16x16x32_bf16 v[196:199], v[28:31], v[136:139], v[196:199]\n\t" \
    "ds_read_b128 v[20:23], %[xp] offset:57344\n\t" \
    "v_mfma_f32_16x16x32_bf16 v[200:203], v[28:31], v[144:147], v[200:203]\n\t" \
    "ds_read_b128 v[24:27], %[xp] offset:58368\n\t" \
    "v_mfma_f32_16x16x32_bf16 v[204:207], v[32:35], v[136:139], v[204:207]\n\t" \
    "ds_read_b128 v[56:59], %[pa] offset:1024\n\t" \
    "s_waitcnt lgkmcnt(6)\n\t" \
    "v_mfma_f32_16x16x32_bf16 v[208:211], v[32:35], v[144:147], v[208:211]\n\t" \
    "buffer_load_dwordx4 %[l16], %[rs], %[a] offen offset:1024 sc1 lds\n\t" \
    "v_mfma_f32_16x16x32_bf16 v[196:199], v[36:39], v[148:151], v[196:199]\n\t" \
    "ds_read_b128 v[28:31], %[xp] offset:59392\n\t" \
    "v_mfma_f32_16x16x32_bf16 v[200:203], v[36:39], v[156:159], v[200:203]\n\t" \
    "ds_read_b128 v[32:35], %[xp] offset:60416\n\t" \
    "v_mfma_f32_16x16x32_bf16 v[204:207], v[40:43], v[148:151], v[204:207]\n\t" \
    "ds_read_b128 v[60:63], %[pa] offset:2048\n\t" \
    "s_waitcnt lgkmcnt(7)\n\t" \
    "v_mfma_f32_16x16x32_bf16 v[208:211], v[40:43], v[156:159], v[208:211]\n\t" \
    "buffer_load_dwordx4 %[l16], %[rs], %[a] offen offset:2048 sc1 lds\n\t" \
    "v_mfma_f32_16x16x32_bf16 v[196:199], v[44:47], v[152:155], v[196:199]\n\t" \
    "ds_read_b128 v[36:39], %[xp] offset:61440\n\t" \
    "s_waitcnt lgkmcnt(7)\n\t" \
    "v_add_f32 v0, v0, v52\n\t" \
    "v_mfma_f32_16x16x32_bf16 v[200:203], v[44:47], v[160:163], v[200:203]\n\t" \
    "ds_read_b128 v[40:43], %[xp] offset:62464\n\t" \
    "v_add_f32 v1, v1, v53\n\t" \
    "v_mfma_f32_16x16x32_bf16 v[204:207], v[48:51], v[152:155], v[204:207]\n\t" \
    "ds_read_b128 v[64:67], %[pa] offset:3072\n\t" \
    "s_waitcnt lgkmcnt(7)\n\t" \
    "v_mfma_f32_16x16x32_bf16 v[208:211], v[48:51], v[160:163], v[208:211]\n\t" \
    "buffer_load_dwordx4 %[l16], %[rs], %[a] offen offset:3072 sc1 lds\n\t" \
    "v_add_f32 v2, v2, v54\n\t" \
    "v_add_f32 v3, v3, v55\n\t" \
    "v_mfma_f32_16x16x32_bf16 v[196:199], v[20:23], v[164:167], v[196:199]\n\t" \
    "ds_read_b128 v[44:47], %[xp] offset:63488\n\t" \
    "s_waitcnt lgkmcnt(7)\n\t" \
    "v_add_f32 v4, v4, v56\n\t" \
    "v_mfma_f32_16x16x32_bf16 v[200:203], v[20:23], v[172:175], v[200:203]\n\t" \
    "ds_read_b128 v[48:51], %[xp] offset:64512\n\t" \
    "v_add_f32 v5, v5, v57\n\t" \
    "v_mfma_f32_16x16x32_bf16 v[204:207], v[24:27], v[164:167], v[204:207]\n\t" \
    "v_add_f32 v6, v6, v58\n\t" \
    "s_waitcnt lgkmcnt(6)\n\t" \
    "v_mfma_f32_16x16x32_bf16 v[208:211], v[24:27], v[172:175], v[208:211]\n\t" \
    "v_add_f32 v7, v7, v59\n\t" \
    "s_waitcnt lgkmcnt(5)\n\t" \
    "v_add_f32 v8, v8, v60\n\t" \
    "v_add_f32 v9, v9, v61\n\t" \
    "v_mfma_f32_16x16x32_bf16 v[196:199], v[28:31], v[168:171], v[196:199]\n\t" \
    "ds_read_b128 v[20:23], %[xp] offset:0\n\t" \
    "v_add_f32 v10, v10, v62\n\t" \
    "v_mfma_f32_16x16x32_bf16 v[200:203], v[28:31], v[176:179], v[200:203]\n\t" \
    "ds_read_b128 v[24:27], %[xp] offset:1024\n\t" \
    "v_add_f32 v11, v11, v63\n\t" \
    "v_mfma_f32_16x16x32_bf16 v[204:207], v[32:35], v[168:171], v[204:207]\n\t" \
    "s_waitcnt lgkmcnt(4)\n\t" \
    "v_add_f32 v12, v12, v64\n\t" \
    "v_mfma_f32_16x16x32_bf16 v[208:211], v[32:35], v[176:179], v[208:211]\n\t" \
    "v_add_f32 v13, v13, v65\n\t" \
    "v_add_f32 v14, v14, v66\n\t" \
    "v_add_f32 v15, v15, v67\n\t" \
    "v_mfma_f32_16x16x32_bf16 v[196:199], v[36:39], v[180:183], v[196:199]\n\t" \
    "ds_read_b128 v[28:31], %[xp] offset:2048\n\t" \
    "v_cvt_pk_bf16_f32 v52, v0, v1\n\t" \
    "v_mfma_f32_16x16x32_bf16 v[200:203], v[36:39], v[188:191], v[200:203]\n\t" \
    "ds_read_b128 v[32:35], %[xp] offset:3072\n\t" \
    "v_cvt_pk_bf16_f32 v53, v2, v3\n\t" \
    "v_mfma_f32_16x16x32_bf16 v[204:207], v[40:43], v[180:183], v[204:207]\n\t" \
    "v_cvt_pk_bf16_f32 v54, v8, v9\n\t" \
    "s_waitcnt lgkmcnt(4)\n\t" \
    "v_mfma_f32_16x16x32_bf16 v[208:211], v[40:43], v[188:191], v[208:211]\n\t" \
    "v_cvt_pk_bf16_f32 v55, v10, v11\n\t" \
    "v_cvt_pk_bf16_f32 v56, v4, v5\n\t" \
    "v_cvt_pk_bf16_f32 v57, v6, v7\n\t" \
    "v_mfma_f32_16x16x32_bf16 v[196:199], v[44:47], v[184:187], v[196:199]\n\t" \
    "ds_read_b128 v[36:39], %[xp] offset:4096\n\t" \
    "v_cvt_pk_bf16_f32 v58, v12, v13\n\t" \
    "v_mfma_f32_16x16x32_bf16 v[200:203], v[44:47], v[192:195], v[200:203]\n\t" \
    "ds_read_b128 v[40:43], %[xp] offset:5120\n\t" \
    "v_cvt_pk_bf16_f32 v59, v14, v15\n\t" \
    "v_mfma_f32_16x16x32_bf16 v[204:207], v[48:51], v[184:187], v[204:207]\n\t" \
    "s_waitcnt lgkmcnt(4)\n\t" \
    "v_mfma_f32_16x16x32_bf16 v[208:211], v[48:51], v[192:195], v[208:211]\n\t" \
    "s_mov_b64 exec, %[em]\n\t" \
    "buffer_store_dwordx4 v[52:55], %[l16], %[rs], %[oo] offen sc1\n\t" \
    "buffer_store_dwordx4 v[56:59], %[l16], %[rs], %[oo] offen offset:1024 sc1\n\t" \
    "s_mov_b64 exec, -1\n\t" \
    "s_add_u32 %[sx], %[sx], 65536\n\t" \
    "s_add_u32 %[t], %[t], 1\n\t" \
    "s_add_u32 %[oo], %[oo], 262144\n\t" \
    "s_waitcnt vmcnt(6)\n\t" \
    "s_cmp_gt_u32 %[t], " STR(SQUICK) " + 1\n\t" \
    "s_cbranch_scc1 B1_%=\n\t" \
    "s_waitcnt vmcnt(0)\n\t" \
    "B1_%=:\n\t" \
    "s_barrier\n\t" \
    "s_cmp_lt_u32 %[t], %[te]\n\t" \
    "s_cbranch_scc1 L1_%=\n\t" \
    "s_cmp_eq_u32 %[fin], %[te]\n\t" \
    "s_cbranch_scc0 E_%=\n\t" \
    "s_nop 4\n\t" \
    "s_barrier\n\t" \
    "ds_read_b128 v[52:55], %[pa] offset:0\n\t" \
    "ds_read_b128 v[56:59], %[pa] offset:1024\n\t" \
    "ds_read_b128 v[60:63], %[pa] offset:2048\n\t" \
    "ds_read_b128 v[64:67], %[pa] offset:3072\n\t" \
    "s_add_u32 %[b], %[oo], 0x1000\n\t" \
    "s_waitcnt lgkmcnt(0)\n\t" \
    "v_add_f32 v196, v196, v52\n\t" \
    "v_add_f32 v197, v197, v53\n\t" \
    "v_add_f32 v198, v198, v54\n\t" \
    "v_add_f32 v199, v199, v55\n\t" \
    "v_add_f32 v200, v200, v56\n\t" \
    "v_add_f32 v201, v201, v57\n\t" \
    "v_add_f32 v202, v202, v58\n\t" \
    "v_add_f32 v203, v203, v59\n\t" \
    "v_add_f32 v204, v204, v60\n\t" \
    "v_add_f32 v205, v205, v61\n\t" \
    "v_add_f32 v206, v206, v62\n\t" \
    "v_add_f32 v207, v207, v63\n\t" \
    "v_add_f32 v208, v208, v64\n\t" \
    "v_add_f32 v209, v209, v65\n\t" \
    "v_add_f32 v210, v210, v66\n\t" \
    "v_add_f32 v211, v211, v67\n\t" \
    "v_cvt_pk_bf16_f32 v52, v196, v197\n\t" \
    "v_cvt_pk_bf16_f32 v53, v198, v199\n\t" \
    "v_cvt_pk_bf16_f32 v54, v204, v205\n\t" \
    "v_cvt_pk_bf16_f32 v55, v206, v207\n\t" \
    "v_cvt_pk_bf16_f32 v56, v200, v201\n\t" \
    "v_cvt_pk_bf16_f32 v57, v202, v203\n\t" \
    "v_cvt_pk_bf16_f32 v58, v208, v209\n\t" \
    "v_cvt_pk_bf16_f32 v59, v210, v211\n\t" \
    "buffer_store_dwordx4 v[52:55], %[l16], %[rs], %[oo] offen sc1\n\t" \
    "buffer_store_dwordx4 v[56:59], %[l16], %[rs], %[oo] offen offset:1024 sc1\n\t" \
    "E_%=:\n\t" \
    "s_setprio 0\n\t" \
    "s_waitcnt lgkmcnt(0)\n\t" \
    "s_nop 7\n\t" \
    : [t] "+s"(t), [sx] "+s"(sx), [oo] "+s"(oo), [a] "=&s"(tmp), [b] "=&s"(tmp2), [em] "=&s"(em), "+{v[20:27]}"(xf[0]), "+{v[28:35]}"(xf[1]), "+{v[36:43]}"(xf[2]), "+{v[196:203]}"(ac[0]), "+{v[204:211]}"(ac[1]) \
    : [te] "s"(TE_), [fin] "s"(FIN_), [kh] "s"(kh), [sd] "s"(sd), [rs] "s"(rs), [l16] "v"(l16), [xp] "v"(xp), [pa] "v"(pa), [fm] "s"(fm), [pm] "s"(pm), [fo] "s"(fo), [po] "s"(po), [pd] "s"(pd), [tag] "s"(P.tag), "{v[68:75]}"(w[0][0]), "{v[76:83]}"(w[0][1]), "{v[84:91]}"(w[1][0]), "{v[92:99]}"(w[1][1]), "{v[100:107]}"(w[2][0]), "{v[108:115]}"(w[2][1]), "{v[116:123]}"(w[3][0]), "{v[124:131]}"(w[3][1]), "{v[132:139]}"(w[4][0]), "{v[140:147]}"(w[4][1]), "{v[148:155]}"(w[5][0]), "{v[156:163]}"(w[5][1]), "{v[164:171]}"(w[6][0]), "{v[172:179]}"(w[6][1]), "{v[180:187]}"(w[7][0]), "{v[188:195]}"(w[7][1]) \
    : "memory", "scc", "v0", "v1", "v2", "v3", "v4", "v5", "v6", "v7", "v8", "v9", "v10", "v11", "v12", "v13", "v14", "v15", "v44", "v45", "v46", "v47", "v48", "v49", "v50", "v51", "v52", "v53", "v54", "v55", "v56", "v57", "v58", "v59", "v60", "v61", "v62", "v63", "v64", "v65", "v66", "v67")

#define S_LOOP_X(TE_, FIN_) \
  asm volatile( \
    "s_cmp_eq_u32 %[kh], 0\n\t" \
    "s_cbranch_scc0 L1_%=\n\t" \
    "L0_%=:\n\t" \
    "s_setprio 0\n\t" \
    "s_nop 3\n\t" \
    "ds_write_b128 %[pa], v[196:199] offset:0\n\t" \
    "ds_write_b128 %[pa], v[200:203] offset:1024\n\t" \
    "ds_write_b128 %[pa], v[204:207] offset:2048\n\t" \
    "ds_write_b128 %[pa], v[208:211] offset:3072\n\t" \
    "s_sub_u32 %[a], %[t], 1\n\t" \
    "v_mov_b32 v53, %[tag]\n\t" \
    "v_mov_b32 v52, %[a]\n\t" \
    "s_sub_u32 %[b], %[t], 2\n\t" \
    "s_cmp_lt_u32 %[b], " STR(SQUICK) "\n\t" \
    "s_cselect_b64 exec, %[fm], 0\n\t" \
    "buffer_store_dwordx2 v[52:53], %[l16], %[rs], %[fo] offen sc1\n\t" \
    "s_mov_b64 exec, -1\n\t" \
    "s_add_u32 m0, %[sd], 49152\n\t" \
    "s_add_u32 %[a], %[sx], 278528\n\t" \
    "s_waitcnt lgkmcnt(0)\n\t" \
    "buffer_load_dwordx4 %[l16], %[rs], %[a] offen offset:0 sc1 lds\n\t" \
    "buffer_load_dwordx4 %[l16], %[rs], %[a] offen offset:1024 sc1 lds\n\t" \
    "buffer_load_dwordx4 %[l16], %[rs], %[a] offen offset:2048 sc1 lds\n\t" \
    "buffer_load_dwordx4 %[l16], %[rs], %[a] offen offset:3072 sc1 lds\n\t" \
    "v_mfma_f32_16x16x32_bf16 v[0:3], v[68:71], v[20:23], 0\n\t" \
    "ds_read_b128 v[44:47], %[xp] offset:6144\n\t" \
    "v_mfma_f32_16x16x32_bf16 v[4:7], v[76:79], v[20:23], 0\n\t" \
    "ds_read_b128 v[48:51], %[xp] offset:7168\n\t" \
    "v_mfma_f32_16x16x32_bf16 v[8:11], v[68:71], v[24:27], 0\n\t" \
    "v_mfma_f32_16x16x32_bf16 v[12:15], v[76:79], v[24:27], 0\n\t" \
    "v_mfma_f32_16x16x32_bf16 v[0:3], v[72:75], v[28:31], v[0:3]\n\t" \
    "ds_read_b128 v[20:23], %[xp] offset:8192\n\t" \
    "v_mfma_f32_16x16x32_bf16 v[4:7], v[80:83], v[28:31], v[4:7]\n\t" \
    "ds_read_b128 v[24:27], %[xp] offset:9216\n\t" \
    "v_mfma_f32_16x16x32_bf16 v[8:11], v[72:75], v[32:35], v[8:11]\n\t" \
    "v_mfma_f32_16x16x32_bf16 v[12:15], v[80:83], v[32:35], v[12:15]\n\t" \
    "v_mfma_f32_16x16x32_bf16 v[0:3], v[84:87], v[36:39], v[0:3]\n\t" \
    "ds_read_b128 v[28:31], %[xp] offset:10240\n\t" \
    "v_mfma_f32_16x16x32_bf16 v[4:7], v[92:95], v[36:39], v[4:7]\n\t" \
    "ds_read_b128 v[32:35], %[xp] offset:11264\n\t" \
    "v_mfma_f32_16x16x32_bf16 v[8:11], v[84:87], v[40:43], v[8:11]\n\t" \
    "s_waitcnt lgkmcnt(4)\n\t" \
    "v_mfma_f32_16x16x32_bf16 v[12:15], v[92:95], v[40:43], v[12:15]\n\t" \
    "v_mfma_f32_16x16x32_bf16 v[0:3], v[88:91], v[44:47], v[0:3]\n\t" \
    "ds_read_b128 v[36:39], %[xp] offset:12288\n\t" \
    "v_mfma_f32_16x16x32_bf16 v[4:7], v[96:99], v[44:47], v[4:7]\n\t" \
    "ds_read_b128 v[40:43], %[xp] offset:13312\n\t" \
    "v_mfma_f32_16x16x32_bf16 v[8:11], v[88:91], v[48:51], v[8:11]\n\t" \
    "s_waitcnt lgkmcnt(4)\n\t" \
    "v_mfma_f32_16x16x32_bf16 v[12:15], v[96:99], v[48:51], v[12:15]\n\t" \
    "v_mfma_f32_16x16x32_bf16 v[0:3], v[100:103], v[20:23], v[0:3]\n\t" \
    "ds_read_b128 v[44:47], %[xp] offset:14336\n\t" \
    "v_mfma_f32_16x16x32_bf16 v[4:7], v[108:111], v[20:23], v[4:7]\n\t" \
    "ds_read_b128 v[48:51], %[xp] offset:15360\n\t" \
    "v_mfma_f32_16x16x32_bf16 v[8:11], v[100:103], v[24:27], v[8:11]\n\t" \
    "s_waitcnt lgkmcnt(4)\n\t" \
    "v_mfma_f32_16x16x32_bf16 v[12:15], v[108:111], v[24:27], v[12:15]\n\t" \
    "v_mfma_f32_16x16x32_bf16 v[0:3], v[104:107], v[28:31], v[0:3]\n\t" \
    "ds_read_b128 v[20:23], %[xp] offset:16384\n\t" \
    "v_mfma_f32_16x16x32_bf16 v[4:7], v[112:115], v[28:31], v[4:7]\n\t" \
    "ds_read_b128 v[24:27], %[xp] offset:17408\n\t" \
    "v_mfma_f32_16x16x32_bf16 v[8:11], v[104:107], v[32:35], v[8:11]\n\t" \
    "s_waitcnt lgkmcnt(4)\n\t" \
    "v_mfma_f32_16x16x32_bf16 v[12:15], v[112:115], v[32:35], v[12:15]\n\t" \
    "v_mfma_f32_16x16x32_bf16 v[0:3], v[116:119], v[36:39], v[0:3]\n\t" \
    "ds_read_b128 v[28:31], %[xp] offset:18432\n\t" \
    "v_mfma_f32_16x16x32_bf16 v[4:7], v[124:127], v[36:39], v[4:7]\n\t" \
    "ds_read_b128 v[32:35], %[xp] offset:19456\n\t" \
    "v_mfma_f32_16x16x32_bf16 v[8:11], v[116:119], v[40:43], v[8:11]\n\t" \
    "s_waitcnt lgkmcnt(4)\n\t" \
    "v_mfma_f32_16x16x32_bf16 v[12:15], v[124:127], v[40:43], v[12:15]\n\t" \
    "v_mfma_f32_16x16x32_bf16 v[0:3], v[120:123], v[44:47], v[0:3]\n\t" \
    "ds_read_b128 v[36:39], %[xp] offset:20480\n\t" \
    "v_mfma_f32_16x16x32_bf16 v[4:7], v[128:131], v[44:47], v[4:7]\n\t" \
    "ds_read_b128 v[40:43], %[xp] offset:21504\n\t" \
    "v_mfma_f32_16x16x32_bf16 v[8:11], v[120:123], v[48:51], v[8:11]\n\t" \
    "s_waitcnt lgkmcnt(4)\n\t" \
    "v_mfma_f32_16x16x32_bf16 v[12:15], v[128:131], v[48:51], v[12:15]\n\t" \
    "s_waitcnt vmcnt(4)\n\t" \
    "s_barrier\n\t" \
    "s_setprio 0\n\t" \
    "s_sub_u32 %[a], %[t], 1\n\t" \
    "v_mov_b32 v53, %[tag]\n\t" \
    "v_mov_b32 v52, %[a]\n\t" \
    "s_cmp_gt_u32 %[t], 1\n\t" \
    "s_cselect_b64 exec, %[fm], 0\n\t" \
    "buffer_store_dwordx2 v[52:53], %[l16], %[rs], %[fo] offen sc1\n\t" \
    "s_mov_b64 exec, %[pm]\n\t" \
    "s_mov_b32 m0, %[pd]\n\t" \
    "s_add_u32 %[a], %[sx], %[po]\n\t" \
    "buffer_load_dwordx4 %[l16], %[rs], %[a] offen sc1 lds\n\t" \
    "s_mov_b64 exec, -1\n\t" \
    "s_add_u32 m0, %[sd], 0\n\t" \
    "s_add_u32 %[a], %[sx], 524288\n\t" \
    "buffer_load_dwordx4 %[l16], %[rs], %[a] offen offset:0 sc1 lds\n\t" \
    "buffer_load_dwordx4 %[l16], %[rs], %[a] offen offset:1024 sc1 lds\n\t" \
    "buffer_load_dwordx4 %[l16], %[rs], %[a] offen offset:2048 sc1 lds\n\t" \
    "buffer_load_dwordx4 %[l16], %[rs], %[a] offen offset:3072 sc1 lds\n\t" \
    "v_mfma_f32_16x16x32_bf16 v[0:3], v[132:135], v[20:23], v[0:3]\n\t" \
    "ds_read_b128 v[44:47], %[xp] offset:22528\n\t" \
    "v_mfma_f32_16x16x32_bf16 v[4:7], v[140:143], v[20:23], v[4:7]\n\t" \
    "ds_read_b128 v[48:51], %[xp] offset:23552\n\t" \
    "v_mfma_f32_16x16x32_bf16 v[8:11], v[132:135], v[24:27], v[8:11]\n\t" \
    "s_waitcnt lgkmcnt(4)\n\t" \
    "v_mfma_f32_16x16x32_bf16 v[12:15], v[140:143], v[24:27], v[12:15]\n\t" \
    "v_mfma_f32_16x16x32_bf16 v[0:3], v[136:139], v[28:31], v[0:3]\n\t" \
    "ds_read_b128 v[20:23], %[xp] offset:24576\n\t" \
    "v_mfma_f32_16x16x32_bf16 v[4:7], v[144:147], v[28:31], v[4:7]\n\t" \
    "ds_read_b128 v[24:27], %[xp] offset:25600\n\t" \
    "v_mfma_f32_16x16x32_bf16 v[8:11], v[136:139], v[32:35], v[8:11]\n\t" \
    "s_waitcnt lgkmcnt(4)\n\t" \
    "v_mfma_f32_16x16x32_bf16 v[12:15], v[144:147], v[32:35], v[12:15]\n\t" \
    "v_mfma_f32_16x16x32_bf16 v[0:3], v[148:151], v[36:39], v[0:3]\n\t" \
    "ds_read_b128 v[28:31], %[xp] offset:26624\n\t" \
    "v_mfma_f32_16x16x32_bf16 v[4:7], v[156:159], v[36:39], v[4:7]\n\t" \
    "ds_read_b128 v[32:35], %[xp] offset:27648\n\t" \
    "v_mfma_f32_16x16x32_bf16 v[8:11], v[148:151], v[40:43], v[8:11]\n\t" \
    "s_waitcnt lgkmcnt(4)\n\t" \
    "v_mfma_f32_16x16x32_bf16 v[12:15], v[156:159], v[40:43], v[12:15]\n\t" \
    "v_mfma_f32_16x16x32_bf16 v[0:3], v[152:155], v[44:47], v[0:3]\n\t" \
    "ds_read_b128 v[36:39], %[xp] offset:28672\n\t" \
    "v_mfma_f32_16x16x32_bf16 v[4:7], v[160:163], v[44:47], v[4:7]\n\t" \
    "ds_read_b128 v[40:43], %[xp] offset:29696\n\t" \
    "v_mfma_f32_16x16x32_bf16 v[8:11], v[152:155], v[48:51], v[8:11]\n\t" \
    "s_waitcnt lgkmcnt(4)\n\t" \
    "v_mfma_f32_16x16x32_bf16 v[12:15], v[160:163], v[48:51], v[12:15]\n\t" \
    "v_mfma_f32_16x16x32_bf16 v[0:3], v[164:167], v[20:23], v[0:3]\n\t" \
    "ds_read_b128 v[44:47], %[xp] offset:30720\n\t" \
    "v_mfma_f32_16x16x32_bf16 v[4:7], v[172:175], v[20:23], v[4:7]\n\t" \
    "ds_read_b128 v[48:51], %[xp] offset:31744\n\t" \
    "v_mfma_f32_16x16x32_bf16 v[8:11], v[164:167], v[24:27], v[8:11]\n\t" \
    "s_waitcnt lgkmcnt(4)\n\t" \
    "v_mfma_f32_16x16x32_bf16 v[12:15], v[172:175], v[24:27], v[12:15]\n\t" \
    "v_mfma_f32_16x16x32_bf16 v[0:3], v[168:171], v[28:31], v[0:3]\n\t" \
    "ds_read_b128 v[20:23], %[xp] offset:32768\n\t" \
    "v_mfma_f32_16x16x32_bf16 v[4:7], v[176:179], v[28:31], v[4:7]\n\t" \
    "ds_read_b128 v[24:27], %[xp] offset:33792\n\t" \
    "v_mfma_f32_16x16x32_bf16 v[8:11], v[168:171], v[32:35], v[8:11]\n\t" \
    "s_waitcnt lgkmcnt(4)\n\t" \
    "v_mfma_f32_16x16x32_bf16 v[12:15], v[176:179], v[32:35], v[12:15]\n\t" \
    "v_mfma_f32_16x16x32_bf16 v[0:3], v[180:183], v[36:39], v[0:3]\n\t" \
    "ds_read_b128 v[28:31], %[xp] offset:34816\n\t" \
    "v_mfma_f32_16x16x32_bf16 v[4:7], v[188:191], v[36:39], v[4:7]\n\t" \
    "ds_read_b128 v[32:35], %[xp] offset:35840\n\t" \
    "v_mfma_f32_16x16x32_bf16 v[8:11], v[180:183], v[40:43], v[8:11]\n\t" \
    "s_waitcnt lgkmcnt(4)\n\t" \
    "v_mfma_f32_16x16x32_bf16 v[12:15], v[188:191], v[40:43], v[12:15]\n\t" \
    "v_mfma_f32_16x16x32_bf16 v[0:3], v[184:187], v[44:47], v[0:3]\n\t" \
    "ds_read_b128 v[36:39], %[xp] offset:36864\n\t" \
    "v_mfma_f32_16x16x32_bf16 v[4:7], v[192:195], v[44:47], v[4:7]\n\t" \
    "ds_read_b128 v[40:43], %[xp] offset:37888\n\t" \
    "v_mfma_f32_16x16x32_bf16 v[8:11], v[184:187], v[48:51], v[8:11]\n\t" \
    "s_waitcnt lgkmcnt(4)\n\t" \
    "v_mfma_f32_16x16x32_bf16 v[12:15], v[192:195], v[48:51], v[12:15]\n\t" \
    "s_add_u32 %[sx], %[sx], 262144\n\t" \
    "s_add_u32 %[t], %[t], 1\n\t" \
    "s_waitcnt vmcnt(4)\n\t" \
    "s_barrier\n\t" \
    "s_setprio 0\n\t" \
    "s_nop 3\n\t" \
    "ds_write_b128 %[pa], v[0:3] offset:0\n\t" \
    "ds_write_b128 %[pa], v[4:7] offset:1024\n\t" \
    "ds_write_b128 %[pa], v[8:11] offset:2048\n\t" \
    "ds_write_b128 %[pa], v[12:15] offset:3072\n\t" \
    "s_sub_u32 %[a], %[t], 1\n\t" \
    "v_mov_b32 v53, %[tag]\n\t" \
    "v_mov_b32 v52, %[a]\n\t" \
    "s_sub_u32 %[b], %[t], 2\n\t" \
    "s_cmp_lt_u32 %[b], " STR(SQUICK) "\n\t" \
    "s_cselect_b64 exec, %[fm], 0\n\t" \
    "buffer_store_dwordx2 v[52:53], %[l16], %[rs], %[fo] offen sc1\n\t" \
    "s_mov_b64 exec, -1\n\t" \
    "s_add_u32 m0, %[sd], 16384\n\t" \
    "s_add_u32 %[a], %[sx], 278528\n\t" \
    "s_waitcnt lgkmcnt(0)\n\t" \
    "buffer_load_dwordx4 %[l16], %[rs], %[a] offen offset:0 sc1 lds\n\t" \
    "buffer_load_dwordx4 %[l16], %[rs], %[a] offen offset:1024 sc1 lds\n\t" \
    "buffer_load_dwordx4 %[l16], %[rs], %[a] offen offset:2048 sc1 lds\n\t" \
    "buffer_load_dwordx4 %[l16], %[rs], %[a] offen offset:3072 sc1 lds\n\t" \
    "v_mfma_f32_16x16x32_bf16 v[196:199], v[68:71], v[20:23], 0\n\t" \
    "ds_read_b128 v[44:47], %[xp] offset:38912\n\t" \
    "v_mfma_f32_16x16x32_bf16 v[200:203], v[76:79], v[20:23], 0\n\t" \
    "ds_read_b128 v[48:51], %[xp] offset:39936\n\t" \
    "v_mfma_f32_16x16x32_bf16 v[204:207], v[68:71], v[24:27], 0\n\t" \
    "v_mfma_f32_16x16x32_bf16 v[208:211], v[76:79], v[24:27], 0\n\t" \
    "v_mfma_f32_16x16x32_bf16 v[196:199], v[72:75], v[28:31], v[196:199]\n\t" \
    "ds_read_b128 v[20:23], %[xp] offset:40960\n\t" \
    "v_mfma_f32_16x16x32_bf16 v[200:203], v[80:83], v[28:31], v[200:203]\n\t" \
    "ds_read_b128 v[24:27], %[xp] offset:41984\n\t" \
    "v_mfma_f32_16x16x32_bf16 v[204:207], v[72:75], v[32:35], v[204:207]\n\t" \
    "v_mfma_f32_16x16x32_bf16 v[208:211], v[80:83], v[32:35], v[208:211]\n\t" \
    "v_mfma_f32_16x16x32_bf16 v[196:199], v[84:87], v[36:39], v[196:199]\n\t" \
    "ds_read_b128 v[28:31], %[xp] offset:43008\n\t" \
    "v_mfma_f32_16x16x32_bf16 v[200:203], v[92:95], v[36:39], v[200:203]\n\t" \
    "ds_read_b128 v[32:35], %[xp] offset:44032\n\t" \
    "v_mfma_f32_16x16x32_bf16 v[204:207], v[84:87], v[40:43], v[204:207]\n\t" \
    "s_waitcnt lgkmcnt(4)\n\t" \
    "v_mfma_f32_16x16x32_bf16 v[208:211], v[92:95], v[40:43], v[208:211]\n\t" \
    "v_mfma_f32_16x16x32_bf16 v[196:199], v[88:91], v[44:47], v[196:199]\n\t" \
    "ds_read_b128 v[36:39], %[xp] offset:45056\n\t" \
    "v_mfma_f32_16x16x32_bf16 v[200:203], v[96:99], v[44:47], v[200:203]\n\t" \
    "ds_read_b128 v[40:43], %[xp] offset:46080\n\t" \
    "v_mfma_f32_16x16x32_bf16 v[204:207], v[88:91], v[48:51], v[204:207]\n\t" \
    "s_waitcnt lgkmcnt(4)\n\t" \
    "v_mfma_f32_16x16x32_bf16 v[208:211], v[96:99], v[48:51], v[208:211]\n\t" \
    "v_mfma_f32_16x16x32_bf16 v[196:199], v[100:103], v[20:23], v[196:199]\n\t" \
    "ds_read_b128 v[44:47], %[xp] offset:47104\n\t" \
    "v_mfma_f32_16x16x32_bf16 v[200:203], v[108:111], v[20:23], v[200:203]\n\t" \
    "ds_read_b128 v[48:51], %[xp] offset:48128\n\t" \
    "v_mfma_f32_16x16x32_bf16 v[204:207], v[100:103], v[24:27], v[204:207]\n\t" \
    "s_waitcnt lgkmcnt(4)\n\t" \
    "v_mfma_f32_16x16x32_bf16 v[208:211], v[108:111], v[24:27], v[208:211]\n\t" \
    "v_mfma_f32_16x16x32_bf16 v[196:199], v[104:107], v[28:31], v[196:199]\n\t" \
    "ds_read_b128 v[20:23], %[xp] offset:49152\n\t" \
    "v_mfma_f32_16x16x32_bf16 v[200:203], v[112:115], v[28:31], v[200:203]\n\t" \
    "ds_read_b128 v[24:27], %[xp] offset:50176\n\t" \
    "v_mfma_f32_16x16x32_bf16 v[204:207], v[104:107], v[32:35], v[204:207]\n\t" \
    "s_waitcnt lgkmcnt(4)\n\t" \
    "v_mfma_f32_16x16x32_bf16 v[208:211], v[112:115], v[32:35], v[208:211]\n\t" \
    "v_mfma_f32_16x16x32_bf16 v[196:199], v[116:119], v[36:39], v[196:199]\n\t" \
    "ds_read_b128 v[28:31], %[xp] offset:51200\n\t" \
    "v_mfma_f32_16x16x32_bf16 v[200:203], v[124:127], v[36:39], v[200:203]\n\t" \
    "ds_read_b128 v[32:35], %[xp] offset:52224\n\t" \
    "v_mfma_f32_16x16x32_bf16 v[204:207], v[116:119], v[40:43], v[204:207]\n\t" \
    "s_waitcnt lgkmcnt(4)\n\t" \
    "v_mfma_f32_16x16x32_bf16 v[208:211], v[124:127], v[40:43], v[208:211]\n\t" \
    "v_mfma_f32_16x16x32_bf16 v[196:199], v[120:123], v[44:47], v[196:199]\n\t" \
    "ds_read_b128 v[36:39], %[xp] offset:53248\n\t" \
    "v_mfma_f32_16x16x32_bf16 v[200:203], v[128:131], v[44:47], v[200:203]\n\t" \
    "ds_read_b128 v[40:43], %[xp] offset:54272\n\t" \
    "v_mfma_f32_16x16x32_bf16 v[204:207], v[120:123], v[48:51], v[204:207]\n\t" \
    "s_waitcnt lgkmcnt(4)\n\t" \
    "v_mfma_f32_16x16x32_bf16 v[208:211], v[128:131], v[48:51], v[208:211]\n\t" \
    "s_waitcnt vmcnt(4)\n\t" \
    "s_barrier\n\t" \
    "s_setprio 0\n\t" \
    "s_sub_u32 %[a], %[t], 1\n\t" \
    "v_mov_b32 v53, %[tag]\n\t" \
    "v_mov_b32 v52, %[a]\n\t" \
    "s_cmp_gt_u32 %[t], 1\n\t" \
    "s_cselect_b64 exec, %[fm], 0\n\t" \
    "buffer_store_dwordx2 v[52:53], %[l16], %[rs], %[fo] offen sc1\n\t" \
    "s_mov_b64 exec, %[pm]\n\t" \
    "s_mov_b32 m0, %[pd]\n\t" \
    "s_add_u32 %[a], %[sx], %[po]\n\t" \
    "buffer_load_dwordx4 %[l16], %[rs], %[a] offen sc1 lds\n\t" \
    "s_mov_b64 exec, -1\n\t" \
    "s_add_u32 m0, %[sd], 32768\n\t" \
    "s_add_u32 %[a], %[sx], 524288\n\t" \
    "buffer_load_dwordx4 %[l16], %[rs], %[a] offen offset:0 sc1 lds\n\t" \
    "buffer_load_dwordx4 %[l16], %[rs], %[a] offen offset:1024 sc1 lds\n\t" \
    "buffer_load_dwordx4 %[l16], %[rs], %[a] offen offset:2048 sc1 lds\n\t" \
    "buffer_load_dwordx4 %[l16], %[rs], %[a] offen offset:3072 sc1 lds\n\t" \
    "v_mfma_f32_16x16x32_bf16 v[196:199], v[132:135], v[20:23], v[196:199]\n\t" \
    "ds_read_b128 v[44:47], %[xp] offset:55296\n\t" \
    "v_mfma_f32_16x16x32_bf16 v[200:203], v[140:143], v[20:23], v[200:203]\n\t" \
    "ds_read_b128 v[48:51], %[xp] offset:56320\n\t" \
    "v_mfma_f32_16x16x32_bf16 v[204:207], v[132:135], v[24:27], v[204:207]\n\t" \
    "s_waitcnt lgkmcnt(4)\n\t" \
    "v_mfma_f32_16x16x32_bf16 v[208:211], v[140:143], v[24:27], v[208:211]\n\t" \
    "v_mfma_f32_16x16x32_bf16 v[196:199], v[136:139], v[28:31], v[196:199]\n\t" \
    "ds_read_b128 v[20:23], %[xp] offset:57344\n\t" \
    "v_mfma_f32_16x16x32_bf16 v[200:203], v[144:147], v[28:31], v[200:203]\n\t" \
    "ds_read_b128 v[24:27], %[xp] offset:58368\n\t" \
    "v_mfma_f32_16x16x32_bf16 v[204:207], v[136:139], v[32:35], v[204:207]\n\t" \
    "s_waitcnt lgkmcnt(4)\n\t" \
    "v_mfma_f32_16x16x32_bf16 v[208:211], v[144:147], v[32:35], v[208:211]\n\t" \
    "v_mfma_f32_16x16x32_bf16 v[196:199], v[148:151], v[36:39], v[196:199]\n\t" \
    "ds_read_b128 v[28:31], %[xp] offset:59392\n\t" \
    "v_mfma_f32_16x16x32_bf16 v[200:203], v[156:159], v[36:39], v[200:203]\n\t" \
    "ds_read_b128 v[32:35], %[xp] offset:60416\n\t" \
    "v_mfma_f32_16x16x32_bf16 v[204:207], v[148:151], v[40:43], v[204:207]\n\t" \
    "s_waitcnt lgkmcnt(4)\n\t" \
    "v_mfma_f32_16x16x32_bf16 v[208:211], v[156:159], v[40:43], v[208:211]\n\t" \
    "v_mfma_f32_16x16x32_bf16 v[196:199], v[152:155], v[44:47], v[196:199]\n\t" \
    "ds_read_b128 v[36:39], %[xp] offset:61440\n\t" \
    "v_mfma_f32_16x16x32_bf16 v[200:203], v[160:163], v[44:47], v[200:203]\n\t" \
    "ds_read_b128 v[40:43], %[xp] offset:62464\n\t" \
    "v_mfma_f32_16x16x32_bf16 v[204:207], v[152:155], v[48:51], v[204:207]\n\t" \
    "s_waitcnt lgkmcnt(4)\n\t" \
    "v_mfma_f32_16x16x32_bf16 v[208:211], v[160:163], v[48:51], v[208:211]\n\t" \
    "v_mfma_f32_16x16x32_bf16 v[196:199], v[164:167], v[20:23], v[196:199]\n\t" \
    "ds_read_b128 v[44:47], %[xp] offset:63488\n\t" \
    "v_mfma_f32_16x16x32_bf16 v[200:203], v[172:175], v[20:23], v[200:203]\n\t" \
    "ds_read_b128 v[48:51], %[xp] offset:64512\n\t" \
    "v_mfma_f32_16x16x32_bf16 v[204:207], v[164:167], v[24:27], v[204:207]\n\t" \
    "s_waitcnt lgkmcnt(4)\n\t" \
    "v_mfma_f32_16x16x32_bf16 v[208:211], v[172:175], v[24:27], v[208:211]\n\t" \
    "v_mfma_f32_16x16x32_bf16 v[196:199], v[168:171], v[28:31], v[196:199]\n\t" \
    "ds_read_b128 v[20:23], %[xp] offset:0\n\t" \
    "v_mfma_f32_16x16x32_bf16 v[200:203], v[176:179], v[28:31], v[200:203]\n\t" \
    "ds_read_b128 v[24:27], %[xp] offset:1024\n\t" \
    "v_mfma_f32_16x16x32_bf16 v[204:207], v[168:171], v[32:35], v[204:207]\n\t" \
    "s_waitcnt lgkmcnt(4)\n\t" \
    "v_mfma_f32_16x16x32_bf16 v[208:211], v[176:179], v[32:35], v[208:211]\n\t" \
    "v_mfma_f32_16x16x32_bf16 v[196:199], v[180:183], v[36:39], v[196:199]\n\t" \
    "ds_read_b128 v[28:31], %[xp] offset:2048\n\t" \
    "v_mfma_f32_16x16x32_bf16 v[200:203], v[188:191], v[36:39], v[200:203]\n\t" \
    "ds_read_b128 v[32:35], %[xp] offset:3072\n\t" \
    "v_mfma_f32_16x16x32_bf16 v[204:207], v[180:183], v[40:43], v[204:207]\n\t" \
    "s_waitcnt lgkmcnt(4)\n\t" \
    "v_mfma_f32_16x16x32_bf16 v[208:211], v[188:191], v[40:43], v[208:211]\n\t" \
    "v_mfma_f32_16x16x32_bf16 v[196:199], v[184:187], v[44:47], v[196:199]\n\t" \
    "ds_read_b128 v[36:39], %[xp] offset:4096\n\t" \
    "v_mfma_f32_16x16x32_bf16 v[200:203], v[192:195], v[44:47], v[200:203]\n\t" \
    "ds_read_b128 v[40:43], %[xp] offset:5120\n\t" \
    "v_mfma_f32_16x16x32_bf16 v[204:207], v[184:187], v[48:51], v[204:207]\n\t" \
    "s_waitcnt lgkmcnt(4)\n\t" \
    "v_mfma_f32_16x16x32_bf16 v[208:211], v[192:195], v[48:51], v[208:211]\n\t" \
    "s_add_u32 %[sx], %[sx], 262144\n\t" \
    "s_add_u32 %[t], %[t], 1\n\t" \
    "s_waitcnt vmcnt(4)\n\t" \
    "s_barrier\n\t" \
    "s_cmp_lt_u32 %[t], %[te]\n\t" \
    "s_cbranch_scc1 L0_%=\n\t" \
    "s_cmp_eq_u32 %[fin], %[te]\n\t" \
    "s_cbranch_scc0 E_%=\n\t" \
    "s_nop 4\n\t" \
    "ds_write_b128 %[pa], v[196:199] offset:0\n\t" \
    "ds_write_b128 %[pa], v[200:203] offset:1024\n\t" \
    "ds_write_b128 %[pa], v[204:207] offset:2048\n\t" \
    "ds_write_b128 %[pa], v[208:211] offset:3072\n\t" \
    "s_waitcnt lgkmcnt(0)\n\t" \
    "s_barrier\n\t" \
    "s_branch E_%=\n\t" \
    "L1_%=:\n\t" \
    "s_setprio 1\n\t" \
    "s_add_u32 m0, %[sd], 49152\n\t" \
    "s_add_u32 %[a], %[sx], 278528\n\t" \
    "v_mfma_f32_16x16x32_bf16 v[0:3], v[68:71], v[20:23], 0\n\t" \
    "ds_read_b128 v[44:47], %[xp] offset:6144\n\t" \
    "v_mfma_f32_16x16x32_bf16 v[4:7], v[76:79], v[20:23], 0\n\t" \
    "ds_read_b128 v[48:51], %[xp] offset:7168\n\t" \
    "v_mfma_f32_16x16x32_bf16 v[8:11], v[68:71], v[24:27], 0\n\t" \
    "s_waitcnt lgkmcnt(4)\n\t" \
    "v_mfma_f32_16x16x32_bf16 v[12:15], v[76:79], v[24:27], 0\n\t" \
    "v_mfma_f32_16x16x32_bf16 v[0:3], v[72:75], v[28:31], v[0:3]\n\t" \
    "ds_read_b128 v[20:23], %[xp] offset:8192\n\t" \
    "v_mfma_f32_16x16x32_bf16 v[4:7], v[80:83], v[28:31], v[4:7]\n\t" \
    "ds_read_b128 v[24:27], %[xp] offset:9216\n\t" \
    "v_mfma_f32_16x16x32_bf16 v[8:11], v[72:75], v[32:35], v[8:11]\n\t" \
    "s_waitcnt lgkmcnt(4)\n\t" \
    "v_mfma_f32_16x16x32_bf16 v[12:15], v[80:83], v[32:35], v[12:15]\n\t" \
    "v_mfma_f32_16x16x32_bf16 v[0:3], v[84:87], v[36:39], v[0:3]\n\t" \
    "ds_read_b128 v[28:31], %[xp] offset:10240\n\t" \
    "v_mfma_f32_16x16x32_bf16 v[4:7], v[92:95], v[36:39], v[4:7]\n\t" \
    "ds_read_b128 v[32:35], %[xp] offset:11264\n\t" \
    "v_mfma_f32_16x16x32_bf16 v[8:11], v[84:87], v[40:43], v[8:11]\n\t" \
    "s_waitcnt lgkmcnt(4)\n\t" \
    "v_mfma_f32_16x16x32_bf16 v[12:15], v[92:95], v[40:43], v[12:15]\n\t" \
    "v_mfma_f32_16x16x32_bf16 v[0:3], v[88:91], v[44:47], v[0:3]\n\t" \
    "ds_read_b128 v[36:39], %[xp] offset:12288\n\t" \
    "v_mfma_f32_16x16x32_bf16 v[4:7], v[96:99], v[44:47], v[4:7]\n\t" \
    "ds_read_b128 v[40:43], %[xp] offset:13312\n\t" \
    "v_mfma_f32_16x16x32_bf16 v[8:11], v[88:91], v[48:51], v[8:11]\n\t" \
    "s_waitcnt lgkmcnt(4)\n\t" \
    "v_mfma_f32_16x16x32_bf16 v[12:15], v[96:99], v[48:51], v[12:15]\n\t" \
    "v_mfma_f32_16x16x32_bf16 v[0:3], v[100:103], v[20:23], v[0:3]\n\t" \
    "ds_read_b128 v[44:47], %[xp] offset:14336\n\t" \
    "v_mfma_f32_16x16x32_bf16 v[4:7], v[108:111], v[20:23], v[4:7]\n\t" \
    "ds_read_b128 v[48:51], %[xp] offset:15360\n\t" \
    "v_mfma_f32_16x16x32_bf16 v[8:11], v[100:103], v[24:27], v[8:11]\n\t" \
    "s_waitcnt lgkmcnt(4)\n\t" \
    "v_mfma_f32_16x16x32_bf16 v[12:15], v[108:111], v[24:27], v[12:15]\n\t" \
    "buffer_load_dwordx4 %[l16], %[rs], %[a] offen offset:0 sc1 lds\n\t" \
    "v_mfma_f32_16x16x32_bf16 v[0:3], v[104:107], v[28:31], v[0:3]\n\t" \
    "ds_read_b128 v[20:23], %[xp] offset:16384\n\t" \
    "v_mfma_f32_16x16x32_bf16 v[4:7], v[112:115], v[28:31], v[4:7]\n\t" \
    "ds_read_b128 v[24:27], %[xp] offset:17408\n\t" \
    "v_mfma_f32_16x16x32_bf16 v[8:11], v[104:107], v[32:35], v[8:11]\n\t" \
    "s_waitcnt lgkmcnt(4)\n\t" \
    "v_mfma_f32_16x16x32_bf16 v[12:15], v[112:115], v[32:35], v[12:15]\n\t" \
    "buffer_load_dwordx4 %[l16], %[rs], %[a] offen offset:1024 sc1 lds\n\t" \
    "v_mfma_f32_16x16x32_bf16 v[0:3], v[116:119], v[36:39], v[0:3]\n\t" \
    "ds_read_b128 v[28:31], %[xp] offset:18432\n\t" \
    "v_mfma_f32_16x16x32_bf16 v[4:7], v[124:127], v[36:39], v[4:7]\n\t" \
    "ds_read_b128 v[32:35], %[xp] offset:19456\n\t" \
    "v_mfma_f32_16x16x32_bf16 v[8:11], v[116:119], v[40:43], v[8:11]\n\t" \
    "s_waitcnt lgkmcnt(4)\n\t" \
    "v_mfma_f32_16x16x32_bf16 v[12:15], v[124:127], v[40:43], v[12:15]\n\t" \
    "buffer_load_dwordx4 %[l16], %[rs], %[a] offen offset:2048 sc1 lds\n\t" \
    "v_mfma_f32_16x16x32_bf16 v[0:3], v[120:123], v[44:47], v[0:3]\n\t" \
    "ds_read_b128 v[36:39], %[xp] offset:20480\n\t" \
    "v_mfma_f32_16x16x32_bf16 v[4:7], v[128:131], v[44:47], v[4:7]\n\t" \
    "ds_read_b128 v[40:43], %[xp] offset:21504\n\t" \
    "v_mfma_f32_16x16x32_bf16 v[8:11], v[120:123], v[48:51], v[8:11]\n\t" \
    "s_waitcnt lgkmcnt(4)\n\t" \
    "v_mfma_f32_16x16x32_bf16 v[12:15], v[128:131], v[48:51], v[12:15]\n\t" \
    "buffer_load_dwordx4 %[l16], %[rs], %[a] offen offset:3072 sc1 lds\n\t" \
    "s_waitcnt vmcnt(4)\n\t" \
    "s_barrier\n\t" \
    "s_setprio 1\n\t" \
    "s_add_u32 m0, %[sd], 0\n\t" \
    "s_add_u32 %[a], %[sx], 524288\n\t" \
    "s_add_u32 %[b], %[oo], 0x1000\n\t" \
    "s_cmp_lg_u32 %[t], 0\n\t" \
    "s_cselect_b64 %[em], -1, 0\n\t" \
    "v_mfma_f32_16x16x32_bf16 v[0:3], v[132:135], v[20:23], v[0:3]\n\t" \
    "ds_read_b128 v[44:47], %[xp] offset:22528\n\t" \
    "v_mfma_f32_16x16x32_bf16 v[4:7], v[140:143], v[20:23], v[4:7]\n\t" \
    "ds_read_b128 v[48:51], %[xp] offset:23552\n\t" \
    "v_mfma_f32_16x16x32_bf16 v[8:11], v[132:135], v[24:27], v[8:11]\n\t" \
    "ds_read_b128 v[52:55], %[pa] offset:0\n\t" \
    "s_waitcnt lgkmcnt(5)\n\t" \
    "v_mfma_f32_16x16x32_bf16 v[12:15], v[140:143], v[24:27], v[12:15]\n\t" \
    "buffer_load_dwordx4 %[l16], %[rs], %[a] offen offset:0 sc1 lds\n\t" \
    "v_mfma_f32_16x16x32_bf16 v[0:3], v[136:139], v[28:31], v[0:3]\n\t" \
    "ds_read_b128 v[20:23], %[xp] offset:24576\n\t" \
    "v_mfma_f32_16x16x32_bf16 v[4:7], v[144:147], v[28:31], v[4:7]\n\t" \
    "ds_read_b128 v[24:27], %[xp] offset:25600\n\t" \
    "v_mfma_f32_16x16x32_bf16 v[8:11], v[136:139], v[32:35], v[8:11]\n\t" \
    "ds_read_b128 v[56:59], %[pa] offset:1024\n\t" \
    "s_waitcnt lgkmcnt(6)\n\t" \
    "v_mfma_f32_16x16x32_bf16 v[12:15], v[144:147], v[32:35], v[12:15]\n\t" \
    "buffer_load_dwordx4 %[l16], %[rs], %[a] offen offset:1024 sc1 lds\n\t" \
    "v_mfma_f32_16x16x32_bf16 v[0:3], v[148:151], v[36:39], v[0:3]\n\t" \
    "ds_read_b128 v[28:31], %[xp] offset:26624\n\t" \
    "v_mfma_f32_16x16x32_bf16 v[4:7], v[156:159], v[36:39], v[4:7]\n\t" \
    "ds_read_b128 v[32:35], %[xp] offset:27648\n\t" \
    "v_mfma_f32_16x16x32_bf16 v[8:11], v[148:151], v[40:43], v[8:11]\n\t" \
    "ds_read_b128 v[60:63], %[pa] offset:2048\n\t" \
    "s_waitcnt lgkmcnt(7)\n\t" \
    "v_mfma_f32_16x16x32_bf16 v[12:15], v[156:159], v[40:43], v[12:15]\n\t" \
    "buffer_load_dwordx4 %[l16], %[rs], %[a] offen offset:2048 sc1 lds\n\t" \
    "v_mfma_f32_16x16x32_bf16 v[0:3], v[152:155], v[44:47], v[0:3]\n\t" \
    "ds_read_b128 v[36:39], %[xp] offset:28672\n\t" \
    "s_waitcnt lgkmcnt(7)\n\t" \
    "v_add_f32 v196, v196, v52\n\t" \
    "v_mfma_f32_16x16x32_bf16 v[4:7], v[160:163], v[44:47], v[4:7]\n\t" \
    "ds_read_b128 v[40:43], %[xp] offset:29696\n\t" \
    "v_add_f32 v197, v197, v53\n\t" \
    "v_mfma_f32_16x16x32_bf16 v[8:11], v[152:155], v[48:51], v[8:11]\n\t" \
    "ds_read_b128 v[64:67], %[pa] offset:3072\n\t" \
    "s_waitcnt lgkmcnt(7)\n\t" \
    "v_mfma_f32_16x16x32_bf16 v[12:15], v[160:163], v[48:51], v[12:15]\n\t" \
    "buffer_load_dwordx4 %[l16], %[rs], %[a] offen offset:3072 sc1 lds\n\t" \
    "v_add_f32 v198, v198, v54\n\t" \
    "v_add_f32 v199, v199, v55\n\t" \
    "v_mfma_f32_16x16x32_bf16 v[0:3], v[164:167], v[20:23], v[0:3]\n\t" \
    "ds_read_b128 v[44:47], %[xp] offset:30720\n\t" \
    "s_waitcnt lgkmcnt(7)\n\t" \
    "v_add_f32 v200, v200, v56\n\t" \
    "v_mfma_f32_16x16x32_bf16 v[4:7], v[172:175], v[20:23], v[4:7]\n\t" \
    "ds_read_b128 v[48:51], %[xp] offset:31744\n\t" \
    "v_add_f32 v201, v201, v57\n\t" \
    "v_mfma_f32_16x16x32_bf16 v[8:11], v[164:167], v[24:27], v[8:11]\n\t" \
    "v_add_f32 v202, v202, v58\n\t" \
    "s_waitcnt lgkmcnt(6)\n\t" \
    "v_mfma_f32_16x16x32_bf16 v[12:15], v[172:175], v[24:27], v[12:15]\n\t" \
    "v_add_f32 v203, v203, v59\n\t" \
    "s_waitcnt lgkmcnt(5)\n\t" \
    "v_add_f32 v204, v204, v60\n\t" \
    "v_add_f32 v205, v205, v61\n\t" \
    "v_mfma_f32_16x16x32_bf16 v[0:3], v[168:171], v[28:31], v[0:3]\n\t" \
    "ds_read_b128 v[20:23], %[xp] offset:32768\n\t" \
    "v_add_f32 v206, v206, v62\n\t" \
    "v_mfma_f32_16x16x32_bf16 v[4:7], v[176:179], v[28:31], v[4:7]\n\t" \
    "ds_read_b128 v[24:27], %[xp] offset:33792\n\t" \
    "v_add_f32 v207, v207, v63\n\t" \
    "v_mfma_f32_16x16x32_bf16 v[8:11], v[168:171], v[32:35], v[8:11]\n\t" \
    "s_waitcnt lgkmcnt(4)\n\t" \
    "v_add_f32 v208, v208, v64\n\t" \
    "v_mfma_f32_16x16x32_bf16 v[12:15], v[176:179], v[32:35], v[12:15]\n\t" \
    "v_add_f32 v209, v209, v65\n\t" \
    "v_add_f32 v210, v210, v66\n\t" \
    "v_add_f32 v211, v211, v67\n\t" \
    "v_mfma_f32_16x16x32_bf16 v[0:3], v[180:183], v[36:39], v[0:3]\n\t" \
    "ds_read_b128 v[28:31], %[xp] offset:34816\n\t" \
    "v_mfma_f32_16x16x32_bf16 v[4:7], v[188:191], v[36:39], v[4:7]\n\t" \
    "ds_read_b128 v[32:35], %[xp] offset:35840\n\t" \
    "s_mov_b64 exec, %[em]\n\t" \
    "buffer_store_dwordx4 v[196:199], %[l16], %[rs], %[oo] offen offset:0 sc1\n\t" \
    "s_mov_b64 exec, -1\n\t" \
    "v_mfma_f32_16x16x32_bf16 v[8:11], v[180:183], v[40:43], v[8:11]\n\t" \
    "s_waitcnt lgkmcnt(4)\n\t" \
    "v_mfma_f32_16x16x32_bf16 v[12:15], v[188:191], v[40:43], v[12:15]\n\t" \
    "s_mov_b64 exec, %[em]\n\t" \
    "buffer_store_dwordx4 v[200:203], %[l16], %[rs], %[oo] offen offset:1024 sc1\n\t" \
    "s_mov_b64 exec, -1\n\t" \
    "v_mfma_f32_16x16x32_bf16 v[0:3], v[184:187], v[44:47], v[0:3]\n\t" \
    "ds_read_b128 v[36:39], %[xp] offset:36864\n\t" \
    "v_mfma_f32_16x16x32_bf16 v[4:7], v[192:195], v[44:47], v[4:7]\n\t" \
    "ds_read_b128 v[40:43], %[xp] offset:37888\n\t" \
    "s_mov_b64 exec, %[em]\n\t" \
    "buffer_store_dwordx4 v[204:207], %[l16], %[rs], %[oo] offen offset:2048 sc1\n\t" \
    "s_mov_b64 exec, -1\n\t" \
    "v_mfma_f32_16x16x32_bf16 v[8:11], v[184:187], v[48:51], v[8:11]\n\t" \
    "s_waitcnt lgkmcnt(4)\n\t" \
    "v_mfma_f32_16x16x32_bf16 v[12:15], v[192:195], v[48:51], v[12:15]\n\t" \
    "s_mov_b64 exec, %[em]\n\t" \
    "buffer_store_dwordx4 v[208:211], %[l16], %[rs], %[oo] offen offset:3072 sc1\n\t" \
    "s_mov_b64 exec, -1\n\t" \
    "s_add_u32 %[sx], %[sx], 262144\n\t" \
    "s_add_u32 %[t], %[t], 1\n\t" \
    "s_add_u32 %[oo], %[oo], 524288\n\t" \
    "s_waitcnt vmcnt(8)\n\t" \
    "s_cmp_gt_u32 %[t], " STR(SQUICK) " + 1\n\t" \
    "s_cbranch_scc1 B0_%=\n\t" \
    "s_waitcnt vmcnt(0)\n\t" \
    "B0_%=:\n\t" \
    "s_barrier\n\t" \
    "s_setprio 1\n\t" \
    "s_add_u32 m0, %[sd], 16384\n\t" \
    "s_add_u32 %[a], %[sx], 278528\n\t" \
    "v_mfma_f32_16x16x32_bf16 v[196:199], v[68:71], v[20:23], 0\n\t" \
    "ds_read_b128 v[44:47], %[xp] offset:38912\n\t" \
    "v_mfma_f32_16x16x32_bf16 v[200:203], v[76:79], v[20:23], 0\n\t" \
    "ds_read_b128 v[48:51], %[xp] offset:39936\n\t" \
    "v_mfma_f32_16x16x32_bf16 v[204:207], v[68:71], v[24:27], 0\n\t" \
    "s_waitcnt lgkmcnt(4)\n\t" \
    "v_mfma_f32_16x16x32_bf16 v[208:211], v[76:79], v[24:27], 0\n\t" \
    "v_mfma_f32_16x16x32_bf16 v[196:199], v[72:75], v[28:31], v[196:199]\n\t" \
    "ds_read_b128 v[20:23], %[xp] offset:40960\n\t" \
    "v_mfma_f32_16x16x32_bf16 v[200:203], v[80:83], v[28:31], v[200:203]\n\t" \
    "ds_read_b128 v[24:27], %[xp] offset:41984\n\t" \
    "v_mfma_f32_16x16x32_bf16 v[204:207], v[72:75], v[32:35], v[204:207]\n\t" \
    "s_waitcnt lgkmcnt(4)\n\t" \
    "v_mfma_f32_16x16x32_bf16 v[208:211], v[80:83], v[32:35], v[208:211]\n\t" \
    "v_mfma_f32_16x16x32_bf16 v[196:199], v[84:87], v[36:39], v[196:199]\n\t" \
    "ds_read_b128 v[28:31], %[xp] offset:43008\n\t" \
    "v_mfma_f32_16x16x32_bf16 v[200:203], v[92:95], v[36:39], v[200:203]\n\t" \
    "ds_read_b128 v[32:35], %[xp] offset:44032\n\t" \
    "v_mfma_f32_16x16x32_bf16 v[204:207], v[84:87], v[40:43], v[204:207]\n\t" \
    "s_waitcnt lgkmcnt(4)\n\t" \
    "v_mfma_f32_16x16x32_bf16 v[208:211], v[92:95], v[40:43], v[208:211]\n\t" \
    "v_mfma_f32_16x16x32_bf16 v[196:199], v[88:91], v[44:47], v[196:199]\n\t" \
    "ds_read_b128 v[36:39], %[xp] offset:45056\n\t" \
    "v_mfma_f32_16x16x32_bf16 v[200:203], v[96:99], v[44:47], v[200:203]\n\t" \
    "ds_read_b128 v[40:43], %[xp] offset:46080\n\t" \
    "v_mfma_f32_16x16x32_bf16 v[204:207], v[88:91], v[48:51], v[204:207]\n\t" \
    "s_waitcnt lgkmcnt(4)\n\t" \
    "v_mfma_f32_16x16x32_bf16 v[208:211], v[96:99], v[48:51], v[208:211]\n\t" \
    "v_mfma_f32_16x16x32_bf16 v[196:199], v[100:103], v[20:23], v[196:199]\n\t" \
    "ds_read_b128 v[44:47], %[xp] offset:47104\n\t" \
    "v_mfma_f32_16x16x32_bf16 v[200:203], v[108:111], v[20:23], v[200:203]\n\t" \
    "ds_read_b128 v[48:51], %[xp] offset:48128\n\t" \
    "v_mfma_f32_16x16x32_bf16 v[204:207], v[100:103], v[24:27], v[204:207]\n\t" \
    "s_waitcnt lgkmcnt(4)\n\t" \
    "v_mfma_f32_16x16x32_bf16 v[208:211], v[108:111], v[24:27], v[208:211]\n\t" \
    "buffer_load_dwordx4 %[l16], %[rs], %[a] offen offset:0 sc1 lds\n\t" \
    "v_mfma_f32_16x16x32_bf16 v[196:199], v[104:107], v[28:31], v[196:199]\n\t" \
    "ds_read_b128 v[20:23], %[xp] offset:49152\n\t" \
    "v_mfma_f32_16x16x32_bf16 v[200:203], v[112:115], v[28:31], v[200:203]\n\t" \
    "ds_read_b128 v[24:27], %[xp] offset:50176\n\t" \
    "v_mfma_f32_16x16x32_bf16 v[204:207], v[104:107], v[32:35], v[204:207]\n\t" \
    "s_waitcnt lgkmcnt(4)\n\t" \
    "v_mfma_f32_16x16x32_bf16 v[208:211], v[112:115], v[32:35], v[208:211]\n\t" \
    "buffer_load_dwordx4 %[l16], %[rs], %[a] offen offset:1024 sc1 lds\n\t" \
    "v_mfma_f32_16x16x32_bf16 v[196:199], v[116:119], v[36:39], v[196:199]\n\t" \
    "ds_read_b128 v[28:31], %[xp] offset:51200\n\t" \
    "v_mfma_f32_16x16x32_bf16 v[200:203], v[124:127], v[36:39], v[200:203]\n\t" \
    "ds_read_b128 v[32:35], %[xp] offset:52224\n\t" \
    "v_mfma_f32_16x16x32_bf16 v[204:207], v[116:119], v[40:43], v[204:207]\n\t" \
    "s_waitcnt lgkmcnt(4)\n\t" \
    "v_mfma_f32_16x16x32_bf16 v[208:211], v[124:127], v[40:43], v[208:211]\n\t" \
    "buffer_load_dwordx4 %[l16], %[rs], %[a] offen offset:2048 sc1 lds\n\t" \
    "v_mfma_f32_16x16x32_bf16 v[196:199], v[120:123], v[44:47], v[196:199]\n\t" \
    "ds_read_b128 v[36:39], %[xp] offset:53248\n\t" \
    "v_mfma_f32_16x16x32_bf16 v[200:203], v[128:131], v[44:47], v[200:203]\n\t" \
    "ds_read_b128 v[40:43], %[xp] offset:54272\n\t" \
    "v_mfma_f32_16x16x32_bf16 v[204:207], v[120:123], v[48:51], v[204:207]\n\t" \
    "s_waitcnt lgkmcnt(4)\n\t" \
    "v_mfma_f32_16x16x32_bf16 v[208:211], v[128:131], v[48:51], v[208:211]\n\t" \
    "buffer_load_dwordx4 %[l16], %[rs], %[a] offen offset:3072 sc1 lds\n\t" \
    "s_waitcnt vmcnt(4)\n\t" \
    "s_barrier\n\t" \
    "s_setprio 1\n\t" \
    "s_add_u32 m0, %[sd], 32768\n\t" \
    "s_add_u32 %[a], %[sx], 524288\n\t" \
    "s_add_u32 %[b], %[oo], 0x1000\n\t" \
    "s_cmp_lg_u32 %[t], 0\n\t" \
    "s_cselect_b64 %[em], -1, 0\n\t" \
    "v_mfma_f32_16x16x32_bf16 v[196:199], v[132:135], v[20:23], v[196:199]\n\t" \
    "ds_read_b128 v[44:47], %[xp] offset:55296\n\t" \
    "v_mfma_f32_16x16x32_bf16 v[200:203], v[140:143], v[20:23], v[200:203]\n\t" \
    "ds_read_b128 v[48:51], %[xp] offset:56320\n\t" \
    "v_mfma_f32_16x16x32_bf16 v[204:207], v[132:135], v[24:27], v[204:207]\n\t" \
    "ds_read_b128 v[52:55], %[pa] offset:0\n\t" \
    "s_waitcnt lgkmcnt(5)\n\t" \
    "v_mfma_f32_16x16x32_bf16 v[208:211], v[140:143], v[24:27], v[208:211]\n\t" \
    "buffer_load_dwordx4 %[l16], %[rs], %[a] offen offset:0 sc1 lds\n\t" \
    "v_mfma_f32_16x16x32_bf16 v[196:199], v[136:139], v[28:31], v[196:199]\n\t" \
    "ds_read_b128 v[20:23], %[xp] offset:57344\n\t" \
    "v_mfma_f32_16x16x32_bf16 v[200:203], v[144:147], v[28:31], v[200:203]\n\t" \
    "ds_read_b128 v[24:27], %[xp] offset:58368\n\t" \
    "v_mfma_f32_16x16x32_bf16 v[204:207], v[136:139], v[32:35], v[204:207]\n\t" \
    "ds_read_b128 v[56:59], %[pa] offset:1024\n\t" \
    "s_waitcnt lgkmcnt(6)\n\t" \
    "v_mfma_f32_16x16x32_bf16 v[208:211], v[144:147], v[32:35], v[208:211]\n\t" \
    "buffer_load_dwordx4 %[l16], %[rs], %[a] offen offset:1024 sc1 lds\n\t" \
    "v_mfma_f32_16x16x32_bf16 v[196:199], v[148:151], v[36:39], v[196:199]\n\t" \
    "ds_read_b128 v[28:31], %[xp] offset:59392\n\t" \
    "v_mfma_f32_16x16x32_bf16 v[200:203], v[156:159], v[36:39], v[200:203]\n\t" \
    "ds_read_b128 v[32:35], %[xp] offset:60416\n\t" \
    "v_mfma_f32_16x16x32_bf16 v[204:207], v[148:151], v[40:43], v[204:207]\n\t" \
    "ds_read_b128 v[60:63], %[pa] offset:2048\n\t" \
    "s_waitcnt lgkmcnt(7)\n\t" \
    "v_mfma_f32_16x16x32_bf16 v[208:211], v[156:159], v[40:43], v[208:211]\n\t" \
    "buffer_load_dwordx4 %[l16], %[rs], %[a] offen offset:2048 sc1 lds\n\t" \
    "v_mfma_f32_16x16x32_bf16 v[196:199], v[152:155], v[44:47], v[196:199]\n\t" \
    "ds_read_b128 v[36:39], %[xp] offset:61440\n\t" \
    "s_waitcnt lgkmcnt(7)\n\t" \
    "v_add_f32 v0, v0, v52\n\t" \
    "v_mfma_f32_16x16x32_bf16 v[200:203], v[160:163], v[44:47], v[200:203]\n\t" \
    "ds_read_b128 v[40:43], %[xp] offset:62464\n\t" \
    "v_add_f32 v1, v1, v53\n\t" \
    "v_mfma_f32_16x16x32_bf16 v[204:207], v[152:155], v[48:51], v[204:207]\n\t" \
    "ds_read_b128 v[64:67], %[pa] offset:3072\n\t" \
    "s_waitcnt lgkmcnt(7)\n\t" \
    "v_mfma_f32_16x16x32_bf16 v[208:211], v[160:163], v[48:51], v[208:211]\n\t" \
    "buffer_load_dwordx4 %[l16], %[rs], %[a] offen offset:3072 sc1 lds\n\t" \
    "v_add_f32 v2, v2, v54\n\t" \
    "v_add_f32 v3, v3, v55\n\t" \
    "v_mfma_f32_16x16x32_bf16 v[196:199], v[164:167], v[20:23], v[196:199]\n\t" \
    "ds_read_b128 v[44:47], %[xp] offset:63488\n\t" \
    "s_waitcnt lgkmcnt(7)\n\t" \
    "v_add_f32 v4, v4, v56\n\t" \
    "v_mfma_f32_16x16x32_bf16 v[200:203], v[172:175], v[20:23], v[200:203]\n\t" \
    "ds_read_b128 v[48:51], %[xp] offset:64512\n\t" \
    "v_add_f32 v5, v5, v57\n\t" \
    "v_mfma_f32_16x16x32_bf16 v[204:207], v[164:167], v[24:27], v[204:207]\n\t" \
    "v_add_f32 v6, v6, v58\n\t" \
    "s_waitcnt lgkmcnt(6)\n\t" \
    "v_mfma_f32_16x16x32_bf16 v[208:211], v[172:175], v[24:27], v[208:211]\n\t" \
    "v_add_f32 v7, v7, v59\n\t" \
    "s_waitcnt lgkmcnt(5)\n\t" \
    "v_add_f32 v8, v8, v60\n\t" \
    "v_add_f32 v9, v9, v61\n\t" \
    "v_mfma_f32_16x16x32_bf16 v[196:199], v[168:171], v[28:31], v[196:199]\n\t" \
    "ds_read_b128 v[20:23], %[xp] offset:0\n\t" \
    "v_add_f32 v10, v10, v62\n\t" \
    "v_mfma_f32_16x16x32_bf16 v[200:203], v[176:179], v[28:31], v[200:203]\n\t" \
    "ds_read_b128 v[24:27], %[xp] offset:1024\n\t" \
    "v_add_f32 v11, v11, v63\n\t" \
    "v_mfma_f32_16x16x32_bf16 v[204:207], v[168:171], v[32:35], v[204:207]\n\t" \
    "s_waitcnt lgkmcnt(4)\n\t" \
    "v_add_f32 v12, v12, v64\n\t" \
    "v_mfma_f32_16x16x32_bf16 v[208:211], v[176:179], v[32:35], v[208:211]\n\t" \
    "v_add_f32 v13, v13, v65\n\t" \
    "v_add_f32 v14, v14, v66\n\t" \
    "v_add_f32 v15, v15, v67\n\t" \
    "v_mfma_f32_16x16x32_bf16 v[196:199], v[180:183], v[36:39], v[196:199]\n\t" \
    "ds_read_b128 v[28:31], %[xp] offset:2048\n\t" \
    "v_mfma_f32_16x16x32_bf16 v[200:203], v[188:191], v[36:39], v[200:203]\n\t" \
    "ds_read_b128 v[32:35], %[xp] offset:3072\n\t" \
    "s_mov_b64 exec, %[em]\n\t" \
    "buffer_store_dwordx4 v[0:3], %[l16], %[rs], %[oo] offen offset:0 sc1\n\t" \
    "s_mov_b64 exec, -1\n\t" \
    "v_mfma_f32_16x16x32_bf16 v[204:207], v[180:183], v[40:43], v[204:207]\n\t" \
    "s_waitcnt lgkmcnt(4)\n\t" \
    "v_mfma_f32_16x16x32_bf16 v[208:211], v[188:191], v[40:43], v[208:211]\n\t" \
    "s_mov_b64 exec, %[em]\n\t" \
    "buffer_store_dwordx4 v[4:7], %[l16], %[rs], %[oo] offen offset:1024 sc1\n\t" \
    "s_mov_b64 exec, -1\n\t" \
    "v_mfma_f32_16x16x32_bf16 v[196:199], v[184:187], v[44:47], v[196:199]\n\t" \
    "ds_read_b128 v[36:39], %[xp] offset:4096\n\t" \
    "v_mfma_f32_16x16x32_bf16 v[200:203], v[192:195], v[44:47], v[200:203]\n\t" \
    "ds_read_b128 v[40:43], %[xp] offset:5120\n\t" \
    "s_mov_b64 exec, %[em]\n\t" \
    "buffer_store_dwordx4 v[8:11], %[l16], %[rs], %[oo] offen offset:2048 sc1\n\t" \
    "s_mov_b64 exec, -1\n\t" \
    "v_mfma_f32_16x16x32_bf16 v[204:207], v[184:187], v[48:51], v[204:207]\n\t" \
    "s_waitcnt lgkmcnt(4)\n\t" \
    "v_mfma_f32_16x16x32_bf16 v[208:211], v[192:195], v[48:51], v[208:211]\n\t" \
    "s_mov_b64 exec, %[em]\n\t" \
    "buffer_store_dwordx4 v[12:15], %[l16], %[rs], %[oo] offen offset:3072 sc1\n\t" \
    "s_mov_b64 exec, -1\n\t" \
    "s_add_u32 %[sx], %[sx], 262144\n\t" \
    "s_add_u32 %[t], %[t], 1\n\t" \
    "s_add_u32 %[oo], %[oo], 524288\n\t" \
    "s_waitcnt vmcnt(8)\n\t" \
    "s_cmp_gt_u32 %[t], " STR(SQUICK) " + 1\n\t" \
    "s_cbranch_scc1 B1_%=\n\t" \
    "s_waitcnt vmcnt(0)\n\t" \
    "B1_%=:\n\t" \
    "s_barrier\n\t" \
    "s_cmp_lt_u32 %[t], %[te]\n\t" \
    "s_cbranch_scc1 L1_%=\n\t" \
    "s_cmp_eq_u32 %[fin], %[te]\n\t" \
    "s_cbranch_scc0 E_%=\n\t" \
    "s_nop 4\n\t" \
    "s_barrier\n\t" \
    "ds_read_b128 v[52:55], %[pa] offset:0\n\t" \
    "ds_read_b128 v[56:59], %[pa] offset:1024\n\t" \
    "ds_read_b128 v[60:63], %[pa] offset:2048\n\t" \
    "ds_read_b128 v[64:67], %[pa] offset:3072\n\t" \
    "s_add_u32 %[b], %[oo], 0x1000\n\t" \
    "s_waitcnt lgkmcnt(0)\n\t" \
    "v_add_f32 v196, v196, v52\n\t" \
    "v_add_f32 v197, v197, v53\n\t" \
    "v_add_f32 v198, v198, v54\n\t" \
    "v_add_f32 v199, v199, v55\n\t" \
    "v_add_f32 v200, v200, v56\n\t" \
    "v_add_f32 v201, v201, v57\n\t" \
    "v_add_f32 v202, v202, v58\n\t" \
    "v_add_f32 v203, v203, v59\n\t" \
    "v_add_f32 v204, v204, v60\n\t" \
    "v_add_f32 v205, v205, v61\n\t" \
    "v_add_f32 v206, v206, v62\n\t" \
    "v_add_f32 v207, v207, v63\n\t" \
    "v_add_f32 v208, v208, v64\n\t" \
    "v_add_f32 v209, v209, v65\n\t" \
    "v_add_f32 v210, v210, v66\n\t" \
    "v_add_f32 v211, v211, v67\n\t" \
    "buffer_store_dwordx4 v[196:199], %[l16], %[rs], %[oo] offen offset:0 sc1\n\t" \
    "buffer_store_dwordx4 v[200:203], %[l16], %[rs], %[oo] offen offset:1024 sc1\n\t" \
    "buffer_store_dwordx4 v[204:207], %[l16], %[rs], %[oo] offen offset:2048 sc1\n\t" \
    "buffer_store_dwordx4 v[208:211], %[l16], %[rs], %[oo] offen offset:3072 sc1\n\t" \
    "E_%=:\n\t" \
    "s_setprio 0\n\t" \
    "s_waitcnt lgkmcnt(0)\n\t" \
    "s_nop 7\n\t" \
    : [t] "+s"(t), [sx] "+s"(sx), [oo] "+s"(oo), [a] "=&s"(tmp), [b] "=&s"(tmp2), [em] "=&s"(em), "+{v[20:27]}"(xf[0]), "+{v[28:35]}"(xf[1]), "+{v[36:43]}"(xf[2]), "+{v[196:203]}"(ac[0]), "+{v[204:211]}"(ac[1]) \
    : [te] "s"(TE_), [fin] "s"(FIN_), [kh] "s"(kh), [sd] "s"(sd), [rs] "s"(rs), [l16] "v"(l16), [xp] "v"(xp), [pa] "v"(pa), [fm] "s"(fm), [pm] "s"(pm), [fo] "s"(fo), [po] "s"(po), [pd] "s"(pd), [tag] "s"(P.tag), "{v[68:75]}"(w[0][0]), "{v[76:83]}"(w[0][1]), "{v[84:91]}"(w[1][0]), "{v[92:99]}"(w[1][1]), "{v[100:107]}"(w[2][0]), "{v[108:115]}"(w[2][1]), "{v[116:123]}"(w[3][0]), "{v[124:131]}"(w[3][1]), "{v[132:139]}"(w[4][0]), "{v[140:147]}"(w[4][1]), "{v[148:155]}"(w[5][0]), "{v[156:163]}"(w[5][1]), "{v[164:171]}"(w[6][0]), "{v[172:179]}"(w[6][1]), "{v[180:187]}"(w[7][0]), "{v[188:195]}"(w[7][1]) \
    : "memory", "scc", "v0", "v1", "v2", "v3", "v4", "v5", "v6", "v7", "v8", "v9", "v10", "v11", "v12", "v13", "v14", "v15", "v44", "v45", "v46", "v47", "v48", "v49", "v50", "v51", "v52", "v53", "v54", "v55", "v56", "v57", "v58", "v59", "v60", "v61", "v62", "v63", "v64", "v65", "v66", "v67")

#define X_ONE() \
  asm volatile( \
    "ds_read_b128 v[20:23], %[xp] offset:0\n\t" \
    "ds_read_b128 v[24:27], %[xp] offset:1024\n\t" \
    "ds_read_b128 v[28:31], %[xp] offset:2048\n\t" \
    "ds_read_b128 v[32:35], %[xp] offset:3072\n\t" \
    "ds_read_b128 v[36:39], %[xp] offset:4096\n\t" \
    "ds_read_b128 v[40:43], %[xp] offset:5120\n\t" \
    "s_waitcnt lgkmcnt(4)\n\t" \
    "v_mfma_f32_16x16x32_bf16 v[0:3], v[68:71], v[20:23], 0\n\t" \
    "ds_read_b128 v[44:47], %[xp] offset:6144\n\t" \
    "v_mfma_f32_16x16x32_bf16 v[4:7], v[76:79], v[20:23], 0\n\t" \
    "ds_read_b128 v[48:51], %[xp] offset:7168\n\t" \
    "v_mfma_f32_16x16x32_bf16 v[8:11], v[68:71], v[24:27], 0\n\t" \
    "s_waitcnt lgkmcnt(4)\n\t" \
    "v_mfma_f32_16x16x32_bf16 v[12:15], v[76:79], v[24:27], 0\n\t" \
    "v_mfma_f32_16x16x32_bf16 v[0:3], v[72:75], v[28:31], v[0:3]\n\t" \
    "ds_read_b128 v[20:23], %[xp] offset:8192\n\t" \
    "v_mfma_f32_16x16x32_bf16 v[4:7], v[80:83], v[28:31], v[4:7]\n\t" \
    "ds_read_b128 v[24:27], %[xp] offset:9216\n\t" \
    "v_mfma_f32_16x16x32_bf16 v[8:11], v[72:75], v[32:35], v[8:11]\n\t" \
    "s_waitcnt lgkmcnt(4)\n\t" \
    "v_mfma_f32_16x16x32_bf16 v[12:15], v[80:83], v[32:35], v[12:15]\n\t" \
    "v_mfma_f32_16x16x32_bf16 v[0:3], v[84:87], v[36:39], v[0:3]\n\t" \
    "ds_read_b128 v[28:31], %[xp] offset:10240\n\t" \
    "v_mfma_f32_16x16x32_bf16 v[4:7], v[92:95], v[36:39], v[4:7]\n\t" \
    "ds_read_b128 v[32:35], %[xp] offset:11264\n\t" \
    "v_mfma_f32_16x16x32_bf16 v[8:11], v[84:87], v[40:43], v[8:11]\n\t" \
    "s_waitcnt lgkmcnt(4)\n\t" \
    "v_mfma_f32_16x16x32_bf16 v[12:15], v[92:95], v[40:43], v[12:15]\n\t" \
    "v_mfma_f32_16x16x32_bf16 v[0:3], v[88:91], v[44:47], v[0:3]\n\t" \
    "ds_read_b128 v[36:39], %[xp] offset:12288\n\t" \
    "v_mfma_f32_16x16x32_bf16 v[4:7], v[96:99], v[44:47], v[4:7]\n\t" \
    "ds_read_b128 v[40:43], %[xp] offset:13312\n\t" \
    "v_mfma_f32_16x16x32_bf16 v[8:11], v[88:91], v[48:51], v[8:11]\n\t" \
    "s_waitcnt lgkmcnt(4)\n\t" \
    "v_mfma_f32_16x16x32_bf16 v[12:15], v[96:99], v[48:51], v[12:15]\n\t" \
    "v_mfma_f32_16x16x32_bf16 v[0:3], v[100:103], v[20:23], v[0:3]\n\t" \
    "ds_read_b128 v[44:47], %[xp] offset:14336\n\t" \
    "v_mfma_f32_16x16x32_bf16 v[4:7], v[108:111], v[20:23], v[4:7]\n\t" \
    "ds_read_b128 v[48:51], %[xp] offset:15360\n\t" \
    "v_mfma_f32_16x16x32_bf16 v[8:11], v[100:103], v[24:27], v[8:11]\n\t" \
    "s_waitcnt lgkmcnt(4)\n\t" \
    "v_mfma_f32_16x16x32_bf16 v[12:15], v[108:111], v[24:27], v[12:15]\n\t" \
    "s_waitcnt vmcnt(0)\n\t" \
    "s_barrier\n\t" \
    "v_mfma_f32_16x16x32_bf16 v[0:3], v[104:107], v[28:31], v[0:3]\n\t" \
    "ds_read_b128 v[20:23], %[xp] offset:16384\n\t" \
    "v_mfma_f32_16x16x32_bf16 v[4:7], v[112:115], v[28:31], v[4:7]\n\t" \
    "ds_read_b128 v[24:27], %[xp] offset:17408\n\t" \
    "v_mfma_f32_16x16x32_bf16 v[8:11], v[104:107], v[32:35], v[8:11]\n\t" \
    "s_waitcnt lgkmcnt(4)\n\t" \
    "v_mfma_f32_16x16x32_bf16 v[12:15], v[112:115], v[32:35], v[12:15]\n\t" \
    "buffer_load_dword v52, %[pv], %[rs], 0 offen sc1\n\t" \
    "v_mfma_f32_16x16x32_bf16 v[0:3], v[116:119], v[36:39], v[0:3]\n\t" \
    "ds_read_b128 v[28:31], %[xp] offset:18432\n\t" \
    "v_mfma_f32_16x16x32_bf16 v[4:7], v[124:127], v[36:39], v[4:7]\n\t" \
    "ds_read_b128 v[32:35], %[xp] offset:19456\n\t" \
    "v_mfma_f32_16x16x32_bf16 v[8:11], v[116:119], v[40:43], v[8:11]\n\t" \
    "s_waitcnt lgkmcnt(4)\n\t" \
    "v_mfma_f32_16x16x32_bf16 v[12:15], v[124:127], v[40:43], v[12:15]\n\t" \
    "v_mfma_f32_16x16x32_bf16 v[0:3], v[120:123], v[44:47], v[0:3]\n\t" \
    "ds_read_b128 v[36:39], %[xp] offset:20480\n\t" \
    "v_mfma_f32_16x16x32_bf16 v[4:7], v[128:131], v[44:47], v[4:7]\n\t" \
    "ds_read_b128 v[40:43], %[xp] offset:21504\n\t" \
    "v_mfma_f32_16x16x32_bf16 v[8:11], v[120:123], v[48:51], v[8:11]\n\t" \
    "s_waitcnt lgkmcnt(4)\n\t" \
    "v_mfma_f32_16x16x32_bf16 v[12:15], v[128:131], v[48:51], v[12:15]\n\t" \
    "v_mfma_f32_16x16x32_bf16 v[0:3], v[132:135], v[20:23], v[0:3]\n\t" \
    "ds_read_b128 v[44:47], %[xp] offset:22528\n\t" \
    "v_mfma_f32_16x16x32_bf16 v[4:7], v[140:143], v[20:23], v[4:7]\n\t" \
    "ds_read_b128 v[48:51], %[xp] offset:23552\n\t" \
    "v_mfma_f32_16x16x32_bf16 v[8:11], v[132:135], v[24:27], v[8:11]\n\t" \
    "s_waitcnt lgkmcnt(4)\n\t" \
    "v_mfma_f32_16x16x32_bf16 v[12:15], v[140:143], v[24:27], v[12:15]\n\t" \
    "v_mfma_f32_16x16x32_bf16 v[0:3], v[136:139], v[28:31], v[0:3]\n\t" \
    "ds_read_b128 v[20:23], %[xp] offset:24576\n\t" \
    "v_mfma_f32_16x16x32_bf16 v[4:7], v[144:147], v[28:31], v[4:7]\n\t" \
    "ds_read_b128 v[24:27], %[xp] offset:25600\n\t" \
    "v_mfma_f32_16x16x32_bf16 v[8:11], v[136:139], v[32:35], v[8:11]\n\t" \
    "s_waitcnt lgkmcnt(4)\n\t" \
    "v_mfma_f32_16x16x32_bf16 v[12:15], v[144:147], v[32:35], v[12:15]\n\t" \
    "v_mfma_f32_16x16x32_bf16 v[0:3], v[148:151], v[36:39], v[0:3]\n\t" \
    "ds_read_b128 v[28:31], %[xp] offset:26624\n\t" \
    "v_mfma_f32_16x16x32_bf16 v[4:7], v[156:159], v[36:39], v[4:7]\n\t" \
    "ds_read_b128 v[32:35], %[xp] offset:27648\n\t" \
    "v_mfma_f32_16x16x32_bf16 v[8:11], v[148:151], v[40:43], v[8:11]\n\t" \
    "s_waitcnt lgkmcnt(4)\n\t" \
    "v_mfma_f32_16x16x32_bf16 v[12:15], v[156:159], v[40:43], v[12:15]\n\t" \
    "v_mfma_f32_16x16x32_bf16 v[0:3], v[152:155], v[44:47], v[0:3]\n\t" \
    "ds_read_b128 v[36:39], %[xp] offset:28672\n\t" \
    "v_mfma_f32_16x16x32_bf16 v[4:7], v[160:163], v[44:47], v[4:7]\n\t" \
    "ds_read_b128 v[40:43], %[xp] offset:29696\n\t" \
    "v_mfma_f32_16x16x32_bf16 v[8:11], v[152:155], v[48:51], v[8:11]\n\t" \
    "s_waitcnt lgkmcnt(4)\n\t" \
    "v_mfma_f32_16x16x32_bf16 v[12:15], v[160:163], v[48:51], v[12:15]\n\t" \
    "v_mfma_f32_16x16x32_bf16 v[0:3], v[164:167], v[20:23], v[0:3]\n\t" \
    "ds_read_b128 v[44:47], %[xp] offset:30720\n\t" \
    "v_mfma_f32_16x16x32_bf16 v[4:7], v[172:175], v[20:23], v[4:7]\n\t" \
    "ds_read_b128 v[48:51], %[xp] offset:31744\n\t" \
    "v_mfma_f32_16x16x32_bf16 v[8:11], v[164:167], v[24:27], v[8:11]\n\t" \
    "s_waitcnt lgkmcnt(4)\n\t" \
    "v_mfma_f32_16x16x32_bf16 v[12:15], v[172:175], v[24:27], v[12:15]\n\t" \
    "v_mfma_f32_16x16x32_bf16 v[0:3], v[168:171], v[28:31], v[0:3]\n\t" \
    "v_mfma_f32_16x16x32_bf16 v[4:7], v[176:179], v[28:31], v[4:7]\n\t" \
    "v_mfma_f32_16x16x32_bf16 v[8:11], v[168:171], v[32:35], v[8:11]\n\t" \
    "s_waitcnt lgkmcnt(2)\n\t" \
    "v_mfma_f32_16x16x32_bf16 v[12:15], v[176:179], v[32:35], v[12:15]\n\t" \
    "v_mfma_f32_16x16x32_bf16 v[0:3], v[180:183], v[36:39], v[0:3]\n\t" \
    "v_mfma_f32_16x16x32_bf16 v[4:7], v[188:191], v[36:39], v[4:7]\n\t" \
    "v_mfma_f32_16x16x32_bf16 v[8:11], v[180:183], v[40:43], v[8:11]\n\t" \
    "s_waitcnt lgkmcnt(0)\n\t" \
    "v_mfma_f32_16x16x32_bf16 v[12:15], v[188:191], v[40:43], v[12:15]\n\t" \
    "v_mfma_f32_16x16x32_bf16 v[0:3], v[184:187], v[44:47], v[0:3]\n\t" \
    "v_mfma_f32_16x16x32_bf16 v[4:7], v[192:195], v[44:47], v[4:7]\n\t" \
    "v_mfma_f32_16x16x32_bf16 v[8:11], v[184:187], v[48:51], v[8:11]\n\t" \
    "v_mfma_f32_16x16x32_bf16 v[12:15], v[192:195], v[48:51], v[12:15]\n\t" \
    "s_waitcnt vmcnt(0)\n\t" \
    "s_nop 7\n\t" \
    : "=&{v52}"(pg), "=&{v[0:3]}"(a[0]), "=&{v[4:7]}"(a[1]), "=&{v[8:11]}"(a[2]), "=&{v[12:15]}"(a[3]) \
    : [xp] "v"(xp), [pv] "v"(pv), [rs] "s"(rs), "{v[68:75]}"(w[0][0]), "{v[76:83]}"(w[0][1]), "{v[84:91]}"(w[1][0]), "{v[92:99]}"(w[1][1]), "{v[100:107]}"(w[2][0]), "{v[108:115]}"(w[2][1]), "{v[116:123]}"(w[3][0]), "{v[124:131]}"(w[3][1]), "{v[132:139]}"(w[4][0]), "{v[140:147]}"(w[4][1]), "{v[148:155]}"(w[5][0]), "{v[156:163]}"(w[5][1]), "{v[164:171]}"(w[6][0]), "{v[172:179]}"(w[6][1]), "{v[180:187]}"(w[7][0]), "{v[188:195]}"(w[7][1]) \
    : "memory", "v20", "v21", "v22", "v23", "v24", "v25", "v26", "v27", "v28", "v29", "v30", "v31", "v32", "v33", "v34", "v35", "v36", "v37", "v38", "v39", "v40", "v41", "v42", "v43", "v44", "v45", "v46", "v47", "v48", "v49", "v50", "v51")

#define H_LOOP1() \
  asm volatile( \
    "v_mov_b32 v20, 0\n\t" \
    "v_mov_b32 v21, 0\n\t" \
    "v_mov_b32 v22, 0\n\t" \
    "v_mov_b32 v23, 0\n\t" \
    "v_mov_b32 v24, 0\n\t" \
    "v_mov_b32 v25, 0\n\t" \
    "v_mov_b32 v26, 0\n\t" \
    "v_mov_b32 v27, 0\n\t" \
    "v_mov_b32 v28, 0\n\t" \
    "v_mov_b32 v29, 0\n\t" \
    "v_mov_b32 v30, 0\n\t" \
    "v_mov_b32 v31, 0\n\t" \
    "v_mov_b32 v32, 0\n\t" \
    "v_mov_b32 v33, 0\n\t" \
    "v_mov_b32 v34, 0\n\t" \
    "v_mov_b32 v35, 0\n\t" \
    "v_mov_b32 v36, 0\n\t" \
    "v_mov_b32 v37, 0\n\t" \
    "v_mov_b32 v38, 0\n\t" \
    "v_mov_b32 v39, 0\n\t" \
    "v_mov_b32 v40, 0\n\t" \
    "v_mov_b32 v41, 0\n\t" \
    "v_mov_b32 v42, 0\n\t" \
    "v_mov_b32 v43, 0\n\t" \
    "v_mov_b32 v44, 0\n\t" \
    "v_mov_b32 v45, 0\n\t" \
    "v_mov_b32 v46, 0\n\t" \
    "v_mov_b32 v47, 0\n\t" \
    "v_mov_b32 v48, 0\n\t" \
    "v_mov_b32 v49, 0\n\t" \
    "v_mov_b32 v50, 0\n\t" \
    "v_mov_b32 v51, 0\n\t" \
    "v_mov_b32 v52, 0\n\t" \
    "v_mov_b32 v53, 0\n\t" \
    "v_mov_b32 v54, 0\n\t" \
    "v_mov_b32 v55, 0\n\t" \
    "v_mov_b32 v56, 0\n\t" \
    "v_mov_b32 v57, 0\n\t" \
    "v_mov_b32 v58, 0\n\t" \
    "v_mov_b32 v59, 0\n\t" \
    "v_mov_b32 v60, 0\n\t" \
    "v_mov_b32 v61, 0\n\t" \
    "v_mov_b32 v62, 0\n\t" \
    "v_mov_b32 v63, 0\n\t" \
    "v_mov_b32 v64, 0\n\t" \
    "v_mov_b32 v65, 0\n\t" \
    "v_mov_b32 v66, 0\n\t" \
    "v_mov_b32 v67, 0\n\t" \
    "v_mov_b32 v68, 0\n\t" \
    "v_mov_b32 v69, 0\n\t" \
    "v_mov_b32 v70, 0\n\t" \
    "v_mov_b32 v71, 0\n\t" \
    "v_mov_b32 v72, 0\n\t" \
    "v_mov_b32 v73, 0\n\t" \
    "v_mov_b32 v74, 0\n\t" \
    "v_mov_b32 v75, 0\n\t" \
    "v_mov_b32 v76, 0\n\t" \
    "v_mov_b32 v77, 0\n\t" \
    "v_mov_b32 v78, 0\n\t" \
    "v_mov_b32 v79, 0\n\t" \
    "v_mov_b32 v80, 0\n\t" \
    "v_mov_b32 v81, 0\n\t" \
    "v_mov_b32 v82, 0\n\t" \
    "v_mov_b32 v83, 0\n\t" \
    "s_waitcnt vmcnt(8)\n\t" \
    "s_barrier\n\t" \
    "ds_read_b64_tr_b16 v[116:117], %[b0] offset:0\n\t" \
    "ds_read_b64_tr_b16 v[118:119], %[b0] offset:1024\n\t" \
    "ds_read_b64_tr_b16 v[124:125], %[b1] offset:0\n\t" \
    "ds_read_b64_tr_b16 v[126:127], %[b1] offset:1024\n\t" \
    "ds_read_b64_tr_b16 v[120:121], %[b0] offset:8192\n\t" \
    "ds_read_b64_tr_b16 v[122:123], %[b0] offset:9216\n\t" \
    "ds_read_b64_tr_b16 v[128:129], %[b1] offset:8192\n\t" \
    "ds_read_b64_tr_b16 v[130:131], %[b1] offset:9216\n\t" \
    "ds_read_b128 v[84:87], %[a] offset:0\n\t" \
    "ds_read_b128 v[88:91], %[a] offset:1024\n\t" \
    "ds_read_b128 v[92:95], %[a] offset:2048\n\t" \
    "ds_read_b128 v[96:99], %[a] offset:3072\n\t" \
    "ds_read_b128 v[100:103], %[a] offset:4096\n\t" \
    "ds_read_b128 v[104:107], %[a] offset:5120\n\t" \
    "ds_read_b128 v[108:111], %[a] offset:6144\n\t" \
    "ds_read_b128 v[112:115], %[a] offset:7168\n\t" \
    "L_%=:\n\t" \
    "s_add_u32 %[d], %[lw], %[c]\n\t" \
    "s_add_u32 %[y], %[c], 49152\n\t" \
    "s_cmp_eq_u32 %[y], 147456\n\t" \
    "s_cselect_b32 %[y], 0, %[y]\n\t" \
    "s_sub_u32 %[x], %[y], %[c]\n\t" \
    "s_mov_b32 %[c], %[y]\n\t" \
    "v_add_u32 %[a], %[x], %[a]\n\t" \
    "v_add_u32 %[b0], %[x], %[b0]\n\t" \
    "v_add_u32 %[b1], %[x], %[b1]\n\t" \
    "s_add_u32 %[x], %[t], 3\n\t" \
    "s_cmp_lt_u32 %[x], %[nt]\n\t" \
    "s_cselect_b32 %[x], %[x], 0\n\t" \
    "s_mul_i32 %[y], %[x], %[kb]\n\t" \
    "s_add_u32 %[y], %[y], %[cb]\n\t" \
    "s_lshl_b32 %[x], %[x], 11\n\t" \
    "s_add_u32 %[x], %[x], %[ca]\n\t" \
    "s_waitcnt vmcnt(4) lgkmcnt(0)\n\t" \
    "s_barrier\n\t" \
    "v_mfma_f32_16x16x32_bf16 v[20:23], v[116:119], v[84:87], v[20:23]\n\t" \
    "ds_read_b64_tr_b16 v[180:181], %[b0] offset:0\n\t" \
    "ds_read_b64_tr_b16 v[182:183], %[b0] offset:1024\n\t" \
    "s_add_u32 m0, %[d], 0\n\t" \
    "v_mfma_f32_16x16x32_bf16 v[24:27], v[124:127], v[84:87], v[24:27]\n\t" \
    "buffer_load_dwordx4 %[vb0], %[rb], %[y] offen lds\n\t" \
    "v_mfma_f32_16x16x32_bf16 v[20:23], v[120:123], v[88:91], v[20:23]\n\t" \
    "ds_read_b64_tr_b16 v[188:189], %[b1] offset:0\n\t" \
    "ds_read_b64_tr_b16 v[190:191], %[b1] offset:1024\n\t" \
    "v_mfma_f32_16x16x32_bf16 v[24:27], v[128:131], v[88:91], v[24:27]\n\t" \
    "ds_read_b64_tr_b16 v[184:185], %[b0] offset:8192\n\t" \
    "ds_read_b64_tr_b16 v[186:187], %[b0] offset:9216\n\t" \
    "v_mfma_f32_16x16x32_bf16 v[28:31], v[116:119], v[92:95], v[28:31]\n\t" \
    "ds_read_b64_tr_b16 v[192:193], %[b1] offset:8192\n\t" \
    "ds_read_b64_tr_b16 v[194:195], %[b1] offset:9216\n\t" \
    "s_add_u32 m0, %[d], 8192\n\t" \
    "v_mfma_f32_16x16x32_bf16 v[32:35], v[124:127], v[92:95], v[32:35]\n\t" \
    "buffer_load_dwordx4 %[vb1], %[rb], %[y] offen lds\n\t" \
    "v_mfma_f32_16x16x32_bf16 v[28:31], v[120:123], v[96:99], v[28:31]\n\t" \
    "ds_read_b128 v[148:151], %[a] offset:0\n\t" \
    "v_mfma_f32_16x16x32_bf16 v[32:35], v[128:131], v[96:99], v[32:35]\n\t" \
    "ds_read_b128 v[152:155], %[a] offset:1024\n\t" \
    "v_mfma_f32_16x16x32_bf16 v[36:39], v[116:119], v[100:103], v[36:39]\n\t" \
    "ds_read_b128 v[156:159], %[a] offset:2048\n\t" \
    "s_add_u32 m0, %[d], 32768\n\t" \
    "v_mfma_f32_16x16x32_bf16 v[40:43], v[124:127], v[100:103], v[40:43]\n\t" \
    "buffer_load_dwordx4 %[va0], %[ra], %[x] offen sc1 lds\n\t" \
    "v_mfma_f32_16x16x32_bf16 v[36:39], v[120:123], v[104:107], v[36:39]\n\t" \
    "ds_read_b128 v[160:163], %[a] offset:3072\n\t" \
    "v_mfma_f32_16x16x32_bf16 v[40:43], v[128:131], v[104:107], v[40:43]\n\t" \
    "ds_read_b128 v[164:167], %[a] offset:4096\n\t" \
    "v_mfma_f32_16x16x32_bf16 v[44:47], v[116:119], v[108:111], v[44:47]\n\t" \
    "ds_read_b128 v[168:171], %[a] offset:5120\n\t" \
    "s_add_u32 m0, %[d], 40960\n\t" \
    "v_mfma_f32_16x16x32_bf16 v[48:51], v[124:127], v[108:111], v[48:51]\n\t" \
    "buffer_load_dwordx4 %[va1], %[ra], %[x] offen sc1 lds\n\t" \
    "v_mfma_f32_16x16x32_bf16 v[44:47], v[120:123], v[112:115], v[44:47]\n\t" \
    "ds_read_b128 v[172:175], %[a] offset:6144\n\t" \
    "v_mfma_f32_16x16x32_bf16 v[48:51], v[128:131], v[112:115], v[48:51]\n\t" \
    "ds_read_b128 v[176:179], %[a] offset:7168\n\t" \
    "s_mov_b32 m0, %[pd]\n\t" \
    "s_cmp_lt_u32 %[ps], %[pe]\n\t" \
    "s_cselect_b64 exec, -1, 0\n\t" \
    "buffer_load_dword %[pv], %[rp], %[ps] offen sc1 lds\n\t" \
    "s_mov_b64 exec, -1\n\t" \
    "s_add_u32 %[ps], %[ps], %[pk]\n\t" \
    "s_add_u32 %[t], %[t], 1\n\t" \
    "s_add_u32 %[d], %[lw], %[c]\n\t" \
    "s_add_u32 %[y], %[c], 49152\n\t" \
    "s_cmp_eq_u32 %[y], 147456\n\t" \
    "s_cselect_b32 %[y], 0, %[y]\n\t" \
    "s_sub_u32 %[x], %[y], %[c]\n\t" \
    "s_mov_b32 %[c], %[y]\n\t" \
    "v_add_u32 %[a], %[x], %[a]\n\t" \
    "v_add_u32 %[b0], %[x], %[b0]\n\t" \
    "v_add_u32 %[b1], %[x], %[b1]\n\t" \
    "s_add_u32 %[x], %[t], 3\n\t" \
    "s_cmp_lt_u32 %[x], %[nt]\n\t" \
    "s_cselect_b32 %[x], %[x], 0\n\t" \
    "s_mul_i32 %[y], %[x], %[kb]\n\t" \
    "s_add_u32 %[y], %[y], %[cb]\n\t" \
    "s_lshl_b32 %[x], %[x], 11\n\t" \
    "s_add_u32 %[x], %[x], %[ca]\n\t" \
    "s_waitcnt vmcnt(4) lgkmcnt(0)\n\t" \
    "s_barrier\n\t" \
    "v_mfma_f32_16x16x32_bf16 v[20:23], v[180:183], v[148:151], v[20:23]\n\t" \
    "ds_read_b64_tr_b16 v[116:117], %[b0] offset:0\n\t" \
    "ds_read_b64_tr_b16 v[118:119], %[b0] offset:1024\n\t" \
    "s_add_u32 m0, %[d], 0\n\t" \
    "v_mfma_f32_16x16x32_bf16 v[24:27], v[188:191], v[148:151], v[24:27]\n\t" \
    "buffer_load_dwordx4 %[vb0], %[rb], %[y] offen lds\n\t" \
    "v_mfma_f32_16x16x32_bf16 v[20:23], v[184:187], v[152:155], v[20:23]\n\t" \
    "ds_read_b64_tr_b16 v[124:125], %[b1] offset:0\n\t" \
    "ds_read_b64_tr_b16 v[126:127], %[b1] offset:1024\n\t" \
    "v_mfma_f32_16x16x32_bf16 v[24:27], v[192:195], v[152:155], v[24:27]\n\t" \
    "ds_read_b64_tr_b16 v[120:121], %[b0] offset:8192\n\t" \
    "ds_read_b64_tr_b16 v[122:123], %[b0] offset:9216\n\t" \
    "v_mfma_f32_16x16x32_bf16 v[28:31], v[180:183], v[156:159], v[28:31]\n\t" \
    "ds_read_b64_tr_b16 v[128:129], %[b1] offset:8192\n\t" \
    "ds_read_b64_tr_b16 v[130:131], %[b1] offset:9216\n\t" \
    "s_add_u32 m0, %[d], 8192\n\t" \
    "v_mfma_f32_16x16x32_bf16 v[32:35], v[188:191], v[156:159], v[32:35]\n\t" \
    "buffer_load_dwordx4 %[vb1], %[rb], %[y] offen lds\n\t" \
    "v_mfma_f32_16x16x32_bf16 v[28:31], v[184:187], v[160:163], v[28:31]\n\t" \
    "ds_read_b128 v[84:87], %[a] offset:0\n\t" \
    "v_mfma_f32_16x16x32_bf16 v[32:35], v[192:195], v[160:163], v[32:35]\n\t" \
    "ds_read_b128 v[88:91], %[a] offset:1024\n\t" \
    "v_mfma_f32_16x16x32_bf16 v[36:39], v[180:183], v[164:167], v[36:39]\n\t" \
    "ds_read_b128 v[92:95], %[a] offset:2048\n\t" \
    "s_add_u32 m0, %[d], 32768\n\t" \
    "v_mfma_f32_16x16x32_bf16 v[40:43], v[188:191], v[164:167], v[40:43]\n\t" \
    "buffer_load_dwordx4 %[va0], %[ra], %[x] offen sc1 lds\n\t" \
    "v_mfma_f32_16x16x32_bf16 v[36:39], v[184:187], v[168:171], v[36:39]\n\t" \
    "ds_read_b128 v[96:99], %[a] offset:3072\n\t" \
    "v_mfma_f32_16x16x32_bf16 v[40:43], v[192:195], v[168:171], v[40:43]\n\t" \
    "ds_read_b128 v[100:103], %[a] offset:4096\n\t" \
    "v_mfma_f32_16x16x32_bf16 v[44:47], v[180:183], v[172:175], v[44:47]\n\t" \
    "ds_read_b128 v[104:107], %[a] offset:5120\n\t" \
    "s_add_u32 m0, %[d], 40960\n\t" \
    "v_mfma_f32_16x16x32_bf16 v[48:51], v[188:191], v[172:175], v[48:51]\n\t" \
    "buffer_load_dwordx4 %[va1], %[ra], %[x] offen sc1 lds\n\t" \
    "v_mfma_f32_16x16x32_bf16 v[44:47], v[184:187], v[176:179], v[44:47]\n\t" \
    "ds_read_b128 v[108:111], %[a] offset:6144\n\t" \
    "v_mfma_f32_16x16x32_bf16 v[48:51], v[192:195], v[176:179], v[48:51]\n\t" \
    "ds_read_b128 v[112:115], %[a] offset:7168\n\t" \
    "s_mov_b32 m0, %[pd]\n\t" \
    "s_cmp_lt_u32 %[ps], %[pe]\n\t" \
    "s_cselect_b64 exec, -1, 0\n\t" \
    "buffer_load_dword %[pv], %[rp], %[ps] offen sc1 lds\n\t" \
    "s_mov_b64 exec, -1\n\t" \
    "s_add_u32 %[ps], %[ps], %[pk]\n\t" \
    "s_add_u32 %[t], %[t], 1\n\t" \
    "s_cmp_lt_u32 %[t], %[nt]\n\t" \
    "s_cbranch_scc1 L_%=\n\t" \
    "s_waitcnt lgkmcnt(0)\n\t" \
    "s_nop 7\n\t" \
    : "=&{v[20:23]}"(acc[0][0][0][0]), "=&{v[24:27]}"(acc[0][0][0][1]), "=&{v[28:31]}"(acc[0][0][1][0]), "=&{v[32:35]}"(acc[0][0][1][1]), "=&{v[36:39]}"(acc[0][0][2][0]), "=&{v[40:43]}"(acc[0][0][2][1]), "=&{v[44:47]}"(acc[0][0][3][0]), "=&{v[48:51]}"(acc[0][0][3][1]), "=&{v[52:55]}"(acc[0][1][0][0]), "=&{v[56:59]}"(acc[0][1][0][1]), "=&{v[60:63]}"(acc[0][1][1][0]), "=&{v[64:67]}"(acc[0][1][1][1]), "=&{v[68:71]}"(acc[0][1][2][0]), "=&{v[72:75]}"(acc[0][1][2][1]), "=&{v[76:79]}"(acc[0][1][3][0]), "=&{v[80:83]}"(acc[0][1][3][1]), [a] "+v"(aoff), [b0] "+v"(boff0), [b1] "+v"(boff1), [c] "+s"(cur), [t] "+s"(t), [ps] "+s"(ps), [d] "=&s"(sd), [x] "=&s"(sx), [y] "=&s"(sy) \
    : [va0] "v"(voffA[0]), [va1] "v"(voffA[1]), [vb0] "v"(voffB[0]), [vb1] "v"(voffB[1]), [pv] "v"(pf.v), [ra] "s"(ra), [rb] "s"(rb), [rp] "s"(rp), [lw] "s"(ldsw), [nt] "s"(nt), [kb] "s"(kB), [ca] "s"(cA), [cb] "s"(cB), [pd] "s"(pd), [pe] "s"(pe), [pk] "s"(pf.step) \
    : "memory", "scc", "v84", "v85", "v86", "v87", "v88", "v89", "v90", "v91", "v92", "v93", "v94", "v95", "v96", "v97", "v98", "v99", "v100", "v101", "v102", "v103", "v104", "v105", "v106", "v107", "v108", "v109", "v110", "v111", "v112", "v113", "v114", "v115", "v116", "v117", "v118", "v119", "v120", "v121", "v122", "v123", "v124", "v125", "v126", "v127", "v128", "v129", "v130", "v131", "v132", "v133", "v134", "v135", "v136", "v137", "v138", "v139", "v140", "v141", "v142", "v143", "v144", "v145", "v146", "v147", "v148", "v149", "v150", "v151", "v152", "v153", "v154", "v155", "v156", "v157", "v158", "v159", "v160", "v161", "v162", "v163", "v164", "v165", "v166", "v167", "v168", "v169", "v170", "v171", "v172", "v173", "v174", "v175", "v176", "v177", "v178", "v179", "v180", "v181", "v182", "v183", "v184", "v185", "v186", "v187", "v188", "v189", "v190", "v191", "v192", "v193", "v194", "v195", "v196", "v197", "v198", "v199", "v200", "v201", "v202", "v203", "v204", "v205", "v206", "v207", "v208", "v209", "v210", "v211")

#define H_LOOP2() \
  asm volatile( \
    "v_mov_b32 v20, 0\n\t" \
    "v_mov_b32 v21, 0\n\t" \
    "v_mov_b32 v22, 0\n\t" \
    "v_mov_b32 v23, 0\n\t" \
    "v_mov_b32 v24, 0\n\t" \
    "v_mov_b32 v25, 0\n\t" \
    "v_mov_b32 v26, 0\n\t" \
    "v_mov_b32 v27, 0\n\t" \
    "v_mov_b32 v28, 0\n\t" \
    "v_mov_b32 v29, 0\n\t" \
    "v_mov_b32 v30, 0\n\t" \
    "v_mov_b32 v31, 0\n\t" \
    "v_mov_b32 v32, 0\n\t" \
    "v_mov_b32 v33, 0\n\t" \
    "v_mov_b32 v34, 0\n\t" \
    "v_mov_b32 v35, 0\n\t" \
    "v_mov_b32 v36, 0\n\t" \
    "v_mov_b32 v37, 0\n\t" \
    "v_mov_b32 v38, 0\n\t" \
    "v_mov_b32 v39, 0\n\t" \
    "v_mov_b32 v40, 0\n\t" \
    "v_mov_b32 v41, 0\n\t" \
    "v_mov_b32 v42, 0\n\t" \
    "v_mov_b32 v43, 0\n\t" \
    "v_mov_b32 v44, 0\n\t" \
    "v_mov_b32 v45, 0\n\t" \
    "v_mov_b32 v46, 0\n\t" \
    "v_mov_b32 v47, 0\n\t" \
    "v_mov_b32 v48, 0\n\t" \
    "v_mov_b32 v49, 0\n\t" \
    "v_mov_b32 v50, 0\n\t" \
    "v_mov_b32 v51, 0\n\t" \
    "v_mov_b32 v52, 0\n\t" \
    "v_mov_b32 v53, 0\n\t" \
    "v_mov_b32 v54, 0\n\t" \
    "v_mov_b32 v55, 0\n\t" \
    "v_mov_b32 v56, 0\n\t" \
    "v_mov_b32 v57, 0\n\t" \
    "v_mov_b32 v58, 0\n\t" \
    "v_mov_b32 v59, 0\n\t" \
    "v_mov_b32 v60, 0\n\t" \
    "v_mov_b32 v61, 0\n\t" \
    "v_mov_b32 v62, 0\n\t" \
    "v_mov_b32 v63, 0\n\t" \
    "v_mov_b32 v64, 0\n\t" \
    "v_mov_b32 v65, 0\n\t" \
    "v_mov_b32 v66, 0\n\t" \
    "v_mov_b32 v67, 0\n\t" \
    "v_mov_b32 v68, 0\n\t" \
    "v_mov_b32 v69, 0\n\t" \
    "v_mov_b32 v70, 0\n\t" \
    "v_mov_b32 v71, 0\n\t" \
    "v_mov_b32 v72, 0\n\t" \
    "v_mov_b32 v73, 0\n\t" \
    "v_mov_b32 v74, 0\n\t" \
    "v_mov_b32 v75, 0\n\t" \
    "v_mov_b32 v76, 0\n\t" \
    "v_mov_b32 v77, 0\n\t" \
    "v_mov_b32 v78, 0\n\t" \
    "v_mov_b32 v79, 0\n\t" \
    "v_mov_b32 v80, 0\n\t" \
    "v_mov_b32 v81, 0\n\t" \
    "v_mov_b32 v82, 0\n\t" \
    "v_mov_b32 v83, 0\n\t" \
    "s_waitcnt vmcnt(12)\n\t" \
    "s_barrier\n\t" \
    "ds_read_b64_tr_b16 v[116:117], %[b0] offset:0\n\t" \
    "ds_read_b64_tr_b16 v[118:119], %[b0] offset:1024\n\t" \
    "ds_read_b64_tr_b16 v[124:125], %[b1] offset:0\n\t" \
    "ds_read_b64_tr_b16 v[126:127], %[b1] offset:1024\n\t" \
    "ds_read_b64_tr_b16 v[120:121], %[b0] offset:8192\n\t" \
    "ds_read_b64_tr_b16 v[122:123], %[b0] offset:9216\n\t" \
    "ds_read_b64_tr_b16 v[128:129], %[b1] offset:8192\n\t" \
    "ds_read_b64_tr_b16 v[130:131], %[b1] offset:9216\n\t" \
    "ds_read_b128 v[84:87], %[a] offset:0\n\t" \
    "ds_read_b128 v[88:91], %[a] offset:1024\n\t" \
    "ds_read_b128 v[92:95], %[a] offset:2048\n\t" \
    "ds_read_b128 v[96:99], %[a] offset:3072\n\t" \
    "ds_read_b128 v[100:103], %[a] offset:4096\n\t" \
    "ds_read_b128 v[104:107], %[a] offset:5120\n\t" \
    "ds_read_b128 v[108:111], %[a] offset:6144\n\t" \
    "ds_read_b128 v[112:115], %[a] offset:7168\n\t" \
    "ds_read_b64_tr_b16 v[132:133], %[b0] offset:16384\n\t" \
    "ds_read_b64_tr_b16 v[134:135], %[b0] offset:17408\n\t" \
    "ds_read_b64_tr_b16 v[140:141], %[b1] offset:16384\n\t" \
    "ds_read_b64_tr_b16 v[142:143], %[b1] offset:17408\n\t" \
    "ds_read_b64_tr_b16 v[136:137], %[b0] offset:24576\n\t" \
    "ds_read_b64_tr_b16 v[138:139], %[b0] offset:25600\n\t" \
    "ds_read_b64_tr_b16 v[144:145], %[b1] offset:24576\n\t" \
    "ds_read_b64_tr_b16 v[146:147], %[b1] offset:25600\n\t" \
    "L_%=:\n\t" \
    "s_add_u32 %[d], %[lw], %[c]\n\t" \
    "s_add_u32 %[y], %[c], 49152\n\t" \
    "s_cmp_eq_u32 %[y], 147456\n\t" \
    "s_cselect_b32 %[y], 0, %[y]\n\t" \
    "s_sub_u32 %[x], %[y], %[c]\n\t" \
    "s_mov_b32 %[c], %[y]\n\t" \
    "v_add_u32 %[a], %[x], %[a]\n\t" \
    "v_add_u32 %[b0], %[x], %[b0]\n\t" \
    "v_add_u32 %[b1], %[x], %[b1]\n\t" \
    "s_add_u32 %[x], %[t], 3\n\t" \
    "s_cmp_lt_u32 %[x], %[nt]\n\t" \
    "s_cselect_b32 %[x], %[x], 0\n\t" \
    "s_mul_i32 %[y], %[x], %[kb]\n\t" \
    "s_add_u32 %[y], %[y], %[cb]\n\t" \
    "s_lshl_b32 %[x], %[x], 11\n\t" \
    "s_add_u32 %[x], %[x], %[ca]\n\t" \
    "s_waitcnt vmcnt(6) lgkmcnt(0)\n\t" \
    "s_barrier\n\t" \
    "v_mfma_f32_16x16x32_bf16 v[20:23], v[116:119], v[84:87], v[20:23]\n\t" \
    "ds_read_b64_tr_b16 v[180:181], %[b0] offset:0\n\t" \
    "s_add_u32 m0, %[d], 0\n\t" \
    "v_mfma_f32_16x16x32_bf16 v[24:27], v[124:127], v[84:87], v[24:27]\n\t" \
    "buffer_load_dwordx4 %[vb0], %[rb], %[y] offen lds\n\t" \
    "v_mfma_f32_16x16x32_bf16 v[20:23], v[120:123], v[88:91], v[20:23]\n\t" \
    "ds_read_b64_tr_b16 v[182:183], %[b0] offset:1024\n\t" \
    "v_mfma_f32_16x16x32_bf16 v[24:27], v[128:131], v[88:91], v[24:27]\n\t" \
    "ds_read_b64_tr_b16 v[188:189], %[b1] offset:0\n\t" \
    "v_mfma_f32_16x16x32_bf16 v[28:31], v[116:119], v[92:95], v[28:31]\n\t" \
    "ds_read_b64_tr_b16 v[190:191], %[b1] offset:1024\n\t" \
    "v_mfma_f32_16x16x32_bf16 v[32:35], v[124:127], v[92:95], v[32:35]\n\t" \
    "ds_read_b64_tr_b16 v[184:185], %[b0] offset:8192\n\t" \
    "s_add_u32 m0, %[d], 8192\n\t" \
    "v_mfma_f32_16x16x32_bf16 v[28:31], v[120:123], v[96:99], v[28:31]\n\t" \
    "buffer_load_dwordx4 %[vb1], %[rb], %[y] offen lds\n\t" \
    "v_mfma_f32_16x16x32_bf16 v[32:35], v[128:131], v[96:99], v[32:35]\n\t" \
    "ds_read_b64_tr_b16 v[186:187], %[b0] offset:9216\n\t" \
    "v_mfma_f32_16x16x32_bf16 v[36:39], v[116:119], v[100:103], v[36:39]\n\t" \
    "ds_read_b64_tr_b16 v[192:193], %[b1] offset:8192\n\t" \
    "v_mfma_f32_16x16x32_bf16 v[40:43], v[124:127], v[100:103], v[40:43]\n\t" \
    "ds_read_b64_tr_b16 v[194:195], %[b1] offset:9216\n\t" \
    "v_mfma_f32_16x16x32_bf16 v[36:39], v[120:123], v[104:107], v[36:39]\n\t" \
    "ds_read_b128 v[148:151], %[a] offset:0\n\t" \
    "s_add_u32 m0, %[d], 32768\n\t" \
    "v_mfma_f32_16x16x32_bf16 v[40:43], v[128:131], v[104:107], v[40:43]\n\t" \
    "buffer_load_dwordx4 %[va0], %[ra], %[x] offen sc1 lds\n\t" \
    "v_mfma_f32_16x16x32_bf16 v[44:47], v[116:119], v[108:111], v[44:47]\n\t" \
    "ds_read_b128 v[152:155], %[a] offset:1024\n\t" \
    "v_mfma_f32_16x16x32_bf16 v[48:51], v[124:127], v[108:111], v[48:51]\n\t" \
    "ds_read_b128 v[156:159], %[a] offset:2048\n\t" \
    "v_mfma_f32_16x16x32_bf16 v[44:47], v[120:123], v[112:115], v[44:47]\n\t" \
    "ds_read_b128 v[160:163], %[a] offset:3072\n\t" \
    "v_mfma_f32_16x16x32_bf16 v[48:51], v[128:131], v[112:115], v[48:51]\n\t" \
    "ds_read_b128 v[164:167], %[a] offset:4096\n\t" \
    "s_add_u32 m0, %[d], 40960\n\t" \
    "v_mfma_f32_16x16x32_bf16 v[52:55], v[132:135], v[84:87], v[52:55]\n\t" \
    "buffer_load_dwordx4 %[va1], %[ra], %[x] offen sc1 lds\n\t" \
    "v_mfma_f32_16x16x32_bf16 v[56:59], v[140:143], v[84:87], v[56:59]\n\t" \
    "ds_read_b128 v[168:171], %[a] offset:5120\n\t" \
    "v_mfma_f32_16x16x32_bf16 v[52:55], v[136:139], v[88:91], v[52:55]\n\t" \
    "ds_read_b128 v[172:175], %[a] offset:6144\n\t" \
    "v_mfma_f32_16x16x32_bf16 v[56:59], v[144:147], v[88:91], v[56:59]\n\t" \
    "ds_read_b128 v[176:179], %[a] offset:7168\n\t" \
    "v_mfma_f32_16x16x32_bf16 v[60:63], v[132:135], v[92:95], v[60:63]\n\t" \
    "ds_read_b64_tr_b16 v[196:197], %[b0] offset:16384\n\t" \
    "s_add_u32 m0, %[d], 16128\n\t" \
    "v_mfma_f32_16x16x32_bf16 v[64:67], v[140:143], v[92:95], v[64:67]\n\t" \
    "buffer_load_dwordx4 %[vb0], %[rb], %[y] offen offset:256 lds\n\t" \
    "v_mfma_f32_16x16x32_bf16 v[60:63], v[136:139], v[96:99], v[60:63]\n\t" \
    "ds_read_b64_tr_b16 v[198:199], %[b0] offset:17408\n\t" \
    "v_mfma_f32_16x16x32_bf16 v[64:67], v[144:147], v[96:99], v[64:67]\n\t" \
    "ds_read_b64_tr_b16 v[204:205], %[b1] offset:16384\n\t" \
    "v_mfma_f32_16x16x32_bf16 v[68:71], v[132:135], v[100:103], v[68:71]\n\t" \
    "ds_read_b64_tr_b16 v[206:207], %[b1] offset:17408\n\t" \
    "v_mfma_f32_16x16x32_bf16 v[72:75], v[140:143], v[100:103], v[72:75]\n\t" \
    "ds_read_b64_tr_b16 v[200:201], %[b0] offset:24576\n\t" \
    "s_add_u32 m0, %[d], 24320\n\t" \
    "v_mfma_f32_16x16x32_bf16 v[68:71], v[136:139], v[104:107], v[68:71]\n\t" \
    "buffer_load_dwordx4 %[vb1], %[rb], %[y] offen offset:256 lds\n\t" \
    "v_mfma_f32_16x16x32_bf16 v[72:75], v[144:147], v[104:107], v[72:75]\n\t" \
    "ds_read_b64_tr_b16 v[202:203], %[b0] offset:25600\n\t" \
    "v_mfma_f32_16x16x32_bf16 v[76:79], v[132:135], v[108:111], v[76:79]\n\t" \
    "ds_read_b64_tr_b16 v[208:209], %[b1] offset:24576\n\t" \
    "v_mfma_f32_16x16x32_bf16 v[80:83], v[140:143], v[108:111], v[80:83]\n\t" \
    "ds_read_b64_tr_b16 v[210:211], %[b1] offset:25600\n\t" \
    "v_mfma_f32_16x16x32_bf16 v[76:79], v[136:139], v[112:115], v[76:79]\n\t" \
    "v_mfma_f32_16x16x32_bf16 v[80:83], v[144:147], v[112:115], v[80:83]\n\t" \
    "s_mov_b32 m0, %[pd]\n\t" \
    "s_cmp_lt_u32 %[ps], %[pe]\n\t" \
    "s_cselect_b64 exec, -1, 0\n\t" \
    "buffer_load_dword %[pv], %[rp], %[ps] offen sc1 lds\n\t" \
    "s_mov_b64 exec, -1\n\t" \
    "s_add_u32 %[ps], %[ps], %[pk]\n\t" \
    "s_add_u32 %[t], %[t], 1\n\t" \
    "s_add_u32 %[d], %[lw], %[c]\n\t" \
    "s_add_u32 %[y], %[c], 49152\n\t" \
    "s_cmp_eq_u32 %[y], 147456\n\t" \
    "s_cselect_b32 %[y], 0, %[y]\n\t" \
    "s_sub_u32 %[x], %[y], %[c]\n\t" \
    "s_mov_b32 %[c], %[y]\n\t" \
    "v_add_u32 %[a], %[x], %[a]\n\t" \
    "v_add_u32 %[b0], %[x], %[b0]\n\t" \
    "v_add_u32 %[b1], %[x], %[b1]\n\t" \
    "s_add_u32 %[x], %[t], 3\n\t" \
    "s_cmp_lt_u32 %[x], %[nt]\n\t" \
    "s_cselect_b32 %[x], %[x], 0\n\t" \
    "s_mul_i32 %[y], %[x], %[kb]\n\t" \
    "s_add_u32 %[y], %[y], %[cb]\n\t" \
    "s_lshl_b32 %[x], %[x], 11\n\t" \
    "s_add_u32 %[x], %[x], %[ca]\n\t" \
    "s_waitcnt vmcnt(6) lgkmcnt(0)\n\t" \
    "s_barrier\n\t" \
    "v_mfma_f32_16x16x32_bf16 v[20:23], v[180:183], v[148:151], v[20:23]\n\t" \
    "ds_read_b64_tr_b16 v[116:117], %[b0] offset:0\n\t" \
    "s_add_u32 m0, %[d], 0\n\t" \
    "v_mfma_f32_16x16x32_bf16 v[24:27], v[188:191], v[148:151], v[24:27]\n\t" \
    "buffer_load_dwordx4 %[vb0], %[rb], %[y] offen lds\n\t" \
    "v_mfma_f32_16x16x32_bf16 v[20:23], v[184:187], v[152:155], v[20:23]\n\t" \
    "ds_read_b64_tr_b16 v[118:119], %[b0] offset:1024\n\t" \
    "v_mfma_f32_16x16x32_bf16 v[24:27], v[192:195], v[152:155], v[24:27]\n\t" \
    "ds_read_b64_tr_b16 v[124:125], %[b1] offset:0\n\t" \
    "v_mfma_f32_16x16x32_bf16 v[28:31], v[180:183], v[156:159], v[28:31]\n\t" \
    "ds_read_b64_tr_b16 v[126:127], %[b1] offset:1024\n\t" \
    "v_mfma_f32_16x16x32_bf16 v[32:35], v[188:191], v[156:159], v[32:35]\n\t" \
    "ds_read_b64_tr_b16 v[120:121], %[b0] offset:8192\n\t" \
    "s_add_u32 m0, %[d], 8192\n\t" \
    "v_mfma_f32_16x16x32_bf16 v[28:31], v[184:187], v[160:163], v[28:31]\n\t" \
    "buffer_load_dwordx4 %[vb1], %[rb], %[y] offen lds\n\t" \
    "v_mfma_f32_16x16x32_bf16 v[32:35], v[192:195], v[160:163], v[32:35]\n\t" \
    "ds_read_b64_tr_b16 v[122:123], %[b0] offset:9216\n\t" \
    "v_mfma_f32_16x16x32_bf16 v[36:39], v[180:183], v[164:167], v[36:39]\n\t" \
    "ds_read_b64_tr_b16 v[128:129], %[b1] offset:8192\n\t" \
    "v_mfma_f32_16x16x32_bf16 v[40:43], v[188:191], v[164:167], v[40:43]\n\t" \
    "ds_read_b64_tr_b16 v[130:131], %[b1] offset:9216\n\t" \
    "v_mfma_f32_16x16x32_bf16 v[36:39], v[184:187], v[168:171], v[36:39]\n\t" \
    "ds_read_b128 v[84:87], %[a] offset:0\n\t" \
    "s_add_u32 m0, %[d], 32768\n\t" \
    "v_mfma_f32_16x16x32_bf16 v[40:43], v[192:195], v[168:171], v[40:43]\n\t" \
    "buffer_load_dwordx4 %[va0], %[ra], %[x] offen sc1 lds\n\t" \
    "v_mfma_f32_16x16x32_bf16 v[44:47], v[180:183], v[172:175], v[44:47]\n\t" \
    "ds_read_b128 v[88:91], %[a] offset:1024\n\t" \
    "v_mfma_f32_16x16x32_bf16 v[48:51], v[188:191], v[172:175], v[48:51]\n\t" \
    "ds_read_b128 v[92:95], %[a] offset:2048\n\t" \
    "v_mfma_f32_16x16x32_bf16 v[44:47], v[184:187], v[176:179], v[44:47]\n\t" \
    "ds_read_b128 v[96:99], %[a] offset:3072\n\t" \
    "v_mfma_f32_16x16x32_bf16 v[48:51], v[192:195], v[176:179], v[48:51]\n\t" \
    "ds_read_b128 v[100:103], %[a] offset:4096\n\t" \
    "s_add_u32 m0, %[d], 40960\n\t" \
    "v_mfma_f32_16x16x32_bf16 v[52:55], v[196:199], v[148:151], v[52:55]\n\t" \
    "buffer_load_dwordx4 %[va1], %[ra], %[x] offen sc1 lds\n\t" \
    "v_mfma_f32_16x16x32_bf16 v[56:59], v[204:207], v[148:151], v[56:59]\n\t" \
    "ds_read_b128 v[104:107], %[a] offset:5120\n\t" \
    "v_mfma_f32_16x16x32_bf16 v[52:55], v[200:203], v[152:155], v[52:55]\n\t" \
    "ds_read_b128 v[108:111], %[a] offset:6144\n\t" \
    "v_mfma_f32_16x16x32_bf16 v[56:59], v[208:211], v[152:155], v[56:59]\n\t" \
    "ds_read_b128 v[112:115], %[a] offset:7168\n\t" \
    "v_mfma_f32_16x16x32_bf16 v[60:63], v[196:199], v[156:159], v[60:63]\n\t" \
    "ds_read_b64_tr_b16 v[132:133], %[b0] offset:16384\n\t" \
    "s_add_u32 m0, %[d], 16128\n\t" \
    "v_mfma_f32_16x16x32_bf16 v[64:67], v[204:207], v[156:159], v[64:67]\n\t" \
    "buffer_load_dwordx4 %[vb0], %[rb], %[y] offen offset:256 lds\n\t" \
    "v_mfma_f32_16x16x32_bf16 v[60:63], v[200:203], v[160:163], v[60:63]\n\t" \
    "ds_read_b64_tr_b16 v[134:135], %[b0] offset:17408\n\t" \
    "v_mfma_f32_16x16x32_bf16 v[64:67], v[208:211], v[160:163], v[64:67]\n\t" \
    "ds_read_b64_tr_b16 v[140:141], %[b1] offset:16384\n\t" \
    "v_mfma_f32_16x16x32_bf16 v[68:71], v[196:199], v[164:167], v[68:71]\n\t" \
    "ds_read_b64_tr_b16 v[142:143], %[b1] offset:17408\n\t" \
    "v_mfma_f32_16x16x32_bf16 v[72:75], v[204:207], v[164:167], v[72:75]\n\t" \
    "ds_read_b64_tr_b16 v[136:137], %[b0] offset:24576\n\t" \
    "s_add_u32 m0, %[d], 24320\n\t" \
    "v_mfma_f32_16x16x32_bf16 v[68:71], v[200:203], v[168:171], v[68:71]\n\t" \
    "buffer_load_dwordx4 %[vb1], %[rb], %[y] offen offset:256 lds\n\t" \
    "v_mfma_f32_16x16x32_bf16 v[72:75], v[208:211], v[168:171], v[72:75]\n\t" \
    "ds_read_b64_tr_b16 v[138:139], %[b0] offset:25600\n\t" \
    "v_mfma_f32_16x16x32_bf16 v[76:79], v[196:199], v[172:175], v[76:79]\n\t" \
    "ds_read_b64_tr_b16 v[144:145], %[b1] offset:24576\n\t" \
    "v_mfma_f32_16x16x32_bf16 v[80:83], v[204:207], v[172:175], v[80:83]\n\t" \
    "ds_read_b64_tr_b16 v[146:147], %[b1] offset:25600\n\t" \
    "v_mfma_f32_16x16x32_bf16 v[76:79], v[200:203], v[176:179], v[76:79]\n\t" \
    "v_mfma_f32_16x16x32_bf16 v[80:83], v[208:211], v[176:179], v[80:83]\n\t" \
    "s_mov_b32 m0, %[pd]\n\t" \
    "s_cmp_lt_u32 %[ps], %[pe]\n\t" \
    "s_cselect_b64 exec, -1, 0\n\t" \
    "buffer_load_dword %[pv], %[rp], %[ps] offen sc1 lds\n\t" \
    "s_mov_b64 exec, -1\n\t" \
    "s_add_u32 %[ps], %[ps], %[pk]\n\t" \
    "s_add_u32 %[t], %[t], 1\n\t" \
    "s_cmp_lt_u32 %[t], %[nt]\n\t" \
    "s_cbranch_scc1 L_%=\n\t" \
    "s_waitcnt lgkmcnt(0)\n\t" \
    "s_nop 7\n\t" \
    : "=&{v[20:23]}"(acc[0][0][0][0]), "=&{v[24:27]}"(acc[0][0][0][1]), "=&{v[28:31]}"(acc[0][0][1][0]), "=&{v[32:35]}"(acc[0][0][1][1]), "=&{v[36:39]}"(acc[0][0][2][0]), "=&{v[40:43]}"(acc[0][0][2][1]), "=&{v[44:47]}"(acc[0][0][3][0]), "=&{v[48:51]}"(acc[0][0][3][1]), "=&{v[52:55]}"(acc[0][1][0][0]), "=&{v[56:59]}"(acc[0][1][0][1]), "=&{v[60:63]}"(acc[0][1][1][0]), "=&{v[64:67]}"(acc[0][1][1][1]), "=&{v[68:71]}"(acc[0][1][2][0]), "=&{v[72:75]}"(acc[0][1][2][1]), "=&{v[76:79]}"(acc[0][1][3][0]), "=&{v[80:83]}"(acc[0][1][3][1]), [a] "+v"(aoff), [b0] "+v"(boff0), [b1] "+v"(boff1), [c] "+s"(cur), [t] "+s"(t), [ps] "+s"(ps), [d] "=&s"(sd), [x] "=&s"(sx), [y] "=&s"(sy) \
    : [va0] "v"(voffA[0]), [va1] "v"(voffA[1]), [vb0] "v"(voffB[0]), [vb1] "v"(voffB[1]), [pv] "v"(pf.v), [ra] "s"(ra), [rb] "s"(rb), [rp] "s"(rp), [lw] "s"(ldsw), [nt] "s"(nt), [kb] "s"(kB), [ca] "s"(cA), [cb] "s"(cB), [pd] "s"(pd), [pe] "s"(pe), [pk] "s"(pf.step) \
    : "memory", "scc", "v84", "v85", "v86", "v87", "v88", "v89", "v90", "v91", "v92", "v93", "v94", "v95", "v96", "v97", "v98", "v99", "v100", "v101", "v102", "v103", "v104", "v105", "v106", "v107", "v108", "v109", "v110", "v111", "v112", "v113", "v114", "v115", "v116", "v117", "v118", "v119", "v120", "v121", "v122", "v123", "v124", "v125", "v126", "v127", "v128", "v129", "v130", "v131", "v132", "v133", "v134", "v135", "v136", "v137", "v138", "v139", "v140", "v141", "v142", "v143", "v144", "v145", "v146", "v147", "v148", "v149", "v150", "v151", "v152", "v153", "v154", "v155", "v156", "v157", "v158", "v159", "v160", "v161", "v162", "v163", "v164", "v165", "v166", "v167", "v168", "v169", "v170", "v171", "v172", "v173", "v174", "v175", "v176", "v177", "v178", "v179", "v180", "v181", "v182", "v183", "v184", "v185", "v186", "v187", "v188", "v189", "v190", "v191", "v192", "v193", "v194", "v195", "v196", "v197", "v198", "v199", "v200", "v201", "v202", "v203", "v204", "v205", "v206", "v207", "v208", "v209", "v210", "v211")
#define AQ_PIN0() asm volatile("" : "+{v[28:35]}"(wr[0][0]), "+{v[36:43]}"(wr[0][1]))
#define AQ_PIN1() asm volatile("" : "+{v[44:51]}"(wr[1][0]), "+{v[52:59]}"(wr[1][1]))
#define AQ_PIN2() asm volatile("" : "+{v[60:67]}"(wr[2][0]), "+{v[68:75]}"(wr[2][1]))
#define AQ_PIN3() asm volatile("" : "+{v[76:83]}"(wr[3][0]), "+{v[84:91]}"(wr[3][1]))
#define AQ_PIN4() asm volatile("" : "+{v[92:99]}"(wr[4][0]), "+{v[100:107]}"(wr[4][1]))
#define AQ_PIN5() asm volatile("" : "+{v[108:115]}"(wr[5][0]), "+{v[116:123]}"(wr[5][1]))
#define AQ_PIN6() asm volatile("" : "+{v[124:131]}"(wr[6][0]), "+{v[132:139]}"(wr[6][1]))
#define AQ_PIN7() asm volatile("" : "+{v[140:147]}"(wr[7][0]), "+{v[148:155]}"(wr[7][1]))
#define AQ_PIN8() asm volatile("" : "+{v[156:163]}"(wr[8][0]), "+{v[164:171]}"(wr[8][1]))
#define AQ_PIN9() asm volatile("" : "+{v[172:179]}"(wr[9][0]), "+{v[180:187]}"(wr[9][1]))
#define AQ_PIN10() asm volatile("" : "+{v[188:195]}"(wr[10][0]), "+{v[196:203]}"(wr[10][1]))
#define AQ_PIN11() asm volatile("" : "+{v[204:211]}"(wr[11][0]), "+{v[212:219]}"(wr[11][1]))
#define AK_PIN0() asm volatile("" : "+{v[20:27]}"(wr[0][0]), "+{v[28:35]}"(wr[0][1]))
#define AK_PIN1() asm volatile("" : "+{v[36:43]}"(wr[1][0]), "+{v[44:51]}"(wr[1][1]))
#define AK_PIN2() asm volatile("" : "+{v[52:59]}"(wr[2][0]), "+{v[60:67]}"(wr[2][1]))
#define AK_PIN3() asm volatile("" : "+{v[68:75]}"(wr[3][0]), "+{v[76:83]}"(wr[3][1]))
#define AK_PIN4() asm volatile("" : "+{v[84:91]}"(wr[4][0]), "+{v[92:99]}"(wr[4][1]))
#define AK_PIN5() asm volatile("" : "+{v[100:107]}"(wr[5][0]), "+{v[108:115]}"(wr[5][1]))
#define AK_PIN6() asm volatile("" : "+{v[116:123]}"(wr[6][0]), "+{v[124:131]}"(wr[6][1]))
#define AK_PIN7() asm volatile("" : "+{v[132:139]}"(wr[7][0]), "+{v[140:147]}"(wr[7][1]))
#define AK_PIN8() asm volatile("" : "+{v[148:155]}"(wr[8][0]), "+{v[156:163]}"(wr[8][1]))
#define AK_PIN9() asm volatile("" : "+{v[164:171]}"(wr[9][0]), "+{v[172:179]}"(wr[9][1]))
#define AK_PIN10() asm volatile("" : "+{v[180:187]}"(wr[10][0]), "+{v[188:195]}"(wr[10][1]))
#define AK_PIN11() asm volatile("" : "+{v[196:203]}"(wr[11][0]), "+{v[204:211]}"(wr[11][1]))
#define V_PIN() asm volatile("" : "+{v[28:35]}"(wv[0]), "+{v[36:43]}"(wv[1]), "+{v[44:51]}"(wv[2]), "+{v[52:59]}"(wv[3]), "+{v[60:67]}"(wv[4]), "+{v[68:75]}"(wv[5]), "+{v[76:83]}"(wv[6]), "+{v[84:91]}"(wv[7]), "+{v[92:99]}"(wv[8]), "+{v[100:107]}"(wv[9]), "+{v[108:115]}"(wv[10]), "+{v[116:123]}"(wv[11]), "+{v[124:131]}"(wv[12]), "+{v[132:139]}"(wv[13]), "+{v[140:147]}"(wv[14]), "+{v[148:155]}"(wv[15]))
#define WOV_PIN0() asm volatile("" : "+{v[156:163]}"(wo[0][0][0]), "+{v[164:171]}"(wo[0][0][1]), "+{v[172:179]}"(wo[0][1][0]), "+{v[180:187]}"(wo[0][1][1]))
#define WOV_PIN1() asm volatile("" : "+{v[188:195]}"(wo[1][0][0]), "+{v[196:203]}"(wo[1][0][1]), "+{v[204:211]}"(wo[1][1][0]), "+{v[212:219]}"(wo[1][1][1]))
#define WOW_PIN0() asm volatile("" : "+{v[28:35]}"(wo[0][0][0]), "+{v[36:43]}"(wo[0][0][1]), "+{v[44:51]}"(wo[0][1][0]), "+{v[52:59]}"(wo[0][1][1]))
#define WOW_PIN1() asm volatile("" : "+{v[60:67]}"(wo[1][0][0]), "+{v[68:75]}"(wo[1][0][1]), "+{v[76:83]}"(wo[1][1][0]), "+{v[84:91]}"(wo[1][1][1]))
#define WOW_PIN2() asm volatile("" : "+{v[92:99]}"(wo[2][0][0]), "+{v[100:107]}"(wo[2][0][1]), "+{v[108:115]}"(wo[2][1][0]), "+{v[116:123]}"(wo[2][1][1]))
#define WOW_PIN3() asm volatile("" : "+{v[124:131]}"(wo[3][0][0]), "+{v[132:139]}"(wo[3][0][1]), "+{v[140:147]}"(wo[3][1][0]), "+{v[148:155]}"(wo[3][1][1]))
#define WOW_PIN4() asm volatile("" : "+{v[156:163]}"(wo[4][0][0]), "+{v[164:171]}"(wo[4][0][1]), "+{v[172:179]}"(wo[4][1][0]), "+{v[180:187]}"(wo[4][1][1]))
#define WOW_PIN5() asm volatile("" : "+{v[188:195]}"(wo[5][0][0]), "+{v[196:203]}"(wo[5][0][1]), "+{v[204:211]}"(wo[5][1][0]), "+{v[212:219]}"(wo[5][1][1]))
#define A_PIN(QW, i) do { switch (i) { case 0: if (QW) AQ_PIN0(); else AK_PIN0(); break; case 1: if (QW) AQ_PIN1(); else AK_PIN1(); break; case 2: if (QW) AQ_PIN2(); else AK_PIN2(); break; case 3: if (QW) AQ_PIN3(); else AK_PIN3(); break; case 4: if (QW) AQ_PIN4(); else AK_PIN4(); break; case 5: if (QW) AQ_PIN5(); else AK_PIN5(); break; case 6: if (QW) AQ_PIN6(); else AK_PIN6(); break; case 7: if (QW) AQ_PIN7(); else AK_PIN7(); break; case 8: if (QW) AQ_PIN8(); else AK_PIN8(); break; case 9: if (QW) AQ_PIN9(); else AK_PIN9(); break; case 10: if (QW) AQ_PIN10(); else AK_PIN10(); break; case 11: if (QW) AQ_PIN11(); else AK_PIN11(); break; } } while (0)
#define WO_PIN(VW, i) do { switch (i) { case 0: if (VW) WOV_PIN0(); else WOW_PIN0(); break; case 1: if (VW) WOV_PIN1(); else WOW_PIN1(); break; case 2: if (!VW) WOW_PIN2(); break; case 3: if (!VW) WOW_PIN3(); break; case 4: if (!VW) WOW_PIN4(); break; case 5: if (!VW) WOW_PIN5(); break; } } while (0)

__device__ __forceinline__ int lds_byte(int r, int c) {
  const int st = (r >> 4) * 2 + (c >> 5), rr = r & 15, cc = c & 31, ob = rr * 64 + cc * 2;
  return st * 1024 + (ob ^ (((ob >> 9) & 1) << 5));
}
#define PFNT ""
#define G_DvoffB dmaw
#define PFH 5
struct Pf { unsigned v, step; bool on, a; };
#define G_PF(t) do { if (pf.on && (t) < nt) asm volatile("s_mov_b32 m0, %3\n\ts_nop 0\n\tbuffer_load_dword %0, %1, %2 offen sc1 " PFNT "lds" \
    :: "v"(pf.v), "s"(pf.a ? ra : rb), "s"((t) * pf.step), "s"((unsigned)(size_t)lds + 148480) : "memory"); } while (0)

#define G_ST1(bufoff, r, so, voff, i) G_D##voff(r, (voff)[i], so, ldsw + (bufoff) + (i) * 8192)
#define G_DvoffA dmas
#define G_STAGE(bufoff, so, voff) do { G_ST1(bufoff, G_R##voff, so, voff, 0); G_ST1(bufoff, G_R##voff, so, voff, 1); } while (0)
#define G_RvoffA ra
#define G_RvoffB rb
#define G_WAIT_V(n) asm volatile("s_waitcnt vmcnt(%0)" :: "n"(n) : "memory")
__device__ __forceinline__ void gemm_h(LAS unsigned char* lds, rsrc_t ra, rsrc_t rb, unsigned cA, unsigned cB, int lda, int ldb, int nt,
                                       const Pf& pf, bool wide, f32x4 (&acc)[1][2][4][2]) {
  const int wid = __builtin_amdgcn_readfirstlane(threadIdx.x >> 6), lane = opq(0), tid = wid * 64 + lane;
  const int wr = wid >> 2, wc = wid & 3, fr = lane & 15, fq = lane >> 4;
  unsigned voffA[2], voffB[2];
  #pragma unroll
  for (int i = 0; i < 2; ++i) {
    voffA[i] = (unsigned)(((wid >> 1) + 4 * i) * 16 * lda + (wid & 1) * 1024 + lane * 16);
    const int b = tid * 16 + i * 8192, r = b >> 8, pos = (b >> 4) & 15;
    const int c = pos ^ (((r & 3) << 1) | (((r >> 3) & 1) << 3));
    voffB[i] = (unsigned)(r * ldb + c * 16);
  }
  const unsigned kB = 64 * ldb;
  const unsigned ldsw = (unsigned)(size_t)lds + (unsigned)wid * 1024u;
  const int q = (lane & 15) >> 2, p = lane & 3;
  unsigned aoff = (unsigned)(size_t)lds + 32768 + lds_byte(wr * 64 + fr, fq * 8);
  unsigned boff0 = (unsigned)(size_t)lds + (8 * fq + q) * 256 + (((wc * 4 + (p >> 1)) ^ ((q << 1) | ((fq & 1) << 3))) << 4) + 8 * (p & 1), boff1 = boff0 ^ 32;
#define H_STAGE(so, t) do { G_STAGE(so, cB + (t) * kB, voffB); G_STAGE((so) + 32768, cA + (t) * 2048, voffA); if (wide) G_STAGE((so) + 16384, cB + (t) * kB + 256, voffB); } while (0)
  #pragma unroll
  for (int t = 3; t < PFH; ++t) G_PF(t);
  H_STAGE(0, 0); H_STAGE(49152, 1); H_STAGE(98304, 2);
  const rsrc_t rp = pf.a ? ra : rb;
  const unsigned pd = (unsigned)(size_t)lds + 148480, pe = pf.on ? nt * pf.step : 0u;
  unsigned cur = 0, t = 0, ps = PFH * pf.step, sd, sx, sy;
  if (wide) H_LOOP2(); else H_LOOP1();
}

__device__ __forceinline__ u32x4 epi_pack(const f32x4 a0, const f32x4 a1) {
  const unsigned x0 = pack2(a0[0], a0[1]), x1 = pack2(a0[2], a0[3]);
  const unsigned y0 = pack2(a1[0], a1[1]), y1 = pack2(a1[2], a1[3]);
  auto s0 = __builtin_amdgcn_permlane16_swap(x0, y0, false, false);
  auto s1 = __builtin_amdgcn_permlane16_swap(x1, y1, false, false);
  return (u32x4){s0[0], s1[0], s0[1], s1[1]};
}
__device__ __forceinline__ unsigned gelu2(unsigned w) {
  const f32x2 x = {lo16(w), hi16(w)};
  const float c0 = -2.f * 1.4426950408889634f * 0.7978845608028654f, c1 = c0 * 0.044715f;
  const f32x2 a = x * (x * x * c1 + c0);
  const f32x2 d = (f32x2){__builtin_amdgcn_exp2f(a[0]), __builtin_amdgcn_exp2f(a[1])} + 1.f;
  const f32x2 y = x * (f32x2){__builtin_amdgcn_rcpf(d[0]), __builtin_amdgcn_rcpf(d[1])};
  return pack2(y[0], y[1]);
}
__device__ __forceinline__ float silu_f(float b) { return b * __builtin_amdgcn_rcpf(1.f + __expf(-b)); }

__device__ __forceinline__ unsigned blk_in(int r, int c) { const int ob = r * 64 + c * 2; return ob ^ (((ob >> 9) & 1) << 5); }
__device__ __forceinline__ unsigned xn_blk(int rb, int cb) { return (unsigned)(((rb >> 1) * 32 + cb) * 2 + (rb & 1)) * 1024u; }

template <int EPI>
__device__ __forceinline__ void epi_rows(const f32x4 (&acc)[1][2][4][2], rsrc_t rs, unsigned obase, int ldc, int pm2, int pn2, int sh, bool wide, int wr, int wc, int ln) {
  const int fq = ln >> 4, cb = 16 * (fq & 1) + 8 * (fq >> 1), fr = (ln & 15) + sh;
  const unsigned lo = (unsigned)((fr >> 4) * 16 * ldc) + blk_in(fr & 15, cb);
  const unsigned ob = (unsigned)(pm2 * 128 + 64 * wr) * (unsigned)ldc + (unsigned)(pn2 * 4 + wc) * 1024u + lo;
  u32x4 r[8];
  if (EPI >= 2) {
    #pragma unroll
    for (int i = 0; i < 8; ++i)
      r[i] = ld16(rs, EPI == 2 ? WS_XN + xn_blk(pm2 * 8 + 4 * wr + (i & 3), pn2 * 4 + 4 * (i >> 2) + wc) + lo : WS_X1 + ob + (i & 3) * 32768 + (i >> 2) * 4096);
  }
  #pragma unroll
  for (int i = 0; i < 8; ++i) {
    if (i == 4 && !wide) break;
    u32x4 w = epi_pack(acc[0][i >> 2][i & 3][0], acc[0][i >> 2][i & 3][1]);
    #pragma unroll
    for (int k = 0; k < 4; ++k) {
      w[k] = EPI < 2 ? gelu2(w[k]) : pack2(lo16(r[i][k]) + lo16(w[k]), hi16(r[i][k]) + hi16(w[k]));
      if (EPI == 3) w[k] = pack2(silu_f(lo16(w[k])), silu_f(hi16(w[k])));
    }
    st16(rs, obase + ob + (unsigned)(16 * (i & 3) * ldc) + (i >> 2) * 4096, w);
  }
}

__device__ __forceinline__ float row_sum16(float v) {
  v += __uint_as_float(__builtin_amdgcn_update_dpp(0, (int)__float_as_uint(v), 0x128, 0xf, 0xf, false));
  v += __uint_as_float(__builtin_amdgcn_update_dpp(0, (int)__float_as_uint(v), 0x124, 0xf, 0xf, false));
  v += __uint_as_float(__builtin_amdgcn_update_dpp(0, (int)__float_as_uint(v), 0x122, 0xf, 0xf, false));
  v += __uint_as_float(__builtin_amdgcn_update_dpp(0, (int)__float_as_uint(v), 0x121, 0xf, 0xf, false));
  return v;
}
__device__ __forceinline__ void ln_rows(u32x4 (&a)[8]) {
  f32x2 v[32];
  #pragma unroll
  for (int i = 0; i < 32; ++i) v[i] = (f32x2){lo16(a[i >> 2][i & 3]), hi16(a[i >> 2][i & 3])};
  f32x2 s[4] = {v[0], v[1], v[2], v[3]};
  #pragma unroll
  for (int i = 4; i < 32; ++i) s[i & 3] += v[i];
  s[0] = (s[0] + s[1]) + (s[2] + s[3]);
  const float mean = row_sum16(s[0][0] + s[0][1]) * (1.f / DM);
  #pragma unroll
  for (int i = 0; i < 32; ++i) { const f32x2 d = v[i] - mean; s[i & 3] = i < 4 ? d * d : s[i & 3] + d * d; }
  s[0] = (s[0] + s[1]) + (s[2] + s[3]);
  const float var = row_sum16(s[0][0] + s[0][1]) * (1.f / DM);
  const float mb = rb1(mean), vb = rb1(var);
  const float rden = __builtin_amdgcn_rcpf(rb1(__builtin_amdgcn_sqrtf(rb1(vb + 1e-5f))));
  #pragma unroll
  for (int i = 0; i < 32; ++i) {
    const f32x2 d = v[i] - mb;
    const unsigned w = pack2(d[0], d[1]);
    const f32x2 y = (f32x2){lo16(w), hi16(w)} * rden;
    a[i >> 2][i & 3] = pack2(y[0], y[1]);
  }
}

template <int N>
__device__ __forceinline__ void fin_sum(rsrc_t rs, unsigned po, u32x4* v) {
  u32x4 pv[N][8];
  #pragma unroll
  for (int i = 0; i < 8 * N; ++i) pv[i >> 3][i & 7] = ld16(rs, po + (i >> 3) * 16384 + ((i >> 1) & 3) * 131072 + (i & 1) * 1024);
#define PF4(i) __builtin_bit_cast(f32x4, pv[c][i])
  #pragma unroll
  for (int c = 0; c < N; ++c) v[c] = epi_pack((PF4(0) + PF4(2)) + (PF4(4) + PF4(6)), (PF4(1) + PF4(3)) + (PF4(5) + PF4(7)));
}
__device__ __forceinline__ void fin_out(const Ptrs& P, unsigned eo, u32x4 v, u32x4 gv, u32x4 xv) {
  #pragma unroll
  for (int k = 0; k < 4; ++k) {
    v[k] = pack2(lo16(v[k]) * lo16(gv[k]), hi16(v[k]) * hi16(gv[k]));
    v[k] = pack2(lo16(xv[k]) + lo16(v[k]), hi16(xv[k]) + hi16(v[k]));
  }
  *(u32x4*)((char*)P.out + eo) = v;
}

__device__ __forceinline__ void mlp_role(LAS unsigned char* lds, const Ptrs& P, int hf, int slot, int wid) {
  const int wr = wid >> 2, wc = wid & 3;
  const int p2 = hf * 8 + (slot >> 2), pn = slot & 3;
  char* ws = P.ws;
  const rsrc_t rs = WS_RSRC(P);
  const u16* const p1 = P.w1; const u16* const p2w = P.w2; const u16* const p3 = P.w3; const u16* const p4 = P.w4;
#define M_DONE(c) do { VMWAIT(); BARRIER(); if (wid == 0) cnt_add(ws, c, opq(0)); } while (0)
  #pragma unroll 1
  for (int code = 0; code < 7; ++code) {
    int ln = opq(0);
    if (wid == 0) {
      if (code == 0) flag_wait(P, F_LN1(p2 * 4 + (ln & 3)), 0, ln);
      else if (code < 3 || code == 6) {
        const unsigned co = CNT_OFF + (code == 1 ? C_W1(min(max(p2 - 1 + min(ln, 2), 0), 15)) : code == 2 ? C_LN(p2) : C_W3(p2)) * 128;
        for (unsigned spins = 0; __any(ld4(rs, co) < 4u);) {
          __builtin_amdgcn_s_sleep(2);
          if (spin_fail(spins, P, ln)) break;
        }
      }
    }
    BARRIER();
    TLW(3 * code);
    const bool w3 = code >= 2 && code < 6, wide = code != 5;
    const int lda = code == 6 ? 7168 : 2048, ldb = w3 ? 7168 : 2048, c2 = w3 ? (code - 2) * 8 + (wide ? 2 * pn : pn) : 2 * pn;
    const rsrc_t rb = __builtin_amdgcn_make_buffer_rsrc((void*)(code == 0 ? p1 : code == 1 ? p2w : w3 ? p3 : p4), 0, code < 2 ? DM * DM * 2 : DM * FF * 2, RSRC_W3);
    const unsigned cA = (code == 0 ? WS_XN2 : code == 1 ? WS_H1 : w3 ? WS_X1N : WS_H3) + p2 * 128 * lda, cB = c2 * 256;
    Pf pf;
    pf.on = wc == 0 && (!wr || code < 3 || code == 6); pf.a = wr;
    ln &= 31;
    pf.v = wr ? cA + (pn * 2 + (ln >> 4)) * 16 * lda + (ln & 15) * 128 : cB + ((slot >> 2) * 8 + (ln >> 2)) * ldb + (ln & (wide ? 3 : 1)) * 128;
    pf.step = wr ? 2048 : 64 * ldb;
    f32x4 acq[1][2][4][2];
    gemm_h(lds, rs, rb, cA, cB, lda, ldb, code == 6 ? 56 : 16, pf, wide, acq);
    TLW(3 * code + 1);
    ln = opq(0);
    if (code == 1) {
      epi_rows<2>(acq, rs, WS_X1, 2048, p2, c2, 0, true, wr, wc, ln);
      M_DONE(C_W2(p2));
      if (wid == 0) cnt_wait(P, C_W2(p2), 4, ln);
      BARRIER();
      const unsigned ro = (unsigned)(p2 * 8 + pn * 2 + wr) * 32768u + ((ln >> 2) & 3) * 1024 + blk_in(wc * 4 + (ln >> 4), (ln & 3) * 8);
      u32x4 v[8];
      #pragma unroll
      for (int i = 0; i < 8; ++i) v[i] = ld16(rs, WS_X1 + ro + i * 4096);
      ln_rows(v);
      #pragma unroll
      for (int i = 0; i < 8; ++i) st16(rs, WS_X1N + ro + i * 4096, v[i]);
      M_DONE(C_LN(p2));
    } else if (code == 6) {
      epi_rows<3>(acq, rs, WS_G, 2048, p2, c2, 0, true, wr, wc, ln);
      M_DONE(C_W4(p2));
    } else {
      VMWAIT();
      if (code == 0 && wid < 4) touch(p2w, DM * DM * 2, ((hf * 32 + slot) * 256 + wid * 64 + ln) * 128, lds);
      epi_rows<1>(acq, rs, code ? WS_H3 : WS_H1, ldb, p2, c2, code ? 0 : (pn & 1) ? -1 - (pn >> 1) : 1 + (pn >> 1), wide, wr, wc, ln);
      if (code == 0 && wid == 0) setup_wait(P, ln);
      if (code == 0 || code == 5) M_DONE(code ? C_W3(p2) : C_W1(p2));
    }
    TLW(3 * code + 2);
  }
}

#define FINE 52
__device__ __forceinline__ void fin_role(LAS unsigned char* lds, const Ptrs& P, int wid) {
  const int wr = wid >> 2, wc = wid & 3;
  const rsrc_t rs = WS_RSRC(P);
  volatile LAS int* tk = (volatile LAS int*)(lds + 147456);
  unsigned tks = 0;
#define POP() do { if (wid == 0 && opq(0) == 0) tks = __hip_atomic_fetch_add((gu32*)(P.ws + CNT_OFF + C_HEAD * 128), 1u, RLX_AGENT); } while (0)
  POP();
  for (int round = 0;; ++round) {
    if (wid == 0 && opq(0) == 0) tk[round & 1] = tks;
    LBARRIER();
    const int u = __builtin_amdgcn_readfirstlane(tk[round & 1]);
    if (u >= 2 * FINE + 8 * (XS3 - FINE)) break;
    const bool big = u < 2 * FINE;
    const int sg = big ? u >> 1 : FINE + ((u - 2 * FINE) >> 3), cc0 = big ? (u & 1) * 4 : (u - 2 * FINE) & 7;
    const int ln = opq(0), fq = ln >> 4, cb = 16 * (fq & 1) + 8 * (fq >> 1);
    const unsigned eo = (unsigned)(sg * 32 + wr * 16 + (ln & 15)) * 2048u + (unsigned)(cc0 * 128 + wc * 32 + cb) * 2u;
    const unsigned go = WS_G + (unsigned)(sg * 2 + wr) * 32768u + (cc0 * 4 + wc) * 1024 + blk_in(ln & 15, cb);
    const unsigned po = WS_PT + (unsigned)sg * 524288u + cc0 * 16384 + wc * 4096 + wr * 2048 + ln * 16;
    const int xg = (sg >= XS1) + (sg >= XS2), want = sg + 1 - (xg == 2 ? XS2 : xg * XS1);
    u32x4 v[4], gv[4], xv[4];
    if (big) {
      if (wid == 2) flag_wait(P, F_PRG(5 + xg, ln & 31), want, ln);
      BARRIER();
      fin_sum<2>(rs, po, v);
      asm volatile("" : "+v"(v[0]), "+v"(v[1]) :: "memory");
      #pragma unroll
      for (int c = 0; c < 4; ++c) xv[c] = __builtin_nontemporal_load((const u32x4*)((const char*)P.x + eo + c * 256));
      fin_sum<2>(rs, po + 32768, v + 2);
      if (wid == 1) cnt_wait(P, C_W4(sg >> 2), 4, ln);
      BARRIER();
      #pragma unroll
      for (int c = 0; c < 4; ++c) gv[c] = ld16(rs, go + c * 4096);
      #pragma unroll
      for (int c = 0; c < 4; ++c) fin_out(P, eo + c * 256, v[c], gv[c], xv[c]);
    } else {
      if (wid == 1) cnt_wait(P, C_W4(sg >> 2), 4, ln);
      BARRIER();
      gv[0] = ld16(rs, go);
      xv[0] = __builtin_nontemporal_load((const u32x4*)((const char*)P.x + eo));
      if (wid == 2) flag_wait<3>(P, F_PRG(5 + xg, (ln & 3) * 8 + cc0), want, ln);
      BARRIER();
      fin_sum<1>(rs, po, v);
      fin_out(P, eo, v[0], gv[0], xv[0]);
    }
    POP();
  }
}

__device__ __forceinline__ u32x4 ldsv(LAS unsigned char* p) { return *(volatile LAS u32x4*)p; }
#define DMA_A dman
#define DMA_V dman
#define DMA_O dmaw
#define TOUCH 0
#define TAUX 0
#define POISON4 ((u32x4){~0u, ~0u, ~0u, ~0u})
#define DLYM 0
#define DLYA 8
#define DLYK (DLYA + 8)
#define DLYO 8
#define FLA 0
#define FLB 0
#define CADM 1
#define CADO 1

#define RTC(id)
#define DCA 2
#define DCB 12
#define DL1 6
#define DL2 14
#define DLB 18
#define ADL 0
#define SN0 150
#define HELP 256
#define BORD 0
#define QT 0
__device__ __forceinline__ void track(int& est, int d, int gain) { const int e = d - (est >> 4); est += (e > 44 ? DCB : e) * gain; }
__device__ __forceinline__ unsigned rfl(unsigned v) { unsigned r; asm volatile("s_nop 0\n\tv_readfirstlane_b32 %0, %1\n\ts_nop 4" : "=s"(r) : "v"(v)); return r; }
__device__ __forceinline__ void plan_wait(rsrc_t rs, unsigned fo, int s, int& lim, unsigned& tm, unsigned& d) {
  while ((d & 255u) != (unsigned)s && (lim -= DEC1) > 0) {
    __builtin_amdgcn_s_sleep(1);
    const u32x2 v = __builtin_amdgcn_raw_buffer_load_b64(rs, 0, fo, AUX_SC1);
    tm = rfl(v.x); d = rfl(v.y);
  }
  const unsigned now = (unsigned)__builtin_amdgcn_s_memrealtime() * 4u;
  tm = now + min((int)(tm - now), 2000);
  d = min(d >> 8, 1000u);
}
#define SPIN_BUDGET (1 << 21)

__device__ __forceinline__ unsigned rec_go(LAS unsigned char* w, const Ptrs& P, u32x2 f, int wid, int lane) {
  if (wid >= 6) {
    if (__any(f.y != P.tag)) f.x = flag_wait(P, wid == 6 ? F_LN0(lane) : F_CAL(lane), 0, lane);
    if (wid == 7) { const unsigned m = near_half(f.x); if (lane == 0) *(volatile LAS unsigned*)w = m; }
  }
  LBARRIER();
  return __builtin_amdgcn_readfirstlane(*(volatile LAS unsigned*)w);
}

#define SLOT(i) ((i) * 16384)
#define LA_MEM SLOT(8)
#define LA_Q SLOT(9)
#define LA_K (SLOT(9) + 2048)
#define LA_P (SLOT(9) + 4096)
#define LA_VM (SLOT(9) + 6144)
#define LA_KX (SLOT(9) + 8192)

template <bool QW>
__device__ __forceinline__ void a_wave(LAS unsigned char* lds, Ptrs& P, int h, int wid) {
  char* ws = P.ws;
  const int lane = opq(0), tid = wid * 64 + lane;
  const int j = wid & 3, g = lane >> 4, q4 = (lane & 15) >> 2, p4 = lane & 3;
  const rsrc_t rs = WS_RSRC(P);
  const rsrc_t rq = __builtin_amdgcn_make_buffer_rsrc((void*)P.Wq, 0, HD * DM * 2, RSRC_W3);
  const rsrc_t rk = __builtin_amdgcn_make_buffer_rsrc((void*)P.Wk, 0, HD * DM * 2, RSRC_W3);
  const unsigned lbase = (unsigned)(size_t)lds;
  int lim = SPIN_BUDGET;
  u32x8 wr[12][2];
  const int trow = 16 * (g >> 1) + 4 * (g & 1) + 8 * (q4 >> 1) + (q4 & 1);
  const int tsw = ((q4 & 1) | ((g & 1) << 1) | ((q4 >> 1) << 2)) << 1;
  const int toff = trow * 256 + (((4 * j + (p4 >> 1)) ^ tsw) << 4) + 8 * (p4 & 1);
  {
    unsigned dv[2];
    #pragma unroll
    for (int x = 0; x < 2; ++x) {
      const int row = 8 * wid + 4 * x + (lane >> 4);
      const int sw = ((row & 1) | (((row >> 2) & 1) << 1) | (((row >> 3) & 1) << 2)) << 1;
      dv[x] = (unsigned)(row * (HD * 2) + h * 256 + (((lane & 15) ^ sw) << 4));
    }
#define A_GRP(n) do { _Pragma("unroll") for (int i = 0; i < 8; ++i) \
      DMA_A(((n) & 1) ? rk : rq, dv[i & 1], (4 * (((n) & 1) ? ((n) >> 1) ^ ((n) >> 2) : (((n) >> 1) + 1) & 3) + (i >> 1)) * 64 * HD * 2, lbase + SLOT(4 * ((n) & 1) + (i >> 1)) + wid * 2048 + (i & 1) * 1024); } while (0)
    A_GRP(0); A_GRP(1);
    #pragma unroll
    for (int n = 0; n < 6; ++n) {
      VMW(8);
      BARRIER();
      if ((n & 1) == (QW ? 0 : 1)) {
        #pragma unroll
        for (int b = 0; b < 4; ++b) {
          LAS unsigned char* t = lds + SLOT(QW ? 0 : 4) + ((b ^ j) << 14);
          wr[4 * (n >> 1) + b][0] = rd_frag<8192, 512>(t + toff);
          wr[4 * (n >> 1) + b][1] = rd_frag<8192, 512>(t + (toff ^ 32));
        }
        #pragma unroll
        for (int b = 0; b < 4; ++b) A_PIN(QW, 4 * (n >> 1) + b);
      }
      LBARRIER();
      A_GRP(n + 2);
    }
  }
  const unsigned cb = comm_base(ws);
  u32x2 f0 = {0u, 0u};
  #pragma unroll
  for (int rd = 0; rd < 2; ++rd) {
    if (rd == 0) VMW(8);
    else {
      if (wid >= 6) f0 = ld8(rs, FLG_OFF + (wid == 6 ? F_LN0(lane) : F_CAL(lane)) * 128);
      const unsigned fv = wid == 3 ? FLG_OFF + F_PRG(min((lane >> 1) & 3, 2), h) * 128 + (lane & 1) * 4 : WS_QF + j * (16u << 20) + h * 8192 + lane * 128;
      if (QW) asm volatile("s_mov_b32 m0, %2\n\ts_nop 0\n\tbuffer_load_dword %0, %1, 0 offen sc1 lds" :: "v"(fv), "s"(rs), "s"(lbase + LA_P + (wid == 3 ? 640 : 1024)) : "memory");
      get_tag(P);
      VMWAIT();
    }
    BARRIER();
    u32x8 f[2][2];
    #pragma unroll
    for (int i = 0; i < 2; ++i) {
      f[i][0] = rd_frag<8192, 512>(lds + SLOT((QW ? 0 : 2) + 4 * rd + i) + toff);
      f[i][1] = rd_frag<8192, 512>(lds + SLOT((QW ? 0 : 2) + 4 * rd + i) + (toff ^ 32));
    }
    asm volatile("" : "+v"(f[0][0]), "+v"(f[0][1]), "+v"(f[1][0]), "+v"(f[1][1]));
    LBARRIER();
    #pragma unroll
    for (int i = 0; i < 2; ++i)
      #pragma unroll
      for (int t = 0; t < 2; ++t) {
        LAS unsigned char* p = lds + SLOT((QW ? 0 : 2) + 4 * rd + i) + j * 4096 + t * 2048 + lane * 16;
        *(LAS u32x4*)p = (u32x4){f[i][t][0], f[i][t][1], f[i][t][2], f[i][t][3]};
        *(LAS u32x4*)(p + 1024) = (u32x4){f[i][t][4], f[i][t][5], f[i][t][6], f[i][t][7]};
      }
  }
  const unsigned mask = rec_go(lds + LA_P + 128, P, f0, wid, lane);
  const int idx = (lane & 1) ? 0xEEEE : 0x4444;
  const unsigned ab_a = lbase + LA_MEM + lane * 16, am_a = ab_a + j * 1024;
  const unsigned t0_a = lbase + SLOT((QW ? 0 : 4) + j) + j * 4096 + lane * 16;
  const unsigned iw_a = lbase + (QW ? LA_Q : LA_K) + j * 512 + q4 * 128 + g * 32 + p4 * 4;
  const unsigned qr_a = lbase + LA_Q + g * 128 + (lane & 7) * 16;
  const unsigned kr_a = lbase + LA_K + g * 128 + (2 * q4 + (lane & 1)) * 16;
  const unsigned fr_a = lbase + LA_P + p4 * 4;
  if (tid < 12) *(LAS unsigned*)(lds + LA_P + tid * 4) = 0u;
  const unsigned vo_v = VB_AT(h) + j * 512 + lane * 4, md = lbase + LA_VM - VB_AT(h);
  const unsigned oo_v = OB_AT((lane >> 2) & 1, h) + (j * 16 + (g >> 1) * 8 + p4 * 2 + (g & 1)) * 16;
  const unsigned long long onej = 1ull << j;
  const unsigned lkx = lbase + LA_KX + j * 2048;
  u32x8 oxr = {};
  const unsigned xo_v = (lane >> 5) * 2048 + (lane & 31) * 16, vl_a = lbase + LA_VM + (j & 1) * 1024 + lane * 4;
  const int dq = 16 * (j >> 1) + (lane & 15) - 4 * g;
  const unsigned fb = step_base(cb, mask, 0);
  unsigned tgt = (unsigned)__builtin_amdgcn_s_memrealtime() * 4u;
  unsigned long long tp = 0;
  unsigned pt = 0, pd = 0;
  const unsigned long long fmask = __builtin_amdgcn_readfirstlane((16u >> wid) & 1u);
  int known = 0;
  __builtin_amdgcn_s_setprio(QW ? 2 : 1);
  LBARRIER();
  report(P, wid);
  TL(72);
  for (int s = 0; s < NSEG; ++s) {
    const unsigned sb = step_base(cb, mask, s);
    const unsigned sbm = sb - (lbase + LA_MEM);
    const unsigned fo = (unsigned)(s * NH + h) * 8192u + j * 2048 - (lbase + LA_MEM);
    GSDECL;
    RTC(h);
    lim = __builtin_amdgcn_readfirstlane(lim);
    if (QW) {
      if (wid == 3 && s > 0) {
        if (opq(0) == 0) __builtin_amdgcn_raw_buffer_store_b32((unsigned)tp * 4u - tgt, rs, 0, fb + FL_O(h), AUX_SC1);
        plan_wait(rs, fb + FL_T(s), s, lim, pt, pd);
        tgt = pt;
      }
      if (s >= known) {
        const u32x2 v = *(volatile LAS u32x2*)(lds + LA_P + 640 + (opq(0) & 3) * 8);
        if (!__any(v.y != P.tag)) known = rfl(min(min(__builtin_amdgcn_readlane(v.x, 0), __builtin_amdgcn_readlane(v.x, 1)), __builtin_amdgcn_readlane(v.x, 2)));
      }
      kv_wait(P, s, h, known, opq(0));
      const unsigned kxo = WS_KF + fo, vxo = WS_VF + fo;
      if (wid < 3) for (int i = 0; i < ADL; ++i) __builtin_amdgcn_s_sleep(1);
      dmas(rs, ab_a, kxo, lkx);
      dmas(rs, ab_a, kxo + 1024, lkx + 1024);
      dmas(rs, ab_a, WS_QF + fo, lbase + LA_P + 1024);
      dmas(rs, ab_a, WS_QF + fo + 1024, lbase + LA_P + 1024);
      if (wid == 2 && s + 1 >= known) {
        const int ln = opq(0);
        const unsigned fv = FLG_OFF + F_PRG(min((ln >> 1) & 3, 2), h) * 128 + (ln & 1) * 4;
        asm volatile("s_mov_b32 m0, %2\n\ts_nop 0\n\tbuffer_load_dword %0, %1, 0 offen sc1 lds" :: "v"(fv), "s"(rs), "s"(lbase + LA_P + 640) : "memory");
      }
      unsigned rg = 0x3c000000u;
      A_MAIN_Q();
      TL(s);
      GSFLUSH(0, 24 + wid); GSFLUSH(1, 4 + wid); GSFLUSH(2, 12 + j); GSFLUSH(3, 16 + j); GSFLUSH(4, 20 + j);
      if (s + PD < NSEG && !(opq(0) & 8)) __builtin_amdgcn_raw_buffer_store_b128(poison4(), rs, oo_v, step_base(cb, mask, s + PD), AUX_SC1);
    } else {
      const unsigned qxo = WS_QF + fo - (j & 1) * 2048, oxo = WS_OX + (unsigned)((s == 0 || s == NSEG - 1 ? s : s - 1) * NH + h) * 8192u + (j & 1) * 4096 + (j >> 1) * 1024;
      A_MAIN_K();
      if (s >= NSEG - 2) {
        __builtin_amdgcn_raw_buffer_store_b128((u32x4){oxr[0], oxr[1], oxr[2], oxr[3]}, rs, xo_v, oxo + (NSEG - 1 - s) * NH * 8192, AUX_SC1);
        __builtin_amdgcn_raw_buffer_store_b128((u32x4){oxr[4], oxr[5], oxr[6], oxr[7]}, rs, xo_v + 512, oxo + (NSEG - 1 - s) * NH * 8192, AUX_SC1);
      }
      if (wid == 7) GSFLUSH(0, 28);
      GSFLUSH(1, 4 + wid);
    }
  }
  VMWAIT();
  BARRIER();
  if (wid == 4) flag_set(P, F_PRG(4, h), NSEG, opq(0));
}

#define TSL 4
#define LB_MEM 0
#define LB_RED 16384
template <bool VW>
__device__ __forceinline__ void b_wave(LAS unsigned char* lds, Ptrs& P, int r, int c, int wid, int bs, int xg) {
  constexpr int NHD = VW ? 2 : 6;
  char* ws = P.ws;
  const int lane = opq(0), tid = wid * 64 + lane;
  const int j = wid & 3, g = lane >> 4, q4 = (lane & 15) >> 2, p4 = lane & 3;
  const int b = r * 32 + c;
  const rsrc_t rs = WS_RSRC(P);
  const rsrc_t rv = __builtin_amdgcn_make_buffer_rsrc((void*)P.Wv, 0, HD * DM * 2, RSRC_W3);
  const rsrc_t ro = __builtin_amdgcn_make_buffer_rsrc((void*)P.Wo, 0, HD * DM * 2, RSRC_W3);
  const unsigned lbase = (unsigned)(size_t)lds;
  int lim = SPIN_BUDGET;
  const int h0 = ((VW ? 2 * j : 8 + 6 * j) + 5 * c) & 31;
  u32x8 wv[16], wo[6][2][2];
  {
    if ((wid & 3) < 2 && xg < 2) {
      const unsigned nk = 8 + 2 * xg, M = (bs * 2 + (wid & 1)) * 64 + lane, row = M / nk, k = M - row * nk;
      if (row < HELP) touch(VW ? P.Wq : P.Wk, HD * DM * 2, (row < 256 ? row + (VW ? 512 : 256) : row + 512) * (HD * 2) + min(xg * 4096 + (k >> 1) * 1024 + 256, 7936u) + (k & 1) * 128, lds);
    }
    const int nl = xg == 2 ? 6 : 5;
    #pragma unroll
    for (int i = 0; i < 2; ++i) {
      const unsigned L = bs * 1024 + wid * 128 + i * 64 + lane, row = L / nl;
      touch(P.Wo, HD * DM * 2, row * (DM * 2) + (c - (bs >> 1)) * 64 + (L - row * nl) * 128, lds);
    }
    VMWAIT();
    BARRIER();
    const int pos = lane >> 2;
    const int prow = (pos & 9) | ((pos & 2) << 1) | ((pos & 4) >> 1);
    const unsigned dv0 = (unsigned)(prow * (DM * 2) + c * 64 + ((lane & 3) << 4));
    const unsigned dv1 = (unsigned)(prow * (DM * 2) + c * 64 + (((lane & 3) ^ 2) << 4));
    const int toff = wid * 16384 + (g * 16 + q4) * 64 + (((p4 >> 1) ^ ((g & 1) << 1)) << 4) + 8 * (p4 & 1);
    #pragma unroll
    for (int rd = 0; rd < NHD / 2; ++rd) {
      #pragma unroll
      for (int hh = 0; hh < 2; ++hh) {
        const int h = (h0 + 2 * rd + hh) & 31;
        #pragma unroll
        for (int i = 0; i < 8; ++i)
          DMA_O(ro, (i & 1) ? dv1 : dv0, (unsigned)(h * 128 + i * 16) * (DM * 2), lbase + wid * 16384 + hh * 8192 + i * 1024);
      }
      VMWAIT();
      #pragma unroll
      for (int hh = 0; hh < 2; ++hh)
        #pragma unroll
        for (int ks = 0; ks < 2; ++ks)
          #pragma unroll
          for (int t = 0; t < 2; ++t)
            wo[2 * rd + hh][ks][t] = rd_frag<4096, 256>(lds + (toff ^ (t * 32)) + hh * 8192 + ks * 512);
      #pragma unroll
      for (int hh = 0; hh < 2; ++hh)
        WO_PIN(VW, 2 * rd + hh);
      asm volatile("s_waitcnt lgkmcnt(0)" ::: "memory");
    }
    const int lr = lane >> 3;
    #pragma unroll
    for (int i = 0; i < 16; ++i) {
      const int sw = (((lr >> 2) & 1) | ((i & 1) << 1)) << 1;
      const unsigned dv = (unsigned)(lr * (HD * 2) + b * 128 + (((lane & 7) ^ sw) << 4));
      DMA_V(rv, dv, (unsigned)(wid * 128 + i * 8) * (HD * 2), lbase + wid * 16384 + i * 1024);
    }
    VMWAIT();
    BARRIER();
    if (VW) {
      const int trow = 16 * (g >> 1) + 4 * (g & 1) + 8 * (q4 >> 1) + (q4 & 1);
      const int tsw = ((g & 1) | ((q4 >> 1) << 1)) << 1;
      LAS unsigned char* tv = lds + trow * 128 + (((2 * j + (p4 >> 1)) ^ tsw) << 4) + 8 * (p4 & 1);
      #pragma unroll
      for (int ks = 0; ks < 16; ++ks) wv[ks] = rd_frag<4096, 256>(tv + ks * 8192);
      V_PIN();
    }
    LBARRIER();
  }
  const unsigned cb = comm_base(ws);
  get_tag(P);
  if (wid == 0) touch(P.x, SEQ * DM * 2, (SEQ - 64 + lane) * 2048 + c * 64, lds);
  const unsigned mask = rec_go(lds + LB_RED + 4096 + 64, P, (u32x2){0u, P.tag - 1u}, wid, lane);
  const int idx = (lane & 1) ? 0xEEEE : 0x4444;
  const unsigned am_a = lbase + LB_MEM + lane * 16;
  const unsigned fb = step_base(cb, mask, 0);
  volatile LAS unsigned* tsl = (volatile LAS unsigned*)(lds + LB_RED + 4096);
  if (tid < 8) tsl[tid] = tid ? 0u : (unsigned)__builtin_amdgcn_s_memrealtime() * 4u;
  unsigned dn = 600;
  int da = 565 * 16, gb = 362 * 16;
  if (VW) __builtin_amdgcn_s_setprio(2);
  LBARRIER();
  report(P, wid);
  TL(72);
  for (int s = 0; s < NSEG; ++s) {
    const unsigned sb = step_base(cb, mask, s);
    const int ln = opq(lane), gg = ln >> 4;
    GSDECL;
    RTC(32 + b);
    const int l16 = ln * 16;
    const int xc = 32 * (c & 1) + 2 * (ln & 15);
    const unsigned mo_v = MF_AT(c >> 1) + ((xc >> 4) * 16 + (4 * r + gg) * 2 + ((xc >> 3) & 1)) * 16 + (xc & 7) * 2;
    if (!VW) {
      const unsigned tm = __builtin_amdgcn_readfirstlane(*tsl), tgt = tm + DLB, la = lbase + LB_MEM + j * 2048 + l16, mo = sb + j * 2048;
      B_POLL();
      if (wid == 7) {
        const int late = (int)((unsigned)__builtin_amdgcn_s_memrealtime() * 4u - tgt) - SN0;
        const unsigned to = tm + dn + (late > 80 ? late : 0);
        if (ln == 0) tsl[1] = to;
        if (b == 0) {
          dn = (da >> 4) + DL1;
          if (ln == 0) { *tsl = to + (gb >> 4) + DL2; st8(rs, fb + FL_T(s + 1), (u32x2){to + (gb >> 4) + DL2, (dn << 8) | (s + 1)}); }
          const unsigned va = ld4(rs, fb + FL_O(ln & 31)), vb = ld4(rs, fb + FL_B(ln));
          if (s > 2) {
            track(da, rfl(wmax(va)), s < 6 ? 6 : DCA);
            track(gb, rfl(wmax(vb)), s < 6 ? 6 : DCA);
          }
        }
      }
      if (wid == 7 && s > 0 && s + 2 < NSEG) __builtin_amdgcn_raw_buffer_store_b32(~0u, rs, mo_v, step_base(cb, mask, s + 2), AUX_SC1);
    } else {
      GS(3);
      const unsigned vo = VB_AT(b >> 1) + ((b & 1) * 4 + j) * 256;
      V_PROJ();
      GS(0); GSFLUSH(0, 4 + j);
      if (s + PD < NSEG && ln < 16) st16(rs, step_base(cb, mask, s + PD) + vo + l16, poison4());
    }
    GSFLUSH(3, 24 + wid);
    if (s + 1 == NSEG) break;
    unsigned so[6];
    #pragma unroll
    for (int i = 0; i < NHD; ++i) { const int h = (h0 + i) & 31; so[i] = __builtin_amdgcn_readfirstlane(sb + OB_AT(r, h)); }
    const unsigned sn = __builtin_amdgcn_readfirstlane(step_base(cb, mask, s + 1));
    const unsigned rw_a = lbase + LB_RED + (wid * 128 + (gg & 1) * 64 + (gg >> 1) * 16 + (ln & 15)) * 4;
    const unsigned rr_a = lbase + LB_RED + ln * 8;
    if (wid == 7) {
      if (b != 0) {
        __builtin_amdgcn_s_sleep(4);
        unsigned tm = 0;
        dn = 0;
        plan_wait(rs, fb + FL_T(s + 1), s + 1, lim, tm, dn);
        if (ln == 0) *tsl = tm;
      }
      const unsigned tgt = rfl(tsl[1]);
      T_WAIT();
    }
    lim = __builtin_amdgcn_readfirstlane(lim);
    unsigned long long t1;
    if (VW) WO_V(); else WO_W();
    if (wid == 0) {
      if (ln == 0) st4(rs, fb + FL_B(b), (unsigned)t1 * 4u - tsl[1]);
    }
    if (wid == 0) {
      GSFLUSH(2, 16);
      TL(s);
    }
    GSFLUSH(1, 8 + wid);
    if (wid == 0 || wid == 4) { GSFLUSH(4, 17 + (wid >> 2)); GSFLUSH(0, 19 + (wid >> 2)); }
  }
  __builtin_amdgcn_s_setprio(0);
  const int ln = opq(0);
  TL(64);
  volatile LAS int* rdy = (volatile LAS int*)(lds + 98304 + 3 * 16384);
  if (tid == 0) *rdy = 61;
  LBARRIER();
#define T_SRC(sg, i) (WS_OX + (unsigned)((sg) * NH + ((h0 + (i)) & 31)) * 8192u + r * 1024)
#define T_MMA(i, k, f) do { const u32x8 w0 = wo[i][(k) & 1][0], w1 = wo[i][(k) & 1][1]; const int e = ((k) >> 1) * 4; \
    a0 = MFMA32(((u32x4){w0[e], w0[e + 1], w0[e + 2], w0[e + 3]}), f, a0); a1 = MFMA32(((u32x4){w1[e], w1[e + 1], w1[e + 2], w1[e + 3]}), f, a1); } while (0)
#define T_DMA(sg, i) do { _Pragma("unroll") for (int k = 0; k < 4; ++k) dmas(rs, ln * 16, T_SRC(sg, i) + k * 2048, lbase + j * 24576 + (i) * 4096 + k * 1024); } while (0)
  const int cb2 = 16 * ((ln >> 4) & 1) + 8 * (ln >> 5);
  const unsigned eo = (unsigned)((NSEG - 1 - j) * 32 + r * 16 + (ln & 15)) * 2048u + (unsigned)(c * 32 + cb2) * 2u;
  u32x4 gv, xv;
  if (VW && wid < NSEG - XS3) {
    xv = *(const u32x4*)((const char*)P.x + eo);
    cnt_wait(P, C_W4(15), 4, ln);
    gv = ld16(rs, WS_G + (unsigned)((NSEG - 1 - j) * 2 + r) * 32768u + c * 1024 + blk_in(ln & 15, cb2));
  }
  bool ahead = false;
  f32x4 k0, k1;
  #pragma unroll 1
  for (int sg = XS3; sg < NSEG; ++sg) {
    f32x4 a0 = zero4(), a1 = zero4();
    if (VW) {
      if (wid == 3) {
        for (int w = __builtin_amdgcn_readfirstlane(*rdy); w <= sg;) {
          w = max(w + 1, -wmax(-(int)flag_wait<TSL>(P, F_PRG(4, ln & 31), w + 1, ln)));
          if (ln == 0) *rdy = w;
        }
      } else while (__builtin_amdgcn_readfirstlane(*rdy) <= sg) __builtin_amdgcn_s_sleep(1);
      u32x4 of[8];
      #pragma unroll
      for (int i = 0; i < 8; ++i) of[i] = __builtin_amdgcn_raw_buffer_load_b128(rs, ln * 16 + (i & 3) * 2048, T_SRC(sg, i >> 2), AUX_SC1);
      #pragma unroll
      for (int i = 0; i < 8; ++i) T_MMA(i >> 2, i & 3, of[i]);
    } else {
      if (!ahead) {
        while (__builtin_amdgcn_readfirstlane(*rdy) <= sg) __builtin_amdgcn_s_sleep(1);
        #pragma unroll
        for (int i = 0; i < NHD; ++i) T_DMA(sg, i);
      }
      VMW(20);
      ahead = __builtin_amdgcn_readfirstlane(*rdy) > sg + 1;
      u32x4 of[2][4];
#define T_RD(i) do { _Pragma("unroll") for (int k = 0; k < 4; ++k) of[(i) & 1][k] = *(const LAS u32x4*)(lds + j * 24576 + (i) * 4096 + k * 1024 + ln * 16); } while (0)
      T_RD(0);
      #pragma unroll
      for (int i = 0; i < NHD; ++i) {
        asm volatile("s_waitcnt lgkmcnt(0)" : "+v"(of[i & 1][0]), "+v"(of[i & 1][1]), "+v"(of[i & 1][2]), "+v"(of[i & 1][3]) :: "memory");
        if (i + 1 < NHD) {
          if (ahead) VMW(16); else G_WAIT_V(16 - 4 * (i < 4 ? i : 4));
          T_RD(i + 1);
        }
        #pragma unroll
        for (int k = 0; k < 4; ++k) {
          if (ahead) dmas(rs, ln * 16, T_SRC(sg + 1, i) + k * 2048, lbase + j * 24576 + i * 4096 + k * 1024);
          T_MMA(i, k, of[i & 1][k]);
          __builtin_amdgcn_sched_barrier(0);
        }
      }
    }
    LAS f32x4* pw = (LAS f32x4*)(lds + 98304 + ((sg & 3) * 8 + wid) * 2048) + ln;
    if (wid == 0 && sg == NSEG - 1) { k0 = a0; k1 = a1; } else { pw[0] = a0; pw[64] = a1; }
  }
  if (VW && wid < NSEG - XS3) {
    LBARRIER();
    const LAS f32x4* pr = (const LAS f32x4*)(lds + 98304 + (3 - j) * 16384) + ln;
    f32x4 t[2];
    #pragma unroll
    for (int k = 0; k < 2; ++k)
      t[k] = (((wid ? pr[k * 64] : k ? k1 : k0) + pr[128 + k * 64]) + (pr[256 + k * 64] + pr[384 + k * 64])) + ((pr[512 + k * 64] + pr[640 + k * 64]) + (pr[768 + k * 64] + pr[896 + k * 64]));
    fin_out(P, eo, epi_pack(t[0], t[1]), gv, xv);
  } else LBARRIER();
}

#define DMA_S dman
#define XDL 4
#define XPC 0
#define PW3 11
#define PW4 25
#define LS_P 131072
template <int MODE>
__device__ __forceinline__ void s_role(LAS unsigned char* lds, Ptrs& P, const u16* W, int h, unsigned obase, int nseg, int fl, int wid, int s0) {
  constexpr bool XM = MODE >= 2;
  constexpr unsigned RS = XM ? DM * 2 : HD * 2, SS = XM ? 262144u : 65536u;
  const int lane = opq(0);
  const int kh = wid >> 2, tp = wid & 3;
  const rsrc_t rs = WS_RSRC(P);
  const rsrc_t rw = __builtin_amdgcn_make_buffer_rsrc((void*)W, 0, HD * DM * 2, RSRC_W3);
  const unsigned lbase = (unsigned)(size_t)lds;
  u32x8 w[8][2];
  if (XM && XDL) {
    if (wid == 0) flag_wait(P, F_PRG(4, h), max(s0 - XDL, 0), lane);
    BARRIER();
  }
  {
    const int g = lane >> 4, q4 = (lane & 15) >> 2, p4 = lane & 3;
    const int trow = XM ? 16 * g + 4 * (q4 >> 1) + (q4 & 1) : 8 * g + q4;
#define S_SW(row) ((((row) & 1) | ((((row) >> (XM ? 2 : 1)) & 1) << 1) | ((((row) >> (XM ? 4 : 3)) & 1) << 2)) << 1)
    const int toff = trow * 256 + (((4 * tp + (p4 >> 1)) ^ S_SW(trow)) << 4) + 8 * (p4 & 1);
    unsigned dv[2];
    #pragma unroll
    for (int x = 0; x < 2; ++x) {
      const int row = 8 * wid + 4 * x + (lane >> 4);
      dv[x] = (unsigned)row * RS + (XM ? (h >> 3) * 1024 * RS + (h & 7) * 256 : h * 256) + (((lane & 15) ^ S_SW(row)) << 4);
    }
#define S_IMG(i) do { DMA_S(rw, dv[0], (i) * 64 * RS, lbase + ((i) & 7) * 16384 + wid * 2048); \
                      DMA_S(rw, dv[1], (i) * 64 * RS, lbase + ((i) & 7) * 16384 + wid * 2048 + 1024); if (XM && XPC) __builtin_amdgcn_s_sleep(XPC); } while (0)
    #pragma unroll
    for (int i = 0; i < 8; ++i) S_IMG(i);
    #pragma unroll
    for (int b = 0; b < 4; ++b) {
      if (b == 3 && !XM) get_tag(P);
      if (b < 3) VMW(8); else VMWAIT();
      BARRIER();
      if (kh == (b >> 1)) {
        #pragma unroll
        for (int i = 4 * (b & 1); i < 4 * (b & 1) + 4; ++i) {
          w[i][0] = rd_frag<XM ? 2048 : 8192, XM ? 512 : 1024>(lds + toff + i * 16384);
          w[i][1] = rd_frag<XM ? 2048 : 8192, XM ? 512 : 1024>(lds + (toff ^ 32) + i * 16384);
        }
      }
      LBARRIER();
      if (b < 2) {
        #pragma unroll
        for (int i = 4 * b + 8; i < 4 * b + 12; ++i) S_IMG(i);
      }
    }
  }
  TL(XM ? 74 : 72);
  const unsigned l16 = lane * 16;
  volatile LAS int* rd = (volatile LAS int*)(lds + LS_P + 20480);
#define X_READY(want) do { \
    if (wid == 0) { \
      const int n_ = -wmax(-(int)flag_wait(P, F_PRG(4, (h & 24) + (lane & 7)), s0 + (want), lane)) - s0; \
      if (lane == 0) *rd = max(n_, want); \
    } \
    LBARRIER(); } while (0)
  if (MODE == 2) X_READY(2);
  else if (!XM) {
    if (wid == 0) flag_wait(P, F_LN0(lane & 7), 0, lane);
    BARRIER();
  }
  unsigned sx = (XM ? WS_OX + s0 * SS + (h >> 3) * 65536 : WS_XN) + kh * 32768 + tp * 4096;
  const unsigned sd = lbase + kh * 65536 + tp * 4096;
  const unsigned xl = kh * 65536 + (XM ? l16 : blk_in(lane & 15, (lane >> 4) * 8));
  const unsigned xp = lbase + xl, pa = lbase + LS_P + tp * 4096 + l16;
#define S_MMA(a, xo, k0, k1) do { _Pragma("unroll") for (int ks = k0; ks < k1; ++ks) _Pragma("unroll") for (int i = 0; i < 4; ++i) { \
    const u32x4 x = *(const LAS u32x4*)(lds + (xo) + ks * 2048 + (i >> 1) * 1024); \
    const u32x8 wf = w[ks >> 1][i & 1]; \
    const u32x4 wh = {wf[(ks & 1) * 4], wf[(ks & 1) * 4 + 1], wf[(ks & 1) * 4 + 2], wf[(ks & 1) * 4 + 3]}; \
    a[i] = MODE == 1 ? MFMA32(x, wh, a[i]) : MFMA32(wh, x, a[i]); } } while (0)
  LAS f32x4* const pq = (LAS f32x4*)(lds + LS_P + tp * 4096 + l16);
  if (MODE == 3) {
#define X_GET(t) do { _Pragma("unroll") for (int i = 0; i < 16; ++i) \
      dmas(rs, l16, sx + (t) * SS + ((i >> 2) & 1) * 32768 + (i >> 3) * 16384 + (i & 3) * 1024, sd + ((i >> 2) & 1) * 65536 + ((t) & 1) * 32768 + (i >> 3) * 16384 + (i & 3) * 1024); } while (0)
    bool have = false;
    const unsigned pv = FLG_OFF + F_PRG(kh ? 7 : 4, kh ? h : (h & 24) + (lane & 7)) * 128;
    if (!kh) __builtin_amdgcn_s_setprio(1);
    #pragma unroll 1
    for (int t = 0; t < nseg; ++t) {
      if (!have && !kh) {
        flag_wait<2>(P, F_PRG(4, (h & 24) + (lane & 7)), s0 + t + 1, lane);
        X_GET(t);
        VMW(8);
      }
      BARRIER();
      const unsigned xp = lbase + xl + (t & 1) * 32768;
      f32x4 a[4];
      unsigned pg;
      X_ONE();
      if (wid == 5) flag_set(P, fl, t, lane);
      have = t + 1 < nseg && !__any(pg < (unsigned)(s0 + t + 2));
      if (!kh && lane == 0) rd[tp] = have;
      pq[kh * 128] = kh ? a[0] : a[2]; pq[kh * 128 + 64] = kh ? a[1] : a[3];
      if (have && !kh) X_GET(t + 1);
      LBARRIER();
      const unsigned oo = obase + (unsigned)(s0 + t) * 0x80000u + (unsigned)h * 16384u + tp * 4096 + kh * 2048 + l16;
      st16(rs, oo, __builtin_bit_cast(u32x4, (kh ? a[2] : a[0]) + pq[128 - kh * 128]));
      st16(rs, oo + 1024, __builtin_bit_cast(u32x4, (kh ? a[3] : a[1]) + pq[192 - kh * 128]));
      if (__any(rd[lane & 3] == 0)) {
        VMWAIT();
        BARRIER();
        if (wid == 5) flag_set(P, fl, t + 1, lane);
      } else if (!kh) VMW(10);
      asm volatile("s_waitcnt lgkmcnt(0)" ::: "memory");
      TL(t);
    }
    __builtin_amdgcn_s_setprio(0);
    TL(65);
    return;
  }
  if (XM) {
    #pragma unroll
    for (int i = 0; i < 12; ++i) dmas(rs, l16, sx + (i >> 3) * SS + ((i >> 2) & 1) * 16384 + (i & 3) * 1024, sd + (i >> 2) * 16384 + (i & 3) * 1024);
    VMWAIT();
    BARRIER();
  } else {
    #pragma unroll
    for (int i = 0; i < 16; ++i) dmas(rs, l16, sx + (i >> 3) * SS + ((i >> 2) & 1) * 16384 + (i & 3) * 1024, sd + ((i >> 2) ^ 2) * 16384 + (i & 3) * 1024);
    VMW(8);
    BARRIER();
    f32x4 a[4] = {};
    S_MMA(a, xl + 32768, 0, 16);
    if (!kh) {
      #pragma unroll
      for (int i = 0; i < 4; ++i) pq[i * 64] = a[i];
    }
    LBARRIER();
    if (kh) {
      #pragma unroll
      for (int i = 0; i < 4; ++i) a[i] += pq[i * 64];
      #pragma unroll
      for (int i = 0; i < 2; ++i) {
        const f32x4 a0 = a[MODE ? i : 2 * i], a1 = a[MODE ? 2 + i : 2 * i + 1];
        if (MODE) st16(rs, obase + h * 8192 + (2 * tp + i) * 1024 + l16, (u32x4){pack2(a0[0], a0[1]), pack2(a0[2], a0[3]), pack2(a1[0], a1[1]), pack2(a1[2], a1[3])});
        else st16(rs, obase + h * 8192 + i * 4096 + tp * 1024 + l16, (u32x4){pack2(a0[0], a1[0]), pack2(a0[1], a1[1]), pack2(a0[2], a1[2]), pack2(a0[3], a1[3])});
      }
    }
    sx += SS;
    #pragma unroll
    for (int i = 0; i < 4; ++i) dmas(rs, l16, sx + SS + i * 1024, sd + 32768 + i * 1024);
    VMW(4);
    BARRIER();
    if (wid == 1) flag_set(P, fl, 1, lane);
    TL(62);
  }
  u32x8 xf[3], ac[2] = {};
  #pragma unroll
  for (int i = 0; i < 3; ++i) {
    const u32x4 a = *(const LAS u32x4*)(lds + xl + i * 2048), b = *(const LAS u32x4*)(lds + xl + i * 2048 + 1024);
    xf[i] = (u32x8){a[0], a[1], a[2], a[3], b[0], b[1], b[2], b[3]};
  }
  unsigned long long fm, pm;
  asm volatile("s_cmp_eq_u32 %2, 1\n\ts_cselect_b64 %0, 1, 0\n\ts_cmp_eq_u32 %3, 1\n\ts_cselect_b64 %1, -1, 0" : "=&s"(fm), "=&s"(pm) : "s"(wid), "s"(XM ? 0 : wid >> 1) : "scc");
  const unsigned fo = FLG_OFF + fl * 128, pd = lbase + LS_P + 16384;
  const unsigned po = 4 * 65536 + h * 2048 + (wid & 1) * 1024 - tp * 4096 - kh * 32768;
  unsigned oo = XM ? obase + (unsigned)h * 16384u + tp * 4096 + (s0 - 1) * 0x80000u : obase + (unsigned)h * 8192u + tp * (MODE ? 2048 : 1024), tmp, tmp2;
  unsigned long long em;
  int t = XM ? 0 : 1;
  if (MODE < 2) {
    #pragma unroll 1
    for (int part = 0; part < 4; ++part) {
      if (part == 1) { TL(63); if (wid == 0) setup_wait(P, lane); }
      if (MODE == 1 && part >= 2) {
        #pragma unroll
        for (int i = 0; i < 4; ++i) {
          const unsigned to = ((i * 32 + h) * 512 + wid * 64 + opq(0)) * 128;
          if (part == 2) touch(P.w3, DM * FF * 2, to, lds); else touch(P.w4, DM * FF * 2, to, lds);
        }
      }
      const int te = part == 0 ? 3 : part == 1 ? PW3 : part == 2 ? PW4 : nseg + 1;
      if (MODE) S_LOOP_V(te, 0); else S_LOOP_K(te, 0);
    }
  } else {
    #pragma unroll 1
    while (t < nseg) {
      X_READY(min(t + 4, nseg));
      const int n = __builtin_amdgcn_readfirstlane(*rd), te = n >= nseg ? nseg : (n - 2) & ~1;
      TL(t);
      S_LOOP_X(te, nseg);
    }
  }
  TL(XM ? 65 : 64);
  VMWAIT();
  BARRIER();
  if (wid == 1) flag_set(P, fl, nseg, lane);
}

#define LND 1
#define CALN 6
__device__ __forceinline__ void setup_role(LAS unsigned char* lds, Ptrs& P, int cu, int wid) {
  const int lane = opq(0), tid = wid * 64 + lane;
  const rsrc_t rs = WS_RSRC(P);
  u32x4 v[8];
  const u32x4* px = (const u32x4*)(P.x + (size_t)(cu * 32 + wid * 4 + (lane >> 4)) * DM) + (lane & 15);
  #pragma unroll
  for (int i = 0; i < 8; ++i) v[i] = __builtin_nontemporal_load(px + i * 16);
  const unsigned epo = __hip_atomic_load((gu32*)(P.ws + EPO_OFF), RLX_AGENT);
  const unsigned cb = comm_base(P.ws);
  const u32x4 z = {0u, 0u, 0u, 0u};
  const int cr = 63 - cu;
  if (wid == 0 && cr < 16) st16(rs, CNT_OFF + cr * 1024 + lane * 16, z);
  if (cr < 4) st16(rs, cb + FL_B(0) + cr * 8192 + tid * 16, z);
  if (cr == 4 && tid < 256) {
    const int g4 = tid >> 6, k = (tid >> 5) & 1 & (g4 >> 1);
    st16(rs, WS_H1 + ((g4 & 1) ? 127u * 32768u : 0u) + (g4 * 8 + ((tid >> 2) & 7)) * 1024 + blk_in((g4 & 1) ? 15 - k : k, (tid & 3) * 8), z);
  }
  #pragma unroll
  for (int i = 0; i < 2; ++i) {
    const int f = i * 54 + cr;
    if (cr < PD * NCHUNK) {
      u32x4 m = {~0u, ~0u, ~0u, ~0u};
      if (f < 4) {
        const int ks = (f >> 1) * 8 + wid;
        m = *(const u32x4*)(P.mem0 + ((lane >> 1) & 7) * DM + 64 * ks + 16 * (lane >> 4) + 8 * (lane & 1));
        m.x = canon1(m.x); m.y = canon1(m.y); m.z = canon1(m.z); m.w = canon1(m.w);
      }
      st16(rs, cb + (unsigned)(f / (NCHUNK * 2)) * CB_STEP + (f % (NCHUNK * 2)) * 8192u + tid * 16, m);
    }
  }
  const unsigned bi = blk_in((wid & 3) * 4 + (lane >> 4), (lane & 3) * 8);
  const int c0 = (lane >> 2) & 3;
  ln_rows(v);
  TL(69);
  if (cu >= 8) for (int i = 0; i < LND; ++i) __builtin_amdgcn_s_sleep(32);
  #pragma unroll
  for (int i = 0; i < 8; ++i) st16(rs, WS_XN + xn_blk(cu * 2 + (wid >> 2), 4 * i + c0) + bi, v[i]);
  P.tag = __builtin_amdgcn_readfirstlane(epo) + 1u;
  ln_rows(v);
  if (cu < 8) {
    VMWAIT();
    BARRIER();
    if (wid == 0) flag_set(P, F_LN0(cu), 1, lane);
    TL(66);
  }
  asm volatile("" ::: "memory");
  #pragma unroll
  for (int i = 0; i < 8; ++i) st16(rs, WS_XN2 + (unsigned)(cu * 2 + (wid >> 2)) * 32768u + (4 * i + c0) * 1024 + bi, v[i]);
  touch(P.w1, DM * DM * 2, ((cu & 31) * 512 + tid) * 128, lds);
  VMW(9);
  BARRIER();
  if (wid == 0) flag_set(P, F_LN0(cu), 1, lane);
  TL(70);
  if (wid == 0) VMW(2); else VMW(1);
  BARRIER();
  if (wid == 0) flag_set(P, F_LN1(cu), 1, lane);
  TL(71);
  if (cu < 32 && (wid >> 1) == 1) {
    const int id = cu * 2 + (wid & 1);
    unsigned best[2] = {65535u, 65535u};
    #pragma unroll
    for (int it = 0; it < CALN; ++it) {
      const unsigned long long c0 = __builtin_amdgcn_s_memtime();
      asm volatile("s_waitcnt lgkmcnt(0)" ::: "memory");
      if (lane == 0) st4(rs, cb + (id & 3) * CB_STEP + (30 + ((id >> 2) & 1)) * 16384u + (it & 1) * 8192u + (id >> 3) * 1024u, 0u);
      VMWAIT();
      const unsigned d = (unsigned)(__builtin_amdgcn_s_memtime() - c0);
      if (it >= 2) best[it & 1] = min(best[it & 1], d);
    }
    flag_set(P, F_CAL(id), (best[0] << 16) | best[1], lane);
  }
}

#define CPF 1
__global__ __launch_bounds__(512, 2) void mega_kernel(const u16* x, const u16* mem0, const u16* Wq, const u16* Wk, const u16* Wv, const u16* Wo,
                                                     const u16* w1, const u16* w2, const u16* w3, const u16* w4, u16* out, char* ws) {
  __shared__ __attribute__((aligned(16))) unsigned char smem[163840];
  LAS unsigned char* lds = (LAS unsigned char*)smem;
  Ptrs P;
  P.x = x; P.mem0 = mem0; P.Wq = Wq; P.Wk = Wk; P.Wv = Wv; P.Wo = Wo; P.w1 = w1; P.w2 = w2; P.w3 = w3; P.w4 = w4; P.out = out; P.ws = ws;
  const int xcd = blockIdx.x & 7, slot = blockIdx.x >> 3;
  const int wid = __builtin_amdgcn_readfirstlane(threadIdx.x >> 6);
  TL(79);
  P.tag = 0;
  if (wid == 7) {
    unsigned long long pc;
    unsigned len;
    asm volatile("s_getpc_b64 %0\nMK_BEG:\n\ts_mov_b32 %1, MK_END-MK_BEG" : "=s"(pc), "=s"(len));
    touch((const void*)pc, len, (slot * 64 + opq(0)) * 128, lds);
  }
  const int na = xcd == 2 ? 8 : 12;
  const bool isa = xcd < 3 && slot < na;
  const int ah = xcd == 2 ? slot * 4 + 2 : xcd * 16 + (slot & 3) * 4 + (slot >> 2) + (slot >> 3), bs = slot - na, br = bs & 1, bc = (xcd == 1 ? 22 : xcd * 5) + (bs >> 1);
  if (isa) {
    if (wid < 4) a_wave<true>(lds, P, ah, wid); else a_wave<false>(lds, P, ah, wid);
  } else if (xcd < 3) {
    if (wid < 4) b_wave<true>(lds, P, br, bc, wid, bs, xcd); else b_wave<false>(lds, P, br, bc, wid, bs, xcd);
  } else {
    if ((xcd & 3) == 3) {
      setup_role(lds, P, (xcd >> 2) * 32 + slot, wid);
    } else if (xcd == 6) s_role<1>(lds, P, Wv, slot, WS_VF, NSEG, F_PRG(2, slot), wid, 0);
    else s_role<0>(lds, P, xcd == 4 ? Wk : Wq, slot, xcd == 4 ? WS_KF : WS_QF, NSEG, F_PRG(xcd - 4, slot), wid, 0);
    if ((xcd & 3) == 3) mlp_role(lds, P, xcd >> 2, slot, wid);
    else if (xcd == 6) s_role<3>(lds, P, Wo, slot, WS_PT, XS3 - XS2, F_PRG(7, slot), wid, XS2);
    else s_role<2>(lds, P, Wo, slot, WS_PT, xcd == 4 ? XS1 : XS2 - XS1, F_PRG(xcd + 1, slot), wid, xcd == 4 ? 0 : XS1);
    report(P, wid);
    fin_role(lds, P, wid);
    if (blockIdx.x == 3 && wid == 0) {
      cnt_wait(P, C_ARR, 256, threadIdx.x);
      if (threadIdx.x == 0) __hip_atomic_store((gu32*)(ws + EPO_OFF), P.tag, RLX_AGENT);
    }
  }
  asm volatile("MK_END:");
  TL(78);
}

extern "C" void kernel_launch(void* const* d_in, const int* in_sizes, int n_in, void* d_out, int out_size,
                              void* d_ws, size_t ws_size, hipStream_t stream) {
  mega_kernel<<<256, 512, 0, stream>>>((const u16*)d_in[0], (const u16*)d_in[1], (const u16*)d_in[2], (const u16*)d_in[3], (const u16*)d_in[4],
                                       (const u16*)d_in[5], (const u16*)d_in[8], (const u16*)d_in[9], (const u16*)d_in[10], (const u16*)d_in[11],
                                       (u16*)d_out, (char*)d_ws);
}
```

```cpp
#include <hip/hip_runtime.h>
#include <hip/hip_bf16.h>
#include <stdint.h>

#define SEQ 2048
#define DM 1024
#define NH 32
#define DH 128
#define HD (NH * DH)
#define MM 8
#define SEG 32
#define NSEG (SEQ / SEG)
#define FF 3584

typedef unsigned short u16;
typedef __attribute__((ext_vector_type(8))) short bf16x8;
typedef __attribute__((ext_vector_type(4))) short bf16x4;
typedef __attribute__((ext_vector_type(4))) float f32x4;
typedef __attribute__((ext_vector_type(4))) unsigned u32x4;
typedef __attribute__((ext_vector_type(2))) unsigned u32x2;
typedef __attribute__((ext_vector_type(2))) float f32x2;
typedef __attribute__((ext_vector_type(2))) __bf16 bf16v2;
typedef __attribute__((ext_vector_type(8))) unsigned u32x8;
#define LAS __attribute__((address_space(3)))
#define GAS __attribute__((address_space(1)))

__device__ __forceinline__ unsigned pack2(float lo, float hi) {
  return __builtin_bit_cast(unsigned, __builtin_convertvector((f32x2){lo, hi}, bf16v2));
}
__device__ __forceinline__ float rb1(float x) { return __uint_as_float(pack2(0.f, x)); }

typedef GAS unsigned gu32;
#define RLX_AGENT __ATOMIC_RELAXED, __HIP_MEMORY_SCOPE_AGENT
#define SPIN_CAP (1u << 20)
#define AUX_SC1 16
#define RSRC_W3 0x00020000
typedef __amdgpu_buffer_rsrc_t rsrc_t;

__device__ __forceinline__ u32x4 ld16(rsrc_t rs, unsigned off) { return __builtin_amdgcn_raw_buffer_load_b128(rs, off, 0, AUX_SC1); }
__device__ __forceinline__ u32x2 ld8(rsrc_t rs, unsigned off) { return __builtin_amdgcn_raw_buffer_load_b64(rs, off, 0, AUX_SC1); }
__device__ __forceinline__ unsigned ld4(rsrc_t rs, unsigned off) { return __builtin_amdgcn_raw_buffer_load_b32(rs, off, 0, AUX_SC1); }
__device__ __forceinline__ void st16(rsrc_t rs, unsigned off, u32x4 v) { __builtin_amdgcn_raw_buffer_store_b128(v, rs, off, 0, AUX_SC1); }
__device__ __forceinline__ void st8(rsrc_t rs, unsigned off, u32x2 v) { __builtin_amdgcn_raw_buffer_store_b64(v, rs, off, 0, AUX_SC1); }
__device__ __forceinline__ void st4(rsrc_t rs, unsigned off, unsigned v) { __builtin_amdgcn_raw_buffer_store_b32(v, rs, off, 0, AUX_SC1); }
__device__ __forceinline__ float lo16(unsigned w) { return __uint_as_float(w << 16); }
__device__ __forceinline__ float hi16(unsigned w) { return __uint_as_float(w & 0xffff0000u); }

__device__ __forceinline__ float xsum16_32(float v) {
  unsigned u = __float_as_uint(v);
  auto a = __builtin_amdgcn_permlane16_swap(u, u, false, false);
  v = __uint_as_float(a[0]) + __uint_as_float(a[1]);
  u = __float_as_uint(v);
  auto b = __builtin_amdgcn_permlane32_swap(u, u, false, false);
  return __uint_as_float(b[0]) + __uint_as_float(b[1]);
}

__device__ __forceinline__ unsigned canon1(unsigned w) { return min(w, 0xfffffffeu); }
__device__ __forceinline__ unsigned max4(u32x4 v) { return max(max(v.x, v.y), max(v.z, v.w)); }
__device__ __forceinline__ int opq(int) { int v; asm volatile("v_mbcnt_lo_u32_b32 %0, -1, 0\n\tv_mbcnt_hi_u32_b32 %0, -1, %0" : "=v"(v)); return v; }
__device__ __forceinline__ u32x4 poison4() { unsigned m = ~0u; asm volatile("" : "+v"(m)); return (u32x4){m, m, m, m}; }
__device__ __forceinline__ f32x4 zero4() { float m = 0.f; asm volatile("" : "+v"(m)); return (f32x4){m, m, m, m}; }
#define MFMA32(a, b, c) __builtin_amdgcn_mfma_f32_16x16x32_bf16(__builtin_bit_cast(bf16x8, a), __builtin_bit_cast(bf16x8, b), c, 0, 0, 0)
#define MFMA16(a, b, c) __builtin_amdgcn_mfma_f32_16x16x16bf16_1k(__builtin_bit_cast(bf16x4, a), __builtin_bit_cast(bf16x4, b), c, 0, 0, 0)
#define LBARRIER() do { asm volatile("s_waitcnt lgkmcnt(0)" ::: "memory"); __builtin_amdgcn_s_barrier(); asm volatile("" ::: "memory"); } while (0)
#define BARRIER() do { asm volatile("" ::: "memory"); __builtin_amdgcn_s_barrier(); asm volatile("" ::: "memory"); } while (0)
#define VMWAIT() asm volatile("s_waitcnt vmcnt(0)" ::: "memory")
#define VMW(n) asm volatile("s_waitcnt vmcnt(" #n ")" ::: "memory")
#define DELAY(n) do { if ((n) > 0) __builtin_amdgcn_s_sleep(n); } while (0)

#define EPO_OFF 0
#define TMO_OFF 128
#define FLG_OFF 256
#define F_LN0(i) (i)
#define F_LN1(i) (64 + (i))
#define F_CAL(i) (128 + (i))
#define F_PRG(t, h) (192 + (t) * 32 + (h))
#define CNT_OFF 65536
#define C_W1(p2) (p2)
#define C_W2(p2) (16 + (p2))
#define C_LN(p2) (32 + (p2))
#define C_W3(p2) (48 + (p2))
#define C_W4(p2) (64 + (p2))
#define C_HEAD 80
#define C_ARR 81
#define NCNT 128
#define WS_XN (1u << 20)
#define WS_XN2 (WS_XN + (4u << 20))
#define WS_QF (WS_XN2 + (4u << 20))
#define WS_KF (WS_QF + (16u << 20))
#define WS_VF (WS_KF + (16u << 20))
#define WS_OX (WS_VF + (16u << 20))
#define WS_H1 (WS_OX + (16u << 20) + (1u << 20))
#define WS_X1 (WS_H1 + (4u << 20) + 65536u)
#define WS_X1N (WS_X1 + (4u << 20))
#define WS_H3 (WS_X1N + (4u << 20))
#define WS_G (WS_H3 + (14u << 20))
#define WS_PT (WS_G + (4u << 20))
#define WS_COMM (WS_PT + (32u << 20))
#define WS_END (WS_COMM + (4u << 20))
#define CB_STEP 524288u
#define PD 3
__device__ __forceinline__ unsigned comm_base(const char* ws) { return WS_COMM + ((0u - ((unsigned)(size_t)ws + WS_COMM)) & 0x1fffffu); }
__device__ __forceinline__ unsigned step_base(unsigned cb, unsigned mask, int s) { return cb + (unsigned)(s & 3) * CB_STEP + mask; }
#define MF_AT(ks) (((ks) >> 3) * 16384u + ((ks) & 7) * 1024u)
#define OB_AT(r, h) ((2u + (r) * 4u + ((h) >> 3)) * 16384u + ((h) & 7) * 1024u)
#define VB_AT(h) ((10u + ((h) >> 2)) * 16384u + ((h) & 3) * 2048u)
#define XS1 22
#define XS2 42
#define XS3 62
#define CSL 8
#define FSL 6
#define SQUICK 3
#define NCHUNK 18
#define FL_O(h) (29u * 16384u + (h) * 128u)
#define FL_B(b) (28u * 16384u + (b) * 128u)
#define FL_T(s) FL_O(32 + ((s) & 3))

struct Ptrs { const u16 *x, *mem0, *Wq, *Wk, *Wv, *Wo, *w1, *w2, *w3, *w4; u16* out; char* ws; unsigned tag; };
#define WS_RSRC(P) __builtin_amdgcn_make_buffer_rsrc((void*)(P).ws, 0, WS_END, RSRC_W3)

__device__ __forceinline__ bool spin_fail(unsigned& spins, const Ptrs& P, int lane) {
  asm volatile("" ::: "memory");
  if (++spins > SPIN_CAP) {
    unsigned tv;
    asm volatile("v_mov_b32 %0, %1" : "=v"(tv) : "s"(P.tag));
    if (lane == 0) __builtin_amdgcn_raw_buffer_store_b32(tv, WS_RSRC(P), 0, TMO_OFF, AUX_SC1);
    return true;
  }
  return (spins & 255u) == 0u && (unsigned)__builtin_amdgcn_readfirstlane(__builtin_amdgcn_raw_buffer_load_b32(WS_RSRC(P), 0, TMO_OFF, AUX_SC1)) == P.tag;
}
__device__ __forceinline__ unsigned cnt_load(char* ws, int idx) {
  return __builtin_amdgcn_readfirstlane(__hip_atomic_load((gu32*)(ws + CNT_OFF + idx * 128), RLX_AGENT));
}
__device__ __forceinline__ void cnt_add(char* ws, int idx, int lane) {
  asm volatile("s_waitcnt vmcnt(0)" ::: "memory");
  if (lane == 0) __hip_atomic_fetch_add((gu32*)(ws + CNT_OFF + idx * 128), 1u, RLX_AGENT);
}
__device__ __forceinline__ void cnt_wait(const Ptrs& P, int idx, unsigned want, int lane) {
  for (unsigned spins = 0;;) {
    if (cnt_load(P.ws, idx) >= want) break;
    __builtin_amdgcn_s_sleep(CSL);
    if (spin_fail(spins, P, lane)) break;
  }
}
template <int SL = FSL>
__device__ __forceinline__ unsigned flag_wait(const Ptrs& P, int f, unsigned want, int lane) {
  const rsrc_t rs = WS_RSRC(P);
  u32x2 v;
  for (unsigned spins = 0;;) {
    v = ld8(rs, FLG_OFF + f * 128);
    if (!__any(v.y != P.tag || v.x < want)) break;
    __builtin_amdgcn_s_sleep(SL);
    if (spin_fail(spins, P, lane)) break;
  }
  asm volatile("" ::: "memory");
  return v.x;
}
__device__ __forceinline__ void flag_set(const Ptrs& P, int f, unsigned val, int lane) {
  if (lane == 0) st8(WS_RSRC(P), FLG_OFF + f * 128, (u32x2){val, P.tag});
}
__device__ __forceinline__ void get_tag(Ptrs& P) { P.tag = __builtin_amdgcn_readfirstlane(__hip_atomic_load((gu32*)(P.ws + EPO_OFF), RLX_AGENT)) + 1u; }
__device__ __forceinline__ void setup_wait(const Ptrs& P, int lane) { flag_wait(P, F_LN0(lane), 0, lane); }
__device__ __forceinline__ void clock_wait(unsigned tgt) {
  while ((int)(tgt - (unsigned)__builtin_amdgcn_s_memrealtime()) > 0) {}
}
__device__ __forceinline__ void report(const Ptrs& P, int wid) {
  if (wid == 0 && opq(0) == 0) __hip_atomic_fetch_add((gu32*)(P.ws + CNT_OFF + C_ARR * 128), 1u, RLX_AGENT);
}
__device__ __forceinline__ void kv_wait(const Ptrs& P, int s, int h, int& known, int lane) {
  if (s < known) return;
  const unsigned v = flag_wait(P, F_PRG(min(lane, 2), h), s + 1, lane);
  known = __builtin_amdgcn_readfirstlane(min(min(__builtin_amdgcn_readlane(v, 0), __builtin_amdgcn_readlane(v, 1)), __builtin_amdgcn_readlane(v, 2)));
}
__device__ __forceinline__ int wmax(int v) {
  v = max(v, __builtin_amdgcn_update_dpp(0, v, 0x128, 0xf, 0xf, false));
  v = max(v, __builtin_amdgcn_update_dpp(0, v, 0x124, 0xf, 0xf, false));
  v = max(v, __builtin_amdgcn_update_dpp(0, v, 0x122, 0xf, 0xf, false));
  v = max(v, __builtin_amdgcn_update_dpp(0, v, 0x121, 0xf, 0xf, false));
  return max(max(__builtin_amdgcn_readlane(v, 0), __builtin_amdgcn_readlane(v, 16)), max(__builtin_amdgcn_readlane(v, 32), __builtin_amdgcn_readlane(v, 48)));
}
__device__ __forceinline__ unsigned near_half(unsigned v) { return wmax(-(int)(v & 65535u)) > wmax(-(int)(v >> 16)) ? 8192u : 0u; }
__device__ __forceinline__ void dmaw(rsrc_t rs, unsigned voff, unsigned soff, unsigned ldsaddr) {
  asm volatile("s_mov_b32 m0, %3\n\ts_nop 0\n\tbuffer_load_dwordx4 %0, %1, %2 offen lds" :: "v"(voff), "s"(rs), "s"(soff), "s"(ldsaddr) : "memory");
}
__device__ __forceinline__ void dman(rsrc_t rs, unsigned voff, unsigned soff, unsigned ldsaddr) {
  asm volatile("s_mov_b32 m0, %3\n\ts_nop 0\n\tbuffer_load_dwordx4 %0, %1, %2 offen nt lds" :: "v"(voff), "s"(rs), "s"(soff), "s"(ldsaddr) : "memory");
}
__device__ __forceinline__ void dmas(rsrc_t rs, unsigned voff, unsigned soff, unsigned ldsaddr) {
  asm volatile("s_mov_b32 m0, %3\n\ts_nop 0\n\tbuffer_load_dwordx4 %0, %1, %2 offen sc1 lds" :: "v"(voff), "s"(rs), "s"(soff), "s"(ldsaddr) : "memory");
}
__device__ __forceinline__ void touch(const void* w, unsigned bytes, unsigned voff, LAS unsigned char* lds) {
  asm volatile("s_mov_b32 m0, %2\n\ts_nop 0\n\tbuffer_load_dword %0, %1, 0 offen lds"
               :: "v"(voff), "s"(__builtin_amdgcn_make_buffer_rsrc((void*)w, 0, bytes, RSRC_W3)), "s"((unsigned)(size_t)lds + 148480) : "memory");
}
__device__ __forceinline__ u32x2 trd(LAS unsigned char* p) {
  return __builtin_bit_cast(u32x2, __builtin_amdgcn_ds_read_tr16_b64_v4i16((LAS bf16x4*)p));
}
template <int BO, int UO>
__device__ __forceinline__ u32x8 rd_frag(LAS unsigned char* p) {
  const u32x2 a = trd(p), b = trd(p + UO), c = trd(p + BO), d = trd(p + BO + UO);
  return (u32x8){a.x, a.y, b.x, b.y, c.x, c.y, d.x, d.y};
}

#define TL(i) do {} while (0)
#define TLW(i) do {} while (0)
#define GSDECL
#define GS(i) do {} while (0)
#define GSFLUSH(k, i) do {} while (0)

#define QPRIO 0
#define VPOS 3
#define VLDI "buffer_load_dword v254, v17, %[rs], %[sb] offen sc1\n\tbuffer_load_dword v255, v17, %[rs], %[sb] offen offset:256 sc1\n\t"
#define VLD(p) VLD##p
#define VLD0
#define VLD1
#define VLD2
#define VLD3 VLDI
#define VLD4
#define DEC1 1
#define DEC2 1
#define DGA
#define DGB
#define DGC
#define DGO
#define DGP
#define DGQ
#define PACE "s_sleep 1\n\t"
#define STR_(x) #x
#define STR(x) STR_(x)
#define GSW
#define GSA(k)
#define GSO(k)
#define A_MAIN_Q() \
  asm volatile( \
    "v_mov_b32 v253, %[tag]\n\t" \
    "buffer_load_dwordx4 v[20:23], v12, %[rs], %[vx] offen sc1\n\t" \
    "buffer_load_dwordx4 v[24:27], v12, %[rs], %[vx] offen offset:1024 sc1\n\t" \
    "ds_read_b128 v[236:239], v9 offset:0\n\t" \
    "ds_read_b128 v[240:243], v9 offset:1024\n\t" \
    "ds_read_b128 v[244:247], v9 offset:2048\n\t" \
    "ds_read_b128 v[248:251], v9 offset:3072\n\t" \
    "v_mov_b32 v0, 0\n\t" \
    "v_mov_b32 v1, 0\n\t" \
    "v_mov_b32 v2, 0\n\t" \
    "v_mov_b32 v3, 0\n\t" \
    "v_mov_b32 v4, 0\n\t" \
    "v_mov_b32 v5, 0\n\t" \
    "v_mov_b32 v6, 0\n\t" \
    "v_mov_b32 v7, 0\n\t" \
    "s_cmp_lg_u32 %[sc], 3\n\t" \
    "s_cbranch_scc1 PB_%=\n\t" \
    "TWA_%=:\n\t" \
    "s_memrealtime s[88:89]\n\t" \
    "s_memtime s[90:91]\n\t" \
    "s_waitcnt lgkmcnt(0)\n\t" \
    "s_lshl_b32 s88, s88, 2\n\t" \
    "s_sub_u32 s89, %[tgt], s88\n\t" \
    "s_cmp_gt_i32 s89, 16\n\t" \
    "s_cbranch_scc1 TWA_%=\n\t" \
    "s_mul_i32 s89, s89, 11\n\t" \
    "s_ashr_i32 s89, s89, 1\n\t" \
    "s_add_u32 s89, s89, s90\n\t" \
    DGB \
    "TCA_%=:\n\t" \
    "s_memtime s[92:93]\n\t" \
    "s_waitcnt lgkmcnt(0)\n\t" \
    "s_sub_u32 s93, s89, s92\n\t" \
    "s_cmp_gt_i32 s93, 0\n\t" \
    "s_cbranch_scc1 TCA_%=\n\t" \
    "PB_%=:\n\t" \
    "s_waitcnt lgkmcnt(0)\n\t" \
    "s_barrier\n\t" \
    "buffer_load_dwordx4 v[220:223], v8, %[rs], %[m0] offen sc1\n\t" \
    "buffer_load_dwordx4 v[224:227], v8, %[rs], %[m1] offen sc1\n\t" \
    "PM_%=:\n\t" \
    "s_waitcnt vmcnt(1)\n\t" \
    "ds_write_b128 v8, v[220:223] offset:0\n\t" \
    "v_max3_u32 v252, v220, v221, v222\n\t" \
    "s_waitcnt vmcnt(0)\n\t" \
    "ds_write_b128 v8, v[224:227] offset:4096\n\t" \
    "v_max3_u32 v252, v252, v223, v224\n\t" \
    "v_max3_u32 v252, v252, v225, v226\n\t" \
    "v_max_u32 v252, v252, v227\n\t" \
    "v_cmp_eq_u32 vcc, -1, v252\n\t" \
    "s_cbranch_vccz PD_%=\n\t" \
    "s_sub_u32 %[lim], %[lim], 1\n\t" \
    "s_cmp_gt_i32 %[lim], 0\n\t" \
    "s_cbranch_scc0 PD_%=\n\t" \
    "s_sleep " STR(CADM) "\n\t" \
    "buffer_load_dwordx4 v[220:223], v8, %[rs], %[m0] offen sc1\n\t" \
    "buffer_load_dwordx4 v[224:227], v8, %[rs], %[m1] offen sc1\n\t" \
    "s_branch PM_%=\n\t" \
    "PD_%=:\n\t" \
    GSA(0) \
    "v_smfmac_f32_16x16x64_bf16 v[0:3], v[220:223], v[236:243], v10\n\t" \
    "v_xor_b32 v9, 16384, v9\n\t" \
    "ds_read_b128 v[236:239], v9 offset:0\n\t" \
    "ds_read_b128 v[240:243], v9 offset:1024\n\t" \
    "v_smfmac_f32_16x16x64_bf16 v[4:7], v[220:223], v[244:251], v10\n\t" \
    "ds_read_b128 v[244:247], v9 offset:2048\n\t" \
    "ds_read_b128 v[248:251], v9 offset:3072\n\t" \
    "s_waitcnt lgkmcnt(4)\n\t" \
    "s_barrier\n\t" \
    "ds_read_b128 v[228:231], v8 offset:8192\n\t" \
    "ds_read_b128 v[232:235], v8 offset:12288\n\t" \
    "v_xor_b32 v8, 1024, v8\n\t" \
    "ds_read_b128 v[220:223], v8 offset:0\n\t" \
    "v_smfmac_f32_16x16x64_bf16 v[0:3], v[224:227], v[28:35], v10\n\t" \
    "v_smfmac_f32_16x16x64_bf16 v[4:7], v[224:227], v[36:43], v10\n\t" \
    "ds_read_b128 v[224:227], v8 offset:4096\n\t" \
    "s_waitcnt lgkmcnt(3)\n\t" \
    "v_smfmac_f32_16x16x64_bf16 v[0:3], v[228:231], v[92:99], v10\n\t" \
    "v_smfmac_f32_16x16x64_bf16 v[4:7], v[228:231], v[100:107], v10\n\t" \
    "ds_read_b128 v[228:231], v8 offset:8192\n\t" \
    "s_waitcnt lgkmcnt(3)\n\t" \
    "v_smfmac_f32_16x16x64_bf16 v[0:3], v[232:235], v[156:163], v10\n\t" \
    "v_smfmac_f32_16x16x64_bf16 v[4:7], v[232:235], v[164:171], v10\n\t" \
    "ds_read_b128 v[232:235], v8 offset:12288\n\t" \
    "s_waitcnt lgkmcnt(3)\n\t" \
    "v_smfmac_f32_16x16x64_bf16 v[0:3], v[220:223], v[236:243], v10\n\t" \
    "v_xor_b32 v9, 49152, v9\n\t" \
    "ds_read_b128 v[236:239], v9 offset:0\n\t" \
    "v_smfmac_f32_16x16x64_bf16 v[4:7], v[220:223], v[244:251], v10\n\t" \
    "ds_read_b128 v[240:243], v9 offset:1024\n\t" \
    "s_waitcnt lgkmcnt(4)\n\t" \
    "v_smfmac_f32_16x16x64_bf16 v[0:3], v[224:227], v[44:51], v10\n\t" \
    "v_xor_b32 v8, 3072, v8\n\t" \
    "ds_read_b128 v[220:223], v8 offset:4096\n\t" \
    "v_smfmac_f32_16x16x64_bf16 v[4:7], v[224:227], v[52:59], v10\n\t" \
    "ds_read_b128 v[244:247], v9 offset:2048\n\t" \
    "s_waitcnt lgkmcnt(5)\n\t" \
    "v_smfmac_f32_16x16x64_bf16 v[0:3], v[228:231], v[108:115], v10\n\t" \
    "ds_read_b128 v[224:227], v8 offset:8192\n\t" \
    "v_smfmac_f32_16x16x64_bf16 v[4:7], v[228:231], v[116:123], v10\n\t" \
    "ds_read_b128 v[248:251], v9 offset:3072\n\t" \
    "s_waitcnt lgkmcnt(6)\n\t" \
    "v_smfmac_f32_16x16x64_bf16 v[0:3], v[232:235], v[172:179], v10\n\t" \
    "ds_read_b128 v[228:231], v8 offset:0\n\t" \
    "v_smfmac_f32_16x16x64_bf16 v[4:7], v[232:235], v[180:187], v10\n\t" \
    "ds_read_b128 v[232:235], v8 offset:12288\n\t" \
    "s_waitcnt lgkmcnt(5)\n\t" \
    "v_smfmac_f32_16x16x64_bf16 v[0:3], v[220:223], v[60:67], v10\n\t" \
    "v_smfmac_f32_16x16x64_bf16 v[4:7], v[220:223], v[68:75], v10\n\t" \
    "v_xor_b32 v8, 1024, v8\n\t" \
    "ds_read_b128 v[220:223], v8 offset:4096\n\t" \
    "s_waitcnt lgkmcnt(4)\n\t" \
    "v_smfmac_f32_16x16x64_bf16 v[0:3], v[224:227], v[124:131], v10\n\t" \
    "v_smfmac_f32_16x16x64_bf16 v[4:7], v[224:227], v[132:139], v10\n\t" \
    "ds_read_b128 v[224:227], v8 offset:8192\n\t" \
    "s_waitcnt lgkmcnt(3)\n\t" \
    "v_smfmac_f32_16x16x64_bf16 v[0:3], v[228:231], v[236:243], v10\n\t" \
    "v_xor_b32 v9, 16384, v9\n\t" \
    "ds_read_b128 v[236:239], v9 offset:0\n\t" \
    "v_smfmac_f32_16x16x64_bf16 v[4:7], v[228:231], v[244:251], v10\n\t" \
    "ds_read_b128 v[240:243], v9 offset:1024\n\t" \
    "s_waitcnt lgkmcnt(4)\n\t" \
    "v_smfmac_f32_16x16x64_bf16 v[0:3], v[232:235], v[188:195], v10\n\t" \
    "ds_read_b128 v[228:231], v8 offset:12288\n\t" \
    "v_smfmac_f32_16x16x64_bf16 v[4:7], v[232:235], v[196:203], v10\n\t" \
    "ds_read_b128 v[244:247], v9 offset:2048\n\t" \
    "s_waitcnt lgkmcnt(5)\n\t" \
    "v_smfmac_f32_16x16x64_bf16 v[0:3], v[220:223], v[76:83], v10\n\t" \
    "ds_read_b128 v[232:235], v8 offset:0\n\t" \
    "v_xor_b32 v8, 3072, v8\n\t" \
    "v_smfmac_f32_16x16x64_bf16 v[4:7], v[220:223], v[84:91], v10\n\t" \
    "ds_read_b128 v[248:251], v9 offset:3072\n\t" \
    "s_waitcnt lgkmcnt(6)\n\t" \
    "v_smfmac_f32_16x16x64_bf16 v[0:3], v[224:227], v[140:147], v10\n\t" \
    "ds_read_b128 v[220:223], v12 offset:24576\n\t" \
    "v_smfmac_f32_16x16x64_bf16 v[4:7], v[224:227], v[148:155], v10\n\t" \
    "ds_read_b128 v[224:227], v12 offset:28672\n\t" \
    "s_waitcnt lgkmcnt(5)\n\t" \
    "v_smfmac_f32_16x16x64_bf16 v[0:3], v[228:231], v[204:211], v10\n\t" \
    "v_smfmac_f32_16x16x64_bf16 v[4:7], v[228:231], v[212:219], v10\n\t" \
    "ds_read_b128 v[228:231], v12 offset:25600\n\t" \
    "s_waitcnt lgkmcnt(3)\n\t" \
    "v_smfmac_f32_16x16x64_bf16 v[0:3], v[232:235], v[236:243], v10\n\t" \
    "v_xor_b32 v9, 49152, v9\n\t" \
    "v_smfmac_f32_16x16x64_bf16 v[4:7], v[232:235], v[244:251], v10\n\t" \
    VLD(4) \
    "s_nop 5\n\t" \
    "v_add_f32 v0, v0, v1\n\t" \
    "v_add_f32 v2, v2, v3\n\t" \
    "v_add_f32 v4, v4, v5\n\t" \
    "v_add_f32 v6, v6, v7\n\t" \
    "v_mul_f32 v0, %[sl], v0\n\t" \
    "v_mul_f32 v2, %[sl], v2\n\t" \
    "v_mul_f32 v4, %[sl], v4\n\t" \
    "v_mul_f32 v6, %[sl], v6\n\t" \
    "v_cvt_pk_bf16_f32 v0, v0, v4\n\t" \
    "v_cvt_pk_bf16_f32 v2, v2, v6\n\t" \
    "ds_write2_b32 v11, v0, v2 offset0:0 offset1:4\n\t" \
    "s_mov_b64 exec, %[onej]\n\t" \
    "ds_write_b32 v16, v253\n\t" \
    "s_mov_b64 exec, -1\n\t" \
    GSA(1) \
    "s_setprio " STR(QPRIO) "\n\t" \
    "ds_read_b128 v[232:235], v12 offset:29696\n\t" \
    "s_nop 5\n\t" \
    "ds_read_b128 v[0:3], v12 offset:26624\n\t" \
    "s_nop 5\n\t" \
    "ds_read_b128 v[4:7], v12 offset:27648\n\t" \
    "s_nop 5\n\t" \
    "ds_read_b32 v252, v16\n\t" \
    "ds_read_b128 v[236:239], v14 offset:0\n\t" \
    "s_nop 5\n\t" \
    "ds_read_b128 v[240:243], v14 offset:512\n\t" \
    "s_nop 5\n\t" \
    "ds_read_b128 v[244:247], v14 offset:1024\n\t" \
    "s_nop 5\n\t" \
    VLD(3) \
    "ds_read_b128 v[248:251], v14 offset:1536\n\t" \
    "s_nop 5\n\t" \
    "s_setprio 0\n\t" \
    "s_waitcnt lgkmcnt(4)\n\t" \
    "v_cmp_ne_u32 vcc, v253, v252\n\t" \
    "s_cbranch_vccnz RP_%=\n\t" \
    "CP_%=:\n\t" \
    VLD(0) \
    "s_waitcnt lgkmcnt(2)\n\t" \
    "v_mfma_f32_16x16x32_bf16 v[220:223], v[220:223], v[236:239], 0\n\t" \
    "v_mfma_f32_16x16x32_bf16 v[224:227], v[224:227], v[236:239], 0\n\t" \
    "v_mfma_f32_16x16x32_bf16 v[220:223], v[228:231], v[240:243], v[220:223]\n\t" \
    "ds_read_b128 v[228:231], v12 offset:30720\n\t" \
    "v_mfma_f32_16x16x32_bf16 v[224:227], v[232:235], v[240:243], v[224:227]\n\t" \
    "ds_read_b128 v[232:235], v12 offset:31744\n\t" \
    "s_setprio " STR(QPRIO) "\n\t" \
    VLD(1) \
    "s_waitcnt lgkmcnt(2)\n\t" \
    "v_mfma_f32_16x16x32_bf16 v[220:223], v[0:3], v[244:247], v[220:223]\n\t" \
    "v_mfma_f32_16x16x32_bf16 v[220:223], v[4:7], v[248:251], v[220:223]\n\t" \
    "s_waitcnt lgkmcnt(1)\n\t" \
    "v_mfma_f32_16x16x32_bf16 v[224:227], v[228:231], v[244:247], v[224:227]\n\t" \
    "s_waitcnt lgkmcnt(0)\n\t" \
    "v_mfma_f32_16x16x32_bf16 v[224:227], v[232:235], v[248:251], v[224:227]\n\t" \
    GSA(2) \
    "s_setprio 2\n\t" \
    "ds_read_b32 v252, v16 offset:16\n\t" \
    "ds_read_b128 v[0:3], v15 offset:0\n\t" \
    "ds_read_b128 v[4:7], v15 offset:512\n\t" \
    "ds_read_b128 v[228:231], v15 offset:1024\n\t" \
    "ds_read_b128 v[232:235], v15 offset:1536\n\t" \
    VLD(2) \
    "v_exp_f32 v220, v220\n\t" \
    "v_exp_f32 v221, v221\n\t" \
    "v_exp_f32 v222, v222\n\t" \
    "v_exp_f32 v223, v223\n\t" \
    "v_exp_f32 v224, v224\n\t" \
    "v_exp_f32 v225, v225\n\t" \
    "v_exp_f32 v226, v226\n\t" \
    "v_exp_f32 v227, v227\n\t" \
    "v_cvt_pk_bf16_f32 v220, v220, v221\n\t" \
    "v_cvt_pk_bf16_f32 v221, v222, v223\n\t" \
    "s_waitcnt lgkmcnt(4)\n\t" \
    "v_cmp_ne_u32 vcc, %[tag], v252\n\t" \
    "s_cbranch_vccnz RK_%=\n\t" \
    "CK_%=:\n\t" \
    "s_waitcnt lgkmcnt(3)\n\t" \
    "v_mfma_f32_16x16x32_bf16 v[0:3], v[0:3], v[236:239], 0\n\t" \
    "v_cvt_pk_bf16_f32 v222, v224, v225\n\t" \
    "v_cvt_pk_bf16_f32 v223, v226, v227\n\t" \
    "s_waitcnt lgkmcnt(2)\n\t" \
    "v_mfma_f32_16x16x32_bf16 v[0:3], v[4:7], v[240:243], v[0:3]\n\t" \
    "v_mov_b32 v224, %[one]\n\t" \
    "v_mov_b32 v225, %[one]\n\t" \
    "s_waitcnt lgkmcnt(1)\n\t" \
    "v_mfma_f32_16x16x32_bf16 v[0:3], v[228:231], v[244:247], v[0:3]\n\t" \
    "v_mov_b32 v226, %[one]\n\t" \
    "v_mov_b32 v227, %[one]\n\t" \
    "s_waitcnt lgkmcnt(0)\n\t" \
    "v_mfma_f32_16x16x32_bf16 v[0:3], v[232:235], v[248:251], v[0:3]\n\t" \
    "v_mfma_f32_16x16x32_bf16 v[244:247], v[224:227], v[220:223], 0\n\t" \
    "v_mov_b32 v229, 0\n\t" \
    "v_mfma_f32_16x16x32_bf16 v[236:239], v[20:23], v[220:223], 0\n\t" \
    "v_mov_b32 v230, 0\n\t" \
    "v_mfma_f32_16x16x32_bf16 v[240:243], v[24:27], v[220:223], 0\n\t" \
    "s_nop 2\n\t" \
    "v_exp_f32 v0, v0\n\t" \
    "v_exp_f32 v1, v1\n\t" \
    "s_nop 0\n\t" \
    "v_cvt_pk_bf16_f32 v228, v0, v1\n\t" \
    "v_cvt_pk_bf16_f32 v231, v0, v1\n\t" \
    GSA(3) \
    "CM_%=:\n\t" \
    "s_nop 0\n\t" \
    "v_mfma_f32_16x16x16_bf16 v[244:247], v[224:225], v[228:229], v[244:247]\n\t" \
    "s_waitcnt vmcnt(0)\n\t" \
    "v_mfma_f32_16x16x16_bf16 v[236:239], v[254:255], v[228:229], v[236:239]\n\t" \
    "v_max_u32 v7, v254, v255\n\t" \
    "v_mfma_f32_16x16x16_bf16 v[240:243], v[254:255], v[230:231], v[240:243]\n\t" \
    "v_cmp_eq_u32 s[88:89], -1, v7\n\t" \
    "s_not_b64 exec, %[hi]\n\t" \
    "s_nop 1\n\t" \
    "v_rcp_f32 v2, v244\n\t" \
    "v_subrev_u32 v253, 0x21800000, v244\n\t" \
    "v_cmp_gt_u32 vcc, v253, %[rg]\n\t" \
    "v_pk_mul_f32 v[236:237], v[236:237], v[2:3] op_sel_hi:[1,0]\n\t" \
    "v_pk_mul_f32 v[238:239], v[238:239], v[2:3] op_sel_hi:[1,0]\n\t" \
    "v_pk_mul_f32 v[240:241], v[240:241], v[2:3] op_sel_hi:[1,0]\n\t" \
    "v_pk_mul_f32 v[242:243], v[242:243], v[2:3] op_sel_hi:[1,0]\n\t" \
    "s_or_b64 vcc, vcc, s[88:89]\n\t" \
    "v_cvt_pk_bf16_f32 v4, v236, v237\n\t" \
    "v_cvt_pk_bf16_f32 v5, v238, v239\n\t" \
    "v_cvt_pk_bf16_f32 v6, v240, v241\n\t" \
    "v_cvt_pk_bf16_f32 v7, v242, v243\n\t" \
    "s_cbranch_scc1 FX_%=\n\t" \
    "buffer_store_dwordx4 v[4:7], v18, %[rs], %[sb] offen sc1\n\t" \
    "s_memrealtime %[tp]\n\t" \
    DGA \
    GSA(4) \
    "v_max3_u32 v252, v4, v5, v6\n\t" \
    "v_max_u32 v252, v252, v7\n\t" \
    "v_cmp_eq_u32 vcc, -1, v252\n\t" \
    "s_cbranch_vccnz RC_%=\n\t" \
    "CC_%=:\n\t" \
    "s_mov_b64 exec, -1\n\t" \
    "v_add_u32 v252, %[vd], v12\n\t" \
    "v_add_u32 v253, %[md], v17\n\t" \
    "ds_write_b128 v252, v[20:23]\n\t" \
    "ds_write_b128 v252, v[24:27] offset:1024\n\t" \
    "ds_write2st64_b32 v253, v254, v255 offset0:0 offset1:1\n\t" \
    "s_waitcnt lgkmcnt(0)\n\t" \
    "s_cmp_lg_u32 %[sc], 3\n\t" \
    "s_cbranch_scc1 NH_%=\n\t" \
    "buffer_load_dwordx2 v[0:1], off, %[rs], %[fl] sc1\n\t" \
    "s_barrier\n\t" \
    "s_waitcnt vmcnt(0)\n\t" \
    "v_readfirstlane_b32 %[pt], v0\n\t" \
    "v_readfirstlane_b32 %[pd], v1\n\t" \
    "s_nop 3\n\t" \
    "s_branch E_%=\n\t" \
    "NH_%=:\n\t" \
    "s_barrier\n\t" \
    "s_branch E_%=\n\t" \
    "RP_%=:\n\t" \
    "s_waitcnt lgkmcnt(0)\n\t" \
    "ds_read_b32 v252, v16\n\t" \
    "ds_read_b128 v[236:239], v14 offset:0\n\t" \
    "ds_read_b128 v[240:243], v14 offset:512\n\t" \
    "ds_read_b128 v[244:247], v14 offset:1024\n\t" \
    "ds_read_b128 v[248:251], v14 offset:1536\n\t" \
    "s_sub_u32 %[lim], %[lim], " STR(DEC1) "\n\t" \
    "s_cmp_gt_i32 %[lim], 0\n\t" \
    "s_waitcnt lgkmcnt(4)\n\t" \
    "s_cbranch_scc0 CP_%=\n\t" \
    "v_cmp_ne_u32 vcc, %[tag], v252\n\t" \
    "s_cbranch_vccnz RP_%=\n\t" \
    "s_branch CP_%=\n\t" \
    "RK_%=:\n\t" \
    "s_waitcnt lgkmcnt(0)\n\t" \
    "ds_read_b32 v252, v16 offset:16\n\t" \
    "ds_read_b128 v[0:3], v15 offset:0\n\t" \
    "ds_read_b128 v[4:7], v15 offset:512\n\t" \
    "ds_read_b128 v[228:231], v15 offset:1024\n\t" \
    "ds_read_b128 v[232:235], v15 offset:1536\n\t" \
    "s_sub_u32 %[lim], %[lim], " STR(DEC1) "\n\t" \
    "s_cmp_gt_i32 %[lim], 0\n\t" \
    "s_waitcnt lgkmcnt(4)\n\t" \
    "s_cbranch_scc0 CK_%=\n\t" \
    "v_cmp_ne_u32 vcc, %[tag], v252\n\t" \
    "s_cbranch_vccnz RK_%=\n\t" \
    "s_branch CK_%=\n\t" \
    "RC_%=:\n\t" \
    "v_min_u32 v4, -2, v4\n\t" \
    "v_min_u32 v5, -2, v5\n\t" \
    "v_min_u32 v6, -2, v6\n\t" \
    "v_min_u32 v7, -2, v7\n\t" \
    "buffer_store_dwordx4 v[4:7], v18, %[rs], %[sb] offen sc1\n\t" \
    "s_branch CC_%=\n\t" \
    "FX_%=:\n\t" \
    "s_mov_b64 exec, -1\n\t" \
    "s_cmp_eq_u64 s[88:89], 0\n\t" \
    "s_cbranch_scc1 RM_%=\n\t" \
    "buffer_load_dword v254, v17, %[rs], %[sb] offen sc1\n\t" \
    "buffer_load_dword v255, v17, %[rs], %[sb] offen offset:256 sc1\n\t" \
    "v_mfma_f32_16x16x32_bf16 v[244:247], v[224:227], v[220:223], 0\n\t" \
    "v_mfma_f32_16x16x32_bf16 v[236:239], v[20:23], v[220:223], 0\n\t" \
    "v_mfma_f32_16x16x32_bf16 v[240:243], v[24:27], v[220:223], 0\n\t" \
    "s_sub_u32 %[lim], %[lim], " STR(DEC2) "\n\t" \
    "s_cmp_gt_i32 %[lim], 0\n\t" \
    "s_cbranch_scc1 CM_%=\n\t" \
    "s_waitcnt vmcnt(0)\n\t" \
    "v_mov_b32 v254, 0\n\t" \
    "v_mov_b32 v255, 0\n\t" \
    "s_branch CM_%=\n\t" \
    "RM_%=:\n\t" \
    "s_mov_b32 %[rg], -1\n\t" \
    "s_nop 7\n\t" \
    "ds_read_b128 v[236:239], v14 offset:0\n\t" \
    "ds_read_b128 v[240:243], v14 offset:512\n\t" \
    "ds_read_b128 v[244:247], v14 offset:1024\n\t" \
    "ds_read_b128 v[248:251], v14 offset:1536\n\t" \
    "ds_read_b128 v[232:235], v12 offset:24576\n\t" \
    "s_waitcnt lgkmcnt(0)\n\t" \
    "v_mfma_f32_16x16x32_bf16 v[220:223], v[232:235], v[236:239], 0\n\t" \
    "ds_read_b128 v[232:235], v12 offset:25600\n\t" \
    "s_waitcnt lgkmcnt(0)\n\t" \
    "v_mfma_f32_16x16x32_bf16 v[220:223], v[232:235], v[240:243], v[220:223]\n\t" \
    "ds_read_b128 v[232:235], v12 offset:26624\n\t" \
    "s_waitcnt lgkmcnt(0)\n\t" \
    "v_mfma_f32_16x16x32_bf16 v[220:223], v[232:235], v[244:247], v[220:223]\n\t" \
    "ds_read_b128 v[232:235], v12 offset:27648\n\t" \
    "s_waitcnt lgkmcnt(0)\n\t" \
    "v_mfma_f32_16x16x32_bf16 v[220:223], v[232:235], v[248:251], v[220:223]\n\t" \
    "ds_read_b128 v[232:235], v12 offset:28672\n\t" \
    "s_waitcnt lgkmcnt(0)\n\t" \
    "v_mfma_f32_16x16x32_bf16 v[224:227], v[232:235], v[236:239], 0\n\t" \
    "ds_read_b128 v[232:235], v12 offset:29696\n\t" \
    "s_waitcnt lgkmcnt(0)\n\t" \
    "v_mfma_f32_16x16x32_bf16 v[224:227], v[232:235], v[240:243], v[224:227]\n\t" \
    "ds_read_b128 v[232:235], v12 offset:30720\n\t" \
    "s_waitcnt lgkmcnt(0)\n\t" \
    "v_mfma_f32_16x16x32_bf16 v[224:227], v[232:235], v[244:247], v[224:227]\n\t" \
    "ds_read_b128 v[232:235], v12 offset:31744\n\t" \
    "s_waitcnt lgkmcnt(0)\n\t" \
    "v_mfma_f32_16x16x32_bf16 v[224:227], v[232:235], v[248:251], v[224:227]\n\t" \
    "ds_read_b128 v[232:235], v15 offset:0\n\t" \
    "s_waitcnt lgkmcnt(0)\n\t" \
    "v_mfma_f32_16x16x32_bf16 v[0:3], v[232:235], v[236:239], 0\n\t" \
    "ds_read_b128 v[232:235], v15 offset:512\n\t" \
    "s_waitcnt lgkmcnt(0)\n\t" \
    "v_mfma_f32_16x16x32_bf16 v[0:3], v[232:235], v[240:243], v[0:3]\n\t" \
    "ds_read_b128 v[232:235], v15 offset:1024\n\t" \
    "s_waitcnt lgkmcnt(0)\n\t" \
    "v_mfma_f32_16x16x32_bf16 v[0:3], v[232:235], v[244:247], v[0:3]\n\t" \
    "ds_read_b128 v[232:235], v15 offset:1536\n\t" \
    "s_waitcnt lgkmcnt(0)\n\t" \
    "v_mfma_f32_16x16x32_bf16 v[0:3], v[232:235], v[248:251], v[0:3]\n\t" \
    "v_max3_f32 v252, v220, v221, v222\n\t" \
    "v_max3_f32 v253, v223, v224, v225\n\t" \
    "v_max3_f32 v252, v252, v226, v227\n\t" \
    "s_nop 4\n\t" \
    "v_max3_f32 v253, v253, v0, v1\n\t" \
    "v_max_f32 v252, v252, v253\n\t" \
    "v_mov_b32 v253, v252\n\t" \
    "v_mov_b32 v2, v252\n\t" \
    "s_nop 1\n\t" \
    "v_permlane16_swap_b32 v253, v2\n\t" \
    "v_max_f32 v252, v253, v2\n\t" \
    "v_max_f32 v253, v253, v2\n\t" \
    "s_nop 1\n\t" \
    "v_permlane32_swap_b32 v252, v253\n\t" \
    "v_max_f32 v252, v252, v253\n\t" \
    "v_sub_f32 v220, v220, v252\n\t" \
    "v_sub_f32 v221, v221, v252\n\t" \
    "v_sub_f32 v222, v222, v252\n\t" \
    "v_sub_f32 v223, v223, v252\n\t" \
    "v_sub_f32 v224, v224, v252\n\t" \
    "v_sub_f32 v225, v225, v252\n\t" \
    "v_sub_f32 v226, v226, v252\n\t" \
    "v_sub_f32 v227, v227, v252\n\t" \
    "v_sub_f32 v0, v0, v252\n\t" \
    "v_sub_f32 v1, v1, v252\n\t" \
    "v_exp_f32 v220, v220\n\t" \
    "v_exp_f32 v221, v221\n\t" \
    "v_exp_f32 v222, v222\n\t" \
    "v_exp_f32 v223, v223\n\t" \
    "v_exp_f32 v224, v224\n\t" \
    "v_exp_f32 v225, v225\n\t" \
    "v_exp_f32 v226, v226\n\t" \
    "v_exp_f32 v227, v227\n\t" \
    "v_cvt_pk_bf16_f32 v220, v220, v221\n\t" \
    "v_cvt_pk_bf16_f32 v221, v222, v223\n\t" \
    "v_cvt_pk_bf16_f32 v222, v224, v225\n\t" \
    "v_cvt_pk_bf16_f32 v223, v226, v227\n\t" \
    "v_mov_b32 v224, %[one]\n\t" \
    "v_mov_b32 v225, %[one]\n\t" \
    "v_mov_b32 v226, %[one]\n\t" \
    "v_mov_b32 v227, %[one]\n\t" \
    "s_nop 1\n\t" \
    "v_mfma_f32_16x16x32_bf16 v[244:247], v[224:227], v[220:223], 0\n\t" \
    "v_mfma_f32_16x16x32_bf16 v[236:239], v[20:23], v[220:223], 0\n\t" \
    "v_mfma_f32_16x16x32_bf16 v[240:243], v[24:27], v[220:223], 0\n\t" \
    "v_exp_f32 v0, v0\n\t" \
    "v_exp_f32 v1, v1\n\t" \
    "s_nop 0\n\t" \
    "v_cvt_pk_bf16_f32 v228, v0, v1\n\t" \
    "v_cvt_pk_bf16_f32 v231, v0, v1\n\t" \
    "s_nop 1\n\t" \
    "s_branch CM_%=\n\t" \
    "E_%=:\n\t" \
    GSW \
    "s_nop 1\n\t" \
    : [lim] "+s"(lim), [rg] "+s"(rg), [tp] "=&s"(tp), [pt] "=&s"(pt), [pd] "=&s"(pd) DGO DGP GSO(0) GSO(1) GSO(2) GSO(3) GSO(4) \
    : "{v8}"(am_a), "{v9}"(t0_a), "{v10}"(idx), "{v11}"(iw_a), "{v12}"(ab_a), "{v14}"(qr_a), "{v15}"(kr_a), "{v16}"(fr_a), "{v17}"(vo_v), "{v18}"(oo_v), [rs] "s"(rs), [sb] "s"(sb), [tag] "s"(0xffffff81u + s), [hi] "s"(0xff00ff00ff00ff00ull), [tgt] "s"(tgt), [sc] "s"(wid), [onej] "s"(onej), [sl] "s"(0x3e0293ee), [one] "s"(0x3f803f80), [m0] "s"(sbm), [m1] "s"(sbm + 4096u), [vx] "s"(vxo), [fl] "s"(fb + FL_T(s + 1)), [vd] "s"(8192 + j * 2048), [md] "s"(md), "{v[28:35]}"(wr[0][0]), "{v[36:43]}"(wr[0][1]), "{v[44:51]}"(wr[1][0]), "{v[52:59]}"(wr[1][1]), "{v[60:67]}"(wr[2][0]), "{v[68:75]}"(wr[2][1]), "{v[76:83]}"(wr[3][0]), "{v[84:91]}"(wr[3][1]), "{v[92:99]}"(wr[4][0]), "{v[100:107]}"(wr[4][1]), "{v[108:115]}"(wr[5][0]), "{v[116:123]}"(wr[5][1]), "{v[124:131]}"(wr[6][0]), "{v[132:139]}"(wr[6][1]), "{v[140:147]}"(wr[7][0]), "{v[148:155]}"(wr[7][1]), "{v[156:163]}"(wr[8][0]), "{v[164:171]}"(wr[8][1]), "{v[172:179]}"(wr[9][0]), "{v[180:187]}"(wr[9][1]), "{v[188:195]}"(wr[10][0]), "{v[196:203]}"(wr[10][1]), "{v[204:211]}"(wr[11][0]), "{v[212:219]}"(wr[11][1]) \
    : "memory", "vcc", "scc", "s88", "s89", "s90", "s91", "s92", "s93", "v0", "v1", "v2", "v3", "v4", "v5", "v6", "v7", "v20", "v21", "v22", "v23", "v24", "v25", "v26", "v27", "v220", "v221", "v222", "v223", "v224", "v225", "v226", "v227", "v228", "v229", "v230", "v231", "v232", "v233", "v234", "v235", "v236", "v237", "v238", "v239", "v240", "v241", "v242", "v243", "v244", "v245", "v246", "v247", "v248", "v249", "v250", "v251", "v252", "v253", "v254", "v255")

#define A_MAIN_K() \
  asm volatile( \
    "ds_read_b128 v[236:239], v9 offset:0\n\t" \
    "ds_read_b128 v[240:243], v9 offset:1024\n\t" \
    "ds_read_b128 v[244:247], v9 offset:2048\n\t" \
    "ds_read_b128 v[248:251], v9 offset:3072\n\t" \
    "v_mov_b32 v0, 0\n\t" \
    "v_mov_b32 v1, 0\n\t" \
    "v_mov_b32 v2, 0\n\t" \
    "v_mov_b32 v3, 0\n\t" \
    "v_mov_b32 v4, 0\n\t" \
    "v_mov_b32 v5, 0\n\t" \
    "v_mov_b32 v6, 0\n\t" \
    "v_mov_b32 v7, 0\n\t" \
    "s_waitcnt lgkmcnt(0)\n\t" \
    "s_barrier\n\t" \
    "buffer_load_dwordx4 v[220:223], v8, %[rs], %[m0] offen sc1\n\t" \
    "buffer_load_dwordx4 v[224:227], v8, %[rs], %[m1] offen sc1\n\t" \
    GSA(3) \
    "v_mov_b32 v252, v252\n\t" \
    "s_nop 2\n\t" \
    "v_mov_b32 v252, v252\n\t" \
    "s_nop 2\n\t" \
    "v_mov_b32 v252, v252\n\t" \
    "s_nop 2\n\t" \
    "v_mov_b32 v252, v252\n\t" \
    "s_nop 2\n\t" \
    "v_mov_b32 v252, v252\n\t" \
    "s_nop 2\n\t" \
    "v_mov_b32 v252, v252\n\t" \
    "s_nop 2\n\t" \
    "v_mov_b32 v252, v252\n\t" \
    "s_nop 2\n\t" \
    "v_mov_b32 v252, v252\n\t" \
    "s_nop 2\n\t" \
    "v_mov_b32 v252, v252\n\t" \
    "s_nop 2\n\t" \
    "v_mov_b32 v252, v252\n\t" \
    "s_nop 2\n\t" \
    "v_mov_b32 v252, v252\n\t" \
    "s_nop 2\n\t" \
    "v_mov_b32 v252, v252\n\t" \
    "s_nop 2\n\t" \
    "v_mov_b32 v252, v252\n\t" \
    "s_nop 2\n\t" \
    "v_mov_b32 v252, v252\n\t" \
    "s_nop 2\n\t" \
    "v_mov_b32 v252, v252\n\t" \
    "s_nop 2\n\t" \
    "v_mov_b32 v252, v252\n\t" \
    "s_nop 2\n\t" \
    "v_mov_b32 v252, v252\n\t" \
    "s_nop 2\n\t" \
    "v_mov_b32 v252, v252\n\t" \
    "s_nop 2\n\t" \
    "v_mov_b32 v252, v252\n\t" \
    "s_nop 2\n\t" \
    "v_mov_b32 v252, v252\n\t" \
    "s_nop 2\n\t" \
    "v_mov_b32 v252, v252\n\t" \
    "s_nop 2\n\t" \
    "v_mov_b32 v252, v252\n\t" \
    "s_nop 2\n\t" \
    "v_mov_b32 v252, v252\n\t" \
    "s_nop 2\n\t" \
    "v_mov_b32 v252, v252\n\t" \
    "s_nop 2\n\t" \
    "v_mov_b32 v252, v252\n\t" \
    "s_nop 2\n\t" \
    "v_mov_b32 v252, v252\n\t" \
    "s_nop 2\n\t" \
    "v_mov_b32 v252, v252\n\t" \
    "s_nop 2\n\t" \
    "v_mov_b32 v252, v252\n\t" \
    "s_nop 2\n\t" \
    "v_mov_b32 v252, v252\n\t" \
    "s_nop 2\n\t" \
    "v_mov_b32 v252, v252\n\t" \
    "s_nop 2\n\t" \
    "PM_%=:\n\t" \
    "s_waitcnt vmcnt(1)\n\t" \
    "ds_write_b128 v8, v[220:223] offset:8192\n\t" \
    "v_max3_u32 v252, v220, v221, v222\n\t" \
    "s_waitcnt vmcnt(0)\n\t" \
    "ds_write_b128 v8, v[224:227] offset:12288\n\t" \
    "v_max3_u32 v252, v252, v223, v224\n\t" \
    "v_max3_u32 v252, v252, v225, v226\n\t" \
    "v_max_u32 v252, v252, v227\n\t" \
    "v_cmp_eq_u32 vcc, -1, v252\n\t" \
    "s_cbranch_vccz PD_%=\n\t" \
    "s_sub_u32 %[lim], %[lim], 1\n\t" \
    "s_cmp_gt_i32 %[lim], 0\n\t" \
    "s_cbranch_scc0 PD_%=\n\t" \
    "s_sleep " STR(CADM) "\n\t" \
    "buffer_load_dwordx4 v[220:223], v8, %[rs], %[m0] offen sc1\n\t" \
    "buffer_load_dwordx4 v[224:227], v8, %[rs], %[m1] offen sc1\n\t" \
    "s_branch PM_%=\n\t" \
    "PD_%=:\n\t" \
    GSA(0) \
    "v_smfmac_f32_16x16x64_bf16 v[0:3], v[220:223], v[236:243], v10\n\t" \
    "v_xor_b32 v9, 16384, v9\n\t" \
    "ds_read_b128 v[236:239], v9 offset:0\n\t" \
    "ds_read_b128 v[240:243], v9 offset:1024\n\t" \
    "v_smfmac_f32_16x16x64_bf16 v[4:7], v[220:223], v[244:251], v10\n\t" \
    "ds_read_b128 v[244:247], v9 offset:2048\n\t" \
    "ds_read_b128 v[248:251], v9 offset:3072\n\t" \
    "s_waitcnt lgkmcnt(4)\n\t" \
    "s_barrier\n\t" \
    DGA \
    "s_cmp_lg_u32 %[st], 63\n\t" \
    "s_cbranch_scc1 NL_%=\n\t" \
    "s_mov_b64 exec, %[fm]\n\t" \
    "v_mov_b32 v252, %[st]\n\t" \
    "v_mov_b32 v253, %[tag]\n\t" \
    "v_mov_b32 v254, 0\n\t" \
    "buffer_store_dwordx2 v[252:253], v254, %[rs], %[po] offen sc1\n\t" \
    "s_mov_b64 exec, -1\n\t" \
    "NL_%=:\n\t" \
    "ds_read_b128 v[228:231], v8 offset:0\n\t" \
    "ds_read_b128 v[232:235], v8 offset:4096\n\t" \
    "v_smfmac_f32_16x16x64_bf16 v[0:3], v[224:227], v[148:155], v10\n\t" \
    "v_xor_b32 v8, 1024, v8\n\t" \
    "ds_read_b128 v[220:223], v8 offset:8192\n\t" \
    "v_smfmac_f32_16x16x64_bf16 v[4:7], v[224:227], v[156:163], v10\n\t" \
    "ds_read_b128 v[224:227], v8 offset:12288\n\t" \
    "buffer_store_dwordx4 v[212:215], v14, %[rs], %[ox] offen offset:0 sc1\n\t" \
    "buffer_store_dwordx4 v[216:219], v14, %[rs], %[ox] offen offset:512 sc1\n\t" \
    "s_waitcnt lgkmcnt(3)\n\t" \
    "v_smfmac_f32_16x16x64_bf16 v[0:3], v[228:231], v[20:27], v10\n\t" \
    "v_smfmac_f32_16x16x64_bf16 v[4:7], v[228:231], v[28:35], v10\n\t" \
    "ds_read_b128 v[228:231], v8 offset:0\n\t" \
    "s_waitcnt lgkmcnt(3)\n\t" \
    "v_smfmac_f32_16x16x64_bf16 v[0:3], v[232:235], v[84:91], v10\n\t" \
    "v_smfmac_f32_16x16x64_bf16 v[4:7], v[232:235], v[92:99], v10\n\t" \
    "ds_read_b128 v[232:235], v8 offset:4096\n\t" \
    "s_waitcnt lgkmcnt(3)\n\t" \
    "v_smfmac_f32_16x16x64_bf16 v[0:3], v[220:223], v[236:243], v10\n\t" \
    "v_xor_b32 v9, 49152, v9\n\t" \
    "ds_read_b128 v[236:239], v9 offset:0\n\t" \
    "v_smfmac_f32_16x16x64_bf16 v[4:7], v[220:223], v[244:251], v10\n\t" \
    "ds_read_b128 v[240:243], v9 offset:1024\n\t" \
    "s_waitcnt lgkmcnt(4)\n\t" \
    "v_smfmac_f32_16x16x64_bf16 v[0:3], v[224:227], v[164:171], v10\n\t" \
    "v_xor_b32 v8, 3072, v8\n\t" \
    "ds_read_b128 v[220:223], v8 offset:12288\n\t" \
    "v_smfmac_f32_16x16x64_bf16 v[4:7], v[224:227], v[172:179], v10\n\t" \
    "ds_read_b128 v[244:247], v9 offset:2048\n\t" \
    "s_waitcnt lgkmcnt(5)\n\t" \
    "v_smfmac_f32_16x16x64_bf16 v[0:3], v[228:231], v[36:43], v10\n\t" \
    "ds_read_b128 v[224:227], v8 offset:0\n\t" \
    "v_smfmac_f32_16x16x64_bf16 v[4:7], v[228:231], v[44:51], v10\n\t" \
    "ds_read_b128 v[248:251], v9 offset:3072\n\t" \
    "s_waitcnt lgkmcnt(6)\n\t" \
    "v_smfmac_f32_16x16x64_bf16 v[0:3], v[232:235], v[100:107], v10\n\t" \
    "ds_read_b128 v[228:231], v8 offset:8192\n\t" \
    "v_smfmac_f32_16x16x64_bf16 v[4:7], v[232:235], v[108:115], v10\n\t" \
    "ds_read_b128 v[232:235], v8 offset:4096\n\t" \
    "s_waitcnt lgkmcnt(5)\n\t" \
    "v_smfmac_f32_16x16x64_bf16 v[0:3], v[220:223], v[180:187], v10\n\t" \
    "v_smfmac_f32_16x16x64_bf16 v[4:7], v[220:223], v[188:195], v10\n\t" \
    "v_xor_b32 v8, 1024, v8\n\t" \
    "ds_read_b128 v[220:223], v8 offset:12288\n\t" \
    "s_waitcnt lgkmcnt(4)\n\t" \
    "v_smfmac_f32_16x16x64_bf16 v[0:3], v[224:227], v[52:59], v10\n\t" \
    "v_smfmac_f32_16x16x64_bf16 v[4:7], v[224:227], v[60:67], v10\n\t" \
    "ds_read_b128 v[224:227], v8 offset:0\n\t" \
    "s_waitcnt lgkmcnt(3)\n\t" \
    "v_smfmac_f32_16x16x64_bf16 v[0:3], v[228:231], v[236:243], v10\n\t" \
    "v_xor_b32 v9, 16384, v9\n\t" \
    "ds_read_b128 v[236:239], v9 offset:0\n\t" \
    "v_smfmac_f32_16x16x64_bf16 v[4:7], v[228:231], v[244:251], v10\n\t" \
    "ds_read_b128 v[240:243], v9 offset:1024\n\t" \
    "s_waitcnt lgkmcnt(4)\n\t" \
    "v_smfmac_f32_16x16x64_bf16 v[0:3], v[232:235], v[116:123], v10\n\t" \
    "ds_read_b128 v[228:231], v8 offset:4096\n\t" \
    "v_smfmac_f32_16x16x64_bf16 v[4:7], v[232:235], v[124:131], v10\n\t" \
    "ds_read_b128 v[244:247], v9 offset:2048\n\t" \
    "s_waitcnt lgkmcnt(5)\n\t" \
    "v_smfmac_f32_16x16x64_bf16 v[0:3], v[220:223], v[196:203], v10\n\t" \
    "ds_read_b128 v[232:235], v8 offset:8192\n\t" \
    "v_xor_b32 v8, 3072, v8\n\t" \
    "v_smfmac_f32_16x16x64_bf16 v[4:7], v[220:223], v[204:211], v10\n\t" \
    "ds_read_b128 v[248:251], v9 offset:3072\n\t" \
    "s_waitcnt lgkmcnt(6)\n\t" \
    "v_smfmac_f32_16x16x64_bf16 v[0:3], v[224:227], v[68:75], v10\n\t" \
    "v_smfmac_f32_16x16x64_bf16 v[4:7], v[224:227], v[76:83], v10\n\t" \
    "s_waitcnt lgkmcnt(3)\n\t" \
    "v_smfmac_f32_16x16x64_bf16 v[0:3], v[228:231], v[132:139], v10\n\t" \
    "v_smfmac_f32_16x16x64_bf16 v[4:7], v[228:231], v[140:147], v10\n\t" \
    "s_waitcnt lgkmcnt(0)\n\t" \
    "v_smfmac_f32_16x16x64_bf16 v[0:3], v[232:235], v[236:243], v10\n\t" \
    "v_xor_b32 v9, 49152, v9\n\t" \
    "v_smfmac_f32_16x16x64_bf16 v[4:7], v[232:235], v[244:251], v10\n\t" \
    "v_mov_b32 v253, %[stag]\n\t" \
    "s_nop 4\n\t" \
    "v_add_f32 v0, v0, v1\n\t" \
    "v_add_f32 v2, v2, v3\n\t" \
    "v_add_f32 v4, v4, v5\n\t" \
    "v_add_f32 v6, v6, v7\n\t" \
    "v_cvt_pk_bf16_f32 v0, v0, v4\n\t" \
    "v_cvt_pk_bf16_f32 v2, v2, v6\n\t" \
    "ds_write2_b32 v11, v0, v2 offset0:0 offset1:4\n\t" \
    "s_mov_b64 exec, %[onej]\n\t" \
    "ds_write_b32 v18, v253 offset:16\n\t" \
    "s_mov_b64 exec, -1\n\t" \
    GSA(1) \
    "s_setprio 0\n\t" \
    "buffer_load_dwordx4 v[236:239], v12, %[rs], %[qx] offen offset:0 sc1\n\t" \
    "buffer_load_dwordx4 v[240:243], v12, %[rs], %[qx] offen offset:1024 sc1\n\t" \
    "buffer_load_dwordx4 v[244:247], v12, %[rs], %[qx] offen offset:2048 sc1\n\t" \
    "buffer_load_dwordx4 v[248:251], v12, %[rs], %[qx] offen offset:3072 sc1\n\t" \
    "v_cmp_le_i32 s[72:73], 0, v16\n\t" \
    "v_cmp_le_i32 s[74:75], 1, v16\n\t" \
    "v_cmp_le_i32 s[76:77], 2, v16\n\t" \
    "v_cmp_le_i32 s[78:79], 3, v16\n\t" \
    "v_cmp_le_i32 s[80:81], 16, v16\n\t" \
    "v_cmp_le_i32 s[82:83], 17, v16\n\t" \
    "v_cmp_le_i32 s[84:85], 18, v16\n\t" \
    "v_cmp_le_i32 s[86:87], 19, v16\n\t" \
    "v_mov_b32 v254, v254\n\t" \
    "s_nop 2\n\t" \
    "v_mov_b32 v254, v254\n\t" \
    "s_nop 2\n\t" \
    "v_mov_b32 v254, v254\n\t" \
    "s_nop 2\n\t" \
    "v_mov_b32 v254, v254\n\t" \
    "s_nop 2\n\t" \
    "v_mov_b32 v254, v254\n\t" \
    "s_nop 2\n\t" \
    "v_mov_b32 v254, v254\n\t" \
    "s_nop 2\n\t" \
    "v_mov_b32 v254, v254\n\t" \
    "s_nop 2\n\t" \
    "v_mov_b32 v254, v254\n\t" \
    "s_nop 2\n\t" \
    "v_mov_b32 v254, v254\n\t" \
    "s_nop 2\n\t" \
    "v_mov_b32 v254, v254\n\t" \
    "s_nop 2\n\t" \
    "v_mov_b32 v254, v254\n\t" \
    "s_nop 2\n\t" \
    "v_mov_b32 v254, v254\n\t" \
    "s_nop 2\n\t" \
    "v_mov_b32 v254, v254\n\t" \
    "s_nop 2\n\t" \
    "v_mov_b32 v254, v254\n\t" \
    "s_nop 2\n\t" \
    "v_mov_b32 v254, v254\n\t" \
    "s_nop 2\n\t" \
    "v_mov_b32 v254, v254\n\t" \
    "s_nop 2\n\t" \
    "v_mov_b32 v254, v254\n\t" \
    "s_nop 2\n\t" \
    "v_mov_b32 v254, v254\n\t" \
    "s_nop 2\n\t" \
    "v_mov_b32 v254, v254\n\t" \
    "s_nop 2\n\t" \
    "v_mov_b32 v254, v254\n\t" \
    "s_nop 2\n\t" \
    "LW_%=:\n\t" \
    "ds_read_b32 v0, v18 offset:16\n\t" \
    "s_sub_u32 %[lim], %[lim], " STR(DEC1) "\n\t" \
    "s_cmp_gt_i32 %[lim], 0\n\t" \
    "v_mov_b32 v254, v254\n\t" \
    "s_nop 2\n\t" \
    "v_mov_b32 v254, v254\n\t" \
    "s_nop 2\n\t" \
    "v_mov_b32 v254, v254\n\t" \
    "s_nop 2\n\t" \
    "v_mov_b32 v254, v254\n\t" \
    "s_nop 2\n\t" \
    "v_mov_b32 v254, v254\n\t" \
    "s_nop 2\n\t" \
    "s_waitcnt lgkmcnt(0)\n\t" \
    "s_cbranch_scc0 CW_%=\n\t" \
    "v_cmp_ne_u32 vcc, v0, v253\n\t" \
    "s_cbranch_vccnz LW_%=\n\t" \
    "CW_%=:\n\t" \
    "ds_read_b128 v[212:215], v12 offset:24576\n\t" \
    "ds_read_b128 v[216:219], v12 offset:28672\n\t" \
    "ds_read_b128 v[220:223], v15 offset:0\n\t" \
    "ds_read_b128 v[224:227], v12 offset:25600\n\t" \
    "ds_read_b128 v[228:231], v12 offset:29696\n\t" \
    "v_add_u32 v253, %[se], v12\n\t" \
    "v_mov_b32 v254, v254\n\t" \
    "s_nop 2\n\t" \
    "v_mov_b32 v254, v254\n\t" \
    "s_nop 2\n\t" \
    "v_mov_b32 v254, v254\n\t" \
    "s_nop 2\n\t" \
    "v_mov_b32 v254, v254\n\t" \
    "s_nop 2\n\t" \
    "s_waitcnt vmcnt(0)\n\t" \
    "s_waitcnt lgkmcnt(4)\n\t" \
    "v_mfma_f32_16x16x32_bf16 v[0:3], v[212:215], v[236:239], 0\n\t" \
    "ds_read_b128 v[212:215], v15 offset:512\n\t" \
    "s_waitcnt lgkmcnt(4)\n\t" \
    "v_mfma_f32_16x16x32_bf16 v[4:7], v[216:219], v[236:239], 0\n\t" \
    "ds_read_b128 v[216:219], v12 offset:26624\n\t" \
    "s_waitcnt lgkmcnt(4)\n\t" \
    "v_mfma_f32_16x16x32_bf16 v[232:235], v[220:223], v[236:239], 0\n\t" \
    "ds_read_b128 v[220:223], v12 offset:30720\n\t" \
    "s_waitcnt lgkmcnt(4)\n\t" \
    "v_mfma_f32_16x16x32_bf16 v[0:3], v[224:227], v[240:243], v[0:3]\n\t" \
    "ds_read_b128 v[224:227], v15 offset:1024\n\t" \
    "s_waitcnt lgkmcnt(4)\n\t" \
    "v_mfma_f32_16x16x32_bf16 v[4:7], v[228:231], v[240:243], v[4:7]\n\t" \
    "ds_read_b128 v[228:231], v12 offset:27648\n\t" \
    "s_waitcnt lgkmcnt(4)\n\t" \
    "v_mfma_f32_16x16x32_bf16 v[232:235], v[212:215], v[240:243], v[232:235]\n\t" \
    "ds_read_b128 v[212:215], v12 offset:31744\n\t" \
    "s_waitcnt lgkmcnt(4)\n\t" \
    "v_mfma_f32_16x16x32_bf16 v[0:3], v[216:219], v[244:247], v[0:3]\n\t" \
    "ds_read_b128 v[216:219], v15 offset:1536\n\t" \
    "s_waitcnt lgkmcnt(4)\n\t" \
    "v_mfma_f32_16x16x32_bf16 v[4:7], v[220:223], v[244:247], v[4:7]\n\t" \
    "s_waitcnt lgkmcnt(3)\n\t" \
    "v_mfma_f32_16x16x32_bf16 v[232:235], v[224:227], v[244:247], v[232:235]\n\t" \
    "s_waitcnt lgkmcnt(2)\n\t" \
    "v_mfma_f32_16x16x32_bf16 v[0:3], v[228:231], v[248:251], v[0:3]\n\t" \
    "s_waitcnt lgkmcnt(1)\n\t" \
    "v_mfma_f32_16x16x32_bf16 v[4:7], v[212:215], v[248:251], v[4:7]\n\t" \
    "s_waitcnt lgkmcnt(0)\n\t" \
    "v_mfma_f32_16x16x32_bf16 v[232:235], v[216:219], v[248:251], v[232:235]\n\t" \
    "s_barrier\n\t" \
    GSA(4) \
    "ds_read_b128 v[236:239], v253 offset:8192\n\t" \
    "ds_read_b128 v[240:243], v253 offset:9216\n\t" \
    "ds_read_b128 v[244:247], v253 offset:10240\n\t" \
    "ds_read_b128 v[248:251], v253 offset:11264\n\t" \
    "ds_read2st64_b32 v[228:229], v17 offset0:0 offset1:1\n\t" \
    "ds_read2st64_b32 v[230:231], v17 offset0:2 offset1:3\n\t" \
    "s_mov_b64 exec, %[fm]\n\t" \
    "v_mov_b32 v220, %[st]\n\t" \
    "v_mov_b32 v221, %[tag]\n\t" \
    "v_mov_b32 v222, 0\n\t" \
    "buffer_store_dwordx2 v[220:221], v222, %[rs], %[po] offen sc1\n\t" \
    "s_mov_b64 exec, -1\n\t" \
    "v_mov_b32 v254, 0xff800000\n\t" \
    "v_cvt_pk_bf16_f32 v0, 0, v0\n\t" \
    "v_cvt_pk_bf16_f32 v1, 0, v1\n\t" \
    "v_cvt_pk_bf16_f32 v2, 0, v2\n\t" \
    "v_cvt_pk_bf16_f32 v3, 0, v3\n\t" \
    "v_cvt_pk_bf16_f32 v4, 0, v4\n\t" \
    "v_cvt_pk_bf16_f32 v5, 0, v5\n\t" \
    "v_cvt_pk_bf16_f32 v6, 0, v6\n\t" \
    "v_cvt_pk_bf16_f32 v7, 0, v7\n\t" \
    "v_cvt_pk_bf16_f32 v232, 0, v232\n\t" \
    "v_cvt_pk_bf16_f32 v233, 0, v233\n\t" \
    "v_mul_f32 v0, 0x3db504f3, v0\n\t" \
    "v_mul_f32 v1, 0x3db504f3, v1\n\t" \
    "v_mul_f32 v2, 0x3db504f3, v2\n\t" \
    "v_mul_f32 v3, 0x3db504f3, v3\n\t" \
    "v_mul_f32 v4, 0x3db504f3, v4\n\t" \
    "v_mul_f32 v5, 0x3db504f3, v5\n\t" \
    "v_mul_f32 v6, 0x3db504f3, v6\n\t" \
    "v_mul_f32 v7, 0x3db504f3, v7\n\t" \
    "v_mul_f32 v232, 0x3db504f3, v232\n\t" \
    "v_mul_f32 v233, 0x3db504f3, v233\n\t" \
    "v_cvt_pk_bf16_f32 v0, 0, v0\n\t" \
    "v_cvt_pk_bf16_f32 v1, 0, v1\n\t" \
    "v_cvt_pk_bf16_f32 v2, 0, v2\n\t" \
    "v_cvt_pk_bf16_f32 v3, 0, v3\n\t" \
    "v_cvt_pk_bf16_f32 v4, 0, v4\n\t" \
    "v_cvt_pk_bf16_f32 v5, 0, v5\n\t" \
    "v_cvt_pk_bf16_f32 v6, 0, v6\n\t" \
    "v_cvt_pk_bf16_f32 v7, 0, v7\n\t" \
    "v_cvt_pk_bf16_f32 v232, 0, v232\n\t" \
    "v_cvt_pk_bf16_f32 v233, 0, v233\n\t" \
    "v_cndmask_b32 v0, v254, v0, s[72:73]\n\t" \
    "v_cndmask_b32 v1, v254, v1, s[74:75]\n\t" \
    "v_cndmask_b32 v2, v254, v2, s[76:77]\n\t" \
    "v_cndmask_b32 v3, v254, v3, s[78:79]\n\t" \
    "v_cndmask_b32 v4, v254, v4, s[80:81]\n\t" \
    "v_cndmask_b32 v5, v254, v5, s[82:83]\n\t" \
    "v_cndmask_b32 v6, v254, v6, s[84:85]\n\t" \
    "v_cndmask_b32 v7, v254, v7, s[86:87]\n\t" \
    "v_max3_f32 v252, v0, v1, v2\n\t" \
    "v_max3_f32 v255, v3, v4, v5\n\t" \
    "v_max3_f32 v254, v6, v7, v232\n\t" \
    "v_max3_f32 v252, v252, v255, v254\n\t" \
    "v_max_f32 v252, v252, v233\n\t" \
    "v_mov_b32 v255, v252\n\t" \
    "v_mov_b32 v254, v252\n\t" \
    "s_nop 1\n\t" \
    "v_permlane16_swap_b32 v255, v254\n\t" \
    "v_max_f32 v252, v255, v254\n\t" \
    "v_max_f32 v255, v255, v254\n\t" \
    "s_nop 1\n\t" \
    "v_permlane32_swap_b32 v252, v255\n\t" \
    "v_max_f32 v252, v252, v255\n\t" \
    "v_mul_f32 v252, 0xbfb8aa3b, v252\n\t" \
    "v_fma_f32 v0, v0, %[l2e], v252\n\t" \
    "v_fma_f32 v1, v1, %[l2e], v252\n\t" \
    "v_fma_f32 v2, v2, %[l2e], v252\n\t" \
    "v_fma_f32 v3, v3, %[l2e], v252\n\t" \
    "v_fma_f32 v4, v4, %[l2e], v252\n\t" \
    "v_fma_f32 v5, v5, %[l2e], v252\n\t" \
    "v_fma_f32 v6, v6, %[l2e], v252\n\t" \
    "v_fma_f32 v7, v7, %[l2e], v252\n\t" \
    "v_fma_f32 v232, v232, %[l2e], v252\n\t" \
    "v_fma_f32 v233, v233, %[l2e], v252\n\t" \
    "v_exp_f32 v0, v0\n\t" \
    "v_exp_f32 v1, v1\n\t" \
    "v_exp_f32 v2, v2\n\t" \
    "v_exp_f32 v3, v3\n\t" \
    "v_exp_f32 v4, v4\n\t" \
    "v_exp_f32 v5, v5\n\t" \
    "v_exp_f32 v6, v6\n\t" \
    "v_exp_f32 v7, v7\n\t" \
    "v_exp_f32 v232, v232\n\t" \
    "v_exp_f32 v233, v233\n\t" \
    "v_add_f32 v252, v0, v1\n\t" \
    "v_add_f32 v235, v2, v3\n\t" \
    "v_add_f32 v254, v4, v5\n\t" \
    "v_add_f32 v255, v6, v7\n\t" \
    "v_add_f32 v234, v232, v233\n\t" \
    "v_add_f32 v252, v252, v235\n\t" \
    "v_add_f32 v254, v254, v255\n\t" \
    "v_add_f32 v252, v252, v254\n\t" \
    "v_add_f32 v252, v252, v234\n\t" \
    "v_mov_b32 v255, v252\n\t" \
    "v_mov_b32 v254, v252\n\t" \
    "s_nop 1\n\t" \
    "v_permlane16_swap_b32 v255, v254\n\t" \
    "v_add_f32 v252, v255, v254\n\t" \
    "v_add_f32 v255, v255, v254\n\t" \
    "s_nop 1\n\t" \
    "v_permlane32_swap_b32 v252, v255\n\t" \
    "v_add_f32 v252, v252, v255\n\t" \
    "v_rcp_f32 v252, v252\n\t" \
    "s_nop 0\n\t" \
    "v_mul_f32 v0, v0, v252\n\t" \
    "v_mul_f32 v1, v1, v252\n\t" \
    "v_mul_f32 v2, v2, v252\n\t" \
    "v_mul_f32 v3, v3, v252\n\t" \
    "v_mul_f32 v4, v4, v252\n\t" \
    "v_mul_f32 v5, v5, v252\n\t" \
    "v_mul_f32 v6, v6, v252\n\t" \
    "v_mul_f32 v7, v7, v252\n\t" \
    "v_mul_f32 v232, v232, v252\n\t" \
    "v_mul_f32 v233, v233, v252\n\t" \
    "v_cvt_pk_bf16_f32 v0, v0, v1\n\t" \
    "v_cvt_pk_bf16_f32 v1, v2, v3\n\t" \
    "v_cvt_pk_bf16_f32 v2, v4, v5\n\t" \
    "v_cvt_pk_bf16_f32 v3, v6, v7\n\t" \
    "v_cvt_pk_bf16_f32 v4, v232, v233\n\t" \
    "v_mov_b32 v5, 0\n\t" \
    "v_mov_b32 v6, 0\n\t" \
    "v_cvt_pk_bf16_f32 v7, v232, v233\n\t" \
    "s_waitcnt lgkmcnt(0)\n\t" \
    "v_mfma_f32_16x16x32_bf16 v[236:239], v[236:239], v[0:3], 0\n\t" \
    "v_mfma_f32_16x16x32_bf16 v[240:243], v[240:243], v[0:3], 0\n\t" \
    "v_mfma_f32_16x16x32_bf16 v[244:247], v[244:247], v[0:3], 0\n\t" \
    "v_mfma_f32_16x16x32_bf16 v[248:251], v[248:251], v[0:3], 0\n\t" \
    "v_mfma_f32_16x16x16_bf16 v[236:239], v[228:229], v[4:5], v[236:239]\n\t" \
    "v_mfma_f32_16x16x16_bf16 v[240:243], v[228:229], v[6:7], v[240:243]\n\t" \
    "v_mfma_f32_16x16x16_bf16 v[244:247], v[230:231], v[4:5], v[244:247]\n\t" \
    "v_mfma_f32_16x16x16_bf16 v[248:251], v[230:231], v[6:7], v[248:251]\n\t" \
    "s_nop 4\n\t" \
    "v_cvt_pk_bf16_f32 v212, v236, v237\n\t" \
    "v_cvt_pk_bf16_f32 v213, v240, v241\n\t" \
    "v_cvt_pk_bf16_f32 v214, v238, v239\n\t" \
    "v_cvt_pk_bf16_f32 v215, v242, v243\n\t" \
    "v_permlane16_swap_b32 v212, v213\n\t" \
    "s_nop 0\n\t" \
    "v_permlane16_swap_b32 v214, v215\n\t" \
    "v_cvt_pk_bf16_f32 v216, v244, v245\n\t" \
    "v_cvt_pk_bf16_f32 v217, v248, v249\n\t" \
    "v_cvt_pk_bf16_f32 v218, v246, v247\n\t" \
    "v_cvt_pk_bf16_f32 v219, v250, v251\n\t" \
    "v_permlane16_swap_b32 v216, v217\n\t" \
    "s_nop 0\n\t" \
    "v_permlane16_swap_b32 v218, v219\n\t" \
    "s_setprio 1\n\t" \
    GSA(2) \
    GSW \
    "s_nop 1\n\t" \
    : [lim] "+s"(lim), "+{v[212:219]}"(oxr) DGO GSO(0) GSO(1) GSO(2) GSO(3) GSO(4) \
    : "{v8}"(am_a), "{v9}"(t0_a), "{v10}"(idx), "{v11}"(iw_a), "{v12}"(ab_a), "{v14}"(xo_v), "{v15}"(kr_a), "{v16}"(dq), "{v17}"(vl_a), "{v18}"(fr_a), [stag] "s"(0xffffff81u + s), [onej] "s"(onej), [rs] "s"(rs), [m0] "s"(sbm + 16384u), [m1] "s"(sbm + 20480u), [st] "s"(s), [tag] "s"(P.tag), [po] "s"(FLG_OFF + F_PRG(4, h) * 128), [fm] "s"(fmask), [qx] "s"(qxo), [qd] "s"(lbase + LA_VM), [qh] "s"((j & 1) * 2048), [ox] "s"(oxo), [se] "s"((j & 1) * 4096), [l2e] "s"(0x3fb8aa3b), "{v[20:27]}"(wr[0][0]), "{v[28:35]}"(wr[0][1]), "{v[36:43]}"(wr[1][0]), "{v[44:51]}"(wr[1][1]), "{v[52:59]}"(wr[2][0]), "{v[60:67]}"(wr[2][1]), "{v[68:75]}"(wr[3][0]), "{v[76:83]}"(wr[3][1]), "{v[84:91]}"(wr[4][0]), "{v[92:99]}"(wr[4][1]), "{v[100:107]}"(wr[5][0]), "{v[108:115]}"(wr[5][1]), "{v[116:123]}"(wr[6][0]), "{v[124:131]}"(wr[6][1]), "{v[132:139]}"(wr[7][0]), "{v[140:147]}"(wr[7][1]), "{v[148:155]}"(wr[8][0]), "{v[156:163]}"(wr[8][1]), "{v[164:171]}"(wr[9][0]), "{v[172:179]}"(wr[9][1]), "{v[180:187]}"(wr[10][0]), "{v[188:195]}"(wr[10][1]), "{v[196:203]}"(wr[11][0]), "{v[204:211]}"(wr[11][1]) \
    : "memory", "scc", "vcc", "s90", "s91", "s72", "s73", "s74", "s75", "s76", "s77", "s78", "s79", "s80", "s81", "s82", "s83", "s84", "s85", "s86", "s87", "v0", "v1", "v2", "v3", "v4", "v5", "v6", "v7", "v220", "v221", "v222", "v223", "v224", "v225", "v226", "v227", "v228", "v229", "v230", "v231", "v232", "v233", "v234", "v235", "v236", "v237", "v238", "v239", "v240", "v241", "v242", "v243", "v244", "v245", "v246", "v247", "v248", "v249", "v250", "v251", "v252", "v253", "v254", "v255")

#define B_POLL() \
  asm volatile( \
    "TWB_%=:\n\t" \
    "s_memrealtime s[88:89]\n\t" \
    "s_memtime s[90:91]\n\t" \
    "s_waitcnt lgkmcnt(0)\n\t" \
    "s_lshl_b32 s88, s88, 2\n\t" \
    "s_sub_u32 s89, %[tgt], s88\n\t" \
    "s_cmp_gt_i32 s89, 16\n\t" \
    "s_cbranch_scc1 TWB_%=\n\t" \
    "s_mul_i32 s89, s89, 11\n\t" \
    "s_ashr_i32 s89, s89, 1\n\t" \
    "s_add_u32 s89, s89, s90\n\t" \
    "TCB_%=:\n\t" \
    "s_memtime s[92:93]\n\t" \
    "s_waitcnt lgkmcnt(0)\n\t" \
    "s_sub_u32 s93, s89, s92\n\t" \
    "s_cmp_gt_i32 s93, 0\n\t" \
    "s_cbranch_scc1 TCB_%=\n\t" \
    "buffer_load_dwordx4 v[220:223], %[l16], %[rs], %[m0] offen offset:0 sc1\n\t" \
    "buffer_load_dwordx4 v[224:227], %[l16], %[rs], %[m0] offen offset:1024 sc1\n\t" \
    "buffer_load_dwordx4 v[228:231], %[l16], %[rs], %[m1] offen offset:0 sc1\n\t" \
    "buffer_load_dwordx4 v[232:235], %[l16], %[rs], %[m1] offen offset:1024 sc1\n\t" \
    "v_mov_b32 v252, v252\n\t" \
    "s_nop 2\n\t" \
    "v_mov_b32 v252, v252\n\t" \
    "s_nop 2\n\t" \
    "v_mov_b32 v252, v252\n\t" \
    "s_nop 2\n\t" \
    "v_mov_b32 v252, v252\n\t" \
    "s_nop 2\n\t" \
    "v_mov_b32 v252, v252\n\t" \
    "s_nop 2\n\t" \
    "v_mov_b32 v252, v252\n\t" \
    "s_nop 2\n\t" \
    "v_mov_b32 v252, v252\n\t" \
    "s_nop 2\n\t" \
    "v_mov_b32 v252, v252\n\t" \
    "s_nop 2\n\t" \
    "v_mov_b32 v252, v252\n\t" \
    "s_nop 2\n\t" \
    "v_mov_b32 v252, v252\n\t" \
    "s_nop 2\n\t" \
    "v_mov_b32 v252, v252\n\t" \
    "s_nop 2\n\t" \
    "v_mov_b32 v252, v252\n\t" \
    "s_nop 2\n\t" \
    "v_mov_b32 v252, v252\n\t" \
    "s_nop 2\n\t" \
    "v_mov_b32 v252, v252\n\t" \
    "s_nop 2\n\t" \
    "v_mov_b32 v252, v252\n\t" \
    "s_nop 2\n\t" \
    "v_mov_b32 v252, v252\n\t" \
    "s_nop 2\n\t" \
    "v_mov_b32 v252, v252\n\t" \
    "s_nop 2\n\t" \
    "v_mov_b32 v252, v252\n\t" \
    "s_nop 2\n\t" \
    "v_mov_b32 v252, v252\n\t" \
    "s_nop 2\n\t" \
    "v_mov_b32 v252, v252\n\t" \
    "s_nop 2\n\t" \
    "v_mov_b32 v252, v252\n\t" \
    "s_nop 2\n\t" \
    "v_mov_b32 v252, v252\n\t" \
    "s_nop 2\n\t" \
    "v_mov_b32 v252, v252\n\t" \
    "s_nop 2\n\t" \
    "v_mov_b32 v252, v252\n\t" \
    "s_nop 2\n\t" \
    "PM_%=:\n\t" \
    "s_waitcnt vmcnt(3)\n\t" \
    "ds_write_b128 %[la], v[220:223] offset:0\n\t" \
    "v_max3_u32 v252, v220, v220, v221\n\t" \
    "v_max3_u32 v252, v252, v222, v223\n\t" \
    "s_waitcnt vmcnt(2)\n\t" \
    "ds_write_b128 %[la], v[224:227] offset:1024\n\t" \
    "v_max3_u32 v252, v252, v224, v225\n\t" \
    "v_max3_u32 v252, v252, v226, v227\n\t" \
    "s_waitcnt vmcnt(1)\n\t" \
    "ds_write_b128 %[la], v[228:231] offset:8192\n\t" \
    "v_max3_u32 v252, v252, v228, v229\n\t" \
    "v_max3_u32 v252, v252, v230, v231\n\t" \
    "s_waitcnt vmcnt(0)\n\t" \
    "ds_write_b128 %[la], v[232:235] offset:9216\n\t" \
    "v_max3_u32 v252, v252, v232, v233\n\t" \
    "v_max3_u32 v252, v252, v234, v235\n\t" \
    "v_cmp_eq_u32 vcc, -1, v252\n\t" \
    "s_cbranch_vccz PD_%=\n\t" \
    "s_sub_u32 %[lim], %[lim], 1\n\t" \
    "s_cmp_gt_i32 %[lim], 0\n\t" \
    "s_cbranch_scc0 PD_%=\n\t" \
    "s_sleep " STR(CADM) "\n\t" \
    "buffer_load_dwordx4 v[220:223], %[l16], %[rs], %[m0] offen offset:0 sc1\n\t" \
    "buffer_load_dwordx4 v[224:227], %[l16], %[rs], %[m0] offen offset:1024 sc1\n\t" \
    "buffer_load_dwordx4 v[228:231], %[l16], %[rs], %[m1] offen offset:0 sc1\n\t" \
    "buffer_load_dwordx4 v[232:235], %[l16], %[rs], %[m1] offen offset:1024 sc1\n\t" \
    "s_branch PM_%=\n\t" \
    "PD_%=:\n\t" \
    GSA(3) \
    "s_waitcnt lgkmcnt(0)\n\t" \
    "s_barrier\n\t" \
    : [lim] "+s"(lim) GSO(3) \
    : [l16] "v"(l16), [la] "v"(la), [rs] "s"(rs), [m0] "s"(mo), [m1] "s"(mo + 16384u), [tgt] "s"(tgt) \
    : "memory", "vcc", "scc", "v252", "s88", "s89", "s90", "s91", "s92", "s93", "v220", "v221", "v222", "v223", "v224", "v225", "v226", "v227", "v228", "v229", "v230", "v231", "v232", "v233", "v234", "v235")

#define T_WAIT() \
  asm volatile( \
    "TWC_%=:\n\t" \
    "s_memrealtime s[88:89]\n\t" \
    "s_memtime s[90:91]\n\t" \
    "s_waitcnt lgkmcnt(0)\n\t" \
    "s_lshl_b32 s88, s88, 2\n\t" \
    "s_sub_u32 s89, %[tgt], s88\n\t" \
    "s_cmp_gt_i32 s89, 16\n\t" \
    "s_cbranch_scc1 TWC_%=\n\t" \
    "s_mul_i32 s89, s89, 11\n\t" \
    "s_ashr_i32 s89, s89, 1\n\t" \
    "s_add_u32 s89, s89, s90\n\t" \
    "TCC_%=:\n\t" \
    "s_memtime s[92:93]\n\t" \
    "s_waitcnt lgkmcnt(0)\n\t" \
    "s_sub_u32 s93, s89, s92\n\t" \
    "s_cmp_gt_i32 s93, 0\n\t" \
    "s_cbranch_scc1 TCC_%=\n\t" \
    :  \
    : [tgt] "s"(tgt) \
    : "s88", "s89", "s90", "s91", "s92", "s93", "memory", "scc")

#define V_PROJ() \
  asm volatile( \
    "v_mov_b32 v0, 0\n\t" \
    "v_mov_b32 v1, 0\n\t" \
    "v_mov_b32 v2, 0\n\t" \
    "v_mov_b32 v3, 0\n\t" \
    "v_mov_b32 v4, 0\n\t" \
    "v_mov_b32 v5, 0\n\t" \
    "v_mov_b32 v6, 0\n\t" \
    "v_mov_b32 v7, 0\n\t" \
    "s_barrier\n\t" \
    "ds_read_b128 v[220:223], %[am] offset:0\n\t" \
    "ds_read_b128 v[224:227], %[am] offset:1024\n\t" \
    "ds_read_b128 v[228:231], %[am] offset:2048\n\t" \
    "ds_read_b128 v[232:235], %[am] offset:3072\n\t" \
    "ds_read_b128 v[236:239], %[am] offset:4096\n\t" \
    "ds_read_b128 v[240:243], %[am] offset:5120\n\t" \
    "ds_read_b128 v[244:247], %[am] offset:6144\n\t" \
    "ds_read_b128 v[248:251], %[am] offset:7168\n\t" \
    "s_waitcnt lgkmcnt(7)\n\t" \
    "v_smfmac_f32_16x16x64_bf16 v[0:3], v[220:223], v[28:35], %[idx]\n\t" \
    "ds_read_b128 v[220:223], %[am] offset:8192\n\t" \
    "s_waitcnt lgkmcnt(7)\n\t" \
    "v_smfmac_f32_16x16x64_bf16 v[4:7], v[224:227], v[36:43], %[idx]\n\t" \
    "ds_read_b128 v[224:227], %[am] offset:9216\n\t" \
    "s_waitcnt lgkmcnt(7)\n\t" \
    "v_smfmac_f32_16x16x64_bf16 v[0:3], v[228:231], v[44:51], %[idx]\n\t" \
    "ds_read_b128 v[228:231], %[am] offset:10240\n\t" \
    "s_waitcnt lgkmcnt(7)\n\t" \
    "v_smfmac_f32_16x16x64_bf16 v[4:7], v[232:235], v[52:59], %[idx]\n\t" \
    "ds_read_b128 v[232:235], %[am] offset:11264\n\t" \
    "s_waitcnt lgkmcnt(7)\n\t" \
    "v_smfmac_f32_16x16x64_bf16 v[0:3], v[236:239], v[60:67], %[idx]\n\t" \
    "ds_read_b128 v[236:239], %[am] offset:12288\n\t" \
    "s_waitcnt lgkmcnt(7)\n\t" \
    "v_smfmac_f32_16x16x64_bf16 v[4:7], v[240:243], v[68:75], %[idx]\n\t" \
    "ds_read_b128 v[240:243], %[am] offset:13312\n\t" \
    "s_waitcnt lgkmcnt(7)\n\t" \
    "v_smfmac_f32_16x16x64_bf16 v[0:3], v[244:247], v[76:83], %[idx]\n\t" \
    "ds_read_b128 v[244:247], %[am] offset:14336\n\t" \
    "s_waitcnt lgkmcnt(7)\n\t" \
    "v_smfmac_f32_16x16x64_bf16 v[4:7], v[248:251], v[84:91], %[idx]\n\t" \
    "ds_read_b128 v[248:251], %[am] offset:15360\n\t" \
    "s_waitcnt lgkmcnt(7)\n\t" \
    "v_smfmac_f32_16x16x64_bf16 v[0:3], v[220:223], v[92:99], %[idx]\n\t" \
    "s_waitcnt lgkmcnt(6)\n\t" \
    "v_smfmac_f32_16x16x64_bf16 v[4:7], v[224:227], v[100:107], %[idx]\n\t" \
    "s_waitcnt lgkmcnt(5)\n\t" \
    "v_smfmac_f32_16x16x64_bf16 v[0:3], v[228:231], v[108:115], %[idx]\n\t" \
    "s_waitcnt lgkmcnt(4)\n\t" \
    "v_smfmac_f32_16x16x64_bf16 v[4:7], v[232:235], v[116:123], %[idx]\n\t" \
    "s_waitcnt lgkmcnt(3)\n\t" \
    "v_smfmac_f32_16x16x64_bf16 v[0:3], v[236:239], v[124:131], %[idx]\n\t" \
    "s_waitcnt lgkmcnt(2)\n\t" \
    "v_smfmac_f32_16x16x64_bf16 v[4:7], v[240:243], v[132:139], %[idx]\n\t" \
    "s_waitcnt lgkmcnt(1)\n\t" \
    "v_smfmac_f32_16x16x64_bf16 v[0:3], v[244:247], v[140:147], %[idx]\n\t" \
    "s_waitcnt lgkmcnt(0)\n\t" \
    "v_smfmac_f32_16x16x64_bf16 v[4:7], v[248:251], v[148:155], %[idx]\n\t" \
    "s_nop 5\n\t" \
    "v_add_f32 v0, v0, v1\n\t" \
    "v_add_f32 v2, v2, v3\n\t" \
    "v_add_f32 v4, v4, v5\n\t" \
    "v_add_f32 v6, v6, v7\n\t" \
    "v_add_f32 v0, v0, v4\n\t" \
    "v_add_f32 v2, v2, v6\n\t" \
    "v_cvt_pk_bf16_f32 v0, v0, v2\n\t" \
    "v_min_u32 v0, -2, v0\n\t" \
    "s_nop 1\n\t" \
    "v_mov_b32_dpp v1, v0 quad_perm:[1,1,1,1] row_mask:0xf bank_mask:0xf\n\t" \
    "v_mov_b32_dpp v2, v0 quad_perm:[2,2,2,2] row_mask:0xf bank_mask:0xf\n\t" \
    "v_mov_b32_dpp v3, v0 quad_perm:[3,3,3,3] row_mask:0xf bank_mask:0xf\n\t" \
    "s_mov_b64 exec, %[qm]\n\t" \
    "buffer_store_dwordx4 v[0:3], %[l4], %[rs], %[vo] offen sc1\n\t" \
    "s_mov_b64 exec, -1\n\t" \
    "s_nop 1\n\t" \
    :  \
    : [am] "v"(am_a), [idx] "v"(idx), [l4] "v"(ln * 4), [rs] "s"(rs), [vo] "s"(sb + vo), [qm] "s"(0x1111111111111111ull), "{v[28:35]}"(wv[0]), "{v[36:43]}"(wv[1]), "{v[44:51]}"(wv[2]), "{v[52:59]}"(wv[3]), "{v[60:67]}"(wv[4]), "{v[68:75]}"(wv[5]), "{v[76:83]}"(wv[6]), "{v[84:91]}"(wv[7]), "{v[92:99]}"(wv[8]), "{v[100:107]}"(wv[9]), "{v[108:115]}"(wv[10]), "{v[116:123]}"(wv[11]), "{v[124:131]}"(wv[12]), "{v[132:139]}"(wv[13]), "{v[140:147]}"(wv[14]), "{v[148:155]}"(wv[15]) \
    : "memory", "v0", "v1", "v2", "v3", "v4", "v5", "v6", "v7", "v220", "v221", "v222", "v223", "v224", "v225", "v226", "v227", "v228", "v229", "v230", "v231", "v232", "v233", "v234", "v235", "v236", "v237", "v238", "v239", "v240", "v241", "v242", "v243", "v244", "v245", "v246", "v247", "v248", "v249", "v250", "v251")

#define WO_V() \
  asm volatile( \
    "v_mov_b32 v0, 0\n\t" \
    "v_mov_b32 v1, 0\n\t" \
    "v_mov_b32 v2, 0\n\t" \
    "v_mov_b32 v3, 0\n\t" \
    "v_mov_b32 v4, 0\n\t" \
    "v_mov_b32 v5, 0\n\t" \
    "v_mov_b32 v6, 0\n\t" \
    "v_mov_b32 v7, 0\n\t" \
    "v_mov_b32 v8, 0\n\t" \
    "v_mov_b32 v9, 0\n\t" \
    "v_mov_b32 v10, 0\n\t" \
    "v_mov_b32 v11, 0\n\t" \
    "v_mov_b32 v12, 0\n\t" \
    "v_mov_b32 v13, 0\n\t" \
    "v_mov_b32 v14, 0\n\t" \
    "v_mov_b32 v15, 0\n\t" \
    "v_mov_b32 v252, 0\n\t" \
    "s_barrier\n\t" \
    GSA(4) \
    DGC \
    "buffer_load_dwordx4 v[220:223], %[l16], %[rs], %[s0] offen sc1\n\t" \
    "buffer_load_dwordx4 v[224:227], %[l16], %[rs], %[s1] offen sc1\n\t" \
    "A_%=:\n\t" \
    "s_waitcnt vmcnt(1)\n\t" \
    "v_smfmac_f32_16x16x64_bf16 v[0:3], v[220:223], v[156:163], %[idx]\n\t" \
    "v_max3_u32 v252, v252, v220, v221\n\t" \
    "v_smfmac_f32_16x16x64_bf16 v[4:7], v[220:223], v[164:171], %[idx]\n\t" \
    "v_max3_u32 v252, v252, v222, v223\n\t" \
    "v_smfmac_f32_16x16x64_bf16 v[8:11], v[220:223], v[172:179], %[idx]\n\t" \
    "v_smfmac_f32_16x16x64_bf16 v[12:15], v[220:223], v[180:187], %[idx]\n\t" \
    "s_waitcnt vmcnt(0)\n\t" \
    "v_smfmac_f32_16x16x64_bf16 v[0:3], v[224:227], v[188:195], %[idx]\n\t" \
    "v_max3_u32 v252, v252, v224, v225\n\t" \
    "v_smfmac_f32_16x16x64_bf16 v[4:7], v[224:227], v[196:203], %[idx]\n\t" \
    "v_max3_u32 v252, v252, v226, v227\n\t" \
    "v_smfmac_f32_16x16x64_bf16 v[8:11], v[224:227], v[204:211], %[idx]\n\t" \
    "v_smfmac_f32_16x16x64_bf16 v[12:15], v[224:227], v[212:219], %[idx]\n\t" \
    "v_cmp_eq_u32 vcc, -1, v252\n\t" \
    "s_cbranch_vccnz SL_%=\n\t" \
    "D_%=:\n\t" \
    GSA(1) \
    "s_nop 2\n\t" \
    "v_add_f32 v0, v0, v1\n\t" \
    "v_add_f32 v2, v2, v3\n\t" \
    "v_add_f32 v4, v4, v5\n\t" \
    "v_add_f32 v6, v6, v7\n\t" \
    "v_add_f32 v8, v8, v9\n\t" \
    "v_add_f32 v10, v10, v11\n\t" \
    "v_add_f32 v12, v12, v13\n\t" \
    "v_add_f32 v14, v14, v15\n\t" \
    "v_permlane32_swap_b32 v0, v4\n\t" \
    "v_permlane32_swap_b32 v2, v6\n\t" \
    "v_permlane32_swap_b32 v8, v12\n\t" \
    "v_permlane32_swap_b32 v10, v14\n\t" \
    "v_add_f32 v0, v0, v12\n\t" \
    "v_add_f32 v2, v2, v14\n\t" \
    "ds_write2_b32 %[rw], v0, v2 offset0:0 offset1:32\n\t" \
    "s_waitcnt lgkmcnt(0)\n\t" \
    "s_barrier\n\t" \
    "s_cmp_eq_u32 %[red], 0\n\t" \
    "s_cbranch_scc0 E_%=\n\t" \
    "ds_read2st64_b64 v[0:3], %[rr] offset0:0 offset1:1\n\t" \
    "ds_read2st64_b64 v[4:7], %[rr] offset0:2 offset1:3\n\t" \
    "ds_read2st64_b64 v[8:11], %[rr] offset0:4 offset1:5\n\t" \
    "ds_read2st64_b64 v[12:15], %[rr] offset0:6 offset1:7\n\t" \
    "s_waitcnt lgkmcnt(3)\n\t" \
    "v_pk_add_f32 v[0:1], v[0:1], v[2:3]\n\t" \
    "s_waitcnt lgkmcnt(2)\n\t" \
    "v_pk_add_f32 v[4:5], v[4:5], v[6:7]\n\t" \
    "s_waitcnt lgkmcnt(1)\n\t" \
    "v_pk_add_f32 v[8:9], v[8:9], v[10:11]\n\t" \
    "v_pk_add_f32 v[0:1], v[0:1], v[4:5]\n\t" \
    "s_waitcnt lgkmcnt(0)\n\t" \
    "v_pk_add_f32 v[12:13], v[12:13], v[14:15]\n\t" \
    "s_nop 0\n\t" \
    "v_pk_add_f32 v[8:9], v[8:9], v[12:13]\n\t" \
    "s_nop 0\n\t" \
    "v_pk_add_f32 v[0:1], v[0:1], v[8:9]\n\t" \
    "s_nop 0\n\t" \
    "v_cvt_pk_bf16_f32 v0, v0, v1\n\t" \
    "v_min_u32 v0, -2, v0\n\t" \
    "buffer_store_dword v0, %[mo], %[rs], %[sn] offen sc1\n\t" \
    "s_memrealtime %[t1]\n\t" \
    DGA \
    "s_waitcnt lgkmcnt(0)\n\t" \
    GSA(2) \
    "s_branch E_%=\n\t" \
    "SL_%=:\n\t" \
    "s_sleep 1\n\t" \
    "buffer_load_dwordx4 v[220:223], %[l16], %[rs], %[s0] offen sc1\n\t" \
    "buffer_load_dwordx4 v[224:227], %[l16], %[rs], %[s1] offen sc1\n\t" \
    "s_sub_u32 %[lim], %[lim], " STR(DEC2) "\n\t" \
    "s_cmp_gt_i32 %[lim], 0\n\t" \
    "s_cbranch_scc0 G_%=\n\t" \
    "s_nop 3\n\t" \
    "v_mov_b32 v0, 0\n\t" \
    "v_mov_b32 v1, 0\n\t" \
    "v_mov_b32 v2, 0\n\t" \
    "v_mov_b32 v3, 0\n\t" \
    "v_mov_b32 v4, 0\n\t" \
    "v_mov_b32 v5, 0\n\t" \
    "v_mov_b32 v6, 0\n\t" \
    "v_mov_b32 v7, 0\n\t" \
    "v_mov_b32 v8, 0\n\t" \
    "v_mov_b32 v9, 0\n\t" \
    "v_mov_b32 v10, 0\n\t" \
    "v_mov_b32 v11, 0\n\t" \
    "v_mov_b32 v12, 0\n\t" \
    "v_mov_b32 v13, 0\n\t" \
    "v_mov_b32 v14, 0\n\t" \
    "v_mov_b32 v15, 0\n\t" \
    "v_mov_b32 v252, 0\n\t" \
    "s_branch A_%=\n\t" \
    "G_%=:\n\t" \
    "s_waitcnt vmcnt(0)\n\t" \
    "s_branch D_%=\n\t" \
    "E_%=:\n\t" \
    GSW \
    : [lim] "+s"(lim) GSO(1) GSO(4), [t1] "=&s"(t1) DGO DGQ GSO(2) \
    : [l16] "v"(l16), [rs] "s"(rs), [idx] "v"(idx), [rw] "v"(rw_a), [s0] "s"(so[0]), [s1] "s"(so[1]), [rr] "v"(rr_a), [mo] "v"(mo_v), [sn] "s"(sn), [red] "s"(wid), "{v[156:163]}"(wo[0][0][0]), "{v[164:171]}"(wo[0][0][1]), "{v[172:179]}"(wo[0][1][0]), "{v[180:187]}"(wo[0][1][1]), "{v[188:195]}"(wo[1][0][0]), "{v[196:203]}"(wo[1][0][1]), "{v[204:211]}"(wo[1][1][0]), "{v[212:219]}"(wo[1][1][1]) \
    : "memory", "vcc", "scc", "v252", "v0", "v1", "v2", "v3", "v4", "v5", "v6", "v7", "v8", "v9", "v10", "v11", "v12", "v13", "v14", "v15", "v220", "v221", "v222", "v223", "v224", "v225", "v226", "v227")

#define WO_W() \
  asm volatile( \
    "v_mov_b32 v0, 0\n\t" \
    "v_mov_b32 v1, 0\n\t" \
    "v_mov_b32 v2, 0\n\t" \
    "v_mov_b32 v3, 0\n\t" \
    "v_mov_b32 v4, 0\n\t" \
    "v_mov_b32 v5, 0\n\t" \
    "v_mov_b32 v6, 0\n\t" \
    "v_mov_b32 v7, 0\n\t" \
    "v_mov_b32 v8, 0\n\t" \
    "v_mov_b32 v9, 0\n\t" \
    "v_mov_b32 v10, 0\n\t" \
    "v_mov_b32 v11, 0\n\t" \
    "v_mov_b32 v12, 0\n\t" \
    "v_mov_b32 v13, 0\n\t" \
    "v_mov_b32 v14, 0\n\t" \
    "v_mov_b32 v15, 0\n\t" \
    "v_mov_b32 v252, 0\n\t" \
    "s_barrier\n\t" \
    GSA(4) \
    "buffer_load_dwordx4 v[220:223], %[l16], %[rs], %[s0] offen sc1\n\t" \
    "s_nop 15\n\t" \
    "buffer_load_dwordx4 v[224:227], %[l16], %[rs], %[s1] offen sc1\n\t" \
    "s_nop 15\n\t" \
    "buffer_load_dwordx4 v[228:231], %[l16], %[rs], %[s2] offen sc1\n\t" \
    "s_nop 15\n\t" \
    "buffer_load_dwordx4 v[232:235], %[l16], %[rs], %[s3] offen sc1\n\t" \
    "s_nop 15\n\t" \
    "buffer_load_dwordx4 v[236:239], %[l16], %[rs], %[s4] offen sc1\n\t" \
    "s_nop 15\n\t" \
    "buffer_load_dwordx4 v[240:243], %[l16], %[rs], %[s5] offen sc1\n\t" \
    "A_%=:\n\t" \
    "s_waitcnt vmcnt(5)\n\t" \
    "v_smfmac_f32_16x16x64_bf16 v[0:3], v[220:223], v[28:35], %[idx]\n\t" \
    "v_max3_u32 v252, v252, v220, v221\n\t" \
    "v_smfmac_f32_16x16x64_bf16 v[4:7], v[220:223], v[36:43], %[idx]\n\t" \
    "v_max3_u32 v252, v252, v222, v223\n\t" \
    "v_smfmac_f32_16x16x64_bf16 v[8:11], v[220:223], v[44:51], %[idx]\n\t" \
    "v_smfmac_f32_16x16x64_bf16 v[12:15], v[220:223], v[52:59], %[idx]\n\t" \
    "s_waitcnt vmcnt(4)\n\t" \
    "v_smfmac_f32_16x16x64_bf16 v[0:3], v[224:227], v[60:67], %[idx]\n\t" \
    "v_max3_u32 v252, v252, v224, v225\n\t" \
    "v_smfmac_f32_16x16x64_bf16 v[4:7], v[224:227], v[68:75], %[idx]\n\t" \
    "v_max3_u32 v252, v252, v226, v227\n\t" \
    "v_smfmac_f32_16x16x64_bf16 v[8:11], v[224:227], v[76:83], %[idx]\n\t" \
    "v_smfmac_f32_16x16x64_bf16 v[12:15], v[224:227], v[84:91], %[idx]\n\t" \
    "s_waitcnt vmcnt(3)\n\t" \
    "v_smfmac_f32_16x16x64_bf16 v[0:3], v[228:231], v[92:99], %[idx]\n\t" \
    "v_max3_u32 v252, v252, v228, v229\n\t" \
    "v_smfmac_f32_16x16x64_bf16 v[4:7], v[228:231], v[100:107], %[idx]\n\t" \
    "v_max3_u32 v252, v252, v230, v231\n\t" \
    "v_smfmac_f32_16x16x64_bf16 v[8:11], v[228:231], v[108:115], %[idx]\n\t" \
    "v_smfmac_f32_16x16x64_bf16 v[12:15], v[228:231], v[116:123], %[idx]\n\t" \
    "s_waitcnt vmcnt(2)\n\t" \
    "v_smfmac_f32_16x16x64_bf16 v[0:3], v[232:235], v[124:131], %[idx]\n\t" \
    "v_max3_u32 v252, v252, v232, v233\n\t" \
    "v_smfmac_f32_16x16x64_bf16 v[4:7], v[232:235], v[132:139], %[idx]\n\t" \
    "v_max3_u32 v252, v252, v234, v235\n\t" \
    "v_smfmac_f32_16x16x64_bf16 v[8:11], v[232:235], v[140:147], %[idx]\n\t" \
    "v_smfmac_f32_16x16x64_bf16 v[12:15], v[232:235], v[148:155], %[idx]\n\t" \
    "s_waitcnt vmcnt(1)\n\t" \
    "v_smfmac_f32_16x16x64_bf16 v[0:3], v[236:239], v[156:163], %[idx]\n\t" \
    "v_max3_u32 v252, v252, v236, v237\n\t" \
    "v_smfmac_f32_16x16x64_bf16 v[4:7], v[236:239], v[164:171], %[idx]\n\t" \
    "v_max3_u32 v252, v252, v238, v239\n\t" \
    "v_smfmac_f32_16x16x64_bf16 v[8:11], v[236:239], v[172:179], %[idx]\n\t" \
    "v_smfmac_f32_16x16x64_bf16 v[12:15], v[236:239], v[180:187], %[idx]\n\t" \
    "s_waitcnt vmcnt(0)\n\t" \
    "v_smfmac_f32_16x16x64_bf16 v[0:3], v[240:243], v[188:195], %[idx]\n\t" \
    "v_max3_u32 v252, v252, v240, v241\n\t" \
    "v_smfmac_f32_16x16x64_bf16 v[4:7], v[240:243], v[196:203], %[idx]\n\t" \
    "v_max3_u32 v252, v252, v242, v243\n\t" \
    "v_smfmac_f32_16x16x64_bf16 v[8:11], v[240:243], v[204:211], %[idx]\n\t" \
    "v_smfmac_f32_16x16x64_bf16 v[12:15], v[240:243], v[212:219], %[idx]\n\t" \
    "v_cmp_eq_u32 vcc, -1, v252\n\t" \
    "s_cbranch_vccnz SL_%=\n\t" \
    "D_%=:\n\t" \
    GSA(1) \
    "s_nop 2\n\t" \
    "v_add_f32 v0, v0, v1\n\t" \
    "v_add_f32 v2, v2, v3\n\t" \
    "v_add_f32 v4, v4, v5\n\t" \
    "v_add_f32 v6, v6, v7\n\t" \
    "v_add_f32 v8, v8, v9\n\t" \
    "v_add_f32 v10, v10, v11\n\t" \
    "v_add_f32 v12, v12, v13\n\t" \
    "v_add_f32 v14, v14, v15\n\t" \
    "v_permlane32_swap_b32 v0, v4\n\t" \
    "v_permlane32_swap_b32 v2, v6\n\t" \
    "v_permlane32_swap_b32 v8, v12\n\t" \
    "v_permlane32_swap_b32 v10, v14\n\t" \
    "v_add_f32 v0, v0, v12\n\t" \
    "v_add_f32 v2, v2, v14\n\t" \
    "ds_write2_b32 %[rw], v0, v2 offset0:0 offset1:32\n\t" \
    "s_waitcnt lgkmcnt(0)\n\t" \
    "s_barrier\n\t" \
    "s_branch E_%=\n\t" \
    "SL_%=:\n\t" \
    "s_sleep 1\n\t" \
    "buffer_load_dwordx4 v[220:223], %[l16], %[rs], %[s0] offen sc1\n\t" \
    "buffer_load_dwordx4 v[224:227], %[l16], %[rs], %[s1] offen sc1\n\t" \
    "buffer_load_dwordx4 v[228:231], %[l16], %[rs], %[s2] offen sc1\n\t" \
    "buffer_load_dwordx4 v[232:235], %[l16], %[rs], %[s3] offen sc1\n\t" \
    "buffer_load_dwordx4 v[236:239], %[l16], %[rs], %[s4] offen sc1\n\t" \
    "buffer_load_dwordx4 v[240:243], %[l16], %[rs], %[s5] offen sc1\n\t" \
    "s_sub_u32 %[lim], %[lim], " STR(DEC2) "\n\t" \
    "s_cmp_gt_i32 %[lim], 0\n\t" \
    "s_cbranch_scc0 G_%=\n\t" \
    "s_nop 3\n\t" \
    "v_mov_b32 v0, 0\n\t" \
    "v_mov_b32 v1, 0\n\t" \
    "v_mov_b32 v2, 0\n\t" \
    "v_mov_b32 v3, 0\n\t" \
    "v_mov_b32 v4, 0\n\t" \
    "v_mov_b32 v5, 0\n\t" \
    "v_mov_b32 v6, 0\n\t" \
    "v_mov_b32 v7, 0\n\t" \
    "v_mov_b32 v8, 0\n\t" \
    "v_mov_b32 v9, 0\n\t" \
    "v_mov_b32 v10, 0\n\t" \
    "v_mov_b32 v11, 0\n\t" \
    "v_mov_b32 v12, 0\n\t" \
    "v_mov_b32 v13, 0\n\t" \
    "v_mov_b32 v14, 0\n\t" \
    "v_mov_b32 v15, 0\n\t" \
    "v_mov_b32 v252, 0\n\t" \
    "s_branch A_%=\n\t" \
    "G_%=:\n\t" \
    "s_waitcnt vmcnt(0)\n\t" \
    "s_branch D_%=\n\t" \
    "E_%=:\n\t" \
    GSW \
    : [lim] "+s"(lim) GSO(1) GSO(4) \
    : [l16] "v"(l16), [rs] "s"(rs), [idx] "v"(idx), [rw] "v"(rw_a), [s0] "s"(so[0]), [s1] "s"(so[1]), [s2] "s"(so[2]), [s3] "s"(so[3]), [s4] "s"(so[4]), [s5] "s"(so[5]), "{v[28:35]}"(wo[0][0][0]), "{v[36:43]}"(wo[0][0][1]), "{v[44:51]}"(wo[0][1][0]), "{v[52:59]}"(wo[0][1][1]), "{v[60:67]}"(wo[1][0][0]), "{v[68:75]}"(wo[1][0][1]), "{v[76:83]}"(wo[1][1][0]), "{v[84:91]}"(wo[1][1][1]), "{v[92:99]}"(wo[2][0][0]), "{v[100:107]}"(wo[2][0][1]), "{v[108:115]}"(wo[2][1][0]), "{v[116:123]}"(wo[2][1][1]), "{v[124:131]}"(wo[3][0][0]), "{v[132:139]}"(wo[3][0][1]), "{v[140:147]}"(wo[3][1][0]), "{v[148:155]}"(wo[3][1][1]), "{v[156:163]}"(wo[4][0][0]), "{v[164:171]}"(wo[4][0][1]), "{v[172:179]}"(wo[4][1][0]), "{v[180:187]}"(wo[4][1][1]), "{v[188:195]}"(wo[5][0][0]), "{v[196:203]}"(wo[5][0][1]), "{v[204:211]}"(wo[5][1][0]), "{v[212:219]}"(wo[5][1][1]) \
    : "memory", "vcc", "scc", "v252", "v0", "v1", "v2", "v3", "v4", "v5", "v6", "v7", "v8", "v9", "v10", "v11", "v12", "v13", "v14", "v15", "v220", "v221", "v222", "v223", "v224", "v225", "v226", "v227", "v228", "v229", "v230", "v231", "v232", "v233", "v234", "v235", "v236", "v237", "v238", "v239", "v240", "v241", "v242", "v243")

#define S_LOOP_K(TE_, FIN_) \
  asm volatile( \
    "s_cmp_eq_u32 %[kh], 0\n\t" \
    "s_cbranch_scc0 L1_%=\n\t" \
    "L0_%=:\n\t" \
    "s_setprio 0\n\t" \
    "s_nop 3\n\t" \
    "ds_write_b128 %[pa], v[196:199] offset:0\n\t" \
    "ds_write_b128 %[pa], v[200:203] offset:1024\n\t" \
    "ds_write_b128 %[pa], v[204:207] offset:2048\n\t" \
    "ds_write_b128 %[pa], v[208:211] offset:3072\n\t" \
    "s_sub_u32 %[a], %[t], 1\n\t" \
    "v_mov_b32 v53, %[tag]\n\t" \
    "v_mov_b32 v52, %[a]\n\t" \
    "s_sub_u32 %[b], %[t], 2\n\t" \
    "s_cmp_lt_u32 %[b], " STR(SQUICK) "\n\t" \
    "s_cselect_b64 exec, %[fm], 0\n\t" \
    "buffer_store_dwordx2 v[52:53], %[l16], %[rs], %[fo] offen sc1\n\t" \
    "s_mov_b64 exec, -1\n\t" \
    "s_add_u32 m0, %[sd], 49152\n\t" \
    "s_add_u32 %[a], %[sx], 81920\n\t" \
    "s_waitcnt lgkmcnt(0)\n\t" \
    "buffer_load_dwordx4 %[l16], %[rs], %[a] offen offset:0 sc1 lds\n\t" \
    "buffer_load_dwordx4 %[l16], %[rs], %[a] offen offset:1024 sc1 lds\n\t" \
    "buffer_load_dwordx4 %[l16], %[rs], %[a] offen offset:2048 sc1 lds\n\t" \
    "buffer_load_dwordx4 %[l16], %[rs], %[a] offen offset:3072 sc1 lds\n\t" \
    "v_mfma_f32_16x16x32_bf16 v[0:3], v[68:71], v[20:23], 0\n\t" \
    "ds_read_b128 v[44:47], %[xp] offset:6144\n\t" \
    "v_mfma_f32_16x16x32_bf16 v[4:7], v[76:79], v[20:23], 0\n\t" \
    "ds_read_b128 v[48:51], %[xp] offset:7168\n\t" \
    "v_mfma_f32_16x16x32_bf16 v[8:11], v[68:71], v[24:27], 0\n\t" \
    "v_mfma_f32_16x16x32_bf16 v[12:15], v[76:79], v[24:27], 0\n\t" \
    "v_mfma_f32_16x16x32_bf16 v[0:3], v[72:75], v[28:31], v[0:3]\n\t" \
    "ds_read_b128 v[20:23], %[xp] offset:8192\n\t" \
    "v_mfma_f32_16x16x32_bf16 v[4:7], v[80:83], v[28:31], v[4:7]\n\t" \
    "ds_read_b128 v[24:27], %[xp] offset:9216\n\t" \
    "v_mfma_f32_16x16x32_bf16 v[8:11], v[72:75], v[32:35], v[8:11]\n\t" \
    "v_mfma_f32_16x16x32_bf16 v[12:15], v[80:83], v[32:35], v[12:15]\n\t" \
    "v_mfma_f32_16x16x32_bf16 v[0:3], v[84:87], v[36:39], v[0:3]\n\t" \
    "ds_read_b128 v[28:31], %[xp] offset:10240\n\t" \
    "v_mfma_f32_16x16x32_bf16 v[4:7], v[92:95], v[36:39], v[4:7]\n\t" \
    "ds_read_b128 v[32:35], %[xp] offset:11264\n\t" \
    "v_mfma_f32_16x16x32_bf16 v[8:11], v[84:87], v[40:43], v[8:11]\n\t" \
    "s_waitcnt lgkmcnt(4)\n\t" \
    "v_mfma_f32_16x16x32_bf16 v[12:15], v[92:95], v[40:43], v[12:15]\n\t" \
    "v_mfma_f32_16x16x32_bf16 v[0:3], v[88:91], v[44:47], v[0:3]\n\t" \
    "ds_read_b128 v[36:39], %[xp] offset:12288\n\t" \
    "v_mfma_f32_16x16x32_bf16 v[4:7], v[96:99], v[44:47], v[4:7]\n\t" \
    "ds_read_b128 v[40:43], %[xp] offset:13312\n\t" \
    "v_mfma_f32_16x16x32_bf16 v[8:11], v[88:91], v[48:51], v[8:11]\n\t" \
    "s_waitcnt lgkmcnt(4)\n\t" \
    "v_mfma_f32_16x16x32_bf16 v[12:15], v[96:99], v[48:51], v[12:15]\n\t" \
    "v_mfma_f32_16x16x32_bf16 v[0:3], v[100:103], v[20:23], v[0:3]\n\t" \
    "ds_read_b128 v[44:47], %[xp] offset:14336\n\t" \
    "v_mfma_f32_16x16x32_bf16 v[4:7], v[108:111], v[20:23], v[4:7]\n\t" \
    "ds_read_b128 v[48:51], %[xp] offset:15360\n\t" \
    "v_mfma_f32_16x16x32_bf16 v[8:11], v[100:103], v[24:27], v[8:11]\n\t" \
    "s_waitcnt lgkmcnt(4)\n\t" \
    "v_mfma_f32_16x16x32_bf16 v[12:15], v[108:111], v[24:27], v[12:15]\n\t" \
    "v_mfma_f32_16x16x32_bf16 v[0:3], v[104:107], v[28:31], v[0:3]\n\t" \
    "ds_read_b128 v[20:23], %[xp] offset:16384\n\t" \
    "v_mfma_f32_16x16x32_bf16 v[4:7], v[112:115], v[28:31], v[4:7]\n\t" \
    "ds_read_b128 v[24:27], %[xp] offset:17408\n\t" \
    "v_mfma_f32_16x16x32_bf16 v[8:11], v[104:107], v[32:35], v[8:11]\n\t" \
    "s_waitcnt lgkmcnt(4)\n\t" \
    "v_mfma_f32_16x16x32_bf16 v[12:15], v[112:115], v[32:35], v[12:15]\n\t" \
    "v_mfma_f32_16x16x32_bf16 v[0:3], v[116:119], v[36:39], v[0:3]\n\t" \
    "ds_read_b128 v[28:31], %[xp] offset:18432\n\t" \
    "v_mfma_f32_16x16x32_bf16 v[4:7], v[124:127], v[36:39], v[4:7]\n\t" \
    "ds_read_b128 v[32:35], %[xp] offset:19456\n\t" \
    "v_mfma_f32_16x16x32_bf16 v[8:11], v[116:119], v[40:43], v[8:11]\n\t" \
    "s_waitcnt lgkmcnt(4)\n\t" \
    "v_mfma_f32_16x16x32_bf16 v[12:15], v[124:127], v[40:43], v[12:15]\n\t" \
    "v_mfma_f32_16x16x32_bf16 v[0:3], v[120:123], v[44:47], v[0:3]\n\t" \
    "ds_read_b128 v[36:39], %[xp] offset:20480\n\t" \
    "v_mfma_f32_16x16x32_bf16 v[4:7], v[128:131], v[44:47], v[4:7]\n\t" \
    "ds_read_b128 v[40:43], %[xp] offset:21504\n\t" \
    "v_mfma_f32_16x16x32_bf16 v[8:11], v[120:123], v[48:51], v[8:11]\n\t" \
    "s_waitcnt lgkmcnt(4)\n\t" \
    "v_mfma_f32_16x16x32_bf16 v[12:15], v[128:131], v[48:51], v[12:15]\n\t" \
    "s_waitcnt vmcnt(4)\n\t" \
    "s_barrier\n\t" \
    "s_setprio 0\n\t" \
    "s_sub_u32 %[a], %[t], 1\n\t" \
    "v_mov_b32 v53, %[tag]\n\t" \
    "v_mov_b32 v52, %[a]\n\t" \
    "s_cmp_gt_u32 %[t], 1\n\t" \
    "s_cselect_b64 exec, %[fm], 0\n\t" \
    "buffer_store_dwordx2 v[52:53], %[l16], %[rs], %[fo] offen sc1\n\t" \
    "s_mov_b64 exec, %[pm]\n\t" \
    "s_mov_b32 m0, %[pd]\n\t" \
    "s_add_u32 %[a], %[sx], %[po]\n\t" \
    "buffer_load_dwordx4 %[l16], %[rs], %[a] offen sc1 lds\n\t" \
    "s_mov_b64 exec, -1\n\t" \
    "s_add_u32 m0, %[sd], 0\n\t" \
    "s_add_u32 %[a], %[sx], 131072\n\t" \
    "buffer_load_dwordx4 %[l16], %[rs], %[a] offen offset:0 sc1 lds\n\t" \
    "buffer_load_dwordx4 %[l16], %[rs], %[a] offen offset:1024 sc1 lds\n\t" \
    "buffer_load_dwordx4 %[l16], %[rs], %[a] offen offset:2048 sc1 lds\n\t" \
    "buffer_load_dwordx4 %[l16], %[rs], %[a] offen offset:3072 sc1 lds\n\t" \
    "v_mfma_f32_16x16x32_bf16 v[0:3], v[132:135], v[20:23], v[0:3]\n\t" \
    "ds_read_b128 v[44:47], %[xp] offset:22528\n\t" \
    "v_mfma_f32_16x16x32_bf16 v[4:7], v[140:143], v[20:23], v[4:7]\n\t" \
    "ds_read_b128 v[48:51], %[xp] offset:23552\n\t" \
    "v_mfma_f32_16x16x32_bf16 v[8:11], v[132:135], v[24:27], v[8:11]\n\t" \
    "s_waitcnt lgkmcnt(4)\n\t" \
    "v_mfma_f32_16x16x32_bf16 v[12:15], v[140:143], v[24:27], v[12:15]\n\t" \
    "v_mfma_f32_16x16x32_bf16 v[0:3], v[136:139], v[28:31], v[0:3]\n\t" \
    "ds_read_b128 v[20:23], %[xp] offset:24576\n\t" \
    "v_mfma_f32_16x16x32_bf16 v[4:7], v[144:147], v[28:31], v[4:7]\n\t" \
    "ds_read_b128 v[24:27], %[xp] offset:25600\n\t" \
    "v_mfma_f32_16x16x32_bf16 v[8:11], v[136:139], v[32:35], v[8:11]\n\t" \
    "s_waitcnt lgkmcnt(4)\n\t" \
    "v_mfma_f32_16x16x32_bf16 v[12:15], v[144:147], v[32:35], v[12:15]\n\t" \
    "v_mfma_f32_16x16x32_bf16 v[0:3], v[148:151], v[36:39], v[0:3]\n\t" \
    "ds_read_b128 v[28:31], %[xp] offset:26624\n\t" \
    "v_mfma_f32_16x16x32_bf16 v[4:7], v[156:159], v[36:39], v[4:7]\n\t" \
    "ds_read_b128 v[32:35], %[xp] offset:27648\n\t" \
    "v_mfma_f32_16x16x32_bf16 v[8:11], v[148:151], v[40:43], v[8:11]\n\t" \
    "s_waitcnt lgkmcnt(4)\n\t" \
    "v_mfma_f32_16x16x32_bf16 v[12:15], v[156:159], v[40:43], v[12:15]\n\t" \
    "v_mfma_f32_16x16x32_bf16 v[0:3], v[152:155], v[44:47], v[0:3]\n\t" \
    "ds_read_b128 v[36:39], %[xp] offset:28672\n\t" \
    "v_mfma_f32_16x16x32_bf16 v[4:7], v[160:163], v[44:47], v[4:7]\n\t" \
    "ds_read_b128 v[40:43], %[xp] offset:29696\n\t" \
    "v_mfma_f32_16x16x32_bf16 v[8:11], v[152:155], v[48:51], v[8:11]\n\t" \
    "s_waitcnt lgkmcnt(4)\n\t" \
    "v_mfma_f32_16x16x32_bf16 v[12:15], v[160:163], v[48:51], v[12:15]\n\t" \
    "v_mfma_f32_16x16x32_bf16 v[0:3], v[164:167], v[20:23], v[0:3]\n\t" \
    "ds_read_b128 v[44:47], %[xp] offset:30720\n\t" \
    "v_mfma_f32_16x16x32_bf16 v[4:7], v[172:175], v[20:23], v[4:7]\n\t" \
    "ds_read_b128 v[48:51], %[xp] offset:31744\n\t" \
    "v_mfma_f32_16x16x32_bf16 v[8:11], v[164:167], v[24:27], v[8:11]\n\t" \
    "s_waitcnt lgkmcnt(4)\n\t" \
    "v_mfma_f32_16x16x32_bf16 v[12:15], v[172:175], v[24:27], v[12:15]\n\t" \
    "v_mfma_f32_16x16x32_bf16 v[0:3], v[168:171], v[28:31], v[0:3]\n\t" \
    "ds_read_b128 v[20:23], %[xp] offset:32768\n\t" \
    "v_mfma_f32_16x16x32_bf16 v[4:7], v[176:179], v[28:31], v[4:7]\n\t" \
    "ds_read_b128 v[24:27], %[xp] offset:33792\n\t" \
    "v_mfma_f32_16x16x32_bf16 v[8:11], v[168:171], v[32:35], v[8:11]\n\t" \
    "s_waitcnt lgkmcnt(4)\n\t" \
    "v_mfma_f32_16x16x32_bf16 v[12:15], v[176:179], v[32:35], v[12:15]\n\t" \
    "v_mfma_f32_16x16x32_bf16 v[0:3], v[180:183], v[36:39], v[0:3]\n\t" \
    "ds_read_b128 v[28:31], %[xp] offset:34816\n\t" \
    "v_mfma_f32_16x16x32_bf16 v[4:7], v[188:191], v[36:39], v[4:7]\n\t" \
    "ds_read_b128 v[32:35], %[xp] offset:35840\n\t" \
    "v_mfma_f32_16x16x32_bf16 v[8:11], v[180:183], v[40:43], v[8:11]\n\t" \
    "s_waitcnt lgkmcnt(4)\n\t" \
    "v_mfma_f32_16x16x32_bf16 v[12:15], v[188:191], v[40:43], v[12:15]\n\t" \
    "v_mfma_f32_16x16x32_bf16 v[0:3], v[184:187], v[44:47], v[0:3]\n\t" \
    "ds_read_b128 v[36:39], %[xp] offset:36864\n\t" \
    "v_mfma_f32_16x16x32_bf16 v[4:7], v[192:195], v[44:47], v[4:7]\n\t" \
    "ds_read_b128 v[40:43], %[xp] offset:37888\n\t" \
    "v_mfma_f32_16x16x32_bf16 v[8:11], v[184:187], v[48:51], v[8:11]\n\t" \
    "s_waitcnt lgkmcnt(4)\n\t" \
    "v_mfma_f32_16x16x32_bf16 v[12:15], v[192:195], v[48:51], v[12:15]\n\t" \
    "s_add_u32 %[sx], %[sx], 65536\n\t" \
    "s_add_u32 %[t], %[t], 1\n\t" \
    "s_waitcnt vmcnt(4)\n\t" \
    "s_barrier\n\t" \
    "s_setprio 0\n\t" \
    "s_nop 3\n\t" \
    "ds_write_b128 %[pa], v[0:3] offset:0\n\t" \
    "ds_write_b128 %[pa], v[4:7] offset:1024\n\t" \
    "ds_write_b128 %[pa], v[8:11] offset:2048\n\t" \
    "ds_write_b128 %[pa], v[12:15] offset:3072\n\t" \
    "s_sub_u32 %[a], %[t], 1\n\t" \
    "v_mov_b32 v53, %[tag]\n\t" \
    "v_mov_b32 v52, %[a]\n\t" \
    "s_sub_u32 %[b], %[t], 2\n\t" \
    "s_cmp_lt_u32 %[b], " STR(SQUICK) "\n\t" \
    "s_cselect_b64 exec, %[fm], 0\n\t" \
    "buffer_store_dwordx2 v[52:53], %[l16], %[rs], %[fo] offen sc1\n\t" \
    "s_mov_b64 exec, -1\n\t" \
    "s_add_u32 m0, %[sd], 16384\n\t" \
    "s_add_u32 %[a], %[sx], 81920\n\t" \
    "s_waitcnt lgkmcnt(0)\n\t" \
    "buffer_load_dwordx4 %[l16], %[rs], %[a] offen offset:0 sc1 lds\n\t" \
    "buffer_load_dwordx4 %[l16], %[rs], %[a] offen offset:1024 sc1 lds\n\t" \
    "buffer_load_dwordx4 %[l16], %[rs], %[a] offen offset:2048 sc1 lds\n\t" \
    "buffer_load_dwordx4 %[l16], %[rs], %[a] offen offset:3072 sc1 lds\n\t" \
    "v_mfma_f32_16x16x32_bf16 v[196:199], v[68:71], v[20:23], 0\n\t" \
    "ds_read_b128 v[44:47], %[xp] offset:38912\n\t" \
    "v_mfma_f32_16x16x32_bf16 v[200:203], v[76:79], v[20:23], 0\n\t" \
    "ds_read_b128 v[48:51], %[xp] offset:39936\n\t" \
    "v_mfma_f32_16x16x32_bf16 v[204:207], v[68:71], v[24:27], 0\n\t" \
    "v_mfma_f32_16x16x32_bf16 v[208:211], v[76:79], v[24:27], 0\n\t" \
    "v_mfma_f32_16x16x32_bf16 v[196:199], v[72:75], v[28:31], v[196:199]\n\t" \
    "ds_read_b128 v[20:23], %[xp] offset:40960\n\t" \
    "v_mfma_f32_16x16x32_bf16 v[200:203], v[80:83], v[28:31], v[200:203]\n\t" \
    "ds_read_b128 v[24:27], %[xp] offset:41984\n\t" \
    "v_mfma_f32_16x16x32_bf16 v[204:207], v[72:75], v[32:35], v[204:207]\n\t" \
    "v_mfma_f32_16x16x32_bf16 v[208:211], v[80:83], v[32:35], v[208:211]\n\t" \
    "v_mfma_f32_16x16x32_bf16 v[196:199], v[84:87], v[36:39], v[196:199]\n\t" \
    "ds_read_b128 v[28:31], %[xp] offset:43008\n\t" \
    "v_mfma_f32_16x16x32_bf16 v[200:203], v[92:95], v[36:39], v[200:203]\n\t" \
    "ds_read_b128 v[32:35], %[xp] offset:44032\n\t" \
    "v_mfma_f32_16x16x32_bf16 v[204:207], v[84:87], v[40:43], v[204:207]\n\t" \
    "s_waitcnt lgkmcnt(4)\n\t" \
    "v_mfma_f32_16x16x32_bf16 v[208:211], v[92:95], v[40:43], v[208:211]\n\t" \
    "v_mfma_f32_16x16x32_bf16 v[196:199], v[88:91], v[44:47], v[196:199]\n\t" \
    "ds_read_b128 v[36:39], %[xp] offset:45056\n\t" \
    "v_mfma_f32_16x16x32_bf16 v[200:203], v[96:99], v[44:47], v[200:203]\n\t" \
    "ds_read_b128 v[40:43], %[xp] offset:46080\n\t" \
    "v_mfma_f32_16x16x32_bf16 v[204:207], v[88:91], v[48:51], v[204:207]\n\t" \
    "s_waitcnt lgkmcnt(4)\n\t" \
    "v_mfma_f32_16x16x32_bf16 v[208:211], v[96:99], v[48:51], v[208:211]\n\t" \
    "v_mfma_f32_16x16x32_bf16 v[196:199], v[100:103], v[20:23], v[196:199]\n\t" \
    "ds_read_b128 v[44:47], %[xp] offset:47104\n\t" \
    "v_mfma_f32_16x16x32_bf16 v[200:203], v[108:111], v[20:23], v[200:203]\n\t" \
    "ds_read_b128 v[48:51], %[xp] offset:48128\n\t" \
    "v_mfma_f32_16x16x32_bf16 v[204:207], v[100:103], v[24:27], v[204:207]\n\t" \
    "s_waitcnt lgkmcnt(4)\n\t" \
    "v_mfma_f32_16x16x32_bf16 v[208:211], v[108:111], v[24:27], v[208:211]\n\t" \
    "v_mfma_f32_16x16x32_bf16 v[196:199], v[104:107], v[28:31], v[196:199]\n\t" \
    "ds_read_b128 v[20:23], %[xp] offset:49152\n\t" \
    "v_mfma_f32_16x16x32_bf16 v[200:203], v[112:115], v[28:31], v[200:203]\n\t" \
    "ds_read_b128 v[24:27], %[xp] offset:50176\n\t" \
    "v_mfma_f32_16x16x32_bf16 v[204:207], v[104:107], v[32:35], v[204:207]\n\t" \
    "s_waitcnt lgkmcnt(4)\n\t" \
    "v_mfma_f32_16x16x32_bf16 v[208:211], v[112:115], v[32:35], v[208:211]\n\t" \
    "v_mfma_f32_16x16x32_bf16 v[196:199], v[116:119], v[36:39], v[196:199]\n\t" \
    "ds_read_b128 v[28:31], %[xp] offset:51200\n\t" \
    "v_mfma_f32_16x16x32_bf16 v[200:203], v[124:127], v[36:39], v[200:203]\n\t" \
    "ds_read_b128 v[32:35], %[xp] offset:52224\n\t" \
    "v_mfma_f32_16x16x32_bf16 v[204:207], v[116:119], v[40:43], v[204:207]\n\t" \
    "s_waitcnt lgkmcnt(4)\n\t" \
    "v_mfma_f32_16x16x32_bf16 v[208:211], v[124:127], v[40:43], v[208:211]\n\t" \
    "v_mfma_f32_16x16x32_bf16 v[196:199], v[120:123], v[44:47], v[196:199]\n\t" \
    "ds_read_b128 v[36:39], %[xp] offset:53248\n\t" \
    "v_mfma_f32_16x16x32_bf16 v[200:203], v[128:131], v[44:47], v[200:203]\n\t" \
    "ds_read_b128 v[40:43], %[xp] offset:54272\n\t" \
    "v_mfma_f32_16x16x32_bf16 v[204:207], v[120:123], v[48:51], v[204:207]\n\t" \
    "s_waitcnt lgkmcnt(4)\n\t" \
    "v_mfma_f32_16x16x32_bf16 v[208:211], v[128:131], v[48:51], v[208:211]\n\t" \
    "s_waitcnt vmcnt(4)\n\t" \
    "s_barrier\n\t" \
    "s_setprio 0\n\t" \
    "s_sub_u32 %[a], %[t], 1\n\t" \
    "v_mov_b32 v53, %[tag]\n\t" \
    "v_mov_b32 v52, %[a]\n\t" \
    "s_cmp_gt_u32 %[t], 1\n\t" \
    "s_cselect_b64 exec, %[fm], 0\n\t" \
    "buffer_store_dwordx2 v[52:53], %[l16], %[rs], %[fo] offen sc1\n\t" \
    "s_mov_b64 exec, %[pm]\n\t" \
    "s_mov_b32 m0, %[pd]\n\t" \
    "s_add_u32 %[a], %[sx], %[po]\n\t" \
    "buffer_load_dwordx4 %[l16], %[rs], %[a] offen sc1 lds\n\t" \
    "s_mov_b64 exec, -1\n\t" \
    "s_add_u32 m0, %[sd], 32768\n\t" \
    "s_add_u32 %[a], %[sx], 131072\n\t" \
    "buffer_load_dwordx4 %[l16], %[rs], %[a] offen offset:0 sc1 lds\n\t" \
    "buffer_load_dwordx4 %[l16], %[rs], %[a] offen offset:1024 sc1 lds\n\t" \
    "buffer_load_dwordx4 %[l16], %[rs], %[a] offen offset:2048 sc1 lds\n\t" \
    "buffer_load_dwordx4 %[l16], %[rs], %[a] offen offset:3072 sc1 lds\n\t" \
    "v_mfma_f32_16x16x32_bf16 v[196:199], v[132:135], v[20:23], v[196:199]\n\t" \
    "ds_read_b128 v[44:47], %[xp] offset:55296\n\t" \
    "v_mfma_f32_16x16x32_bf16 v[200:203], v[140:143], v[20:23], v[200:203]\n\t" \
    "ds_read_b128 v[48:51], %[xp] offset:56320\n\t" \
    "v_mfma_f32_16x16x32_bf16 v[204:207], v[132:135], v[24:27], v[204:207]\n\t" \
    "s_waitcnt lgkmcnt(4)\n\t" \
    "v_mfma_f32_16x16x32_bf16 v[208:211], v[140:143], v[24:27], v[208:211]\n\t" \
    "v_mfma_f32_16x16x32_bf16 v[196:199], v[136:139], v[28:31], v[196:199]\n\t" \
    "ds_read_b128 v[20:23], %[xp] offset:57344\n\t" \
    "v_mfma_f32_16x16x32_bf16 v[200:203], v[144:147], v[28:31], v[200:203]\n\t" \
    "ds_read_b128 v[24:27], %[xp] offset:58368\n\t" \
    "v_mfma_f32_16x16x32_bf16 v[204:207], v[136:139], v[32:35], v[204:207]\n\t" \
    "s_waitcnt lgkmcnt(4)\n\t" \
    "v_mfma_f32_16x16x32_bf16 v[208:211], v[144:147], v[32:35], v[208:211]\n\t" \
    "v_mfma_f32_16x16x32_bf16 v[196:199], v[148:151], v[36:39], v[196:199]\n\t" \
    "ds_read_b128 v[28:31], %[xp] offset:59392\n\t" \
    "v_mfma_f32_16x16x32_bf16 v[200:203], v[156:159], v[36:39], v[200:203]\n\t" \
    "ds_read_b128 v[32:35], %[xp] offset:60416\n\t" \
    "v_mfma_f32_16x16x32_bf16 v[204:207], v[148:151], v[40:43], v[204:207]\n\t" \
    "s_waitcnt lgkmcnt(4)\n\t" \
    "v_mfma_f32_16x16x32_bf16 v[208:211], v[156:159], v[40:43], v[208:211]\n\t" \
    "v_mfma_f32_16x16x32_bf16 v[196:199], v[152:155], v[44:47], v[196:199]\n\t" \
    "ds_read_b128 v[36:39], %[xp] offset:61440\n\t" \
    "v_mfma_f32_16x16x32_bf16 v[200:203], v[160:163], v[44:47], v[200:203]\n\t" \
    "ds_read_b128 v[40:43], %[xp] offset:62464\n\t" \
    "v_mfma_f32_16x16x32_bf16 v[204:207], v[152:155], v[48:51], v[204:207]\n\t" \
    "s_waitcnt lgkmcnt(4)\n\t" \
    "v_mfma_f32_16x16x32_bf16 v[208:211], v[160:163], v[48:51], v[208:211]\n\t" \
    "v_mfma_f32_16x16x32_bf16 v[196:199], v[164:167], v[20:23], v[196:199]\n\t" \
    "ds_read_b128 v[44:47], %[xp] offset:63488\n\t" \
    "v_mfma_f32_16x16x32_bf16 v[200:203], v[172:175], v[20:23], v[200:203]\n\t" \
    "ds_read_b128 v[48:51], %[xp] offset:64512\n\t" \
    "v_mfma_f32_16x16x32_bf16 v[204:207], v[164:167], v[24:27], v[204:207]\n\t" \
    "s_waitcnt lgkmcnt(4)\n\t" \
    "v_mfma_f32_16x16x32_bf16 v[208:211], v[172:175], v[24:27], v[208:211]\n\t" \
    "v_mfma_f32_16x16x32_bf16 v[196:199], v[168:171], v[28:31], v[196:199]\n\t" \
    "ds_read_b128 v[20:23], %[xp] offset:0\n\t" \
    "v_mfma_f32_16x16x32_bf16 v[200:203], v[176:179], v[28:31], v[200:203]\n\t" \
    "ds_read_b128 v[24:27], %[xp] offset:1024\n\t" \
    "v_mfma_f32_16x16x32_bf16 v[204:207], v[168:171], v[32:35], v[204:207]\n\t" \
    "s_waitcnt lgkmcnt(4)\n\t" \
    "v_mfma_f32_16x16x32_bf16 v[208:211], v[176:179], v[32:35], v[208:211]\n\t" \
    "v_mfma_f32_16x16x32_bf16 v[196:199], v[180:183], v[36:39], v[196:199]\n\t" \
    "ds_read_b128 v[28:31], %[xp] offset:2048\n\t" \
    "v_mfma_f32_16x16x32_bf16 v[200:203], v[188:191], v[36:39], v[200:203]\n\t" \
    "ds_read_b128 v[32:35], %[xp] offset:3072\n\t" \
    "v_mfma_f32_16x16x32_bf16 v[204:207], v[180:183], v[40:43], v[204:207]\n\t" \
    "s_waitcnt lgkmcnt(4)\n\t" \
    "v_mfma_f32_16x16x32_bf16 v[208:211], v[188:191], v[40:43], v[208:211]\n\t" \
    "v_mfma_f32_16x16x32_bf16 v[196:199], v[184:187], v[44:47], v[196:199]\n\t" \
    "ds_read_b128 v[36:39], %[xp] offset:4096\n\t" \
    "v_mfma_f32_16x16x32_bf16 v[200:203], v[192:195], v[44:47], v[200:203]\n\t" \
    "ds_read_b128 v[40:43], %[xp] offset:5120\n\t" \
    "v_mfma_f32_16x16x32_bf16 v[204:207], v[184:187], v[48:51], v[204:207]\n\t" \
    "s_waitcnt lgkmcnt(4)\n\t" \
    "v_mfma_f32_16x16x32_bf16 v[208:211], v[192:195], v[48:51], v[208:211]\n\t" \
    "s_add_u32 %[sx], %[sx], 65536\n\t" \
    "s_add_u32 %[t], %[t], 1\n\t" \
    "s_waitcnt vmcnt(4)\n\t" \
    "s_barrier\n\t" \
    "s_cmp_lt_u32 %[t], %[te]\n\t" \
    "s_cbranch_scc1 L0_%=\n\t" \
    "s_cmp_eq_u32 %[fin], %[te]\n\t" \
    "s_cbranch_scc0 E_%=\n\t" \
    "s_nop 4\n\t" \
    "ds_write_b128 %[pa], v[196:199] offset:0\n\t" \
    "ds_write_b128 %[pa], v[200:203] offset:1024\n\t" \
    "ds_write_b128 %[pa], v[204:207] offset:2048\n\t" \
    "ds_write_b128 %[pa], v[208:211] offset:3072\n\t" \
    "s_waitcnt lgkmcnt(0)\n\t" \
    "s_barrier\n\t" \
    "s_branch E_%=\n\t" \
    "L1_%=:\n\t" \
    "s_setprio 1\n\t" \
    "s_add_u32 m0, %[sd], 49152\n\t" \
    "s_add_u32 %[a], %[sx], 81920\n\t" \
    "v_mfma_f32_16x16x32_bf16 v[0:3], v[68:71], v[20:23], 0\n\t" \
    "ds_read_b128 v[44:47], %[xp] offset:6144\n\t" \
    "v_mfma_f32_16x16x32_bf16 v[4:7], v[76:79], v[20:23], 0\n\t" \
    "ds_read_b128 v[48:51], %[xp] offset:7168\n\t" \
    "v_mfma_f32_16x16x32_bf16 v[8:11], v[68:71], v[24:27], 0\n\t" \
    "s_waitcnt lgkmcnt(4)\n\t" \
    "v_mfma_f32_16x16x32_bf16 v[12:15], v[76:79], v[24:27], 0\n\t" \
    "v_mfma_f32_16x16x32_bf16 v[0:3], v[72:75], v[28:31], v[0:3]\n\t" \
    "ds_read_b128 v[20:23], %[xp] offset:8192\n\t" \
    "v_mfma_f32_16x16x32_bf16 v[4:7], v[80:83], v[28:31], v[4:7]\n\t" \
    "ds_read_b128 v[24:27], %[xp] offset:9216\n\t" \
    "v_mfma_f32_16x16x32_bf16 v[8:11], v[72:75], v[32:35], v[8:11]\n\t" \
    "s_waitcnt lgkmcnt(4)\n\t" \
    "v_mfma_f32_16x16x32_bf16 v[12:15], v[80:83], v[32:35], v[12:15]\n\t" \
    "v_mfma_f32_16x16x32_bf16 v[0:3], v[84:87], v[36:39], v[0:3]\n\t" \
    "ds_read_b128 v[28:31], %[xp] offset:10240\n\t" \
    "v_mfma_f32_16x16x32_bf16 v[4:7], v[92:95], v[36:39], v[4:7]\n\t" \
    "ds_read_b128 v[32:35], %[xp] offset:11264\n\t" \
    "v_mfma_f32_16x16x32_bf16 v[8:11], v[84:87], v[40:43], v[8:11]\n\t" \
    "s_waitcnt lgkmcnt(4)\n\t" \
    "v_mfma_f32_16x16x32_bf16 v[12:15], v[92:95], v[40:43], v[12:15]\n\t" \
    "v_mfma_f32_16x16x32_bf16 v[0:3], v[88:91], v[44:47], v[0:3]\n\t" \
    "ds_read_b128 v[36:39], %[xp] offset:12288\n\t" \
    "v_mfma_f32_16x16x32_bf16 v[4:7], v[96:99], v[44:47], v[4:7]\n\t" \
    "ds_read_b128 v[40:43], %[xp] offset:13312\n\t" \
    "v_mfma_f32_16x16x32_bf16 v[8:11], v[88:91], v[48:51], v[8:11]\n\t" \
    "s_waitcnt lgkmcnt(4)\n\t" \
    "v_mfma_f32_16x16x32_bf16 v[12:15], v[96:99], v[48:51], v[12:15]\n\t" \
    "v_mfma_f32_16x16x32_bf16 v[0:3], v[100:103], v[20:23], v[0:3]\n\t" \
    "ds_read_b128 v[44:47], %[xp] offset:14336\n\t" \
    "v_mfma_f32_16x16x32_bf16 v[4:7], v[108:111], v[20:23], v[4:7]\n\t" \
    "ds_read_b128 v[48:51], %[xp] offset:15360\n\t" \
    "v_mfma_f32_16x16x32_bf16 v[8:11], v[100:103], v[24:27], v[8:11]\n\t" \
    "s_waitcnt lgkmcnt(4)\n\t" \
    "v_mfma_f32_16x16x32_bf16 v[12:15], v[108:111], v[24:27], v[12:15]\n\t" \
    "buffer_load_dwordx4 %[l16], %[rs], %[a] offen offset:0 sc1 lds\n\t" \
    "v_mfma_f32_16x16x32_bf16 v[0:3], v[104:107], v[28:31], v[0:3]\n\t" \
    "ds_read_b128 v[20:23], %[xp] offset:16384\n\t" \
    "v_mfma_f32_16x16x32_bf16 v[4:7], v[112:115], v[28:31], v[4:7]\n\t" \
    "ds_read_b128 v[24:27], %[xp] offset:17408\n\t" \
    "v_mfma_f32_16x16x32_bf16 v[8:11], v[104:107], v[32:35], v[8:11]\n\t" \
    "s_waitcnt lgkmcnt(4)\n\t" \
    "v_mfma_f32_16x16x32_bf16 v[12:15], v[112:115], v[32:35], v[12:15]\n\t" \
    "buffer_load_dwordx4 %[l16], %[rs], %[a] offen offset:1024 sc1 lds\n\t" \
    "v_mfma_f32_16x16x32_bf16 v[0:3], v[116:119], v[36:39], v[0:3]\n\t" \
    "ds_read_b128 v[28:31], %[xp] offset:18432\n\t" \
    "v_mfma_f32_16x16x32_bf16 v[4:7], v[124:127], v[36:39], v[4:7]\n\t" \
    "ds_read_b128 v[32:35], %[xp] offset:19456\n\t" \
    "v_mfma_f32_16x16x32_bf16 v[8:11], v[116:119], v[40:43], v[8:11]\n\t" \
    "s_waitcnt lgkmcnt(4)\n\t" \
    "v_mfma_f32_16x16x32_bf16 v[12:15], v[124:127], v[40:43], v[12:15]\n\t" \
    "buffer_load_dwordx4 %[l16], %[rs], %[a] offen offset:2048 sc1 lds\n\t" \
    "v_mfma_f32_16x16x32_bf16 v[0:3], v[120:123], v[44:47], v[0:3]\n\t" \
    "ds_read_b128 v[36:39], %[xp] offset:20480\n\t" \
    "v_mfma_f32_16x16x32_bf16 v[4:7], v[128:131], v[44:47], v[4:7]\n\t" \
    "ds_read_b128 v[40:43], %[xp] offset:21504\n\t" \
    "v_mfma_f32_16x16x32_bf16 v[8:11], v[120:123], v[48:51], v[8:11]\n\t" \
    "s_waitcnt lgkmcnt(4)\n\t" \
    "v_mfma_f32_16x16x32_bf16 v[12:15], v[128:131], v[48:51], v[12:15]\n\t" \
    "buffer_load_dwordx4 %[l16], %[rs], %[a] offen offset:3072 sc1 lds\n\t" \
    "s_waitcnt vmcnt(4)\n\t" \
    "s_barrier\n\t" \
    "s_setprio 1\n\t" \
    "s_add_u32 m0, %[sd], 0\n\t" \
    "s_add_u32 %[a], %[sx], 131072\n\t" \
    "s_add_u32 %[b], %[oo], 0x1000\n\t" \
    "s_cmp_lg_u32 %[t], 1\n\t" \
    "s_cselect_b64 %[em], -1, 0\n\t" \
    "v_mfma_f32_16x16x32_bf16 v[0:3], v[132:135], v[20:23], v[0:3]\n\t" \
    "ds_read_b128 v[44:47], %[xp] offset:22528\n\t" \
    "v_mfma_f32_16x16x32_bf16 v[4:7], v[140:143], v[20:23], v[4:7]\n\t" \
    "ds_read_b128 v[48:51], %[xp] offset:23552\n\t" \
    "v_mfma_f32_16x16x32_bf16 v[8:11], v[132:135], v[24:27], v[8:11]\n\t" \
    "ds_read_b128 v[52:55], %[pa] offset:0\n\t" \
    "s_waitcnt lgkmcnt(5)\n\t" \
    "v_mfma_f32_16x16x32_bf16 v[12:15], v[140:143], v[24:27], v[12:15]\n\t" \
    "buffer_load_dwordx4 %[l16], %[rs], %[a] offen offset:0 sc1 lds\n\t" \
    "v_mfma_f32_16x16x32_bf16 v[0:3], v[136:139], v[28:31], v[0:3]\n\t" \
    "ds_read_b128 v[20:23], %[xp] offset:24576\n\t" \
    "v_mfma_f32_16x16x32_bf16 v[4:7], v[144:147], v[28:31], v[4:7]\n\t" \
    "ds_read_b128 v[24:27], %[xp] offset:25600\n\t" \
    "v_mfma_f32_16x16x32_bf16 v[8:11], v[136:139], v[32:35], v[8:11]\n\t" \
    "ds_read_b128 v[56:59], %[pa] offset:1024\n\t" \
    "s_waitcnt lgkmcnt(6)\n\t" \
    "v_mfma_f32_16x16x32_bf16 v[12:15], v[144:147], v[32:35], v[12:15]\n\t" \
    "buffer_load_dwordx4 %[l16], %[rs], %[a] offen offset:1024 sc1 lds\n\t" \
    "v_mfma_f32_16x16x32_bf16 v[0:3], v[148:151], v[36:39], v[0:3]\n\t" \
    "ds_read_b128 v[28:31], %[xp] offset:26624\n\t" \
    "v_mfma_f32_16x16x32_bf16 v[4:7], v[156:159], v[36:39], v[4:7]\n\t" \
    "ds_read_b128 v[32:35], %[xp] offset:27648\n\t" \
    "v_mfma_f32_16x16x32_bf16 v[8:11], v[148:151], v[40:43], v[8:11]\n\t" \
    "ds_read_b128 v[60:63], %[pa] offset:2048\n\t" \
    "s_waitcnt lgkmcnt(7)\n\t" \
    "v_mfma_f32_16x16x32_bf16 v[12:15], v[156:159], v[40:43], v[12:15]\n\t" \
    "buffer_load_dwordx4 %[l16], %[rs], %[a] offen offset:2048 sc1 lds\n\t" \
    "v_mfma_f32_16x16x32_bf16 v[0:3], v[152:155], v[44:47], v[0:3]\n\t" \
    "ds_read_b128 v[36:39], %[xp] offset:28672\n\t" \
    "s_waitcnt lgkmcnt(7)\n\t" \
    "v_add_f32 v196, v196, v52\n\t" \
    "v_mfma_f32_16x16x32_bf16 v[4:7], v[160:163], v[44:47], v[4:7]\n\t" \
    "ds_read_b128 v[40:43], %[xp] offset:29696\n\t" \
    "v_add_f32 v197, v197, v53\n\t" \
    "v_mfma_f32_16x16x32_bf16 v[8:11], v[152:155], v[48:51], v[8:11]\n\t" \
    "ds_read_b128 v[64:67], %[pa] offset:3072\n\t" \
    "s_waitcnt lgkmcnt(7)\n\t" \
    "v_mfma_f32_16x16x32_bf16 v[12:15], v[160:163], v[48:51], v[12:15]\n\t" \
    "buffer_load_dwordx4 %[l16], %[rs], %[a] offen offset:3072 sc1 lds\n\t" \
    "v_add_f32 v198, v198, v54\n\t" \
    "v_add_f32 v199, v199, v55\n\t" \
    "v_mfma_f32_16x16x32_bf16 v[0:3], v[164:167], v[20:23], v[0:3]\n\t" \
    "ds_read_b128 v[44:47], %[xp] offset:30720\n\t" \
    "s_waitcnt lgkmcnt(7)\n\t" \
    "v_add_f32 v200, v200, v56\n\t" \
    "v_mfma_f32_16x16x32_bf16 v[4:7], v[172:175], v[20:23], v[4:7]\n\t" \
    "ds_read_b128 v[48:51], %[xp] offset:31744\n\t" \
    "v_add_f32 v201, v201, v57\n\t" \
    "v_mfma_f32_16x16x32_bf16 v[8:11], v[164:167], v[24:27], v[8:11]\n\t" \
    "v_add_f32 v202, v202, v58\n\t" \
    "s_waitcnt lgkmcnt(6)\n\t" \
    "v_mfma_f32_16x16x32_bf16 v[12:15], v[172:175], v[24:27], v[12:15]\n\t" \
    "v_add_f32 v203, v203, v59\n\t" \
    "s_waitcnt lgkmcnt(5)\n\t" \
    "v_add_f32 v204, v204, v60\n\t" \
    "v_add_f32 v205, v205, v61\n\t" \
    "v_mfma_f32_16x16x32_bf16 v[0:3], v[168:171], v[28:31], v[0:3]\n\t" \
    "ds_read_b128 v[20:23], %[xp] offset:32768\n\t" \
    "v_add_f32 v206, v206, v62\n\t" \
    "v_mfma_f32_16x16x32_bf16 v[4:7], v[176:179], v[28:31], v[4:7]\n\t" \
    "ds_read_b128 v[24:27], %[xp] offset:33792\n\t" \
    "v_add_f32 v207, v207, v63\n\t" \
    "v_mfma_f32_16x16x32_bf16 v[8:11], v[168:171], v[32:35], v[8:11]\n\t" \
    "s_waitcnt lgkmcnt(4)\n\t" \
    "v_add_f32 v208, v208, v64\n\t" \
    "v_mfma_f32_16x16x32_bf16 v[12:15], v[176:179], v[32:35], v[12:15]\n\t" \
    "v_add_f32 v209, v209, v65\n\t" \
    "v_add_f32 v210, v210, v66\n\t" \
    "v_add_f32 v211, v211, v67\n\t" \
    "v_mfma_f32_16x16x32_bf16 v[0:3], v[180:183], v[36:39], v[0:3]\n\t" \
    "ds_read_b128 v[28:31], %[xp] offset:34816\n\t" \
    "v_cvt_pk_bf16_f32 v52, v196, v200\n\t" \
    "v_mfma_f32_16x16x32_bf16 v[4:7], v[188:191], v[36:39], v[4:7]\n\t" \
    "ds_read_b128 v[32:35], %[xp] offset:35840\n\t" \
    "v_cvt_pk_bf16_f32 v53, v197, v201\n\t" \
    "v_mfma_f32_16x16x32_bf16 v[8:11], v[180:183], v[40:43], v[8:11]\n\t" \
    "v_cvt_pk_bf16_f32 v54, v198, v202\n\t" \
    "s_waitcnt lgkmcnt(4)\n\t" \
    "v_mfma_f32_16x16x32_bf16 v[12:15], v[188:191], v[40:43], v[12:15]\n\t" \
    "v_cvt_pk_bf16_f32 v55, v199, v203\n\t" \
    "v_cvt_pk_bf16_f32 v56, v204, v208\n\t" \
    "v_cvt_pk_bf16_f32 v57, v205, v209\n\t" \
    "v_mfma_f32_16x16x32_bf16 v[0:3], v[184:187], v[44:47], v[0:3]\n\t" \
    "ds_read_b128 v[36:39], %[xp] offset:36864\n\t" \
    "v_cvt_pk_bf16_f32 v58, v206, v210\n\t" \
    "v_mfma_f32_16x16x32_bf16 v[4:7], v[192:195], v[44:47], v[4:7]\n\t" \
    "ds_read_b128 v[40:43], %[xp] offset:37888\n\t" \
    "v_cvt_pk_bf16_f32 v59, v207, v211\n\t" \
    "v_mfma_f32_16x16x32_bf16 v[8:11], v[184:187], v[48:51], v[8:11]\n\t" \
    "s_waitcnt lgkmcnt(4)\n\t" \
    "v_mfma_f32_16x16x32_bf16 v[12:15], v[192:195], v[48:51], v[12:15]\n\t" \
    "s_mov_b64 exec, %[em]\n\t" \
    "buffer_store_dwordx4 v[52:55], %[l16], %[rs], %[oo] offen sc1\n\t" \
    "buffer_store_dwordx4 v[56:59], %[l16], %[rs], %[b] offen sc1\n\t" \
    "s_mov_b64 exec, -1\n\t" \
    "s_add_u32 %[sx], %[sx], 65536\n\t" \
    "s_add_u32 %[t], %[t], 1\n\t" \
    "s_add_u32 %[oo], %[oo], 262144\n\t" \
    "s_waitcnt vmcnt(6)\n\t" \
    "s_cmp_gt_u32 %[t], " STR(SQUICK) " + 1\n\t" \
    "s_cbranch_scc1 B0_%=\n\t" \
    "s_waitcnt vmcnt(0)\n\t" \
    "B0_%=:\n\t" \
    "s_barrier\n\t" \
    "s_setprio 1\n\t" \
    "s_add_u32 m0, %[sd], 16384\n\t" \
    "s_add_u32 %[a], %[sx], 81920\n\t" \
    "v_mfma_f32_16x16x32_bf16 v[196:199], v[68:71], v[20:23], 0\n\t" \
    "ds_read_b128 v[44:47], %[xp] offset:38912\n\t" \
    "v_mfma_f32_16x16x32_bf16 v[200:203], v[76:79], v[20:23], 0\n\t" \
    "ds_read_b128 v[48:51], %[xp] offset:39936\n\t" \
    "v_mfma_f32_16x16x32_bf16 v[204:207], v[68:71], v[24:27], 0\n\t" \
    "s_waitcnt lgkmcnt(4)\n\t" \
    "v_mfma_f32_16x16x32_bf16 v[208:211], v[76:79], v[24:27], 0\n\t" \
    "v_mfma_f32_16x16x32_bf16 v[196:199], v[72:75], v[28:31], v[196:199]\n\t" \
    "ds_read_b128 v[20:23], %[xp] offset:40960\n\t" \
    "v_mfma_f32_16x16x32_bf16 v[200:203], v[80:83], v[28:31], v[200:203]\n\t" \
    "ds_read_b128 v[24:27], %[xp] offset:41984\n\t" \
    "v_mfma_f32_16x16x32_bf16 v[204:207], v[72:75], v[32:35], v[204:207]\n\t" \
    "s_waitcnt lgkmcnt(4)\n\t" \
    "v_mfma_f32_16x16x32_bf16 v[208:211], v[80:83], v[32:35], v[208:211]\n\t" \
    "v_mfma_f32_16x16x32_bf16 v[196:199], v[84:87], v[36:39], v[196:199]\n\t" \
    "ds_read_b128 v[28:31], %[xp] offset:43008\n\t" \
    "v_mfma_f32_16x16x32_bf16 v[200:203], v[92:95], v[36:39], v[200:203]\n\t" \
    "ds_read_b128 v[32:35], %[xp] offset:44032\n\t" \
    "v_mfma_f32_16x16x32_bf16 v[204:207], v[84:87], v[40:43], v[204:207]\n\t" \
    "s_waitcnt lgkmcnt(4)\n\t" \
    "v_mfma_f32_16x16x32_bf16 v[208:211], v[92:95], v[40:43], v[208:211]\n\t" \
    "v_mfma_f32_16x16x32_bf16 v[196:199], v[88:91], v[44:47], v[196:199]\n\t" \
    "ds_read_b128 v[36:39], %[xp] offset:45056\n\t" \
    "v_mfma_f32_16x16x32_bf16 v[200:203], v[96:99], v[44:47], v[200:203]\n\t" \
    "ds_read_b128 v[40:43], %[xp] offset:46080\n\t" \
    "v_mfma_f32_16x16x32_bf16 v[204:207], v[88:91], v[48:51], v[204:207]\n\t" \
    "s_waitcnt lgkmcnt(4)\n\t" \
    "v_mfma_f32_16x16x32_bf16 v[208:211], v[96:99], v[48:51], v[208:211]\n\t" \
    "v_mfma_f32_16x16x32_bf16 v[196:199], v[100:103], v[20:23], v[196:199]\n\t" \
    "ds_read_b128 v[44:47], %[xp] offset:47104\n\t" \
    "v_mfma_f32_16x16x32_bf16 v[200:203], v[108:111], v[20:23], v[200:203]\n\t" \
    "ds_read_b128 v[48:51], %[xp] offset:48128\n\t" \
    "v_mfma_f32_16x16x32_bf16 v[204:207], v[100:103], v[24:27], v[204:207]\n\t" \
    "s_waitcnt lgkmcnt(4)\n\t" \
    "v_mfma_f32_16x16x32_bf16 v[208:211], v[108:111], v[24:27], v[208:211]\n\t" \
    "buffer_load_dwordx4 %[l16], %[rs], %[a] offen offset:0 sc1 lds\n\t" \
    "v_mfma_f32_16x16x32_bf16 v[196:199], v[104:107], v[28:31], v[196:199]\n\t" \
    "ds_read_b128 v[20:23], %[xp] offset:49152\n\t" \
    "v_mfma_f32_16x16x32_bf16 v[200:203], v[112:115], v[28:31], v[200:203]\n\t" \
    "ds_read_b128 v[24:27], %[xp] offset:50176\n\t" \
    "v_mfma_f32_16x16x32_bf16 v[204:207], v[104:107], v[32:35], v[204:207]\n\t" \
    "s_waitcnt lgkmcnt(4)\n\t" \
    "v_mfma_f32_16x16x32_bf16 v[208:211], v[112:115], v[32:35], v[208:211]\n\t" \
    "buffer_load_dwordx4 %[l16], %[rs], %[a] offen offset:1024 sc1 lds\n\t" \
    "v_mfma_f32_16x16x32_bf16 v[196:199], v[116:119], v[36:39], v[196:199]\n\t" \
    "ds_read_b128 v[28:31], %[xp] offset:51200\n\t" \
    "v_mfma_f32_16x16x32_bf16 v[200:203], v[124:127], v[36:39], v[200:203]\n\t" \
    "ds_read_b128 v[32:35], %[xp] offset:52224\n\t" \
    "v_mfma_f32_16x16x32_bf16 v[204:207], v[116:119], v[40:43], v[204:207]\n\t" \
    "s_waitcnt lgkmcnt(4)\n\t" \
    "v_mfma_f32_16x16x32_bf16 v[208:211], v[124:127], v[40:43], v[208:211]\n\t" \
    "buffer_load_dwordx4 %[l16], %[rs], %[a] offen offset:2048 sc1 lds\n\t" \
    "v_mfma_f32_16x16x32_bf16 v[196:199], v[120:123], v[44:47], v[196:199]\n\t" \
    "ds_read_b128 v[36:39], %[xp] offset:53248\n\t" \
    "v_mfma_f32_16x16x32_bf16 v[200:203], v[128:131], v[44:47], v[200:203]\n\t" \
    "ds_read_b128 v[40:43], %[xp] offset:54272\n\t" \
    "v_mfma_f32_16x16x32_bf16 v[204:207], v[120:123], v[48:51], v[204:207]\n\t" \
    "s_waitcnt lgkmcnt(4)\n\t" \
    "v_mfma_f32_16x16x32_bf16 v[208:211], v[128:131], v[48:51], v[208:211]\n\t" \
    "buffer_load_dwordx4 %[l16], %[rs], %[a] offen offset:3072 sc1 lds\n\t" \
    "s_waitcnt vmcnt(4)\n\t" \
    "s_barrier\n\t" \
    "s_setprio 1\n\t" \
    "s_add_u32 m0, %[sd], 32768\n\t" \
    "s_add_u32 %[a], %[sx], 131072\n\t" \
    "s_add_u32 %[b], %[oo], 0x1000\n\t" \
    "s_cmp_lg_u32 %[t], 1\n\t" \
    "s_cselect_b64 %[em], -1, 0\n\t" \
    "v_mfma_f32_16x16x32_bf16 v[196:199], v[132:135], v[20:23], v[196:199]\n\t" \
    "ds_read_b128 v[44:47], %[xp] offset:55296\n\t" \
    "v_mfma_f32_16x16x32_bf16 v[200:203], v[140:143], v[20:23], v[200:203]\n\t" \
    "ds_read_b128 v[48:51], %[xp] offset:56320\n\t" \
    "v_mfma_f32_16x16x32_bf16 v[204:207], v[132:135], v[24:27], v[204:207]\n\t" \
    "ds_read_b128 v[52:55], %[pa] offset:0\n\t" \
    "s_waitcnt lgkmcnt(5)\n\t" \
    "v_mfma_f32_16x16x32_bf16 v[208:211], v[140:143], v[24:27], v[208:211]\n\t" \
    "buffer_load_dwordx4 %[l16], %[rs], %[a] offen offset:0 sc1 lds\n\t" \
    "v_mfma_f32_16x16x32_bf16 v[196:199], v[136:139], v[28:31], v[196:199]\n\t" \
    "ds_read_b128 v[20:23], %[xp] offset:57344\n\t" \
    "v_mfma_f32_16x16x32_bf16 v[200:203], v[144:147], v[28:31], v[200:203]\n\t" \
    "ds_read_b128 v[24:27], %[xp] offset:58368\n\t" \
    "v_mfma_f32_16x16x32_bf16 v[204:207], v[136:139], v[32:35], v[204:207]\n\t" \
    "ds_read_b128 v[56:59], %[pa] offset:1024\n\t" \
    "s_waitcnt lgkmcnt(6)\n\t" \
    "v_mfma_f32_16x16x32_bf16 v[208:211], v[144:147], v[32:35], v[208:211]\n\t" \
    "buffer_load_dwordx4 %[l16], %[rs], %[a] offen offset:1024 sc1 lds\n\t" \
    "v_mfma_f32_16x16x32_bf16 v[196:199], v[148:151], v[36:39], v[196:199]\n\t" \
    "ds_read_b128 v[28:31], %[xp] offset:59392\n\t" \
    "v_mfma_f32_16x16x32_bf16 v[200:203], v[156:159], v[36:39], v[200:203]\n\t" \
    "ds_read_b128 v[32:35], %[xp] offset:60416\n\t" \
    "v_mfma_f32_16x16x32_bf16 v[204:207], v[148:151], v[40:43], v[204:207]\n\t" \
    "ds_read_b128 v[60:63], %[pa] offset:2048\n\t" \
    "s_waitcnt lgkmcnt(7)\n\t" \
    "v_mfma_f32_16x16x32_bf16 v[208:211], v[156:159], v[40:43], v[208:211]\n\t" \
    "buffer_load_dwordx4 %[l16], %[rs], %[a] offen offset:2048 sc1 lds\n\t" \
    "v_mfma_f32_16x16x32_bf16 v[196:199], v[152:155], v[44:47], v[196:199]\n\t" \
    "ds_read_b128 v[36:39], %[xp] offset:61440\n\t" \
    "s_waitcnt lgkmcnt(7)\n\t" \
    "v_add_f32 v0, v0, v52\n\t" \
    "v_mfma_f32_16x16x32_bf16 v[200:203], v[160:163], v[44:47], v[200:203]\n\t" \
    "ds_read_b128 v[40:43], %[xp] offset:62464\n\t" \
    "v_add_f32 v1, v1, v53\n\t" \
    "v_mfma_f32_16x16x32_bf16 v[204:207], v[152:155], v[48:51], v[204:207]\n\t" \
    "ds_read_b128 v[64:67], %[pa] offset:3072\n\t" \
    "s_waitcnt lgkmcnt(7)\n\t" \
    "v_mfma_f32_16x16x32_bf16 v[208:211], v[160:163], v[48:51], v[208:211]\n\t" \
    "buffer_load_dwordx4 %[l16], %[rs], %[a] offen offset:3072 sc1 lds\n\t" \
    "v_add_f32 v2, v2, v54\n\t" \
    "v_add_f32 v3, v3, v55\n\t" \
    "v_mfma_f32_16x16x32_bf16 v[196:199], v[164:167], v[20:23], v[196:199]\n\t" \
    "ds_read_b128 v[44:47], %[xp] offset:63488\n\t" \
    "s_waitcnt lgkmcnt(7)\n\t" \
    "v_add_f32 v4, v4, v56\n\t" \
    "v_mfma_f32_16x16x32_bf16 v[200:203], v[172:175], v[20:23], v[200:203]\n\t" \
    "ds_read_b128 v[48:51], %[xp] offset:64512\n\t" \
    "v_add_f32 v5, v5, v57\n\t" \
    "v_mfma_f32_16x16x32_bf16 v[204:207], v[164:167], v[24:27], v[204:207]\n\t" \
    "v_add_f32 v6, v6, v58\n\t" \
    "s_waitcnt lgkmcnt(6)\n\t" \
    "v_mfma_f32_16x16x32_bf16 v[208:211], v[172:175], v[24:27], v[208:211]\n\t" \
    "v_add_f32 v7, v7, v59\n\t" \
    "s_waitcnt lgkmcnt(5)\n\t" \
    "v_add_f32 v8, v8, v60\n\t" \
    "v_add_f32 v9, v9, v61\n\t" \
    "v_mfma_f32_16x16x32_bf16 v[196:199], v[168:171], v[28:31], v[196:199]\n\t" \
    "ds_read_b128 v[20:23], %[xp] offset:0\n\t" \
    "v_add_f32 v10, v10, v62\n\t" \
    "v_mfma_f32_16x16x32_bf16 v[200:203], v[176:179], v[28:31], v[200:203]\n\t" \
    "ds_read_b128 v[24:27], %[xp] offset:1024\n\t" \
    "v_add_f32 v11, v11, v63\n\t" \
    "v_mfma_f32_16x16x32_bf16 v[204:207], v[168:171], v[32:35], v[204:207]\n\t" \
    "s_waitcnt lgkmcnt(4)\n\t" \
    "v_add_f32 v12, v12, v64\n\t" \
    "v_mfma_f32_16x16x32_bf16 v[208:211], v[176:179], v[32:35], v[208:211]\n\t" \
    "v_add_f32 v13, v13, v65\n\t" \
    "v_add_f32 v14, v14, v66\n\t" \
    "v_add_f32 v15, v15, v67\n\t" \
    "v_mfma_f32_16x16x32_bf16 v[196:199], v[180:183], v[36:39], v[196:199]\n\t" \
    "ds_read_b128 v[28:31], %[xp] offset:2048\n\t" \
    "v_cvt_pk_bf16_f32 v52, v0, v4\n\t" \
    "v_mfma_f32_16x16x32_bf16 v[200:203], v[188:191], v[36:39], v[200:203]\n\t" \
    "ds_read_b128 v[32:35], %[xp] offset:3072\n\t" \
    "v_cvt_pk_bf16_f32 v53, v1, v5\n\t" \
    "v_mfma_f32_16x16x32_bf16 v[204:207], v[180:183], v[40:43], v[204:207]\n\t" \
    "v_cvt_pk_bf16_f32 v54, v2, v6\n\t" \
    "s_waitcnt lgkmcnt(4)\n\t" \
    "v_mfma_f32_16x16x32_bf16 v[208:211], v[188:191], v[40:43], v[208:211]\n\t" \
    "v_cvt_pk_bf16_f32 v55, v3, v7\n\t" \
    "v_cvt_pk_bf16_f32 v56, v8, v12\n\t" \
    "v_cvt_pk_bf16_f32 v57, v9, v13\n\t" \
    "v_mfma_f32_16x16x32_bf16 v[196:199], v[184:187], v[44:47], v[196:199]\n\t" \
    "ds_read_b128 v[36:39], %[xp] offset:4096\n\t" \
    "v_cvt_pk_bf16_f32 v58, v10, v14\n\t" \
    "v_mfma_f32_16x16x32_bf16 v[200:203], v[192:195], v[44:47], v[200:203]\n\t" \
    "ds_read_b128 v[40:43], %[xp] offset:5120\n\t" \
    "v_cvt_pk_bf16_f32 v59, v11, v15\n\t" \
    "v_mfma_f32_16x16x32_bf16 v[204:207], v[184:187], v[48:51], v[204:207]\n\t" \
    "s_waitcnt lgkmcnt(4)\n\t" \
    "v_mfma_f32_16x16x32_bf16 v[208:211], v[192:195], v[48:51], v[208:211]\n\t" \
    "s_mov_b64 exec, %[em]\n\t" \
    "buffer_store_dwordx4 v[52:55], %[l16], %[rs], %[oo] offen sc1\n\t" \
    "buffer_store_dwordx4 v[56:59], %[l16], %[rs], %[b] offen sc1\n\t" \
    "s_mov_b64 exec, -1\n\t" \
    "s_add_u32 %[sx], %[sx], 65536\n\t" \
    "s_add_u32 %[t], %[t], 1\n\t" \
    "s_add_u32 %[oo], %[oo], 262144\n\t" \
    "s_waitcnt vmcnt(6)\n\t" \
    "s_cmp_gt_u32 %[t], " STR(SQUICK) " + 1\n\t" \
    "s_cbranch_scc1 B1_%=\n\t" \
    "s_waitcnt vmcnt(0)\n\t" \
    "B1_%=:\n\t" \
    "s_barrier\n\t" \
    "s_cmp_lt_u32 %[t], %[te]\n\t" \
    "s_cbranch_scc1 L1_%=\n\t" \
    "s_cmp_eq_u32 %[fin], %[te]\n\t" \
    "s_cbranch_scc0 E_%=\n\t" \
    "s_nop 4\n\t" \
    "s_barrier\n\t" \
    "ds_read_b128 v[52:55], %[pa] offset:0\n\t" \
    "ds_read_b128 v[56:59], %[pa] offset:1024\n\t" \
    "ds_read_b128 v[60:63], %[pa] offset:2048\n\t" \
    "ds_read_b128 v[64:67], %[pa] offset:3072\n\t" \
    "s_add_u32 %[b], %[oo], 0x1000\n\t" \
    "s_waitcnt lgkmcnt(0)\n\t" \
    "v_add_f32 v196, v196, v52\n\t" \
    "v_add_f32 v197, v197, v53\n\t" \
    "v_add_f32 v198, v198, v54\n\t" \
    "v_add_f32 v199, v199, v55\n\t" \
    "v_add_f32 v200, v200, v56\n\t" \
    "v_add_f32 v201, v201, v57\n\t" \
    "v_add_f32 v202, v202, v58\n\t" \
    "v_add_f32 v203, v203, v59\n\t" \
    "v_add_f32 v204, v204, v60\n\t" \
    "v_add_f32 v205, v205, v61\n\t" \
    "v_add_f32 v206, v206, v62\n\t" \
    "v_add_f32 v207, v207, v63\n\t" \
    "v_add_f32 v208, v208, v64\n\t" \
    "v_add_f32 v209, v209, v65\n\t" \
    "v_add_f32 v210, v210, v66\n\t" \
    "v_add_f32 v211, v211, v67\n\t" \
    "v_cvt_pk_bf16_f32 v52, v196, v200\n\t" \
    "v_cvt_pk_bf16_f32 v53, v197, v201\n\t" \
    "v_cvt_pk_bf16_f32 v54, v198, v202\n\t" \
    "v_cvt_pk_bf16_f32 v55, v199, v203\n\t" \
    "v_cvt_pk_bf16_f32 v56, v204, v208\n\t" \
    "v_cvt_pk_bf16_f32 v57, v205, v209\n\t" \
    "v_cvt_pk_bf16_f32 v58, v206, v210\n\t" \
    "v_cvt_pk_bf16_f32 v59, v207, v211\n\t" \
    "buffer_store_dwordx4 v[52:55], %[l16], %[rs], %[oo] offen sc1\n\t" \
    "buffer_store_dwordx4 v[56:59], %[l16], %[rs], %[b] offen sc1\n\t" \
    "E_%=:\n\t" \
    "s_setprio 0\n\t" \
    "s_waitcnt lgkmcnt(0)\n\t" \
    "s_nop 7\n\t" \
    : [t] "+s"(t), [sx] "+s"(sx), [oo] "+s"(oo), [a] "=&s"(tmp), [b] "=&s"(tmp2), [em] "=&s"(em), "+{v[20:27]}"(xf[0]), "+{v[28:35]}"(xf[1]), "+{v[36:43]}"(xf[2]), "+{v[196:203]}"(ac[0]), "+{v[204:211]}"(ac[1]) \
    : [te] "s"(TE_), [fin] "s"(FIN_), [kh] "s"(kh), [sd] "s"(sd), [rs] "s"(rs), [l16] "v"(l16), [xp] "v"(xp), [pa] "v"(pa), [fm] "s"(fm), [pm] "s"(pm), [fo] "s"(fo), [po] "s"(po), [pd] "s"(pd), [tag] "s"(P.tag), "{v[68:75]}"(w[0][0]), "{v[76:83]}"(w[0][1]), "{v[84:91]}"(w[1][0]), "{v[92:99]}"(w[1][1]), "{v[100:107]}"(w[2][0]), "{v[108:115]}"(w[2][1]), "{v[116:123]}"(w[3][0]), "{v[124:131]}"(w[3][1]), "{v[132:139]}"(w[4][0]), "{v[140:147]}"(w[4][1]), "{v[148:155]}"(w[5][0]), "{v[156:163]}"(w[5][1]), "{v[164:171]}"(w[6][0]), "{v[172:179]}"(w[6][1]), "{v[180:187]}"(w[7][0]), "{v[188:195]}"(w[7][1]) \
    : "memory", "scc", "v0", "v1", "v2", "v3", "v4", "v5", "v6", "v7", "v8", "v9", "v10", "v11", "v12", "v13", "v14", "v15", "v44", "v45", "v46", "v47", "v48", "v49", "v50", "v51", "v52", "v53", "v54", "v55", "v56", "v57", "v58", "v59", "v60", "v61", "v62", "v63", "v64", "v65", "v66", "v67")

#define S_LOOP_V(TE_, FIN_) \
  asm volatile( \
    "s_cmp_eq_u32 %[kh], 0\n\t" \
    "s_cbranch_scc0 L1_%=\n\t" \
    "L0_%=:\n\t" \
    "s_setprio 0\n\t" \
    "s_nop 3\n\t" \
    "ds_write_b128 %[pa], v[196:199] offset:0\n\t" \
    "ds_write_b128 %[pa], v[200:203] offset:1024\n\t" \
    "ds_write_b128 %[pa], v[204:207] offset:2048\n\t" \
    "ds_write_b128 %[pa], v[208:211] offset:3072\n\t" \
    "s_sub_u32 %[a], %[t], 1\n\t" \
    "v_mov_b32 v53, %[tag]\n\t" \
    "v_mov_b32 v52, %[a]\n\t" \
    "s_sub_u32 %[b], %[t], 2\n\t" \
    "s_cmp_lt_u32 %[b], " STR(SQUICK) "\n\t" \
    "s_cselect_b64 exec, %[fm], 0\n\t" \
    "buffer_store_dwordx2 v[52:53], %[l16], %[rs], %[fo] offen sc1\n\t" \
    "s_mov_b64 exec, -1\n\t" \
    "s_add_u32 m0, %[sd], 49152\n\t" \
    "s_add_u32 %[a], %[sx], 81920\n\t" \
    "s_waitcnt lgkmcnt(0)\n\t" \
    "buffer_load_dwordx4 %[l16], %[rs], %[a] offen offset:0 sc1 lds\n\t" \
    "buffer_load_dwordx4 %[l16], %[rs], %[a] offen offset:1024 sc1 lds\n\t" \
    "buffer_load_dwordx4 %[l16], %[rs], %[a] offen offset:2048 sc1 lds\n\t" \
    "buffer_load_dwordx4 %[l16], %[rs], %[a] offen offset:3072 sc1 lds\n\t" \
    "v_mfma_f32_16x16x32_bf16 v[0:3], v[20:23], v[68:71], 0\n\t" \
    "ds_read_b128 v[44:47], %[xp] offset:6144\n\t" \
    "v_mfma_f32_16x16x32_bf16 v[4:7], v[20:23], v[76:79], 0\n\t" \
    "ds_read_b128 v[48:51], %[xp] offset:7168\n\t" \
    "v_mfma_f32_16x16x32_bf16 v[8:11], v[24:27], v[68:71], 0\n\t" \
    "v_mfma_f32_16x16x32_bf16 v[12:15], v[24:27], v[76:79], 0\n\t" \
    "v_mfma_f32_16x16x32_bf16 v[0:3], v[28:31], v[72:75], v[0:3]\n\t" \
    "ds_read_b128 v[20:23], %[xp] offset:8192\n\t" \
    "v_mfma_f32_16x16x32_bf16 v[4:7], v[28:31], v[80:83], v[4:7]\n\t" \
    "ds_read_b128 v[24:27], %[xp] offset:9216\n\t" \
    "v_mfma_f32_16x16x32_bf16 v[8:11], v[32:35], v[72:75], v[8:11]\n\t" \
    "v_mfma_f32_16x16x32_bf16 v[12:15], v[32:35], v[80:83], v[12:15]\n\t" \
    "v_mfma_f32_16x16x32_bf16 v[0:3], v[36:39], v[84:87], v[0:3]\n\t" \
    "ds_read_b128 v[28:31], %[xp] offset:10240\n\t" \
    "v_mfma_f32_16x16x32_bf16 v[4:7], v[36:39], v[92:95], v[4:7]\n\t" \
    "ds_read_b128 v[32:35], %[xp] offset:11264\n\t" \
    "v_mfma_f32_16x16x32_bf16 v[8:11], v[40:43], v[84:87], v[8:11]\n\t" \
    "s_waitcnt lgkmcnt(4)\n\t" \
    "v_mfma_f32_16x16x32_bf16 v[12:15], v[40:43], v[92:95], v[12:15]\n\t" \
    "v_mfma_f32_16x16x32_bf16 v[0:3], v[44:47], v[88:91], v[0:3]\n\t" \
    "ds_read_b128 v[36:39], %[xp] offset:12288\n\t" \
    "v_mfma_f32_16x16x32_bf16 v[4:7], v[44:47], v[96:99], v[4:7]\n\t" \
    "ds_read_b128 v[40:43], %[xp] offset:13312\n\t" \
    "v_mfma_f32_16x16x32_bf16 v[8:11], v[48:51], v[88:91], v[8:11]\n\t" \
    "s_waitcnt lgkmcnt(4)\n\t" \
    "v_mfma_f32_16x16x32_bf16 v[12:15], v[48:51], v[96:99], v[12:15]\n\t" \
    "v_mfma_f32_16x16x32_bf16 v[0:3], v[20:23], v[100:103], v[0:3]\n\t" \
    "ds_read_b128 v[44:47], %[xp] offset:14336\n\t" \
    "v_mfma_f32_16x16x32_bf16 v[4:7], v[20:23], v[108:111], v[4:7]\n\t" \
    "ds_read_b128 v[48:51], %[xp] offset:15360\n\t" \
    "v_mfma_f32_16x16x32_bf16 v[8:11], v[24:27], v[100:103], v[8:11]\n\t" \
    "s_waitcnt lgkmcnt(4)\n\t" \
    "v_mfma_f32_16x16x32_bf16 v[12:15], v[24:27], v[108:111], v[12:15]\n\t" \
    "v_mfma_f32_16x16x32_bf16 v[0:3], v[28:31], v[104:107], v[0:3]\n\t" \
    "ds_read_b128 v[20:23], %[xp] offset:16384\n\t" \
    "v_mfma_f32_16x16x32_bf16 v[4:7], v[28:31], v[112:115], v[4:7]\n\t" \
    "ds_read_b128 v[24:27], %[xp] offset:17408\n\t" \
    "v_mfma_f32_16x16x32_bf16 v[8:11], v[32:35], v[104:107], v[8:11]\n\t" \
    "s_waitcnt lgkmcnt(4)\n\t" \
    "v_mfma_f32_16x16x32_bf16 v[12:15], v[32:35], v[112:115], v[12:15]\n\t" \
    "v_mfma_f32_16x16x32_bf16 v[0:3], v[36:39], v[116:119], v[0:3]\n\t" \
    "ds_read_b128 v[28:31], %[xp] offset:18432\n\t" \
    "v_mfma_f32_16x16x32_bf16 v[4:7], v[36:39], v[124:127], v[4:7]\n\t" \
    "ds_read_b128 v[32:35], %[xp] offset:19456\n\t" \
    "v_mfma_f32_16x16x32_bf16 v[8:11], v[40:43], v[116:119], v[8:11]\n\t" \
    "s_waitcnt lgkmcnt(4)\n\t" \
    "v_mfma_f32_16x16x32_bf16 v[12:15], v[40:43], v[124:127], v[12:15]\n\t" \
    "v_mfma_f32_16x16x32_bf16 v[0:3], v[44:47], v[120:123], v[0:3]\n\t" \
    "ds_read_b128 v[36:39], %[xp] offset:20480\n\t" \
    "v_mfma_f32_16x16x32_bf16 v[4:7], v[44:47], v[128:131], v[4:7]\n\t" \
    "ds_read_b128 v[40:43], %[xp] offset:21504\n\t" \
    "v_mfma_f32_16x16x32_bf16 v[8:11], v[48:51], v[120:123], v[8:11]\n\t" \
    "s_waitcnt lgkmcnt(4)\n\t" \
    "v_mfma_f32_16x16x32_bf16 v[12:15], v[48:51], v[128:131], v[12:15]\n\t" \
    "s_waitcnt vmcnt(4)\n\t" \
    "s_barrier\n\t" \
    "s_setprio 0\n\t" \
    "s_sub_u32 %[a], %[t], 1\n\t" \
    "v_mov_b32 v53, %[tag]\n\t" \
    "v_mov_b32 v52, %[a]\n\t" \
    "s_cmp_gt_u32 %[t], 1\n\t" \
    "s_cselect_b64 exec, %[fm], 0\n\t" \
    "buffer_store_dwordx2 v[52:53], %[l16], %[rs], %[fo] offen sc1\n\t" \
    "s_mov_b64 exec, %[pm]\n\t" \
    "s_mov_b32 m0, %[pd]\n\t" \
    "s_add_u32 %[a], %[sx], %[po]\n\t" \
    "buffer_load_dwordx4 %[l16], %[rs], %[a] offen sc1 lds\n\t" \
    "s_mov_b64 exec, -1\n\t" \
    "s_add_u32 m0, %[sd], 0\n\t" \
    "s_add_u32 %[a], %[sx], 131072\n\t" \
    "buffer_load_dwordx4 %[l16], %[rs], %[a] offen offset:0 sc1 lds\n\t" \
    "buffer_load_dwordx4 %[l16], %[rs], %[a] offen offset:1024 sc1 lds\n\t" \
    "buffer_load_dwordx4 %[l16], %[rs], %[a] offen offset:2048 sc1 lds\n\t" \
    "buffer_load_dwordx4 %[l16], %[rs], %[a] offen offset:3072 sc1 lds\n\t" \
    "v_mfma_f32_16x16x32_bf16 v[0:3], v[20:23], v[132:135], v[0:3]\n\t" \
    "ds_read_b128 v[44:47], %[xp] offset:22528\n\t" \
    "v_mfma_f32_16x16x32_bf16 v[4:7], v[20:23], v[140:143], v[4:7]\n\t" \
    "ds_read_b128 v[48:51], %[xp] offset:23552\n\t" \
    "v_mfma_f32_16x16x32_bf16 v[8:11], v[24:27], v[132:135], v[8:11]\n\t" \
    "s_waitcnt lgkmcnt(4)\n\t" \
    "v_mfma_f32_16x16x32_bf16 v[12:15], v[24:27], v[140:143], v[12:15]\n\t" \
    "v_mfma_f32_16x16x32_bf16 v[0:3], v[28:31], v[136:139], v[0:3]\n\t" \
    "ds_read_b128 v[20:23], %[xp] offset:24576\n\t" \
    "v_mfma_f32_16x16x32_bf16 v[4:7], v[28:31], v[144:147], v[4:7]\n\t" \
    "ds_read_b128 v[24:27], %[xp] offset:25600\n\t" \
    "v_mfma_f32_16x16x32_bf16 v[8:11], v[32:35], v[136:139], v[8:11]\n\t" \
    "s_waitcnt lgkmcnt(4)\n\t" \
    "v_mfma_f32_16x16x32_bf16 v[12:15], v[32:35], v[144:147], v[12:15]\n\t" \
    "v_mfma_f32_16x16x32_bf16 v[0:3], v[36:39], v[148:151], v[0:3]\n\t" \
    "ds_read_b128 v[28:31], %[xp] offset:26624\n\t" \
    "v_mfma_f32_16x16x32_bf16 v[4:7], v[36:39], v[156:159], v[4:7]\n\t" \
    "ds_read_b128 v[32:35], %[xp] offset:27648\n\t" \
    "v_mfma_f32_16x16x32_bf16 v[8:11], v[40:43], v[148:151], v[8:11]\n\t" \
    "s_waitcnt lgkmcnt(4)\n\t" \
    "v_mfma_f32_16x16x32_bf16 v[12:15], v[40:43], v[156:159], v[12:15]\n\t" \
    "v_mfma_f32_16x16x32_bf16 v[0:3], v[44:47], v[152:155], v[0:3]\n\t" \
    "ds_read_b128 v[36:39], %[xp] offset:28672\n\t" \
    "v_mfma_f32_16x16x32_bf16 v[4:7], v[44:47], v[160:163], v[4:7]\n\t" \
    "ds_read_b128 v[40:43], %[xp] offset:29696\n\t" \
    "v_mfma_f32_16x16x32_bf16 v[8:11], v[48:51], v[152:155], v[8:11]\n\t" \
    "s_waitcnt lgkmcnt(4)\n\t" \
    "v_mfma_f32_16x16x32_bf16 v[12:15], v[48:51], v[160:163], v[12:15]\n\t" \
    "v_mfma_f32_16x16x32_bf16 v[0:3], v[20:23], v[164:167], v[0:3]\n\t" \
    "ds_read_b128 v[44:47], %[xp] offset:30720\n\t" \
    "v_mfma_f32_16x16x32_bf16 v[4:7], v[20:23], v[172:175], v[4:7]\n\t" \
    "ds_read_b128 v[48:51], %[xp] offset:31744\n\t" \
    "v_mfma_f32_16x16x32_bf16 v[8:11], v[24:27], v[164:167], v[8:11]\n\t" \
    "s_waitcnt lgkmcnt(4)\n\t" \
    "v_mfma_f32_16x16x32_bf16 v[12:15], v[24:27], v[172:175], v[12:15]\n\t" \
    "v_mfma_f32_16x16x32_bf16 v[0:3], v[28:31], v[168:171], v[0:3]\n\t" \
    "ds_read_b128 v[20:23], %[xp] offset:32768\n\t" \
    "v_mfma_f32_16x16x32_bf16 v[4:7], v[28:31], v[176:179], v[4:7]\n\t" \
    "ds_read_b128 v[24:27], %[xp] offset:33792\n\t" \
    "v_mfma_f32_16x16x32_bf16 v[8:11], v[32:35], v[168:171], v[8:11]\n\t" \
    "s_waitcnt lgkmcnt(4)\n\t" \
    "v_mfma_f32_16x16x32_bf16 v[12:15], v[32:35], v[176:179], v[12:15]\n\t" \
    "v_mfma_f32_16x16x32_bf16 v[0:3], v[36:39], v[180:183], v[0:3]\n\t" \
    "ds_read_b128 v[28:31], %[xp] offset:34816\n\t" \
    "v_mfma_f32_16x16x32_bf16 v[4:7], v[36:39], v[188:191], v[4:7]\n\t" \
    "ds_read_b128 v[32:35], %[xp] offset:35840\n\t" \
    "v_mfma_f32_16x16x32_bf16 v[8:11], v[40:43], v[180:183], v[8:11]\n\t" \
    "s_waitcnt lgkmcnt(4)\n\t" \
    "v_mfma_f32_16x16x32_bf16 v[12:15], v[40:43], v[188:191], v[12:15]\n\t" \
    "v_mfma_f32_16x16x32_bf16 v[0:3], v[44:47], v[184:187], v[0:3]\n\t" \
    "ds_read_b128 v[36:39], %[xp] offset:36864\n\t" \
    "v_mfma_f32_16x16x32_bf16 v[4:7], v[44:47], v[192:195], v[4:7]\n\t" \
    "ds_read_b128 v[40:43], %[xp] offset:37888\n\t" \
    "v_mfma_f32_16x16x32_bf16 v[8:11], v[48:51], v[184:187], v[8:11]\n\t" \
    "s_waitcnt lgkmcnt(4)\n\t" \
    "v_mfma_f32_16x16x32_bf16 v[12:15], v[48:51], v[192:195], v[12:15]\n\t" \
    "s_add_u32 %[sx], %[sx], 65536\n\t" \
    "s_add_u32 %[t], %[t], 1\n\t" \
    "s_waitcnt vmcnt(4)\n\t" \
    "s_barrier\n\t" \
    "s_setprio 0\n\t" \
    "s_nop 3\n\t" \
    "ds_write_b128 %[pa], v[0:3] offset:0\n\t" \
    "ds_write_b128 %[pa], v[4:7] offset:1024\n\t" \
    "ds_write_b128 %[pa], v[8:11] offset:2048\n\t" \
    "ds_write_b128 %[pa], v[12:15] offset:3072\n\t" \
    "s_sub_u32 %[a], %[t], 1\n\t" \
    "v_mov_b32 v53, %[tag]\n\t" \
    "v_mov_b32 v52, %[a]\n\t" \
    "s_sub_u32 %[b], %[t], 2\n\t" \
    "s_cmp_lt_u32 %[b], " STR(SQUICK) "\n\t" \
    "s_cselect_b64 exec, %[fm], 0\n\t" \
    "buffer_store_dwordx2 v[52:53], %[l16], %[rs], %[fo] offen sc1\n\t" \
    "s_mov_b64 exec, -1\n\t" \
    "s_add_u32 m0, %[sd], 16384\n\t" \
    "s_add_u32 %[a], %[sx], 81920\n\t" \
    "s_waitcnt lgkmcnt(0)\n\t" \
    "buffer_load_dwordx4 %[l16], %[rs], %[a] offen offset:0 sc1 lds\n\t" \
    "buffer_load_dwordx4 %[l16], %[rs], %[a] offen offset:1024 sc1 lds\n\t" \
    "buffer_load_dwordx4 %[l16], %[rs], %[a] offen offset:2048 sc1 lds\n\t" \
    "buffer_load_dwordx4 %[l16], %[rs], %[a] offen offset:3072 sc1 lds\n\t" \
    "v_mfma_f32_16x16x32_bf16 v[196:199], v[20:23], v[68:71], 0\n\t" \
    "ds_read_b128 v[44:47], %[xp] offset:38912\n\t" \
    "v_mfma_f32_16x16x32_bf16 v[200:203], v[20:23], v[76:79], 0\n\t" \
    "ds_read_b128 v[48:51], %[xp] offset:39936\n\t" \
    "v_mfma_f32_16x16x32_bf16 v[204:207], v[24:27], v[68:71], 0\n\t" \
    "v_mfma_f32_16x16x32_bf16 v[208:211], v[24:27], v[76:79], 0\n\t" \
    "v_mfma_f32_16x16x32_bf16 v[196:199], v[28:31], v[72:75], v[196:199]\n\t" \
    "ds_read_b128 v[20:23], %[xp] offset:40960\n\t" \
    "v_mfma_f32_16x16x32_bf16 v[200:203], v[28:31], v[80:83], v[200:203]\n\t" \
    "ds_read_b128 v[24:27], %[xp] offset:41984\n\t" \
    "v_mfma_f32_16x16x32_bf16 v[204:207], v[32:35], v[72:75], v[204:207]\n\t" \
    "v_mfma_f32_16x16x32_bf16 v[208:211], v[32:35], v[80:83], v[208:211]\n\t" \
    "v_mfma_f32_16x16x32_bf16 v[196:199], v[36:39], v[84:87], v[196:199]\n\t" \
    "ds_read_b128 v[28:31], %[xp] offset:43008\n\t" \
    "v_mfma_f32_16x16x32_bf16 v[200:203], v[36:39], v[92:95], v[200:203]\n\t" \
    "ds_read_b128 v[32:35], %[xp] offset:44032\n\t" \
    "v_mfma_f32_16x16x32_bf16 v[204:207], v[40:43], v[84:87], v[204:207]\n\t" \
    "s_waitcnt lgkmcnt(4)\n\t" \
    "v_mfma_f32_16x16x32_bf16 v[208:211], v[40:43], v[92:95], v[208:211]\n\t" \
    "v_mfma_f32_16x16x32_bf16 v[196:199], v[44:47], v[88:91], v[196:199]\n\t" \
    "ds_read_b128 v[36:39], %[xp] offset:45056\n\t" \
    "v_mfma_f32_16x16x32_bf16 v[200:203], v[44:47], v[96:99], v[200:203]\n\t" \
    "ds_read_b128 v[40:43], %[xp] offset:46080\n\t" \
    "v_mfma_f32_16x16x32_bf16 v[204:207], v[48:51], v[88:91], v[204:207]\n\t" \
    "s_waitcnt lgkmcnt(4)\n\t" \
    "v_mfma_f32_16x16x32_bf16 v[208:211], v[48:51], v[96:99], v[208:211]\n\t" \
    "v_mfma_f32_16x16x32_bf16 v[196:199], v[20:23], v[100:103], v[196:199]\n\t" \
    "ds_read_b128 v[44:47], %[xp] offset:47104\n\t" \
    "v_mfma_f32_16x16x32_bf16 v[200:203], v[20:23], v[108:111], v[200:203]\n\t" \
    "ds_read_b128 v[48:51], %[xp] offset:48128\n\t" \
    "v_mfma_f32_16x16x32_bf16 v[204:207], v[24:27], v[100:103], v[204:207]\n\t" \
    "s_waitcnt lgkmcnt(4)\n\t" \
    "v_mfma_f32_16x16x32_bf16 v[208:211], v[24:27], v[108:111], v[208:211]\n\t" \
    "v_mfma_f32_16x16x32_bf16 v[196:199], v[28:31], v[104:107], v[196:199]\n\t" \
    "ds_read_b128 v[20:23], %[xp] offset:49152\n\t" \
    "v_mfma_f32_16x16x32_bf16 v[200:203], v[28:31], v[112:115], v[200:203]\n\t" \
    "ds_read_b128 v[24:27], %[xp] offset:50176\n\t" \
    "v_mfma_f32_16x16x32_bf16 v[204:207], v[32:35], v[104:107], v[204:207]\n\t" \
    "s_waitcnt lgkmcnt(4)\n\t" \
    "v_mfma_f32_16x16x32_bf16 v[208:211], v[32:35], v[112:115], v[208:211]\n\t" \
    "v_mfma_f32_16x16x32_bf16 v[196:199], v[36:39], v[116:119], v[196:199]\n\t" \
    "ds_read_b128 v[28:31], %[xp] offset:51200\n\t" \
    "v_mfma_f32_16x16x32_bf16 v[200:203], v[36:39], v[124:127], v[200:203]\n\t" \
    "ds_read_b128 v[32:35], %[xp] offset:52224\n\t" \
    "v_mfma_f32_16x16x32_bf16 v[204:207], v[40:43], v[116:119], v[204:207]\n\t" \
    "s_waitcnt lgkmcnt(4)\n\t" \
    "v_mfma_f32_16x16x32_bf16 v[208:211], v[40:43], v[124:127], v[208:211]\n\t" \
    "v_mfma_f32_16x16x32_bf16 v[196:199], v[44:47], v[120:123], v[196:199]\n\t" \
    "ds_read_b128 v[36:39], %[xp] offset:53248\n\t" \
    "v_mfma_f32_16x16x32_bf16 v[200:203], v[44:47], v[128:131], v[200:203]\n\t" \
    "ds_read_b128 v[40:43], %[xp] offset:54272\n\t" \
    "v_mfma_f32_16x16x32_bf16 v[204:207], v[48:51], v[120:123], v[204:207]\n\t" \
    "s_waitcnt lgkmcnt(4)\n\t" \
    "v_mfma_f32_16x16x32_bf16 v[208:211], v[48:51], v[128:131], v[208:211]\n\t" \
    "s_waitcnt vmcnt(4)\n\t" \
    "s_barrier\n\t" \
    "s_setprio 0\n\t" \
    "s_sub_u32 %[a], %[t], 1\n\t" \
    "v_mov_b32 v53, %[tag]\n\t" \
    "v_mov_b32 v52, %[a]\n\t" \
    "s_cmp_gt_u32 %[t], 1\n\t" \
    "s_cselect_b64 exec, %[fm], 0\n\t" \
    "buffer_store_dwordx2 v[52:53], %[l16], %[rs], %[fo] offen sc1\n\t" \
    "s_mov_b64 exec, %[pm]\n\t" \
    "s_mov_b32 m0, %[pd]\n\t" \
    "s_add_u32 %[a], %[sx], %[po]\n\t" \
    "buffer_load_dwordx4 %[l16], %[rs], %[a] offen sc1 lds\n\t" \
    "s_mov_b64 exec, -1\n\t" \
    "s_add_u32 m0, %[sd], 32768\n\t" \
    "s_add_u32 %[a], %[sx], 131072\n\t" \
    "buffer_load_dwordx4 %[l16], %[rs], %[a] offen offset:0 sc1 lds\n\t" \
    "buffer_load_dwordx4 %[l16], %[rs], %[a] offen offset:1024 sc1 lds\n\t" \
    "buffer_load_dwordx4 %[l16], %[rs], %[a] offen offset:2048 sc1 lds\n\t" \
    "buffer_load_dwordx4 %[l16], %[rs], %[a] offen offset:3072 sc1 lds\n\t" \
    "v_mfma_f32_16x16x32_bf16 v[196:199], v[20:23], v[132:135], v[196:199]\n\t" \
    "ds_read_b128 v[44:47], %[xp] offset:55296\n\t" \
    "v_mfma_f32_16x16x32_bf16 v[200:203], v[20:23], v[140:143], v[200:203]\n\t" \
    "ds_read_b128 v[48:51], %[xp] offset:56320\n\t" \
    "v_mfma_f32_16x16x32_bf16 v[204:207], v[24:27], v[132:135], v[204:207]\n\t" \
    "s_waitcnt lgkmcnt(4)\n\t" \
    "v_mfma_f32_16x16x32_bf16 v[208:211], v[24:27], v[140:143], v[208:211]\n\t" \
    "v_mfma_f32_16x16x32_bf16 v[196:199], v[28:31], v[136:139], v[196:199]\n\t" \
    "ds_read_b128 v[20:23], %[xp] offset:57344\n\t" \
    "v_mfma_f32_16x16x32_bf16 v[200:203], v[28:31], v[144:147], v[200:203]\n\t" \
    "ds_read_b128 v[24:27], %[xp] offset:58368\n\t" \
    "v_mfma_f32_16x16x32_bf16 v[204:207], v[32:35], v[136:139], v[204:207]\n\t" \
    "s_waitcnt lgkmcnt(4)\n\t" \
    "v_mfma_f32_16x16x32_bf16 v[208:211], v[32:35], v[144:147], v[208:211]\n\t" \
    "v_mfma_f32_16x16x32_bf16 v[196:199], v[36:39], v[148:151], v[196:199]\n\t" \
    "ds_read_b128 v[28:31], %[xp] offset:59392\n\t" \
    "v_mfma_f32_16x16x32_bf16 v[200:203], v[36:39], v[156:159], v[200:203]\n\t" \
    "ds_read_b128 v[32:35], %[xp] offset:60416\n\t" \
    "v_mfma_f32_16x16x32_bf16 v[204:207], v[40:43], v[148:151], v[204:207]\n\t" \
    "s_waitcnt lgkmcnt(4)\n\t" \
    "v_mfma_f32_16x16x32_bf16 v[208:211], v[40:43], v[156:159], v[208:211]\n\t" \
    "v_mfma_f32_16x16x32_bf16 v[196:199], v[44:47], v[152:155], v[196:199]\n\t" \
    "ds_read_b128 v[36:39], %[xp] offset:61440\n\t" \
    "v_mfma_f32_16x16x32_bf16 v[200:203], v[44:47], v[160:163], v[200:203]\n\t" \
    "ds_read_b128 v[40:43], %[xp] offset:62464\n\t" \
    "v_mfma_f32_16x16x32_bf16 v[204:207], v[48:51], v[152:155], v[204:207]\n\t" \
    "s_waitcnt lgkmcnt(4)\n\t" \
    "v_mfma_f32_16x16x32_bf16 v[208:211], v[48:51], v[160:163], v[208:211]\n\t" \
    "v_mfma_f32_16x16x32_bf16 v[196:199], v[20:23], v[164:167], v[196:199]\n\t" \
    "ds_read_b128 v[44:47], %[xp] offset:63488\n\t" \
    "v_mfma_f32_16x16x32_bf16 v[200:203], v[20:23], v[172:175], v[200:203]\n\t" \
    "ds_read_b128 v[48:51], %[xp] offset:64512\n\t" \
    "v_mfma_f32_16x16x32_bf16 v[204:207], v[24:27], v[164:167], v[204:207]\n\t" \
    "s_waitcnt lgkmcnt(4)\n\t" \
    "v_mfma_f32_16x16x32_bf16 v[208:211], v[24:27], v[172:175], v[208:211]\n\t" \
    "v_mfma_f32_16x16x32_bf16 v[196:199], v[28:31], v[168:171], v[196:199]\n\t" \
    "ds_read_b128 v[20:23], %[xp] offset:0\n\t" \
    "v_mfma_f32_16x16x32_bf16 v[200:203], v[28:31], v[176:179], v[200:203]\n\t" \
    "ds_read_b128 v[24:27], %[xp] offset:1024\n\t" \
    "v_mfma_f32_16x16x32_bf16 v[204:207], v[32:35], v[168:171], v[204:207]\n\t" \
    "s_waitcnt lgkmcnt(4)\n\t" \
    "v_mfma_f32_16x16x32_bf16 v[208:211], v[32:35], v[176:179], v[208:211]\n\t" \
    "v_mfma_f32_16x16x32_bf16 v[196:199], v[36:39], v[180:183], v[196:199]\n\t" \
    "ds_read_b128 v[28:31], %[xp] offset:2048\n\t" \
    "v_mfma_f32_16x16x32_bf16 v[200:203], v[36:39], v[188:191], v[200:203]\n\t" \
    "ds_read_b128 v[32:35], %[xp] offset:3072\n\t" \
    "v_mfma_f32_16x16x32_bf16 v[204:207], v[40:43], v[180:183], v[204:207]\n\t" \
    "s_waitcnt lgkmcnt(4)\n\t" \
    "v_mfma_f32_16x16x32_bf16 v[208:211], v[40:43], v[188:191], v[208:211]\n\t" \
    "v_mfma_f32_16x16x32_bf16 v[196:199], v[44:47], v[184:187], v[196:199]\n\t" \
    "ds_read_b128 v[36:39], %[xp] offset:4096\n\t" \
    "v_mfma_f32_16x16x32_bf16 v[200:203], v[44:47], v[192:195], v[200:203]\n\t" \
    "ds_read_b128 v[40:43], %[xp] offset:5120\n\t" \
    "v_mfma_f32_16x16x32_bf16 v[204:207], v[48:51], v[184:187], v[204:207]\n\t" \
    "s_waitcnt lgkmcnt(4)\n\t" \
    "v_mfma_f32_16x16x32_bf16 v[208:211], v[48:51], v[192:195], v[208:211]\n\t" \
    "s_add_u32 %[sx], %[sx], 65536\n\t" \
    "s_add_u32 %[t], %[t], 1\n\t" \
    "s_waitcnt vmcnt(4)\n\t" \
    "s_barrier\n\t" \
    "s_cmp_lt_u32 %[t], %[te]\n\t" \
    "s_cbranch_scc1 L0_%=\n\t" \
    "s_cmp_eq_u32 %[fin], %[te]\n\t" \
    "s_cbranch_scc0 E_%=\n\t" \
    "s_nop 4\n\t" \
    "ds_write_b128 %[pa], v[196:199] offset:0\n\t" \
    "ds_write_b128 %[pa], v[200:203] offset:1024\n\t" \
    "ds_write_b128 %[pa], v[204:207] offset:2048\n\t" \
    "ds_write_b128 %[pa], v[208:211] offset:3072\n\t" \
    "s_waitcnt lgkmcnt(0)\n\t" \
    "s_barrier\n\t" \
    "s_branch E_%=\n\t" \
    "L1_%=:\n\t" \
    "s_setprio 1\n\t" \
    "s_add_u32 m0, %[sd], 49152\n\t" \
    "s_add_u32 %[a], %[sx], 81920\n\t" \
    "v_mfma_f32_16x16x32_bf16 v[0:3], v[20:23], v[68:71], 0\n\t" \
    "ds_read_b128 v[44:47], %[xp] offset:6144\n\t" \
    "v_mfma_f32_16x16x32_bf16 v[4:7], v[20:23], v[76:79], 0\n\t" \
    "ds_read_b128 v[48:51], %[xp] offset:7168\n\t" \
    "v_mfma_f32_16x16x32_bf16 v[8:11], v[24:27], v[68:71], 0\n\t" \
    "s_waitcnt lgkmcnt(4)\n\t" \
    "v_mfma_f32_16x16x32_bf16 v[12:15], v[24:27], v[76:79], 0\n\t" \
    "v_mfma_f32_16x16x32_bf16 v[0:3], v[28:31], v[72:75], v[0:3]\n\t" \
    "ds_read_b128 v[20:23], %[xp] offset:8192\n\t" \
    "v_mfma_f32_16x16x32_bf16 v[4:7], v[28:31], v[80:83], v[4:7]\n\t" \
    "ds_read_b128 v[24:27], %[xp] offset:9216\n\t" \
    "v_mfma_f32_16x16x32_bf16 v[8:11], v[32:35], v[72:75], v[8:11]\n\t" \
    "s_waitcnt lgkmcnt(4)\n\t" \
    "v_mfma_f32_16x16x32_bf16 v[12:15], v[32:35], v[80:83], v[12:15]\n\t" \
    "v_mfma_f32_16x16x32_bf16 v[0:3], v[36:39], v[84:87], v[0:3]\n\t" \
    "ds_read_b128 v[28:31], %[xp] offset:10240\n\t" \
    "v_mfma_f32_16x16x32_bf16 v[4:7], v[36:39], v[92:95], v[4:7]\n\t" \
    "ds_read_b128 v[32:35], %[xp] offset:11264\n\t" \
    "v_mfma_f32_16x16x32_bf16 v[8:11], v[40:43], v[84:87], v[8:11]\n\t" \
    "s_waitcnt lgkmcnt(4)\n\t" \
    "v_mfma_f32_16x16x32_bf16 v[12:15], v[40:43], v[92:95], v[12:15]\n\t" \
    "v_mfma_f32_16x16x32_bf16 v[0:3], v[44:47], v[88:91], v[0:3]\n\t" \
    "ds_read_b128 v[36:39], %[xp] offset:12288\n\t" \
    "v_mfma_f32_16x16x32_bf16 v[4:7], v[44:47], v[96:99], v[4:7]\n\t" \
    "ds_read_b128 v[40:43], %[xp] offset:13312\n\t" \
    "v_mfma_f32_16x16x32_bf16 v[8:11], v[48:51], v[88:91], v[8:11]\n\t" \
    "s_waitcnt lgkmcnt(4)\n\t" \
    "v_mfma_f32_16x16x32_bf16 v[12:15], v[48:51], v[96:99], v[12:15]\n\t" \
    "v_mfma_f32_16x16x32_bf16 v[0:3], v[20:23], v[100:103], v[0:3]\n\t" \
    "ds_read_b128 v[44:47], %[xp] offset:14336\n\t" \
    "v_mfma_f32_16x16x32_bf16 v[4:7], v[20:23], v[108:111], v[4:7]\n\t" \
    "ds_read_b128 v[48:51], %[xp] offset:15360\n\t" \
    "v_mfma_f32_16x16x32_bf16 v[8:11], v[24:27], v[100:103], v[8:11]\n\t" \
    "s_waitcnt lgkmcnt(4)\n\t" \
    "v_mfma_f32_16x16x32_bf16 v[12:15], v[24:27], v[108:111], v[12:15]\n\t" \
    "buffer_load_dwordx4 %[l16], %[rs], %[a] offen offset:0 sc1 lds\n\t" \
    "v_mfma_f32_16x16x32_bf16 v[0:3], v[28:31], v[104:107], v[0:3]\n\t" \
    "ds_read_b128 v[20:23], %[xp] offset:16384\n\t" \
    "v_mfma_f32_16x16x32_bf16 v[4:7], v[28:31], v[112:115], v[4:7]\n\t" \
    "ds_read_b128 v[24:27], %[xp] offset:17408\n\t" \
    "v_mfma_f32_16x16x32_bf16 v[8:11], v[32:35], v[104:107], v[8:11]\n\t" \
    "s_waitcnt lgkmcnt(4)\n\t" \
    "v_mfma_f32_16x16x32_bf16 v[12:15], v[32:35], v[112:115], v[12:15]\n\t" \
    "buffer_load_dwordx4 %[l16], %[rs], %[a] offen offset:1024 sc1 lds\n\t" \
    "v_mfma_f32_16x16x32_bf16 v[0:3], v[36:39], v[116:119], v[0:3]\n\t" \
    "ds_read_b128 v[28:31], %[xp] offset:18432\n\t" \
    "v_mfma_f32_16x16x32_bf16 v[4:7], v[36:39], v[124:127], v[4:7]\n\t" \
    "ds_read_b128 v[32:35], %[xp] offset:19456\n\t" \
    "v_mfma_f32_16x16x32_bf16 v[8:11], v[40:43], v[116:119], v[8:11]\n\t" \
    "s_waitcnt lgkmcnt(4)\n\t" \
    "v_mfma_f32_16x16x32_bf16 v[12:15], v[40:43], v[124:127], v[12:15]\n\t" \
    "buffer_load_dwordx4 %[l16], %[rs], %[a] offen offset:2048 sc1 lds\n\t" \
    "v_mfma_f32_16x16x32_bf16 v[0:3], v[44:47], v[120:123], v[0:3]\n\t" \
    "ds_read_b128 v[36:39], %[xp] offset:20480\n\t" \
    "v_mfma_f32_16x16x32_bf16 v[4:7], v[44:47], v[128:131], v[4:7]\n\t" \
    "ds_read_b128 v[40:43], %[xp] offset:21504\n\t" \
    "v_mfma_f32_16x16x32_bf16 v[8:11], v[48:51], v[120:123], v[8:11]\n\t" \
    "s_waitcnt lgkmcnt(4)\n\t" \
    "v_mfma_f32_16x16x32_bf16 v[12:15], v[48:51], v[128:131], v[12:15]\n\t" \
    "buffer_load_dwordx4 %[l16], %[rs], %[a] offen offset:3072 sc1 lds\n\t" \
    "s_waitcnt vmcnt(4)\n\t" \
    "s_barrier\n\t" \
    "s_setprio 1\n\t" \
    "s_add_u32 m0, %[sd], 0\n\t" \
    "s_add_u32 %[a], %[sx], 131072\n\t" \
    "s_add_u32 %[b], %[oo], 0x1000\n\t" \
    "s_cmp_lg_u32 %[t], 1\n\t" \
    "s_cselect_b64 %[em], -1, 0\n\t" \
    "v_mfma_f32_16x16x32_bf16 v[0:3], v[20:23], v[132:135], v[0:3]\n\t" \
    "ds_read_b128 v[44:47], %[xp] offset:22528\n\t" \
    "v_mfma_f32_16x16x32_bf16 v[4:7], v[20:23], v[140:143], v[4:7]\n\t" \
    "ds_read_b128 v[48:51], %[xp] offset:23552\n\t" \
    "v_mfma_f32_16x16x32_bf16 v[8:11], v[24:27], v[132:135], v[8:11]\n\t" \
    "ds_read_b128 v[52:55], %[pa] offset:0\n\t" \
    "s_waitcnt lgkmcnt(5)\n\t" \
    "v_mfma_f32_16x16x32_bf16 v[12:15], v[24:27], v[140:143], v[12:15]\n\t" \
    "buffer_load_dwordx4 %[l16], %[rs], %[a] offen offset:0 sc1 lds\n\t" \
    "v_mfma_f32_16x16x32_bf16 v[0:3], v[28:31], v[136:139], v[0:3]\n\t" \
    "ds_read_b128 v[20:23], %[xp] offset:24576\n\t" \
    "v_mfma_f32_16x16x32_bf16 v[4:7], v[28:31], v[144:147], v[4:7]\n\t" \
    "ds_read_b128 v[24:27], %[xp] offset:25600\n\t" \
    "v_mfma_f32_16x16x32_bf16 v[8:11], v[32:35], v[136:139], v[8:11]\n\t" \
    "ds_read_b128 v[56:59], %[pa] offset:1024\n\t" \
    "s_waitcnt lgkmcnt(6)\n\t" \
    "v_mfma_f32_16x16x32_bf16 v[12:15], v[32:35], v[144:147], v[12:15]\n\t" \
    "buffer_load_dwordx4 %[l16], %[rs], %[a] offen offset:1024 sc1 lds\n\t" \
    "v_mfma_f32_16x16x32_bf16 v[0:3], v[36:39], v[148:151], v[0:3]\n\t" \
    "ds_read_b128 v[28:31], %[xp] offset:26624\n\t" \
    "v_mfma_f32_16x16x32_bf16 v[4:7], v[36:39], v[156:159], v[4:7]\n\t" \
    "ds_read_b128 v[32:35], %[xp] offset:27648\n\t" \
    "v_mfma_f32_16x16x32_bf16 v[8:11], v[40:43], v[148:151], v[8:11]\n\t" \
    "ds_read_b128 v[60:63], %[pa] offset:2048\n\t" \
    "s_waitcnt lgkmcnt(7)\n\t" \
    "v_mfma_f32_16x16x32_bf16 v[12:15], v[40:43], v[156:159], v[12:15]\n\t" \
    "buffer_load_dwordx4 %[l16], %[rs], %[a] offen offset:2048 sc1 lds\n\t" \
    "v_mfma_f32_16x16x32_bf16 v[0:3], v[44:47], v[152:155], v[0:3]\n\t" \
    "ds_read_b128 v[36:39], %[xp] offset:28672\n\t" \
    "s_waitcnt lgkmcnt(7)\n\t" \
    "v_add_f32 v196, v196, v52\n\t" \
    "v_mfma_f32_16x16x32_bf16 v[4:7], v[44:47], v[160:163], v[4:7]\n\t" \
    "ds_read_b128 v[40:43], %[xp] offset:29696\n\t" \
    "v_add_f32 v197, v197, v53\n\t" \
    "v_mfma_f32_16x16x32_bf16 v[8:11], v[48:51], v[152:155], v[8:11]\n\t" \
    "ds_read_b128 v[64:67], %[pa] offset:3072\n\t" \
    "s_waitcnt lgkmcnt(7)\n\t" \
    "v_mfma_f32_16x16x32_bf16 v[12:15], v[48:51], v[160:163], v[12:15]\n\t" \
    "buffer_load_dwordx4 %[l16], %[rs], %[a] offen offset:3072 sc1 lds\n\t" \
    "v_add_f32 v198, v198, v54\n\t" \
    "v_add_f32 v199, v199, v55\n\t" \
    "v_mfma_f32_16x16x32_bf16 v[0:3], v[20:23], v[164:167], v[0:3]\n\t" \
    "ds_read_b128 v[44:47], %[xp] offset:30720\n\t" \
    "s_waitcnt lgkmcnt(7)\n\t" \
    "v_add_f32 v200, v200, v56\n\t" \
    "v_mfma_f32_16x16x32_bf16 v[4:7], v[20:23], v[172:175], v[4:7]\n\t" \
    "ds_read_b128 v[48:51], %[xp] offset:31744\n\t" \
    "v_add_f32 v201, v201, v57\n\t" \
    "v_mfma_f32_16x16x32_bf16 v[8:11], v[24:27], v[164:167], v[8:11]\n\t" \
    "v_add_f32 v202, v202, v58\n\t" \
    "s_waitcnt lgkmcnt(6)\n\t" \
    "v_mfma_f32_16x16x32_bf16 v[12:15], v[24:27], v[172:175], v[12:15]\n\t" \
    "v_add_f32 v203, v203, v59\n\t" \
    "s_waitcnt lgkmcnt(5)\n\t" \
    "v_add_f32 v204, v204, v60\n\t" \
    "v_add_f32 v205, v205, v61\n\t" \
    "v_mfma_f32_16x16x32_bf16 v[0:3], v[28:31], v[168:171], v[0:3]\n\t" \
    "ds_read_b128 v[20:23], %[xp] offset:32768\n\t" \
    "v_add_f32 v206, v206, v62\n\t" \
    "v_mfma_f32_16x16x32_bf16 v[4:7], v[28:31], v[176:179], v[4:7]\n\t" \
    "ds_read_b128 v[24:27], %[xp] offset:33792\n\t" \
    "v_add_f32 v207, v207, v63\n\t" \
    "v_mfma_f32_16x16x32_bf16 v[8:11], v[32:35], v[168:171], v[8:11]\n\t" \
    "s_waitcnt lgkmcnt(4)\n\t" \
    "v_add_f32 v208, v208, v64\n\t" \
    "v_mfma_f32_16x16x32_bf16 v[12:15], v[32:35], v[176:179], v[12:15]\n\t" \
    "v_add_f32 v209, v209, v65\n\t" \
    "v_add_f32 v210, v210, v66\n\t" \
    "v_add_f32 v211, v211, v67\n\t" \
    "v_mfma_f32_16x16x32_bf16 v[0:3], v[36:39], v[180:183], v[0:3]\n\t" \
    "ds_read_b128 v[28:31], %[xp] offset:34816\n\t" \
    "v_cvt_pk_bf16_f32 v52, v196, v197\n\t" \
    "v_mfma_f32_16x16x32_bf16 v[4:7], v[36:39], v[188:191], v[4:7]\n\t" \
    "ds_read_b128 v[32:35], %[xp] offset:35840\n\t" \
    "v_cvt_pk_bf16_f32 v53, v198, v199\n\t" \
    "v_mfma_f32_16x16x32_bf16 v[8:11], v[40:43], v[180:183], v[8:11]\n\t" \
    "v_cvt_pk_bf16_f32 v54, v204, v205\n\t" \
    "s_waitcnt lgkmcnt(4)\n\t" \
    "v_mfma_f32_16x16x32_bf16 v[12:15], v[40:43], v[188:191], v[12:15]\n\t" \
    "v_cvt_pk_bf16_f32 v55, v206, v207\n\t" \
    "v_cvt_pk_bf16_f32 v56, v200, v201\n\t" \
    "v_cvt_pk_bf16_f32 v57, v202, v203\n\t" \
    "v_mfma_f32_16x16x32_bf16 v[0:3], v[44:47], v[184:187], v[0:3]\n\t" \
    "ds_read_b128 v[36:39], %[xp] offset:36864\n\t" \
    "v_cvt_pk_bf16_f32 v58, v208, v209\n\t" \
    "v_mfma_f32_16x16x32_bf16 v[4:7], v[44:47], v[192:195], v[4:7]\n\t" \
    "ds_read_b128 v[40:43], %[xp] offset:37888\n\t" \
    "v_cvt_pk_bf16_f32 v59, v210, v211\n\t" \
    "v_mfma_f32_16x16x32_bf16 v[8:11], v[48:51], v[184:187], v[8:11]\n\t" \
    "s_waitcnt lgkmcnt(4)\n\t" \
    "v_mfma_f32_16x16x32_bf16 v[12:15], v[48:51], v[192:195], v[12:15]\n\t" \
    "s_mov_b64 exec, %[em]\n\t" \
    "buffer_store_dwordx4 v[52:55], %[l16], %[rs], %[oo] offen sc1\n\t" \
    "buffer_store_dwordx4 v[56:59], %[l16], %[rs], %[oo] offen offset:1024 sc1\n\t" \
    "s_mov_b64 exec, -1\n\t" \
    "s_add_u32 %[sx], %[sx], 65536\n\t" \
    "s_add_u32 %[t], %[t], 1\n\t" \
    "s_add_u32 %[oo], %[oo], 262144\n\t" \
    "s_waitcnt vmcnt(6)\n\t" \
    "s_cmp_gt_u32 %[t], " STR(SQUICK) " + 1\n\t" \
    "s_cbranch_scc1 B0_%=\n\t" \
    "s_waitcnt vmcnt(0)\n\t" \
    "B0_%=:\n\t" \
    "s_barrier\n\t" \
    "s_setprio 1\n\t" \
    "s_add_u32 m0, %[sd], 16384\n\t" \
    "s_add_u32 %[a], %[sx], 81920\n\t" \
    "v_mfma_f32_16x16x32_bf16 v[196:199], v[20:23], v[68:71], 0\n\t" \
    "ds_read_b128 v[44:47], %[xp] offset:38912\n\t" \
    "v_mfma_f32_16x16x32_bf16 v[200:203], v[20:23], v[76:79], 0\n\t" \
    "ds_read_b128 v[48:51], %[xp] offset:39936\n\t" \
    "v_mfma_f32_16x16x32_bf16 v[204:207], v[24:27], v[68:71], 0\n\t" \
    "s_waitcnt lgkmcnt(4)\n\t" \
    "v_mfma_f32_16x16x32_bf16 v[208:211], v[24:27], v[76:79], 0\n\t" \
    "v_mfma_f32_16x16x32_bf16 v[196:199], v[28:31], v[72:75], v[196:199]\n\t" \
    "ds_read_b128 v[20:23], %[xp] offset:40960\n\t" \
    "v_mfma_f32_16x16x32_bf16 v[200:203], v[28:31], v[80:83], v[200:203]\n\t" \
    "ds_read_b128 v[24:27], %[xp] offset:41984\n\t" \
    "v_mfma_f32_16x16x32_bf16 v[204:207], v[32:35], v[72:75], v[204:207]\n\t" \
    "s_waitcnt lgkmcnt(4)\n\t" \
    "v_mfma_f32_16x16x32_bf16 v[208:211], v[32:35], v[80:83], v[208:211]\n\t" \
    "v_mfma_f32_16x16x32_bf16 v[196:199], v[36:39], v[84:87], v[196:199]\n\t" \
    "ds_read_b128 v[28:31], %[xp] offset:43008\n\t" \
    "v_mfma_f32_16x16x32_bf16 v[200:203], v[36:39], v[92:95], v[200:203]\n\t" \
    "ds_read_b128 v[32:35], %[xp] offset:44032\n\t" \
    "v_mfma_f32_16x16x32_bf16 v[204:207], v[40:43], v[84:87], v[204:207]\n\t" \
    "s_waitcnt lgkmcnt(4)\n\t" \
    "v_mfma_f32_16x16x32_bf16 v[208:211], v[40:43], v[92:95], v[208:211]\n\t" \
    "v_mfma_f32_16x16x32_bf16 v[196:199], v[44:47], v[88:91], v[196:199]\n\t" \
    "ds_read_b128 v[36:39], %[xp] offset:45056\n\t" \
    "v_mfma_f32_16x16x32_bf16 v[200:203], v[44:47], v[96:99], v[200:203]\n\t" \
    "ds_read_b128 v[40:43], %[xp] offset:46080\n\t" \
    "v_mfma_f32_16x16x32_bf16 v[204:207], v[48:51], v[88:91], v[204:207]\n\t" \
    "s_waitcnt lgkmcnt(4)\n\t" \
    "v_mfma_f32_16x16x32_bf16 v[208:211], v[48:51], v[96:99], v[208:211]\n\t" \
    "v_mfma_f32_16x16x32_bf16 v[196:199], v[20:23], v[100:103], v[196:199]\n\t" \
    "ds_read_b128 v[44:47], %[xp] offset:47104\n\t" \
    "v_mfma_f32_16x16x32_bf16 v[200:203], v[20:23], v[108:111], v[200:203]\n\t" \
    "ds_read_b128 v[48:51], %[xp] offset:48128\n\t" \
    "v_mfma_f32_16x16x32_bf16 v[204:207], v[24:27], v[100:103], v[204:207]\n\t" \
    "s_waitcnt lgkmcnt(4)\n\t" \
    "v_mfma_f32_16x16x32_bf16 v[208:211], v[24:27], v[108:111], v[208:211]\n\t" \
    "buffer_load_dwordx4 %[l16], %[rs], %[a] offen offset:0 sc1 lds\n\t" \
    "v_mfma_f32_16x16x32_bf16 v[196:199], v[28:31], v[104:107], v[196:199]\n\t" \
    "ds_read_b128 v[20:23], %[xp] offset:49152\n\t" \
    "v_mfma_f32_16x16x32_bf16 v[200:203], v[28:31], v[112:115], v[200:203]\n\t" \
    "ds_read_b128 v[24:27], %[xp] offset:50176\n\t" \
    "v_mfma_f32_16x16x32_bf16 v[204:207], v[32:35], v[104:107], v[204:207]\n\t" \
    "s_waitcnt lgkmcnt(4)\n\t" \
    "v_mfma_f32_16x16x32_bf16 v[208:211], v[32:35], v[112:115], v[208:211]\n\t" \
    "buffer_load_dwordx4 %[l16], %[rs], %[a] offen offset:1024 sc1 lds\n\t" \
    "v_mfma_f32_16x16x32_bf16 v[196:199], v[36:39], v[116:119], v[196:199]\n\t" \
    "ds_read_b128 v[28:31], %[xp] offset:51200\n\t" \
    "v_mfma_f32_16x16x32_bf16 v[200:203], v[36:39], v[124:127], v[200:203]\n\t" \
    "ds_read_b128 v[32:35], %[xp] offset:52224\n\t" \
    "v_mfma_f32_16x16x32_bf16 v[204:207], v[40:43], v[116:119], v[204:207]\n\t" \
    "s_waitcnt lgkmcnt(4)\n\t" \
    "v_mfma_f32_16x16x32_bf16 v[208:211], v[40:43], v[124:127], v[208:211]\n\t" \
    "buffer_load_dwordx4 %[l16], %[rs], %[a] offen offset:2048 sc1 lds\n\t" \
    "v_mfma_f32_16x16x32_bf16 v[196:199], v[44:47], v[120:123], v[196:199]\n\t" \
    "ds_read_b128 v[36:39], %[xp] offset:53248\n\t" \
    "v_mfma_f32_16x16x32_bf16 v[200:203], v[44:47], v[128:131], v[200:203]\n\t" \
    "ds_read_b128 v[40:43], %[xp] offset:54272\n\t" \
    "v_mfma_f32_16x16x32_bf16 v[204:207], v[48:51], v[120:123], v[204:207]\n\t" \
    "s_waitcnt lgkmcnt(4)\n\t" \
    "v_mfma_f32_16x16x32_bf16 v[208:211], v[48:51], v[128:131], v[208:211]\n\t" \
    "buffer_load_dwordx4 %[l16], %[rs], %[a] offen offset:3072 sc1 lds\n\t" \
    "s_waitcnt vmcnt(4)\n\t" \
    "s_barrier\n\t" \
    "s_setprio 1\n\t" \
    "s_add_u32 m0, %[sd], 32768\n\t" \
    "s_add_u32 %[a], %[sx], 131072\n\t" \
    "s_add_u32 %[b], %[oo], 0x1000\n\t" \
    "s_cmp_lg_u32 %[t], 1\n\t" \
    "s_cselect_b64 %[em], -1, 0\n\t" \
    "v_mfma_f32_16x16x32_bf16 v[196:199], v[20:23], v[132:135], v[196:199]\n\t" \
    "ds_read_b128 v[44:47], %[xp] offset:55296\n\t" \
    "v_mfma_f32_16x16x32_bf16 v[200:203], v[20:23], v[140:143], v[200:203]\n\t" \
    "ds_read_b128 v[48:51], %[xp] offset:56320\n\t" \
    "v_mfma_f32_16x16x32_bf16 v[204:207], v[24:27], v[132:135], v[204:207]\n\t" \
    "ds_read_b128 v[52:55], %[pa] offset:0\n\t" \
    "s_waitcnt lgkmcnt(5)\n\t" \
    "v_mfma_f32_16x16x32_bf16 v[208:211], v[24:27], v[140:143], v[208:211]\n\t" \
    "buffer_load_dwordx4 %[l16], %[rs], %[a] offen offset:0 sc1 lds\n\t" \
    "v_mfma_f32_16x16x32_bf16 v[196:199], v[28:31], v[136:139], v[196:199]\n\t" \
    "ds_read_b128 v[20:23], %[xp] offset:57344\n\t" \
    "v_mfma_f32_16x16x32_bf16 v[200:203], v[28:31], v[144:147], v[200:203]\n\t" \
    "ds_read_b128 v[24:27], %[xp] offset:58368\n\t" \
    "v_mfma_f32_16x16x32_bf16 v[204:207], v[32:35], v[136:139], v[204:207]\n\t" \
    "ds_read_b128 v[56:59], %[pa] offset:1024\n\t" \
    "s_waitcnt lgkmcnt(6)\n\t" \
    "v_mfma_f32_16x16x32_bf16 v[208:211], v[32:35], v[144:147], v[208:211]\n\t" \
    "buffer_load_dwordx4 %[l16], %[rs], %[a] offen offset:1024 sc1 lds\n\t" \
    "v_mfma_f32_16x16x32_bf16 v[196:199], v[36:39], v[148:151], v[196:199]\n\t" \
    "ds_read_b128 v[28:31], %[xp] offset:59392\n\t" \
    "v_mfma_f32_16x16x32_bf16 v[200:203], v[36:39], v[156:159], v[200:203]\n\t" \
    "ds_read_b128 v[32:35], %[xp] offset:60416\n\t" \
    "v_mfma_f32_16x16x32_bf16 v[204:207], v[40:43], v[148:151], v[204:207]\n\t" \
    "ds_read_b128 v[60:63], %[pa] offset:2048\n\t" \
    "s_waitcnt lgkmcnt(7)\n\t" \
    "v_mfma_f32_16x16x32_bf16 v[208:211], v[40:43], v[156:159], v[208:211]\n\t" \
    "buffer_load_dwordx4 %[l16], %[rs], %[a] offen offset:2048 sc1 lds\n\t" \
    "v_mfma_f32_16x16x32_bf16 v[196:199], v[44:47], v[152:155], v[196:199]\n\t" \
    "ds_read_b128 v[36:39], %[xp] offset:61440\n\t" \
    "s_waitcnt lgkmcnt(7)\n\t" \
    "v_add_f32 v0, v0, v52\n\t" \
    "v_mfma_f32_16x16x32_bf16 v[200:203], v[44:47], v[160:163], v[200:203]\n\t" \
    "ds_read_b128 v[40:43], %[xp] offset:62464\n\t" \
    "v_add_f32 v1, v1, v53\n\t" \
    "v_mfma_f32_16x16x32_bf16 v[204:207], v[48:51], v[152:155], v[204:207]\n\t" \
    "ds_read_b128 v[64:67], %[pa] offset:3072\n\t" \
    "s_waitcnt lgkmcnt(7)\n\t" \
    "v_mfma_f32_16x16x32_bf16 v[208:211], v[48:51], v[160:163], v[208:211]\n\t" \
    "buffer_load_dwordx4 %[l16], %[rs], %[a] offen offset:3072 sc1 lds\n\t" \
    "v_add_f32 v2, v2, v54\n\t" \
    "v_add_f32 v3, v3, v55\n\t" \
    "v_mfma_f32_16x16x32_bf16 v[196:199], v[20:23], v[164:167], v[196:199]\n\t" \
    "ds_read_b128 v[44:47], %[xp] offset:63488\n\t" \
    "s_waitcnt lgkmcnt(7)\n\t" \
    "v_add_f32 v4, v4, v56\n\t" \
    "v_mfma_f32_16x16x32_bf16 v[200:203], v[20:23], v[172:175], v[200:203]\n\t" \
    "ds_read_b128 v[48:51], %[xp] offset:64512\n\t" \
    "v_add_f32 v5, v5, v57\n\t" \
    "v_mfma_f32_16x16x32_bf16 v[204:207], v[24:27], v[164:167], v[204:207]\n\t" \
    "v_add_f32 v6, v6, v58\n\t" \
    "s_waitcnt lgkmcnt(6)\n\t" \
    "v_mfma_f32_16x16x32_bf16 v[208:211], v[24:27], v[172:175], v[208:211]\n\t" \
    "v_add_f32 v7, v7, v59\n\t" \
    "s_waitcnt lgkmcnt(5)\n\t" \
    "v_add_f32 v8, v8, v60\n\t" \
    "v_add_f32 v9, v9, v61\n\t" \
    "v_mfma_f32_16x16x32_bf16 v[196:199], v[28:31], v[168:171], v[196:199]\n\t" \
    "ds_read_b128 v[20:23], %[xp] offset:0\n\t" \
    "v_add_f32 v10, v10, v62\n\t" \
    "v_mfma_f32_16x16x32_bf16 v[200:203], v[28:31], v[176:179], v[200:203]\n\t" \
    "ds_read_b128 v[24:27], %[xp] offset:1024\n\t" \
    "v_add_f32 v11, v11, v63\n\t" \
    "v_mfma_f32_16x16x32_bf16 v[204:207], v[32:35], v[168:171], v[204:207]\n\t" \
    "s_waitcnt lgkmcnt(4)\n\t" \
    "v_add_f32 v12, v12, v64\n\t" \
    "v_mfma_f32_16x16x32_bf16 v[208:211], v[32:35], v[176:179], v[208:211]\n\t" \
    "v_add_f32 v13, v13, v65\n\t" \
    "v_add_f32 v14, v14, v66\n\t" \
    "v_add_f32 v15, v15, v67\n\t" \
    "v_mfma_f32_16x16x32_bf16 v[196:199], v[36:39], v[180:183], v[196:199]\n\t" \
    "ds_read_b128 v[28:31], %[xp] offset:2048\n\t" \
    "v_cvt_pk_bf16_f32 v52, v0, v1\n\t" \
    "v_mfma_f32_16x16x32_bf16 v[200:203], v[36:39], v[188:191], v[200:203]\n\t" \
    "ds_read_b128 v[32:35], %[xp] offset:3072\n\t" \
    "v_cvt_pk_bf16_f32 v53, v2, v3\n\t" \
    "v_mfma_f32_16x16x32_bf16 v[204:207], v[40:43], v[180:183], v[204:207]\n\t" \
    "v_cvt_pk_bf16_f32 v54, v8, v9\n\t" \
    "s_waitcnt lgkmcnt(4)\n\t" \
    "v_mfma_f32_16x16x32_bf16 v[208:211], v[40:43], v[188:191], v[208:211]\n\t" \
    "v_cvt_pk_bf16_f32 v55, v10, v11\n\t" \
    "v_cvt_pk_bf16_f32 v56, v4, v5\n\t" \
    "v_cvt_pk_bf16_f32 v57, v6, v7\n\t" \
    "v_mfma_f32_16x16x32_bf16 v[196:199], v[44:47], v[184:187], v[196:199]\n\t" \
    "ds_read_b128 v[36:39], %[xp] offset:4096\n\t" \
    "v_cvt_pk_bf16_f32 v58, v12, v13\n\t" \
    "v_mfma_f32_16x16x32_bf16 v[200:203], v[44:47], v[192:195], v[200:203]\n\t" \
    "ds_read_b128 v[40:43], %[xp] offset:5120\n\t" \
    "v_cvt_pk_bf16_f32 v59, v14, v15\n\t" \
    "v_mfma_f32_16x16x32_bf16 v[204:207], v[48:51], v[184:187], v[204:207]\n\t" \
    "s_waitcnt lgkmcnt(4)\n\t" \
    "v_mfma_f32_16x16x32_bf16 v[208:211], v[48:51], v[192:195], v[208:211]\n\t" \
    "s_mov_b64 exec, %[em]\n\t" \
    "buffer_store_dwordx4 v[52:55], %[l16], %[rs], %[oo] offen sc1\n\t" \
    "buffer_store_dwordx4 v[56:59], %[l16], %[rs], %[oo] offen offset:1024 sc1\n\t" \
    "s_mov_b64 exec, -1\n\t" \
    "s_add_u32 %[sx], %[sx], 65536\n\t" \
    "s_add_u32 %[t], %[t], 1\n\t" \
    "s_add_u32 %[oo], %[oo], 262144\n\t" \
    "s_waitcnt vmcnt(6)\n\t" \
    "s_cmp_gt_u32 %[t], " STR(SQUICK) " + 1\n\t" \
    "s_cbranch_scc1 B1_%=\n\t" \
    "s_waitcnt vmcnt(0)\n\t" \
    "B1_%=:\n\t" \
    "s_barrier\n\t" \
    "s_cmp_lt_u32 %[t], %[te]\n\t" \
    "s_cbranch_scc1 L1_%=\n\t" \
    "s_cmp_eq_u32 %[fin], %[te]\n\t" \
    "s_cbranch_scc0 E_%=\n\t" \
    "s_nop 4\n\t" \
    "s_barrier\n\t" \
    "ds_read_b128 v[52:55], %[pa] offset:0\n\t" \
    "ds_read_b128 v[56:59], %[pa] offset:1024\n\t" \
    "ds_read_b128 v[60:63], %[pa] offset:2048\n\t" \
    "ds_read_b128 v[64:67], %[pa] offset:3072\n\t" \
    "s_add_u32 %[b], %[oo], 0x1000\n\t" \
    "s_waitcnt lgkmcnt(0)\n\t" \
    "v_add_f32 v196, v196, v52\n\t" \
    "v_add_f32 v197, v197, v53\n\t" \
    "v_add_f32 v198, v198, v54\n\t" \
    "v_add_f32 v199, v199, v55\n\t" \
    "v_add_f32 v200, v200, v56\n\t" \
    "v_add_f32 v201, v201, v57\n\t" \
    "v_add_f32 v202, v202, v58\n\t" \
    "v_add_f32 v203, v203, v59\n\t" \
    "v_add_f32 v204, v204, v60\n\t" \
    "v_add_f32 v205, v205, v61\n\t" \
    "v_add_f32 v206, v206, v62\n\t" \
    "v_add_f32 v207, v207, v63\n\t" \
    "v_add_f32 v208, v208, v64\n\t" \
    "v_add_f32 v209, v209, v65\n\t" \
    "v_add_f32 v210, v210, v66\n\t" \
    "v_add_f32 v211, v211, v67\n\t" \
    "v_cvt_pk_bf16_f32 v52, v196, v197\n\t" \
    "v_cvt_pk_bf16_f32 v53, v198, v199\n\t" \
    "v_cvt_pk_bf16_f32 v54, v204, v205\n\t" \
    "v_cvt_pk_bf16_f32 v55, v206, v207\n\t" \
    "v_cvt_pk_bf16_f32 v56, v200, v201\n\t" \
    "v_cvt_pk_bf16_f32 v57, v202, v203\n\t" \
    "v_cvt_pk_bf16_f32 v58, v208, v209\n\t" \
    "v_cvt_pk_bf16_f32 v59, v210, v211\n\t" \
    "buffer_store_dwordx4 v[52:55], %[l16], %[rs], %[oo] offen sc1\n\t" \
    "buffer_store_dwordx4 v[56:59], %[l16], %[rs], %[oo] offen offset:1024 sc1\n\t" \
    "E_%=:\n\t" \
    "s_setprio 0\n\t" \
    "s_waitcnt lgkmcnt(0)\n\t" \
    "s_nop 7\n\t" \
    : [t] "+s"(t), [sx] "+s"(sx), [oo] "+s"(oo), [a] "=&s"(tmp), [b] "=&s"(tmp2), [em] "=&s"(em), "+{v[20:27]}"(xf[0]), "+{v[28:35]}"(xf[1]), "+{v[36:43]}"(xf[2]), "+{v[196:203]}"(ac[0]), "+{v[204:211]}"(ac[1]) \
    : [te] "s"(TE_), [fin] "s"(FIN_), [kh] "s"(kh), [sd] "s"(sd), [rs] "s"(rs), [l16] "v"(l16), [xp] "v"(xp), [pa] "v"(pa), [fm] "s"(fm), [pm] "s"(pm), [fo] "s"(fo), [po] "s"(po), [pd] "s"(pd), [tag] "s"(P.tag), "{v[68:75]}"(w[0][0]), "{v[76:83]}"(w[0][1]), "{v[84:91]}"(w[1][0]), "{v[92:99]}"(w[1][1]), "{v[100:107]}"(w[2][0]), "{v[108:115]}"(w[2][1]), "{v[116:123]}"(w[3][0]), "{v[124:131]}"(w[3][1]), "{v[132:139]}"(w[4][0]), "{v[140:147]}"(w[4][1]), "{v[148:155]}"(w[5][0]), "{v[156:163]}"(w[5][1]), "{v[164:171]}"(w[6][0]), "{v[172:179]}"(w[6][1]), "{v[180:187]}"(w[7][0]), "{v[188:195]}"(w[7][1]) \
    : "memory", "scc", "v0", "v1", "v2", "v3", "v4", "v5", "v6", "v7", "v8", "v9", "v10", "v11", "v12", "v13", "v14", "v15", "v44", "v45", "v46", "v47", "v48", "v49", "v50", "v51", "v52", "v53", "v54", "v55", "v56", "v57", "v58", "v59", "v60", "v61", "v62", "v63", "v64", "v65", "v66", "v67")

#define S_LOOP_X(TE_, FIN_) \
  asm volatile( \
    "s_cmp_eq_u32 %[kh], 0\n\t" \
    "s_cbranch_scc0 L1_%=\n\t" \
    "L0_%=:\n\t" \
    "s_setprio 0\n\t" \
    "s_nop 3\n\t" \
    "ds_write_b128 %[pa], v[196:199] offset:0\n\t" \
    "ds_write_b128 %[pa], v[200:203] offset:1024\n\t" \
    "ds_write_b128 %[pa], v[204:207] offset:2048\n\t" \
    "ds_write_b128 %[pa], v[208:211] offset:3072\n\t" \
    "s_sub_u32 %[a], %[t], 1\n\t" \
    "v_mov_b32 v53, %[tag]\n\t" \
    "v_mov_b32 v52, %[a]\n\t" \
    "s_sub_u32 %[b], %[t], 2\n\t" \
    "s_cmp_lt_u32 %[b], " STR(SQUICK) "\n\t" \
    "s_cselect_b64 exec, %[fm], 0\n\t" \
    "buffer_store_dwordx2 v[52:53], %[l16], %[rs], %[fo] offen sc1\n\t" \
    "s_mov_b64 exec, -1\n\t" \
    "s_add_u32 m0, %[sd], 49152\n\t" \
    "s_add_u32 %[a], %[sx], 278528\n\t" \
    "s_waitcnt lgkmcnt(0)\n\t" \
    "buffer_load_dwordx4 %[l16], %[rs], %[a] offen offset:0 sc1 lds\n\t" \
    "buffer_load_dwordx4 %[l16], %[rs], %[a] offen offset:1024 sc1 lds\n\t" \
    "buffer_load_dwordx4 %[l16], %[rs], %[a] offen offset:2048 sc1 lds\n\t" \
    "buffer_load_dwordx4 %[l16], %[rs], %[a] offen offset:3072 sc1 lds\n\t" \
    "v_mfma_f32_16x16x32_bf16 v[0:3], v[68:71], v[20:23], 0\n\t" \
    "ds_read_b128 v[44:47], %[xp] offset:6144\n\t" \
    "v_mfma_f32_16x16x32_bf16 v[4:7], v[76:79], v[20:23], 0\n\t" \
    "ds_read_b128 v[48:51], %[xp] offset:7168\n\t" \
    "v_mfma_f32_16x16x32_bf16 v[8:11], v[68:71], v[24:27], 0\n\t" \
    "v_mfma_f32_16x16x32_bf16 v[12:15], v[76:79], v[24:27], 0\n\t" \
    "v_mfma_f32_16x16x32_bf16 v[0:3], v[72:75], v[28:31], v[0:3]\n\t" \
    "ds_read_b128 v[20:23], %[xp] offset:8192\n\t" \
    "v_mfma_f32_16x16x32_bf16 v[4:7], v[80:83], v[28:31], v[4:7]\n\t" \
    "ds_read_b128 v[24:27], %[xp] offset:9216\n\t" \
    "v_mfma_f32_16x16x32_bf16 v[8:11], v[72:75], v[32:35], v[8:11]\n\t" \
    "v_mfma_f32_16x16x32_bf16 v[12:15], v[80:83], v[32:35], v[12:15]\n\t" \
    "v_mfma_f32_16x16x32_bf16 v[0:3], v[84:87], v[36:39], v[0:3]\n\t" \
    "ds_read_b128 v[28:31], %[xp] offset:10240\n\t" \
    "v_mfma_f32_16x16x32_bf16 v[4:7], v[92:95], v[36:39], v[4:7]\n\t" \
    "ds_read_b128 v[32:35], %[xp] offset:11264\n\t" \
    "v_mfma_f32_16x16x32_bf16 v[8:11], v[84:87], v[40:43], v[8:11]\n\t" \
    "s_waitcnt lgkmcnt(4)\n\t" \
    "v_mfma_f32_16x16x32_bf16 v[12:15], v[92:95], v[40:43], v[12:15]\n\t" \
    "v_mfma_f32_16x16x32_bf16 v[0:3], v[88:91], v[44:47], v[0:3]\n\t" \
    "ds_read_b128 v[36:39], %[xp] offset:12288\n\t" \
    "v_mfma_f32_16x16x32_bf16 v[4:7], v[96:99], v[44:47], v[4:7]\n\t" \
    "ds_read_b128 v[40:43], %[xp] offset:13312\n\t" \
    "v_mfma_f32_16x16x32_bf16 v[8:11], v[88:91], v[48:51], v[8:11]\n\t" \
    "s_waitcnt lgkmcnt(4)\n\t" \
    "v_mfma_f32_16x16x32_bf16 v[12:15], v[96:99], v[48:51], v[12:15]\n\t" \
    "v_mfma_f32_16x16x32_bf16 v[0:3], v[100:103], v[20:23], v[0:3]\n\t" \
    "ds_read_b128 v[44:47], %[xp] offset:14336\n\t" \
    "v_mfma_f32_16x16x32_bf16 v[4:7], v[108:111], v[20:23], v[4:7]\n\t" \
    "ds_read_b128 v[48:51], %[xp] offset:15360\n\t" \
    "v_mfma_f32_16x16x32_bf16 v[8:11], v[100:103], v[24:27], v[8:11]\n\t" \
    "s_waitcnt lgkmcnt(4)\n\t" \
    "v_mfma_f32_16x16x32_bf16 v[12:15], v[108:111], v[24:27], v[12:15]\n\t" \
    "v_mfma_f32_16x16x32_bf16 v[0:3], v[104:107], v[28:31], v[0:3]\n\t" \
    "ds_read_b128 v[20:23], %[xp] offset:16384\n\t" \
    "v_mfma_f32_16x16x32_bf16 v[4:7], v[112:115], v[28:31], v[4:7]\n\t" \
    "ds_read_b128 v[24:27], %[xp] offset:17408\n\t" \
    "v_mfma_f32_16x16x32_bf16 v[8:11], v[104:107], v[32:35], v[8:11]\n\t" \
    "s_waitcnt lgkmcnt(4)\n\t" \
    "v_mfma_f32_16x16x32_bf16 v[12:15], v[112:115], v[32:35], v[12:15]\n\t" \
    "v_mfma_f32_16x16x32_bf16 v[0:3], v[116:119], v[36:39], v[0:3]\n\t" \
    "ds_read_b128 v[28:31], %[xp] offset:18432\n\t" \
    "v_mfma_f32_16x16x32_bf16 v[4:7], v[124:127], v[36:39], v[4:7]\n\t" \
    "ds_read_b128 v[32:35], %[xp] offset:19456\n\t" \
    "v_mfma_f32_16x16x32_bf16 v[8:11], v[116:119], v[40:43], v[8:11]\n\t" \
    "s_waitcnt lgkmcnt(4)\n\t" \
    "v_mfma_f32_16x16x32_bf16 v[12:15], v[124:127], v[40:43], v[12:15]\n\t" \
    "v_mfma_f32_16x16x32_bf16 v[0:3], v[120:123], v[44:47], v[0:3]\n\t" \
    "ds_read_b128 v[36:39], %[xp] offset:20480\n\t" \
    "v_mfma_f32_16x16x32_bf16 v[4:7], v[128:131], v[44:47], v[4:7]\n\t" \
    "ds_read_b128 v[40:43], %[xp] offset:21504\n\t" \
    "v_mfma_f32_16x16x32_bf16 v[8:11], v[120:123], v[48:51], v[8:11]\n\t" \
    "s_waitcnt lgkmcnt(4)\n\t" \
    "v_mfma_f32_16x16x32_bf16 v[12:15], v[128:131], v[48:51], v[12:15]\n\t" \
    "s_waitcnt vmcnt(4)\n\t" \
    "s_barrier\n\t" \
    "s_setprio 0\n\t" \
    "s_sub_u32 %[a], %[t], 1\n\t" \
    "v_mov_b32 v53, %[tag]\n\t" \
    "v_mov_b32 v52, %[a]\n\t" \
    "s_cmp_gt_u32 %[t], 1\n\t" \
    "s_cselect_b64 exec, %[fm], 0\n\t" \
    "buffer_store_dwordx2 v[52:53], %[l16], %[rs], %[fo] offen sc1\n\t" \
    "s_mov_b64 exec, %[pm]\n\t" \
    "s_mov_b32 m0, %[pd]\n\t" \
    "s_add_u32 %[a], %[sx], %[po]\n\t" \
    "buffer_load_dwordx4 %[l16], %[rs], %[a] offen sc1 lds\n\t" \
    "s_mov_b64 exec, -1\n\t" \
    "s_add_u32 m0, %[sd], 0\n\t" \
    "s_add_u32 %[a], %[sx], 524288\n\t" \
    "buffer_load_dwordx4 %[l16], %[rs], %[a] offen offset:0 sc1 lds\n\t" \
    "buffer_load_dwordx4 %[l16], %[rs], %[a] offen offset:1024 sc1 lds\n\t" \
    "buffer_load_dwordx4 %[l16], %[rs], %[a] offen offset:2048 sc1 lds\n\t" \
    "buffer_load_dwordx4 %[l16], %[rs], %[a] offen offset:3072 sc1 lds\n\t" \
    "v_mfma_f32_16x16x32_bf16 v[0:3], v[132:135], v[20:23], v[0:3]\n\t" \
    "ds_read_b128 v[44:47], %[xp] offset:22528\n\t" \
    "v_mfma_f32_16x16x32_bf16 v[4:7], v[140:143], v[20:23], v[4:7]\n\t" \
    "ds_read_b128 v[48:51], %[xp] offset:23552\n\t" \
    "v_mfma_f32_16x16x32_bf16 v[8:11], v[132:135], v[24:27], v[8:11]\n\t" \
    "s_waitcnt lgkmcnt(4)\n\t" \
    "v_mfma_f32_16x16x32_bf16 v[12:15], v[140:143], v[24:27], v[12:15]\n\t" \
    "v_mfma_f32_16x16x32_bf16 v[0:3], v[136:139], v[28:31], v[0:3]\n\t" \
    "ds_read_b128 v[20:23], %[xp] offset:24576\n\t" \
    "v_mfma_f32_16x16x32_bf16 v[4:7], v[144:147], v[28:31], v[4:7]\n\t" \
    "ds_read_b128 v[24:27], %[xp] offset:25600\n\t" \
    "v_mfma_f32_16x16x32_bf16 v[8:11], v[136:139], v[32:35], v[8:11]\n\t" \
    "s_waitcnt lgkmcnt(4)\n\t" \
    "v_mfma_f32_16x16x32_bf16 v[12:15], v[144:147], v[32:35], v[12:15]\n\t" \
    "v_mfma_f32_16x16x32_bf16 v[0:3], v[148:151], v[36:39], v[0:3]\n\t" \
    "ds_read_b128 v[28:31], %[xp] offset:26624\n\t" \
    "v_mfma_f32_16x16x32_bf16 v[4:7], v[156:159], v[36:39], v[4:7]\n\t" \
    "ds_read_b128 v[32:35], %[xp] offset:27648\n\t" \
    "v_mfma_f32_16x16x32_bf16 v[8:11], v[148:151], v[40:43], v[8:11]\n\t" \
    "s_waitcnt lgkmcnt(4)\n\t" \
    "v_mfma_f32_16x16x32_bf16 v[12:15], v[156:159], v[40:43], v[12:15]\n\t" \
    "v_mfma_f32_16x16x32_bf16 v[0:3], v[152:155], v[44:47], v[0:3]\n\t" \
    "ds_read_b128 v[36:39], %[xp] offset:28672\n\t" \
    "v_mfma_f32_16x16x32_bf16 v[4:7], v[160:163], v[44:47], v[4:7]\n\t" \
    "ds_read_b128 v[40:43], %[xp] offset:29696\n\t" \
    "v_mfma_f32_16x16x32_bf16 v[8:11], v[152:155], v[48:51], v[8:11]\n\t" \
    "s_waitcnt lgkmcnt(4)\n\t" \
    "v_mfma_f32_16x16x32_bf16 v[12:15], v[160:163], v[48:51], v[12:15]\n\t" \
    "v_mfma_f32_16x16x32_bf16 v[0:3], v[164:167], v[20:23], v[0:3]\n\t" \
    "ds_read_b128 v[44:47], %[xp] offset:30720\n\t" \
    "v_mfma_f32_16x16x32_bf16 v[4:7], v[172:175], v[20:23], v[4:7]\n\t" \
    "ds_read_b128 v[48:51], %[xp] offset:31744\n\t" \
    "v_mfma_f32_16x16x32_bf16 v[8:11], v[164:167], v[24:27], v[8:11]\n\t" \
    "s_waitcnt lgkmcnt(4)\n\t" \
    "v_mfma_f32_16x16x32_bf16 v[12:15], v[172:175], v[24:27], v[12:15]\n\t" \
    "v_mfma_f32_16x16x32_bf16 v[0:3], v[168:171], v[28:31], v[0:3]\n\t" \
    "ds_read_b128 v[20:23], %[xp] offset:32768\n\t" \
    "v_mfma_f32_16x16x32_bf16 v[4:7], v[176:179], v[28:31], v[4:7]\n\t" \
    "ds_read_b128 v[24:27], %[xp] offset:33792\n\t" \
    "v_mfma_f32_16x16x32_bf16 v[8:11], v[168:171], v[32:35], v[8:11]\n\t" \
    "s_waitcnt lgkmcnt(4)\n\t" \
    "v_mfma_f32_16x16x32_bf16 v[12:15], v[176:179], v[32:35], v[12:15]\n\t" \
    "v_mfma_f32_16x16x32_bf16 v[0:3], v[180:183], v[36:39], v[0:3]\n\t" \
    "ds_read_b128 v[28:31], %[xp] offset:34816\n\t" \
    "v_mfma_f32_16x16x32_bf16 v[4:7], v[188:191], v[36:39], v[4:7]\n\t" \
    "ds_read_b128 v[32:35], %[xp] offset:35840\n\t" \
    "v_mfma_f32_16x16x32_bf16 v[8:11], v[180:183], v[40:43], v[8:11]\n\t" \
    "s_waitcnt lgkmcnt(4)\n\t" \
    "v_mfma_f32_16x16x32_bf16 v[12:15], v[188:191], v[40:43], v[12:15]\n\t" \
    "v_mfma_f32_16x16x32_bf16 v[0:3], v[184:187], v[44:47], v[0:3]\n\t" \
    "ds_read_b128 v[36:39], %[xp] offset:36864\n\t" \
    "v_mfma_f32_16x16x32_bf16 v[4:7], v[192:195], v[44:47], v[4:7]\n\t" \
    "ds_read_b128 v[40:43], %[xp] offset:37888\n\t" \
    "v_mfma_f32_16x16x32_bf16 v[8:11], v[184:187], v[48:51], v[8:11]\n\t" \
    "s_waitcnt lgkmcnt(4)\n\t" \
    "v_mfma_f32_16x16x32_bf16 v[12:15], v[192:195], v[48:51], v[12:15]\n\t" \
    "s_add_u32 %[sx], %[sx], 262144\n\t" \
    "s_add_u32 %[t], %[t], 1\n\t" \
    "s_waitcnt vmcnt(4)\n\t" \
    "s_barrier\n\t" \
    "s_setprio 0\n\t" \
    "s_nop 3\n\t" \
    "ds_write_b128 %[pa], v[0:3] offset:0\n\t" \
    "ds_write_b128 %[pa], v[4:7] offset:1024\n\t" \
    "ds_write_b128 %[pa], v[8:11] offset:2048\n\t" \
    "ds_write_b128 %[pa], v[12:15] offset:3072\n\t" \
    "s_sub_u32 %[a], %[t], 1\n\t" \
    "v_mov_b32 v53, %[tag]\n\t" \
    "v_mov_b32 v52, %[a]\n\t" \
    "s_sub_u32 %[b], %[t], 2\n\t" \
    "s_cmp_lt_u32 %[b], " STR(SQUICK) "\n\t" \
    "s_cselect_b64 exec, %[fm], 0\n\t" \
    "buffer_store_dwordx2 v[52:53], %[l16], %[rs], %[fo] offen sc1\n\t" \
    "s_mov_b64 exec, -1\n\t" \
    "s_add_u32 m0, %[sd], 16384\n\t" \
    "s_add_u32 %[a], %[sx], 278528\n\t" \
    "s_waitcnt lgkmcnt(0)\n\t" \
    "buffer_load_dwordx4 %[l16], %[rs], %[a] offen offset:0 sc1 lds\n\t" \
    "buffer_load_dwordx4 %[l16], %[rs], %[a] offen offset:1024 sc1 lds\n\t" \
    "buffer_load_dwordx4 %[l16], %[rs], %[a] offen offset:2048 sc1 lds\n\t" \
    "buffer_load_dwordx4 %[l16], %[rs], %[a] offen offset:3072 sc1 lds\n\t" \
    "v_mfma_f32_16x16x32_bf16 v[196:199], v[68:71], v[20:23], 0\n\t" \
    "ds_read_b128 v[44:47], %[xp] offset:38912\n\t" \
    "v_mfma_f32_16x16x32_bf16 v[200:203], v[76:79], v[20:23], 0\n\t" \
    "ds_read_b128 v[48:51], %[xp] offset:39936\n\t" \
    "v_mfma_f32_16x16x32_bf16 v[204:207], v[68:71], v[24:27], 0\n\t" \
    "v_mfma_f32_16x16x32_bf16 v[208:211], v[76:79], v[24:27], 0\n\t" \
    "v_mfma_f32_16x16x32_bf16 v[196:199], v[72:75], v[28:31], v[196:199]\n\t" \
    "ds_read_b128 v[20:23], %[xp] offset:40960\n\t" \
    "v_mfma_f32_16x16x32_bf16 v[200:203], v[80:83], v[28:31], v[200:203]\n\t" \
    "ds_read_b128 v[24:27], %[xp] offset:41984\n\t" \
    "v_mfma_f32_16x16x32_bf16 v[204:207], v[72:75], v[32:35], v[204:207]\n\t" \
    "v_mfma_f32_16x16x32_bf16 v[208:211], v[80:83], v[32:35], v[208:211]\n\t" \
    "v_mfma_f32_16x16x32_bf16 v[196:199], v[84:87], v[36:39], v[196:199]\n\t" \
    "ds_read_b128 v[28:31], %[xp] offset:43008\n\t" \
    "v_mfma_f32_16x16x32_bf16 v[200:203], v[92:95], v[36:39], v[200:203]\n\t" \
    "ds_read_b128 v[32:35], %[xp] offset:44032\n\t" \
    "v_mfma_f32_16x16x32_bf16 v[204:207], v[84:87], v[40:43], v[204:207]\n\t" \
    "s_waitcnt lgkmcnt(4)\n\t" \
    "v_mfma_f32_16x16x32_bf16 v[208:211], v[92:95], v[40:43], v[208:211]\n\t" \
    "v_mfma_f32_16x16x32_bf16 v[196:199], v[88:91], v[44:47], v[196:199]\n\t" \
    "ds_read_b128 v[36:39], %[xp] offset:45056\n\t" \
    "v_mfma_f32_16x16x32_bf16 v[200:203], v[96:99], v[44:47], v[200:203]\n\t" \
    "ds_read_b128 v[40:43], %[xp] offset:46080\n\t" \
    "v_mfma_f32_16x16x32_bf16 v[204:207], v[88:91], v[48:51], v[204:207]\n\t" \
    "s_waitcnt lgkmcnt(4)\n\t" \
    "v_mfma_f32_16x16x32_bf16 v[208:211], v[96:99], v[48:51], v[208:211]\n\t" \
    "v_mfma_f32_16x16x32_bf16 v[196:199], v[100:103], v[20:23], v[196:199]\n\t" \
    "ds_read_b128 v[44:47], %[xp] offset:47104\n\t" \
    "v_mfma_f32_16x16x32_bf16 v[200:203], v[108:111], v[20:23], v[200:203]\n\t" \
    "ds_read_b128 v[48:51], %[xp] offset:48128\n\t" \
    "v_mfma_f32_16x16x32_bf16 v[204:207], v[100:103], v[24:27], v[204:207]\n\t" \
    "s_waitcnt lgkmcnt(4)\n\t" \
    "v_mfma_f32_16x16x32_bf16 v[208:211], v[108:111], v[24:27], v[208:211]\n\t" \
    "v_mfma_f32_16x16x32_bf16 v[196:199], v[104:107], v[28:31], v[196:199]\n\t" \
    "ds_read_b128 v[20:23], %[xp] offset:49152\n\t" \
    "v_mfma_f32_16x16x32_bf16 v[200:203], v[112:115], v[28:31], v[200:203]\n\t" \
    "ds_read_b128 v[24:27], %[xp] offset:50176\n\t" \
    "v_mfma_f32_16x16x32_bf16 v[204:207], v[104:107], v[32:35], v[204:207]\n\t" \
    "s_waitcnt lgkmcnt(4)\n\t" \
    "v_mfma_f32_16x16x32_bf16 v[208:211], v[112:115], v[32:35], v[208:211]\n\t" \
    "v_mfma_f32_16x16x32_bf16 v[196:199], v[116:119], v[36:39], v[196:199]\n\t" \
    "ds_read_b128 v[28:31], %[xp] offset:51200\n\t" \
    "v_mfma_f32_16x16x32_bf16 v[200:203], v[124:127], v[36:39], v[200:203]\n\t" \
    "ds_read_b128 v[32:35], %[xp] offset:52224\n\t" \
    "v_mfma_f32_16x16x32_bf16 v[204:207], v[116:119], v[40:43], v[204:207]\n\t" \
    "s_waitcnt lgkmcnt(4)\n\t" \
    "v_mfma_f32_16x16x32_bf16 v[208:211], v[124:127], v[40:43], v[208:211]\n\t" \
    "v_mfma_f32_16x16x32_bf16 v[196:199], v[120:123], v[44:47], v[196:199]\n\t" \
    "ds_read_b128 v[36:39], %[xp] offset:53248\n\t" \
    "v_mfma_f32_16x16x32_bf16 v[200:203], v[128:131], v[44:47], v[200:203]\n\t" \
    "ds_read_b128 v[40:43], %[xp] offset:54272\n\t" \
    "v_mfma_f32_16x16x32_bf16 v[204:207], v[120:123], v[48:51], v[204:207]\n\t" \
    "s_waitcnt lgkmcnt(4)\n\t" \
    "v_mfma_f32_16x16x32_bf16 v[208:211], v[128:131], v[48:51], v[208:211]\n\t" \
    "s_waitcnt vmcnt(4)\n\t" \
    "s_barrier\n\t" \
    "s_setprio 0\n\t" \
    "s_sub_u32 %[a], %[t], 1\n\t" \
    "v_mov_b32 v53, %[tag]\n\t" \
    "v_mov_b32 v52, %[a]\n\t" \
    "s_cmp_gt_u32 %[t], 1\n\t" \
    "s_cselect_b64 exec, %[fm], 0\n\t" \
    "buffer_store_dwordx2 v[52:53], %[l16], %[rs], %[fo] offen sc1\n\t" \
    "s_mov_b64 exec, %[pm]\n\t" \
    "s_mov_b32 m0, %[pd]\n\t" \
    "s_add_u32 %[a], %[sx], %[po]\n\t" \
    "buffer_load_dwordx4 %[l16], %[rs], %[a] offen sc1 lds\n\t" \
    "s_mov_b64 exec, -1\n\t" \
    "s_add_u32 m0, %[sd], 32768\n\t" \
    "s_add_u32 %[a], %[sx], 524288\n\t" \
    "buffer_load_dwordx4 %[l16], %[rs], %[a] offen offset:0 sc1 lds\n\t" \
    "buffer_load_dwordx4 %[l16], %[rs], %[a] offen offset:1024 sc1 lds\n\t" \
    "buffer_load_dwordx4 %[l16], %[rs], %[a] offen offset:2048 sc1 lds\n\t" \
    "buffer_load_dwordx4 %[l16], %[rs], %[a] offen offset:3072 sc1 lds\n\t" \
    "v_mfma_f32_16x16x32_bf16 v[196:199], v[132:135], v[20:23], v[196:199]\n\t" \
    "ds_read_b128 v[44:47], %[xp] offset:55296\n\t" \
    "v_mfma_f32_16x16x32_bf16 v[200:203], v[140:143], v[20:23], v[200:203]\n\t" \
    "ds_read_b128 v[48:51], %[xp] offset:56320\n\t" \
    "v_mfma_f32_16x16x32_bf16 v[204:207], v[132:135], v[24:27], v[204:207]\n\t" \
    "s_waitcnt lgkmcnt(4)\n\t" \
    "v_mfma_f32_16x16x32_bf16 v[208:211], v[140:143], v[24:27], v[208:211]\n\t" \
    "v_mfma_f32_16x16x32_bf16 v[196:199], v[136:139], v[28:31], v[196:199]\n\t" \
    "ds_read_b128 v[20:23], %[xp] offset:57344\n\t" \
    "v_mfma_f32_16x16x32_bf16 v[200:203], v[144:147], v[28:31], v[200:203]\n\t" \
    "ds_read_b128 v[24:27], %[xp] offset:58368\n\t" \
    "v_mfma_f32_16x16x32_bf16 v[204:207], v[136:139], v[32:35], v[204:207]\n\t" \
    "s_waitcnt lgkmcnt(4)\n\t" \
    "v_mfma_f32_16x16x32_bf16 v[208:211], v[144:147], v[32:35], v[208:211]\n\t" \
    "v_mfma_f32_16x16x32_bf16 v[196:199], v[148:151], v[36:39], v[196:199]\n\t" \
    "ds_read_b128 v[28:31], %[xp] offset:59392\n\t" \
    "v_mfma_f32_16x16x32_bf16 v[200:203], v[156:159], v[36:39], v[200:203]\n\t" \
    "ds_read_b128 v[32:35], %[xp] offset:60416\n\t" \
    "v_mfma_f32_16x16x32_bf16 v[204:207], v[148:151], v[40:43], v[204:207]\n\t" \
    "s_waitcnt lgkmcnt(4)\n\t" \
    "v_mfma_f32_16x16x32_bf16 v[208:211], v[156:159], v[40:43], v[208:211]\n\t" \
    "v_mfma_f32_16x16x32_bf16 v[196:199], v[152:155], v[44:47], v[196:199]\n\t" \
    "ds_read_b128 v[36:39], %[xp] offset:61440\n\t" \
    "v_mfma_f32_16x16x32_bf16 v[200:203], v[160:163], v[44:47], v[200:203]\n\t" \
    "ds_read_b128 v[40:43], %[xp] offset:62464\n\t" \
    "v_mfma_f32_16x16x32_bf16 v[204:207], v[152:155], v[48:51], v[204:207]\n\t" \
    "s_waitcnt lgkmcnt(4)\n\t" \
    "v_mfma_f32_16x16x32_bf16 v[208:211], v[160:163], v[48:51], v[208:211]\n\t" \
    "v_mfma_f32_16x16x32_bf16 v[196:199], v[164:167], v[20:23], v[196:199]\n\t" \
    "ds_read_b128 v[44:47], %[xp] offset:63488\n\t" \
    "v_mfma_f32_16x16x32_bf16 v[200:203], v[172:175], v[20:23], v[200:203]\n\t" \
    "ds_read_b128 v[48:51], %[xp] offset:64512\n\t" \
    "v_mfma_f32_16x16x32_bf16 v[204:207], v[164:167], v[24:27], v[204:207]\n\t" \
    "s_waitcnt lgkmcnt(4)\n\t" \
    "v_mfma_f32_16x16x32_bf16 v[208:211], v[172:175], v[24:27], v[208:211]\n\t" \
    "v_mfma_f32_16x16x32_bf16 v[196:199], v[168:171], v[28:31], v[196:199]\n\t" \
    "ds_read_b128 v[20:23], %[xp] offset:0\n\t" \
    "v_mfma_f32_16x16x32_bf16 v[200:203], v[176:179], v[28:31], v[200:203]\n\t" \
    "ds_read_b128 v[24:27], %[xp] offset:1024\n\t" \
    "v_mfma_f32_16x16x32_bf16 v[204:207], v[168:171], v[32:35], v[204:207]\n\t" \
    "s_waitcnt lgkmcnt(4)\n\t" \
    "v_mfma_f32_16x16x32_bf16 v[208:211], v[176:179], v[32:35], v[208:211]\n\t" \
    "v_mfma_f32_16x16x32_bf16 v[196:199], v[180:183], v[36:39], v[196:199]\n\t" \
    "ds_read_b128 v[28:31], %[xp] offset:2048\n\t" \
    "v_mfma_f32_16x16x32_bf16 v[200:203], v[188:191], v[36:39], v[200:203]\n\t" \
    "ds_read_b128 v[32:35], %[xp] offset:3072\n\t" \
    "v_mfma_f32_16x16x32_bf16 v[204:207], v[180:183], v[40:43], v[204:207]\n\t" \
    "s_waitcnt lgkmcnt(4)\n\t" \
    "v_mfma_f32_16x16x32_bf16 v[208:211], v[188:191], v[40:43], v[208:211]\n\t" \
    "v_mfma_f32_16x16x32_bf16 v[196:199], v[184:187], v[44:47], v[196:199]\n\t" \
    "ds_read_b128 v[36:39], %[xp] offset:4096\n\t" \
    "v_mfma_f32_16x16x32_bf16 v[200:203], v[192:195], v[44:47], v[200:203]\n\t" \
    "ds_read_b128 v[40:43], %[xp] offset:5120\n\t" \
    "v_mfma_f32_16x16x32_bf16 v[204:207], v[184:187], v[48:51], v[204:207]\n\t" \
    "s_waitcnt lgkmcnt(4)\n\t" \
    "v_mfma_f32_16x16x32_bf16 v[208:211], v[192:195], v[48:51], v[208:211]\n\t" \
    "s_add_u32 %[sx], %[sx], 262144\n\t" \
    "s_add_u32 %[t], %[t], 1\n\t" \
    "s_waitcnt vmcnt(4)\n\t" \
    "s_barrier\n\t" \
    "s_cmp_lt_u32 %[t], %[te]\n\t" \
    "s_cbranch_scc1 L0_%=\n\t" \
    "s_cmp_eq_u32 %[fin], %[te]\n\t" \
    "s_cbranch_scc0 E_%=\n\t" \
    "s_nop 4\n\t" \
    "ds_write_b128 %[pa], v[196:199] offset:0\n\t" \
    "ds_write_b128 %[pa], v[200:203] offset:1024\n\t" \
    "ds_write_b128 %[pa], v[204:207] offset:2048\n\t" \
    "ds_write_b128 %[pa], v[208:211] offset:3072\n\t" \
    "s_waitcnt lgkmcnt(0)\n\t" \
    "s_barrier\n\t" \
    "s_branch E_%=\n\t" \
    "L1_%=:\n\t" \
    "s_setprio 1\n\t" \
    "s_add_u32 m0, %[sd], 49152\n\t" \
    "s_add_u32 %[a], %[sx], 278528\n\t" \
    "v_mfma_f32_16x16x32_bf16 v[0:3], v[68:71], v[20:23], 0\n\t" \
    "ds_read_b128 v[44:47], %[xp] offset:6144\n\t" \
    "v_mfma_f32_16x16x32_bf16 v[4:7], v[76:79], v[20:23], 0\n\t" \
    "ds_read_b128 v[48:51], %[xp] offset:7168\n\t" \
    "v_mfma_f32_16x16x32_bf16 v[8:11], v[68:71], v[24:27], 0\n\t" \
    "s_waitcnt lgkmcnt(4)\n\t" \
    "v_mfma_f32_16x16x32_bf16 v[12:15], v[76:79], v[24:27], 0\n\t" \
    "v_mfma_f32_16x16x32_bf16 v[0:3], v[72:75], v[28:31], v[0:3]\n\t" \
    "ds_read_b128 v[20:23], %[xp] offset:8192\n\t" \
    "v_mfma_f32_16x16x32_bf16 v[4:7], v[80:83], v[28:31], v[4:7]\n\t" \
    "ds_read_b128 v[24:27], %[xp] offset:9216\n\t" \
    "v_mfma_f32_16x16x32_bf16 v[8:11], v[72:75], v[32:35], v[8:11]\n\t" \
    "s_waitcnt lgkmcnt(4)\n\t" \
    "v_mfma_f32_16x16x32_bf16 v[12:15], v[80:83], v[32:35], v[12:15]\n\t" \
    "v_mfma_f32_16x16x32_bf16 v[0:3], v[84:87], v[36:39], v[0:3]\n\t" \
    "ds_read_b128 v[28:31], %[xp] offset:10240\n\t" \
    "v_mfma_f32_16x16x32_bf16 v[4:7], v[92:95], v[36:39], v[4:7]\n\t" \
    "ds_read_b128 v[32:35], %[xp] offset:11264\n\t" \
    "v_mfma_f32_16x16x32_bf16 v[8:11], v[84:87], v[40:43], v[8:11]\n\t" \
    "s_waitcnt lgkmcnt(4)\n\t" \
    "v_mfma_f32_16x16x32_bf16 v[12:15], v[92:95], v[40:43], v[12:15]\n\t" \
    "v_mfma_f32_16x16x32_bf16 v[0:3], v[88:91], v[44:47], v[0:3]\n\t" \
    "ds_read_b128 v[36:39], %[xp] offset:12288\n\t" \
    "v_mfma_f32_16x16x32_bf16 v[4:7], v[96:99], v[44:47], v[4:7]\n\t" \
    "ds_read_b128 v[40:43], %[xp] offset:13312\n\t" \
    "v_mfma_f32_16x16x32_bf16 v[8:11], v[88:91], v[48:51], v[8:11]\n\t" \
    "s_waitcnt lgkmcnt(4)\n\t" \
    "v_mfma_f32_16x16x32_bf16 v[12:15], v[96:99], v[48:51], v[12:15]\n\t" \
    "v_mfma_f32_16x16x32_bf16 v[0:3], v[100:103], v[20:23], v[0:3]\n\t" \
    "ds_read_b128 v[44:47], %[xp] offset:14336\n\t" \
    "v_mfma_f32_16x16x32_bf16 v[4:7], v[108:111], v[20:23], v[4:7]\n\t" \
    "ds_read_b128 v[48:51], %[xp] offset:15360\n\t" \
    "v_mfma_f32_16x16x32_bf16 v[8:11], v[100:103], v[24:27], v[8:11]\n\t" \
    "s_waitcnt lgkmcnt(4)\n\t" \
    "v_mfma_f32_16x16x32_bf16 v[12:15], v[108:111], v[24:27], v[12:15]\n\t" \
    "buffer_load_dwordx4 %[l16], %[rs], %[a] offen offset:0 sc1 lds\n\t" \
    "v_mfma_f32_16x16x32_bf16 v[0:3], v[104:107], v[28:31], v[0:3]\n\t" \
    "ds_read_b128 v[20:23], %[xp] offset:16384\n\t" \
    "v_mfma_f32_16x16x32_bf16 v[4:7], v[112:115], v[28:31], v[4:7]\n\t" \
    "ds_read_b128 v[24:27], %[xp] offset:17408\n\t" \
    "v_mfma_f32_16x16x32_bf16 v[8:11], v[104:107], v[32:35], v[8:11]\n\t" \
    "s_waitcnt lgkmcnt(4)\n\t" \
    "v_mfma_f32_16x16x32_bf16 v[12:15], v[112:115], v[32:35], v[12:15]\n\t" \
    "buffer_load_dwordx4 %[l16], %[rs], %[a] offen offset:1024 sc1 lds\n\t" \
    "v_mfma_f32_16x16x32_bf16 v[0:3], v[116:119], v[36:39], v[0:3]\n\t" \
    "ds_read_b128 v[28:31], %[xp] offset:18432\n\t" \
    "v_mfma_f32_16x16x32_bf16 v[4:7], v[124:127], v[36:39], v[4:7]\n\t" \
    "ds_read_b128 v[32:35], %[xp] offset:19456\n\t" \
    "v_mfma_f32_16x16x32_bf16 v[8:11], v[116:119], v[40:43], v[8:11]\n\t" \
    "s_waitcnt lgkmcnt(4)\n\t" \
    "v_mfma_f32_16x16x32_bf16 v[12:15], v[124:127], v[40:43], v[12:15]\n\t" \
    "buffer_load_dwordx4 %[l16], %[rs], %[a] offen offset:2048 sc1 lds\n\t" \
    "v_mfma_f32_16x16x32_bf16 v[0:3], v[120:123], v[44:47], v[0:3]\n\t" \
    "ds_read_b128 v[36:39], %[xp] offset:20480\n\t" \
    "v_mfma_f32_16x16x32_bf16 v[4:7], v[128:131], v[44:47], v[4:7]\n\t" \
    "ds_read_b128 v[40:43], %[xp] offset:21504\n\t" \
    "v_mfma_f32_16x16x32_bf16 v[8:11], v[120:123], v[48:51], v[8:11]\n\t" \
    "s_waitcnt lgkmcnt(4)\n\t" \
    "v_mfma_f32_16x16x32_bf16 v[12:15], v[128:131], v[48:51], v[12:15]\n\t" \
    "buffer_load_dwordx4 %[l16], %[rs], %[a] offen offset:3072 sc1 lds\n\t" \
    "s_waitcnt vmcnt(4)\n\t" \
    "s_barrier\n\t" \
    "s_setprio 1\n\t" \
    "s_add_u32 m0, %[sd], 0\n\t" \
    "s_add_u32 %[a], %[sx], 524288\n\t" \
    "s_add_u32 %[b], %[oo], 0x1000\n\t" \
    "s_cmp_lg_u32 %[t], 0\n\t" \
    "s_cselect_b64 %[em], -1, 0\n\t" \
    "v_mfma_f32_16x16x32_bf16 v[0:3], v[132:135], v[20:23], v[0:3]\n\t" \
    "ds_read_b128 v[44:47], %[xp] offset:22528\n\t" \
    "v_mfma_f32_16x16x32_bf16 v[4:7], v[140:143], v[20:23], v[4:7]\n\t" \
    "ds_read_b128 v[48:51], %[xp] offset:23552\n\t" \
    "v_mfma_f32_16x16x32_bf16 v[8:11], v[132:135], v[24:27], v[8:11]\n\t" \
    "ds_read_b128 v[52:55], %[pa] offset:0\n\t" \
    "s_waitcnt lgkmcnt(5)\n\t" \
    "v_mfma_f32_16x16x32_bf16 v[12:15], v[140:143], v[24:27], v[12:15]\n\t" \
    "buffer_load_dwordx4 %[l16], %[rs], %[a] offen offset:0 sc1 lds\n\t" \
    "v_mfma_f32_16x16x32_bf16 v[0:3], v[136:139], v[28:31], v[0:3]\n\t" \
    "ds_read_b128 v[20:23], %[xp] offset:24576\n\t" \
    "v_mfma_f32_16x16x32_bf16 v[4:7], v[144:147], v[28:31], v[4:7]\n\t" \
    "ds_read_b128 v[24:27], %[xp] offset:25600\n\t" \
    "v_mfma_f32_16x16x32_bf16 v[8:11], v[136:139], v[32:35], v[8:11]\n\t" \
    "ds_read_b128 v[56:59], %[pa] offset:1024\n\t" \
    "s_waitcnt lgkmcnt(6)\n\t" \
    "v_mfma_f32_16x16x32_bf16 v[12:15], v[144:147], v[32:35], v[12:15]\n\t" \
    "buffer_load_dwordx4 %[l16], %[rs], %[a] offen offset:1024 sc1 lds\n\t" \
    "v_mfma_f32_16x16x32_bf16 v[0:3], v[148:151], v[36:39], v[0:3]\n\t" \
    "ds_read_b128 v[28:31], %[xp] offset:26624\n\t" \
    "v_mfma_f32_16x16x32_bf16 v[4:7], v[156:159], v[36:39], v[4:7]\n\t" \
    "ds_read_b128 v[32:35], %[xp] offset:27648\n\t" \
    "v_mfma_f32_16x16x32_bf16 v[8:11], v[148:151], v[40:43], v[8:11]\n\t" \
    "ds_read_b128 v[60:63], %[pa] offset:2048\n\t" \
    "s_waitcnt lgkmcnt(7)\n\t" \
    "v_mfma_f32_16x16x32_bf16 v[12:15], v[156:159], v[40:43], v[12:15]\n\t" \
    "buffer_load_dwordx4 %[l16], %[rs], %[a] offen offset:2048 sc1 lds\n\t" \
    "v_mfma_f32_16x16x32_bf16 v[0:3], v[152:155], v[44:47], v[0:3]\n\t" \
    "ds_read_b128 v[36:39], %[xp] offset:28672\n\t" \
    "s_waitcnt lgkmcnt(7)\n\t" \
    "v_add_f32 v196, v196, v52\n\t" \
    "v_mfma_f32_16x16x32_bf16 v[4:7], v[160:163], v[44:47], v[4:7]\n\t" \
    "ds_read_b128 v[40:43], %[xp] offset:29696\n\t" \
    "v_add_f32 v197, v197, v53\n\t" \
    "v_mfma_f32_16x16x32_bf16 v[8:11], v[152:155], v[48:51], v[8:11]\n\t" \
    "ds_read_b128 v[64:67], %[pa] offset:3072\n\t" \
    "s_waitcnt lgkmcnt(7)\n\t" \
    "v_mfma_f32_16x16x32_bf16 v[12:15], v[160:163], v[48:51], v[12:15]\n\t" \
    "buffer_load_dwordx4 %[l16], %[rs], %[a] offen offset:3072 sc1 lds\n\t" \
    "v_add_f32 v198, v198, v54\n\t" \
    "v_add_f32 v199, v199, v55\n\t" \
    "v_mfma_f32_16x16x32_bf16 v[0:3], v[164:167], v[20:23], v[0:3]\n\t" \
    "ds_read_b128 v[44:47], %[xp] offset:30720\n\t" \
    "s_waitcnt lgkmcnt(7)\n\t" \
    "v_add_f32 v200, v200, v56\n\t" \
    "v_mfma_f32_16x16x32_bf16 v[4:7], v[172:175], v[20:23], v[4:7]\n\t" \
    "ds_read_b128 v[48:51], %[xp] offset:31744\n\t" \
    "v_add_f32 v201, v201, v57\n\t" \
    "v_mfma_f32_16x16x32_bf16 v[8:11], v[164:167], v[24:27], v[8:11]\n\t" \
    "v_add_f32 v202, v202, v58\n\t" \
    "s_waitcnt lgkmcnt(6)\n\t" \
    "v_mfma_f32_16x16x32_bf16 v[12:15], v[172:175], v[24:27], v[12:15]\n\t" \
    "v_add_f32 v203, v203, v59\n\t" \
    "s_waitcnt lgkmcnt(5)\n\t" \
    "v_add_f32 v204, v204, v60\n\t" \
    "v_add_f32 v205, v205, v61\n\t" \
    "v_mfma_f32_16x16x32_bf16 v[0:3], v[168:171], v[28:31], v[0:3]\n\t" \
    "ds_read_b128 v[20:23], %[xp] offset:32768\n\t" \
    "v_add_f32 v206, v206, v62\n\t" \
    "v_mfma_f32_16x16x32_bf16 v[4:7], v[176:179], v[28:31], v[4:7]\n\t" \
    "ds_read_b128 v[24:27], %[xp] offset:33792\n\t" \
    "v_add_f32 v207, v207, v63\n\t" \
    "v_mfma_f32_16x16x32_bf16 v[8:11], v[168:171], v[32:35], v[8:11]\n\t" \
    "s_waitcnt lgkmcnt(4)\n\t" \
    "v_add_f32 v208, v208, v64\n\t" \
    "v_mfma_f32_16x16x32_bf16 v[12:15], v[176:179], v[32:35], v[12:15]\n\t" \
    "v_add_f32 v209, v209, v65\n\t" \
    "v_add_f32 v210, v210, v66\n\t" \
    "v_add_f32 v211, v211, v67\n\t" \
    "v_mfma_f32_16x16x32_bf16 v[0:3], v[180:183], v[36:39], v[0:3]\n\t" \
    "ds_read_b128 v[28:31], %[xp] offset:34816\n\t" \
    "v_mfma_f32_16x16x32_bf16 v[4:7], v[188:191], v[36:39], v[4:7]\n\t" \
    "ds_read_b128 v[32:35], %[xp] offset:35840\n\t" \
    "s_mov_b64 exec, %[em]\n\t" \
    "buffer_store_dwordx4 v[196:199], %[l16], %[rs], %[oo] offen offset:0 sc1\n\t" \
    "s_mov_b64 exec, -1\n\t" \
    "v_mfma_f32_16x16x32_bf16 v[8:11], v[180:183], v[40:43], v[8:11]\n\t" \
    "s_waitcnt lgkmcnt(4)\n\t" \
    "v_mfma_f32_16x16x32_bf16 v[12:15], v[188:191], v[40:43], v[12:15]\n\t" \
    "s_mov_b64 exec, %[em]\n\t" \
    "buffer_store_dwordx4 v[200:203], %[l16], %[rs], %[oo] offen offset:1024 sc1\n\t" \
    "s_mov_b64 exec, -1\n\t" \
    "v_mfma_f32_16x16x32_bf16 v[0:3], v[184:187], v[44:47], v[0:3]\n\t" \
    "ds_read_b128 v[36:39], %[xp] offset:36864\n\t" \
    "v_mfma_f32_16x16x32_bf16 v[4:7], v[192:195], v[44:47], v[4:7]\n\t" \
    "ds_read_b128 v[40:43], %[xp] offset:37888\n\t" \
    "s_mov_b64 exec, %[em]\n\t" \
    "buffer_store_dwordx4 v[204:207], %[l16], %[rs], %[oo] offen offset:2048 sc1\n\t" \
    "s_mov_b64 exec, -1\n\t" \
    "v_mfma_f32_16x16x32_bf16 v[8:11], v[184:187], v[48:51], v[8:11]\n\t" \
    "s_waitcnt lgkmcnt(4)\n\t" \
    "v_mfma_f32_16x16x32_bf16 v[12:15], v[192:195], v[48:51], v[12:15]\n\t" \
    "s_mov_b64 exec, %[em]\n\t" \
    "buffer_store_dwordx4 v[208:211], %[l16], %[rs], %[oo] offen offset:3072 sc1\n\t" \
    "s_mov_b64 exec, -1\n\t" \
    "s_add_u32 %[sx], %[sx], 262144\n\t" \
    "s_add_u32 %[t], %[t], 1\n\t" \
    "s_add_u32 %[oo], %[oo], 524288\n\t" \
    "s_waitcnt vmcnt(8)\n\t" \
    "s_cmp_gt_u32 %[t], " STR(SQUICK) " + 1\n\t" \
    "s_cbranch_scc1 B0_%=\n\t" \
    "s_waitcnt vmcnt(0)\n\t" \
    "B0_%=:\n\t" \
    "s_barrier\n\t" \
    "s_setprio 1\n\t" \
    "s_add_u32 m0, %[sd], 16384\n\t" \
    "s_add_u32 %[a], %[sx], 278528\n\t" \
    "v_mfma_f32_16x16x32_bf16 v[196:199], v[68:71], v[20:23], 0\n\t" \
    "ds_read_b128 v[44:47], %[xp] offset:38912\n\t" \
    "v_mfma_f32_16x16x32_bf16 v[200:203], v[76:79], v[20:23], 0\n\t" \
    "ds_read_b128 v[48:51], %[xp] offset:39936\n\t" \
    "v_mfma_f32_16x16x32_bf16 v[204:207], v[68:71], v[24:27], 0\n\t" \
    "s_waitcnt lgkmcnt(4)\n\t" \
    "v_mfma_f32_16x16x32_bf16 v[208:211], v[76:79], v[24:27], 0\n\t" \
    "v_mfma_f32_16x16x32_bf16 v[196:199], v[72:75], v[28:31], v[196:199]\n\t" \
    "ds_read_b128 v[20:23], %[xp] offset:40960\n\t" \
    "v_mfma_f32_16x16x32_bf16 v[200:203], v[80:83], v[28:31], v[200:203]\n\t" \
    "ds_read_b128 v[24:27], %[xp] offset:41984\n\t" \
    "v_mfma_f32_16x16x32_bf16 v[204:207], v[72:75], v[32:35], v[204:207]\n\t" \
    "s_waitcnt lgkmcnt(4)\n\t" \
    "v_mfma_f32_16x16x32_bf16 v[208:211], v[80:83], v[32:35], v[208:211]\n\t" \
    "v_mfma_f32_16x16x32_bf16 v[196:199], v[84:87], v[36:39], v[196:199]\n\t" \
    "ds_read_b128 v[28:31], %[xp] offset:43008\n\t" \
    "v_mfma_f32_16x16x32_bf16 v[200:203], v[92:95], v[36:39], v[200:203]\n\t" \
    "ds_read_b128 v[32:35], %[xp] offset:44032\n\t" \
    "v_mfma_f32_16x16x32_bf16 v[204:207], v[84:87], v[40:43], v[204:207]\n\t" \
    "s_waitcnt lgkmcnt(4)\n\t" \
    "v_mfma_f32_16x16x32_bf16 v[208:211], v[92:95], v[40:43], v[208:211]\n\t" \
    "v_mfma_f32_16x16x32_bf16 v[196:199], v[88:91], v[44:47], v[196:199]\n\t" \
    "ds_read_b128 v[36:39], %[xp] offset:45056\n\t" \
    "v_mfma_f32_16x16x32_bf16 v[200:203], v[96:99], v[44:47], v[200:203]\n\t" \
    "ds_read_b128 v[40:43], %[xp] offset:46080\n\t" \
    "v_mfma_f32_16x16x32_bf16 v[204:207], v[88:91], v[48:51], v[204:207]\n\t" \
    "s_waitcnt lgkmcnt(4)\n\t" \
    "v_mfma_f32_16x16x32_bf16 v[208:211], v[96:99], v[48:51], v[208:211]\n\t" \
    "v_mfma_f32_16x16x32_bf16 v[196:199], v[100:103], v[20:23], v[196:199]\n\t" \
    "ds_read_b128 v[44:47], %[xp] offset:47104\n\t" \
    "v_mfma_f32_16x16x32_bf16 v[200:203], v[108:111], v[20:23], v[200:203]\n\t" \
    "ds_read_b128 v[48:51], %[xp] offset:48128\n\t" \
    "v_mfma_f32_16x16x32_bf16 v[204:207], v[100:103], v[24:27], v[204:207]\n\t" \
    "s_waitcnt lgkmcnt(4)\n\t" \
    "v_mfma_f32_16x16x32_bf16 v[208:211], v[108:111], v[24:27], v[208:211]\n\t" \
    "buffer_load_dwordx4 %[l16], %[rs], %[a] offen offset:0 sc1 lds\n\t" \
    "v_mfma_f32_16x16x32_bf16 v[196:199], v[104:107], v[28:31], v[196:199]\n\t" \
    "ds_read_b128 v[20:23], %[xp] offset:49152\n\t" \
    "v_mfma_f32_16x16x32_bf16 v[200:203], v[112:115], v[28:31], v[200:203]\n\t" \
    "ds_read_b128 v[24:27], %[xp] offset:50176\n\t" \
    "v_mfma_f32_16x16x32_bf16 v[204:207], v[104:107], v[32:35], v[204:207]\n\t" \
    "s_waitcnt lgkmcnt(4)\n\t" \
    "v_mfma_f32_16x16x32_bf16 v[208:211], v[112:115], v[32:35], v[208:211]\n\t" \
    "buffer_load_dwordx4 %[l16], %[rs], %[a] offen offset:1024 sc1 lds\n\t" \
    "v_mfma_f32_16x16x32_bf16 v[196:199], v[116:119], v[36:39], v[196:199]\n\t" \
    "ds_read_b128 v[28:31], %[xp] offset:51200\n\t" \
    "v_mfma_f32_16x16x32_bf16 v[200:203], v[124:127], v[36:39], v[200:203]\n\t" \
    "ds_read_b128 v[32:35], %[xp] offset:52224\n\t" \
    "v_mfma_f32_16x16x32_bf16 v[204:207], v[116:119], v[40:43], v[204:207]\n\t" \
    "s_waitcnt lgkmcnt(4)\n\t" \
    "v_mfma_f32_16x16x32_bf16 v[208:211], v[124:127], v[40:43], v[208:211]\n\t" \
    "buffer_load_dwordx4 %[l16], %[rs], %[a] offen offset:2048 sc1 lds\n\t" \
    "v_mfma_f32_16x16x32_bf16 v[196:199], v[120:123], v[44:47], v[196:199]\n\t" \
    "ds_read_b128 v[36:39], %[xp] offset:53248\n\t" \
    "v_mfma_f32_16x16x32_bf16 v[200:203], v[128:131], v[44:47], v[200:203]\n\t" \
    "ds_read_b128 v[40:43], %[xp] offset:54272\n\t" \
    "v_mfma_f32_16x16x32_bf16 v[204:207], v[120:123], v[48:51], v[204:207]\n\t" \
    "s_waitcnt lgkmcnt(4)\n\t" \
    "v_mfma_f32_16x16x32_bf16 v[208:211], v[128:131], v[48:51], v[208:211]\n\t" \
    "buffer_load_dwordx4 %[l16], %[rs], %[a] offen offset:3072 sc1 lds\n\t" \
    "s_waitcnt vmcnt(4)\n\t" \
    "s_barrier\n\t" \
    "s_setprio 1\n\t" \
    "s_add_u32 m0, %[sd], 32768\n\t" \
    "s_add_u32 %[a], %[sx], 524288\n\t" \
    "s_add_u32 %[b], %[oo], 0x1000\n\t" \
    "s_cmp_lg_u32 %[t], 0\n\t" \
    "s_cselect_b64 %[em], -1, 0\n\t" \
    "v_mfma_f32_16x16x32_bf16 v[196:199], v[132:135], v[20:23], v[196:199]\n\t" \
    "ds_read_b128 v[44:47], %[xp] offset:55296\n\t" \
    "v_mfma_f32_16x16x32_bf16 v[200:203], v[140:143], v[20:23], v[200:203]\n\t" \
    "ds_read_b128 v[48:51], %[xp] offset:56320\n\t" \
    "v_mfma_f32_16x16x32_bf16 v[204:207], v[132:135], v[24:27], v[204:207]\n\t" \
    "ds_read_b128 v[52:55], %[pa] offset:0\n\t" \
    "s_waitcnt lgkmcnt(5)\n\t" \
    "v_mfma_f32_16x16x32_bf16 v[208:211], v[140:143], v[24:27], v[208:211]\n\t" \
    "buffer_load_dwordx4 %[l16], %[rs], %[a] offen offset:0 sc1 lds\n\t" \
    "v_mfma_f32_16x16x32_bf16 v[196:199], v[136:139], v[28:31], v[196:199]\n\t" \
    "ds_read_b128 v[20:23], %[xp] offset:57344\n\t" \
    "v_mfma_f32_16x16x32_bf16 v[200:203], v[144:147], v[28:31], v[200:203]\n\t" \
    "ds_read_b128 v[24:27], %[xp] offset:58368\n\t" \
    "v_mfma_f32_16x16x32_bf16 v[204:207], v[136:139], v[32:35], v[204:207]\n\t" \
    "ds_read_b128 v[56:59], %[pa] offset:1024\n\t" \
    "s_waitcnt lgkmcnt(6)\n\t" \
    "v_mfma_f32_16x16x32_bf16 v[208:211], v[144:147], v[32:35], v[208:211]\n\t" \
    "buffer_load_dwordx4 %[l16], %[rs], %[a] offen offset:1024 sc1 lds\n\t" \
    "v_mfma_f32_16x16x32_bf16 v[196:199], v[148:151], v[36:39], v[196:199]\n\t" \
    "ds_read_b128 v[28:31], %[xp] offset:59392\n\t" \
    "v_mfma_f32_16x16x32_bf16 v[200:203], v[156:159], v[36:39], v[200:203]\n\t" \
    "ds_read_b128 v[32:35], %[xp] offset:60416\n\t" \
    "v_mfma_f32_16x16x32_bf16 v[204:207], v[148:151], v[40:43], v[204:207]\n\t" \
    "ds_read_b128 v[60:63], %[pa] offset:2048\n\t" \
    "s_waitcnt lgkmcnt(7)\n\t" \
    "v_mfma_f32_16x16x32_bf16 v[208:211], v[156:159], v[40:43], v[208:211]\n\t" \
    "buffer_load_dwordx4 %[l16], %[rs], %[a] offen offset:2048 sc1 lds\n\t" \
    "v_mfma_f32_16x16x32_bf16 v[196:199], v[152:155], v[44:47], v[196:199]\n\t" \
    "ds_read_b128 v[36:39], %[xp] offset:61440\n\t" \
    "s_waitcnt lgkmcnt(7)\n\t" \
    "v_add_f32 v0, v0, v52\n\t" \
    "v_mfma_f32_16x16x32_bf16 v[200:203], v[160:163], v[44:47], v[200:203]\n\t" \
    "ds_read_b128 v[40:43], %[xp] offset:62464\n\t" \
    "v_add_f32 v1, v1, v53\n\t" \
    "v_mfma_f32_16x16x32_bf16 v[204:207], v[152:155], v[48:51], v[204:207]\n\t" \
    "ds_read_b128 v[64:67], %[pa] offset:3072\n\t" \
    "s_waitcnt lgkmcnt(7)\n\t" \
    "v_mfma_f32_16x16x32_bf16 v[208:211], v[160:163], v[48:51], v[208:211]\n\t" \
    "buffer_load_dwordx4 %[l16], %[rs], %[a] offen offset:3072 sc1 lds\n\t" \
    "v_add_f32 v2, v2, v54\n\t" \
    "v_add_f32 v3, v3, v55\n\t" \
    "v_mfma_f32_16x16x32_bf16 v[196:199], v[164:167], v[20:23], v[196:199]\n\t" \
    "ds_read_b128 v[44:47], %[xp] offset:63488\n\t" \
    "s_waitcnt lgkmcnt(7)\n\t" \
    "v_add_f32 v4, v4, v56\n\t" \
    "v_mfma_f32_16x16x32_bf16 v[200:203], v[172:175], v[20:23], v[200:203]\n\t" \
    "ds_read_b128 v[48:51], %[xp] offset:64512\n\t" \
    "v_add_f32 v5, v5, v57\n\t" \
    "v_mfma_f32_16x16x32_bf16 v[204:207], v[164:167], v[24:27], v[204:207]\n\t" \
    "v_add_f32 v6, v6, v58\n\t" \
    "s_waitcnt lgkmcnt(6)\n\t" \
    "v_mfma_f32_16x16x32_bf16 v[208:211], v[172:175], v[24:27], v[208:211]\n\t" \
    "v_add_f32 v7, v7, v59\n\t" \
    "s_waitcnt lgkmcnt(5)\n\t" \
    "v_add_f32 v8, v8, v60\n\t" \
    "v_add_f32 v9, v9, v61\n\t" \
    "v_mfma_f32_16x16x32_bf16 v[196:199], v[168:171], v[28:31], v[196:199]\n\t" \
    "ds_read_b128 v[20:23], %[xp] offset:0\n\t" \
    "v_add_f32 v10, v10, v62\n\t" \
    "v_mfma_f32_16x16x32_bf16 v[200:203], v[176:179], v[28:31], v[200:203]\n\t" \
    "ds_read_b128 v[24:27], %[xp] offset:1024\n\t" \
    "v_add_f32 v11, v11, v63\n\t" \
    "v_mfma_f32_16x16x32_bf16 v[204:207], v[168:171], v[32:35], v[204:207]\n\t" \
    "s_waitcnt lgkmcnt(4)\n\t" \
    "v_add_f32 v12, v12, v64\n\t" \
    "v_mfma_f32_16x16x32_bf16 v[208:211], v[176:179], v[32:35], v[208:211]\n\t" \
    "v_add_f32 v13, v13, v65\n\t" \
    "v_add_f32 v14, v14, v66\n\t" \
    "v_add_f32 v15, v15, v67\n\t" \
    "v_mfma_f32_16x16x32_bf16 v[196:199], v[180:183], v[36:39], v[196:199]\n\t" \
    "ds_read_b128 v[28:31], %[xp] offset:2048\n\t" \
    "v_mfma_f32_16x16x32_bf16 v[200:203], v[188:191], v[36:39], v[200:203]\n\t" \
    "ds_read_b128 v[32:35], %[xp] offset:3072\n\t" \
    "s_mov_b64 exec, %[em]\n\t" \
    "buffer_store_dwordx4 v[0:3], %[l16], %[rs], %[oo] offen offset:0 sc1\n\t" \
    "s_mov_b64 exec, -1\n\t" \
    "v_mfma_f32_16x16x32_bf16 v[204:207], v[180:183], v[40:43], v[204:207]\n\t" \
    "s_waitcnt lgkmcnt(4)\n\t" \
    "v_mfma_f32_16x16x32_bf16 v[208:211], v[188:191], v[40:43], v[208:211]\n\t" \
    "s_mov_b64 exec, %[em]\n\t" \
    "buffer_store_dwordx4 v[4:7], %[l16], %[rs], %[oo] offen offset:1024 sc1\n\t" \
    "s_mov_b64 exec, -1\n\t" \
    "v_mfma_f32_16x16x32_bf16 v[196:199], v[184:187], v[44:47], v[196:199]\n\t" \
    "ds_read_b128 v[36:39], %[xp] offset:4096\n\t" \
    "v_mfma_f32_16x16x32_bf16 v[200:203], v[192:195], v[44:47], v[200:203]\n\t" \
    "ds_read_b128 v[40:43], %[xp] offset:5120\n\t" \
    "s_mov_b64 exec, %[em]\n\t" \
    "buffer_store_dwordx4 v[8:11], %[l16], %[rs], %[oo] offen offset:2048 sc1\n\t" \
    "s_mov_b64 exec, -1\n\t" \
    "v_mfma_f32_16x16x32_bf16 v[204:207], v[184:187], v[48:51], v[204:207]\n\t" \
    "s_waitcnt lgkmcnt(4)\n\t" \
    "v_mfma_f32_16x16x32_bf16 v[208:211], v[192:195], v[48:51], v[208:211]\n\t" \
    "s_mov_b64 exec, %[em]\n\t" \
    "buffer_store_dwordx4 v[12:15], %[l16], %[rs], %[oo] offen offset:3072 sc1\n\t" \
    "s_mov_b64 exec, -1\n\t" \
    "s_add_u32 %[sx], %[sx], 262144\n\t" \
    "s_add_u32 %[t], %[t], 1\n\t" \
    "s_add_u32 %[oo], %[oo], 524288\n\t" \
    "s_waitcnt vmcnt(8)\n\t" \
    "s_cmp_gt_u32 %[t], " STR(SQUICK) " + 1\n\t" \
    "s_cbranch_scc1 B1_%=\n\t" \
    "s_waitcnt vmcnt(0)\n\t" \
    "B1_%=:\n\t" \
    "s_barrier\n\t" \
    "s_cmp_lt_u32 %[t], %[te]\n\t" \
    "s_cbranch_scc1 L1_%=\n\t" \
    "s_cmp_eq_u32 %[fin], %[te]\n\t" \
    "s_cbranch_scc0 E_%=\n\t" \
    "s_nop 4\n\t" \
    "s_barrier\n\t" \
    "ds_read_b128 v[52:55], %[pa] offset:0\n\t" \
    "ds_read_b128 v[56:59], %[pa] offset:1024\n\t" \
    "ds_read_b128 v[60:63], %[pa] offset:2048\n\t" \
    "ds_read_b128 v[64:67], %[pa] offset:3072\n\t" \
    "s_add_u32 %[b], %[oo], 0x1000\n\t" \
    "s_waitcnt lgkmcnt(0)\n\t" \
    "v_add_f32 v196, v196, v52\n\t" \
    "v_add_f32 v197, v197, v53\n\t" \
    "v_add_f32 v198, v198, v54\n\t" \
    "v_add_f32 v199, v199, v55\n\t" \
    "v_add_f32 v200, v200, v56\n\t" \
    "v_add_f32 v201, v201, v57\n\t" \
    "v_add_f32 v202, v202, v58\n\t" \
    "v_add_f32 v203, v203, v59\n\t" \
    "v_add_f32 v204, v204, v60\n\t" \
    "v_add_f32 v205, v205, v61\n\t" \
    "v_add_f32 v206, v206, v62\n\t" \
    "v_add_f32 v207, v207, v63\n\t" \
    "v_add_f32 v208, v208, v64\n\t" \
    "v_add_f32 v209, v209, v65\n\t" \
    "v_add_f32 v210, v210, v66\n\t" \
    "v_add_f32 v211, v211, v67\n\t" \
    "buffer_store_dwordx4 v[196:199], %[l16], %[rs], %[oo] offen offset:0 sc1\n\t" \
    "buffer_store_dwordx4 v[200:203], %[l16], %[rs], %[oo] offen offset:1024 sc1\n\t" \
    "buffer_store_dwordx4 v[204:207], %[l16], %[rs], %[oo] offen offset:2048 sc1\n\t" \
    "buffer_store_dwordx4 v[208:211], %[l16], %[rs], %[oo] offen offset:3072 sc1\n\t" \
    "E_%=:\n\t" \
    "s_setprio 0\n\t" \
    "s_waitcnt lgkmcnt(0)\n\t" \
    "s_nop 7\n\t" \
    : [t] "+s"(t), [sx] "+s"(sx), [oo] "+s"(oo), [a] "=&s"(tmp), [b] "=&s"(tmp2), [em] "=&s"(em), "+{v[20:27]}"(xf[0]), "+{v[28:35]}"(xf[1]), "+{v[36:43]}"(xf[2]), "+{v[196:203]}"(ac[0]), "+{v[204:211]}"(ac[1]) \
    : [te] "s"(TE_), [fin] "s"(FIN_), [kh] "s"(kh), [sd] "s"(sd), [rs] "s"(rs), [l16] "v"(l16), [xp] "v"(xp), [pa] "v"(pa), [fm] "s"(fm), [pm] "s"(pm), [fo] "s"(fo), [po] "s"(po), [pd] "s"(pd), [tag] "s"(P.tag), "{v[68:75]}"(w[0][0]), "{v[76:83]}"(w[0][1]), "{v[84:91]}"(w[1][0]), "{v[92:99]}"(w[1][1]), "{v[100:107]}"(w[2][0]), "{v[108:115]}"(w[2][1]), "{v[116:123]}"(w[3][0]), "{v[124:131]}"(w[3][1]), "{v[132:139]}"(w[4][0]), "{v[140:147]}"(w[4][1]), "{v[148:155]}"(w[5][0]), "{v[156:163]}"(w[5][1]), "{v[164:171]}"(w[6][0]), "{v[172:179]}"(w[6][1]), "{v[180:187]}"(w[7][0]), "{v[188:195]}"(w[7][1]) \
    : "memory", "scc", "v0", "v1", "v2", "v3", "v4", "v5", "v6", "v7", "v8", "v9", "v10", "v11", "v12", "v13", "v14", "v15", "v44", "v45", "v46", "v47", "v48", "v49", "v50", "v51", "v52", "v53", "v54", "v55", "v56", "v57", "v58", "v59", "v60", "v61", "v62", "v63", "v64", "v65", "v66", "v67")

#define ONE_X(XP_) \
  asm volatile( \
    "ds_read_b128 v[20:23], %[xp] offset:0\n\t" \
    "ds_read_b128 v[24:27], %[xp] offset:1024\n\t" \
    "ds_read_b128 v[28:31], %[xp] offset:2048\n\t" \
    "ds_read_b128 v[32:35], %[xp] offset:3072\n\t" \
    "ds_read_b128 v[36:39], %[xp] offset:4096\n\t" \
    "ds_read_b128 v[40:43], %[xp] offset:5120\n\t" \
    "s_waitcnt lgkmcnt(4)\n\t" \
    "v_mfma_f32_16x16x32_bf16 v[0:3], v[68:71], v[20:23], 0\n\t" \
    "ds_read_b128 v[44:47], %[xp] offset:6144\n\t" \
    "v_mfma_f32_16x16x32_bf16 v[4:7], v[76:79], v[20:23], 0\n\t" \
    "ds_read_b128 v[48:51], %[xp] offset:7168\n\t" \
    "v_mfma_f32_16x16x32_bf16 v[8:11], v[68:71], v[24:27], 0\n\t" \
    "s_waitcnt lgkmcnt(4)\n\t" \
    "v_mfma_f32_16x16x32_bf16 v[12:15], v[76:79], v[24:27], 0\n\t" \
    "v_mfma_f32_16x16x32_bf16 v[0:3], v[72:75], v[28:31], v[0:3]\n\t" \
    "ds_read_b128 v[20:23], %[xp] offset:8192\n\t" \
    "v_mfma_f32_16x16x32_bf16 v[4:7], v[80:83], v[28:31], v[4:7]\n\t" \
    "ds_read_b128 v[24:27], %[xp] offset:9216\n\t" \
    "v_mfma_f32_16x16x32_bf16 v[8:11], v[72:75], v[32:35], v[8:11]\n\t" \
    "s_waitcnt lgkmcnt(4)\n\t" \
    "v_mfma_f32_16x16x32_bf16 v[12:15], v[80:83], v[32:35], v[12:15]\n\t" \
    "v_mfma_f32_16x16x32_bf16 v[0:3], v[84:87], v[36:39], v[0:3]\n\t" \
    "ds_read_b128 v[28:31], %[xp] offset:10240\n\t" \
    "v_mfma_f32_16x16x32_bf16 v[4:7], v[92:95], v[36:39], v[4:7]\n\t" \
    "ds_read_b128 v[32:35], %[xp] offset:11264\n\t" \
    "v_mfma_f32_16x16x32_bf16 v[8:11], v[84:87], v[40:43], v[8:11]\n\t" \
    "s_waitcnt lgkmcnt(4)\n\t" \
    "v_mfma_f32_16x16x32_bf16 v[12:15], v[92:95], v[40:43], v[12:15]\n\t" \
    "v_mfma_f32_16x16x32_bf16 v[0:3], v[88:91], v[44:47], v[0:3]\n\t" \
    "ds_read_b128 v[36:39], %[xp] offset:12288\n\t" \
    "v_mfma_f32_16x16x32_bf16 v[4:7], v[96:99], v[44:47], v[4:7]\n\t" \
    "ds_read_b128 v[40:43], %[xp] offset:13312\n\t" \
    "v_mfma_f32_16x16x32_bf16 v[8:11], v[88:91], v[48:51], v[8:11]\n\t" \
    "s_waitcnt lgkmcnt(4)\n\t" \
    "v_mfma_f32_16x16x32_bf16 v[12:15], v[96:99], v[48:51], v[12:15]\n\t" \
    "v_mfma_f32_16x16x32_bf16 v[0:3], v[100:103], v[20:23], v[0:3]\n\t" \
    "ds_read_b128 v[44:47], %[xp] offset:14336\n\t" \
    "v_mfma_f32_16x16x32_bf16 v[4:7], v[108:111], v[20:23], v[4:7]\n\t" \
    "ds_read_b128 v[48:51], %[xp] offset:15360\n\t" \
    "v_mfma_f32_16x16x32_bf16 v[8:11], v[100:103], v[24:27], v[8:11]\n\t" \
    "s_waitcnt lgkmcnt(4)\n\t" \
    "v_mfma_f32_16x16x32_bf16 v[12:15], v[108:111], v[24:27], v[12:15]\n\t" \
    "s_waitcnt vmcnt(0)\n\t" \
    "s_barrier\n\t" \
    "v_mfma_f32_16x16x32_bf16 v[0:3], v[104:107], v[28:31], v[0:3]\n\t" \
    "ds_read_b128 v[20:23], %[xp] offset:16384\n\t" \
    "v_mfma_f32_16x16x32_bf16 v[4:7], v[112:115], v[28:31], v[4:7]\n\t" \
    "ds_read_b128 v[24:27], %[xp] offset:17408\n\t" \
    "v_mfma_f32_16x16x32_bf16 v[8:11], v[104:107], v[32:35], v[8:11]\n\t" \
    "s_waitcnt lgkmcnt(4)\n\t" \
    "v_mfma_f32_16x16x32_bf16 v[12:15], v[112:115], v[32:35], v[12:15]\n\t" \
    "buffer_load_dword v52, %[pv], %[rs], 0 offen sc1\n\t" \
    "v_mfma_f32_16x16x32_bf16 v[0:3], v[116:119], v[36:39], v[0:3]\n\t" \
    "ds_read_b128 v[28:31], %[xp] offset:18432\n\t" \
    "v_mfma_f32_16x16x32_bf16 v[4:7], v[124:127], v[36:39], v[4:7]\n\t" \
    "ds_read_b128 v[32:35], %[xp] offset:19456\n\t" \
    "v_mfma_f32_16x16x32_bf16 v[8:11], v[116:119], v[40:43], v[8:11]\n\t" \
    "s_waitcnt lgkmcnt(4)\n\t" \
    "v_mfma_f32_16x16x32_bf16 v[12:15], v[124:127], v[40:43], v[12:15]\n\t" \
    "v_mfma_f32_16x16x32_bf16 v[0:3], v[120:123], v[44:47], v[0:3]\n\t" \
    "ds_read_b128 v[36:39], %[xp] offset:20480\n\t" \
    "v_mfma_f32_16x16x32_bf16 v[4:7], v[128:131], v[44:47], v[4:7]\n\t" \
    "ds_read_b128 v[40:43], %[xp] offset:21504\n\t" \
    "v_mfma_f32_16x16x32_bf16 v[8:11], v[120:123], v[48:51], v[8:11]\n\t" \
    "s_waitcnt lgkmcnt(4)\n\t" \
    "v_mfma_f32_16x16x32_bf16 v[12:15], v[128:131], v[48:51], v[12:15]\n\t" \
    "v_mfma_f32_16x16x32_bf16 v[0:3], v[132:135], v[20:23], v[0:3]\n\t" \
    "ds_read_b128 v[44:47], %[xp] offset:22528\n\t" \
    "v_mfma_f32_16x16x32_bf16 v[4:7], v[140:143], v[20:23], v[4:7]\n\t" \
    "ds_read_b128 v[48:51], %[xp] offset:23552\n\t" \
    "v_mfma_f32_16x16x32_bf16 v[8:11], v[132:135], v[24:27], v[8:11]\n\t" \
    "s_waitcnt lgkmcnt(4)\n\t" \
    "v_mfma_f32_16x16x32_bf16 v[12:15], v[140:143], v[24:27], v[12:15]\n\t" \
    "v_mfma_f32_16x16x32_bf16 v[0:3], v[136:139], v[28:31], v[0:3]\n\t" \
    "ds_read_b128 v[20:23], %[xp] offset:24576\n\t" \
    "v_mfma_f32_16x16x32_bf16 v[4:7], v[144:147], v[28:31], v[4:7]\n\t" \
    "ds_read_b128 v[24:27], %[xp] offset:25600\n\t" \
    "v_mfma_f32_16x16x32_bf16 v[8:11], v[136:139], v[32:35], v[8:11]\n\t" \
    "s_waitcnt lgkmcnt(4)\n\t" \
    "v_mfma_f32_16x16x32_bf16 v[12:15], v[144:147], v[32:35], v[12:15]\n\t" \
    "v_mfma_f32_16x16x32_bf16 v[0:3], v[148:151], v[36:39], v[0:3]\n\t" \
    "ds_read_b128 v[28:31], %[xp] offset:26624\n\t" \
    "v_mfma_f32_16x16x32_bf16 v[4:7], v[156:159], v[36:39], v[4:7]\n\t" \
    "ds_read_b128 v[32:35], %[xp] offset:27648\n\t" \
    "v_mfma_f32_16x16x32_bf16 v[8:11], v[148:151], v[40:43], v[8:11]\n\t" \
    "s_waitcnt lgkmcnt(4)\n\t" \
    "v_mfma_f32_16x16x32_bf16 v[12:15], v[156:159], v[40:43], v[12:15]\n\t" \
    "v_mfma_f32_16x16x32_bf16 v[0:3], v[152:155], v[44:47], v[0:3]\n\t" \
    "ds_read_b128 v[36:39], %[xp] offset:28672\n\t" \
    "v_mfma_f32_16x16x32_bf16 v[4:7], v[160:163], v[44:47], v[4:7]\n\t" \
    "ds_read_b128 v[40:43], %[xp] offset:29696\n\t" \
    "v_mfma_f32_16x16x32_bf16 v[8:11], v[152:155], v[48:51], v[8:11]\n\t" \
    "s_waitcnt lgkmcnt(4)\n\t" \
    "v_mfma_f32_16x16x32_bf16 v[12:15], v[160:163], v[48:51], v[12:15]\n\t" \
    "v_mfma_f32_16x16x32_bf16 v[0:3], v[164:167], v[20:23], v[0:3]\n\t" \
    "ds_read_b128 v[44:47], %[xp] offset:30720\n\t" \
    "v_mfma_f32_16x16x32_bf16 v[4:7], v[172:175], v[20:23], v[4:7]\n\t" \
    "ds_read_b128 v[48:51], %[xp] offset:31744\n\t" \
    "v_mfma_f32_16x16x32_bf16 v[8:11], v[164:167], v[24:27], v[8:11]\n\t" \
    "s_waitcnt lgkmcnt(4)\n\t" \
    "v_mfma_f32_16x16x32_bf16 v[12:15], v[172:175], v[24:27], v[12:15]\n\t" \
    "v_mfma_f32_16x16x32_bf16 v[0:3], v[168:171], v[28:31], v[0:3]\n\t" \
    "v_mfma_f32_16x16x32_bf16 v[4:7], v[176:179], v[28:31], v[4:7]\n\t" \
    "v_mfma_f32_16x16x32_bf16 v[8:11], v[168:171], v[32:35], v[8:11]\n\t" \
    "s_waitcnt lgkmcnt(2)\n\t" \
    "v_mfma_f32_16x16x32_bf16 v[12:15], v[176:179], v[32:35], v[12:15]\n\t" \
    "v_mfma_f32_16x16x32_bf16 v[0:3], v[180:183], v[36:39], v[0:3]\n\t" \
    "v_mfma_f32_16x16x32_bf16 v[4:7], v[188:191], v[36:39], v[4:7]\n\t" \
    "v_mfma_f32_16x16x32_bf16 v[8:11], v[180:183], v[40:43], v[8:11]\n\t" \
    "s_waitcnt lgkmcnt(0)\n\t" \
    "v_mfma_f32_16x16x32_bf16 v[12:15], v[188:191], v[40:43], v[12:15]\n\t" \
    "v_mfma_f32_16x16x32_bf16 v[0:3], v[184:187], v[44:47], v[0:3]\n\t" \
    "v_mfma_f32_16x16x32_bf16 v[4:7], v[192:195], v[44:47], v[4:7]\n\t" \
    "v_mfma_f32_16x16x32_bf16 v[8:11], v[184:187], v[48:51], v[8:11]\n\t" \
    "v_mfma_f32_16x16x32_bf16 v[12:15], v[192:195], v[48:51], v[12:15]\n\t" \
    "s_waitcnt vmcnt(0)\n\t" \
    "s_nop 7\n\t" \
    : "=&{v52}"(pg), "=&{v[0:3]}"(a[0]), "=&{v[4:7]}"(a[1]), "=&{v[8:11]}"(a[2]), "=&{v[12:15]}"(a[3]) \
    : [xp] "v"(XP_), [pv] "v"(pv), [rs] "s"(rs), "{v[68:75]}"(w[0][0]), "{v[76:83]}"(w[0][1]), "{v[84:91]}"(w[1][0]), "{v[92:99]}"(w[1][1]), "{v[100:107]}"(w[2][0]), "{v[108:115]}"(w[2][1]), "{v[116:123]}"(w[3][0]), "{v[124:131]}"(w[3][1]), "{v[132:139]}"(w[4][0]), "{v[140:147]}"(w[4][1]), "{v[148:155]}"(w[5][0]), "{v[156:163]}"(w[5][1]), "{v[164:171]}"(w[6][0]), "{v[172:179]}"(w[6][1]), "{v[180:187]}"(w[7][0]), "{v[188:195]}"(w[7][1]) \
    : "memory", "v20", "v21", "v22", "v23", "v24", "v25", "v26", "v27", "v28", "v29", "v30", "v31", "v32", "v33", "v34", "v35", "v36", "v37", "v38", "v39", "v40", "v41", "v42", "v43", "v44", "v45", "v46", "v47", "v48", "v49", "v50", "v51")

#define ONE_K(XP_) \
  asm volatile( \
    "ds_read_b128 v[20:23], %[xp] offset:0\n\t" \
    "ds_read_b128 v[24:27], %[xp] offset:1024\n\t" \
    "ds_read_b128 v[28:31], %[xp] offset:2048\n\t" \
    "ds_read_b128 v[32:35], %[xp] offset:3072\n\t" \
    "ds_read_b128 v[36:39], %[xp] offset:4096\n\t" \
    "ds_read_b128 v[40:43], %[xp] offset:5120\n\t" \
    "s_waitcnt lgkmcnt(4)\n\t" \
    "v_mfma_f32_16x16x32_bf16 v[0:3], v[68:71], v[20:23], 0\n\t" \
    "ds_read_b128 v[44:47], %[xp] offset:6144\n\t" \
    "v_mfma_f32_16x16x32_bf16 v[4:7], v[76:79], v[20:23], 0\n\t" \
    "ds_read_b128 v[48:51], %[xp] offset:7168\n\t" \
    "v_mfma_f32_16x16x32_bf16 v[8:11], v[68:71], v[24:27], 0\n\t" \
    "s_waitcnt lgkmcnt(4)\n\t" \
    "v_mfma_f32_16x16x32_bf16 v[12:15], v[76:79], v[24:27], 0\n\t" \
    "v_mfma_f32_16x16x32_bf16 v[0:3], v[72:75], v[28:31], v[0:3]\n\t" \
    "ds_read_b128 v[20:23], %[xp] offset:8192\n\t" \
    "v_mfma_f32_16x16x32_bf16 v[4:7], v[80:83], v[28:31], v[4:7]\n\t" \
    "ds_read_b128 v[24:27], %[xp] offset:9216\n\t" \
    "v_mfma_f32_16x16x32_bf16 v[8:11], v[72:75], v[32:35], v[8:11]\n\t" \
    "s_waitcnt lgkmcnt(4)\n\t" \
    "v_mfma_f32_16x16x32_bf16 v[12:15], v[80:83], v[32:35], v[12:15]\n\t" \
    "v_mfma_f32_16x16x32_bf16 v[0:3], v[84:87], v[36:39], v[0:3]\n\t" \
    "ds_read_b128 v[28:31], %[xp] offset:10240\n\t" \
    "v_mfma_f32_16x16x32_bf16 v[4:7], v[92:95], v[36:39], v[4:7]\n\t" \
    "ds_read_b128 v[32:35], %[xp] offset:11264\n\t" \
    "v_mfma_f32_16x16x32_bf16 v[8:11], v[84:87], v[40:43], v[8:11]\n\t" \
    "s_waitcnt lgkmcnt(4)\n\t" \
    "v_mfma_f32_16x16x32_bf16 v[12:15], v[92:95], v[40:43], v[12:15]\n\t" \
    "v_mfma_f32_16x16x32_bf16 v[0:3], v[88:91], v[44:47], v[0:3]\n\t" \
    "ds_read_b128 v[36:39], %[xp] offset:12288\n\t" \
    "v_mfma_f32_16x16x32_bf16 v[4:7], v[96:99], v[44:47], v[4:7]\n\t" \
    "ds_read_b128 v[40:43], %[xp] offset:13312\n\t" \
    "v_mfma_f32_16x16x32_bf16 v[8:11], v[88:91], v[48:51], v[8:11]\n\t" \
    "s_waitcnt lgkmcnt(4)\n\t" \
    "v_mfma_f32_16x16x32_bf16 v[12:15], v[96:99], v[48:51], v[12:15]\n\t" \
    "v_mfma_f32_16x16x32_bf16 v[0:3], v[100:103], v[20:23], v[0:3]\n\t" \
    "ds_read_b128 v[44:47], %[xp] offset:14336\n\t" \
    "v_mfma_f32_16x16x32_bf16 v[4:7], v[108:111], v[20:23], v[4:7]\n\t" \
    "ds_read_b128 v[48:51], %[xp] offset:15360\n\t" \
    "v_mfma_f32_16x16x32_bf16 v[8:11], v[100:103], v[24:27], v[8:11]\n\t" \
    "s_waitcnt lgkmcnt(4)\n\t" \
    "v_mfma_f32_16x16x32_bf16 v[12:15], v[108:111], v[24:27], v[12:15]\n\t" \
    "s_waitcnt vmcnt(8)\n\t" \
    "s_barrier\n\t" \
    "v_mfma_f32_16x16x32_bf16 v[0:3], v[104:107], v[28:31], v[0:3]\n\t" \
    "ds_read_b128 v[20:23], %[xp] offset:16384\n\t" \
    "v_mfma_f32_16x16x32_bf16 v[4:7], v[112:115], v[28:31], v[4:7]\n\t" \
    "ds_read_b128 v[24:27], %[xp] offset:17408\n\t" \
    "v_mfma_f32_16x16x32_bf16 v[8:11], v[104:107], v[32:35], v[8:11]\n\t" \
    "s_waitcnt lgkmcnt(4)\n\t" \
    "v_mfma_f32_16x16x32_bf16 v[12:15], v[112:115], v[32:35], v[12:15]\n\t" \
    "v_mfma_f32_16x16x32_bf16 v[0:3], v[116:119], v[36:39], v[0:3]\n\t" \
    "ds_read_b128 v[28:31], %[xp] offset:18432\n\t" \
    "v_mfma_f32_16x16x32_bf16 v[4:7], v[124:127], v[36:39], v[4:7]\n\t" \
    "ds_read_b128 v[32:35], %[xp] offset:19456\n\t" \
    "v_mfma_f32_16x16x32_bf16 v[8:11], v[116:119], v[40:43], v[8:11]\n\t" \
    "s_waitcnt lgkmcnt(4)\n\t" \
    "v_mfma_f32_16x16x32_bf16 v[12:15], v[124:127], v[40:43], v[12:15]\n\t" \
    "v_mfma_f32_16x16x32_bf16 v[0:3], v[120:123], v[44:47], v[0:3]\n\t" \
    "ds_read_b128 v[36:39], %[xp] offset:20480\n\t" \
    "v_mfma_f32_16x16x32_bf16 v[4:7], v[128:131], v[44:47], v[4:7]\n\t" \
    "ds_read_b128 v[40:43], %[xp] offset:21504\n\t" \
    "v_mfma_f32_16x16x32_bf16 v[8:11], v[120:123], v[48:51], v[8:11]\n\t" \
    "s_waitcnt lgkmcnt(4)\n\t" \
    "v_mfma_f32_16x16x32_bf16 v[12:15], v[128:131], v[48:51], v[12:15]\n\t" \
    "v_mfma_f32_16x16x32_bf16 v[0:3], v[132:135], v[20:23], v[0:3]\n\t" \
    "ds_read_b128 v[44:47], %[xp] offset:22528\n\t" \
    "v_mfma_f32_16x16x32_bf16 v[4:7], v[140:143], v[20:23], v[4:7]\n\t" \
    "ds_read_b128 v[48:51], %[xp] offset:23552\n\t" \
    "v_mfma_f32_16x16x32_bf16 v[8:11], v[132:135], v[24:27], v[8:11]\n\t" \
    "s_waitcnt lgkmcnt(4)\n\t" \
    "v_mfma_f32_16x16x32_bf16 v[12:15], v[140:143], v[24:27], v[12:15]\n\t" \
    "v_mfma_f32_16x16x32_bf16 v[0:3], v[136:139], v[28:31], v[0:3]\n\t" \
    "ds_read_b128 v[20:23], %[xp] offset:24576\n\t" \
    "v_mfma_f32_16x16x32_bf16 v[4:7], v[144:147], v[28:31], v[4:7]\n\t" \
    "ds_read_b128 v[24:27], %[xp] offset:25600\n\t" \
    "v_mfma_f32_16x16x32_bf16 v[8:11], v[136:139], v[32:35], v[8:11]\n\t" \
    "s_waitcnt lgkmcnt(4)\n\t" \
    "v_mfma_f32_16x16x32_bf16 v[12:15], v[144:147], v[32:35], v[12:15]\n\t" \
    "v_mfma_f32_16x16x32_bf16 v[0:3], v[148:151], v[36:39], v[0:3]\n\t" \
    "ds_read_b128 v[28:31], %[xp] offset:26624\n\t" \
    "v_mfma_f32_16x16x32_bf16 v[4:7], v[156:159], v[36:39], v[4:7]\n\t" \
    "ds_read_b128 v[32:35], %[xp] offset:27648\n\t" \
    "v_mfma_f32_16x16x32_bf16 v[8:11], v[148:151], v[40:43], v[8:11]\n\t" \
    "s_waitcnt lgkmcnt(4)\n\t" \
    "v_mfma_f32_16x16x32_bf16 v[12:15], v[156:159], v[40:43], v[12:15]\n\t" \
    "v_mfma_f32_16x16x32_bf16 v[0:3], v[152:155], v[44:47], v[0:3]\n\t" \
    "ds_read_b128 v[36:39], %[xp] offset:28672\n\t" \
    "v_mfma_f32_16x16x32_bf16 v[4:7], v[160:163], v[44:47], v[4:7]\n\t" \
    "ds_read_b128 v[40:43], %[xp] offset:29696\n\t" \
    "v_mfma_f32_16x16x32_bf16 v[8:11], v[152:155], v[48:51], v[8:11]\n\t" \
    "s_waitcnt lgkmcnt(4)\n\t" \
    "v_mfma_f32_16x16x32_bf16 v[12:15], v[160:163], v[48:51], v[12:15]\n\t" \
    "v_mfma_f32_16x16x32_bf16 v[0:3], v[164:167], v[20:23], v[0:3]\n\t" \
    "ds_read_b128 v[44:47], %[xp] offset:30720\n\t" \
    "v_mfma_f32_16x16x32_bf16 v[4:7], v[172:175], v[20:23], v[4:7]\n\t" \
    "ds_read_b128 v[48:51], %[xp] offset:31744\n\t" \
    "v_mfma_f32_16x16x32_bf16 v[8:11], v[164:167], v[24:27], v[8:11]\n\t" \
    "s_waitcnt lgkmcnt(4)\n\t" \
    "v_mfma_f32_16x16x32_bf16 v[12:15], v[172:175], v[24:27], v[12:15]\n\t" \
    "v_mfma_f32_16x16x32_bf16 v[0:3], v[168:171], v[28:31], v[0:3]\n\t" \
    "v_mfma_f32_16x16x32_bf16 v[4:7], v[176:179], v[28:31], v[4:7]\n\t" \
    "v_mfma_f32_16x16x32_bf16 v[8:11], v[168:171], v[32:35], v[8:11]\n\t" \
    "s_waitcnt lgkmcnt(2)\n\t" \
    "v_mfma_f32_16x16x32_bf16 v[12:15], v[176:179], v[32:35], v[12:15]\n\t" \
    "v_mfma_f32_16x16x32_bf16 v[0:3], v[180:183], v[36:39], v[0:3]\n\t" \
    "v_mfma_f32_16x16x32_bf16 v[4:7], v[188:191], v[36:39], v[4:7]\n\t" \
    "v_mfma_f32_16x16x32_bf16 v[8:11], v[180:183], v[40:43], v[8:11]\n\t" \
    "s_waitcnt lgkmcnt(0)\n\t" \
    "v_mfma_f32_16x16x32_bf16 v[12:15], v[188:191], v[40:43], v[12:15]\n\t" \
    "v_mfma_f32_16x16x32_bf16 v[0:3], v[184:187], v[44:47], v[0:3]\n\t" \
    "v_mfma_f32_16x16x32_bf16 v[4:7], v[192:195], v[44:47], v[4:7]\n\t" \
    "v_mfma_f32_16x16x32_bf16 v[8:11], v[184:187], v[48:51], v[8:11]\n\t" \
    "v_mfma_f32_16x16x32_bf16 v[12:15], v[192:195], v[48:51], v[12:15]\n\t" \
    "s_nop 7\n\t" \
    : "=&{v[0:3]}"(a[0]), "=&{v[4:7]}"(a[1]), "=&{v[8:11]}"(a[2]), "=&{v[12:15]}"(a[3]) \
    : [xp] "v"(XP_), "{v[68:75]}"(w[0][0]), "{v[76:83]}"(w[0][1]), "{v[84:91]}"(w[1][0]), "{v[92:99]}"(w[1][1]), "{v[100:107]}"(w[2][0]), "{v[108:115]}"(w[2][1]), "{v[116:123]}"(w[3][0]), "{v[124:131]}"(w[3][1]), "{v[132:139]}"(w[4][0]), "{v[140:147]}"(w[4][1]), "{v[148:155]}"(w[5][0]), "{v[156:163]}"(w[5][1]), "{v[164:171]}"(w[6][0]), "{v[172:179]}"(w[6][1]), "{v[180:187]}"(w[7][0]), "{v[188:195]}"(w[7][1]) \
    : "memory", "v20", "v21", "v22", "v23", "v24", "v25", "v26", "v27", "v28", "v29", "v30", "v31", "v32", "v33", "v34", "v35", "v36", "v37", "v38", "v39", "v40", "v41", "v42", "v43", "v44", "v45", "v46", "v47", "v48", "v49", "v50", "v51")

#define ONE_V(XP_) \
  asm volatile( \
    "ds_read_b128 v[20:23], %[xp] offset:0\n\t" \
    "ds_read_b128 v[24:27], %[xp] offset:1024\n\t" \
    "ds_read_b128 v[28:31], %[xp] offset:2048\n\t" \
    "ds_read_b128 v[32:35], %[xp] offset:3072\n\t" \
    "ds_read_b128 v[36:39], %[xp] offset:4096\n\t" \
    "ds_read_b128 v[40:43], %[xp] offset:5120\n\t" \
    "s_waitcnt lgkmcnt(4)\n\t" \
    "v_mfma_f32_16x16x32_bf16 v[0:3], v[20:23], v[68:71], 0\n\t" \
    "ds_read_b128 v[44:47], %[xp] offset:6144\n\t" \
    "v_mfma_f32_16x16x32_bf16 v[4:7], v[20:23], v[76:79], 0\n\t" \
    "ds_read_b128 v[48:51], %[xp] offset:7168\n\t" \
    "v_mfma_f32_16x16x32_bf16 v[8:11], v[24:27], v[68:71], 0\n\t" \
    "s_waitcnt lgkmcnt(4)\n\t" \
    "v_mfma_f32_16x16x32_bf16 v[12:15], v[24:27], v[76:79], 0\n\t" \
    "v_mfma_f32_16x16x32_bf16 v[0:3], v[28:31], v[72:75], v[0:3]\n\t" \
    "ds_read_b128 v[20:23], %[xp] offset:8192\n\t" \
    "v_mfma_f32_16x16x32_bf16 v[4:7], v[28:31], v[80:83], v[4:7]\n\t" \
    "ds_read_b128 v[24:27], %[xp] offset:9216\n\t" \
    "v_mfma_f32_16x16x32_bf16 v[8:11], v[32:35], v[72:75], v[8:11]\n\t" \
    "s_waitcnt lgkmcnt(4)\n\t" \
    "v_mfma_f32_16x16x32_bf16 v[12:15], v[32:35], v[80:83], v[12:15]\n\t" \
    "v_mfma_f32_16x16x32_bf16 v[0:3], v[36:39], v[84:87], v[0:3]\n\t" \
    "ds_read_b128 v[28:31], %[xp] offset:10240\n\t" \
    "v_mfma_f32_16x16x32_bf16 v[4:7], v[36:39], v[92:95], v[4:7]\n\t" \
    "ds_read_b128 v[32:35], %[xp] offset:11264\n\t" \
    "v_mfma_f32_16x16x32_bf16 v[8:11], v[40:43], v[84:87], v[8:11]\n\t" \
    "s_waitcnt lgkmcnt(4)\n\t" \
    "v_mfma_f32_16x16x32_bf16 v[12:15], v[40:43], v[92:95], v[12:15]\n\t" \
    "v_mfma_f32_16x16x32_bf16 v[0:3], v[44:47], v[88:91], v[0:3]\n\t" \
    "ds_read_b128 v[36:39], %[xp] offset:12288\n\t" \
    "v_mfma_f32_16x16x32_bf16 v[4:7], v[44:47], v[96:99], v[4:7]\n\t" \
    "ds_read_b128 v[40:43], %[xp] offset:13312\n\t" \
    "v_mfma_f32_16x16x32_bf16 v[8:11], v[48:51], v[88:91], v[8:11]\n\t" \
    "s_waitcnt lgkmcnt(4)\n\t" \
    "v_mfma_f32_16x16x32_bf16 v[12:15], v[48:51], v[96:99], v[12:15]\n\t" \
    "v_mfma_f32_16x16x32_bf16 v[0:3], v[20:23], v[100:103], v[0:3]\n\t" \
    "ds_read_b128 v[44:47], %[xp] offset:14336\n\t" \
    "v_mfma_f32_16x16x32_bf16 v[4:7], v[20:23], v[108:111], v[4:7]\n\t" \
    "ds_read_b128 v[48:51], %[xp] offset:15360\n\t" \
    "v_mfma_f32_16x16x32_bf16 v[8:11], v[24:27], v[100:103], v[8:11]\n\t" \
    "s_waitcnt lgkmcnt(4)\n\t" \
    "v_mfma_f32_16x16x32_bf16 v[12:15], v[24:27], v[108:111], v[12:15]\n\t" \
    "s_waitcnt vmcnt(8)\n\t" \
    "s_barrier\n\t" \
    "v_mfma_f32_16x16x32_bf16 v[0:3], v[28:31], v[104:107], v[0:3]\n\t" \
    "ds_read_b128 v[20:23], %[xp] offset:16384\n\t" \
    "v_mfma_f32_16x16x32_bf16 v[4:7], v[28:31], v[112:115], v[4:7]\n\t" \
    "ds_read_b128 v[24:27], %[xp] offset:17408\n\t" \
    "v_mfma_f32_16x16x32_bf16 v[8:11], v[32:35], v[104:107], v[8:11]\n\t" \
    "s_waitcnt lgkmcnt(4)\n\t" \
    "v_mfma_f32_16x16x32_bf16 v[12:15], v[32:35], v[112:115], v[12:15]\n\t" \
    "v_mfma_f32_16x16x32_bf16 v[0:3], v[36:39], v[116:119], v[0:3]\n\t" \
    "ds_read_b128 v[28:31], %[xp] offset:18432\n\t" \
    "v_mfma_f32_16x16x32_bf16 v[4:7], v[36:39], v[124:127], v[4:7]\n\t" \
    "ds_read_b128 v[32:35], %[xp] offset:19456\n\t" \
    "v_mfma_f32_16x16x32_bf16 v[8:11], v[40:43], v[116:119], v[8:11]\n\t" \
    "s_waitcnt lgkmcnt(4)\n\t" \
    "v_mfma_f32_16x16x32_bf16 v[12:15], v[40:43], v[124:127], v[12:15]\n\t" \
    "v_mfma_f32_16x16x32_bf16 v[0:3], v[44:47], v[120:123], v[0:3]\n\t" \
    "ds_read_b128 v[36:39], %[xp] offset:20480\n\t" \
    "v_mfma_f32_16x16x32_bf16 v[4:7], v[44:47], v[128:131], v[4:7]\n\t" \
    "ds_read_b128 v[40:43], %[xp] offset:21504\n\t" \
    "v_mfma_f32_16x16x32_bf16 v[8:11], v[48:51], v[120:123], v[8:11]\n\t" \
    "s_waitcnt lgkmcnt(4)\n\t" \
    "v_mfma_f32_16x16x32_bf16 v[12:15], v[48:51], v[128:131], v[12:15]\n\t" \
    "v_mfma_f32_16x16x32_bf16 v[0:3], v[20:23], v[132:135], v[0:3]\n\t" \
    "ds_read_b128 v[44:47], %[xp] offset:22528\n\t" \
    "v_mfma_f32_16x16x32_bf16 v[4:7], v[20:23], v[140:143], v[4:7]\n\t" \
    "ds_read_b128 v[48:51], %[xp] offset:23552\n\t" \
    "v_mfma_f32_16x16x32_bf16 v[8:11], v[24:27], v[132:135], v[8:11]\n\t" \
    "s_waitcnt lgkmcnt(4)\n\t" \
    "v_mfma_f32_16x16x32_bf16 v[12:15], v[24:27], v[140:143], v[12:15]\n\t" \
    "v_mfma_f32_16x16x32_bf16 v[0:3], v[28:31], v[136:139], v[0:3]\n\t" \
    "ds_read_b128 v[20:23], %[xp] offset:24576\n\t" \
    "v_mfma_f32_16x16x32_bf16 v[4:7], v[28:31], v[144:147], v[4:7]\n\t" \
    "ds_read_b128 v[24:27], %[xp] offset:25600\n\t" \
    "v_mfma_f32_16x16x32_bf16 v[8:11], v[32:35], v[136:139], v[8:11]\n\t" \
    "s_waitcnt lgkmcnt(4)\n\t" \
    "v_mfma_f32_16x16x32_bf16 v[12:15], v[32:35], v[144:147], v[12:15]\n\t" \
    "v_mfma_f32_16x16x32_bf16 v[0:3], v[36:39], v[148:151], v[0:3]\n\t" \
    "ds_read_b128 v[28:31], %[xp] offset:26624\n\t" \
    "v_mfma_f32_16x16x32_bf16 v[4:7], v[36:39], v[156:159], v[4:7]\n\t" \
    "ds_read_b128 v[32:35], %[xp] offset:27648\n\t" \
    "v_mfma_f32_16x16x32_bf16 v[8:11], v[40:43], v[148:151], v[8:11]\n\t" \
    "s_waitcnt lgkmcnt(4)\n\t" \
    "v_mfma_f32_16x16x32_bf16 v[12:15], v[40:43], v[156:159], v[12:15]\n\t" \
    "v_mfma_f32_16x16x32_bf16 v[0:3], v[44:47], v[152:155], v[0:3]\n\t" \
    "ds_read_b128 v[36:39], %[xp] offset:28672\n\t" \
    "v_mfma_f32_16x16x32_bf16 v[4:7], v[44:47], v[160:163], v[4:7]\n\t" \
    "ds_read_b128 v[40:43], %[xp] offset:29696\n\t" \
    "v_mfma_f32_16x16x32_bf16 v[8:11], v[48:51], v[152:155], v[8:11]\n\t" \
    "s_waitcnt lgkmcnt(4)\n\t" \
    "v_mfma_f32_16x16x32_bf16 v[12:15], v[48:51], v[160:163], v[12:15]\n\t" \
    "v_mfma_f32_16x16x32_bf16 v[0:3], v[20:23], v[164:167], v[0:3]\n\t" \
    "ds_read_b128 v[44:47], %[xp] offset:30720\n\t" \
    "v_mfma_f32_16x16x32_bf16 v[4:7], v[20:23], v[172:175], v[4:7]\n\t" \
    "ds_read_b128 v[48:51], %[xp] offset:31744\n\t" \
    "v_mfma_f32_16x16x32_bf16 v[8:11], v[24:27], v[164:167], v[8:11]\n\t" \
    "s_waitcnt lgkmcnt(4)\n\t" \
    "v_mfma_f32_16x16x32_bf16 v[12:15], v[24:27], v[172:175], v[12:15]\n\t" \
    "v_mfma_f32_16x16x32_bf16 v[0:3], v[28:31], v[168:171], v[0:3]\n\t" \
    "v_mfma_f32_16x16x32_bf16 v[4:7], v[28:31], v[176:179], v[4:7]\n\t" \
    "v_mfma_f32_16x16x32_bf16 v[8:11], v[32:35], v[168:171], v[8:11]\n\t" \
    "s_waitcnt lgkmcnt(2)\n\t" \
    "v_mfma_f32_16x16x32_bf16 v[12:15], v[32:35], v[176:179], v[12:15]\n\t" \
    "v_mfma_f32_16x16x32_bf16 v[0:3], v[36:39], v[180:183], v[0:3]\n\t" \
    "v_mfma_f32_16x16x32_bf16 v[4:7], v[36:39], v[188:191], v[4:7]\n\t" \
    "v_mfma_f32_16x16x32_bf16 v[8:11], v[40:43], v[180:183], v[8:11]\n\t" \
    "s_waitcnt lgkmcnt(0)\n\t" \
    "v_mfma_f32_16x16x32_bf16 v[12:15], v[40:43], v[188:191], v[12:15]\n\t" \
    "v_mfma_f32_16x16x32_bf16 v[0:3], v[44:47], v[184:187], v[0:3]\n\t" \
    "v_mfma_f32_16x16x32_bf16 v[4:7], v[44:47], v[192:195], v[4:7]\n\t" \
    "v_mfma_f32_16x16x32_bf16 v[8:11], v[48:51], v[184:187], v[8:11]\n\t" \
    "v_mfma_f32_16x16x32_bf16 v[12:15], v[48:51], v[192:195], v[12:15]\n\t" \
    "s_nop 7\n\t" \
    : "=&{v[0:3]}"(a[0]), "=&{v[4:7]}"(a[1]), "=&{v[8:11]}"(a[2]), "=&{v[12:15]}"(a[3]) \
    : [xp] "v"(XP_), "{v[68:75]}"(w[0][0]), "{v[76:83]}"(w[0][1]), "{v[84:91]}"(w[1][0]), "{v[92:99]}"(w[1][1]), "{v[100:107]}"(w[2][0]), "{v[108:115]}"(w[2][1]), "{v[116:123]}"(w[3][0]), "{v[124:131]}"(w[3][1]), "{v[132:139]}"(w[4][0]), "{v[140:147]}"(w[4][1]), "{v[148:155]}"(w[5][0]), "{v[156:163]}"(w[5][1]), "{v[164:171]}"(w[6][0]), "{v[172:179]}"(w[6][1]), "{v[180:187]}"(w[7][0]), "{v[188:195]}"(w[7][1]) \
    : "memory", "v20", "v21", "v22", "v23", "v24", "v25", "v26", "v27", "v28", "v29", "v30", "v31", "v32", "v33", "v34", "v35", "v36", "v37", "v38", "v39", "v40", "v41", "v42", "v43", "v44", "v45", "v46", "v47", "v48", "v49", "v50", "v51")

#define H_LOOP1() \
  asm volatile( \
    "v_mov_b32 v20, 0\n\t" \
    "v_mov_b32 v21, 0\n\t" \
    "v_mov_b32 v22, 0\n\t" \
    "v_mov_b32 v23, 0\n\t" \
    "v_mov_b32 v24, 0\n\t" \
    "v_mov_b32 v25, 0\n\t" \
    "v_mov_b32 v26, 0\n\t" \
    "v_mov_b32 v27, 0\n\t" \
    "v_mov_b32 v28, 0\n\t" \
    "v_mov_b32 v29, 0\n\t" \
    "v_mov_b32 v30, 0\n\t" \
    "v_mov_b32 v31, 0\n\t" \
    "v_mov_b32 v32, 0\n\t" \
    "v_mov_b32 v33, 0\n\t" \
    "v_mov_b32 v34, 0\n\t" \
    "v_mov_b32 v35, 0\n\t" \
    "v_mov_b32 v36, 0\n\t" \
    "v_mov_b32 v37, 0\n\t" \
    "v_mov_b32 v38, 0\n\t" \
    "v_mov_b32 v39, 0\n\t" \
    "v_mov_b32 v40, 0\n\t" \
    "v_mov_b32 v41, 0\n\t" \
    "v_mov_b32 v42, 0\n\t" \
    "v_mov_b32 v43, 0\n\t" \
    "v_mov_b32 v44, 0\n\t" \
    "v_mov_b32 v45, 0\n\t" \
    "v_mov_b32 v46, 0\n\t" \
    "v_mov_b32 v47, 0\n\t" \
    "v_mov_b32 v48, 0\n\t" \
    "v_mov_b32 v49, 0\n\t" \
    "v_mov_b32 v50, 0\n\t" \
    "v_mov_b32 v51, 0\n\t" \
    "v_mov_b32 v52, 0\n\t" \
    "v_mov_b32 v53, 0\n\t" \
    "v_mov_b32 v54, 0\n\t" \
    "v_mov_b32 v55, 0\n\t" \
    "v_mov_b32 v56, 0\n\t" \
    "v_mov_b32 v57, 0\n\t" \
    "v_mov_b32 v58, 0\n\t" \
    "v_mov_b32 v59, 0\n\t" \
    "v_mov_b32 v60, 0\n\t" \
    "v_mov_b32 v61, 0\n\t" \
    "v_mov_b32 v62, 0\n\t" \
    "v_mov_b32 v63, 0\n\t" \
    "v_mov_b32 v64, 0\n\t" \
    "v_mov_b32 v65, 0\n\t" \
    "v_mov_b32 v66, 0\n\t" \
    "v_mov_b32 v67, 0\n\t" \
    "v_mov_b32 v68, 0\n\t" \
    "v_mov_b32 v69, 0\n\t" \
    "v_mov_b32 v70, 0\n\t" \
    "v_mov_b32 v71, 0\n\t" \
    "v_mov_b32 v72, 0\n\t" \
    "v_mov_b32 v73, 0\n\t" \
    "v_mov_b32 v74, 0\n\t" \
    "v_mov_b32 v75, 0\n\t" \
    "v_mov_b32 v76, 0\n\t" \
    "v_mov_b32 v77, 0\n\t" \
    "v_mov_b32 v78, 0\n\t" \
    "v_mov_b32 v79, 0\n\t" \
    "v_mov_b32 v80, 0\n\t" \
    "v_mov_b32 v81, 0\n\t" \
    "v_mov_b32 v82, 0\n\t" \
    "v_mov_b32 v83, 0\n\t" \
    "s_waitcnt vmcnt(8)\n\t" \
    "s_barrier\n\t" \
    "ds_read_b64_tr_b16 v[116:117], %[b0] offset:0\n\t" \
    "ds_read_b64_tr_b16 v[118:119], %[b0] offset:1024\n\t" \
    "ds_read_b64_tr_b16 v[124:125], %[b1] offset:0\n\t" \
    "ds_read_b64_tr_b16 v[126:127], %[b1] offset:1024\n\t" \
    "ds_read_b64_tr_b16 v[120:121], %[b0] offset:8192\n\t" \
    "ds_read_b64_tr_b16 v[122:123], %[b0] offset:9216\n\t" \
    "ds_read_b64_tr_b16 v[128:129], %[b1] offset:8192\n\t" \
    "ds_read_b64_tr_b16 v[130:131], %[b1] offset:9216\n\t" \
    "ds_read_b128 v[84:87], %[a] offset:0\n\t" \
    "ds_read_b128 v[88:91], %[a] offset:1024\n\t" \
    "ds_read_b128 v[92:95], %[a] offset:2048\n\t" \
    "ds_read_b128 v[96:99], %[a] offset:3072\n\t" \
    "ds_read_b128 v[100:103], %[a] offset:4096\n\t" \
    "ds_read_b128 v[104:107], %[a] offset:5120\n\t" \
    "ds_read_b128 v[108:111], %[a] offset:6144\n\t" \
    "ds_read_b128 v[112:115], %[a] offset:7168\n\t" \
    "L_%=:\n\t" \
    "s_add_u32 %[d], %[lw], %[c]\n\t" \
    "s_add_u32 %[y], %[c], 49152\n\t" \
    "s_cmp_eq_u32 %[y], 147456\n\t" \
    "s_cselect_b32 %[y], 0, %[y]\n\t" \
    "s_sub_u32 %[x], %[y], %[c]\n\t" \
    "s_mov_b32 %[c], %[y]\n\t" \
    "v_add_u32 %[a], %[x], %[a]\n\t" \
    "v_add_u32 %[b0], %[x], %[b0]\n\t" \
    "v_add_u32 %[b1], %[x], %[b1]\n\t" \
    "s_add_u32 %[x], %[t], 3\n\t" \
    "s_cmp_lt_u32 %[x], %[nt]\n\t" \
    "s_cselect_b32 %[x], %[x], 0\n\t" \
    "s_mul_i32 %[y], %[x], %[kb]\n\t" \
    "s_add_u32 %[y], %[y], %[cb]\n\t" \
    "s_lshl_b32 %[x], %[x], 11\n\t" \
    "s_add_u32 %[x], %[x], %[ca]\n\t" \
    "s_waitcnt vmcnt(4) lgkmcnt(0)\n\t" \
    "s_barrier\n\t" \
    "v_mfma_f32_16x16x32_bf16 v[20:23], v[116:119], v[84:87], v[20:23]\n\t" \
    "ds_read_b64_tr_b16 v[180:181], %[b0] offset:0\n\t" \
    "ds_read_b64_tr_b16 v[182:183], %[b0] offset:1024\n\t" \
    "s_add_u32 m0, %[d], 0\n\t" \
    "v_mfma_f32_16x16x32_bf16 v[24:27], v[124:127], v[84:87], v[24:27]\n\t" \
    "buffer_load_dwordx4 %[vb0], %[rb], %[y] offen lds\n\t" \
    "v_mfma_f32_16x16x32_bf16 v[20:23], v[120:123], v[88:91], v[20:23]\n\t" \
    "ds_read_b64_tr_b16 v[188:189], %[b1] offset:0\n\t" \
    "ds_read_b64_tr_b16 v[190:191], %[b1] offset:1024\n\t" \
    "v_mfma_f32_16x16x32_bf16 v[24:27], v[128:131], v[88:91], v[24:27]\n\t" \
    "ds_read_b64_tr_b16 v[184:185], %[b0] offset:8192\n\t" \
    "ds_read_b64_tr_b16 v[186:187], %[b0] offset:9216\n\t" \
    "v_mfma_f32_16x16x32_bf16 v[28:31], v[116:119], v[92:95], v[28:31]\n\t" \
    "ds_read_b64_tr_b16 v[192:193], %[b1] offset:8192\n\t" \
    "ds_read_b64_tr_b16 v[194:195], %[b1] offset:9216\n\t" \
    "s_add_u32 m0, %[d], 8192\n\t" \
    "v_mfma_f32_16x16x32_bf16 v[32:35], v[124:127], v[92:95], v[32:35]\n\t" \
    "buffer_load_dwordx4 %[vb1], %[rb], %[y] offen lds\n\t" \
    "v_mfma_f32_16x16x32_bf16 v[28:31], v[120:123], v[96:99], v[28:31]\n\t" \
    "ds_read_b128 v[148:151], %[a] offset:0\n\t" \
    "v_mfma_f32_16x16x32_bf16 v[32:35], v[128:131], v[96:99], v[32:35]\n\t" \
    "ds_read_b128 v[152:155], %[a] offset:1024\n\t" \
    "v_mfma_f32_16x16x32_bf16 v[36:39], v[116:119], v[100:103], v[36:39]\n\t" \
    "ds_read_b128 v[156:159], %[a] offset:2048\n\t" \
    "s_add_u32 m0, %[d], 32768\n\t" \
    "v_mfma_f32_16x16x32_bf16 v[40:43], v[124:127], v[100:103], v[40:43]\n\t" \
    "buffer_load_dwordx4 %[va0], %[ra], %[x] offen sc1 lds\n\t" \
    "v_mfma_f32_16x16x32_bf16 v[36:39], v[120:123], v[104:107], v[36:39]\n\t" \
    "ds_read_b128 v[160:163], %[a] offset:3072\n\t" \
    "v_mfma_f32_16x16x32_bf16 v[40:43], v[128:131], v[104:107], v[40:43]\n\t" \
    "ds_read_b128 v[164:167], %[a] offset:4096\n\t" \
    "v_mfma_f32_16x16x32_bf16 v[44:47], v[116:119], v[108:111], v[44:47]\n\t" \
    "ds_read_b128 v[168:171], %[a] offset:5120\n\t" \
    "s_add_u32 m0, %[d], 40960\n\t" \
    "v_mfma_f32_16x16x32_bf16 v[48:51], v[124:127], v[108:111], v[48:51]\n\t" \
    "buffer_load_dwordx4 %[va1], %[ra], %[x] offen sc1 lds\n\t" \
    "v_mfma_f32_16x16x32_bf16 v[44:47], v[120:123], v[112:115], v[44:47]\n\t" \
    "ds_read_b128 v[172:175], %[a] offset:6144\n\t" \
    "v_mfma_f32_16x16x32_bf16 v[48:51], v[128:131], v[112:115], v[48:51]\n\t" \
    "ds_read_b128 v[176:179], %[a] offset:7168\n\t" \
    "s_mov_b32 m0, %[pd]\n\t" \
    "s_cmp_lt_u32 %[ps], %[pe]\n\t" \
    "s_cselect_b64 exec, -1, 0\n\t" \
    "buffer_load_dword %[pv], %[rp], %[ps] offen sc1 lds\n\t" \
    "s_mov_b64 exec, -1\n\t" \
    "s_add_u32 %[ps], %[ps], %[pk]\n\t" \
    "s_add_u32 %[t], %[t], 1\n\t" \
    "s_add_u32 %[d], %[lw], %[c]\n\t" \
    "s_add_u32 %[y], %[c], 49152\n\t" \
    "s_cmp_eq_u32 %[y], 147456\n\t" \
    "s_cselect_b32 %[y], 0, %[y]\n\t" \
    "s_sub_u32 %[x], %[y], %[c]\n\t" \
    "s_mov_b32 %[c], %[y]\n\t" \
    "v_add_u32 %[a], %[x], %[a]\n\t" \
    "v_add_u32 %[b0], %[x], %[b0]\n\t" \
    "v_add_u32 %[b1], %[x], %[b1]\n\t" \
    "s_add_u32 %[x], %[t], 3\n\t" \
    "s_cmp_lt_u32 %[x], %[nt]\n\t" \
    "s_cselect_b32 %[x], %[x], 0\n\t" \
    "s_mul_i32 %[y], %[x], %[kb]\n\t" \
    "s_add_u32 %[y], %[y], %[cb]\n\t" \
    "s_lshl_b32 %[x], %[x], 11\n\t" \
    "s_add_u32 %[x], %[x], %[ca]\n\t" \
    "s_waitcnt vmcnt(4) lgkmcnt(0)\n\t" \
    "s_barrier\n\t" \
    "v_mfma_f32_16x16x32_bf16 v[20:23], v[180:183], v[148:151], v[20:23]\n\t" \
    "ds_read_b64_tr_b16 v[116:117], %[b0] offset:0\n\t" \
    "ds_read_b64_tr_b16 v[118:119], %[b0] offset:1024\n\t" \
    "s_add_u32 m0, %[d], 0\n\t" \
    "v_mfma_f32_16x16x32_bf16 v[24:27], v[188:191], v[148:151], v[24:27]\n\t" \
    "buffer_load_dwordx4 %[vb0], %[rb], %[y] offen lds\n\t" \
    "v_mfma_f32_16x16x32_bf16 v[20:23], v[184:187], v[152:155], v[20:23]\n\t" \
    "ds_read_b64_tr_b16 v[124:125], %[b1] offset:0\n\t" \
    "ds_read_b64_tr_b16 v[126:127], %[b1] offset:1024\n\t" \
    "v_mfma_f32_16x16x32_bf16 v[24:27], v[192:195], v[152:155], v[24:27]\n\t" \
    "ds_read_b64_tr_b16 v[120:121], %[b0] offset:8192\n\t" \
    "ds_read_b64_tr_b16 v[122:123], %[b0] offset:9216\n\t" \
    "v_mfma_f32_16x16x32_bf16 v[28:31], v[180:183], v[156:159], v[28:31]\n\t" \
    "ds_read_b64_tr_b16 v[128:129], %[b1] offset:8192\n\t" \
    "ds_read_b64_tr_b16 v[130:131], %[b1] offset:9216\n\t" \
    "s_add_u32 m0, %[d], 8192\n\t" \
    "v_mfma_f32_16x16x32_bf16 v[32:35], v[188:191], v[156:159], v[32:35]\n\t" \
    "buffer_load_dwordx4 %[vb1], %[rb], %[y] offen lds\n\t" \
    "v_mfma_f32_16x16x32_bf16 v[28:31], v[184:187], v[160:163], v[28:31]\n\t" \
    "ds_read_b128 v[84:87], %[a] offset:0\n\t" \
    "v_mfma_f32_16x16x32_bf16 v[32:35], v[192:195], v[160:163], v[32:35]\n\t" \
    "ds_read_b128 v[88:91], %[a] offset:1024\n\t" \
    "v_mfma_f32_16x16x32_bf16 v[36:39], v[180:183], v[164:167], v[36:39]\n\t" \
    "ds_read_b128 v[92:95], %[a] offset:2048\n\t" \
    "s_add_u32 m0, %[d], 32768\n\t" \
    "v_mfma_f32_16x16x32_bf16 v[40:43], v[188:191], v[164:167], v[40:43]\n\t" \
    "buffer_load_dwordx4 %[va0], %[ra], %[x] offen sc1 lds\n\t" \
    "v_mfma_f32_16x16x32_bf16 v[36:39], v[184:187], v[168:171], v[36:39]\n\t" \
    "ds_read_b128 v[96:99], %[a] offset:3072\n\t" \
    "v_mfma_f32_16x16x32_bf16 v[40:43], v[192:195], v[168:171], v[40:43]\n\t" \
    "ds_read_b128 v[100:103], %[a] offset:4096\n\t" \
    "v_mfma_f32_16x16x32_bf16 v[44:47], v[180:183], v[172:175], v[44:47]\n\t" \
    "ds_read_b128 v[104:107], %[a] offset:5120\n\t" \
    "s_add_u32 m0, %[d], 40960\n\t" \
    "v_mfma_f32_16x16x32_bf16 v[48:51], v[188:191], v[172:175], v[48:51]\n\t" \
    "buffer_load_dwordx4 %[va1], %[ra], %[x] offen sc1 lds\n\t" \
    "v_mfma_f32_16x16x32_bf16 v[44:47], v[184:187], v[176:179], v[44:47]\n\t" \
    "ds_read_b128 v[108:111], %[a] offset:6144\n\t" \
    "v_mfma_f32_16x16x32_bf16 v[48:51], v[192:195], v[176:179], v[48:51]\n\t" \
    "ds_read_b128 v[112:115], %[a] offset:7168\n\t" \
    "s_mov_b32 m0, %[pd]\n\t" \
    "s_cmp_lt_u32 %[ps], %[pe]\n\t" \
    "s_cselect_b64 exec, -1, 0\n\t" \
    "buffer_load_dword %[pv], %[rp], %[ps] offen sc1 lds\n\t" \
    "s_mov_b64 exec, -1\n\t" \
    "s_add_u32 %[ps], %[ps], %[pk]\n\t" \
    "s_add_u32 %[t], %[t], 1\n\t" \
    "s_cmp_lt_u32 %[t], %[nt]\n\t" \
    "s_cbranch_scc1 L_%=\n\t" \
    "s_waitcnt lgkmcnt(0)\n\t" \
    "s_nop 7\n\t" \
    : "=&{v[20:23]}"(acc[0][0][0][0]), "=&{v[24:27]}"(acc[0][0][0][1]), "=&{v[28:31]}"(acc[0][0][1][0]), "=&{v[32:35]}"(acc[0][0][1][1]), "=&{v[36:39]}"(acc[0][0][2][0]), "=&{v[40:43]}"(acc[0][0][2][1]), "=&{v[44:47]}"(acc[0][0][3][0]), "=&{v[48:51]}"(acc[0][0][3][1]), "=&{v[52:55]}"(acc[0][1][0][0]), "=&{v[56:59]}"(acc[0][1][0][1]), "=&{v[60:63]}"(acc[0][1][1][0]), "=&{v[64:67]}"(acc[0][1][1][1]), "=&{v[68:71]}"(acc[0][1][2][0]), "=&{v[72:75]}"(acc[0][1][2][1]), "=&{v[76:79]}"(acc[0][1][3][0]), "=&{v[80:83]}"(acc[0][1][3][1]), [a] "+v"(aoff), [b0] "+v"(boff0), [b1] "+v"(boff1), [c] "+s"(cur), [t] "+s"(t), [ps] "+s"(ps), [d] "=&s"(sd), [x] "=&s"(sx), [y] "=&s"(sy) \
    : [va0] "v"(voffA[0]), [va1] "v"(voffA[1]), [vb0] "v"(voffB[0]), [vb1] "v"(voffB[1]), [pv] "v"(pf.v), [ra] "s"(ra), [rb] "s"(rb), [rp] "s"(rp), [lw] "s"(ldsw), [nt] "s"(nt), [kb] "s"(kB), [ca] "s"(cA), [cb] "s"(cB), [pd] "s"(pd), [pe] "s"(pe), [pk] "s"(pf.step) \
    : "memory", "scc", "v84", "v85", "v86", "v87", "v88", "v89", "v90", "v91", "v92", "v93", "v94", "v95", "v96", "v97", "v98", "v99", "v100", "v101", "v102", "v103", "v104", "v105", "v106", "v107", "v108", "v109", "v110", "v111", "v112", "v113", "v114", "v115", "v116", "v117", "v118", "v119", "v120", "v121", "v122", "v123", "v124", "v125", "v126", "v127", "v128", "v129", "v130", "v131", "v132", "v133", "v134", "v135", "v136", "v137", "v138", "v139", "v140", "v141", "v142", "v143", "v144", "v145", "v146", "v147", "v148", "v149", "v150", "v151", "v152", "v153", "v154", "v155", "v156", "v157", "v158", "v159", "v160", "v161", "v162", "v163", "v164", "v165", "v166", "v167", "v168", "v169", "v170", "v171", "v172", "v173", "v174", "v175", "v176", "v177", "v178", "v179", "v180", "v181", "v182", "v183", "v184", "v185", "v186", "v187", "v188", "v189", "v190", "v191", "v192", "v193", "v194", "v195", "v196", "v197", "v198", "v199", "v200", "v201", "v202", "v203", "v204", "v205", "v206", "v207", "v208", "v209", "v210", "v211")

#define H_LOOP2() \
  asm volatile( \
    "v_mov_b32 v20, 0\n\t" \
    "v_mov_b32 v21, 0\n\t" \
    "v_mov_b32 v22, 0\n\t" \
    "v_mov_b32 v23, 0\n\t" \
    "v_mov_b32 v24, 0\n\t" \
    "v_mov_b32 v25, 0\n\t" \
    "v_mov_b32 v26, 0\n\t" \
    "v_mov_b32 v27, 0\n\t" \
    "v_mov_b32 v28, 0\n\t" \
    "v_mov_b32 v29, 0\n\t" \
    "v_mov_b32 v30, 0\n\t" \
    "v_mov_b32 v31, 0\n\t" \
    "v_mov_b32 v32, 0\n\t" \
    "v_mov_b32 v33, 0\n\t" \
    "v_mov_b32 v34, 0\n\t" \
    "v_mov_b32 v35, 0\n\t" \
    "v_mov_b32 v36, 0\n\t" \
    "v_mov_b32 v37, 0\n\t" \
    "v_mov_b32 v38, 0\n\t" \
    "v_mov_b32 v39, 0\n\t" \
    "v_mov_b32 v40, 0\n\t" \
    "v_mov_b32 v41, 0\n\t" \
    "v_mov_b32 v42, 0\n\t" \
    "v_mov_b32 v43, 0\n\t" \
    "v_mov_b32 v44, 0\n\t" \
    "v_mov_b32 v45, 0\n\t" \
    "v_mov_b32 v46, 0\n\t" \
    "v_mov_b32 v47, 0\n\t" \
    "v_mov_b32 v48, 0\n\t" \
    "v_mov_b32 v49, 0\n\t" \
    "v_mov_b32 v50, 0\n\t" \
    "v_mov_b32 v51, 0\n\t" \
    "v_mov_b32 v52, 0\n\t" \
    "v_mov_b32 v53, 0\n\t" \
    "v_mov_b32 v54, 0\n\t" \
    "v_mov_b32 v55, 0\n\t" \
    "v_mov_b32 v56, 0\n\t" \
    "v_mov_b32 v57, 0\n\t" \
    "v_mov_b32 v58, 0\n\t" \
    "v_mov_b32 v59, 0\n\t" \
    "v_mov_b32 v60, 0\n\t" \
    "v_mov_b32 v61, 0\n\t" \
    "v_mov_b32 v62, 0\n\t" \
    "v_mov_b32 v63, 0\n\t" \
    "v_mov_b32 v64, 0\n\t" \
    "v_mov_b32 v65, 0\n\t" \
    "v_mov_b32 v66, 0\n\t" \
    "v_mov_b32 v67, 0\n\t" \
    "v_mov_b32 v68, 0\n\t" \
    "v_mov_b32 v69, 0\n\t" \
    "v_mov_b32 v70, 0\n\t" \
    "v_mov_b32 v71, 0\n\t" \
    "v_mov_b32 v72, 0\n\t" \
    "v_mov_b32 v73, 0\n\t" \
    "v_mov_b32 v74, 0\n\t" \
    "v_mov_b32 v75, 0\n\t" \
    "v_mov_b32 v76, 0\n\t" \
    "v_mov_b32 v77, 0\n\t" \
    "v_mov_b32 v78, 0\n\t" \
    "v_mov_b32 v79, 0\n\t" \
    "v_mov_b32 v80, 0\n\t" \
    "v_mov_b32 v81, 0\n\t" \
    "v_mov_b32 v82, 0\n\t" \
    "v_mov_b32 v83, 0\n\t" \
    "s_waitcnt vmcnt(12)\n\t" \
    "s_barrier\n\t" \
    "ds_read_b64_tr_b16 v[116:117], %[b0] offset:0\n\t" \
    "ds_read_b64_tr_b16 v[118:119], %[b0] offset:1024\n\t" \
    "ds_read_b64_tr_b16 v[124:125], %[b1] offset:0\n\t" \
    "ds_read_b64_tr_b16 v[126:127], %[b1] offset:1024\n\t" \
    "ds_read_b64_tr_b16 v[120:121], %[b0] offset:8192\n\t" \
    "ds_read_b64_tr_b16 v[122:123], %[b0] offset:9216\n\t" \
    "ds_read_b64_tr_b16 v[128:129], %[b1] offset:8192\n\t" \
    "ds_read_b64_tr_b16 v[130:131], %[b1] offset:9216\n\t" \
    "ds_read_b128 v[84:87], %[a] offset:0\n\t" \
    "ds_read_b128 v[88:91], %[a] offset:1024\n\t" \
    "ds_read_b128 v[92:95], %[a] offset:2048\n\t" \
    "ds_read_b128 v[96:99], %[a] offset:3072\n\t" \
    "ds_read_b128 v[100:103], %[a] offset:4096\n\t" \
    "ds_read_b128 v[104:107], %[a] offset:5120\n\t" \
    "ds_read_b128 v[108:111], %[a] offset:6144\n\t" \
    "ds_read_b128 v[112:115], %[a] offset:7168\n\t" \
    "ds_read_b64_tr_b16 v[132:133], %[b0] offset:16384\n\t" \
    "ds_read_b64_tr_b16 v[134:135], %[b0] offset:17408\n\t" \
    "ds_read_b64_tr_b16 v[140:141], %[b1] offset:16384\n\t" \
    "ds_read_b64_tr_b16 v[142:143], %[b1] offset:17408\n\t" \
    "ds_read_b64_tr_b16 v[136:137], %[b0] offset:24576\n\t" \
    "ds_read_b64_tr_b16 v[138:139], %[b0] offset:25600\n\t" \
    "ds_read_b64_tr_b16 v[144:145], %[b1] offset:24576\n\t" \
    "ds_read_b64_tr_b16 v[146:147], %[b1] offset:25600\n\t" \
    "L_%=:\n\t" \
    "s_add_u32 %[d], %[lw], %[c]\n\t" \
    "s_add_u32 %[y], %[c], 49152\n\t" \
    "s_cmp_eq_u32 %[y], 147456\n\t" \
    "s_cselect_b32 %[y], 0, %[y]\n\t" \
    "s_sub_u32 %[x], %[y], %[c]\n\t" \
    "s_mov_b32 %[c], %[y]\n\t" \
    "v_add_u32 %[a], %[x], %[a]\n\t" \
    "v_add_u32 %[b0], %[x], %[b0]\n\t" \
    "v_add_u32 %[b1], %[x], %[b1]\n\t" \
    "s_add_u32 %[x], %[t], 3\n\t" \
    "s_cmp_lt_u32 %[x], %[nt]\n\t" \
    "s_cselect_b32 %[x], %[x], 0\n\t" \
    "s_mul_i32 %[y], %[x], %[kb]\n\t" \
    "s_add_u32 %[y], %[y], %[cb]\n\t" \
    "s_lshl_b32 %[x], %[x], 11\n\t" \
    "s_add_u32 %[x], %[x], %[ca]\n\t" \
    "s_waitcnt vmcnt(6) lgkmcnt(0)\n\t" \
    "s_barrier\n\t" \
    "v_mfma_f32_16x16x32_bf16 v[20:23], v[116:119], v[84:87], v[20:23]\n\t" \
    "ds_read_b64_tr_b16 v[180:181], %[b0] offset:0\n\t" \
    "s_add_u32 m0, %[d], 0\n\t" \
    "v_mfma_f32_16x16x32_bf16 v[24:27], v[124:127], v[84:87], v[24:27]\n\t" \
    "buffer_load_dwordx4 %[vb0], %[rb], %[y] offen lds\n\t" \
    "v_mfma_f32_16x16x32_bf16 v[20:23], v[120:123], v[88:91], v[20:23]\n\t" \
    "ds_read_b64_tr_b16 v[182:183], %[b0] offset:1024\n\t" \
    "v_mfma_f32_16x16x32_bf16 v[24:27], v[128:131], v[88:91], v[24:27]\n\t" \
    "ds_read_b64_tr_b16 v[188:189], %[b1] offset:0\n\t" \
    "v_mfma_f32_16x16x32_bf16 v[28:31], v[116:119], v[92:95], v[28:31]\n\t" \
    "ds_read_b64_tr_b16 v[190:191], %[b1] offset:1024\n\t" \
    "v_mfma_f32_16x16x32_bf16 v[32:35], v[124:127], v[92:95], v[32:35]\n\t" \
    "ds_read_b64_tr_b16 v[184:185], %[b0] offset:8192\n\t" \
    "s_add_u32 m0, %[d], 8192\n\t" \
    "v_mfma_f32_16x16x32_bf16 v[28:31], v[120:123], v[96:99], v[28:31]\n\t" \
    "buffer_load_dwordx4 %[vb1], %[rb], %[y] offen lds\n\t" \
    "v_mfma_f32_16x16x32_bf16 v[32:35], v[128:131], v[96:99], v[32:35]\n\t" \
    "ds_read_b64_tr_b16 v[186:187], %[b0] offset:9216\n\t" \
    "v_mfma_f32_16x16x32_bf16 v[36:39], v[116:119], v[100:103], v[36:39]\n\t" \
    "ds_read_b64_tr_b16 v[192:193], %[b1] offset:8192\n\t" \
    "v_mfma_f32_16x16x32_bf16 v[40:43], v[124:127], v[100:103], v[40:43]\n\t" \
    "ds_read_b64_tr_b16 v[194:195], %[b1] offset:9216\n\t" \
    "v_mfma_f32_16x16x32_bf16 v[36:39], v[120:123], v[104:107], v[36:39]\n\t" \
    "ds_read_b128 v[148:151], %[a] offset:0\n\t" \
    "s_add_u32 m0, %[d], 32768\n\t" \
    "v_mfma_f32_16x16x32_bf16 v[40:43], v[128:131], v[104:107], v[40:43]\n\t" \
    "buffer_load_dwordx4 %[va0], %[ra], %[x] offen sc1 lds\n\t" \
    "v_mfma_f32_16x16x32_bf16 v[44:47], v[116:119], v[108:111], v[44:47]\n\t" \
    "ds_read_b128 v[152:155], %[a] offset:1024\n\t" \
    "v_mfma_f32_16x16x32_bf16 v[48:51], v[124:127], v[108:111], v[48:51]\n\t" \
    "ds_read_b128 v[156:159], %[a] offset:2048\n\t" \
    "v_mfma_f32_16x16x32_bf16 v[44:47], v[120:123], v[112:115], v[44:47]\n\t" \
    "ds_read_b128 v[160:163], %[a] offset:3072\n\t" \
    "v_mfma_f32_16x16x32_bf16 v[48:51], v[128:131], v[112:115], v[48:51]\n\t" \
    "ds_read_b128 v[164:167], %[a] offset:4096\n\t" \
    "s_add_u32 m0, %[d], 40960\n\t" \
    "v_mfma_f32_16x16x32_bf16 v[52:55], v[132:135], v[84:87], v[52:55]\n\t" \
    "buffer_load_dwordx4 %[va1], %[ra], %[x] offen sc1 lds\n\t" \
    "v_mfma_f32_16x16x32_bf16 v[56:59], v[140:143], v[84:87], v[56:59]\n\t" \
    "ds_read_b128 v[168:171], %[a] offset:5120\n\t" \
    "v_mfma_f32_16x16x32_bf16 v[52:55], v[136:139], v[88:91], v[52:55]\n\t" \
    "ds_read_b128 v[172:175], %[a] offset:6144\n\t" \
    "v_mfma_f32_16x16x32_bf16 v[56:59], v[144:147], v[88:91], v[56:59]\n\t" \
    "ds_read_b128 v[176:179], %[a] offset:7168\n\t" \
    "v_mfma_f32_16x16x32_bf16 v[60:63], v[132:135], v[92:95], v[60:63]\n\t" \
    "ds_read_b64_tr_b16 v[196:197], %[b0] offset:16384\n\t" \
    "s_add_u32 m0, %[d], 16128\n\t" \
    "v_mfma_f32_16x16x32_bf16 v[64:67], v[140:143], v[92:95], v[64:67]\n\t" \
    "buffer_load_dwordx4 %[vb0], %[rb], %[y] offen offset:256 lds\n\t" \
    "v_mfma_f32_16x16x32_bf16 v[60:63], v[136:139], v[96:99], v[60:63]\n\t" \
    "ds_read_b64_tr_b16 v[198:199], %[b0] offset:17408\n\t" \
    "v_mfma_f32_16x16x32_bf16 v[64:67], v[144:147], v[96:99], v[64:67]\n\t" \
    "ds_read_b64_tr_b16 v[204:205], %[b1] offset:16384\n\t" \
    "v_mfma_f32_16x16x32_bf16 v[68:71], v[132:135], v[100:103], v[68:71]\n\t" \
    "ds_read_b64_tr_b16 v[206:207], %[b1] offset:17408\n\t" \
    "v_mfma_f32_16x16x32_bf16 v[72:75], v[140:143], v[100:103], v[72:75]\n\t" \
    "ds_read_b64_tr_b16 v[200:201], %[b0] offset:24576\n\t" \
    "s_add_u32 m0, %[d], 24320\n\t" \
    "v_mfma_f32_16x16x32_bf16 v[68:71], v[136:139], v[104:107], v[68:71]\n\t" \
    "buffer_load_dwordx4 %[vb1], %[rb], %[y] offen offset:256 lds\n\t" \
    "v_mfma_f32_16x16x32_bf16 v[72:75], v[144:147], v[104:107], v[72:75]\n\t" \
    "ds_read_b64_tr_b16 v[202:203], %[b0] offset:25600\n\t" \
    "v_mfma_f32_16x16x32_bf16 v[76:79], v[132:135], v[108:111], v[76:79]\n\t" \
    "ds_read_b64_tr_b16 v[208:209], %[b1] offset:24576\n\t" \
    "v_mfma_f32_16x16x32_bf16 v[80:83], v[140:143], v[108:111], v[80:83]\n\t" \
    "ds_read_b64_tr_b16 v[210:211], %[b1] offset:25600\n\t" \
    "v_mfma_f32_16x16x32_bf16 v[76:79], v[136:139], v[112:115], v[76:79]\n\t" \
    "v_mfma_f32_16x16x32_bf16 v[80:83], v[144:147], v[112:115], v[80:83]\n\t" \
    "s_mov_b32 m0, %[pd]\n\t" \
    "s_cmp_lt_u32 %[ps], %[pe]\n\t" \
    "s_cselect_b64 exec, -1, 0\n\t" \
    "buffer_load_dword %[pv], %[rp], %[ps] offen sc1 lds\n\t" \
    "s_mov_b64 exec, -1\n\t" \
    "s_add_u32 %[ps], %[ps], %[pk]\n\t" \
    "s_add_u32 %[t], %[t], 1\n\t" \
    "s_add_u32 %[d], %[lw], %[c]\n\t" \
    "s_add_u32 %[y], %[c], 49152\n\t" \
    "s_cmp_eq_u32 %[y], 147456\n\t" \
    "s_cselect_b32 %[y], 0, %[y]\n\t" \
    "s_sub_u32 %[x], %[y], %[c]\n\t" \
    "s_mov_b32 %[c], %[y]\n\t" \
    "v_add_u32 %[a], %[x], %[a]\n\t" \
    "v_add_u32 %[b0], %[x], %[b0]\n\t" \
    "v_add_u32 %[b1], %[x], %[b1]\n\t" \
    "s_add_u32 %[x], %[t], 3\n\t" \
    "s_cmp_lt_u32 %[x], %[nt]\n\t" \
    "s_cselect_b32 %[x], %[x], 0\n\t" \
    "s_mul_i32 %[y], %[x], %[kb]\n\t" \
    "s_add_u32 %[y], %[y], %[cb]\n\t" \
    "s_lshl_b32 %[x], %[x], 11\n\t" \
    "s_add_u32 %[x], %[x], %[ca]\n\t" \
    "s_waitcnt vmcnt(6) lgkmcnt(0)\n\t" \
    "s_barrier\n\t" \
    "v_mfma_f32_16x16x32_bf16 v[20:23], v[180:183], v[148:151], v[20:23]\n\t" \
    "ds_read_b64_tr_b16 v[116:117], %[b0] offset:0\n\t" \
    "s_add_u32 m0, %[d], 0\n\t" \
    "v_mfma_f32_16x16x32_bf16 v[24:27], v[188:191], v[148:151], v[24:27]\n\t" \
    "buffer_load_dwordx4 %[vb0], %[rb], %[y] offen lds\n\t" \
    "v_mfma_f32_16x16x32_bf16 v[20:23], v[184:187], v[152:155], v[20:23]\n\t" \
    "ds_read_b64_tr_b16 v[118:119], %[b0] offset:1024\n\t" \
    "v_mfma_f32_16x16x32_bf16 v[24:27], v[192:195], v[152:155], v[24:27]\n\t" \
    "ds_read_b64_tr_b16 v[124:125], %[b1] offset:0\n\t" \
    "v_mfma_f32_16x16x32_bf16 v[28:31], v[180:183], v[156:159], v[28:31]\n\t" \
    "ds_read_b64_tr_b16 v[126:127], %[b1] offset:1024\n\t" \
    "v_mfma_f32_16x16x32_bf16 v[32:35], v[188:191], v[156:159], v[32:35]\n\t" \
    "ds_read_b64_tr_b16 v[120:121], %[b0] offset:8192\n\t" \
    "s_add_u32 m0, %[d], 8192\n\t" \
    "v_mfma_f32_16x16x32_bf16 v[28:31], v[184:187], v[160:163], v[28:31]\n\t" \
    "buffer_load_dwordx4 %[vb1], %[rb], %[y] offen lds\n\t" \
    "v_mfma_f32_16x16x32_bf16 v[32:35], v[192:195], v[160:163], v[32:35]\n\t" \
    "ds_read_b64_tr_b16 v[122:123], %[b0] offset:9216\n\t" \
    "v_mfma_f32_16x16x32_bf16 v[36:39], v[180:183], v[164:167], v[36:39]\n\t" \
    "ds_read_b64_tr_b16 v[128:129], %[b1] offset:8192\n\t" \
    "v_mfma_f32_16x16x32_bf16 v[40:43], v[188:191], v[164:167], v[40:43]\n\t" \
    "ds_read_b64_tr_b16 v[130:131], %[b1] offset:9216\n\t" \
    "v_mfma_f32_16x16x32_bf16 v[36:39], v[184:187], v[168:171], v[36:39]\n\t" \
    "ds_read_b128 v[84:87], %[a] offset:0\n\t" \
    "s_add_u32 m0, %[d], 32768\n\t" \
    "v_mfma_f32_16x16x32_bf16 v[40:43], v[192:195], v[168:171], v[40:43]\n\t" \
    "buffer_load_dwordx4 %[va0], %[ra], %[x] offen sc1 lds\n\t" \
    "v_mfma_f32_16x16x32_bf16 v[44:47], v[180:183], v[172:175], v[44:47]\n\t" \
    "ds_read_b128 v[88:91], %[a] offset:1024\n\t" \
    "v_mfma_f32_16x16x32_bf16 v[48:51], v[188:191], v[172:175], v[48:51]\n\t" \
    "ds_read_b128 v[92:95], %[a] offset:2048\n\t" \
    "v_mfma_f32_16x16x32_bf16 v[44:47], v[184:187], v[176:179], v[44:47]\n\t" \
    "ds_read_b128 v[96:99], %[a] offset:3072\n\t" \
    "v_mfma_f32_16x16x32_bf16 v[48:51], v[192:195], v[176:179], v[48:51]\n\t" \
    "ds_read_b128 v[100:103], %[a] offset:4096\n\t" \
    "s_add_u32 m0, %[d], 40960\n\t" \
    "v_mfma_f32_16x16x32_bf16 v[52:55], v[196:199], v[148:151], v[52:55]\n\t" \
    "buffer_load_dwordx4 %[va1], %[ra], %[x] offen sc1 lds\n\t" \
    "v_mfma_f32_16x16x32_bf16 v[56:59], v[204:207], v[148:151], v[56:59]\n\t" \
    "ds_read_b128 v[104:107], %[a] offset:5120\n\t" \
    "v_mfma_f32_16x16x32_bf16 v[52:55], v[200:203], v[152:155], v[52:55]\n\t" \
    "ds_read_b128 v[108:111], %[a] offset:6144\n\t" \
    "v_mfma_f32_16x16x32_bf16 v[56:59], v[208:211], v[152:155], v[56:59]\n\t" \
    "ds_read_b128 v[112:115], %[a] offset:7168\n\t" \
    "v_mfma_f32_16x16x32_bf16 v[60:63], v[196:199], v[156:159], v[60:63]\n\t" \
    "ds_read_b64_tr_b16 v[132:133], %[b0] offset:16384\n\t" \
    "s_add_u32 m0, %[d], 16128\n\t" \
    "v_mfma_f32_16x16x32_bf16 v[64:67], v[204:207], v[156:159], v[64:67]\n\t" \
    "buffer_load_dwordx4 %[vb0], %[rb], %[y] offen offset:256 lds\n\t" \
    "v_mfma_f32_16x16x32_bf16 v[60:63], v[200:203], v[160:163], v[60:63]\n\t" \
    "ds_read_b64_tr_b16 v[134:135], %[b0] offset:17408\n\t" \
    "v_mfma_f32_16x16x32_bf16 v[64:67], v[208:211], v[160:163], v[64:67]\n\t" \
    "ds_read_b64_tr_b16 v[140:141], %[b1] offset:16384\n\t" \
    "v_mfma_f32_16x16x32_bf16 v[68:71], v[196:199], v[164:167], v[68:71]\n\t" \
    "ds_read_b64_tr_b16 v[142:143], %[b1] offset:17408\n\t" \
    "v_mfma_f32_16x16x32_bf16 v[72:75], v[204:207], v[164:167], v[72:75]\n\t" \
    "ds_read_b64_tr_b16 v[136:137], %[b0] offset:24576\n\t" \
    "s_add_u32 m0, %[d], 24320\n\t" \
    "v_mfma_f32_16x16x32_bf16 v[68:71], v[200:203], v[168:171], v[68:71]\n\t" \
    "buffer_load_dwordx4 %[vb1], %[rb], %[y] offen offset:256 lds\n\t" \
    "v_mfma_f32_16x16x32_bf16 v[72:75], v[208:211], v[168:171], v[72:75]\n\t" \
    "ds_read_b64_tr_b16 v[138:139], %[b0] offset:25600\n\t" \
    "v_mfma_f32_16x16x32_bf16 v[76:79], v[196:199], v[172:175], v[76:79]\n\t" \
    "ds_read_b64_tr_b16 v[144:145], %[b1] offset:24576\n\t" \
    "v_mfma_f32_16x16x32_bf16 v[80:83], v[204:207], v[172:175], v[80:83]\n\t" \
    "ds_read_b64_tr_b16 v[146:147], %[b1] offset:25600\n\t" \
    "v_mfma_f32_16x16x32_bf16 v[76:79], v[200:203], v[176:179], v[76:79]\n\t" \
    "v_mfma_f32_16x16x32_bf16 v[80:83], v[208:211], v[176:179], v[80:83]\n\t" \
    "s_mov_b32 m0, %[pd]\n\t" \
    "s_cmp_lt_u32 %[ps], %[pe]\n\t" \
    "s_cselect_b64 exec, -1, 0\n\t" \
    "buffer_load_dword %[pv], %[rp], %[ps] offen sc1 lds\n\t" \
    "s_mov_b64 exec, -1\n\t" \
    "s_add_u32 %[ps], %[ps], %[pk]\n\t" \
    "s_add_u32 %[t], %[t], 1\n\t" \
    "s_cmp_lt_u32 %[t], %[nt]\n\t" \
    "s_cbranch_scc1 L_%=\n\t" \
    "s_waitcnt lgkmcnt(0)\n\t" \
    "s_nop 7\n\t" \
    : "=&{v[20:23]}"(acc[0][0][0][0]), "=&{v[24:27]}"(acc[0][0][0][1]), "=&{v[28:31]}"(acc[0][0][1][0]), "=&{v[32:35]}"(acc[0][0][1][1]), "=&{v[36:39]}"(acc[0][0][2][0]), "=&{v[40:43]}"(acc[0][0][2][1]), "=&{v[44:47]}"(acc[0][0][3][0]), "=&{v[48:51]}"(acc[0][0][3][1]), "=&{v[52:55]}"(acc[0][1][0][0]), "=&{v[56:59]}"(acc[0][1][0][1]), "=&{v[60:63]}"(acc[0][1][1][0]), "=&{v[64:67]}"(acc[0][1][1][1]), "=&{v[68:71]}"(acc[0][1][2][0]), "=&{v[72:75]}"(acc[0][1][2][1]), "=&{v[76:79]}"(acc[0][1][3][0]), "=&{v[80:83]}"(acc[0][1][3][1]), [a] "+v"(aoff), [b0] "+v"(boff0), [b1] "+v"(boff1), [c] "+s"(cur), [t] "+s"(t), [ps] "+s"(ps), [d] "=&s"(sd), [x] "=&s"(sx), [y] "=&s"(sy) \
    : [va0] "v"(voffA[0]), [va1] "v"(voffA[1]), [vb0] "v"(voffB[0]), [vb1] "v"(voffB[1]), [pv] "v"(pf.v), [ra] "s"(ra), [rb] "s"(rb), [rp] "s"(rp), [lw] "s"(ldsw), [nt] "s"(nt), [kb] "s"(kB), [ca] "s"(cA), [cb] "s"(cB), [pd] "s"(pd), [pe] "s"(pe), [pk] "s"(pf.step) \
    : "memory", "scc", "v84", "v85", "v86", "v87", "v88", "v89", "v90", "v91", "v92", "v93", "v94", "v95", "v96", "v97", "v98", "v99", "v100", "v101", "v102", "v103", "v104", "v105", "v106", "v107", "v108", "v109", "v110", "v111", "v112", "v113", "v114", "v115", "v116", "v117", "v118", "v119", "v120", "v121", "v122", "v123", "v124", "v125", "v126", "v127", "v128", "v129", "v130", "v131", "v132", "v133", "v134", "v135", "v136", "v137", "v138", "v139", "v140", "v141", "v142", "v143", "v144", "v145", "v146", "v147", "v148", "v149", "v150", "v151", "v152", "v153", "v154", "v155", "v156", "v157", "v158", "v159", "v160", "v161", "v162", "v163", "v164", "v165", "v166", "v167", "v168", "v169", "v170", "v171", "v172", "v173", "v174", "v175", "v176", "v177", "v178", "v179", "v180", "v181", "v182", "v183", "v184", "v185", "v186", "v187", "v188", "v189", "v190", "v191", "v192", "v193", "v194", "v195", "v196", "v197", "v198", "v199", "v200", "v201", "v202", "v203", "v204", "v205", "v206", "v207", "v208", "v209", "v210", "v211")
#define AQ_PIN0() asm volatile("" : "+{v[28:35]}"(wr[0][0]), "+{v[36:43]}"(wr[0][1]))
#define AQ_PIN1() asm volatile("" : "+{v[44:51]}"(wr[1][0]), "+{v[52:59]}"(wr[1][1]))
#define AQ_PIN2() asm volatile("" : "+{v[60:67]}"(wr[2][0]), "+{v[68:75]}"(wr[2][1]))
#define AQ_PIN3() asm volatile("" : "+{v[76:83]}"(wr[3][0]), "+{v[84:91]}"(wr[3][1]))
#define AQ_PIN4() asm volatile("" : "+{v[92:99]}"(wr[4][0]), "+{v[100:107]}"(wr[4][1]))
#define AQ_PIN5() asm volatile("" : "+{v[108:115]}"(wr[5][0]), "+{v[116:123]}"(wr[5][1]))
#define AQ_PIN6() asm volatile("" : "+{v[124:131]}"(wr[6][0]), "+{v[132:139]}"(wr[6][1]))
#define AQ_PIN7() asm volatile("" : "+{v[140:147]}"(wr[7][0]), "+{v[148:155]}"(wr[7][1]))
#define AQ_PIN8() asm volatile("" : "+{v[156:163]}"(wr[8][0]), "+{v[164:171]}"(wr[8][1]))
#define AQ_PIN9() asm volatile("" : "+{v[172:179]}"(wr[9][0]), "+{v[180:187]}"(wr[9][1]))
#define AQ_PIN10() asm volatile("" : "+{v[188:195]}"(wr[10][0]), "+{v[196:203]}"(wr[10][1]))
#define AQ_PIN11() asm volatile("" : "+{v[204:211]}"(wr[11][0]), "+{v[212:219]}"(wr[11][1]))
#define AK_PIN0() asm volatile("" : "+{v[20:27]}"(wr[0][0]), "+{v[28:35]}"(wr[0][1]))
#define AK_PIN1() asm volatile("" : "+{v[36:43]}"(wr[1][0]), "+{v[44:51]}"(wr[1][1]))
#define AK_PIN2() asm volatile("" : "+{v[52:59]}"(wr[2][0]), "+{v[60:67]}"(wr[2][1]))
#define AK_PIN3() asm volatile("" : "+{v[68:75]}"(wr[3][0]), "+{v[76:83]}"(wr[3][1]))
#define AK_PIN4() asm volatile("" : "+{v[84:91]}"(wr[4][0]), "+{v[92:99]}"(wr[4][1]))
#define AK_PIN5() asm volatile("" : "+{v[100:107]}"(wr[5][0]), "+{v[108:115]}"(wr[5][1]))
#define AK_PIN6() asm volatile("" : "+{v[116:123]}"(wr[6][0]), "+{v[124:131]}"(wr[6][1]))
#define AK_PIN7() asm volatile("" : "+{v[132:139]}"(wr[7][0]), "+{v[140:147]}"(wr[7][1]))
#define AK_PIN8() asm volatile("" : "+{v[148:155]}"(wr[8][0]), "+{v[156:163]}"(wr[8][1]))
#define AK_PIN9() asm volatile("" : "+{v[164:171]}"(wr[9][0]), "+{v[172:179]}"(wr[9][1]))
#define AK_PIN10() asm volatile("" : "+{v[180:187]}"(wr[10][0]), "+{v[188:195]}"(wr[10][1]))
#define AK_PIN11() asm volatile("" : "+{v[196:203]}"(wr[11][0]), "+{v[204:211]}"(wr[11][1]))
#define V_PIN() asm volatile("" : "+{v[28:35]}"(wv[0]), "+{v[36:43]}"(wv[1]), "+{v[44:51]}"(wv[2]), "+{v[52:59]}"(wv[3]), "+{v[60:67]}"(wv[4]), "+{v[68:75]}"(wv[5]), "+{v[76:83]}"(wv[6]), "+{v[84:91]}"(wv[7]), "+{v[92:99]}"(wv[8]), "+{v[100:107]}"(wv[9]), "+{v[108:115]}"(wv[10]), "+{v[116:123]}"(wv[11]), "+{v[124:131]}"(wv[12]), "+{v[132:139]}"(wv[13]), "+{v[140:147]}"(wv[14]), "+{v[148:155]}"(wv[15]))
#define WOV_PIN0() asm volatile("" : "+{v[156:163]}"(wo[0][0][0]), "+{v[164:171]}"(wo[0][0][1]), "+{v[172:179]}"(wo[0][1][0]), "+{v[180:187]}"(wo[0][1][1]))
#define WOV_PIN1() asm volatile("" : "+{v[188:195]}"(wo[1][0][0]), "+{v[196:203]}"(wo[1][0][1]), "+{v[204:211]}"(wo[1][1][0]), "+{v[212:219]}"(wo[1][1][1]))
#define WOW_PIN0() asm volatile("" : "+{v[28:35]}"(wo[0][0][0]), "+{v[36:43]}"(wo[0][0][1]), "+{v[44:51]}"(wo[0][1][0]), "+{v[52:59]}"(wo[0][1][1]))
#define WOW_PIN1() asm volatile("" : "+{v[60:67]}"(wo[1][0][0]), "+{v[68:75]}"(wo[1][0][1]), "+{v[76:83]}"(wo[1][1][0]), "+{v[84:91]}"(wo[1][1][1]))
#define WOW_PIN2() asm volatile("" : "+{v[92:99]}"(wo[2][0][0]), "+{v[100:107]}"(wo[2][0][1]), "+{v[108:115]}"(wo[2][1][0]), "+{v[116:123]}"(wo[2][1][1]))
#define WOW_PIN3() asm volatile("" : "+{v[124:131]}"(wo[3][0][0]), "+{v[132:139]}"(wo[3][0][1]), "+{v[140:147]}"(wo[3][1][0]), "+{v[148:155]}"(wo[3][1][1]))
#define WOW_PIN4() asm volatile("" : "+{v[156:163]}"(wo[4][0][0]), "+{v[164:171]}"(wo[4][0][1]), "+{v[172:179]}"(wo[4][1][0]), "+{v[180:187]}"(wo[4][1][1]))
#define WOW_PIN5() asm volatile("" : "+{v[188:195]}"(wo[5][0][0]), "+{v[196:203]}"(wo[5][0][1]), "+{v[204:211]}"(wo[5][1][0]), "+{v[212:219]}"(wo[5][1][1]))
#define A_PIN(QW, i) do { switch (i) { case 0: if (QW) AQ_PIN0(); else AK_PIN0(); break; case 1: if (QW) AQ_PIN1(); else AK_PIN1(); break; case 2: if (QW) AQ_PIN2(); else AK_PIN2(); break; case 3: if (QW) AQ_PIN3(); else AK_PIN3(); break; case 4: if (QW) AQ_PIN4(); else AK_PIN4(); break; case 5: if (QW) AQ_PIN5(); else AK_PIN5(); break; case 6: if (QW) AQ_PIN6(); else AK_PIN6(); break; case 7: if (QW) AQ_PIN7(); else AK_PIN7(); break; case 8: if (QW) AQ_PIN8(); else AK_PIN8(); break; case 9: if (QW) AQ_PIN9(); else AK_PIN9(); break; case 10: if (QW) AQ_PIN10(); else AK_PIN10(); break; case 11: if (QW) AQ_PIN11(); else AK_PIN11(); break; } } while (0)
#define WO_PIN(VW, i) do { switch (i) { case 0: if (VW) WOV_PIN0(); else WOW_PIN0(); break; case 1: if (VW) WOV_PIN1(); else WOW_PIN1(); break; case 2: if (!VW) WOW_PIN2(); break; case 3: if (!VW) WOW_PIN3(); break; case 4: if (!VW) WOW_PIN4(); break; case 5: if (!VW) WOW_PIN5(); break; } } while (0)

__device__ __forceinline__ int lds_byte(int r, int c) {
  const int st = (r >> 4) * 2 + (c >> 5), rr = r & 15, cc = c & 31, ob = rr * 64 + cc * 2;
  return st * 1024 + (ob ^ (((ob >> 9) & 1) << 5));
}
#define PFNT ""
#define G_DvoffB dmaw
#define PFH 5
struct Pf { unsigned v, step; bool on, a; };
#define G_PF(t) do { if (pf.on && (t) < nt) asm volatile("s_mov_b32 m0, %3\n\ts_nop 0\n\tbuffer_load_dword %0, %1, %2 offen sc1 " PFNT "lds" \
    :: "v"(pf.v), "s"(pf.a ? ra : rb), "s"((t) * pf.step), "s"((unsigned)(size_t)lds + 148480) : "memory"); } while (0)

#define G_ST1(bufoff, r, so, voff, i) G_D##voff(r, (voff)[i], so, ldsw + (bufoff) + (i) * 8192)
#define G_DvoffA dmas
#define G_STAGE(bufoff, so, voff) do { G_ST1(bufoff, G_R##voff, so, voff, 0); G_ST1(bufoff, G_R##voff, so, voff, 1); } while (0)
#define G_RvoffA ra
#define G_RvoffB rb
#define G_WAIT_V(n) asm volatile("s_waitcnt vmcnt(%0)" :: "n"(n) : "memory")
__device__ __forceinline__ void gemm_h(LAS unsigned char* lds, rsrc_t ra, rsrc_t rb, unsigned cA, unsigned cB, int lda, int ldb, int nt,
                                       const Pf& pf, bool wide, f32x4 (&acc)[1][2][4][2]) {
  const int wid = __builtin_amdgcn_readfirstlane(threadIdx.x >> 6), lane = opq(0), tid = wid * 64 + lane;
  const int wr = wid >> 2, wc = wid & 3, fr = lane & 15, fq = lane >> 4;
  unsigned voffA[2], voffB[2];
  #pragma unroll
  for (int i = 0; i < 2; ++i) {
    voffA[i] = (unsigned)(((wid >> 1) + 4 * i) * 16 * lda + (wid & 1) * 1024 + lane * 16);
    const int b = tid * 16 + i * 8192, r = b >> 8, pos = (b >> 4) & 15;
    const int c = pos ^ (((r & 3) << 1) | (((r >> 3) & 1) << 3));
    voffB[i] = (unsigned)(r * ldb + c * 16);
  }
  const unsigned kB = 64 * ldb;
  const unsigned ldsw = (unsigned)(size_t)lds + (unsigned)wid * 1024u;
  const int q = (lane & 15) >> 2, p = lane & 3;
  unsigned aoff = (unsigned)(size_t)lds + 32768 + lds_byte(wr * 64 + fr, fq * 8);
  unsigned boff0 = (unsigned)(size_t)lds + (8 * fq + q) * 256 + (((wc * 4 + (p >> 1)) ^ ((q << 1) | ((fq & 1) << 3))) << 4) + 8 * (p & 1), boff1 = boff0 ^ 32;
#define H_STAGE(so, t) do { G_STAGE(so, cB + (t) * kB, voffB); G_STAGE((so) + 32768, cA + (t) * 2048, voffA); if (wide) G_STAGE((so) + 16384, cB + (t) * kB + 256, voffB); } while (0)
  #pragma unroll
  for (int t = 3; t < PFH; ++t) G_PF(t);
  H_STAGE(0, 0); H_STAGE(49152, 1); H_STAGE(98304, 2);
  const rsrc_t rp = pf.a ? ra : rb;
  const unsigned pd = (unsigned)(size_t)lds + 148480, pe = pf.on ? nt * pf.step : 0u;
  unsigned cur = 0, t = 0, ps = PFH * pf.step, sd, sx, sy;
  if (wide) H_LOOP2(); else H_LOOP1();
}

__device__ __forceinline__ u32x4 epi_pack(const f32x4 a0, const f32x4 a1) {
  const unsigned x0 = pack2(a0[0], a0[1]), x1 = pack2(a0[2], a0[3]);
  const unsigned y0 = pack2(a1[0], a1[1]), y1 = pack2(a1[2], a1[3]);
  auto s0 = __builtin_amdgcn_permlane16_swap(x0, y0, false, false);
  auto s1 = __builtin_amdgcn_permlane16_swap(x1, y1, false, false);
  return (u32x4){s0[0], s1[0], s0[1], s1[1]};
}
__device__ __forceinline__ unsigned gelu2(unsigned w) {
  const f32x2 x = {lo16(w), hi16(w)};
  const float c0 = -2.f * 1.4426950408889634f * 0.7978845608028654f, c1 = c0 * 0.044715f;
  const f32x2 a = x * (x * x * c1 + c0);
  const f32x2 d = (f32x2){__builtin_amdgcn_exp2f(a[0]), __builtin_amdgcn_exp2f(a[1])} + 1.f;
  const f32x2 y = x * (f32x2){__builtin_amdgcn_rcpf(d[0]), __builtin_amdgcn_rcpf(d[1])};
  return pack2(y[0], y[1]);
}
__device__ __forceinline__ float silu_f(float b) { return b * __builtin_amdgcn_rcpf(1.f + __expf(-b)); }

__device__ __forceinline__ unsigned blk_in(int r, int c) { const int ob = r * 64 + c * 2; return ob ^ (((ob >> 9) & 1) << 5); }
__device__ __forceinline__ unsigned xn_blk(int rb, int cb) { return (unsigned)(((rb >> 1) * 32 + cb) * 2 + (rb & 1)) * 1024u; }

template <int EPI>
__device__ __forceinline__ void epi_rows(const f32x4 (&acc)[1][2][4][2], rsrc_t rs, unsigned obase, int ldc, int pm2, int pn2, int sh, bool wide, int wr, int wc, int ln) {
  const int fq = ln >> 4, cb = 16 * (fq & 1) + 8 * (fq >> 1), fr = (ln & 15) + sh;
  const unsigned lo = (unsigned)((fr >> 4) * 16 * ldc) + blk_in(fr & 15, cb);
  const unsigned ob = (unsigned)(pm2 * 128 + 64 * wr) * (unsigned)ldc + (unsigned)(pn2 * 4 + wc) * 1024u + lo;
  u32x4 r[8];
  if (EPI >= 2) {
    #pragma unroll
    for (int i = 0; i < 8; ++i)
      r[i] = ld16(rs, EPI == 2 ? WS_XN + xn_blk(pm2 * 8 + 4 * wr + (i & 3), pn2 * 4 + 4 * (i >> 2) + wc) + lo : WS_X1 + ob + (i & 3) * 32768 + (i >> 2) * 4096);
  }
  #pragma unroll
  for (int i = 0; i < 8; ++i) {
    if (i == 4 && !wide) break;
    u32x4 w = epi_pack(acc[0][i >> 2][i & 3][0], acc[0][i >> 2][i & 3][1]);
    #pragma unroll
    for (int k = 0; k < 4; ++k) {
      w[k] = EPI < 2 ? gelu2(w[k]) : pack2(lo16(r[i][k]) + lo16(w[k]), hi16(r[i][k]) + hi16(w[k]));
      if (EPI == 3) w[k] = pack2(silu_f(lo16(w[k])), silu_f(hi16(w[k])));
    }
    st16(rs, obase + ob + (unsigned)(16 * (i & 3) * ldc) + (i >> 2) * 4096, w);
  }
}

__device__ __forceinline__ float row_sum16(float v) {
  v += __uint_as_float(__builtin_amdgcn_update_dpp(0, (int)__float_as_uint(v), 0x128, 0xf, 0xf, false));
  v += __uint_as_float(__builtin_amdgcn_update_dpp(0, (int)__float_as_uint(v), 0x124, 0xf, 0xf, false));
  v += __uint_as_float(__builtin_amdgcn_update_dpp(0, (int)__float_as_uint(v), 0x122, 0xf, 0xf, false));
  v += __uint_as_float(__builtin_amdgcn_update_dpp(0, (int)__float_as_uint(v), 0x121, 0xf, 0xf, false));
  return v;
}
__device__ __forceinline__ void ln_rows(u32x4 (&a)[8]) {
  f32x2 v[32];
  #pragma unroll
  for (int i = 0; i < 32; ++i) v[i] = (f32x2){lo16(a[i >> 2][i & 3]), hi16(a[i >> 2][i & 3])};
  f32x2 s[4] = {v[0], v[1], v[2], v[3]};
  #pragma unroll
  for (int i = 4; i < 32; ++i) s[i & 3] += v[i];
  s[0] = (s[0] + s[1]) + (s[2] + s[3]);
  const float mean = row_sum16(s[0][0] + s[0][1]) * (1.f / DM);
  #pragma unroll
  for (int i = 0; i < 32; ++i) { const f32x2 d = v[i] - mean; s[i & 3] = i < 4 ? d * d : s[i & 3] + d * d; }
  s[0] = (s[0] + s[1]) + (s[2] + s[3]);
  const float var = row_sum16(s[0][0] + s[0][1]) * (1.f / DM);
  const float mb = rb1(mean), vb = rb1(var);
  const float rden = __builtin_amdgcn_rcpf(rb1(__builtin_amdgcn_sqrtf(rb1(vb + 1e-5f))));
  #pragma unroll
  for (int i = 0; i < 32; ++i) {
    const f32x2 d = v[i] - mb;
    const unsigned w = pack2(d[0], d[1]);
    const f32x2 y = (f32x2){lo16(w), hi16(w)} * rden;
    a[i >> 2][i & 3] = pack2(y[0], y[1]);
  }
}

template <int N>
__device__ __forceinline__ void fin_sum(rsrc_t rs, unsigned po, u32x4* v) {
  u32x4 pv[N][8];
  #pragma unroll
  for (int i = 0; i < 8 * N; ++i) pv[i >> 3][i & 7] = ld16(rs, po + (i >> 3) * 16384 + ((i >> 1) & 3) * 131072 + (i & 1) * 1024);
#define PF4(i) __builtin_bit_cast(f32x4, pv[c][i])
  #pragma unroll
  for (int c = 0; c < N; ++c) v[c] = epi_pack((PF4(0) + PF4(2)) + (PF4(4) + PF4(6)), (PF4(1) + PF4(3)) + (PF4(5) + PF4(7)));
}
__device__ __forceinline__ void fin_out(const Ptrs& P, unsigned eo, u32x4 v, u32x4 gv, u32x4 xv) {
  #pragma unroll
  for (int k = 0; k < 4; ++k) {
    v[k] = pack2(lo16(v[k]) * lo16(gv[k]), hi16(v[k]) * hi16(gv[k]));
    v[k] = pack2(lo16(xv[k]) + lo16(v[k]), hi16(xv[k]) + hi16(v[k]));
  }
  *(u32x4*)((char*)P.out + eo) = v;
}

__device__ __forceinline__ void mlp_role(LAS unsigned char* lds, const Ptrs& P, int hf, int slot, int wid) {
  const int wr = wid >> 2, wc = wid & 3;
  const int p2 = hf * 8 + (slot >> 2), pn = slot & 3;
  char* ws = P.ws;
  const rsrc_t rs = WS_RSRC(P);
  const u16* const p1 = P.w1; const u16* const p2w = P.w2; const u16* const p3 = P.w3; const u16* const p4 = P.w4;
#define M_DONE(c) do { VMWAIT(); BARRIER(); if (wid == 0) cnt_add(ws, c, opq(0)); } while (0)
  #pragma unroll 1
  for (int code = 0; code < 7; ++code) {
    int ln = opq(0);
    if (wid == 0) {
      if (code == 0) flag_wait(P, F_LN1(p2 * 4 + (ln & 3)), 0, ln);
      else if (code < 3 || code == 6) {
        const unsigned co = CNT_OFF + (code == 1 ? C_W1(min(max(p2 - 1 + min(ln, 2), 0), 15)) : code == 2 ? C_LN(p2) : C_W3(p2)) * 128;
        for (unsigned spins = 0; __any(ld4(rs, co) < 4u);) {
          __builtin_amdgcn_s_sleep(2);
          if (spin_fail(spins, P, ln)) break;
        }
      }
    }
    BARRIER();
    TLW(3 * code);
    const bool w3 = code >= 2 && code < 6, wide = code != 5;
    const int lda = code == 6 ? 7168 : 2048, ldb = w3 ? 7168 : 2048, c2 = w3 ? (code - 2) * 8 + (wide ? 2 * pn : pn) : 2 * pn;
    const rsrc_t rb = __builtin_amdgcn_make_buffer_rsrc((void*)(code == 0 ? p1 : code == 1 ? p2w : w3 ? p3 : p4), 0, code < 2 ? DM * DM * 2 : DM * FF * 2, RSRC_W3);
    const unsigned cA = (code == 0 ? WS_XN2 : code == 1 ? WS_H1 : w3 ? WS_X1N : WS_H3) + p2 * 128 * lda, cB = c2 * 256;
    Pf pf;
    pf.on = wc == 0 && (!wr || code < 3 || code == 6); pf.a = wr;
    ln &= 31;
    pf.v = wr ? cA + (pn * 2 + (ln >> 4)) * 16 * lda + (ln & 15) * 128 : cB + ((slot >> 2) * 8 + (ln >> 2)) * ldb + (ln & (wide ? 3 : 1)) * 128;
    pf.step = wr ? 2048 : 64 * ldb;
    f32x4 acq[1][2][4][2];
    gemm_h(lds, rs, rb, cA, cB, lda, ldb, code == 6 ? 56 : 16, pf, wide, acq);
    TLW(3 * code + 1);
    ln = opq(0);
    if (code == 1) {
      epi_rows<2>(acq, rs, WS_X1, 2048, p2, c2, 0, true, wr, wc, ln);
      M_DONE(C_W2(p2));
      if (wid == 0) cnt_wait(P, C_W2(p2), 4, ln);
      BARRIER();
      const unsigned ro = (unsigned)(p2 * 8 + pn * 2 + wr) * 32768u + ((ln >> 2) & 3) * 1024 + blk_in(wc * 4 + (ln >> 4), (ln & 3) * 8);
      u32x4 v[8];
      #pragma unroll
      for (int i = 0; i < 8; ++i) v[i] = ld16(rs, WS_X1 + ro + i * 4096);
      ln_rows(v);
      #pragma unroll
      for (int i = 0; i < 8; ++i) st16(rs, WS_X1N + ro + i * 4096, v[i]);
      M_DONE(C_LN(p2));
    } else if (code == 6) {
      epi_rows<3>(acq, rs, WS_G, 2048, p2, c2, 0, true, wr, wc, ln);
      M_DONE(C_W4(p2));
    } else {
      VMWAIT();
      if (code == 0 && wid < 4) touch(p2w, DM * DM * 2, ((hf * 32 + slot) * 256 + wid * 64 + ln) * 128, lds);
      epi_rows<1>(acq, rs, code ? WS_H3 : WS_H1, ldb, p2, c2, code ? 0 : (pn & 1) ? -1 - (pn >> 1) : 1 + (pn >> 1), wide, wr, wc, ln);
      if (code == 0 && wid == 0) setup_wait(P, ln);
      if (code == 0 || code == 5) M_DONE(code ? C_W3(p2) : C_W1(p2));
    }
    TLW(3 * code + 2);
  }
}

#define FINE 52
__device__ __forceinline__ void fin_role(LAS unsigned char* lds, const Ptrs& P, int wid) {
  const int wr = wid >> 2, wc = wid & 3;
  const rsrc_t rs = WS_RSRC(P);
  volatile LAS int* tk = (volatile LAS int*)(lds + 147456);
  unsigned tks = 0;
#define POP() do { if (wid == 0 && opq(0) == 0) tks = __hip_atomic_fetch_add((gu32*)(P.ws + CNT_OFF + C_HEAD * 128), 1u, RLX_AGENT); } while (0)
  POP();
  for (int round = 0;; ++round) {
    if (wid == 0 && opq(0) == 0) tk[round & 1] = tks;
    LBARRIER();
    const int u = __builtin_amdgcn_readfirstlane(tk[round & 1]);
    if (u >= 2 * FINE + 8 * (XS3 - FINE)) break;
    const bool big = u < 2 * FINE;
    const int sg = big ? u >> 1 : FINE + ((u - 2 * FINE) >> 3), cc0 = big ? (u & 1) * 4 : (u - 2 * FINE) & 7;
    const int ln = opq(0), fq = ln >> 4, cb = 16 * (fq & 1) + 8 * (fq >> 1);
    const unsigned eo = (unsigned)(sg * 32 + wr * 16 + (ln & 15)) * 2048u + (unsigned)(cc0 * 128 + wc * 32 + cb) * 2u;
    const unsigned go = WS_G + (unsigned)(sg * 2 + wr) * 32768u + (cc0 * 4 + wc) * 1024 + blk_in(ln & 15, cb);
    const unsigned po = WS_PT + (unsigned)sg * 524288u + cc0 * 16384 + wc * 4096 + wr * 2048 + ln * 16;
    const int xg = (sg >= XS1) + (sg >= XS2), want = sg + 1 - (xg == 2 ? XS2 : xg * XS1);
    u32x4 v[4], gv[4], xv[4];
    if (big) {
      if (wid == 2) flag_wait(P, F_PRG(5 + xg, ln & 31), want, ln);
      BARRIER();
      fin_sum<2>(rs, po, v);
      asm volatile("" : "+v"(v[0]), "+v"(v[1]) :: "memory");
      #pragma unroll
      for (int c = 0; c < 4; ++c) xv[c] = __builtin_nontemporal_load((const u32x4*)((const char*)P.x + eo + c * 256));
      fin_sum<2>(rs, po + 32768, v + 2);
      if (wid == 1) cnt_wait(P, C_W4(sg >> 2), 4, ln);
      BARRIER();
      #pragma unroll
      for (int c = 0; c < 4; ++c) gv[c] = ld16(rs, go + c * 4096);
      #pragma unroll
      for (int c = 0; c < 4; ++c) fin_out(P, eo + c * 256, v[c], gv[c], xv[c]);
    } else {
      if (wid == 1) cnt_wait(P, C_W4(sg >> 2), 4, ln);
      BARRIER();
      gv[0] = ld16(rs, go);
      xv[0] = __builtin_nontemporal_load((const u32x4*)((const char*)P.x + eo));
      if (wid == 2) flag_wait<3>(P, F_PRG(5 + xg, (ln & 3) * 8 + cc0), want, ln);
      BARRIER();
      fin_sum<1>(rs, po, v);
      fin_out(P, eo, v[0], gv[0], xv[0]);
    }
    POP();
  }
}

__device__ __forceinline__ u32x4 ldsv(LAS unsigned char* p) { return *(volatile LAS u32x4*)p; }
#define DMA_A dman
#define DMA_V dman
#define DMA_O dmaw
#define TOUCH 0
#define TAUX 0
#define POISON4 ((u32x4){~0u, ~0u, ~0u, ~0u})
#define DLYM 0
#define DLYA 8
#define DLYK (DLYA + 8)
#define DLYO 8
#define FLA 0
#define FLB 0
#define CADM 1
#define CADO 1

#define RTC(id)
#define DCA 2
#define DCB 12
#define DL1 6
#define DL2 14
#define DLB 18
#define ADL 0
#define SN0 150
#define HELP 256
#define BORD 0
#define QT 0
__device__ __forceinline__ void track(int& est, int d, int gain) { const int e = d - (est >> 4); est += (e > 44 ? DCB : e) * gain; }
__device__ __forceinline__ unsigned rfl(unsigned v) { unsigned r; asm volatile("s_nop 0\n\tv_readfirstlane_b32 %0, %1\n\ts_nop 4" : "=s"(r) : "v"(v)); return r; }
__device__ __forceinline__ void plan_wait(rsrc_t rs, unsigned fo, int s, int& lim, unsigned& tm, unsigned& d) {
  while ((d & 255u) != (unsigned)s && (lim -= DEC1) > 0) {
    __builtin_amdgcn_s_sleep(1);
    const u32x2 v = __builtin_amdgcn_raw_buffer_load_b64(rs, 0, fo, AUX_SC1);
    tm = rfl(v.x); d = rfl(v.y);
  }
  const unsigned now = (unsigned)__builtin_amdgcn_s_memrealtime() * 4u;
  tm = now + min((int)(tm - now), 2000);
  d = min(d >> 8, 1000u);
}
#define SPIN_BUDGET (1 << 21)

__device__ __forceinline__ unsigned rec_go(LAS unsigned char* w, const Ptrs& P, u32x2 f, int wid, int lane) {
  if (wid >= 6) {
    if (__any(f.y != P.tag)) f.x = flag_wait(P, wid == 6 ? F_LN0(lane) : F_CAL(lane), 0, lane);
    if (wid == 7) { const unsigned m = near_half(f.x); if (lane == 0) *(volatile LAS unsigned*)w = m; }
  }
  LBARRIER();
  return __builtin_amdgcn_readfirstlane(*(volatile LAS unsigned*)w);
}

#define SLOT(i) ((i) * 16384)
#define LA_MEM SLOT(8)
#define LA_Q SLOT(9)
#define LA_K (SLOT(9) + 2048)
#define LA_P (SLOT(9) + 4096)
#define LA_VM (SLOT(9) + 6144)
#define LA_KX (SLOT(9) + 8192)

template <bool QW>
__device__ __forceinline__ void a_wave(LAS unsigned char* lds, Ptrs& P, int h, int wid) {
  char* ws = P.ws;
  const int lane = opq(0), tid = wid * 64 + lane;
  const int j = wid & 3, g = lane >> 4, q4 = (lane & 15) >> 2, p4 = lane & 3;
  const rsrc_t rs = WS_RSRC(P);
  const rsrc_t rq = __builtin_amdgcn_make_buffer_rsrc((void*)P.Wq, 0, HD * DM * 2, RSRC_W3);
  const rsrc_t rk = __builtin_amdgcn_make_buffer_rsrc((void*)P.Wk, 0, HD * DM * 2, RSRC_W3);
  const unsigned lbase = (unsigned)(size_t)lds;
  int lim = SPIN_BUDGET;
  u32x8 wr[12][2];
  const int trow = 16 * (g >> 1) + 4 * (g & 1) + 8 * (q4 >> 1) + (q4 & 1);
  const int tsw = ((q4 & 1) | ((g & 1) << 1) | ((q4 >> 1) << 2)) << 1;
  const int toff = trow * 256 + (((4 * j + (p4 >> 1)) ^ tsw) << 4) + 8 * (p4 & 1);
  {
    unsigned dv[2];
    #pragma unroll
    for (int x = 0; x < 2; ++x) {
      const int row = 8 * wid + 4 * x + (lane >> 4);
      const int sw = ((row & 1) | (((row >> 2) & 1) << 1) | (((row >> 3) & 1) << 2)) << 1;
      dv[x] = (unsigned)(row * (HD * 2) + h * 256 + (((lane & 15) ^ sw) << 4));
    }
#define A_GRP(n) do { _Pragma("unroll") for (int i = 0; i < 8; ++i) \
      DMA_A(((n) & 1) ? rk : rq, dv[i & 1], (4 * (((n) & 1) ? ((n) >> 1) ^ ((n) >> 2) : (((n) >> 1) + 1) & 3) + (i >> 1)) * 64 * HD * 2, lbase + SLOT(4 * ((n) & 1) + (i >> 1)) + wid * 2048 + (i & 1) * 1024); } while (0)
    A_GRP(0); A_GRP(1);
    #pragma unroll
    for (int n = 0; n < 6; ++n) {
      VMW(8);
      BARRIER();
      if ((n & 1) == (QW ? 0 : 1)) {
        #pragma unroll
        for (int b = 0; b < 4; ++b) {
          LAS unsigned char* t = lds + SLOT(QW ? 0 : 4) + ((b ^ j) << 14);
          wr[4 * (n >> 1) + b][0] = rd_frag<8192, 512>(t + toff);
          wr[4 * (n >> 1) + b][1] = rd_frag<8192, 512>(t + (toff ^ 32));
        }
        #pragma unroll
        for (int b = 0; b < 4; ++b) A_PIN(QW, 4 * (n >> 1) + b);
      }
      LBARRIER();
      A_GRP(n + 2);
    }
  }
  const unsigned cb = comm_base(ws);
  u32x2 f0 = {0u, 0u};
  #pragma unroll
  for (int rd = 0; rd < 2; ++rd) {
    if (rd == 0) VMW(8);
    else {
      if (wid >= 6) f0 = ld8(rs, FLG_OFF + (wid == 6 ? F_LN0(lane) : F_CAL(lane)) * 128);
      const unsigned fv = wid == 3 ? FLG_OFF + F_PRG(min((lane >> 1) & 3, 2), h) * 128 + (lane & 1) * 4 : WS_QF + j * (16u << 20) + h * 8192 + lane * 128;
      if (QW) asm volatile("s_mov_b32 m0, %2\n\ts_nop 0\n\tbuffer_load_dword %0, %1, 0 offen sc1 lds" :: "v"(fv), "s"(rs), "s"(lbase + LA_P + (wid == 3 ? 640 : 1024)) : "memory");
      get_tag(P);
      VMWAIT();
    }
    BARRIER();
    u32x8 f[2][2];
    #pragma unroll
    for (int i = 0; i < 2; ++i) {
      f[i][0] = rd_frag<8192, 512>(lds + SLOT((QW ? 0 : 2) + 4 * rd + i) + toff);
      f[i][1] = rd_frag<8192, 512>(lds + SLOT((QW ? 0 : 2) + 4 * rd + i) + (toff ^ 32));
    }
    asm volatile("" : "+v"(f[0][0]), "+v"(f[0][1]), "+v"(f[1][0]), "+v"(f[1][1]));
    LBARRIER();
    #pragma unroll
    for (int i = 0; i < 2; ++i)
      #pragma unroll
      for (int t = 0; t < 2; ++t) {
        LAS unsigned char* p = lds + SLOT((QW ? 0 : 2) + 4 * rd + i) + j * 4096 + t * 2048 + lane * 16;
        *(LAS u32x4*)p = (u32x4){f[i][t][0], f[i][t][1], f[i][t][2], f[i][t][3]};
        *(LAS u32x4*)(p + 1024) = (u32x4){f[i][t][4], f[i][t][5], f[i][t][6], f[i][t][7]};
      }
  }
  const unsigned mask = rec_go(lds + LA_P + 128, P, f0, wid, lane);
  const int idx = (lane & 1) ? 0xEEEE : 0x4444;
  const unsigned ab_a = lbase + LA_MEM + lane * 16, am_a = ab_a + j * 1024;
  const unsigned t0_a = lbase + SLOT((QW ? 0 : 4) + j) + j * 4096 + lane * 16;
  const unsigned iw_a = lbase + (QW ? LA_Q : LA_K) + j * 512 + q4 * 128 + g * 32 + p4 * 4;
  const unsigned qr_a = lbase + LA_Q + g * 128 + (lane & 7) * 16;
  const unsigned kr_a = lbase + LA_K + g * 128 + (2 * q4 + (lane & 1)) * 16;
  const unsigned fr_a = lbase + LA_P + p4 * 4;
  if (tid < 12) *(LAS unsigned*)(lds + LA_P + tid * 4) = 0u;
  const unsigned vo_v = VB_AT(h) + j * 512 + lane * 4, md = lbase + LA_VM - VB_AT(h);
  const unsigned oo_v = OB_AT((lane >> 2) & 1, h) + (j * 16 + (g >> 1) * 8 + p4 * 2 + (g & 1)) * 16;
  const unsigned long long onej = 1ull << j;
  const unsigned lkx = lbase + LA_KX + j * 2048;
  u32x8 oxr = {};
  const unsigned xo_v = (lane >> 5) * 2048 + (lane & 31) * 16, vl_a = lbase + LA_VM + (j & 1) * 1024 + lane * 4;
  const int dq = 16 * (j >> 1) + (lane & 15) - 4 * g;
  const unsigned fb = step_base(cb, mask, 0);
  unsigned tgt = (unsigned)__builtin_amdgcn_s_memrealtime() * 4u;
  unsigned long long tp = 0;
  unsigned pt = 0, pd = 0;
  const unsigned long long fmask = __builtin_amdgcn_readfirstlane((16u >> wid) & 1u);
  int known = 0;
  __builtin_amdgcn_s_setprio(QW ? 2 : 1);
  LBARRIER();
  report(P, wid);
  TL(72);
  for (int s = 0; s < NSEG; ++s) {
    const unsigned sb = step_base(cb, mask, s);
    const unsigned sbm = sb - (lbase + LA_MEM);
    const unsigned fo = (unsigned)(s * NH + h) * 8192u + j * 2048 - (lbase + LA_MEM);
    GSDECL;
    RTC(h);
    lim = __builtin_amdgcn_readfirstlane(lim);
    if (QW) {
      if (wid == 3 && s > 0) {
        if (opq(0) == 0) __builtin_amdgcn_raw_buffer_store_b32((unsigned)tp * 4u - tgt, rs, 0, fb + FL_O(h), AUX_SC1);
        plan_wait(rs, fb + FL_T(s), s, lim, pt, pd);
        tgt = pt;
      }
      if (s >= known) {
        const u32x2 v = *(volatile LAS u32x2*)(lds + LA_P + 640 + (opq(0) & 3) * 8);
        if (!__any(v.y != P.tag)) known = rfl(min(min(__builtin_amdgcn_readlane(v.x, 0), __builtin_amdgcn_readlane(v.x, 1)), __builtin_amdgcn_readlane(v.x, 2)));
      }
      kv_wait(P, s, h, known, opq(0));
      const unsigned kxo = WS_KF + fo, vxo = WS_VF + fo;
      if (wid < 3) for (int i = 0; i < ADL; ++i) __builtin_amdgcn_s_sleep(1);
      dmas(rs, ab_a, kxo, lkx);
      dmas(rs, ab_a, kxo + 1024, lkx + 1024);
      dmas(rs, ab_a, WS_QF + fo, lbase + LA_P + 1024);
      dmas(rs, ab_a, WS_QF + fo + 1024, lbase + LA_P + 1024);
      if (wid == 2 && s + 1 >= known) {
        const int ln = opq(0);
        const unsigned fv = FLG_OFF + F_PRG(min((ln >> 1) & 3, 2), h) * 128 + (ln & 1) * 4;
        asm volatile("s_mov_b32 m0, %2\n\ts_nop 0\n\tbuffer_load_dword %0, %1, 0 offen sc1 lds" :: "v"(fv), "s"(rs), "s"(lbase + LA_P + 640) : "memory");
      }
      unsigned rg = 0x3c000000u;
      A_MAIN_Q();
      TL(s);
      GSFLUSH(0, 24 + wid); GSFLUSH(1, 4 + wid); GSFLUSH(2, 12 + j); GSFLUSH(3, 16 + j); GSFLUSH(4, 20 + j);
      if (s + PD < NSEG && !(opq(0) & 8)) __builtin_amdgcn_raw_buffer_store_b128(poison4(), rs, oo_v, step_base(cb, mask, s + PD), AUX_SC1);
    } else {
      const unsigned qxo = WS_QF + fo - (j & 1) * 2048, oxo = WS_OX + (unsigned)((s == 0 || s == NSEG - 1 ? s : s - 1) * NH + h) * 8192u + (j & 1) * 4096 + (j >> 1) * 1024;
      A_MAIN_K();
      if (s >= NSEG - 2) {
        __builtin_amdgcn_raw_buffer_store_b128((u32x4){oxr[0], oxr[1], oxr[2], oxr[3]}, rs, xo_v, oxo + (NSEG - 1 - s) * NH * 8192, AUX_SC1);
        __builtin_amdgcn_raw_buffer_store_b128((u32x4){oxr[4], oxr[5], oxr[6], oxr[7]}, rs, xo_v + 512, oxo + (NSEG - 1 - s) * NH * 8192, AUX_SC1);
      }
      if (wid == 7) GSFLUSH(0, 28);
      GSFLUSH(1, 4 + wid);
    }
  }
  VMWAIT();
  BARRIER();
  if (wid == 4) flag_set(P, F_PRG(4, h), NSEG, opq(0));
}

#define TSL 4
#define LB_MEM 0
#define LB_RED 16384
template <bool VW>
__device__ __forceinline__ void b_wave(LAS unsigned char* lds, Ptrs& P, int r, int c, int wid, int bs, int xg) {
  constexpr int NHD = VW ? 2 : 6;
  char* ws = P.ws;
  const int lane = opq(0), tid = wid * 64 + lane;
  const int j = wid & 3, g = lane >> 4, q4 = (lane & 15) >> 2, p4 = lane & 3;
  const int b = r * 32 + c;
  const rsrc_t rs = WS_RSRC(P);
  const rsrc_t rv = __builtin_amdgcn_make_buffer_rsrc((void*)P.Wv, 0, HD * DM * 2, RSRC_W3);
  const rsrc_t ro = __builtin_amdgcn_make_buffer_rsrc((void*)P.Wo, 0, HD * DM * 2, RSRC_W3);
  const unsigned lbase = (unsigned)(size_t)lds;
  int lim = SPIN_BUDGET;
  const int h0 = ((VW ? 2 * j : 8 + 6 * j) + 5 * c) & 31;
  u32x8 wv[16], wo[6][2][2];
  {
    if ((wid & 3) < 2 && xg < 2) {
      const unsigned nk = 8 + 2 * xg, M = (bs * 2 + (wid & 1)) * 64 + lane, row = M / nk, k = M - row * nk;
      if (row < HELP) touch(VW ? P.Wq : P.Wk, HD * DM * 2, (row < 256 ? row + (VW ? 512 : 256) : row + 512) * (HD * 2) + min(xg * 4096 + (k >> 1) * 1024 + 256, 7936u) + (k & 1) * 128, lds);
    }
    const int nl = xg == 2 ? 6 : 5;
    #pragma unroll
    for (int i = 0; i < 2; ++i) {
      const unsigned L = bs * 1024 + wid * 128 + i * 64 + lane, row = L / nl;
      touch(P.Wo, HD * DM * 2, row * (DM * 2) + (c - (bs >> 1)) * 64 + (L - row * nl) * 128, lds);
    }
    VMWAIT();
    BARRIER();
    const int pos = lane >> 2;
    const int prow = (pos & 9) | ((pos & 2) << 1) | ((pos & 4) >> 1);
    const unsigned dv0 = (unsigned)(prow * (DM * 2) + c * 64 + ((lane & 3) << 4));
    const unsigned dv1 = (unsigned)(prow * (DM * 2) + c * 64 + (((lane & 3) ^ 2) << 4));
    const int toff = wid * 16384 + (g * 16 + q4) * 64 + (((p4 >> 1) ^ ((g & 1) << 1)) << 4) + 8 * (p4 & 1);
    #pragma unroll
    for (int rd = 0; rd < NHD / 2; ++rd) {
      #pragma unroll
      for (int hh = 0; hh < 2; ++hh) {
        const int h = (h0 + 2 * rd + hh) & 31;
        #pragma unroll
        for (int i = 0; i < 8; ++i)
          DMA_O(ro, (i & 1) ? dv1 : dv0, (unsigned)(h * 128 + i * 16) * (DM * 2), lbase + wid * 16384 + hh * 8192 + i * 1024);
      }
      VMWAIT();
      #pragma unroll
      for (int hh = 0; hh < 2; ++hh)
        #pragma unroll
        for (int ks = 0; ks < 2; ++ks)
          #pragma unroll
          for (int t = 0; t < 2; ++t)
            wo[2 * rd + hh][ks][t] = rd_frag<4096, 256>(lds + (toff ^ (t * 32)) + hh * 8192 + ks * 512);
      #pragma unroll
      for (int hh = 0; hh < 2; ++hh)
        WO_PIN(VW, 2 * rd + hh);
      asm volatile("s_waitcnt lgkmcnt(0)" ::: "memory");
    }
    const int lr = lane >> 3;
    #pragma unroll
    for (int i = 0; i < 16; ++i) {
      const int sw = (((lr >> 2) & 1) | ((i & 1) << 1)) << 1;
      const unsigned dv = (unsigned)(lr * (HD * 2) + b * 128 + (((lane & 7) ^ sw) << 4));
      DMA_V(rv, dv, (unsigned)(wid * 128 + i * 8) * (HD * 2), lbase + wid * 16384 + i * 1024);
    }
    VMWAIT();
    BARRIER();
    if (VW) {
      const int trow = 16 * (g >> 1) + 4 * (g & 1) + 8 * (q4 >> 1) + (q4 & 1);
      const int tsw = ((g & 1) | ((q4 >> 1) << 1)) << 1;
      LAS unsigned char* tv = lds + trow * 128 + (((2 * j + (p4 >> 1)) ^ tsw) << 4) + 8 * (p4 & 1);
      #pragma unroll
      for (int ks = 0; ks < 16; ++ks) wv[ks] = rd_frag<4096, 256>(tv + ks * 8192);
      V_PIN();
    }
    LBARRIER();
  }
  const unsigned cb = comm_base(ws);
  get_tag(P);
  if (wid == 0) touch(P.x, SEQ * DM * 2, (SEQ - 64 + lane) * 2048 + c * 64, lds);
  const unsigned mask = rec_go(lds + LB_RED + 4096 + 64, P, (u32x2){0u, P.tag - 1u}, wid, lane);
  const int idx = (lane & 1) ? 0xEEEE : 0x4444;
  const unsigned am_a = lbase + LB_MEM + lane * 16;
  const unsigned fb = step_base(cb, mask, 0);
  volatile LAS unsigned* tsl = (volatile LAS unsigned*)(lds + LB_RED + 4096);
  if (tid < 8) tsl[tid] = tid ? 0u : (unsigned)__builtin_amdgcn_s_memrealtime() * 4u;
  unsigned dn = 600;
  int da = 565 * 16, gb = 362 * 16;
  if (VW) __builtin_amdgcn_s_setprio(2);
  LBARRIER();
  report(P, wid);
  TL(72);
  for (int s = 0; s < NSEG; ++s) {
    const unsigned sb = step_base(cb, mask, s);
    const int ln = opq(lane), gg = ln >> 4;
    GSDECL;
    RTC(32 + b);
    const int l16 = ln * 16;
    const int xc = 32 * (c & 1) + 2 * (ln & 15);
    const unsigned mo_v = MF_AT(c >> 1) + ((xc >> 4) * 16 + (4 * r + gg) * 2 + ((xc >> 3) & 1)) * 16 + (xc & 7) * 2;
    if (!VW) {
      const unsigned tm = __builtin_amdgcn_readfirstlane(*tsl), tgt = tm + DLB, la = lbase + LB_MEM + j * 2048 + l16, mo = sb + j * 2048;
      B_POLL();
      if (wid == 7) {
        const int late = (int)((unsigned)__builtin_amdgcn_s_memrealtime() * 4u - tgt) - SN0;
        const unsigned to = tm + dn + (late > 80 ? late : 0);
        if (ln == 0) tsl[1] = to;
        if (b == 0) {
          dn = (da >> 4) + DL1;
          if (ln == 0) { *tsl = to + (gb >> 4) + DL2; st8(rs, fb + FL_T(s + 1), (u32x2){to + (gb >> 4) + DL2, (dn << 8) | (s + 1)}); }
          const unsigned va = ld4(rs, fb + FL_O(ln & 31)), vb = ld4(rs, fb + FL_B(ln));
          if (s > 2) {
            track(da, rfl(wmax(va)), s < 6 ? 6 : DCA);
            track(gb, rfl(wmax(vb)), s < 6 ? 6 : DCA);
          }
        }
      }
      if (wid == 7 && s > 0 && s + 2 < NSEG) __builtin_amdgcn_raw_buffer_store_b32(~0u, rs, mo_v, step_base(cb, mask, s + 2), AUX_SC1);
    } else {
      GS(3);
      const unsigned vo = VB_AT(b >> 1) + ((b & 1) * 4 + j) * 256;
      V_PROJ();
      GS(0); GSFLUSH(0, 4 + j);
      if (s + PD < NSEG && ln < 16) st16(rs, step_base(cb, mask, s + PD) + vo + l16, poison4());
    }
    GSFLUSH(3, 24 + wid);
    if (s + 1 == NSEG) break;
    unsigned so[6];
    #pragma unroll
    for (int i = 0; i < NHD; ++i) { const int h = (h0 + i) & 31; so[i] = __builtin_amdgcn_readfirstlane(sb + OB_AT(r, h)); }
    const unsigned sn = __builtin_amdgcn_readfirstlane(step_base(cb, mask, s + 1));
    const unsigned rw_a = lbase + LB_RED + (wid * 128 + (gg & 1) * 64 + (gg >> 1) * 16 + (ln & 15)) * 4;
    const unsigned rr_a = lbase + LB_RED + ln * 8;
    if (wid == 7) {
      if (b != 0) {
        __builtin_amdgcn_s_sleep(4);
        unsigned tm = 0;
        dn = 0;
        plan_wait(rs, fb + FL_T(s + 1), s + 1, lim, tm, dn);
        if (ln == 0) *tsl = tm;
      }
      const unsigned tgt = rfl(tsl[1]);
      T_WAIT();
    }
    lim = __builtin_amdgcn_readfirstlane(lim);
    unsigned long long t1;
    if (VW) WO_V(); else WO_W();
    if (wid == 0) {
      if (ln == 0) st4(rs, fb + FL_B(b), (unsigned)t1 * 4u - tsl[1]);
    }
    if (wid == 0) {
      GSFLUSH(2, 16);
      TL(s);
    }
    GSFLUSH(1, 8 + wid);
    if (wid == 0 || wid == 4) { GSFLUSH(4, 17 + (wid >> 2)); GSFLUSH(0, 19 + (wid >> 2)); }
  }
  __builtin_amdgcn_s_setprio(0);
  const int ln = opq(0);
  TL(64);
  volatile LAS int* rdy = (volatile LAS int*)(lds + 98304 + 3 * 16384);
  if (tid == 0) *rdy = 61;
  LBARRIER();
#define T_SRC(sg, i) (WS_OX + (unsigned)((sg) * NH + ((h0 + (i)) & 31)) * 8192u + r * 1024)
#define T_MMA(i, k, f) do { const u32x8 w0 = wo[i][(k) & 1][0], w1 = wo[i][(k) & 1][1]; const int e = ((k) >> 1) * 4; \
    a0 = MFMA32(((u32x4){w0[e], w0[e + 1], w0[e + 2], w0[e + 3]}), f, a0); a1 = MFMA32(((u32x4){w1[e], w1[e + 1], w1[e + 2], w1[e + 3]}), f, a1); } while (0)
#define T_DMA(sg, i) do { _Pragma("unroll") for (int k = 0; k < 4; ++k) dmas(rs, ln * 16, T_SRC(sg, i) + k * 2048, lbase + j * 24576 + (i) * 4096 + k * 1024); } while (0)
  const int cb2 = 16 * ((ln >> 4) & 1) + 8 * (ln >> 5);
  const unsigned eo = (unsigned)((NSEG - 1 - j) * 32 + r * 16 + (ln & 15)) * 2048u + (unsigned)(c * 32 + cb2) * 2u;
  u32x4 gv, xv;
  if (VW && wid < NSEG - XS3) {
    xv = *(const u32x4*)((const char*)P.x + eo);
    cnt_wait(P, C_W4(15), 4, ln);
    gv = ld16(rs, WS_G + (unsigned)((NSEG - 1 - j) * 2 + r) * 32768u + c * 1024 + blk_in(ln & 15, cb2));
  }
  bool ahead = false;
  f32x4 k0, k1;
  #pragma unroll 1
  for (int sg = XS3; sg < NSEG; ++sg) {
    f32x4 a0 = zero4(), a1 = zero4();
    if (VW) {
      if (wid == 3) {
        for (int w = __builtin_amdgcn_readfirstlane(*rdy); w <= sg;) {
          w = max(w + 1, -wmax(-(int)flag_wait<TSL>(P, F_PRG(4, ln & 31), w + 1, ln)));
          if (ln == 0) *rdy = w;
        }
      } else while (__builtin_amdgcn_readfirstlane(*rdy) <= sg) __builtin_amdgcn_s_sleep(1);
      u32x4 of[8];
      #pragma unroll
      for (int i = 0; i < 8; ++i) of[i] = __builtin_amdgcn_raw_buffer_load_b128(rs, ln * 16 + (i & 3) * 2048, T_SRC(sg, i >> 2), AUX_SC1);
      #pragma unroll
      for (int i = 0; i < 8; ++i) T_MMA(i >> 2, i & 3, of[i]);
    } else {
      if (!ahead) {
        while (__builtin_amdgcn_readfirstlane(*rdy) <= sg) __builtin_amdgcn_s_sleep(1);
        #pragma unroll
        for (int i = 0; i < NHD; ++i) T_DMA(sg, i);
      }
      VMW(20);
      ahead = __builtin_amdgcn_readfirstlane(*rdy) > sg + 1;
      u32x4 of[2][4];
#define T_RD(i) do { _Pragma("unroll") for (int k = 0; k < 4; ++k) of[(i) & 1][k] = *(const LAS u32x4*)(lds + j * 24576 + (i) * 4096 + k * 1024 + ln * 16); } while (0)
      T_RD(0);
      #pragma unroll
      for (int i = 0; i < NHD; ++i) {
        asm volatile("s_waitcnt lgkmcnt(0)" : "+v"(of[i & 1][0]), "+v"(of[i & 1][1]), "+v"(of[i & 1][2]), "+v"(of[i & 1][3]) :: "memory");
        if (i + 1 < NHD) {
          if (ahead) VMW(16); else G_WAIT_V(16 - 4 * (i < 4 ? i : 4));
          T_RD(i + 1);
        }
        #pragma unroll
        for (int k = 0; k < 4; ++k) {
          if (ahead) dmas(rs, ln * 16, T_SRC(sg + 1, i) + k * 2048, lbase + j * 24576 + i * 4096 + k * 1024);
          T_MMA(i, k, of[i & 1][k]);
          __builtin_amdgcn_sched_barrier(0);
        }
      }
    }
    LAS f32x4* pw = (LAS f32x4*)(lds + 98304 + ((sg & 3) * 8 + wid) * 2048) + ln;
    if (wid == 0 && sg == NSEG - 1) { k0 = a0; k1 = a1; } else { pw[0] = a0; pw[64] = a1; }
  }
  if (VW && wid < NSEG - XS3) {
    LBARRIER();
    const LAS f32x4* pr = (const LAS f32x4*)(lds + 98304 + (3 - j) * 16384) + ln;
    f32x4 t[2];
    #pragma unroll
    for (int k = 0; k < 2; ++k)
      t[k] = (((wid ? pr[k * 64] : k ? k1 : k0) + pr[128 + k * 64]) + (pr[256 + k * 64] + pr[384 + k * 64])) + ((pr[512 + k * 64] + pr[640 + k * 64]) + (pr[768 + k * 64] + pr[896 + k * 64]));
    fin_out(P, eo, epi_pack(t[0], t[1]), gv, xv);
  } else LBARRIER();
}

#define DMA_S dman
#define XDL 4
#define XPC 0
#define PW3 11
#define PW4 25
#define SDL 0
#define LS_P 131072
template <int MODE>
__device__ __forceinline__ void s_role(LAS unsigned char* lds, Ptrs& P, const u16* W, int h, unsigned obase, int nseg, int fl, int wid, int s0) {
  constexpr bool XM = MODE >= 2;
  constexpr unsigned RS = XM ? DM * 2 : HD * 2, SS = XM ? 262144u : 65536u;
  const int lane = opq(0);
  const int kh = wid >> 2, tp = wid & 3;
  const rsrc_t rs = WS_RSRC(P);
  const rsrc_t rw = __builtin_amdgcn_make_buffer_rsrc((void*)W, 0, HD * DM * 2, RSRC_W3);
  const unsigned lbase = (unsigned)(size_t)lds;
  u32x8 w[8][2];
  unsigned f0 = 0;
  if (!XM) for (int i = 0; i < SDL; ++i) __builtin_amdgcn_s_sleep(32);
  if (XM && XDL) {
    if (wid == 0) flag_wait(P, F_PRG(4, h), max(s0 - XDL, 0), lane);
    BARRIER();
  }
  {
    const int g = lane >> 4, q4 = (lane & 15) >> 2, p4 = lane & 3;
    const int trow = XM ? 16 * g + 4 * (q4 >> 1) + (q4 & 1) : 8 * g + q4;
#define S_SW(row) ((((row) & 1) | ((((row) >> (XM ? 2 : 1)) & 1) << 1) | ((((row) >> (XM ? 4 : 3)) & 1) << 2)) << 1)
    const int toff = trow * 256 + (((4 * tp + (p4 >> 1)) ^ S_SW(trow)) << 4) + 8 * (p4 & 1);
    unsigned dv[2];
    #pragma unroll
    for (int x = 0; x < 2; ++x) {
      const int row = 8 * wid + 4 * x + (lane >> 4);
      dv[x] = (unsigned)row * RS + (XM ? (h >> 3) * 1024 * RS + (h & 7) * 256 : h * 256) + (((lane & 15) ^ S_SW(row)) << 4);
    }
#define S_IMG(i) do { DMA_S(rw, dv[0], (i) * 64 * RS, lbase + ((i) & 7) * 16384 + wid * 2048); \
                      DMA_S(rw, dv[1], (i) * 64 * RS, lbase + ((i) & 7) * 16384 + wid * 2048 + 1024); if (XM && XPC) __builtin_amdgcn_s_sleep(XPC); } while (0)
    #pragma unroll
    for (int i = 0; i < 8; ++i) S_IMG(i);
    #pragma unroll
    for (int b = 0; b < 4; ++b) {
      if (b == 3 && !XM) { f0 = ld4(rs, FLG_OFF + F_LN0(lane & 7) * 128 + 4); get_tag(P); }
      if (b < 3) VMW(8); else VMWAIT();
      BARRIER();
      if (kh == (b >> 1)) {
        #pragma unroll
        for (int i = 4 * (b & 1); i < 4 * (b & 1) + 4; ++i) {
          w[i][0] = rd_frag<XM ? 2048 : 8192, XM ? 512 : 1024>(lds + toff + i * 16384);
          w[i][1] = rd_frag<XM ? 2048 : 8192, XM ? 512 : 1024>(lds + (toff ^ 32) + i * 16384);
        }
      }
      LBARRIER();
      if (b < 2) {
        #pragma unroll
        for (int i = 4 * b + 8; i < 4 * b + 12; ++i) S_IMG(i);
      }
    }
  }
  TL(XM ? 74 : 72);
  const unsigned l16 = lane * 16;
  volatile LAS int* rd = (volatile LAS int*)(lds + LS_P + 20480);
#define X_READY(want) do { \
    if (wid == 0) { \
      const int n_ = -wmax(-(int)flag_wait(P, F_PRG(4, (h & 24) + (lane & 7)), s0 + (want), lane)) - s0; \
      if (lane == 0) *rd = max(n_, want); \
    } \
    LBARRIER(); } while (0)
  if (MODE == 2) X_READY(2);
  else if (!XM) {
    if (__any(f0 != P.tag)) flag_wait(P, F_LN0(lane & 7), 0, lane);
  }
  unsigned sx = (XM ? WS_OX + s0 * SS + (h >> 3) * 65536 : WS_XN) + kh * 32768 + tp * 4096;
  const unsigned sd = lbase + kh * 65536 + tp * 4096;
  const unsigned xl = kh * 65536 + (XM ? l16 : blk_in(lane & 15, (lane >> 4) * 8));
  const unsigned xp = lbase + xl, pa = lbase + LS_P + tp * 4096 + l16;
  LAS f32x4* const pq = (LAS f32x4*)(lds + LS_P + tp * 4096 + l16);
  if (MODE == 3) {
#define X_GET(t) do { _Pragma("unroll") for (int i = 0; i < 16; ++i) \
      dmas(rs, l16, sx + (t) * SS + ((i >> 2) & 1) * 32768 + (i >> 3) * 16384 + (i & 3) * 1024, sd + ((i >> 2) & 1) * 65536 + ((t) & 1) * 32768 + (i >> 3) * 16384 + (i & 3) * 1024); } while (0)
    bool have = false;
    const unsigned pv = FLG_OFF + F_PRG(kh ? 7 : 4, kh ? h : (h & 24) + (lane & 7)) * 128;
    if (!kh) __builtin_amdgcn_s_setprio(1);
    #pragma unroll 1
    for (int t = 0; t < nseg; ++t) {
      if (!have && !kh) {
        flag_wait<2>(P, F_PRG(4, (h & 24) + (lane & 7)), s0 + t + 1, lane);
        X_GET(t);
        VMW(8);
      }
      BARRIER();
      f32x4 a[4];
      unsigned pg;
      ONE_X(xp + (t & 1) * 32768);
      if (wid == 5) flag_set(P, fl, t, lane);
      have = t + 1 < nseg && !__any(pg < (unsigned)(s0 + t + 2));
      if (!kh && lane == 0) rd[tp] = have;
      pq[kh * 128] = kh ? a[0] : a[2]; pq[kh * 128 + 64] = kh ? a[1] : a[3];
      if (have && !kh) X_GET(t + 1);
      LBARRIER();
      const unsigned oo = obase + (unsigned)(s0 + t) * 0x80000u + (unsigned)h * 16384u + tp * 4096 + kh * 2048 + l16;
      st16(rs, oo, __builtin_bit_cast(u32x4, (kh ? a[2] : a[0]) + pq[128 - kh * 128]));
      st16(rs, oo + 1024, __builtin_bit_cast(u32x4, (kh ? a[3] : a[1]) + pq[192 - kh * 128]));
      if (__any(rd[lane & 3] == 0)) {
        VMWAIT();
        BARRIER();
        if (wid == 5) flag_set(P, fl, t + 1, lane);
      } else if (!kh) VMW(10);
      asm volatile("s_waitcnt lgkmcnt(0)" ::: "memory");
      TL(t);
    }
    __builtin_amdgcn_s_setprio(0);
    TL(65);
    return;
  }
  if (XM) {
    #pragma unroll
    for (int i = 0; i < 12; ++i) dmas(rs, l16, sx + (i >> 3) * SS + ((i >> 2) & 1) * 16384 + (i & 3) * 1024, sd + (i >> 2) * 16384 + (i & 3) * 1024);
    VMWAIT();
    BARRIER();
  } else {
    #pragma unroll
    for (int i = 0; i < 16; ++i) dmas(rs, l16, sx + (i >> 3) * SS + ((i >> 2) & 1) * 16384 + (i & 3) * 1024, sd + ((i >> 2) ^ 2) * 16384 + (i & 3) * 1024);
    VMW(12);
    BARRIER();
    f32x4 a[4];
    if (MODE) ONE_V(xp + 32768); else ONE_K(xp + 32768);
    pq[kh * 128] = kh ? a[0] : a[MODE ? 1 : 2]; pq[kh * 128 + 64] = kh ? a[MODE ? 2 : 1] : a[3];
    LBARRIER();
    const f32x4 a0 = (kh ? a[MODE ? 1 : 2] : a[0]) + pq[128 - kh * 128], a1 = (kh ? a[3] : a[MODE ? 2 : 1]) + pq[192 - kh * 128];
    if (MODE) st16(rs, obase + h * 8192 + (2 * tp + kh) * 1024 + l16, (u32x4){pack2(a0[0], a0[1]), pack2(a0[2], a0[3]), pack2(a1[0], a1[1]), pack2(a1[2], a1[3])});
    else st16(rs, obase + h * 8192 + kh * 4096 + tp * 1024 + l16, (u32x4){pack2(a0[0], a1[0]), pack2(a0[1], a1[1]), pack2(a0[2], a1[2]), pack2(a0[3], a1[3])});
    sx += SS;
    #pragma unroll
    for (int i = 0; i < 4; ++i) dmas(rs, l16, sx + SS + i * 1024, sd + 32768 + i * 1024);
    VMW(4);
    BARRIER();
    if (wid == 1) flag_set(P, fl, 1, lane);
    TL(62);
  }
  u32x8 xf[3], ac[2] = {};
  #pragma unroll
  for (int i = 0; i < 3; ++i) {
    const u32x4 a = *(const LAS u32x4*)(lds + xl + i * 2048), b = *(const LAS u32x4*)(lds + xl + i * 2048 + 1024);
    xf[i] = (u32x8){a[0], a[1], a[2], a[3], b[0], b[1], b[2], b[3]};
  }
  unsigned long long fm, pm;
  asm volatile("s_cmp_eq_u32 %2, 1\n\ts_cselect_b64 %0, 1, 0\n\ts_cmp_eq_u32 %3, 1\n\ts_cselect_b64 %1, -1, 0" : "=&s"(fm), "=&s"(pm) : "s"(wid), "s"(XM ? 0 : wid >> 1) : "scc");
  const unsigned fo = FLG_OFF + fl * 128, pd = lbase + LS_P + 16384;
  const unsigned po = 4 * 65536 + h * 2048 + (wid & 1) * 1024 - tp * 4096 - kh * 32768;
  unsigned oo = XM ? obase + (unsigned)h * 16384u + tp * 4096 + (s0 - 1) * 0x80000u : obase + (unsigned)h * 8192u + tp * (MODE ? 2048 : 1024), tmp, tmp2;
  unsigned long long em;
  int t = XM ? 0 : 1;
  if (MODE < 2) {
    #pragma unroll 1
    for (int part = 0; part < 4; ++part) {
      if (part == 1) { TL(63); if (wid == 0) setup_wait(P, lane); }
      if (MODE == 1 && part >= 2) {
        #pragma unroll
        for (int i = 0; i < 4; ++i) {
          const unsigned to = ((i * 32 + h) * 512 + wid * 64 + opq(0)) * 128;
          if (part == 2) touch(P.w3, DM * FF * 2, to, lds); else touch(P.w4, DM * FF * 2, to, lds);
        }
      }
      const int te = part == 0 ? 3 : part == 1 ? PW3 : part == 2 ? PW4 : nseg + 1;
      if (MODE) S_LOOP_V(te, 0); else S_LOOP_K(te, 0);
    }
  } else {
    #pragma unroll 1
    while (t < nseg) {
      X_READY(min(t + 4, nseg));
      const int n = __builtin_amdgcn_readfirstlane(*rd), te = n >= nseg ? nseg : (n - 2) & ~1;
      TL(t);
      S_LOOP_X(te, nseg);
    }
  }
  TL(XM ? 65 : 64);
  VMWAIT();
  BARRIER();
  if (wid == 1) flag_set(P, fl, nseg, lane);
}

#define LND 1
#define CALN 6
__device__ __forceinline__ void setup_role(LAS unsigned char* lds, Ptrs& P, int cu, int wid) {
  const int lane = opq(0), tid = wid * 64 + lane;
  const rsrc_t rs = WS_RSRC(P);
  u32x4 v[8];
  const u32x4* px = (const u32x4*)(P.x + (size_t)(cu * 32 + wid * 4 + (lane >> 4)) * DM) + (lane & 15);
  #pragma unroll
  for (int i = 0; i < 8; ++i) v[i] = __builtin_nontemporal_load(px + i * 16);
  const unsigned epo = __hip_atomic_load((gu32*)(P.ws + EPO_OFF), RLX_AGENT);
  const unsigned cb = comm_base(P.ws);
  const u32x4 z = {0u, 0u, 0u, 0u};
  const int cr = 63 - cu;
  if (wid == 0 && cr < 16) st16(rs, CNT_OFF + cr * 1024 + lane * 16, z);
  if (cr < 4) st16(rs, cb + FL_B(0) + cr * 8192 + tid * 16, z);
  if (cr == 4 && tid < 256) {
    const int g4 = tid >> 6, k = (tid >> 5) & 1 & (g4 >> 1);
    st16(rs, WS_H1 + ((g4 & 1) ? 127u * 32768u : 0u) + (g4 * 8 + ((tid >> 2) & 7)) * 1024 + blk_in((g4 & 1) ? 15 - k : k, (tid & 3) * 8), z);
  }
  #pragma unroll
  for (int i = 0; i < 2; ++i) {
    const int f = i * 54 + cr;
    if (cr < PD * NCHUNK) {
      u32x4 m = {~0u, ~0u, ~0u, ~0u};
      if (f < 4) {
        const int ks = (f >> 1) * 8 + wid;
        m = *(const u32x4*)(P.mem0 + ((lane >> 1) & 7) * DM + 64 * ks + 16 * (lane >> 4) + 8 * (lane & 1));
        m.x = canon1(m.x); m.y = canon1(m.y); m.z = canon1(m.z); m.w = canon1(m.w);
      }
      st16(rs, cb + (unsigned)(f / (NCHUNK * 2)) * CB_STEP + (f % (NCHUNK * 2)) * 8192u + tid * 16, m);
    }
  }
  const unsigned bi = blk_in((wid & 3) * 4 + (lane >> 4), (lane & 3) * 8);
  const int c0 = (lane >> 2) & 3;
  ln_rows(v);
  TL(69);
  if (cu >= 8) for (int i = 0; i < LND; ++i) __builtin_amdgcn_s_sleep(32);
  #pragma unroll
  for (int i = 0; i < 8; ++i) st16(rs, WS_XN + xn_blk(cu * 2 + (wid >> 2), 4 * i + c0) + bi, v[i]);
  P.tag = __builtin_amdgcn_readfirstlane(epo) + 1u;
  ln_rows(v);
  if (cu < 8) {
    VMWAIT();
    BARRIER();
    if (wid == 0) flag_set(P, F_LN0(cu), 1, lane);
    TL(66);
  }
  asm volatile("" ::: "memory");
  #pragma unroll
  for (int i = 0; i < 8; ++i) st16(rs, WS_XN2 + (unsigned)(cu * 2 + (wid >> 2)) * 32768u + (4 * i + c0) * 1024 + bi, v[i]);
  touch(P.w1, DM * DM * 2, ((cu & 31) * 512 + tid) * 128, lds);
  VMW(9);
  BARRIER();
  if (wid == 0) flag_set(P, F_LN0(cu), 1, lane);
  TL(70);
  if (wid == 0) VMW(2); else VMW(1);
  BARRIER();
  if (wid == 0) flag_set(P, F_LN1(cu), 1, lane);
  TL(71);
  if (cu < 32 && (wid >> 1) == 1) {
    const int id = cu * 2 + (wid & 1);
    unsigned best[2] = {65535u, 65535u};
    #pragma unroll
    for (int it = 0; it < CALN; ++it) {
      const unsigned long long c0 = __builtin_amdgcn_s_memtime();
      asm volatile("s_waitcnt lgkmcnt(0)" ::: "memory");
      if (lane == 0) st4(rs, cb + (id & 3) * CB_STEP + (30 + ((id >> 2) & 1)) * 16384u + (it & 1) * 8192u + (id >> 3) * 1024u, 0u);
      VMWAIT();
      const unsigned d = (unsigned)(__builtin_amdgcn_s_memtime() - c0);
      if (it >= 2) best[it & 1] = min(best[it & 1], d);
    }
    flag_set(P, F_CAL(id), (best[0] << 16) | best[1], lane);
  }
}

#define TLBP 0
#define CPF 1
__global__ __launch_bounds__(512, 2) void mega_kernel(const u16* x, const u16* mem0, const u16* Wq, const u16* Wk, const u16* Wv, const u16* Wo,
                                                     const u16* w1, const u16* w2, const u16* w3, const u16* w4, u16* out, char* ws) {
  __shared__ __attribute__((aligned(16))) unsigned char smem[163840];
  LAS unsigned char* lds = (LAS unsigned char*)smem;
  Ptrs P;
  P.x = x; P.mem0 = mem0; P.Wq = Wq; P.Wk = Wk; P.Wv = Wv; P.Wo = Wo; P.w1 = w1; P.w2 = w2; P.w3 = w3; P.w4 = w4; P.out = out; P.ws = ws;
  const int xcd = blockIdx.x & 7, slot = blockIdx.x >> 3;
  const int wid = __builtin_amdgcn_readfirstlane(threadIdx.x >> 6);
  TL(79);
  P.tag = 0;
  if (wid == 7) {
    unsigned long long pc;
    unsigned len;
    asm volatile("s_getpc_b64 %0\nMK_BEG:\n\ts_mov_b32 %1, MK_END-MK_BEG" : "=s"(pc), "=s"(len));
    touch((const void*)pc, len, (slot * 64 + opq(0)) * 128, lds);
  }
  const int na = xcd == 2 ? 8 : 12;
  const bool isa = xcd < 3 && slot < na;
  const int ah = xcd == 2 ? slot * 4 + 2 : xcd * 16 + (slot & 3) * 4 + (slot >> 2) + (slot >> 3), bs = slot - na, br = bs & 1, bc = (xcd == 1 ? 22 : xcd * 5) + (bs >> 1);
  if (isa) {
    if (wid < 4) a_wave<true>(lds, P, ah, wid); else a_wave<false>(lds, P, ah, wid);
  } else if (xcd < 3) {
    if (wid < 4) b_wave<true>(lds, P, br, bc, wid, bs, xcd); else b_wave<false>(lds, P, br, bc, wid, bs, xcd);
  } else {
    if ((xcd & 3) == 3) {
      setup_role(lds, P, (xcd >> 2) * 32 + slot, wid);
    } else if (xcd == 6) s_role<1>(lds, P, Wv, slot, WS_VF, NSEG, F_PRG(2, slot), wid, 0);
    else s_role<0>(lds, P, xcd == 4 ? Wk : Wq, slot, xcd == 4 ? WS_KF : WS_QF, NSEG, F_PRG(xcd - 4, slot), wid, 0);
    if ((xcd & 3) == 3) mlp_role(lds, P, xcd >> 2, slot, wid);
    else if (xcd == 6) s_role<3>(lds, P, Wo, slot, WS_PT, XS3 - XS2, F_PRG(7, slot), wid, XS2);
    else s_role<2>(lds, P, Wo, slot, WS_PT, xcd == 4 ? XS1 : XS2 - XS1, F_PRG(xcd + 1, slot), wid, xcd == 4 ? 0 : XS1);
    report(P, wid);
    fin_role(lds, P, wid);
    if (blockIdx.x == 3 && wid == 0) {
      cnt_wait(P, C_ARR, 256, threadIdx.x);
      if (threadIdx.x == 0) __hip_atomic_store((gu32*)(ws + EPO_OFF), P.tag, RLX_AGENT);
    }
  }
  asm volatile("MK_END:");
  TL(78);
}

extern "C" void kernel_launch(void* const* d_in, const int* in_sizes, int n_in, void* d_out, int out_size,
                              void* d_ws, size_t ws_size, hipStream_t stream) {
  mega_kernel<<<256, 512, 0, stream>>>((const u16*)d_in[0], (const u16*)d_in[1], (const u16*)d_in[2], (const u16*)d_in[3], (const u16*)d_in[4],
                                       (const u16*)d_in[5], (const u16*)d_in[8], (const u16*)d_in[9], (const u16*)d_in[10], (const u16*)d_in[11],
                                       (u16*)d_out, (char*)d_ws);
}
```

```cpp
#include <hip/hip_runtime.h>
#include <hip/hip_bf16.h>
#include <stdint.h>

#define SEQ 2048
#define DM 1024
#define NH 32
#define DH 128
#define HD (NH * DH)
#define MM 8
#define SEG 32
#define NSEG (SEQ / SEG)
#define FF 3584

typedef unsigned short u16;
typedef __attribute__((ext_vector_type(8))) short bf16x8;
typedef __attribute__((ext_vector_type(4))) short bf16x4;
typedef __attribute__((ext_vector_type(4))) float f32x4;
typedef __attribute__((ext_vector_type(4))) unsigned u32x4;
typedef __attribute__((ext_vector_type(2))) unsigned u32x2;
typedef __attribute__((ext_vector_type(2))) float f32x2;
typedef __attribute__((ext_vector_type(2))) __bf16 bf16v2;
typedef __attribute__((ext_vector_type(8))) unsigned u32x8;
#define LAS __attribute__((address_space(3)))
#define GAS __attribute__((address_space(1)))

__device__ __forceinline__ unsigned pack2(float lo, float hi) {
  return __builtin_bit_cast(unsigned, __builtin_convertvector((f32x2){lo, hi}, bf16v2));
}
__device__ __forceinline__ float rb1(float x) { return __uint_as_float(pack2(0.f, x)); }

typedef GAS unsigned gu32;
#define RLX_AGENT __ATOMIC_RELAXED, __HIP_MEMORY_SCOPE_AGENT
#define SPIN_CAP (1u << 20)
#define AUX_SC1 16
#define RSRC_W3 0x00020000
typedef __amdgpu_buffer_rsrc_t rsrc_t;

__device__ __forceinline__ u32x4 ld16(rsrc_t rs, unsigned off) { return __builtin_amdgcn_raw_buffer_load_b128(rs, off, 0, AUX_SC1); }
__device__ __forceinline__ u32x2 ld8(rsrc_t rs, unsigned off) { return __builtin_amdgcn_raw_buffer_load_b64(rs, off, 0, AUX_SC1); }
__device__ __forceinline__ unsigned ld4(rsrc_t rs, unsigned off) { return __builtin_amdgcn_raw_buffer_load_b32(rs, off, 0, AUX_SC1); }
__device__ __forceinline__ void st16(rsrc_t rs, unsigned off, u32x4 v) { __builtin_amdgcn_raw_buffer_store_b128(v, rs, off, 0, AUX_SC1); }
__device__ __forceinline__ void st8(rsrc_t rs, unsigned off, u32x2 v) { __builtin_amdgcn_raw_buffer_store_b64(v, rs, off, 0, AUX_SC1); }
__device__ __forceinline__ void st4(rsrc_t rs, unsigned off, unsigned v) { __builtin_amdgcn_raw_buffer_store_b32(v, rs, off, 0, AUX_SC1); }
__device__ __forceinline__ float lo16(unsigned w) { return __uint_as_float(w << 16); }
__device__ __forceinline__ float hi16(unsigned w) { return __uint_as_float(w & 0xffff0000u); }

__device__ __forceinline__ float xsum16_32(float v) {
  unsigned u = __float_as_uint(v);
  auto a = __builtin_amdgcn_permlane16_swap(u, u, false, false);
  v = __uint_as_float(a[0]) + __uint_as_float(a[1]);
  u = __float_as_uint(v);
  auto b = __builtin_amdgcn_permlane32_swap(u, u, false, false);
  return __uint_as_float(b[0]) + __uint_as_float(b[1]);
}

__device__ __forceinline__ unsigned canon1(unsigned w) { return min(w, 0xfffffffeu); }
__device__ __forceinline__ unsigned max4(u32x4 v) { return max(max(v.x, v.y), max(v.z, v.w)); }
__device__ __forceinline__ int opq(int) { int v; asm volatile("v_mbcnt_lo_u32_b32 %0, -1, 0\n\tv_mbcnt_hi_u32_b32 %0, -1, %0" : "=v"(v)); return v; }
__device__ __forceinline__ u32x4 poison4() { unsigned m = ~0u; asm volatile("" : "+v"(m)); return (u32x4){m, m, m, m}; }
__device__ __forceinline__ f32x4 zero4() { float m = 0.f; asm volatile("" : "+v"(m)); return (f32x4){m, m, m, m}; }
#define MFMA32(a, b, c) __builtin_amdgcn_mfma_f32_16x16x32_bf16(__builtin_bit_cast(bf16x8, a), __builtin_bit_cast(bf16x8, b), c, 0, 0, 0)
#define MFMA16(a, b, c) __builtin_amdgcn_mfma_f32_16x16x16bf16_1k(__builtin_bit_cast(bf16x4, a), __builtin_bit_cast(bf16x4, b), c, 0, 0, 0)
#define LBARRIER() do { asm volatile("s_waitcnt lgkmcnt(0)" ::: "memory"); __builtin_amdgcn_s_barrier(); asm volatile("" ::: "memory"); } while (0)
#define BARRIER() do { asm volatile("" ::: "memory"); __builtin_amdgcn_s_barrier(); asm volatile("" ::: "memory"); } while (0)
#define VMWAIT() asm volatile("s_waitcnt vmcnt(0)" ::: "memory")
#define VMW(n) asm volatile("s_waitcnt vmcnt(" #n ")" ::: "memory")
#define DELAY(n) do { if ((n) > 0) __builtin_amdgcn_s_sleep(n); } while (0)

#define EPO_OFF 0
#define TMO_OFF 128
#define FLG_OFF 256
#define F_LN0(i) (i)
#define F_LN1(i) (64 + (i))
#define F_CAL(i) (128 + (i))
#define F_PRG(t, h) (192 + (t) * 32 + (h))
#define CNT_OFF 65536
#define C_W1(p2) (p2)
#define C_W2(p2) (16 + (p2))
#define C_LN(p2) (32 + (p2))
#define C_W3(p2) (48 + (p2))
#define C_W4(p2) (64 + (p2))
#define C_HEAD 80
#define C_ARR 81
#define NCNT 128
#define WS_XN (1u << 20)
#define WS_XN2 (WS_XN + (4u << 20))
#define WS_QF (WS_XN2 + (4u << 20))
#define WS_KF (WS_QF + (16u << 20))
#define WS_VF (WS_KF + (16u << 20))
#define WS_OX (WS_VF + (16u << 20))
#define WS_H1 (WS_OX + (16u << 20) + (1u << 20))
#define WS_X1 (WS_H1 + (4u << 20) + 65536u)
#define WS_X1N (WS_X1 + (4u << 20))
#define WS_H3 (WS_X1N + (4u << 20))
#define WS_G (WS_H3 + (14u << 20))
#define WS_PT (WS_G + (4u << 20))
#define WS_COMM (WS_PT + (32u << 20))
#define WS_END (WS_COMM + (4u << 20))
#define CB_STEP 524288u
#define PD 3
__device__ __forceinline__ unsigned comm_base(const char* ws) { return WS_COMM + ((0u - ((unsigned)(size_t)ws + WS_COMM)) & 0x1fffffu); }
__device__ __forceinline__ unsigned step_base(unsigned cb, unsigned mask, int s) { return cb + (unsigned)(s & 3) * CB_STEP + mask; }
#define MF_AT(ks) (((ks) >> 3) * 16384u + ((ks) & 7) * 1024u)
#define OB_AT(r, h) ((2u + (r) * 4u + ((h) >> 3)) * 16384u + ((h) & 7) * 1024u)
#define VB_AT(h) ((10u + ((h) >> 2)) * 16384u + ((h) & 3) * 2048u)
#define XS1 22
#define XS2 42
#define XS3 62
#define CSL 8
#define FSL 6
#define SQUICK 3
#define NCHUNK 18
#define FL_O(h) (29u * 16384u + (h) * 128u)
#define FL_B(b) (28u * 16384u + (b) * 128u)
#define FL_T(s) FL_O(32 + ((s) & 3))

struct Ptrs { const u16 *x, *mem0, *Wq, *Wk, *Wv, *Wo, *w1, *w2, *w3, *w4; u16* out; char* ws; unsigned tag; };
#define WS_RSRC(P) __builtin_amdgcn_make_buffer_rsrc((void*)(P).ws, 0, WS_END, RSRC_W3)

__device__ __forceinline__ bool spin_fail(unsigned& spins, const Ptrs& P, int lane) {
  asm volatile("" ::: "memory");
  if (++spins > SPIN_CAP) {
    unsigned tv;
    asm volatile("v_mov_b32 %0, %1" : "=v"(tv) : "s"(P.tag));
    if (lane == 0) __builtin_amdgcn_raw_buffer_store_b32(tv, WS_RSRC(P), 0, TMO_OFF, AUX_SC1);
    return true;
  }
  return (spins & 255u) == 0u && (unsigned)__builtin_amdgcn_readfirstlane(__builtin_amdgcn_raw_buffer_load_b32(WS_RSRC(P), 0, TMO_OFF, AUX_SC1)) == P.tag;
}
__device__ __forceinline__ unsigned cnt_load(char* ws, int idx) {
  return __builtin_amdgcn_readfirstlane(__hip_atomic_load((gu32*)(ws + CNT_OFF + idx * 128), RLX_AGENT));
}
__device__ __forceinline__ void cnt_add(char* ws, int idx, int lane) {
  asm volatile("s_waitcnt vmcnt(0)" ::: "memory");
  if (lane == 0) __hip_atomic_fetch_add((gu32*)(ws + CNT_OFF + idx * 128), 1u, RLX_AGENT);
}
__device__ __forceinline__ void cnt_wait(const Ptrs& P, int idx, unsigned want, int lane) {
  for (unsigned spins = 0;;) {
    if (cnt_load(P.ws, idx) >= want) break;
    __builtin_amdgcn_s_sleep(CSL);
    if (spin_fail(spins, P, lane)) break;
  }
}
template <int SL = FSL>
__device__ __forceinline__ unsigned flag_wait(const Ptrs& P, int f, unsigned want, int lane) {
  const rsrc_t rs = WS_RSRC(P);
  u32x2 v;
  for (unsigned spins = 0;;) {
    v = ld8(rs, FLG_OFF + f * 128);
    if (!__any(v.y != P.tag || v.x < want)) break;
    __builtin_amdgcn_s_sleep(SL);
    if (spin_fail(spins, P, lane)) break;
  }
  asm volatile("" ::: "memory");
  return v.x;
}
__device__ __forceinline__ void flag_set(const Ptrs& P, int f, unsigned val, int lane) {
  if (lane == 0) st8(WS_RSRC(P), FLG_OFF + f * 128, (u32x2){val, P.tag});
}
__device__ __forceinline__ void get_tag(Ptrs& P) { P.tag = __builtin_amdgcn_readfirstlane(__hip_atomic_load((gu32*)(P.ws + EPO_OFF), RLX_AGENT)) + 1u; }
__device__ __forceinline__ void setup_wait(const Ptrs& P, int lane) { flag_wait(P, F_LN0(lane), 0, lane); }
__device__ __forceinline__ void clock_wait(unsigned tgt) {
  while ((int)(tgt - (unsigned)__builtin_amdgcn_s_memrealtime()) > 0) {}
}
__device__ __forceinline__ void report(const Ptrs& P, int wid) {
  if (wid == 0 && opq(0) == 0) __hip_atomic_fetch_add((gu32*)(P.ws + CNT_OFF + C_ARR * 128), 1u, RLX_AGENT);
}
__device__ __forceinline__ void kv_wait(const Ptrs& P, int s, int h, int& known, int lane) {
  if (s < known) return;
  const unsigned v = flag_wait(P, F_PRG(min(lane, 2), h), s + 1, lane);
  known = __builtin_amdgcn_readfirstlane(min(min(__builtin_amdgcn_readlane(v, 0), __builtin_amdgcn_readlane(v, 1)), __builtin_amdgcn_readlane(v, 2)));
}
__device__ __forceinline__ int wmax(int v) {
  v = max(v, __builtin_amdgcn_update_dpp(0, v, 0x128, 0xf, 0xf, false));
  v = max(v, __builtin_amdgcn_update_dpp(0, v, 0x124, 0xf, 0xf, false));
  v = max(v, __builtin_amdgcn_update_dpp(0, v, 0x122, 0xf, 0xf, false));
  v = max(v, __builtin_amdgcn_update_dpp(0, v, 0x121, 0xf, 0xf, false));
  return max(max(__builtin_amdgcn_readlane(v, 0), __builtin_amdgcn_readlane(v, 16)), max(__builtin_amdgcn_readlane(v, 32), __builtin_amdgcn_readlane(v, 48)));
}
__device__ __forceinline__ unsigned near_half(unsigned v) { return wmax(-(int)(v & 65535u)) > wmax(-(int)(v >> 16)) ? 8192u : 0u; }
__device__ __forceinline__ void dmaw(rsrc_t rs, unsigned voff, unsigned soff, unsigned ldsaddr) {
  asm volatile("s_mov_b32 m0, %3\n\ts_nop 0\n\tbuffer_load_dwordx4 %0, %1, %2 offen lds" :: "v"(voff), "s"(rs), "s"(soff), "s"(ldsaddr) : "memory");
}
__device__ __forceinline__ void dman(rsrc_t rs, unsigned voff, unsigned soff, unsigned ldsaddr) {
  asm volatile("s_mov_b32 m0, %3\n\ts_nop 0\n\tbuffer_load_dwordx4 %0, %1, %2 offen nt lds" :: "v"(voff), "s"(rs), "s"(soff), "s"(ldsaddr) : "memory");
}
__device__ __forceinline__ void dmas(rsrc_t rs, unsigned voff, unsigned soff, unsigned ldsaddr) {
  asm volatile("s_mov_b32 m0, %3\n\ts_nop 0\n\tbuffer_load_dwordx4 %0, %1, %2 offen sc1 lds" :: "v"(voff), "s"(rs), "s"(soff), "s"(ldsaddr) : "memory");
}
__device__ __forceinline__ void touch(const void* w, unsigned bytes, unsigned voff, LAS unsigned char* lds) {
  asm volatile("s_mov_b32 m0, %2\n\ts_nop 0\n\tbuffer_load_dword %0, %1, 0 offen lds"
               :: "v"(voff), "s"(__builtin_amdgcn_make_buffer_rsrc((void*)w, 0, bytes, RSRC_W3)), "s"((unsigned)(size_t)lds + 148480) : "memory");
}
__device__ __forceinline__ u32x2 trd(LAS unsigned char* p) {
  return __builtin_bit_cast(u32x2, __builtin_amdgcn_ds_read_tr16_b64_v4i16((LAS bf16x4*)p));
}
template <int BO, int UO>
__device__ __forceinline__ u32x8 rd_frag(LAS unsigned char* p) {
  const u32x2 a = trd(p), b = trd(p + UO), c = trd(p + BO), d = trd(p + BO + UO);
  return (u32x8){a.x, a.y, b.x, b.y, c.x, c.y, d.x, d.y};
}

#define TL(i) do {} while (0)
#define TLW(i) do {} while (0)
#define GSDECL
#define GS(i) do {} while (0)
#define GSFLUSH(k, i) do {} while (0)

#define QPRIO 0
#define VPOS 3
#define VLDI "buffer_load_dword v254, v17, %[rs], %[sb] offen sc1\n\tbuffer_load_dword v255, v17, %[rs], %[sb] offen offset:256 sc1\n\t"
#define VLD(p) VLD##p
#define VLD0
#define VLD1
#define VLD2
#define VLD3 VLDI
#define VLD4
#define DEC1 1
#define DEC2 1
#define DGA
#define DGB
#define DGC
#define DGE
#define DGO
#define DGP
#define DGQ
#define PACE "s_sleep 1\n\t"
#define STR_(x) #x
#define STR(x) STR_(x)
#define GSW
#define GSA(k)
#define GSO(k)
#define A_MAIN_Q() \
  asm volatile( \
    "v_mov_b32 v253, %[tag]\n\t" \
    "buffer_load_dwordx4 v[20:23], v12, %[rs], %[vx] offen sc1\n\t" \
    "buffer_load_dwordx4 v[24:27], v12, %[rs], %[vx] offen offset:1024 sc1\n\t" \
    "ds_read_b128 v[236:239], v9 offset:0\n\t" \
    "ds_read_b128 v[240:243], v9 offset:1024\n\t" \
    "ds_read_b128 v[244:247], v9 offset:2048\n\t" \
    "ds_read_b128 v[248:251], v9 offset:3072\n\t" \
    "v_mov_b32 v0, 0\n\t" \
    "v_mov_b32 v1, 0\n\t" \
    "v_mov_b32 v2, 0\n\t" \
    "v_mov_b32 v3, 0\n\t" \
    "v_mov_b32 v4, 0\n\t" \
    "v_mov_b32 v5, 0\n\t" \
    "v_mov_b32 v6, 0\n\t" \
    "v_mov_b32 v7, 0\n\t" \
    "s_cmp_lg_u32 %[sc], 3\n\t" \
    "s_cbranch_scc1 PB_%=\n\t" \
    "s_getpc_b64 s[92:93]\n\t" \
    "TPA_%=:\n\t" \
    "s_add_u32 s92, s92, TEA_%=-TPA_%=\n\t" \
    "s_addc_u32 s93, s93, 0\n\t" \
    "s_lshr_b32 s91, %[tgt], 2\n\t" \
    "s_sub_u32 s91, s91, 3\n\t" \
    "TWA_%=:\n\t" \
    "s_memrealtime s[88:89]\n\t" \
    DGE \
    "s_waitcnt lgkmcnt(0)\n\t" \
    "s_sub_u32 s89, s91, s88\n\t" \
    "s_lshl_b32 s89, s89, 2\n\t" \
    "s_cmp_gt_i32 s89, 0\n\t" \
    "s_cbranch_scc1 TWA_%=\n\t" \
    "s_lshl_b32 s88, s88, 2\n\t" \
    "s_sub_u32 s89, %[tgt], s88\n\t" \
    "s_max_i32 s89, s89, 0\n\t" \
    "s_mul_i32 s89, s89, 11\n\t" \
    "s_lshr_b32 s89, s89, 1\n\t" \
    DGB \
    "s_and_b32 s89, s89, 0x7c\n\t" \
    "s_sub_u32 s92, s92, s89\n\t" \
    "s_subb_u32 s93, s93, 0\n\t" \
    "s_setpc_b64 s[92:93]\n\t" \
    "s_nop 0\n\t" \
    "s_nop 0\n\t" \
    "s_nop 0\n\t" \
    "s_nop 0\n\t" \
    "s_nop 0\n\t" \
    "s_nop 0\n\t" \
    "s_nop 0\n\t" \
    "s_nop 0\n\t" \
    "s_nop 0\n\t" \
    "s_nop 0\n\t" \
    "s_nop 0\n\t" \
    "s_nop 0\n\t" \
    "s_nop 0\n\t" \
    "s_nop 0\n\t" \
    "s_nop 0\n\t" \
    "s_nop 0\n\t" \
    "s_nop 0\n\t" \
    "s_nop 0\n\t" \
    "s_nop 0\n\t" \
    "s_nop 0\n\t" \
    "s_nop 0\n\t" \
    "s_nop 0\n\t" \
    "TEA_%=:\n\t" \
    "PB_%=:\n\t" \
    "s_waitcnt lgkmcnt(0)\n\t" \
    "s_barrier\n\t" \
    "buffer_load_dwordx4 v[220:223], v8, %[rs], %[m0] offen sc1\n\t" \
    "buffer_load_dwordx4 v[224:227], v8, %[rs], %[m1] offen sc1\n\t" \
    "PM_%=:\n\t" \
    "s_waitcnt vmcnt(1)\n\t" \
    "ds_write_b128 v8, v[220:223] offset:0\n\t" \
    "v_max3_u32 v252, v220, v221, v222\n\t" \
    "s_waitcnt vmcnt(0)\n\t" \
    "ds_write_b128 v8, v[224:227] offset:4096\n\t" \
    "v_max3_u32 v252, v252, v223, v224\n\t" \
    "v_max3_u32 v252, v252, v225, v226\n\t" \
    "v_max_u32 v252, v252, v227\n\t" \
    "v_cmp_eq_u32 vcc, -1, v252\n\t" \
    "s_cbranch_vccz PD_%=\n\t" \
    "s_sub_u32 %[lim], %[lim], 1\n\t" \
    "s_cmp_gt_i32 %[lim], 0\n\t" \
    "s_cbranch_scc0 PD_%=\n\t" \
    "s_sleep " STR(CADM) "\n\t" \
    "buffer_load_dwordx4 v[220:223], v8, %[rs], %[m0] offen sc1\n\t" \
    "buffer_load_dwordx4 v[224:227], v8, %[rs], %[m1] offen sc1\n\t" \
    "s_branch PM_%=\n\t" \
    "PD_%=:\n\t" \
    GSA(0) \
    "v_smfmac_f32_16x16x64_bf16 v[0:3], v[220:223], v[236:243], v10\n\t" \
    "v_xor_b32 v9, 16384, v9\n\t" \
    "ds_read_b128 v[236:239], v9 offset:0\n\t" \
    "ds_read_b128 v[240:243], v9 offset:1024\n\t" \
    "v_smfmac_f32_16x16x64_bf16 v[4:7], v[220:223], v[244:251], v10\n\t" \
    "ds_read_b128 v[244:247], v9 offset:2048\n\t" \
    "ds_read_b128 v[248:251], v9 offset:3072\n\t" \
    "s_waitcnt lgkmcnt(4)\n\t" \
    "s_barrier\n\t" \
    "ds_read_b128 v[228:231], v8 offset:8192\n\t" \
    "ds_read_b128 v[232:235], v8 offset:12288\n\t" \
    "v_xor_b32 v8, 1024, v8\n\t" \
    "ds_read_b128 v[220:223], v8 offset:4096\n\t" \
    "v_smfmac_f32_16x16x64_bf16 v[0:3], v[224:227], v[28:35], v10\n\t" \
    "v_smfmac_f32_16x16x64_bf16 v[4:7], v[224:227], v[36:43], v10\n\t" \
    "ds_read_b128 v[224:227], v8 offset:0\n\t" \
    "s_waitcnt lgkmcnt(3)\n\t" \
    "v_smfmac_f32_16x16x64_bf16 v[0:3], v[228:231], v[92:99], v10\n\t" \
    "v_smfmac_f32_16x16x64_bf16 v[4:7], v[228:231], v[100:107], v10\n\t" \
    "ds_read_b128 v[228:231], v8 offset:8192\n\t" \
    "s_waitcnt lgkmcnt(3)\n\t" \
    "v_smfmac_f32_16x16x64_bf16 v[0:3], v[232:235], v[156:163], v10\n\t" \
    "v_smfmac_f32_16x16x64_bf16 v[4:7], v[232:235], v[164:171], v10\n\t" \
    "ds_read_b128 v[232:235], v8 offset:12288\n\t" \
    "s_waitcnt lgkmcnt(3)\n\t" \
    "v_smfmac_f32_16x16x64_bf16 v[0:3], v[220:223], v[44:51], v10\n\t" \
    "v_smfmac_f32_16x16x64_bf16 v[4:7], v[220:223], v[52:59], v10\n\t" \
    "v_xor_b32 v8, 3072, v8\n\t" \
    "ds_read_b128 v[220:223], v8 offset:4096\n\t" \
    "s_waitcnt lgkmcnt(3)\n\t" \
    "v_smfmac_f32_16x16x64_bf16 v[0:3], v[224:227], v[236:243], v10\n\t" \
    "v_xor_b32 v9, 49152, v9\n\t" \
    "ds_read_b128 v[236:239], v9 offset:0\n\t" \
    "v_smfmac_f32_16x16x64_bf16 v[4:7], v[224:227], v[244:251], v10\n\t" \
    "ds_read_b128 v[240:243], v9 offset:1024\n\t" \
    "s_waitcnt lgkmcnt(4)\n\t" \
    "v_smfmac_f32_16x16x64_bf16 v[0:3], v[228:231], v[108:115], v10\n\t" \
    "ds_read_b128 v[224:227], v8 offset:8192\n\t" \
    "v_smfmac_f32_16x16x64_bf16 v[4:7], v[228:231], v[116:123], v10\n\t" \
    "ds_read_b128 v[244:247], v9 offset:2048\n\t" \
    "s_waitcnt lgkmcnt(5)\n\t" \
    "v_smfmac_f32_16x16x64_bf16 v[0:3], v[232:235], v[172:179], v10\n\t" \
    "ds_read_b128 v[228:231], v8 offset:0\n\t" \
    "v_smfmac_f32_16x16x64_bf16 v[4:7], v[232:235], v[180:187], v10\n\t" \
    "ds_read_b128 v[248:251], v9 offset:3072\n\t" \
    "s_waitcnt lgkmcnt(6)\n\t" \
    "v_smfmac_f32_16x16x64_bf16 v[0:3], v[220:223], v[60:67], v10\n\t" \
    "ds_read_b128 v[232:235], v8 offset:12288\n\t" \
    "v_smfmac_f32_16x16x64_bf16 v[4:7], v[220:223], v[68:75], v10\n\t" \
    "v_xor_b32 v8, 1024, v8\n\t" \
    "ds_read_b128 v[220:223], v8 offset:4096\n\t" \
    "s_waitcnt lgkmcnt(5)\n\t" \
    "v_smfmac_f32_16x16x64_bf16 v[0:3], v[224:227], v[124:131], v10\n\t" \
    "v_smfmac_f32_16x16x64_bf16 v[4:7], v[224:227], v[132:139], v10\n\t" \
    "ds_read_b128 v[224:227], v8 offset:8192\n\t" \
    "s_waitcnt lgkmcnt(3)\n\t" \
    "v_smfmac_f32_16x16x64_bf16 v[0:3], v[228:231], v[236:243], v10\n\t" \
    "v_xor_b32 v9, 16384, v9\n\t" \
    "ds_read_b128 v[236:239], v9 offset:0\n\t" \
    "v_smfmac_f32_16x16x64_bf16 v[4:7], v[228:231], v[244:251], v10\n\t" \
    "ds_read_b128 v[240:243], v9 offset:1024\n\t" \
    "s_waitcnt lgkmcnt(4)\n\t" \
    "v_smfmac_f32_16x16x64_bf16 v[0:3], v[232:235], v[188:195], v10\n\t" \
    "ds_read_b128 v[228:231], v8 offset:12288\n\t" \
    "v_smfmac_f32_16x16x64_bf16 v[4:7], v[232:235], v[196:203], v10\n\t" \
    "ds_read_b128 v[244:247], v9 offset:2048\n\t" \
    "s_waitcnt lgkmcnt(5)\n\t" \
    "v_smfmac_f32_16x16x64_bf16 v[0:3], v[220:223], v[76:83], v10\n\t" \
    "ds_read_b128 v[232:235], v8 offset:0\n\t" \
    "v_xor_b32 v8, 3072, v8\n\t" \
    "v_smfmac_f32_16x16x64_bf16 v[4:7], v[220:223], v[84:91], v10\n\t" \
    "ds_read_b128 v[248:251], v9 offset:3072\n\t" \
    "s_waitcnt lgkmcnt(6)\n\t" \
    "v_smfmac_f32_16x16x64_bf16 v[0:3], v[224:227], v[140:147], v10\n\t" \
    "ds_read_b128 v[220:223], v12 offset:24576\n\t" \
    "v_smfmac_f32_16x16x64_bf16 v[4:7], v[224:227], v[148:155], v10\n\t" \
    "ds_read_b128 v[224:227], v12 offset:28672\n\t" \
    "s_waitcnt lgkmcnt(5)\n\t" \
    "v_smfmac_f32_16x16x64_bf16 v[0:3], v[228:231], v[204:211], v10\n\t" \
    "v_smfmac_f32_16x16x64_bf16 v[4:7], v[228:231], v[212:219], v10\n\t" \
    "ds_read_b128 v[228:231], v12 offset:25600\n\t" \
    "s_waitcnt lgkmcnt(3)\n\t" \
    "v_smfmac_f32_16x16x64_bf16 v[0:3], v[232:235], v[236:243], v10\n\t" \
    "v_xor_b32 v9, 49152, v9\n\t" \
    "v_smfmac_f32_16x16x64_bf16 v[4:7], v[232:235], v[244:251], v10\n\t" \
    VLD(4) \
    "s_nop 5\n\t" \
    "v_add_f32 v0, v0, v1\n\t" \
    "v_add_f32 v2, v2, v3\n\t" \
    "v_add_f32 v4, v4, v5\n\t" \
    "v_add_f32 v6, v6, v7\n\t" \
    "v_mul_f32 v0, %[sl], v0\n\t" \
    "v_mul_f32 v2, %[sl], v2\n\t" \
    "v_mul_f32 v4, %[sl], v4\n\t" \
    "v_mul_f32 v6, %[sl], v6\n\t" \
    "v_cvt_pk_bf16_f32 v0, v0, v4\n\t" \
    "v_cvt_pk_bf16_f32 v2, v2, v6\n\t" \
    "ds_write2_b32 v11, v0, v2 offset0:0 offset1:4\n\t" \
    "s_mov_b64 exec, %[onej]\n\t" \
    "ds_write_b32 v16, v253\n\t" \
    "s_mov_b64 exec, -1\n\t" \
    GSA(1) \
    "s_setprio 0\n\t" \
    "ds_read_b128 v[232:235], v12 offset:29696\n\t" \
    "s_nop 1\n\t" \
    "ds_read_b128 v[0:3], v12 offset:26624\n\t" \
    "s_nop 1\n\t" \
    "ds_read_b128 v[4:7], v12 offset:27648\n\t" \
    "s_nop 1\n\t" \
    "ds_read_b32 v252, v16\n\t" \
    "ds_read_b128 v[236:239], v14 offset:0\n\t" \
    "s_nop 1\n\t" \
    "ds_read_b128 v[240:243], v14 offset:512\n\t" \
    "s_nop 1\n\t" \
    "ds_read_b128 v[244:247], v14 offset:1024\n\t" \
    "s_nop 1\n\t" \
    "s_setprio 2\n\t" \
    VLD(3) \
    "s_setprio 0\n\t" \
    "ds_read_b128 v[248:251], v14 offset:1536\n\t" \
    "s_nop 1\n\t" \
    "s_setprio 2\n\t" \
    "s_waitcnt lgkmcnt(4)\n\t" \
    "v_cmp_ne_u32 vcc, v253, v252\n\t" \
    "s_cbranch_vccnz RP_%=\n\t" \
    "CP_%=:\n\t" \
    "s_setprio 2\n\t" \
    VLD(0) \
    "s_waitcnt lgkmcnt(2)\n\t" \
    "v_mfma_f32_16x16x32_bf16 v[220:223], v[220:223], v[236:239], 0\n\t" \
    "v_mfma_f32_16x16x32_bf16 v[224:227], v[224:227], v[236:239], 0\n\t" \
    "v_mfma_f32_16x16x32_bf16 v[220:223], v[228:231], v[240:243], v[220:223]\n\t" \
    "ds_read_b128 v[228:231], v12 offset:30720\n\t" \
    "v_mfma_f32_16x16x32_bf16 v[224:227], v[232:235], v[240:243], v[224:227]\n\t" \
    "ds_read_b128 v[232:235], v12 offset:31744\n\t" \
    "s_setprio " STR(QPRIO) "\n\t" \
    VLD(1) \
    "s_waitcnt lgkmcnt(2)\n\t" \
    "v_mfma_f32_16x16x32_bf16 v[220:223], v[0:3], v[244:247], v[220:223]\n\t" \
    "v_mfma_f32_16x16x32_bf16 v[220:223], v[4:7], v[248:251], v[220:223]\n\t" \
    "s_waitcnt lgkmcnt(1)\n\t" \
    "v_mfma_f32_16x16x32_bf16 v[224:227], v[228:231], v[244:247], v[224:227]\n\t" \
    "s_waitcnt lgkmcnt(0)\n\t" \
    "v_mfma_f32_16x16x32_bf16 v[224:227], v[232:235], v[248:251], v[224:227]\n\t" \
    GSA(2) \
    "s_setprio 2\n\t" \
    "s_nop 2\n\t" \
    "v_exp_f32 v220, v220\n\t" \
    "v_exp_f32 v221, v221\n\t" \
    "v_exp_f32 v222, v222\n\t" \
    "v_exp_f32 v223, v223\n\t" \
    "v_exp_f32 v224, v224\n\t" \
    "v_exp_f32 v225, v225\n\t" \
    "v_exp_f32 v226, v226\n\t" \
    "v_exp_f32 v227, v227\n\t" \
    "ds_read_b32 v252, v16 offset:16\n\t" \
    "ds_read_b128 v[0:3], v12 offset:22528\n\t" \
    "ds_read_b128 v[4:7], v12 offset:23552\n\t" \
    VLD(2) \
    "v_cvt_pk_bf16_f32 v220, v220, v221\n\t" \
    "v_cvt_pk_bf16_f32 v221, v222, v223\n\t" \
    "v_cvt_pk_bf16_f32 v222, v224, v225\n\t" \
    "v_cvt_pk_bf16_f32 v223, v226, v227\n\t" \
    "v_mov_b32 v224, 0\n\t" \
    "v_mov_b32 v225, 0\n\t" \
    "v_mfma_f32_16x16x32_bf16 v[228:231], v[20:23], v[220:223], 0\n\t" \
    "v_mov_b32 v226, 0\n\t" \
    "v_mov_b32 v227, 0\n\t" \
    "v_mfma_f32_16x16x32_bf16 v[232:235], v[24:27], v[220:223], 0\n\t" \
    "s_waitcnt lgkmcnt(2)\n\t" \
    "v_cmp_ne_u32 vcc, %[tag], v252\n\t" \
    "s_cbranch_vccnz RK_%=\n\t" \
    "CK_%=:\n\t" \
    "s_waitcnt lgkmcnt(1)\n\t" \
    "v_smfmac_f32_16x16x64_bf16 v[224:227], v[0:3], v[236:243], v10\n\t" \
    "s_waitcnt lgkmcnt(0)\n\t" \
    "v_smfmac_f32_16x16x64_bf16 v[224:227], v[4:7], v[244:251], v10\n\t" \
    "v_mov_b32 v236, %[one]\n\t" \
    "v_mov_b32 v237, %[one]\n\t" \
    "v_mov_b32 v238, %[one]\n\t" \
    "v_mov_b32 v239, %[one]\n\t" \
    "v_mov_b32 v1, 0\n\t" \
    "v_mov_b32 v2, 0\n\t" \
    "v_mfma_f32_16x16x32_bf16 v[240:243], v[236:239], v[220:223], 0\n\t" \
    "s_nop 0\n\t" \
    "v_add_f32 v4, v224, v225\n\t" \
    "v_add_f32 v5, v226, v227\n\t" \
    "v_exp_f32 v4, v4\n\t" \
    "v_exp_f32 v5, v5\n\t" \
    "s_nop 0\n\t" \
    "v_cvt_pk_bf16_f32 v0, v4, v5\n\t" \
    "v_cvt_pk_bf16_f32 v3, v4, v5\n\t" \
    GSA(3) \
    "CM_%=:\n\t" \
    "s_nop 0\n\t" \
    "v_mfma_f32_16x16x16_bf16 v[240:243], v[236:237], v[0:1], v[240:243]\n\t" \
    "s_waitcnt vmcnt(0)\n\t" \
    "v_mfma_f32_16x16x16_bf16 v[228:231], v[254:255], v[0:1], v[228:231]\n\t" \
    "v_max_u32 v7, v254, v255\n\t" \
    "v_mfma_f32_16x16x16_bf16 v[232:235], v[254:255], v[2:3], v[232:235]\n\t" \
    "v_cmp_eq_u32 s[88:89], -1, v7\n\t" \
    "s_not_b64 exec, %[hi]\n\t" \
    "s_nop 1\n\t" \
    "v_rcp_f32 v252, v240\n\t" \
    "v_subrev_u32 v253, 0x21800000, v240\n\t" \
    "v_cmp_gt_u32 vcc, v253, %[rg]\n\t" \
    "v_pk_mul_f32 v[228:229], v[228:229], v[252:253] op_sel_hi:[1,0]\n\t" \
    "v_pk_mul_f32 v[230:231], v[230:231], v[252:253] op_sel_hi:[1,0]\n\t" \
    "v_pk_mul_f32 v[232:233], v[232:233], v[252:253] op_sel_hi:[1,0]\n\t" \
    "v_pk_mul_f32 v[234:235], v[234:235], v[252:253] op_sel_hi:[1,0]\n\t" \
    "s_or_b64 vcc, vcc, s[88:89]\n\t" \
    "v_cvt_pk_bf16_f32 v4, v228, v229\n\t" \
    "v_cvt_pk_bf16_f32 v5, v230, v231\n\t" \
    "v_cvt_pk_bf16_f32 v6, v232, v233\n\t" \
    "v_cvt_pk_bf16_f32 v7, v234, v235\n\t" \
    "s_cbranch_scc1 FX_%=\n\t" \
    "buffer_store_dwordx4 v[4:7], v18, %[rs], %[sb] offen sc1\n\t" \
    "s_memrealtime %[tp]\n\t" \
    DGA \
    GSA(4) \
    "v_max3_u32 v252, v4, v5, v6\n\t" \
    "v_max_u32 v252, v252, v7\n\t" \
    "v_cmp_eq_u32 vcc, -1, v252\n\t" \
    "s_cbranch_vccnz RC_%=\n\t" \
    "CC_%=:\n\t" \
    "s_mov_b64 exec, -1\n\t" \
    "v_add_u32 v252, %[vd], v12\n\t" \
    "v_add_u32 v253, %[md], v17\n\t" \
    "ds_write_b128 v252, v[20:23]\n\t" \
    "ds_write_b128 v252, v[24:27] offset:1024\n\t" \
    "ds_write2st64_b32 v253, v254, v255 offset0:0 offset1:1\n\t" \
    "s_waitcnt lgkmcnt(0)\n\t" \
    "s_cmp_lg_u32 %[sc], 3\n\t" \
    "s_cbranch_scc1 NH_%=\n\t" \
    "buffer_load_dwordx2 v[0:1], off, %[rs], %[fl] sc1\n\t" \
    "s_barrier\n\t" \
    "s_waitcnt vmcnt(0)\n\t" \
    "v_readfirstlane_b32 %[pt], v0\n\t" \
    "v_readfirstlane_b32 %[pd], v1\n\t" \
    "s_nop 3\n\t" \
    "s_branch E_%=\n\t" \
    "NH_%=:\n\t" \
    "s_barrier\n\t" \
    "s_branch E_%=\n\t" \
    "RP_%=:\n\t" \
    "s_waitcnt lgkmcnt(0)\n\t" \
    "ds_read_b32 v252, v16\n\t" \
    "ds_read_b128 v[236:239], v14 offset:0\n\t" \
    "ds_read_b128 v[240:243], v14 offset:512\n\t" \
    "ds_read_b128 v[244:247], v14 offset:1024\n\t" \
    "ds_read_b128 v[248:251], v14 offset:1536\n\t" \
    "s_sub_u32 %[lim], %[lim], " STR(DEC1) "\n\t" \
    "s_cmp_gt_i32 %[lim], 0\n\t" \
    "s_waitcnt lgkmcnt(4)\n\t" \
    "s_cbranch_scc0 CP_%=\n\t" \
    "v_cmp_ne_u32 vcc, %[tag], v252\n\t" \
    "s_cbranch_vccnz RP_%=\n\t" \
    "s_branch CP_%=\n\t" \
    "RK_%=:\n\t" \
    "s_waitcnt lgkmcnt(0)\n\t" \
    "ds_read_b32 v252, v16 offset:16\n\t" \
    "ds_read_b128 v[0:3], v12 offset:22528\n\t" \
    "ds_read_b128 v[4:7], v12 offset:23552\n\t" \
    "s_sub_u32 %[lim], %[lim], " STR(DEC1) "\n\t" \
    "s_cmp_gt_i32 %[lim], 0\n\t" \
    "s_waitcnt lgkmcnt(2)\n\t" \
    "s_cbranch_scc0 CK_%=\n\t" \
    "v_cmp_ne_u32 vcc, %[tag], v252\n\t" \
    "s_cbranch_vccnz RK_%=\n\t" \
    "s_branch CK_%=\n\t" \
    "RC_%=:\n\t" \
    "v_min_u32 v4, -2, v4\n\t" \
    "v_min_u32 v5, -2, v5\n\t" \
    "v_min_u32 v6, -2, v6\n\t" \
    "v_min_u32 v7, -2, v7\n\t" \
    "buffer_store_dwordx4 v[4:7], v18, %[rs], %[sb] offen sc1\n\t" \
    "s_branch CC_%=\n\t" \
    "FX_%=:\n\t" \
    "s_mov_b64 exec, -1\n\t" \
    "s_cmp_eq_u64 s[88:89], 0\n\t" \
    "s_cbranch_scc1 RM_%=\n\t" \
    "buffer_load_dword v254, v17, %[rs], %[sb] offen sc1\n\t" \
    "buffer_load_dword v255, v17, %[rs], %[sb] offen offset:256 sc1\n\t" \
    "v_mfma_f32_16x16x32_bf16 v[228:231], v[20:23], v[220:223], 0\n\t" \
    "v_mfma_f32_16x16x32_bf16 v[232:235], v[24:27], v[220:223], 0\n\t" \
    "v_mfma_f32_16x16x32_bf16 v[240:243], v[236:239], v[220:223], 0\n\t" \
    "s_sub_u32 %[lim], %[lim], " STR(DEC2) "\n\t" \
    "s_cmp_gt_i32 %[lim], 0\n\t" \
    "s_cbranch_scc1 CM_%=\n\t" \
    "s_waitcnt vmcnt(0)\n\t" \
    "v_mov_b32 v254, 0\n\t" \
    "v_mov_b32 v255, 0\n\t" \
    "s_branch CM_%=\n\t" \
    "RM_%=:\n\t" \
    "s_mov_b32 %[rg], -1\n\t" \
    "s_nop 7\n\t" \
    "v_add_f32 v4, v224, v225\n\t" \
    "v_add_f32 v5, v226, v227\n\t" \
    "ds_read_b128 v[236:239], v14 offset:0\n\t" \
    "ds_read_b128 v[240:243], v14 offset:512\n\t" \
    "ds_read_b128 v[244:247], v14 offset:1024\n\t" \
    "ds_read_b128 v[248:251], v14 offset:1536\n\t" \
    "ds_read_b128 v[232:235], v12 offset:24576\n\t" \
    "s_waitcnt lgkmcnt(0)\n\t" \
    "v_mfma_f32_16x16x32_bf16 v[220:223], v[232:235], v[236:239], 0\n\t" \
    "ds_read_b128 v[232:235], v12 offset:25600\n\t" \
    "s_waitcnt lgkmcnt(0)\n\t" \
    "v_mfma_f32_16x16x32_bf16 v[220:223], v[232:235], v[240:243], v[220:223]\n\t" \
    "ds_read_b128 v[232:235], v12 offset:26624\n\t" \
    "s_waitcnt lgkmcnt(0)\n\t" \
    "v_mfma_f32_16x16x32_bf16 v[220:223], v[232:235], v[244:247], v[220:223]\n\t" \
    "ds_read_b128 v[232:235], v12 offset:27648\n\t" \
    "s_waitcnt lgkmcnt(0)\n\t" \
    "v_mfma_f32_16x16x32_bf16 v[220:223], v[232:235], v[248:251], v[220:223]\n\t" \
    "ds_read_b128 v[232:235], v12 offset:28672\n\t" \
    "s_waitcnt lgkmcnt(0)\n\t" \
    "v_mfma_f32_16x16x32_bf16 v[224:227], v[232:235], v[236:239], 0\n\t" \
    "ds_read_b128 v[232:235], v12 offset:29696\n\t" \
    "s_waitcnt lgkmcnt(0)\n\t" \
    "v_mfma_f32_16x16x32_bf16 v[224:227], v[232:235], v[240:243], v[224:227]\n\t" \
    "ds_read_b128 v[232:235], v12 offset:30720\n\t" \
    "s_waitcnt lgkmcnt(0)\n\t" \
    "v_mfma_f32_16x16x32_bf16 v[224:227], v[232:235], v[244:247], v[224:227]\n\t" \
    "ds_read_b128 v[232:235], v12 offset:31744\n\t" \
    "s_waitcnt lgkmcnt(0)\n\t" \
    "v_mfma_f32_16x16x32_bf16 v[224:227], v[232:235], v[248:251], v[224:227]\n\t" \
    "v_max3_f32 v252, v220, v221, v222\n\t" \
    "s_nop 6\n\t" \
    "v_max3_f32 v253, v223, v224, v225\n\t" \
    "v_max3_f32 v252, v252, v226, v227\n\t" \
    "v_max3_f32 v253, v253, v4, v5\n\t" \
    "v_max_f32 v252, v252, v253\n\t" \
    "v_mov_b32 v253, v252\n\t" \
    "v_mov_b32 v6, v252\n\t" \
    "s_nop 1\n\t" \
    "v_permlane16_swap_b32 v253, v6\n\t" \
    "v_max_f32 v252, v253, v6\n\t" \
    "v_max_f32 v253, v253, v6\n\t" \
    "s_nop 1\n\t" \
    "v_permlane32_swap_b32 v252, v253\n\t" \
    "v_max_f32 v252, v252, v253\n\t" \
    "v_sub_f32 v220, v220, v252\n\t" \
    "v_sub_f32 v221, v221, v252\n\t" \
    "v_sub_f32 v222, v222, v252\n\t" \
    "v_sub_f32 v223, v223, v252\n\t" \
    "v_sub_f32 v224, v224, v252\n\t" \
    "v_sub_f32 v225, v225, v252\n\t" \
    "v_sub_f32 v226, v226, v252\n\t" \
    "v_sub_f32 v227, v227, v252\n\t" \
    "v_sub_f32 v4, v4, v252\n\t" \
    "v_sub_f32 v5, v5, v252\n\t" \
    "v_exp_f32 v220, v220\n\t" \
    "v_exp_f32 v221, v221\n\t" \
    "v_exp_f32 v222, v222\n\t" \
    "v_exp_f32 v223, v223\n\t" \
    "v_exp_f32 v224, v224\n\t" \
    "v_exp_f32 v225, v225\n\t" \
    "v_exp_f32 v226, v226\n\t" \
    "v_exp_f32 v227, v227\n\t" \
    "v_cvt_pk_bf16_f32 v220, v220, v221\n\t" \
    "v_cvt_pk_bf16_f32 v221, v222, v223\n\t" \
    "v_cvt_pk_bf16_f32 v222, v224, v225\n\t" \
    "v_cvt_pk_bf16_f32 v223, v226, v227\n\t" \
    "v_mov_b32 v236, %[one]\n\t" \
    "v_mov_b32 v237, %[one]\n\t" \
    "v_mov_b32 v238, %[one]\n\t" \
    "v_mov_b32 v239, %[one]\n\t" \
    "v_mfma_f32_16x16x32_bf16 v[228:231], v[20:23], v[220:223], 0\n\t" \
    "v_mfma_f32_16x16x32_bf16 v[232:235], v[24:27], v[220:223], 0\n\t" \
    "v_mfma_f32_16x16x32_bf16 v[240:243], v[236:239], v[220:223], 0\n\t" \
    "v_exp_f32 v4, v4\n\t" \
    "v_exp_f32 v5, v5\n\t" \
    "s_nop 0\n\t" \
    "v_cvt_pk_bf16_f32 v0, v4, v5\n\t" \
    "v_cvt_pk_bf16_f32 v3, v4, v5\n\t" \
    "s_nop 1\n\t" \
    "s_branch CM_%=\n\t" \
    "E_%=:\n\t" \
    GSW \
    "s_nop 1\n\t" \
    : [lim] "+s"(lim), [rg] "+s"(rg), [tp] "=&s"(tp), [pt] "=&s"(pt), [pd] "=&s"(pd) DGO DGP GSO(0) GSO(1) GSO(2) GSO(3) GSO(4) \
    : "{v8}"(am_a), "{v9}"(t0_a), "{v10}"(idx), "{v11}"(iw_a), "{v12}"(ab_a), "{v14}"(qr_a), "{v16}"(fr_a), "{v17}"(vo_v), "{v18}"(oo_v), [rs] "s"(rs), [sb] "s"(sb), [tag] "s"(0xffffff81u + s), [hi] "s"(0xff00ff00ff00ff00ull), [tgt] "s"(tgt), [sc] "s"(wid), [onej] "s"(onej), [sl] "s"(0x3e0293ee), [one] "s"(0x3f803f80), [m0] "s"(sbm), [m1] "s"(sbm + 4096u), [vx] "s"(vxo), [fl] "s"(fb + FL_T(s + 1)), [vd] "s"(8192 + j * 2048), [md] "s"(md), "{v[28:35]}"(wr[0][0]), "{v[36:43]}"(wr[0][1]), "{v[44:51]}"(wr[1][0]), "{v[52:59]}"(wr[1][1]), "{v[60:67]}"(wr[2][0]), "{v[68:75]}"(wr[2][1]), "{v[76:83]}"(wr[3][0]), "{v[84:91]}"(wr[3][1]), "{v[92:99]}"(wr[4][0]), "{v[100:107]}"(wr[4][1]), "{v[108:115]}"(wr[5][0]), "{v[116:123]}"(wr[5][1]), "{v[124:131]}"(wr[6][0]), "{v[132:139]}"(wr[6][1]), "{v[140:147]}"(wr[7][0]), "{v[148:155]}"(wr[7][1]), "{v[156:163]}"(wr[8][0]), "{v[164:171]}"(wr[8][1]), "{v[172:179]}"(wr[9][0]), "{v[180:187]}"(wr[9][1]), "{v[188:195]}"(wr[10][0]), "{v[196:203]}"(wr[10][1]), "{v[204:211]}"(wr[11][0]), "{v[212:219]}"(wr[11][1]) \
    : "memory", "vcc", "scc", "s88", "s89", "s90", "s91", "s92", "s93", "s94", "s95", "v0", "v1", "v2", "v3", "v4", "v5", "v6", "v7", "v20", "v21", "v22", "v23", "v24", "v25", "v26", "v27", "v220", "v221", "v222", "v223", "v224", "v225", "v226", "v227", "v228", "v229", "v230", "v231", "v232", "v233", "v234", "v235", "v236", "v237", "v238", "v239", "v240", "v241", "v242", "v243", "v244", "v245", "v246", "v247", "v248", "v249", "v250", "v251", "v252", "v253", "v254", "v255")

#define A_MAIN_K() \
  asm volatile( \
    "ds_read_b128 v[236:239], v9 offset:0\n\t" \
    "ds_read_b128 v[240:243], v9 offset:1024\n\t" \
    "ds_read_b128 v[244:247], v9 offset:2048\n\t" \
    "ds_read_b128 v[248:251], v9 offset:3072\n\t" \
    "v_mov_b32 v0, 0\n\t" \
    "v_mov_b32 v1, 0\n\t" \
    "v_mov_b32 v2, 0\n\t" \
    "v_mov_b32 v3, 0\n\t" \
    "v_mov_b32 v4, 0\n\t" \
    "v_mov_b32 v5, 0\n\t" \
    "v_mov_b32 v6, 0\n\t" \
    "v_mov_b32 v7, 0\n\t" \
    "s_waitcnt lgkmcnt(0)\n\t" \
    "s_barrier\n\t" \
    "buffer_load_dwordx4 v[220:223], v8, %[rs], %[m0] offen sc1\n\t" \
    "buffer_load_dwordx4 v[224:227], v8, %[rs], %[m1] offen sc1\n\t" \
    GSA(3) \
    "v_mov_b32 v252, v252\n\t" \
    "s_nop 2\n\t" \
    "v_mov_b32 v252, v252\n\t" \
    "s_nop 2\n\t" \
    "v_mov_b32 v252, v252\n\t" \
    "s_nop 2\n\t" \
    "v_mov_b32 v252, v252\n\t" \
    "s_nop 2\n\t" \
    "v_mov_b32 v252, v252\n\t" \
    "s_nop 2\n\t" \
    "v_mov_b32 v252, v252\n\t" \
    "s_nop 2\n\t" \
    "v_mov_b32 v252, v252\n\t" \
    "s_nop 2\n\t" \
    "v_mov_b32 v252, v252\n\t" \
    "s_nop 2\n\t" \
    "v_mov_b32 v252, v252\n\t" \
    "s_nop 2\n\t" \
    "v_mov_b32 v252, v252\n\t" \
    "s_nop 2\n\t" \
    "v_mov_b32 v252, v252\n\t" \
    "s_nop 2\n\t" \
    "v_mov_b32 v252, v252\n\t" \
    "s_nop 2\n\t" \
    "v_mov_b32 v252, v252\n\t" \
    "s_nop 2\n\t" \
    "v_mov_b32 v252, v252\n\t" \
    "s_nop 2\n\t" \
    "v_mov_b32 v252, v252\n\t" \
    "s_nop 2\n\t" \
    "v_mov_b32 v252, v252\n\t" \
    "s_nop 2\n\t" \
    "v_mov_b32 v252, v252\n\t" \
    "s_nop 2\n\t" \
    "v_mov_b32 v252, v252\n\t" \
    "s_nop 2\n\t" \
    "v_mov_b32 v252, v252\n\t" \
    "s_nop 2\n\t" \
    "v_mov_b32 v252, v252\n\t" \
    "s_nop 2\n\t" \
    "v_mov_b32 v252, v252\n\t" \
    "s_nop 2\n\t" \
    "v_mov_b32 v252, v252\n\t" \
    "s_nop 2\n\t" \
    "v_mov_b32 v252, v252\n\t" \
    "s_nop 2\n\t" \
    "v_mov_b32 v252, v252\n\t" \
    "s_nop 2\n\t" \
    "v_mov_b32 v252, v252\n\t" \
    "s_nop 2\n\t" \
    "v_mov_b32 v252, v252\n\t" \
    "s_nop 2\n\t" \
    "v_mov_b32 v252, v252\n\t" \
    "s_nop 2\n\t" \
    "v_mov_b32 v252, v252\n\t" \
    "s_nop 2\n\t" \
    "v_mov_b32 v252, v252\n\t" \
    "s_nop 2\n\t" \
    "v_mov_b32 v252, v252\n\t" \
    "s_nop 2\n\t" \
    "PM_%=:\n\t" \
    "s_waitcnt vmcnt(1)\n\t" \
    "ds_write_b128 v8, v[220:223] offset:8192\n\t" \
    "v_max3_u32 v252, v220, v221, v222\n\t" \
    "s_waitcnt vmcnt(0)\n\t" \
    "ds_write_b128 v8, v[224:227] offset:12288\n\t" \
    "v_max3_u32 v252, v252, v223, v224\n\t" \
    "v_max3_u32 v252, v252, v225, v226\n\t" \
    "v_max_u32 v252, v252, v227\n\t" \
    "v_cmp_eq_u32 vcc, -1, v252\n\t" \
    "s_cbranch_vccz PD_%=\n\t" \
    "s_sub_u32 %[lim], %[lim], 1\n\t" \
    "s_cmp_gt_i32 %[lim], 0\n\t" \
    "s_cbranch_scc0 PD_%=\n\t" \
    "s_sleep " STR(CADM) "\n\t" \
    "buffer_load_dwordx4 v[220:223], v8, %[rs], %[m0] offen sc1\n\t" \
    "buffer_load_dwordx4 v[224:227], v8, %[rs], %[m1] offen sc1\n\t" \
    "s_branch PM_%=\n\t" \
    "PD_%=:\n\t" \
    GSA(0) \
    "v_smfmac_f32_16x16x64_bf16 v[0:3], v[220:223], v[236:243], v10\n\t" \
    "v_xor_b32 v9, 16384, v9\n\t" \
    "v_smfmac_f32_16x16x64_bf16 v[4:7], v[220:223], v[244:251], v10\n\t" \
    "s_waitcnt lgkmcnt(0)\n\t" \
    "s_barrier\n\t" \
    DGA \
    "s_cmp_lg_u32 %[st], 63\n\t" \
    "s_cbranch_scc1 NL_%=\n\t" \
    "s_mov_b64 exec, %[fm]\n\t" \
    "v_mov_b32 v252, %[st]\n\t" \
    "v_mov_b32 v253, %[tag]\n\t" \
    "v_mov_b32 v254, 0\n\t" \
    "buffer_store_dwordx2 v[252:253], v254, %[rs], %[po] offen sc1\n\t" \
    "s_mov_b64 exec, -1\n\t" \
    "NL_%=:\n\t" \
    "ds_read_b128 v[236:239], v9 offset:0\n\t" \
    "ds_read_b128 v[240:243], v9 offset:1024\n\t" \
    "ds_read_b128 v[244:247], v9 offset:2048\n\t" \
    "ds_read_b128 v[248:251], v9 offset:3072\n\t" \
    "ds_read_b128 v[228:231], v8 offset:0\n\t" \
    "ds_read_b128 v[232:235], v8 offset:4096\n\t" \
    "v_smfmac_f32_16x16x64_bf16 v[0:3], v[224:227], v[148:155], v10\n\t" \
    "v_xor_b32 v8, 1024, v8\n\t" \
    "ds_read_b128 v[220:223], v8 offset:8192\n\t" \
    "v_smfmac_f32_16x16x64_bf16 v[4:7], v[224:227], v[156:163], v10\n\t" \
    "ds_read_b128 v[224:227], v8 offset:12288\n\t" \
    "buffer_store_dwordx4 v[212:215], v14, %[rs], %[ox] offen offset:0 sc1\n\t" \
    "buffer_store_dwordx4 v[216:219], v14, %[rs], %[ox] offen offset:512 sc1\n\t" \
    "s_waitcnt lgkmcnt(3)\n\t" \
    "v_smfmac_f32_16x16x64_bf16 v[0:3], v[228:231], v[20:27], v10\n\t" \
    "v_smfmac_f32_16x16x64_bf16 v[4:7], v[228:231], v[28:35], v10\n\t" \
    "ds_read_b128 v[228:231], v8 offset:0\n\t" \
    "s_waitcnt lgkmcnt(3)\n\t" \
    "v_smfmac_f32_16x16x64_bf16 v[0:3], v[232:235], v[84:91], v10\n\t" \
    "v_smfmac_f32_16x16x64_bf16 v[4:7], v[232:235], v[92:99], v10\n\t" \
    "ds_read_b128 v[232:235], v8 offset:4096\n\t" \
    "s_waitcnt lgkmcnt(3)\n\t" \
    "v_smfmac_f32_16x16x64_bf16 v[0:3], v[220:223], v[236:243], v10\n\t" \
    "v_xor_b32 v9, 49152, v9\n\t" \
    "ds_read_b128 v[236:239], v9 offset:0\n\t" \
    "v_smfmac_f32_16x16x64_bf16 v[4:7], v[220:223], v[244:251], v10\n\t" \
    "ds_read_b128 v[240:243], v9 offset:1024\n\t" \
    "s_waitcnt lgkmcnt(4)\n\t" \
    "v_smfmac_f32_16x16x64_bf16 v[0:3], v[224:227], v[164:171], v10\n\t" \
    "v_xor_b32 v8, 3072, v8\n\t" \
    "ds_read_b128 v[220:223], v8 offset:12288\n\t" \
    "v_smfmac_f32_16x16x64_bf16 v[4:7], v[224:227], v[172:179], v10\n\t" \
    "ds_read_b128 v[244:247], v9 offset:2048\n\t" \
    "s_waitcnt lgkmcnt(5)\n\t" \
    "v_smfmac_f32_16x16x64_bf16 v[0:3], v[228:231], v[36:43], v10\n\t" \
    "ds_read_b128 v[224:227], v8 offset:0\n\t" \
    "v_smfmac_f32_16x16x64_bf16 v[4:7], v[228:231], v[44:51], v10\n\t" \
    "ds_read_b128 v[248:251], v9 offset:3072\n\t" \
    "s_waitcnt lgkmcnt(6)\n\t" \
    "v_smfmac_f32_16x16x64_bf16 v[0:3], v[232:235], v[100:107], v10\n\t" \
    "ds_read_b128 v[228:231], v8 offset:8192\n\t" \
    "v_smfmac_f32_16x16x64_bf16 v[4:7], v[232:235], v[108:115], v10\n\t" \
    "ds_read_b128 v[232:235], v8 offset:4096\n\t" \
    "s_waitcnt lgkmcnt(5)\n\t" \
    "v_smfmac_f32_16x16x64_bf16 v[0:3], v[220:223], v[180:187], v10\n\t" \
    "v_smfmac_f32_16x16x64_bf16 v[4:7], v[220:223], v[188:195], v10\n\t" \
    "v_xor_b32 v8, 1024, v8\n\t" \
    "ds_read_b128 v[220:223], v8 offset:12288\n\t" \
    "s_waitcnt lgkmcnt(4)\n\t" \
    "v_smfmac_f32_16x16x64_bf16 v[0:3], v[224:227], v[52:59], v10\n\t" \
    "v_smfmac_f32_16x16x64_bf16 v[4:7], v[224:227], v[60:67], v10\n\t" \
    "ds_read_b128 v[224:227], v8 offset:0\n\t" \
    "s_waitcnt lgkmcnt(3)\n\t" \
    "v_smfmac_f32_16x16x64_bf16 v[0:3], v[228:231], v[236:243], v10\n\t" \
    "v_xor_b32 v9, 16384, v9\n\t" \
    "ds_read_b128 v[236:239], v9 offset:0\n\t" \
    "v_smfmac_f32_16x16x64_bf16 v[4:7], v[228:231], v[244:251], v10\n\t" \
    "ds_read_b128 v[240:243], v9 offset:1024\n\t" \
    "s_waitcnt lgkmcnt(4)\n\t" \
    "v_smfmac_f32_16x16x64_bf16 v[0:3], v[232:235], v[116:123], v10\n\t" \
    "ds_read_b128 v[228:231], v8 offset:4096\n\t" \
    "v_smfmac_f32_16x16x64_bf16 v[4:7], v[232:235], v[124:131], v10\n\t" \
    "ds_read_b128 v[244:247], v9 offset:2048\n\t" \
    "s_waitcnt lgkmcnt(5)\n\t" \
    "v_smfmac_f32_16x16x64_bf16 v[0:3], v[220:223], v[196:203], v10\n\t" \
    "ds_read_b128 v[232:235], v8 offset:8192\n\t" \
    "v_xor_b32 v8, 3072, v8\n\t" \
    "v_smfmac_f32_16x16x64_bf16 v[4:7], v[220:223], v[204:211], v10\n\t" \
    "ds_read_b128 v[248:251], v9 offset:3072\n\t" \
    "s_waitcnt lgkmcnt(6)\n\t" \
    "v_smfmac_f32_16x16x64_bf16 v[0:3], v[224:227], v[68:75], v10\n\t" \
    "v_smfmac_f32_16x16x64_bf16 v[4:7], v[224:227], v[76:83], v10\n\t" \
    "s_waitcnt lgkmcnt(3)\n\t" \
    "v_smfmac_f32_16x16x64_bf16 v[0:3], v[228:231], v[132:139], v10\n\t" \
    "v_smfmac_f32_16x16x64_bf16 v[4:7], v[228:231], v[140:147], v10\n\t" \
    "s_waitcnt lgkmcnt(0)\n\t" \
    "v_smfmac_f32_16x16x64_bf16 v[0:3], v[232:235], v[236:243], v10\n\t" \
    "v_xor_b32 v9, 49152, v9\n\t" \
    "v_smfmac_f32_16x16x64_bf16 v[4:7], v[232:235], v[244:251], v10\n\t" \
    "s_setprio 3\n\t" \
    "v_mov_b32 v253, %[stag]\n\t" \
    "s_nop 3\n\t" \
    "v_add_f32 v0, v0, v1\n\t" \
    "v_add_f32 v2, v2, v3\n\t" \
    "v_add_f32 v4, v4, v5\n\t" \
    "v_add_f32 v6, v6, v7\n\t" \
    "v_cvt_pk_bf16_f32 v0, v0, v4\n\t" \
    "v_cvt_pk_bf16_f32 v2, v2, v6\n\t" \
    "ds_write2_b32 v13, v0, v2 offset0:0 offset1:8\n\t" \
    "s_mov_b64 exec, %[onej]\n\t" \
    "ds_write_b32 v18, v253 offset:16\n\t" \
    "s_mov_b64 exec, -1\n\t" \
    "s_setprio 0\n\t" \
    "ds_write2_b32 v11, v0, v2 offset0:0 offset1:4\n\t" \
    "s_mov_b64 exec, %[onej]\n\t" \
    "ds_write_b32 v18, v253 offset:32\n\t" \
    "s_mov_b64 exec, -1\n\t" \
    GSA(1) \
    "s_setprio 0\n\t" \
    "buffer_load_dwordx4 v[236:239], v12, %[rs], %[qx] offen offset:0 sc1\n\t" \
    "buffer_load_dwordx4 v[240:243], v12, %[rs], %[qx] offen offset:1024 sc1\n\t" \
    "buffer_load_dwordx4 v[244:247], v12, %[rs], %[qx] offen offset:2048 sc1\n\t" \
    "buffer_load_dwordx4 v[248:251], v12, %[rs], %[qx] offen offset:3072 sc1\n\t" \
    "v_cmp_le_i32 s[72:73], 0, v16\n\t" \
    "v_cmp_le_i32 s[74:75], 1, v16\n\t" \
    "v_cmp_le_i32 s[76:77], 2, v16\n\t" \
    "v_cmp_le_i32 s[78:79], 3, v16\n\t" \
    "v_cmp_le_i32 s[80:81], 16, v16\n\t" \
    "v_cmp_le_i32 s[82:83], 17, v16\n\t" \
    "v_cmp_le_i32 s[84:85], 18, v16\n\t" \
    "v_cmp_le_i32 s[86:87], 19, v16\n\t" \
    "v_mov_b32 v254, v254\n\t" \
    "s_nop 2\n\t" \
    "v_mov_b32 v254, v254\n\t" \
    "s_nop 2\n\t" \
    "v_mov_b32 v254, v254\n\t" \
    "s_nop 2\n\t" \
    "v_mov_b32 v254, v254\n\t" \
    "s_nop 2\n\t" \
    "v_mov_b32 v254, v254\n\t" \
    "s_nop 2\n\t" \
    "v_mov_b32 v254, v254\n\t" \
    "s_nop 2\n\t" \
    "v_mov_b32 v254, v254\n\t" \
    "s_nop 2\n\t" \
    "v_mov_b32 v254, v254\n\t" \
    "s_nop 2\n\t" \
    "v_mov_b32 v254, v254\n\t" \
    "s_nop 2\n\t" \
    "v_mov_b32 v254, v254\n\t" \
    "s_nop 2\n\t" \
    "v_mov_b32 v254, v254\n\t" \
    "s_nop 2\n\t" \
    "v_mov_b32 v254, v254\n\t" \
    "s_nop 2\n\t" \
    "v_mov_b32 v254, v254\n\t" \
    "s_nop 2\n\t" \
    "v_mov_b32 v254, v254\n\t" \
    "s_nop 2\n\t" \
    "v_mov_b32 v254, v254\n\t" \
    "s_nop 2\n\t" \
    "v_mov_b32 v254, v254\n\t" \
    "s_nop 2\n\t" \
    "v_mov_b32 v254, v254\n\t" \
    "s_nop 2\n\t" \
    "v_mov_b32 v254, v254\n\t" \
    "s_nop 2\n\t" \
    "v_mov_b32 v254, v254\n\t" \
    "s_nop 2\n\t" \
    "v_mov_b32 v254, v254\n\t" \
    "s_nop 2\n\t" \
    "LW_%=:\n\t" \
    "ds_read_b32 v0, v18 offset:32\n\t" \
    "s_sub_u32 %[lim], %[lim], " STR(DEC1) "\n\t" \
    "s_cmp_gt_i32 %[lim], 0\n\t" \
    "v_mov_b32 v254, v254\n\t" \
    "s_nop 2\n\t" \
    "v_mov_b32 v254, v254\n\t" \
    "s_nop 2\n\t" \
    "v_mov_b32 v254, v254\n\t" \
    "s_nop 2\n\t" \
    "v_mov_b32 v254, v254\n\t" \
    "s_nop 2\n\t" \
    "v_mov_b32 v254, v254\n\t" \
    "s_nop 2\n\t" \
    "s_waitcnt lgkmcnt(0)\n\t" \
    "s_cbranch_scc0 CW_%=\n\t" \
    "v_cmp_ne_u32 vcc, v0, v253\n\t" \
    "s_cbranch_vccnz LW_%=\n\t" \
    "CW_%=:\n\t" \
    "ds_read_b128 v[212:215], v12 offset:24576\n\t" \
    "ds_read_b128 v[216:219], v12 offset:28672\n\t" \
    "ds_read_b128 v[220:223], v15 offset:0\n\t" \
    "ds_read_b128 v[224:227], v12 offset:25600\n\t" \
    "ds_read_b128 v[228:231], v12 offset:29696\n\t" \
    "v_add_u32 v253, %[se], v12\n\t" \
    "v_mov_b32 v254, v254\n\t" \
    "s_nop 2\n\t" \
    "v_mov_b32 v254, v254\n\t" \
    "s_nop 2\n\t" \
    "v_mov_b32 v254, v254\n\t" \
    "s_nop 2\n\t" \
    "v_mov_b32 v254, v254\n\t" \
    "s_nop 2\n\t" \
    "s_waitcnt vmcnt(0)\n\t" \
    "s_waitcnt lgkmcnt(4)\n\t" \
    "v_mfma_f32_16x16x32_bf16 v[0:3], v[212:215], v[236:239], 0\n\t" \
    "ds_read_b128 v[212:215], v15 offset:512\n\t" \
    "s_waitcnt lgkmcnt(4)\n\t" \
    "v_mfma_f32_16x16x32_bf16 v[4:7], v[216:219], v[236:239], 0\n\t" \
    "ds_read_b128 v[216:219], v12 offset:26624\n\t" \
    "s_waitcnt lgkmcnt(4)\n\t" \
    "v_mfma_f32_16x16x32_bf16 v[232:235], v[220:223], v[236:239], 0\n\t" \
    "ds_read_b128 v[220:223], v12 offset:30720\n\t" \
    "s_waitcnt lgkmcnt(4)\n\t" \
    "v_mfma_f32_16x16x32_bf16 v[0:3], v[224:227], v[240:243], v[0:3]\n\t" \
    "ds_read_b128 v[224:227], v15 offset:1024\n\t" \
    "s_waitcnt lgkmcnt(4)\n\t" \
    "v_mfma_f32_16x16x32_bf16 v[4:7], v[228:231], v[240:243], v[4:7]\n\t" \
    "ds_read_b128 v[228:231], v12 offset:27648\n\t" \
    "s_waitcnt lgkmcnt(4)\n\t" \
    "v_mfma_f32_16x16x32_bf16 v[232:235], v[212:215], v[240:243], v[232:235]\n\t" \
    "ds_read_b128 v[212:215], v12 offset:31744\n\t" \
    "s_waitcnt lgkmcnt(4)\n\t" \
    "v_mfma_f32_16x16x32_bf16 v[0:3], v[216:219], v[244:247], v[0:3]\n\t" \
    "ds_read_b128 v[216:219], v15 offset:1536\n\t" \
    "s_waitcnt lgkmcnt(4)\n\t" \
    "v_mfma_f32_16x16x32_bf16 v[4:7], v[220:223], v[244:247], v[4:7]\n\t" \
    "s_waitcnt lgkmcnt(3)\n\t" \
    "v_mfma_f32_16x16x32_bf16 v[232:235], v[224:227], v[244:247], v[232:235]\n\t" \
    "s_waitcnt lgkmcnt(2)\n\t" \
    "v_mfma_f32_16x16x32_bf16 v[0:3], v[228:231], v[248:251], v[0:3]\n\t" \
    "s_waitcnt lgkmcnt(1)\n\t" \
    "v_mfma_f32_16x16x32_bf16 v[4:7], v[212:215], v[248:251], v[4:7]\n\t" \
    "s_waitcnt lgkmcnt(0)\n\t" \
    "v_mfma_f32_16x16x32_bf16 v[232:235], v[216:219], v[248:251], v[232:235]\n\t" \
    "s_barrier\n\t" \
    GSA(4) \
    "ds_read_b128 v[236:239], v253 offset:8192\n\t" \
    "ds_read_b128 v[240:243], v253 offset:9216\n\t" \
    "ds_read_b128 v[244:247], v253 offset:10240\n\t" \
    "ds_read_b128 v[248:251], v253 offset:11264\n\t" \
    "ds_read2st64_b32 v[228:229], v17 offset0:0 offset1:1\n\t" \
    "ds_read2st64_b32 v[230:231], v17 offset0:2 offset1:3\n\t" \
    "s_mov_b64 exec, %[fm]\n\t" \
    "v_mov_b32 v220, %[st]\n\t" \
    "v_mov_b32 v221, %[tag]\n\t" \
    "v_mov_b32 v222, 0\n\t" \
    "buffer_store_dwordx2 v[220:221], v222, %[rs], %[po] offen sc1\n\t" \
    "s_mov_b64 exec, -1\n\t" \
    "v_mov_b32 v254, 0xff800000\n\t" \
    "v_cvt_pk_bf16_f32 v0, 0, v0\n\t" \
    "v_cvt_pk_bf16_f32 v1, 0, v1\n\t" \
    "v_cvt_pk_bf16_f32 v2, 0, v2\n\t" \
    "v_cvt_pk_bf16_f32 v3, 0, v3\n\t" \
    "v_cvt_pk_bf16_f32 v4, 0, v4\n\t" \
    "v_cvt_pk_bf16_f32 v5, 0, v5\n\t" \
    "v_cvt_pk_bf16_f32 v6, 0, v6\n\t" \
    "v_cvt_pk_bf16_f32 v7, 0, v7\n\t" \
    "v_cvt_pk_bf16_f32 v232, 0, v232\n\t" \
    "v_cvt_pk_bf16_f32 v233, 0, v233\n\t" \
    "v_mul_f32 v0, 0x3db504f3, v0\n\t" \
    "v_mul_f32 v1, 0x3db504f3, v1\n\t" \
    "v_mul_f32 v2, 0x3db504f3, v2\n\t" \
    "v_mul_f32 v3, 0x3db504f3, v3\n\t" \
    "v_mul_f32 v4, 0x3db504f3, v4\n\t" \
    "v_mul_f32 v5, 0x3db504f3, v5\n\t" \
    "v_mul_f32 v6, 0x3db504f3, v6\n\t" \
    "v_mul_f32 v7, 0x3db504f3, v7\n\t" \
    "v_mul_f32 v232, 0x3db504f3, v232\n\t" \
    "v_mul_f32 v233, 0x3db504f3, v233\n\t" \
    "v_cvt_pk_bf16_f32 v0, 0, v0\n\t" \
    "v_cvt_pk_bf16_f32 v1, 0, v1\n\t" \
    "v_cvt_pk_bf16_f32 v2, 0, v2\n\t" \
    "v_cvt_pk_bf16_f32 v3, 0, v3\n\t" \
    "v_cvt_pk_bf16_f32 v4, 0, v4\n\t" \
    "v_cvt_pk_bf16_f32 v5, 0, v5\n\t" \
    "v_cvt_pk_bf16_f32 v6, 0, v6\n\t" \
    "v_cvt_pk_bf16_f32 v7, 0, v7\n\t" \
    "v_cvt_pk_bf16_f32 v232, 0, v232\n\t" \
    "v_cvt_pk_bf16_f32 v233, 0, v233\n\t" \
    "v_cndmask_b32 v0, v254, v0, s[72:73]\n\t" \
    "v_cndmask_b32 v1, v254, v1, s[74:75]\n\t" \
    "v_cndmask_b32 v2, v254, v2, s[76:77]\n\t" \
    "v_cndmask_b32 v3, v254, v3, s[78:79]\n\t" \
    "v_cndmask_b32 v4, v254, v4, s[80:81]\n\t" \
    "v_cndmask_b32 v5, v254, v5, s[82:83]\n\t" \
    "v_cndmask_b32 v6, v254, v6, s[84:85]\n\t" \
    "v_cndmask_b32 v7, v254, v7, s[86:87]\n\t" \
    "v_max3_f32 v252, v0, v1, v2\n\t" \
    "v_max3_f32 v255, v3, v4, v5\n\t" \
    "v_max3_f32 v254, v6, v7, v232\n\t" \
    "v_max3_f32 v252, v252, v255, v254\n\t" \
    "v_max_f32 v252, v252, v233\n\t" \
    "v_mov_b32 v255, v252\n\t" \
    "v_mov_b32 v254, v252\n\t" \
    "s_nop 1\n\t" \
    "v_permlane16_swap_b32 v255, v254\n\t" \
    "v_max_f32 v252, v255, v254\n\t" \
    "v_max_f32 v255, v255, v254\n\t" \
    "s_nop 1\n\t" \
    "v_permlane32_swap_b32 v252, v255\n\t" \
    "v_max_f32 v252, v252, v255\n\t" \
    "v_mul_f32 v252, 0xbfb8aa3b, v252\n\t" \
    "v_fma_f32 v0, v0, %[l2e], v252\n\t" \
    "v_fma_f32 v1, v1, %[l2e], v252\n\t" \
    "v_fma_f32 v2, v2, %[l2e], v252\n\t" \
    "v_fma_f32 v3, v3, %[l2e], v252\n\t" \
    "v_fma_f32 v4, v4, %[l2e], v252\n\t" \
    "v_fma_f32 v5, v5, %[l2e], v252\n\t" \
    "v_fma_f32 v6, v6, %[l2e], v252\n\t" \
    "v_fma_f32 v7, v7, %[l2e], v252\n\t" \
    "v_fma_f32 v232, v232, %[l2e], v252\n\t" \
    "v_fma_f32 v233, v233, %[l2e], v252\n\t" \
    "v_exp_f32 v0, v0\n\t" \
    "v_exp_f32 v1, v1\n\t" \
    "v_exp_f32 v2, v2\n\t" \
    "v_exp_f32 v3, v3\n\t" \
    "v_exp_f32 v4, v4\n\t" \
    "v_exp_f32 v5, v5\n\t" \
    "v_exp_f32 v6, v6\n\t" \
    "v_exp_f32 v7, v7\n\t" \
    "v_exp_f32 v232, v232\n\t" \
    "v_exp_f32 v233, v233\n\t" \
    "v_add_f32 v252, v0, v1\n\t" \
    "v_add_f32 v235, v2, v3\n\t" \
    "v_add_f32 v254, v4, v5\n\t" \
    "v_add_f32 v255, v6, v7\n\t" \
    "v_add_f32 v234, v232, v233\n\t" \
    "v_add_f32 v252, v252, v235\n\t" \
    "v_add_f32 v254, v254, v255\n\t" \
    "v_add_f32 v252, v252, v254\n\t" \
    "v_add_f32 v252, v252, v234\n\t" \
    "v_mov_b32 v255, v252\n\t" \
    "v_mov_b32 v254, v252\n\t" \
    "s_nop 1\n\t" \
    "v_permlane16_swap_b32 v255, v254\n\t" \
    "v_add_f32 v252, v255, v254\n\t" \
    "v_add_f32 v255, v255, v254\n\t" \
    "s_nop 1\n\t" \
    "v_permlane32_swap_b32 v252, v255\n\t" \
    "v_add_f32 v252, v252, v255\n\t" \
    "v_rcp_f32 v252, v252\n\t" \
    "s_nop 0\n\t" \
    "v_mul_f32 v0, v0, v252\n\t" \
    "v_mul_f32 v1, v1, v252\n\t" \
    "v_mul_f32 v2, v2, v252\n\t" \
    "v_mul_f32 v3, v3, v252\n\t" \
    "v_mul_f32 v4, v4, v252\n\t" \
    "v_mul_f32 v5, v5, v252\n\t" \
    "v_mul_f32 v6, v6, v252\n\t" \
    "v_mul_f32 v7, v7, v252\n\t" \
    "v_mul_f32 v232, v232, v252\n\t" \
    "v_mul_f32 v233, v233, v252\n\t" \
    "v_cvt_pk_bf16_f32 v0, v0, v1\n\t" \
    "v_cvt_pk_bf16_f32 v1, v2, v3\n\t" \
    "v_cvt_pk_bf16_f32 v2, v4, v5\n\t" \
    "v_cvt_pk_bf16_f32 v3, v6, v7\n\t" \
    "v_cvt_pk_bf16_f32 v4, v232, v233\n\t" \
    "v_mov_b32 v5, 0\n\t" \
    "v_mov_b32 v6, 0\n\t" \
    "v_cvt_pk_bf16_f32 v7, v232, v233\n\t" \
    "s_waitcnt lgkmcnt(0)\n\t" \
    "v_mfma_f32_16x16x32_bf16 v[236:239], v[236:239], v[0:3], 0\n\t" \
    "v_mfma_f32_16x16x32_bf16 v[240:243], v[240:243], v[0:3], 0\n\t" \
    "v_mfma_f32_16x16x32_bf16 v[244:247], v[244:247], v[0:3], 0\n\t" \
    "v_mfma_f32_16x16x32_bf16 v[248:251], v[248:251], v[0:3], 0\n\t" \
    "v_mfma_f32_16x16x16_bf16 v[236:239], v[228:229], v[4:5], v[236:239]\n\t" \
    "v_mfma_f32_16x16x16_bf16 v[240:243], v[228:229], v[6:7], v[240:243]\n\t" \
    "v_mfma_f32_16x16x16_bf16 v[244:247], v[230:231], v[4:5], v[244:247]\n\t" \
    "v_mfma_f32_16x16x16_bf16 v[248:251], v[230:231], v[6:7], v[248:251]\n\t" \
    "s_nop 4\n\t" \
    "v_cvt_pk_bf16_f32 v212, v236, v237\n\t" \
    "v_cvt_pk_bf16_f32 v213, v240, v241\n\t" \
    "v_cvt_pk_bf16_f32 v214, v238, v239\n\t" \
    "v_cvt_pk_bf16_f32 v215, v242, v243\n\t" \
    "v_permlane16_swap_b32 v212, v213\n\t" \
    "s_nop 0\n\t" \
    "v_permlane16_swap_b32 v214, v215\n\t" \
    "v_cvt_pk_bf16_f32 v216, v244, v245\n\t" \
    "v_cvt_pk_bf16_f32 v217, v248, v249\n\t" \
    "v_cvt_pk_bf16_f32 v218, v246, v247\n\t" \
    "v_cvt_pk_bf16_f32 v219, v250, v251\n\t" \
    "v_permlane16_swap_b32 v216, v217\n\t" \
    "s_nop 0\n\t" \
    "v_permlane16_swap_b32 v218, v219\n\t" \
    "s_setprio 1\n\t" \
    GSA(2) \
    GSW \
    "s_nop 1\n\t" \
    : [lim] "+s"(lim), "+{v[212:219]}"(oxr) DGO GSO(0) GSO(1) GSO(2) GSO(3) GSO(4) \
    : "{v8}"(am_a), "{v9}"(t0_a), "{v10}"(idx), "{v11}"(iw_a), "{v12}"(ab_a), "{v13}"(sw_a), "{v14}"(xo_v), "{v15}"(kr_a), "{v16}"(dq), "{v17}"(vl_a), "{v18}"(fr_a), [stag] "s"(0xffffff81u + s), [onej] "s"(onej), [rs] "s"(rs), [m0] "s"(sbm + 16384u), [m1] "s"(sbm + 20480u), [st] "s"(s), [tag] "s"(P.tag), [po] "s"(FLG_OFF + F_PRG(4, h) * 128), [fm] "s"(fmask), [qx] "s"(qxo), [qd] "s"(lbase + LA_VM), [qh] "s"((j & 1) * 2048), [ox] "s"(oxo), [se] "s"((j & 1) * 4096), [l2e] "s"(0x3fb8aa3b), "{v[20:27]}"(wr[0][0]), "{v[28:35]}"(wr[0][1]), "{v[36:43]}"(wr[1][0]), "{v[44:51]}"(wr[1][1]), "{v[52:59]}"(wr[2][0]), "{v[60:67]}"(wr[2][1]), "{v[68:75]}"(wr[3][0]), "{v[76:83]}"(wr[3][1]), "{v[84:91]}"(wr[4][0]), "{v[92:99]}"(wr[4][1]), "{v[100:107]}"(wr[5][0]), "{v[108:115]}"(wr[5][1]), "{v[116:123]}"(wr[6][0]), "{v[124:131]}"(wr[6][1]), "{v[132:139]}"(wr[7][0]), "{v[140:147]}"(wr[7][1]), "{v[148:155]}"(wr[8][0]), "{v[156:163]}"(wr[8][1]), "{v[164:171]}"(wr[9][0]), "{v[172:179]}"(wr[9][1]), "{v[180:187]}"(wr[10][0]), "{v[188:195]}"(wr[10][1]), "{v[196:203]}"(wr[11][0]), "{v[204:211]}"(wr[11][1]) \
    : "memory", "scc", "vcc", "s90", "s91", "s72", "s73", "s74", "s75", "s76", "s77", "s78", "s79", "s80", "s81", "s82", "s83", "s84", "s85", "s86", "s87", "v0", "v1", "v2", "v3", "v4", "v5", "v6", "v7", "v220", "v221", "v222", "v223", "v224", "v225", "v226", "v227", "v228", "v229", "v230", "v231", "v232", "v233", "v234", "v235", "v236", "v237", "v238", "v239", "v240", "v241", "v242", "v243", "v244", "v245", "v246", "v247", "v248", "v249", "v250", "v251", "v252", "v253", "v254", "v255")

#define B_POLL() \
  asm volatile( \
    "s_getpc_b64 s[92:93]\n\t" \
    "TPB_%=:\n\t" \
    "s_add_u32 s92, s92, TEB_%=-TPB_%=\n\t" \
    "s_addc_u32 s93, s93, 0\n\t" \
    "s_lshr_b32 s91, %[tgt], 2\n\t" \
    "s_sub_u32 s91, s91, 3\n\t" \
    "TWB_%=:\n\t" \
    "s_memrealtime s[88:89]\n\t" \
    DGE \
    "s_waitcnt lgkmcnt(0)\n\t" \
    "s_sub_u32 s89, s91, s88\n\t" \
    "s_lshl_b32 s89, s89, 2\n\t" \
    "s_cmp_gt_i32 s89, 0\n\t" \
    "s_cbranch_scc1 TWB_%=\n\t" \
    "s_lshl_b32 s88, s88, 2\n\t" \
    "s_sub_u32 s89, %[tgt], s88\n\t" \
    "s_max_i32 s89, s89, 0\n\t" \
    "s_mul_i32 s89, s89, 11\n\t" \
    "s_lshr_b32 s89, s89, 1\n\t" \
    "s_and_b32 s89, s89, 0x7c\n\t" \
    "s_sub_u32 s92, s92, s89\n\t" \
    "s_subb_u32 s93, s93, 0\n\t" \
    "s_setpc_b64 s[92:93]\n\t" \
    "s_nop 0\n\t" \
    "s_nop 0\n\t" \
    "s_nop 0\n\t" \
    "s_nop 0\n\t" \
    "s_nop 0\n\t" \
    "s_nop 0\n\t" \
    "s_nop 0\n\t" \
    "s_nop 0\n\t" \
    "s_nop 0\n\t" \
    "s_nop 0\n\t" \
    "s_nop 0\n\t" \
    "s_nop 0\n\t" \
    "s_nop 0\n\t" \
    "s_nop 0\n\t" \
    "s_nop 0\n\t" \
    "s_nop 0\n\t" \
    "s_nop 0\n\t" \
    "s_nop 0\n\t" \
    "s_nop 0\n\t" \
    "s_nop 0\n\t" \
    "s_nop 0\n\t" \
    "s_nop 0\n\t" \
    "TEB_%=:\n\t" \
    "buffer_load_dwordx4 v[220:223], %[l16], %[rs], %[m0] offen offset:0 sc1\n\t" \
    "buffer_load_dwordx4 v[224:227], %[l16], %[rs], %[m0] offen offset:1024 sc1\n\t" \
    "buffer_load_dwordx4 v[228:231], %[l16], %[rs], %[m1] offen offset:0 sc1\n\t" \
    "buffer_load_dwordx4 v[232:235], %[l16], %[rs], %[m1] offen offset:1024 sc1\n\t" \
    "v_mov_b32 v252, v252\n\t" \
    "s_nop 2\n\t" \
    "v_mov_b32 v252, v252\n\t" \
    "s_nop 2\n\t" \
    "v_mov_b32 v252, v252\n\t" \
    "s_nop 2\n\t" \
    "v_mov_b32 v252, v252\n\t" \
    "s_nop 2\n\t" \
    "v_mov_b32 v252, v252\n\t" \
    "s_nop 2\n\t" \
    "v_mov_b32 v252, v252\n\t" \
    "s_nop 2\n\t" \
    "v_mov_b32 v252, v252\n\t" \
    "s_nop 2\n\t" \
    "v_mov_b32 v252, v252\n\t" \
    "s_nop 2\n\t" \
    "v_mov_b32 v252, v252\n\t" \
    "s_nop 2\n\t" \
    "v_mov_b32 v252, v252\n\t" \
    "s_nop 2\n\t" \
    "v_mov_b32 v252, v252\n\t" \
    "s_nop 2\n\t" \
    "v_mov_b32 v252, v252\n\t" \
    "s_nop 2\n\t" \
    "v_mov_b32 v252, v252\n\t" \
    "s_nop 2\n\t" \
    "v_mov_b32 v252, v252\n\t" \
    "s_nop 2\n\t" \
    "v_mov_b32 v252, v252\n\t" \
    "s_nop 2\n\t" \
    "v_mov_b32 v252, v252\n\t" \
    "s_nop 2\n\t" \
    "v_mov_b32 v252, v252\n\t" \
    "s_nop 2\n\t" \
    "v_mov_b32 v252, v252\n\t" \
    "s_nop 2\n\t" \
    "v_mov_b32 v252, v252\n\t" \
    "s_nop 2\n\t" \
    "v_mov_b32 v252, v252\n\t" \
    "s_nop 2\n\t" \
    "v_mov_b32 v252, v252\n\t" \
    "s_nop 2\n\t" \
    "v_mov_b32 v252, v252\n\t" \
    "s_nop 2\n\t" \
    "v_mov_b32 v252, v252\n\t" \
    "s_nop 2\n\t" \
    "v_mov_b32 v252, v252\n\t" \
    "s_nop 2\n\t" \
    "PM_%=:\n\t" \
    "s_waitcnt vmcnt(3)\n\t" \
    "ds_write_b128 %[la], v[220:223] offset:0\n\t" \
    "v_max3_u32 v252, v220, v220, v221\n\t" \
    "v_max3_u32 v252, v252, v222, v223\n\t" \
    "s_waitcnt vmcnt(2)\n\t" \
    "ds_write_b128 %[la], v[224:227] offset:1024\n\t" \
    "v_max3_u32 v252, v252, v224, v225\n\t" \
    "v_max3_u32 v252, v252, v226, v227\n\t" \
    "s_waitcnt vmcnt(1)\n\t" \
    "ds_write_b128 %[la], v[228:231] offset:8192\n\t" \
    "v_max3_u32 v252, v252, v228, v229\n\t" \
    "v_max3_u32 v252, v252, v230, v231\n\t" \
    "s_waitcnt vmcnt(0)\n\t" \
    "ds_write_b128 %[la], v[232:235] offset:9216\n\t" \
    "v_max3_u32 v252, v252, v232, v233\n\t" \
    "v_max3_u32 v252, v252, v234, v235\n\t" \
    "v_cmp_eq_u32 vcc, -1, v252\n\t" \
    "s_cbranch_vccz PD_%=\n\t" \
    "s_sub_u32 %[lim], %[lim], 1\n\t" \
    "s_cmp_gt_i32 %[lim], 0\n\t" \
    "s_cbranch_scc0 PD_%=\n\t" \
    "s_sleep " STR(CADM) "\n\t" \
    "buffer_load_dwordx4 v[220:223], %[l16], %[rs], %[m0] offen offset:0 sc1\n\t" \
    "buffer_load_dwordx4 v[224:227], %[l16], %[rs], %[m0] offen offset:1024 sc1\n\t" \
    "buffer_load_dwordx4 v[228:231], %[l16], %[rs], %[m1] offen offset:0 sc1\n\t" \
    "buffer_load_dwordx4 v[232:235], %[l16], %[rs], %[m1] offen offset:1024 sc1\n\t" \
    "s_branch PM_%=\n\t" \
    "PD_%=:\n\t" \
    GSA(3) \
    "s_waitcnt lgkmcnt(0)\n\t" \
    "s_barrier\n\t" \
    : [lim] "+s"(lim) GSO(3) \
    : [l16] "v"(l16), [la] "v"(la), [rs] "s"(rs), [m0] "s"(mo), [m1] "s"(mo + 16384u), [tgt] "s"(tgt) \
    : "memory", "vcc", "scc", "v252", "s88", "s89", "s90", "s91", "s92", "s93", "s94", "s95", "v220", "v221", "v222", "v223", "v224", "v225", "v226", "v227", "v228", "v229", "v230", "v231", "v232", "v233", "v234", "v235")

#define T_WAIT() \
  asm volatile( \
    "s_getpc_b64 s[92:93]\n\t" \
    "TPC_%=:\n\t" \
    "s_add_u32 s92, s92, TEC_%=-TPC_%=\n\t" \
    "s_addc_u32 s93, s93, 0\n\t" \
    "s_lshr_b32 s91, %[tgt], 2\n\t" \
    "s_sub_u32 s91, s91, 3\n\t" \
    "TWC_%=:\n\t" \
    "s_memrealtime s[88:89]\n\t" \
    DGE \
    "s_waitcnt lgkmcnt(0)\n\t" \
    "s_sub_u32 s89, s91, s88\n\t" \
    "s_lshl_b32 s89, s89, 2\n\t" \
    "s_cmp_gt_i32 s89, 0\n\t" \
    "s_cbranch_scc1 TWC_%=\n\t" \
    "s_lshl_b32 s88, s88, 2\n\t" \
    "s_sub_u32 s89, %[tgt], s88\n\t" \
    "s_max_i32 s89, s89, 0\n\t" \
    "s_mul_i32 s89, s89, 11\n\t" \
    "s_lshr_b32 s89, s89, 1\n\t" \
    "s_and_b32 s89, s89, 0x7c\n\t" \
    "s_sub_u32 s92, s92, s89\n\t" \
    "s_subb_u32 s93, s93, 0\n\t" \
    "s_setpc_b64 s[92:93]\n\t" \
    "s_nop 0\n\t" \
    "s_nop 0\n\t" \
    "s_nop 0\n\t" \
    "s_nop 0\n\t" \
    "s_nop 0\n\t" \
    "s_nop 0\n\t" \
    "s_nop 0\n\t" \
    "s_nop 0\n\t" \
    "s_nop 0\n\t" \
    "s_nop 0\n\t" \
    "s_nop 0\n\t" \
    "s_nop 0\n\t" \
    "s_nop 0\n\t" \
    "s_nop 0\n\t" \
    "s_nop 0\n\t" \
    "s_nop 0\n\t" \
    "s_nop 0\n\t" \
    "s_nop 0\n\t" \
    "s_nop 0\n\t" \
    "s_nop 0\n\t" \
    "s_nop 0\n\t" \
    "s_nop 0\n\t" \
    "TEC_%=:\n\t" \
    :  \
    : [tgt] "s"(tgt) \
    : "s88", "s89", "s90", "s91", "s92", "s93", "s94", "s95", "memory", "scc")

#define V_PROJ() \
  asm volatile( \
    "v_mov_b32 v0, 0\n\t" \
    "v_mov_b32 v1, 0\n\t" \
    "v_mov_b32 v2, 0\n\t" \
    "v_mov_b32 v3, 0\n\t" \
    "v_mov_b32 v4, 0\n\t" \
    "v_mov_b32 v5, 0\n\t" \
    "v_mov_b32 v6, 0\n\t" \
    "v_mov_b32 v7, 0\n\t" \
    "s_barrier\n\t" \
    "ds_read_b128 v[220:223], %[am] offset:0\n\t" \
    "ds_read_b128 v[224:227], %[am] offset:1024\n\t" \
    "ds_read_b128 v[228:231], %[am] offset:2048\n\t" \
    "ds_read_b128 v[232:235], %[am] offset:3072\n\t" \
    "ds_read_b128 v[236:239], %[am] offset:4096\n\t" \
    "ds_read_b128 v[240:243], %[am] offset:5120\n\t" \
    "ds_read_b128 v[244:247], %[am] offset:6144\n\t" \
    "ds_read_b128 v[248:251], %[am] offset:7168\n\t" \
    "s_waitcnt lgkmcnt(7)\n\t" \
    "v_smfmac_f32_16x16x64_bf16 v[0:3], v[220:223], v[28:35], %[idx]\n\t" \
    "ds_read_b128 v[220:223], %[am] offset:8192\n\t" \
    "s_waitcnt lgkmcnt(7)\n\t" \
    "v_smfmac_f32_16x16x64_bf16 v[4:7], v[224:227], v[36:43], %[idx]\n\t" \
    "ds_read_b128 v[224:227], %[am] offset:9216\n\t" \
    "s_waitcnt lgkmcnt(7)\n\t" \
    "v_smfmac_f32_16x16x64_bf16 v[0:3], v[228:231], v[44:51], %[idx]\n\t" \
    "ds_read_b128 v[228:231], %[am] offset:10240\n\t" \
    "s_waitcnt lgkmcnt(7)\n\t" \
    "v_smfmac_f32_16x16x64_bf16 v[4:7], v[232:235], v[52:59], %[idx]\n\t" \
    "ds_read_b128 v[232:235], %[am] offset:11264\n\t" \
    "s_waitcnt lgkmcnt(7)\n\t" \
    "v_smfmac_f32_16x16x64_bf16 v[0:3], v[236:239], v[60:67], %[idx]\n\t" \
    "ds_read_b128 v[236:239], %[am] offset:12288\n\t" \
    "s_waitcnt lgkmcnt(7)\n\t" \
    "v_smfmac_f32_16x16x64_bf16 v[4:7], v[240:243], v[68:75], %[idx]\n\t" \
    "ds_read_b128 v[240:243], %[am] offset:13312\n\t" \
    "s_waitcnt lgkmcnt(7)\n\t" \
    "v_smfmac_f32_16x16x64_bf16 v[0:3], v[244:247], v[76:83], %[idx]\n\t" \
    "ds_read_b128 v[244:247], %[am] offset:14336\n\t" \
    "s_waitcnt lgkmcnt(7)\n\t" \
    "v_smfmac_f32_16x16x64_bf16 v[4:7], v[248:251], v[84:91], %[idx]\n\t" \
    "ds_read_b128 v[248:251], %[am] offset:15360\n\t" \
    "s_waitcnt lgkmcnt(7)\n\t" \
    "v_smfmac_f32_16x16x64_bf16 v[0:3], v[220:223], v[92:99], %[idx]\n\t" \
    "s_waitcnt lgkmcnt(6)\n\t" \
    "v_smfmac_f32_16x16x64_bf16 v[4:7], v[224:227], v[100:107], %[idx]\n\t" \
    "s_waitcnt lgkmcnt(5)\n\t" \
    "v_smfmac_f32_16x16x64_bf16 v[0:3], v[228:231], v[108:115], %[idx]\n\t" \
    "s_waitcnt lgkmcnt(4)\n\t" \
    "v_smfmac_f32_16x16x64_bf16 v[4:7], v[232:235], v[116:123], %[idx]\n\t" \
    "s_waitcnt lgkmcnt(3)\n\t" \
    "v_smfmac_f32_16x16x64_bf16 v[0:3], v[236:239], v[124:131], %[idx]\n\t" \
    "s_waitcnt lgkmcnt(2)\n\t" \
    "v_smfmac_f32_16x16x64_bf16 v[4:7], v[240:243], v[132:139], %[idx]\n\t" \
    "s_waitcnt lgkmcnt(1)\n\t" \
    "v_smfmac_f32_16x16x64_bf16 v[0:3], v[244:247], v[140:147], %[idx]\n\t" \
    "s_waitcnt lgkmcnt(0)\n\t" \
    "v_smfmac_f32_16x16x64_bf16 v[4:7], v[248:251], v[148:155], %[idx]\n\t" \
    "s_nop 5\n\t" \
    "v_add_f32 v0, v0, v1\n\t" \
    "v_add_f32 v2, v2, v3\n\t" \
    "v_add_f32 v4, v4, v5\n\t" \
    "v_add_f32 v6, v6, v7\n\t" \
    "v_add_f32 v0, v0, v4\n\t" \
    "v_add_f32 v2, v2, v6\n\t" \
    "v_cvt_pk_bf16_f32 v0, v0, v2\n\t" \
    "v_min_u32 v0, -2, v0\n\t" \
    "s_nop 1\n\t" \
    "v_mov_b32_dpp v1, v0 quad_perm:[1,1,1,1] row_mask:0xf bank_mask:0xf\n\t" \
    "v_mov_b32_dpp v2, v0 quad_perm:[2,2,2,2] row_mask:0xf bank_mask:0xf\n\t" \
    "v_mov_b32_dpp v3, v0 quad_perm:[3,3,3,3] row_mask:0xf bank_mask:0xf\n\t" \
    "s_mov_b64 exec, %[qm]\n\t" \
    "buffer_store_dwordx4 v[0:3], %[l4], %[rs], %[vo] offen sc1\n\t" \
    "s_mov_b64 exec, -1\n\t" \
    "s_nop 1\n\t" \
    :  \
    : [am] "v"(am_a), [idx] "v"(idx), [l4] "v"(ln * 4), [rs] "s"(rs), [vo] "s"(sb + vo), [qm] "s"(0x1111111111111111ull), "{v[28:35]}"(wv[0]), "{v[36:43]}"(wv[1]), "{v[44:51]}"(wv[2]), "{v[52:59]}"(wv[3]), "{v[60:67]}"(wv[4]), "{v[68:75]}"(wv[5]), "{v[76:83]}"(wv[6]), "{v[84:91]}"(wv[7]), "{v[92:99]}"(wv[8]), "{v[100:107]}"(wv[9]), "{v[108:115]}"(wv[10]), "{v[116:123]}"(wv[11]), "{v[124:131]}"(wv[12]), "{v[132:139]}"(wv[13]), "{v[140:147]}"(wv[14]), "{v[148:155]}"(wv[15]) \
    : "memory", "v0", "v1", "v2", "v3", "v4", "v5", "v6", "v7", "v220", "v221", "v222", "v223", "v224", "v225", "v226", "v227", "v228", "v229", "v230", "v231", "v232", "v233", "v234", "v235", "v236", "v237", "v238", "v239", "v240", "v241", "v242", "v243", "v244", "v245", "v246", "v247", "v248", "v249", "v250", "v251")

#define WO_V() \
  asm volatile( \
    "v_mov_b32 v0, 0\n\t" \
    "v_mov_b32 v1, 0\n\t" \
    "v_mov_b32 v2, 0\n\t" \
    "v_mov_b32 v3, 0\n\t" \
    "v_mov_b32 v4, 0\n\t" \
    "v_mov_b32 v5, 0\n\t" \
    "v_mov_b32 v6, 0\n\t" \
    "v_mov_b32 v7, 0\n\t" \
    "v_mov_b32 v8, 0\n\t" \
    "v_mov_b32 v9, 0\n\t" \
    "v_mov_b32 v10, 0\n\t" \
    "v_mov_b32 v11, 0\n\t" \
    "v_mov_b32 v12, 0\n\t" \
    "v_mov_b32 v13, 0\n\t" \
    "v_mov_b32 v14, 0\n\t" \
    "v_mov_b32 v15, 0\n\t" \
    "v_mov_b32 v252, 0\n\t" \
    "s_barrier\n\t" \
    GSA(4) \
    DGC \
    "buffer_load_dwordx4 v[220:223], %[l16], %[rs], %[s0] offen sc1\n\t" \
    "s_nop 7\n\t" \
    "buffer_load_dwordx4 v[224:227], %[l16], %[rs], %[s1] offen sc1\n\t" \
    "A_%=:\n\t" \
    "s_waitcnt vmcnt(1)\n\t" \
    "v_smfmac_f32_16x16x64_bf16 v[0:3], v[220:223], v[156:163], %[idx]\n\t" \
    "v_max3_u32 v252, v252, v220, v221\n\t" \
    "v_smfmac_f32_16x16x64_bf16 v[4:7], v[220:223], v[164:171], %[idx]\n\t" \
    "v_max3_u32 v252, v252, v222, v223\n\t" \
    "v_smfmac_f32_16x16x64_bf16 v[8:11], v[220:223], v[172:179], %[idx]\n\t" \
    "v_smfmac_f32_16x16x64_bf16 v[12:15], v[220:223], v[180:187], %[idx]\n\t" \
    "s_waitcnt vmcnt(0)\n\t" \
    "v_smfmac_f32_16x16x64_bf16 v[0:3], v[224:227], v[188:195], %[idx]\n\t" \
    "v_max3_u32 v252, v252, v224, v225\n\t" \
    "v_smfmac_f32_16x16x64_bf16 v[4:7], v[224:227], v[196:203], %[idx]\n\t" \
    "v_max3_u32 v252, v252, v226, v227\n\t" \
    "v_smfmac_f32_16x16x64_bf16 v[8:11], v[224:227], v[204:211], %[idx]\n\t" \
    "v_smfmac_f32_16x16x64_bf16 v[12:15], v[224:227], v[212:219], %[idx]\n\t" \
    "s_setprio 0\n\t" \
    "v_cmp_eq_u32 vcc, -1, v252\n\t" \
    "s_cbranch_vccnz SL_%=\n\t" \
    "D_%=:\n\t" \
    GSA(1) \
    "s_nop 2\n\t" \
    "v_add_f32 v0, v0, v1\n\t" \
    "v_add_f32 v2, v2, v3\n\t" \
    "v_add_f32 v4, v4, v5\n\t" \
    "v_add_f32 v6, v6, v7\n\t" \
    "v_add_f32 v8, v8, v9\n\t" \
    "v_add_f32 v10, v10, v11\n\t" \
    "v_add_f32 v12, v12, v13\n\t" \
    "v_add_f32 v14, v14, v15\n\t" \
    "v_permlane32_swap_b32 v0, v4\n\t" \
    "v_permlane32_swap_b32 v2, v6\n\t" \
    "v_permlane32_swap_b32 v8, v12\n\t" \
    "v_permlane32_swap_b32 v10, v14\n\t" \
    "v_add_f32 v0, v0, v12\n\t" \
    "v_add_f32 v2, v2, v14\n\t" \
    "ds_write2_b32 %[rw], v0, v2 offset0:0 offset1:32\n\t" \
    "s_setprio 2\n\t" \
    "s_waitcnt lgkmcnt(0)\n\t" \
    "s_barrier\n\t" \
    "s_cmp_eq_u32 %[red], 0\n\t" \
    "s_cbranch_scc0 E_%=\n\t" \
    "ds_read2st64_b64 v[0:3], %[rr] offset0:0 offset1:1\n\t" \
    "ds_read2st64_b64 v[4:7], %[rr] offset0:2 offset1:3\n\t" \
    "ds_read2st64_b64 v[8:11], %[rr] offset0:4 offset1:5\n\t" \
    "ds_read2st64_b64 v[12:15], %[rr] offset0:6 offset1:7\n\t" \
    "s_waitcnt lgkmcnt(3)\n\t" \
    "v_pk_add_f32 v[0:1], v[0:1], v[2:3]\n\t" \
    "s_waitcnt lgkmcnt(2)\n\t" \
    "v_pk_add_f32 v[4:5], v[4:5], v[6:7]\n\t" \
    "s_waitcnt lgkmcnt(1)\n\t" \
    "v_pk_add_f32 v[8:9], v[8:9], v[10:11]\n\t" \
    "v_pk_add_f32 v[0:1], v[0:1], v[4:5]\n\t" \
    "s_waitcnt lgkmcnt(0)\n\t" \
    "v_pk_add_f32 v[12:13], v[12:13], v[14:15]\n\t" \
    "s_nop 0\n\t" \
    "v_pk_add_f32 v[8:9], v[8:9], v[12:13]\n\t" \
    "s_nop 0\n\t" \
    "v_pk_add_f32 v[0:1], v[0:1], v[8:9]\n\t" \
    "s_nop 0\n\t" \
    "v_cvt_pk_bf16_f32 v0, v0, v1\n\t" \
    "v_min_u32 v0, -2, v0\n\t" \
    "buffer_store_dword v0, %[mo], %[rs], %[sn] offen sc1\n\t" \
    "s_memrealtime %[t1]\n\t" \
    DGA \
    "s_waitcnt lgkmcnt(0)\n\t" \
    GSA(2) \
    "s_branch E_%=\n\t" \
    "SL_%=:\n\t" \
    "s_sleep 1\n\t" \
    "buffer_load_dwordx4 v[220:223], %[l16], %[rs], %[s0] offen sc1\n\t" \
    "buffer_load_dwordx4 v[224:227], %[l16], %[rs], %[s1] offen sc1\n\t" \
    "s_sub_u32 %[lim], %[lim], " STR(DEC2) "\n\t" \
    "s_cmp_gt_i32 %[lim], 0\n\t" \
    "s_cbranch_scc0 G_%=\n\t" \
    "s_nop 3\n\t" \
    "v_mov_b32 v0, 0\n\t" \
    "v_mov_b32 v1, 0\n\t" \
    "v_mov_b32 v2, 0\n\t" \
    "v_mov_b32 v3, 0\n\t" \
    "v_mov_b32 v4, 0\n\t" \
    "v_mov_b32 v5, 0\n\t" \
    "v_mov_b32 v6, 0\n\t" \
    "v_mov_b32 v7, 0\n\t" \
    "v_mov_b32 v8, 0\n\t" \
    "v_mov_b32 v9, 0\n\t" \
    "v_mov_b32 v10, 0\n\t" \
    "v_mov_b32 v11, 0\n\t" \
    "v_mov_b32 v12, 0\n\t" \
    "v_mov_b32 v13, 0\n\t" \
    "v_mov_b32 v14, 0\n\t" \
    "v_mov_b32 v15, 0\n\t" \
    "v_mov_b32 v252, 0\n\t" \
    "s_branch A_%=\n\t" \
    "G_%=:\n\t" \
    "s_waitcnt vmcnt(0)\n\t" \
    "s_branch D_%=\n\t" \
    "E_%=:\n\t" \
    GSW \
    : [lim] "+s"(lim) GSO(1) GSO(4), [t1] "=&s"(t1) DGO DGQ GSO(2) \
    : [l16] "v"(l16), [rs] "s"(rs), [idx] "v"(idx), [rw] "v"(rw_a), [s0] "s"(so[0]), [s1] "s"(so[1]), [rr] "v"(rr_a), [mo] "v"(mo_v), [sn] "s"(sn), [red] "s"(wid), "{v[156:163]}"(wo[0][0][0]), "{v[164:171]}"(wo[0][0][1]), "{v[172:179]}"(wo[0][1][0]), "{v[180:187]}"(wo[0][1][1]), "{v[188:195]}"(wo[1][0][0]), "{v[196:203]}"(wo[1][0][1]), "{v[204:211]}"(wo[1][1][0]), "{v[212:219]}"(wo[1][1][1]) \
    : "memory", "vcc", "scc", "v252", "v0", "v1", "v2", "v3", "v4", "v5", "v6", "v7", "v8", "v9", "v10", "v11", "v12", "v13", "v14", "v15", "v220", "v221", "v222", "v223", "v224", "v225", "v226", "v227")

#define WO_W() \
  asm volatile( \
    "v_mov_b32 v0, 0\n\t" \
    "v_mov_b32 v1, 0\n\t" \
    "v_mov_b32 v2, 0\n\t" \
    "v_mov_b32 v3, 0\n\t" \
    "v_mov_b32 v4, 0\n\t" \
    "v_mov_b32 v5, 0\n\t" \
    "v_mov_b32 v6, 0\n\t" \
    "v_mov_b32 v7, 0\n\t" \
    "v_mov_b32 v8, 0\n\t" \
    "v_mov_b32 v9, 0\n\t" \
    "v_mov_b32 v10, 0\n\t" \
    "v_mov_b32 v11, 0\n\t" \
    "v_mov_b32 v12, 0\n\t" \
    "v_mov_b32 v13, 0\n\t" \
    "v_mov_b32 v14, 0\n\t" \
    "v_mov_b32 v15, 0\n\t" \
    "v_mov_b32 v252, 0\n\t" \
    "s_setprio 1\n\t" \
    "s_barrier\n\t" \
    GSA(4) \
    "buffer_load_dwordx4 v[220:223], %[l16], %[rs], %[s0] offen sc1\n\t" \
    "s_nop 15\n\t" \
    "buffer_load_dwordx4 v[224:227], %[l16], %[rs], %[s1] offen sc1\n\t" \
    "s_nop 15\n\t" \
    "buffer_load_dwordx4 v[228:231], %[l16], %[rs], %[s2] offen sc1\n\t" \
    "s_nop 15\n\t" \
    "buffer_load_dwordx4 v[232:235], %[l16], %[rs], %[s3] offen sc1\n\t" \
    "s_nop 15\n\t" \
    "buffer_load_dwordx4 v[236:239], %[l16], %[rs], %[s4] offen sc1\n\t" \
    "s_nop 15\n\t" \
    "buffer_load_dwordx4 v[240:243], %[l16], %[rs], %[s5] offen sc1\n\t" \
    "A_%=:\n\t" \
    "s_waitcnt vmcnt(5)\n\t" \
    "v_smfmac_f32_16x16x64_bf16 v[0:3], v[220:223], v[28:35], %[idx]\n\t" \
    "v_max3_u32 v252, v252, v220, v221\n\t" \
    "v_smfmac_f32_16x16x64_bf16 v[4:7], v[220:223], v[36:43], %[idx]\n\t" \
    "v_max3_u32 v252, v252, v222, v223\n\t" \
    "v_smfmac_f32_16x16x64_bf16 v[8:11], v[220:223], v[44:51], %[idx]\n\t" \
    "v_smfmac_f32_16x16x64_bf16 v[12:15], v[220:223], v[52:59], %[idx]\n\t" \
    "s_waitcnt vmcnt(4)\n\t" \
    "v_smfmac_f32_16x16x64_bf16 v[0:3], v[224:227], v[60:67], %[idx]\n\t" \
    "v_max3_u32 v252, v252, v224, v225\n\t" \
    "v_smfmac_f32_16x16x64_bf16 v[4:7], v[224:227], v[68:75], %[idx]\n\t" \
    "v_max3_u32 v252, v252, v226, v227\n\t" \
    "v_smfmac_f32_16x16x64_bf16 v[8:11], v[224:227], v[76:83], %[idx]\n\t" \
    "v_smfmac_f32_16x16x64_bf16 v[12:15], v[224:227], v[84:91], %[idx]\n\t" \
    "s_waitcnt vmcnt(3)\n\t" \
    "v_smfmac_f32_16x16x64_bf16 v[0:3], v[228:231], v[92:99], %[idx]\n\t" \
    "v_max3_u32 v252, v252, v228, v229\n\t" \
    "v_smfmac_f32_16x16x64_bf16 v[4:7], v[228:231], v[100:107], %[idx]\n\t" \
    "v_max3_u32 v252, v252, v230, v231\n\t" \
    "v_smfmac_f32_16x16x64_bf16 v[8:11], v[228:231], v[108:115], %[idx]\n\t" \
    "v_smfmac_f32_16x16x64_bf16 v[12:15], v[228:231], v[116:123], %[idx]\n\t" \
    "s_waitcnt vmcnt(2)\n\t" \
    "v_smfmac_f32_16x16x64_bf16 v[0:3], v[232:235], v[124:131], %[idx]\n\t" \
    "v_max3_u32 v252, v252, v232, v233\n\t" \
    "v_smfmac_f32_16x16x64_bf16 v[4:7], v[232:235], v[132:139], %[idx]\n\t" \
    "v_max3_u32 v252, v252, v234, v235\n\t" \
    "v_smfmac_f32_16x16x64_bf16 v[8:11], v[232:235], v[140:147], %[idx]\n\t" \
    "v_smfmac_f32_16x16x64_bf16 v[12:15], v[232:235], v[148:155], %[idx]\n\t" \
    "s_waitcnt vmcnt(1)\n\t" \
    "v_smfmac_f32_16x16x64_bf16 v[0:3], v[236:239], v[156:163], %[idx]\n\t" \
    "v_max3_u32 v252, v252, v236, v237\n\t" \
    "v_smfmac_f32_16x16x64_bf16 v[4:7], v[236:239], v[164:171], %[idx]\n\t" \
    "v_max3_u32 v252, v252, v238, v239\n\t" \
    "v_smfmac_f32_16x16x64_bf16 v[8:11], v[236:239], v[172:179], %[idx]\n\t" \
    "v_smfmac_f32_16x16x64_bf16 v[12:15], v[236:239], v[180:187], %[idx]\n\t" \
    "s_waitcnt vmcnt(0)\n\t" \
    "v_smfmac_f32_16x16x64_bf16 v[0:3], v[240:243], v[188:195], %[idx]\n\t" \
    "v_max3_u32 v252, v252, v240, v241\n\t" \
    "v_smfmac_f32_16x16x64_bf16 v[4:7], v[240:243], v[196:203], %[idx]\n\t" \
    "v_max3_u32 v252, v252, v242, v243\n\t" \
    "v_smfmac_f32_16x16x64_bf16 v[8:11], v[240:243], v[204:211], %[idx]\n\t" \
    "v_smfmac_f32_16x16x64_bf16 v[12:15], v[240:243], v[212:219], %[idx]\n\t" \
    "v_cmp_eq_u32 vcc, -1, v252\n\t" \
    "s_cbranch_vccnz SL_%=\n\t" \
    "D_%=:\n\t" \
    GSA(1) \
    "s_nop 2\n\t" \
    "v_add_f32 v0, v0, v1\n\t" \
    "v_add_f32 v2, v2, v3\n\t" \
    "v_add_f32 v4, v4, v5\n\t" \
    "v_add_f32 v6, v6, v7\n\t" \
    "v_add_f32 v8, v8, v9\n\t" \
    "v_add_f32 v10, v10, v11\n\t" \
    "v_add_f32 v12, v12, v13\n\t" \
    "v_add_f32 v14, v14, v15\n\t" \
    "v_permlane32_swap_b32 v0, v4\n\t" \
    "v_permlane32_swap_b32 v2, v6\n\t" \
    "v_permlane32_swap_b32 v8, v12\n\t" \
    "v_permlane32_swap_b32 v10, v14\n\t" \
    "v_add_f32 v0, v0, v12\n\t" \
    "v_add_f32 v2, v2, v14\n\t" \
    "ds_write2_b32 %[rw], v0, v2 offset0:0 offset1:32\n\t" \
    "s_waitcnt lgkmcnt(0)\n\t" \
    "s_barrier\n\t" \
    "s_branch E_%=\n\t" \
    "SL_%=:\n\t" \
    "s_sleep 1\n\t" \
    "buffer_load_dwordx4 v[220:223], %[l16], %[rs], %[s0] offen sc1\n\t" \
    "buffer_load_dwordx4 v[224:227], %[l16], %[rs], %[s1] offen sc1\n\t" \
    "buffer_load_dwordx4 v[228:231], %[l16], %[rs], %[s2] offen sc1\n\t" \
    "buffer_load_dwordx4 v[232:235], %[l16], %[rs], %[s3] offen sc1\n\t" \
    "buffer_load_dwordx4 v[236:239], %[l16], %[rs], %[s4] offen sc1\n\t" \
    "buffer_load_dwordx4 v[240:243], %[l16], %[rs], %[s5] offen sc1\n\t" \
    "s_sub_u32 %[lim], %[lim], " STR(DEC2) "\n\t" \
    "s_cmp_gt_i32 %[lim], 0\n\t" \
    "s_cbranch_scc0 G_%=\n\t" \
    "s_nop 3\n\t" \
    "v_mov_b32 v0, 0\n\t" \
    "v_mov_b32 v1, 0\n\t" \
    "v_mov_b32 v2, 0\n\t" \
    "v_mov_b32 v3, 0\n\t" \
    "v_mov_b32 v4, 0\n\t" \
    "v_mov_b32 v5, 0\n\t" \
    "v_mov_b32 v6, 0\n\t" \
    "v_mov_b32 v7, 0\n\t" \
    "v_mov_b32 v8, 0\n\t" \
    "v_mov_b32 v9, 0\n\t" \
    "v_mov_b32 v10, 0\n\t" \
    "v_mov_b32 v11, 0\n\t" \
    "v_mov_b32 v12, 0\n\t" \
    "v_mov_b32 v13, 0\n\t" \
    "v_mov_b32 v14, 0\n\t" \
    "v_mov_b32 v15, 0\n\t" \
    "v_mov_b32 v252, 0\n\t" \
    "s_branch A_%=\n\t" \
    "G_%=:\n\t" \
    "s_waitcnt vmcnt(0)\n\t" \
    "s_branch D_%=\n\t" \
    "E_%=:\n\t" \
    GSW \
    : [lim] "+s"(lim) GSO(1) GSO(4) \
    : [l16] "v"(l16), [rs] "s"(rs), [idx] "v"(idx), [rw] "v"(rw_a), [s0] "s"(so[0]), [s1] "s"(so[1]), [s2] "s"(so[2]), [s3] "s"(so[3]), [s4] "s"(so[4]), [s5] "s"(so[5]), "{v[28:35]}"(wo[0][0][0]), "{v[36:43]}"(wo[0][0][1]), "{v[44:51]}"(wo[0][1][0]), "{v[52:59]}"(wo[0][1][1]), "{v[60:67]}"(wo[1][0][0]), "{v[68:75]}"(wo[1][0][1]), "{v[76:83]}"(wo[1][1][0]), "{v[84:91]}"(wo[1][1][1]), "{v[92:99]}"(wo[2][0][0]), "{v[100:107]}"(wo[2][0][1]), "{v[108:115]}"(wo[2][1][0]), "{v[116:123]}"(wo[2][1][1]), "{v[124:131]}"(wo[3][0][0]), "{v[132:139]}"(wo[3][0][1]), "{v[140:147]}"(wo[3][1][0]), "{v[148:155]}"(wo[3][1][1]), "{v[156:163]}"(wo[4][0][0]), "{v[164:171]}"(wo[4][0][1]), "{v[172:179]}"(wo[4][1][0]), "{v[180:187]}"(wo[4][1][1]), "{v[188:195]}"(wo[5][0][0]), "{v[196:203]}"(wo[5][0][1]), "{v[204:211]}"(wo[5][1][0]), "{v[212:219]}"(wo[5][1][1]) \
    : "memory", "vcc", "scc", "v252", "v0", "v1", "v2", "v3", "v4", "v5", "v6", "v7", "v8", "v9", "v10", "v11", "v12", "v13", "v14", "v15", "v220", "v221", "v222", "v223", "v224", "v225", "v226", "v227", "v228", "v229", "v230", "v231", "v232", "v233", "v234", "v235", "v236", "v237", "v238", "v239", "v240", "v241", "v242", "v243")

#define S_LOOP_K(TE_, FIN_) \
  asm volatile( \
    "s_cmp_eq_u32 %[kh], 0\n\t" \
    "s_cbranch_scc0 L1_%=\n\t" \
    "L0_%=:\n\t" \
    "s_setprio 0\n\t" \
    "s_nop 3\n\t" \
    "ds_write_b128 %[pa], v[196:199] offset:0\n\t" \
    "ds_write_b128 %[pa], v[200:203] offset:1024\n\t" \
    "ds_write_b128 %[pa], v[204:207] offset:2048\n\t" \
    "ds_write_b128 %[pa], v[208:211] offset:3072\n\t" \
    "s_sub_u32 %[a], %[t], 1\n\t" \
    "v_mov_b32 v53, %[tag]\n\t" \
    "v_mov_b32 v52, %[a]\n\t" \
    "s_sub_u32 %[b], %[t], 2\n\t" \
    "s_cmp_lt_u32 %[b], " STR(SQUICK) "\n\t" \
    "s_cselect_b64 exec, %[fm], 0\n\t" \
    "buffer_store_dwordx2 v[52:53], %[l16], %[rs], %[fo] offen sc1\n\t" \
    "s_mov_b64 exec, -1\n\t" \
    "s_add_u32 m0, %[sd], 49152\n\t" \
    "s_add_u32 %[a], %[sx], 81920\n\t" \
    "s_waitcnt lgkmcnt(0)\n\t" \
    "buffer_load_dwordx4 %[l16], %[rs], %[a] offen offset:0 sc1 lds\n\t" \
    "buffer_load_dwordx4 %[l16], %[rs], %[a] offen offset:1024 sc1 lds\n\t" \
    "buffer_load_dwordx4 %[l16], %[rs], %[a] offen offset:2048 sc1 lds\n\t" \
    "buffer_load_dwordx4 %[l16], %[rs], %[a] offen offset:3072 sc1 lds\n\t" \
    "v_mfma_f32_16x16x32_bf16 v[0:3], v[68:71], v[20:23], 0\n\t" \
    "ds_read_b128 v[44:47], %[xp] offset:6144\n\t" \
    "v_mfma_f32_16x16x32_bf16 v[4:7], v[76:79], v[20:23], 0\n\t" \
    "ds_read_b128 v[48:51], %[xp] offset:7168\n\t" \
    "v_mfma_f32_16x16x32_bf16 v[8:11], v[68:71], v[24:27], 0\n\t" \
    "v_mfma_f32_16x16x32_bf16 v[12:15], v[76:79], v[24:27], 0\n\t" \
    "v_mfma_f32_16x16x32_bf16 v[0:3], v[72:75], v[28:31], v[0:3]\n\t" \
    "ds_read_b128 v[20:23], %[xp] offset:8192\n\t" \
    "v_mfma_f32_16x16x32_bf16 v[4:7], v[80:83], v[28:31], v[4:7]\n\t" \
    "ds_read_b128 v[24:27], %[xp] offset:9216\n\t" \
    "v_mfma_f32_16x16x32_bf16 v[8:11], v[72:75], v[32:35], v[8:11]\n\t" \
    "v_mfma_f32_16x16x32_bf16 v[12:15], v[80:83], v[32:35], v[12:15]\n\t" \
    "v_mfma_f32_16x16x32_bf16 v[0:3], v[84:87], v[36:39], v[0:3]\n\t" \
    "ds_read_b128 v[28:31], %[xp] offset:10240\n\t" \
    "v_mfma_f32_16x16x32_bf16 v[4:7], v[92:95], v[36:39], v[4:7]\n\t" \
    "ds_read_b128 v[32:35], %[xp] offset:11264\n\t" \
    "v_mfma_f32_16x16x32_bf16 v[8:11], v[84:87], v[40:43], v[8:11]\n\t" \
    "s_waitcnt lgkmcnt(4)\n\t" \
    "v_mfma_f32_16x16x32_bf16 v[12:15], v[92:95], v[40:43], v[12:15]\n\t" \
    "v_mfma_f32_16x16x32_bf16 v[0:3], v[88:91], v[44:47], v[0:3]\n\t" \
    "ds_read_b128 v[36:39], %[xp] offset:12288\n\t" \
    "v_mfma_f32_16x16x32_bf16 v[4:7], v[96:99], v[44:47], v[4:7]\n\t" \
    "ds_read_b128 v[40:43], %[xp] offset:13312\n\t" \
    "v_mfma_f32_16x16x32_bf16 v[8:11], v[88:91], v[48:51], v[8:11]\n\t" \
    "s_waitcnt lgkmcnt(4)\n\t" \
    "v_mfma_f32_16x16x32_bf16 v[12:15], v[96:99], v[48:51], v[12:15]\n\t" \
    "v_mfma_f32_16x16x32_bf16 v[0:3], v[100:103], v[20:23], v[0:3]\n\t" \
    "ds_read_b128 v[44:47], %[xp] offset:14336\n\t" \
    "v_mfma_f32_16x16x32_bf16 v[4:7], v[108:111], v[20:23], v[4:7]\n\t" \
    "ds_read_b128 v[48:51], %[xp] offset:15360\n\t" \
    "v_mfma_f32_16x16x32_bf16 v[8:11], v[100:103], v[24:27], v[8:11]\n\t" \
    "s_waitcnt lgkmcnt(4)\n\t" \
    "v_mfma_f32_16x16x32_bf16 v[12:15], v[108:111], v[24:27], v[12:15]\n\t" \
    "v_mfma_f32_16x16x32_bf16 v[0:3], v[104:107], v[28:31], v[0:3]\n\t" \
    "ds_read_b128 v[20:23], %[xp] offset:16384\n\t" \
    "v_mfma_f32_16x16x32_bf16 v[4:7], v[112:115], v[28:31], v[4:7]\n\t" \
    "ds_read_b128 v[24:27], %[xp] offset:17408\n\t" \
    "v_mfma_f32_16x16x32_bf16 v[8:11], v[104:107], v[32:35], v[8:11]\n\t" \
    "s_waitcnt lgkmcnt(4)\n\t" \
    "v_mfma_f32_16x16x32_bf16 v[12:15], v[112:115], v[32:35], v[12:15]\n\t" \
    "v_mfma_f32_16x16x32_bf16 v[0:3], v[116:119], v[36:39], v[0:3]\n\t" \
    "ds_read_b128 v[28:31], %[xp] offset:18432\n\t" \
    "v_mfma_f32_16x16x32_bf16 v[4:7], v[124:127], v[36:39], v[4:7]\n\t" \
    "ds_read_b128 v[32:35], %[xp] offset:19456\n\t" \
    "v_mfma_f32_16x16x32_bf16 v[8:11], v[116:119], v[40:43], v[8:11]\n\t" \
    "s_waitcnt lgkmcnt(4)\n\t" \
    "v_mfma_f32_16x16x32_bf16 v[12:15], v[124:127], v[40:43], v[12:15]\n\t" \
    "v_mfma_f32_16x16x32_bf16 v[0:3], v[120:123], v[44:47], v[0:3]\n\t" \
    "ds_read_b128 v[36:39], %[xp] offset:20480\n\t" \
    "v_mfma_f32_16x16x32_bf16 v[4:7], v[128:131], v[44:47], v[4:7]\n\t" \
    "ds_read_b128 v[40:43], %[xp] offset:21504\n\t" \
    "v_mfma_f32_16x16x32_bf16 v[8:11], v[120:123], v[48:51], v[8:11]\n\t" \
    "s_waitcnt lgkmcnt(4)\n\t" \
    "v_mfma_f32_16x16x32_bf16 v[12:15], v[128:131], v[48:51], v[12:15]\n\t" \
    "s_waitcnt vmcnt(4)\n\t" \
    "s_barrier\n\t" \
    "s_setprio 0\n\t" \
    "s_sub_u32 %[a], %[t], 1\n\t" \
    "v_mov_b32 v53, %[tag]\n\t" \
    "v_mov_b32 v52, %[a]\n\t" \
    "s_cmp_gt_u32 %[t], 1\n\t" \
    "s_cselect_b64 exec, %[fm], 0\n\t" \
    "buffer_store_dwordx2 v[52:53], %[l16], %[rs], %[fo] offen sc1\n\t" \
    "s_mov_b64 exec, %[pm]\n\t" \
    "s_mov_b32 m0, %[pd]\n\t" \
    "s_add_u32 %[a], %[sx], %[po]\n\t" \
    "buffer_load_dwordx4 %[l16], %[rs], %[a] offen sc1 lds\n\t" \
    "s_mov_b64 exec, -1\n\t" \
    "s_add_u32 m0, %[sd], 0\n\t" \
    "s_add_u32 %[a], %[sx], 131072\n\t" \
    "buffer_load_dwordx4 %[l16], %[rs], %[a] offen offset:0 sc1 lds\n\t" \
    "buffer_load_dwordx4 %[l16], %[rs], %[a] offen offset:1024 sc1 lds\n\t" \
    "buffer_load_dwordx4 %[l16], %[rs], %[a] offen offset:2048 sc1 lds\n\t" \
    "buffer_load_dwordx4 %[l16], %[rs], %[a] offen offset:3072 sc1 lds\n\t" \
    "v_mfma_f32_16x16x32_bf16 v[0:3], v[132:135], v[20:23], v[0:3]\n\t" \
    "ds_read_b128 v[44:47], %[xp] offset:22528\n\t" \
    "v_mfma_f32_16x16x32_bf16 v[4:7], v[140:143], v[20:23], v[4:7]\n\t" \
    "ds_read_b128 v[48:51], %[xp] offset:23552\n\t" \
    "v_mfma_f32_16x16x32_bf16 v[8:11], v[132:135], v[24:27], v[8:11]\n\t" \
    "s_waitcnt lgkmcnt(4)\n\t" \
    "v_mfma_f32_16x16x32_bf16 v[12:15], v[140:143], v[24:27], v[12:15]\n\t" \
    "v_mfma_f32_16x16x32_bf16 v[0:3], v[136:139], v[28:31], v[0:3]\n\t" \
    "ds_read_b128 v[20:23], %[xp] offset:24576\n\t" \
    "v_mfma_f32_16x16x32_bf16 v[4:7], v[144:147], v[28:31], v[4:7]\n\t" \
    "ds_read_b128 v[24:27], %[xp] offset:25600\n\t" \
    "v_mfma_f32_16x16x32_bf16 v[8:11], v[136:139], v[32:35], v[8:11]\n\t" \
    "s_waitcnt lgkmcnt(4)\n\t" \
    "v_mfma_f32_16x16x32_bf16 v[12:15], v[144:147], v[32:35], v[12:15]\n\t" \
    "v_mfma_f32_16x16x32_bf16 v[0:3], v[148:151], v[36:39], v[0:3]\n\t" \
    "ds_read_b128 v[28:31], %[xp] offset:26624\n\t" \
    "v_mfma_f32_16x16x32_bf16 v[4:7], v[156:159], v[36:39], v[4:7]\n\t" \
    "ds_read_b128 v[32:35], %[xp] offset:27648\n\t" \
    "v_mfma_f32_16x16x32_bf16 v[8:11], v[148:151], v[40:43], v[8:11]\n\t" \
    "s_waitcnt lgkmcnt(4)\n\t" \
    "v_mfma_f32_16x16x32_bf16 v[12:15], v[156:159], v[40:43], v[12:15]\n\t" \
    "v_mfma_f32_16x16x32_bf16 v[0:3], v[152:155], v[44:47], v[0:3]\n\t" \
    "ds_read_b128 v[36:39], %[xp] offset:28672\n\t" \
    "v_mfma_f32_16x16x32_bf16 v[4:7], v[160:163], v[44:47], v[4:7]\n\t" \
    "ds_read_b128 v[40:43], %[xp] offset:29696\n\t" \
    "v_mfma_f32_16x16x32_bf16 v[8:11], v[152:155], v[48:51], v[8:11]\n\t" \
    "s_waitcnt lgkmcnt(4)\n\t" \
    "v_mfma_f32_16x16x32_bf16 v[12:15], v[160:163], v[48:51], v[12:15]\n\t" \
    "v_mfma_f32_16x16x32_bf16 v[0:3], v[164:167], v[20:23], v[0:3]\n\t" \
    "ds_read_b128 v[44:47], %[xp] offset:30720\n\t" \
    "v_mfma_f32_16x16x32_bf16 v[4:7], v[172:175], v[20:23], v[4:7]\n\t" \
    "ds_read_b128 v[48:51], %[xp] offset:31744\n\t" \
    "v_mfma_f32_16x16x32_bf16 v[8:11], v[164:167], v[24:27], v[8:11]\n\t" \
    "s_waitcnt lgkmcnt(4)\n\t" \
    "v_mfma_f32_16x16x32_bf16 v[12:15], v[172:175], v[24:27], v[12:15]\n\t" \
    "v_mfma_f32_16x16x32_bf16 v[0:3], v[168:171], v[28:31], v[0:3]\n\t" \
    "ds_read_b128 v[20:23], %[xp] offset:32768\n\t" \
    "v_mfma_f32_16x16x32_bf16 v[4:7], v[176:179], v[28:31], v[4:7]\n\t" \
    "ds_read_b128 v[24:27], %[xp] offset:33792\n\t" \
    "v_mfma_f32_16x16x32_bf16 v[8:11], v[168:171], v[32:35], v[8:11]\n\t" \
    "s_waitcnt lgkmcnt(4)\n\t" \
    "v_mfma_f32_16x16x32_bf16 v[12:15], v[176:179], v[32:35], v[12:15]\n\t" \
    "v_mfma_f32_16x16x32_bf16 v[0:3], v[180:183], v[36:39], v[0:3]\n\t" \
    "ds_read_b128 v[28:31], %[xp] offset:34816\n\t" \
    "v_mfma_f32_16x16x32_bf16 v[4:7], v[188:191], v[36:39], v[4:7]\n\t" \
    "ds_read_b128 v[32:35], %[xp] offset:35840\n\t" \
    "v_mfma_f32_16x16x32_bf16 v[8:11], v[180:183], v[40:43], v[8:11]\n\t" \
    "s_waitcnt lgkmcnt(4)\n\t" \
    "v_mfma_f32_16x16x32_bf16 v[12:15], v[188:191], v[40:43], v[12:15]\n\t" \
    "v_mfma_f32_16x16x32_bf16 v[0:3], v[184:187], v[44:47], v[0:3]\n\t" \
    "ds_read_b128 v[36:39], %[xp] offset:36864\n\t" \
    "v_mfma_f32_16x16x32_bf16 v[4:7], v[192:195], v[44:47], v[4:7]\n\t" \
    "ds_read_b128 v[40:43], %[xp] offset:37888\n\t" \
    "v_mfma_f32_16x16x32_bf16 v[8:11], v[184:187], v[48:51], v[8:11]\n\t" \
    "s_waitcnt lgkmcnt(4)\n\t" \
    "v_mfma_f32_16x16x32_bf16 v[12:15], v[192:195], v[48:51], v[12:15]\n\t" \
    "s_add_u32 %[sx], %[sx], 65536\n\t" \
    "s_add_u32 %[t], %[t], 1\n\t" \
    "s_waitcnt vmcnt(4)\n\t" \
    "s_barrier\n\t" \
    "s_setprio 0\n\t" \
    "s_nop 3\n\t" \
    "ds_write_b128 %[pa], v[0:3] offset:0\n\t" \
    "ds_write_b128 %[pa], v[4:7] offset:1024\n\t" \
    "ds_write_b128 %[pa], v[8:11] offset:2048\n\t" \
    "ds_write_b128 %[pa], v[12:15] offset:3072\n\t" \
    "s_sub_u32 %[a], %[t], 1\n\t" \
    "v_mov_b32 v53, %[tag]\n\t" \
    "v_mov_b32 v52, %[a]\n\t" \
    "s_sub_u32 %[b], %[t], 2\n\t" \
    "s_cmp_lt_u32 %[b], " STR(SQUICK) "\n\t" \
    "s_cselect_b64 exec, %[fm], 0\n\t" \
    "buffer_store_dwordx2 v[52:53], %[l16], %[rs], %[fo] offen sc1\n\t" \
    "s_mov_b64 exec, -1\n\t" \
    "s_add_u32 m0, %[sd], 16384\n\t" \
    "s_add_u32 %[a], %[sx], 81920\n\t" \
    "s_waitcnt lgkmcnt(0)\n\t" \
    "buffer_load_dwordx4 %[l16], %[rs], %[a] offen offset:0 sc1 lds\n\t" \
    "buffer_load_dwordx4 %[l16], %[rs], %[a] offen offset:1024 sc1 lds\n\t" \
    "buffer_load_dwordx4 %[l16], %[rs], %[a] offen offset:2048 sc1 lds\n\t" \
    "buffer_load_dwordx4 %[l16], %[rs], %[a] offen offset:3072 sc1 lds\n\t" \
    "v_mfma_f32_16x16x32_bf16 v[196:199], v[68:71], v[20:23], 0\n\t" \
    "ds_read_b128 v[44:47], %[xp] offset:38912\n\t" \
    "v_mfma_f32_16x16x32_bf16 v[200:203], v[76:79], v[20:23], 0\n\t" \
    "ds_read_b128 v[48:51], %[xp] offset:39936\n\t" \
    "v_mfma_f32_16x16x32_bf16 v[204:207], v[68:71], v[24:27], 0\n\t" \
    "v_mfma_f32_16x16x32_bf16 v[208:211], v[76:79], v[24:27], 0\n\t" \
    "v_mfma_f32_16x16x32_bf16 v[196:199], v[72:75], v[28:31], v[196:199]\n\t" \
    "ds_read_b128 v[20:23], %[xp] offset:40960\n\t" \
    "v_mfma_f32_16x16x32_bf16 v[200:203], v[80:83], v[28:31], v[200:203]\n\t" \
    "ds_read_b128 v[24:27], %[xp] offset:41984\n\t" \
    "v_mfma_f32_16x16x32_bf16 v[204:207], v[72:75], v[32:35], v[204:207]\n\t" \
    "v_mfma_f32_16x16x32_bf16 v[208:211], v[80:83], v[32:35], v[208:211]\n\t" \
    "v_mfma_f32_16x16x32_bf16 v[196:199], v[84:87], v[36:39], v[196:199]\n\t" \
    "ds_read_b128 v[28:31], %[xp] offset:43008\n\t" \
    "v_mfma_f32_16x16x32_bf16 v[200:203], v[92:95], v[36:39], v[200:203]\n\t" \
    "ds_read_b128 v[32:35], %[xp] offset:44032\n\t" \
    "v_mfma_f32_16x16x32_bf16 v[204:207], v[84:87], v[40:43], v[204:207]\n\t" \
    "s_waitcnt lgkmcnt(4)\n\t" \
    "v_mfma_f32_16x16x32_bf16 v[208:211], v[92:95], v[40:43], v[208:211]\n\t" \
    "v_mfma_f32_16x16x32_bf16 v[196:199], v[88:91], v[44:47], v[196:199]\n\t" \
    "ds_read_b128 v[36:39], %[xp] offset:45056\n\t" \
    "v_mfma_f32_16x16x32_bf16 v[200:203], v[96:99], v[44:47], v[200:203]\n\t" \
    "ds_read_b128 v[40:43], %[xp] offset:46080\n\t" \
    "v_mfma_f32_16x16x32_bf16 v[204:207], v[88:91], v[48:51], v[204:207]\n\t" \
    "s_waitcnt lgkmcnt(4)\n\t" \
    "v_mfma_f32_16x16x32_bf16 v[208:211], v[96:99], v[48:51], v[208:211]\n\t" \
    "v_mfma_f32_16x16x32_bf16 v[196:199], v[100:103], v[20:23], v[196:199]\n\t" \
    "ds_read_b128 v[44:47], %[xp] offset:47104\n\t" \
    "v_mfma_f32_16x16x32_bf16 v[200:203], v[108:111], v[20:23], v[200:203]\n\t" \
    "ds_read_b128 v[48:51], %[xp] offset:48128\n\t" \
    "v_mfma_f32_16x16x32_bf16 v[204:207], v[100:103], v[24:27], v[204:207]\n\t" \
    "s_waitcnt lgkmcnt(4)\n\t" \
    "v_mfma_f32_16x16x32_bf16 v[208:211], v[108:111], v[24:27], v[208:211]\n\t" \
    "v_mfma_f32_16x16x32_bf16 v[196:199], v[104:107], v[28:31], v[196:199]\n\t" \
    "ds_read_b128 v[20:23], %[xp] offset:49152\n\t" \
    "v_mfma_f32_16x16x32_bf16 v[200:203], v[112:115], v[28:31], v[200:203]\n\t" \
    "ds_read_b128 v[24:27], %[xp] offset:50176\n\t" \
    "v_mfma_f32_16x16x32_bf16 v[204:207], v[104:107], v[32:35], v[204:207]\n\t" \
    "s_waitcnt lgkmcnt(4)\n\t" \
    "v_mfma_f32_16x16x32_bf16 v[208:211], v[112:115], v[32:35], v[208:211]\n\t" \
    "v_mfma_f32_16x16x32_bf16 v[196:199], v[116:119], v[36:39], v[196:199]\n\t" \
    "ds_read_b128 v[28:31], %[xp] offset:51200\n\t" \
    "v_mfma_f32_16x16x32_bf16 v[200:203], v[124:127], v[36:39], v[200:203]\n\t" \
    "ds_read_b128 v[32:35], %[xp] offset:52224\n\t" \
    "v_mfma_f32_16x16x32_bf16 v[204:207], v[116:119], v[40:43], v[204:207]\n\t" \
    "s_waitcnt lgkmcnt(4)\n\t" \
    "v_mfma_f32_16x16x32_bf16 v[208:211], v[124:127], v[40:43], v[208:211]\n\t" \
    "v_mfma_f32_16x16x32_bf16 v[196:199], v[120:123], v[44:47], v[196:199]\n\t" \
    "ds_read_b128 v[36:39], %[xp] offset:53248\n\t" \
    "v_mfma_f32_16x16x32_bf16 v[200:203], v[128:131], v[44:47], v[200:203]\n\t" \
    "ds_read_b128 v[40:43], %[xp] offset:54272\n\t" \
    "v_mfma_f32_16x16x32_bf16 v[204:207], v[120:123], v[48:51], v[204:207]\n\t" \
    "s_waitcnt lgkmcnt(4)\n\t" \
    "v_mfma_f32_16x16x32_bf16 v[208:211], v[128:131], v[48:51], v[208:211]\n\t" \
    "s_waitcnt vmcnt(4)\n\t" \
    "s_barrier\n\t" \
    "s_setprio 0\n\t" \
    "s_sub_u32 %[a], %[t], 1\n\t" \
    "v_mov_b32 v53, %[tag]\n\t" \
    "v_mov_b32 v52, %[a]\n\t" \
    "s_cmp_gt_u32 %[t], 1\n\t" \
    "s_cselect_b64 exec, %[fm], 0\n\t" \
    "buffer_store_dwordx2 v[52:53], %[l16], %[rs], %[fo] offen sc1\n\t" \
    "s_mov_b64 exec, %[pm]\n\t" \
    "s_mov_b32 m0, %[pd]\n\t" \
    "s_add_u32 %[a], %[sx], %[po]\n\t" \
    "buffer_load_dwordx4 %[l16], %[rs], %[a] offen sc1 lds\n\t" \
    "s_mov_b64 exec, -1\n\t" \
    "s_add_u32 m0, %[sd], 32768\n\t" \
    "s_add_u32 %[a], %[sx], 131072\n\t" \
    "buffer_load_dwordx4 %[l16], %[rs], %[a] offen offset:0 sc1 lds\n\t" \
    "buffer_load_dwordx4 %[l16], %[rs], %[a] offen offset:1024 sc1 lds\n\t" \
    "buffer_load_dwordx4 %[l16], %[rs], %[a] offen offset:2048 sc1 lds\n\t" \
    "buffer_load_dwordx4 %[l16], %[rs], %[a] offen offset:3072 sc1 lds\n\t" \
    "v_mfma_f32_16x16x32_bf16 v[196:199], v[132:135], v[20:23], v[196:199]\n\t" \
    "ds_read_b128 v[44:47], %[xp] offset:55296\n\t" \
    "v_mfma_f32_16x16x32_bf16 v[200:203], v[140:143], v[20:23], v[200:203]\n\t" \
    "ds_read_b128 v[48:51], %[xp] offset:56320\n\t" \
    "v_mfma_f32_16x16x32_bf16 v[204:207], v[132:135], v[24:27], v[204:207]\n\t" \
    "s_waitcnt lgkmcnt(4)\n\t" \
    "v_mfma_f32_16x16x32_bf16 v[208:211], v[140:143], v[24:27], v[208:211]\n\t" \
    "v_mfma_f32_16x16x32_bf16 v[196:199], v[136:139], v[28:31], v[196:199]\n\t" \
    "ds_read_b128 v[20:23], %[xp] offset:57344\n\t" \
    "v_mfma_f32_16x16x32_bf16 v[200:203], v[144:147], v[28:31], v[200:203]\n\t" \
    "ds_read_b128 v[24:27], %[xp] offset:58368\n\t" \
    "v_mfma_f32_16x16x32_bf16 v[204:207], v[136:139], v[32:35], v[204:207]\n\t" \
    "s_waitcnt lgkmcnt(4)\n\t" \
    "v_mfma_f32_16x16x32_bf16 v[208:211], v[144:147], v[32:35], v[208:211]\n\t" \
    "v_mfma_f32_16x16x32_bf16 v[196:199], v[148:151], v[36:39], v[196:199]\n\t" \
    "ds_read_b128 v[28:31], %[xp] offset:59392\n\t" \
    "v_mfma_f32_16x16x32_bf16 v[200:203], v[156:159], v[36:39], v[200:203]\n\t" \
    "ds_read_b128 v[32:35], %[xp] offset:60416\n\t" \
    "v_mfma_f32_16x16x32_bf16 v[204:207], v[148:151], v[40:43], v[204:207]\n\t" \
    "s_waitcnt lgkmcnt(4)\n\t" \
    "v_mfma_f32_16x16x32_bf16 v[208:211], v[156:159], v[40:43], v[208:211]\n\t" \
    "v_mfma_f32_16x16x32_bf16 v[196:199], v[152:155], v[44:47], v[196:199]\n\t" \
    "ds_read_b128 v[36:39], %[xp] offset:61440\n\t" \
    "v_mfma_f32_16x16x32_bf16 v[200:203], v[160:163], v[44:47], v[200:203]\n\t" \
    "ds_read_b128 v[40:43], %[xp] offset:62464\n\t" \
    "v_mfma_f32_16x16x32_bf16 v[204:207], v[152:155], v[48:51], v[204:207]\n\t" \
    "s_waitcnt lgkmcnt(4)\n\t" \
    "v_mfma_f32_16x16x32_bf16 v[208:211], v[160:163], v[48:51], v[208:211]\n\t" \
    "v_mfma_f32_16x16x32_bf16 v[196:199], v[164:167], v[20:23], v[196:199]\n\t" \
    "ds_read_b128 v[44:47], %[xp] offset:63488\n\t" \
    "v_mfma_f32_16x16x32_bf16 v[200:203], v[172:175], v[20:23], v[200:203]\n\t" \
    "ds_read_b128 v[48:51], %[xp] offset:64512\n\t" \
    "v_mfma_f32_16x16x32_bf16 v[204:207], v[164:167], v[24:27], v[204:207]\n\t" \
    "s_waitcnt lgkmcnt(4)\n\t" \
    "v_mfma_f32_16x16x32_bf16 v[208:211], v[172:175], v[24:27], v[208:211]\n\t" \
    "v_mfma_f32_16x16x32_bf16 v[196:199], v[168:171], v[28:31], v[196:199]\n\t" \
    "ds_read_b128 v[20:23], %[xp] offset:0\n\t" \
    "v_mfma_f32_16x16x32_bf16 v[200:203], v[176:179], v[28:31], v[200:203]\n\t" \
    "ds_read_b128 v[24:27], %[xp] offset:1024\n\t" \
    "v_mfma_f32_16x16x32_bf16 v[204:207], v[168:171], v[32:35], v[204:207]\n\t" \
    "s_waitcnt lgkmcnt(4)\n\t" \
    "v_mfma_f32_16x16x32_bf16 v[208:211], v[176:179], v[32:35], v[208:211]\n\t" \
    "v_mfma_f32_16x16x32_bf16 v[196:199], v[180:183], v[36:39], v[196:199]\n\t" \
    "ds_read_b128 v[28:31], %[xp] offset:2048\n\t" \
    "v_mfma_f32_16x16x32_bf16 v[200:203], v[188:191], v[36:39], v[200:203]\n\t" \
    "ds_read_b128 v[32:35], %[xp] offset:3072\n\t" \
    "v_mfma_f32_16x16x32_bf16 v[204:207], v[180:183], v[40:43], v[204:207]\n\t" \
    "s_waitcnt lgkmcnt(4)\n\t" \
    "v_mfma_f32_16x16x32_bf16 v[208:211], v[188:191], v[40:43], v[208:211]\n\t" \
    "v_mfma_f32_16x16x32_bf16 v[196:199], v[184:187], v[44:47], v[196:199]\n\t" \
    "ds_read_b128 v[36:39], %[xp] offset:4096\n\t" \
    "v_mfma_f32_16x16x32_bf16 v[200:203], v[192:195], v[44:47], v[200:203]\n\t" \
    "ds_read_b128 v[40:43], %[xp] offset:5120\n\t" \
    "v_mfma_f32_16x16x32_bf16 v[204:207], v[184:187], v[48:51], v[204:207]\n\t" \
    "s_waitcnt lgkmcnt(4)\n\t" \
    "v_mfma_f32_16x16x32_bf16 v[208:211], v[192:195], v[48:51], v[208:211]\n\t" \
    "s_add_u32 %[sx], %[sx], 65536\n\t" \
    "s_add_u32 %[t], %[t], 1\n\t" \
    "s_waitcnt vmcnt(4)\n\t" \
    "s_barrier\n\t" \
    "s_cmp_lt_u32 %[t], %[te]\n\t" \
    "s_cbranch_scc1 L0_%=\n\t" \
    "s_cmp_eq_u32 %[fin], %[te]\n\t" \
    "s_cbranch_scc0 E_%=\n\t" \
    "s_nop 4\n\t" \
    "ds_write_b128 %[pa], v[196:199] offset:0\n\t" \
    "ds_write_b128 %[pa], v[200:203] offset:1024\n\t" \
    "ds_write_b128 %[pa], v[204:207] offset:2048\n\t" \
    "ds_write_b128 %[pa], v[208:211] offset:3072\n\t" \
    "s_waitcnt lgkmcnt(0)\n\t" \
    "s_barrier\n\t" \
    "s_branch E_%=\n\t" \
    "L1_%=:\n\t" \
    "s_setprio 1\n\t" \
    "s_add_u32 m0, %[sd], 49152\n\t" \
    "s_add_u32 %[a], %[sx], 81920\n\t" \
    "v_mfma_f32_16x16x32_bf16 v[0:3], v[68:71], v[20:23], 0\n\t" \
    "ds_read_b128 v[44:47], %[xp] offset:6144\n\t" \
    "v_mfma_f32_16x16x32_bf16 v[4:7], v[76:79], v[20:23], 0\n\t" \
    "ds_read_b128 v[48:51], %[xp] offset:7168\n\t" \
    "v_mfma_f32_16x16x32_bf16 v[8:11], v[68:71], v[24:27], 0\n\t" \
    "s_waitcnt lgkmcnt(4)\n\t" \
    "v_mfma_f32_16x16x32_bf16 v[12:15], v[76:79], v[24:27], 0\n\t" \
    "v_mfma_f32_16x16x32_bf16 v[0:3], v[72:75], v[28:31], v[0:3]\n\t" \
    "ds_read_b128 v[20:23], %[xp] offset:8192\n\t" \
    "v_mfma_f32_16x16x32_bf16 v[4:7], v[80:83], v[28:31], v[4:7]\n\t" \
    "ds_read_b128 v[24:27], %[xp] offset:9216\n\t" \
    "v_mfma_f32_16x16x32_bf16 v[8:11], v[72:75], v[32:35], v[8:11]\n\t" \
    "s_waitcnt lgkmcnt(4)\n\t" \
    "v_mfma_f32_16x16x32_bf16 v[12:15], v[80:83], v[32:35], v[12:15]\n\t" \
    "v_mfma_f32_16x16x32_bf16 v[0:3], v[84:87], v[36:39], v[0:3]\n\t" \
    "ds_read_b128 v[28:31], %[xp] offset:10240\n\t" \
    "v_mfma_f32_16x16x32_bf16 v[4:7], v[92:95], v[36:39], v[4:7]\n\t" \
    "ds_read_b128 v[32:35], %[xp] offset:11264\n\t" \
    "v_mfma_f32_16x16x32_bf16 v[8:11], v[84:87], v[40:43], v[8:11]\n\t" \
    "s_waitcnt lgkmcnt(4)\n\t" \
    "v_mfma_f32_16x16x32_bf16 v[12:15], v[92:95], v[40:43], v[12:15]\n\t" \
    "v_mfma_f32_16x16x32_bf16 v[0:3], v[88:91], v[44:47], v[0:3]\n\t" \
    "ds_read_b128 v[36:39], %[xp] offset:12288\n\t" \
    "v_mfma_f32_16x16x32_bf16 v[4:7], v[96:99], v[44:47], v[4:7]\n\t" \
    "ds_read_b128 v[40:43], %[xp] offset:13312\n\t" \
    "v_mfma_f32_16x16x32_bf16 v[8:11], v[88:91], v[48:51], v[8:11]\n\t" \
    "s_waitcnt lgkmcnt(4)\n\t" \
    "v_mfma_f32_16x16x32_bf16 v[12:15], v[96:99], v[48:51], v[12:15]\n\t" \
    "v_mfma_f32_16x16x32_bf16 v[0:3], v[100:103], v[20:23], v[0:3]\n\t" \
    "ds_read_b128 v[44:47], %[xp] offset:14336\n\t" \
    "v_mfma_f32_16x16x32_bf16 v[4:7], v[108:111], v[20:23], v[4:7]\n\t" \
    "ds_read_b128 v[48:51], %[xp] offset:15360\n\t" \
    "v_mfma_f32_16x16x32_bf16 v[8:11], v[100:103], v[24:27], v[8:11]\n\t" \
    "s_waitcnt lgkmcnt(4)\n\t" \
    "v_mfma_f32_16x16x32_bf16 v[12:15], v[108:111], v[24:27], v[12:15]\n\t" \
    "buffer_load_dwordx4 %[l16], %[rs], %[a] offen offset:0 sc1 lds\n\t" \
    "v_mfma_f32_16x16x32_bf16 v[0:3], v[104:107], v[28:31], v[0:3]\n\t" \
    "ds_read_b128 v[20:23], %[xp] offset:16384\n\t" \
    "v_mfma_f32_16x16x32_bf16 v[4:7], v[112:115], v[28:31], v[4:7]\n\t" \
    "ds_read_b128 v[24:27], %[xp] offset:17408\n\t" \
    "v_mfma_f32_16x16x32_bf16 v[8:11], v[104:107], v[32:35], v[8:11]\n\t" \
    "s_waitcnt lgkmcnt(4)\n\t" \
    "v_mfma_f32_16x16x32_bf16 v[12:15], v[112:115], v[32:35], v[12:15]\n\t" \
    "buffer_load_dwordx4 %[l16], %[rs], %[a] offen offset:1024 sc1 lds\n\t" \
    "v_mfma_f32_16x16x32_bf16 v[0:3], v[116:119], v[36:39], v[0:3]\n\t" \
    "ds_read_b128 v[28:31], %[xp] offset:18432\n\t" \
    "v_mfma_f32_16x16x32_bf16 v[4:7], v[124:127], v[36:39], v[4:7]\n\t" \
    "ds_read_b128 v[32:35], %[xp] offset:19456\n\t" \
    "v_mfma_f32_16x16x32_bf16 v[8:11], v[116:119], v[40:43], v[8:11]\n\t" \
    "s_waitcnt lgkmcnt(4)\n\t" \
    "v_mfma_f32_16x16x32_bf16 v[12:15], v[124:127], v[40:43], v[12:15]\n\t" \
    "buffer_load_dwordx4 %[l16], %[rs], %[a] offen offset:2048 sc1 lds\n\t" \
    "v_mfma_f32_16x16x32_bf16 v[0:3], v[120:123], v[44:47], v[0:3]\n\t" \
    "ds_read_b128 v[36:39], %[xp] offset:20480\n\t" \
    "v_mfma_f32_16x16x32_bf16 v[4:7], v[128:131], v[44:47], v[4:7]\n\t" \
    "ds_read_b128 v[40:43], %[xp] offset:21504\n\t" \
    "v_mfma_f32_16x16x32_bf16 v[8:11], v[120:123], v[48:51], v[8:11]\n\t" \
    "s_waitcnt lgkmcnt(4)\n\t" \
    "v_mfma_f32_16x16x32_bf16 v[12:15], v[128:131], v[48:51], v[12:15]\n\t" \
    "buffer_load_dwordx4 %[l16], %[rs], %[a] offen offset:3072 sc1 lds\n\t" \
    "s_waitcnt vmcnt(4)\n\t" \
    "s_barrier\n\t" \
    "s_setprio 1\n\t" \
    "s_add_u32 m0, %[sd], 0\n\t" \
    "s_add_u32 %[a], %[sx], 131072\n\t" \
    "s_add_u32 %[b], %[oo], 0x1000\n\t" \
    "s_cmp_lg_u32 %[t], 1\n\t" \
    "s_cselect_b64 %[em], -1, 0\n\t" \
    "v_mfma_f32_16x16x32_bf16 v[0:3], v[132:135], v[20:23], v[0:3]\n\t" \
    "ds_read_b128 v[44:47], %[xp] offset:22528\n\t" \
    "v_mfma_f32_16x16x32_bf16 v[4:7], v[140:143], v[20:23], v[4:7]\n\t" \
    "ds_read_b128 v[48:51], %[xp] offset:23552\n\t" \
    "v_mfma_f32_16x16x32_bf16 v[8:11], v[132:135], v[24:27], v[8:11]\n\t" \
    "ds_read_b128 v[52:55], %[pa] offset:0\n\t" \
    "s_waitcnt lgkmcnt(5)\n\t" \
    "v_mfma_f32_16x16x32_bf16 v[12:15], v[140:143], v[24:27], v[12:15]\n\t" \
    "buffer_load_dwordx4 %[l16], %[rs], %[a] offen offset:0 sc1 lds\n\t" \
    "v_mfma_f32_16x16x32_bf16 v[0:3], v[136:139], v[28:31], v[0:3]\n\t" \
    "ds_read_b128 v[20:23], %[xp] offset:24576\n\t" \
    "v_mfma_f32_16x16x32_bf16 v[4:7], v[144:147], v[28:31], v[4:7]\n\t" \
    "ds_read_b128 v[24:27], %[xp] offset:25600\n\t" \
    "v_mfma_f32_16x16x32_bf16 v[8:11], v[136:139], v[32:35], v[8:11]\n\t" \
    "ds_read_b128 v[56:59], %[pa] offset:1024\n\t" \
    "s_waitcnt lgkmcnt(6)\n\t" \
    "v_mfma_f32_16x16x32_bf16 v[12:15], v[144:147], v[32:35], v[12:15]\n\t" \
    "buffer_load_dwordx4 %[l16], %[rs], %[a] offen offset:1024 sc1 lds\n\t" \
    "v_mfma_f32_16x16x32_bf16 v[0:3], v[148:151], v[36:39], v[0:3]\n\t" \
    "ds_read_b128 v[28:31], %[xp] offset:26624\n\t" \
    "v_mfma_f32_16x16x32_bf16 v[4:7], v[156:159], v[36:39], v[4:7]\n\t" \
    "ds_read_b128 v[32:35], %[xp] offset:27648\n\t" \
    "v_mfma_f32_16x16x32_bf16 v[8:11], v[148:151], v[40:43], v[8:11]\n\t" \
    "ds_read_b128 v[60:63], %[pa] offset:2048\n\t" \
    "s_waitcnt lgkmcnt(7)\n\t" \
    "v_mfma_f32_16x16x32_bf16 v[12:15], v[156:159], v[40:43], v[12:15]\n\t" \
    "buffer_load_dwordx4 %[l16], %[rs], %[a] offen offset:2048 sc1 lds\n\t" \
    "v_mfma_f32_16x16x32_bf16 v[0:3], v[152:155], v[44:47], v[0:3]\n\t" \
    "ds_read_b128 v[36:39], %[xp] offset:28672\n\t" \
    "s_waitcnt lgkmcnt(7)\n\t" \
    "v_add_f32 v196, v196, v52\n\t" \
    "v_mfma_f32_16x16x32_bf16 v[4:7], v[160:163], v[44:47], v[4:7]\n\t" \
    "ds_read_b128 v[40:43], %[xp] offset:29696\n\t" \
    "v_add_f32 v197, v197, v53\n\t" \
    "v_mfma_f32_16x16x32_bf16 v[8:11], v[152:155], v[48:51], v[8:11]\n\t" \
    "ds_read_b128 v[64:67], %[pa] offset:3072\n\t" \
    "s_waitcnt lgkmcnt(7)\n\t" \
    "v_mfma_f32_16x16x32_bf16 v[12:15], v[160:163], v[48:51], v[12:15]\n\t" \
    "buffer_load_dwordx4 %[l16], %[rs], %[a] offen offset:3072 sc1 lds\n\t" \
    "v_add_f32 v198, v198, v54\n\t" \
    "v_add_f32 v199, v199, v55\n\t" \
    "v_mfma_f32_16x16x32_bf16 v[0:3], v[164:167], v[20:23], v[0:3]\n\t" \
    "ds_read_b128 v[44:47], %[xp] offset:30720\n\t" \
    "s_waitcnt lgkmcnt(7)\n\t" \
    "v_add_f32 v200, v200, v56\n\t" \
    "v_mfma_f32_16x16x32_bf16 v[4:7], v[172:175], v[20:23], v[4:7]\n\t" \
    "ds_read_b128 v[48:51], %[xp] offset:31744\n\t" \
    "v_add_f32 v201, v201, v57\n\t" \
    "v_mfma_f32_16x16x32_bf16 v[8:11], v[164:167], v[24:27], v[8:11]\n\t" \
    "v_add_f32 v202, v202, v58\n\t" \
    "s_waitcnt lgkmcnt(6)\n\t" \
    "v_mfma_f32_16x16x32_bf16 v[12:15], v[172:175], v[24:27], v[12:15]\n\t" \
    "v_add_f32 v203, v203, v59\n\t" \
    "s_waitcnt lgkmcnt(5)\n\t" \
    "v_add_f32 v204, v204, v60\n\t" \
    "v_add_f32 v205, v205, v61\n\t" \
    "v_mfma_f32_16x16x32_bf16 v[0:3], v[168:171], v[28:31], v[0:3]\n\t" \
    "ds_read_b128 v[20:23], %[xp] offset:32768\n\t" \
    "v_add_f32 v206, v206, v62\n\t" \
    "v_mfma_f32_16x16x32_bf16 v[4:7], v[176:179], v[28:31], v[4:7]\n\t" \
    "ds_read_b128 v[24:27], %[xp] offset:33792\n\t" \
    "v_add_f32 v207, v207, v63\n\t" \
    "v_mfma_f32_16x16x32_bf16 v[8:11], v[168:171], v[32:35], v[8:11]\n\t" \
    "s_waitcnt lgkmcnt(4)\n\t" \
    "v_add_f32 v208, v208, v64\n\t" \
    "v_mfma_f32_16x16x32_bf16 v[12:15], v[176:179], v[32:35], v[12:15]\n\t" \
    "v_add_f32 v209, v209, v65\n\t" \
    "v_add_f32 v210, v210, v66\n\t" \
    "v_add_f32 v211, v211, v67\n\t" \
    "v_mfma_f32_16x16x32_bf16 v[0:3], v[180:183], v[36:39], v[0:3]\n\t" \
    "ds_read_b128 v[28:31], %[xp] offset:34816\n\t" \
    "v_cvt_pk_bf16_f32 v52, v196, v200\n\t" \
    "v_mfma_f32_16x16x32_bf16 v[4:7], v[188:191], v[36:39], v[4:7]\n\t" \
    "ds_read_b128 v[32:35], %[xp] offset:35840\n\t" \
    "v_cvt_pk_bf16_f32 v53, v197, v201\n\t" \
    "v_mfma_f32_16x16x32_bf16 v[8:11], v[180:183], v[40:43], v[8:11]\n\t" \
    "v_cvt_pk_bf16_f32 v54, v198, v202\n\t" \
    "s_waitcnt lgkmcnt(4)\n\t" \
    "v_mfma_f32_16x16x32_bf16 v[12:15], v[188:191], v[40:43], v[12:15]\n\t" \
    "v_cvt_pk_bf16_f32 v55, v199, v203\n\t" \
    "v_cvt_pk_bf16_f32 v56, v204, v208\n\t" \
    "v_cvt_pk_bf16_f32 v57, v205, v209\n\t" \
    "v_mfma_f32_16x16x32_bf16 v[0:3], v[184:187], v[44:47], v[0:3]\n\t" \
    "ds_read_b128 v[36:39], %[xp] offset:36864\n\t" \
    "v_cvt_pk_bf16_f32 v58, v206, v210\n\t" \
    "v_mfma_f32_16x16x32_bf16 v[4:7], v[192:195], v[44:47], v[4:7]\n\t" \
    "ds_read_b128 v[40:43], %[xp] offset:37888\n\t" \
    "v_cvt_pk_bf16_f32 v59, v207, v211\n\t" \
    "v_mfma_f32_16x16x32_bf16 v[8:11], v[184:187], v[48:51], v[8:11]\n\t" \
    "s_waitcnt lgkmcnt(4)\n\t" \
    "v_mfma_f32_16x16x32_bf16 v[12:15], v[192:195], v[48:51], v[12:15]\n\t" \
    "s_mov_b64 exec, %[em]\n\t" \
    "buffer_store_dwordx4 v[52:55], %[l16], %[rs], %[oo] offen sc1\n\t" \
    "buffer_store_dwordx4 v[56:59], %[l16], %[rs], %[b] offen sc1\n\t" \
    "s_mov_b64 exec, -1\n\t" \
    "s_add_u32 %[sx], %[sx], 65536\n\t" \
    "s_add_u32 %[t], %[t], 1\n\t" \
    "s_add_u32 %[oo], %[oo], 262144\n\t" \
    "s_waitcnt vmcnt(6)\n\t" \
    "s_cmp_gt_u32 %[t], " STR(SQUICK) " + 1\n\t" \
    "s_cbranch_scc1 B0_%=\n\t" \
    "s_waitcnt vmcnt(0)\n\t" \
    "B0_%=:\n\t" \
    "s_barrier\n\t" \
    "s_setprio 1\n\t" \
    "s_add_u32 m0, %[sd], 16384\n\t" \
    "s_add_u32 %[a], %[sx], 81920\n\t" \
    "v_mfma_f32_16x16x32_bf16 v[196:199], v[68:71], v[20:23], 0\n\t" \
    "ds_read_b128 v[44:47], %[xp] offset:38912\n\t" \
    "v_mfma_f32_16x16x32_bf16 v[200:203], v[76:79], v[20:23], 0\n\t" \
    "ds_read_b128 v[48:51], %[xp] offset:39936\n\t" \
    "v_mfma_f32_16x16x32_bf16 v[204:207], v[68:71], v[24:27], 0\n\t" \
    "s_waitcnt lgkmcnt(4)\n\t" \
    "v_mfma_f32_16x16x32_bf16 v[208:211], v[76:79], v[24:27], 0\n\t" \
    "v_mfma_f32_16x16x32_bf16 v[196:199], v[72:75], v[28:31], v[196:199]\n\t" \
    "ds_read_b128 v[20:23], %[xp] offset:40960\n\t" \
    "v_mfma_f32_16x16x32_bf16 v[200:203], v[80:83], v[28:31], v[200:203]\n\t" \
    "ds_read_b128 v[24:27], %[xp] offset:41984\n\t" \
    "v_mfma_f32_16x16x32_bf16 v[204:207], v[72:75], v[32:35], v[204:207]\n\t" \
    "s_waitcnt lgkmcnt(4)\n\t" \
    "v_mfma_f32_16x16x32_bf16 v[208:211], v[80:83], v[32:35], v[208:211]\n\t" \
    "v_mfma_f32_16x16x32_bf16 v[196:199], v[84:87], v[36:39], v[196:199]\n\t" \
    "ds_read_b128 v[28:31], %[xp] offset:43008\n\t" \
    "v_mfma_f32_16x16x32_bf16 v[200:203], v[92:95], v[36:39], v[200:203]\n\t" \
    "ds_read_b128 v[32:35], %[xp] offset:44032\n\t" \
    "v_mfma_f32_16x16x32_bf16 v[204:207], v[84:87], v[40:43], v[204:207]\n\t" \
    "s_waitcnt lgkmcnt(4)\n\t" \
    "v_mfma_f32_16x16x32_bf16 v[208:211], v[92:95], v[40:43], v[208:211]\n\t" \
    "v_mfma_f32_16x16x32_bf16 v[196:199], v[88:91], v[44:47], v[196:199]\n\t" \
    "ds_read_b128 v[36:39], %[xp] offset:45056\n\t" \
    "v_mfma_f32_16x16x32_bf16 v[200:203], v[96:99], v[44:47], v[200:203]\n\t" \
    "ds_read_b128 v[40:43], %[xp] offset:46080\n\t" \
    "v_mfma_f32_16x16x32_bf16 v[204:207], v[88:91], v[48:51], v[204:207]\n\t" \
    "s_waitcnt lgkmcnt(4)\n\t" \
    "v_mfma_f32_16x16x32_bf16 v[208:211], v[96:99], v[48:51], v[208:211]\n\t" \
    "v_mfma_f32_16x16x32_bf16 v[196:199], v[100:103], v[20:23], v[196:199]\n\t" \
    "ds_read_b128 v[44:47], %[xp] offset:47104\n\t" \
    "v_mfma_f32_16x16x32_bf16 v[200:203], v[108:111], v[20:23], v[200:203]\n\t" \
    "ds_read_b128 v[48:51], %[xp] offset:48128\n\t" \
    "v_mfma_f32_16x16x32_bf16 v[204:207], v[100:103], v[24:27], v[204:207]\n\t" \
    "s_waitcnt lgkmcnt(4)\n\t" \
    "v_mfma_f32_16x16x32_bf16 v[208:211], v[108:111], v[24:27], v[208:211]\n\t" \
    "buffer_load_dwordx4 %[l16], %[rs], %[a] offen offset:0 sc1 lds\n\t" \
    "v_mfma_f32_16x16x32_bf16 v[196:199], v[104:107], v[28:31], v[196:199]\n\t" \
    "ds_read_b128 v[20:23], %[xp] offset:49152\n\t" \
    "v_mfma_f32_16x16x32_bf16 v[200:203], v[112:115], v[28:31], v[200:203]\n\t" \
    "ds_read_b128 v[24:27], %[xp] offset:50176\n\t" \
    "v_mfma_f32_16x16x32_bf16 v[204:207], v[104:107], v[32:35], v[204:207]\n\t" \
    "s_waitcnt lgkmcnt(4)\n\t" \
    "v_mfma_f32_16x16x32_bf16 v[208:211], v[112:115], v[32:35], v[208:211]\n\t" \
    "buffer_load_dwordx4 %[l16], %[rs], %[a] offen offset:1024 sc1 lds\n\t" \
    "v_mfma_f32_16x16x32_bf16 v[196:199], v[116:119], v[36:39], v[196:199]\n\t" \
    "ds_read_b128 v[28:31], %[xp] offset:51200\n\t" \
    "v_mfma_f32_16x16x32_bf16 v[200:203], v[124:127], v[36:39], v[200:203]\n\t" \
    "ds_read_b128 v[32:35], %[xp] offset:52224\n\t" \
    "v_mfma_f32_16x16x32_bf16 v[204:207], v[116:119], v[40:43], v[204:207]\n\t" \
    "s_waitcnt lgkmcnt(4)\n\t" \
    "v_mfma_f32_16x16x32_bf16 v[208:211], v[124:127], v[40:43], v[208:211]\n\t" \
    "buffer_load_dwordx4 %[l16], %[rs], %[a] offen offset:2048 sc1 lds\n\t" \
    "v_mfma_f32_16x16x32_bf16 v[196:199], v[120:123], v[44:47], v[196:199]\n\t" \
    "ds_read_b128 v[36:39], %[xp] offset:53248\n\t" \
    "v_mfma_f32_16x16x32_bf16 v[200:203], v[128:131], v[44:47], v[200:203]\n\t" \
    "ds_read_b128 v[40:43], %[xp] offset:54272\n\t" \
    "v_mfma_f32_16x16x32_bf16 v[204:207], v[120:123], v[48:51], v[204:207]\n\t" \
    "s_waitcnt lgkmcnt(4)\n\t" \
    "v_mfma_f32_16x16x32_bf16 v[208:211], v[128:131], v[48:51], v[208:211]\n\t" \
    "buffer_load_dwordx4 %[l16], %[rs], %[a] offen offset:3072 sc1 lds\n\t" \
    "s_waitcnt vmcnt(4)\n\t" \
    "s_barrier\n\t" \
    "s_setprio 1\n\t" \
    "s_add_u32 m0, %[sd], 32768\n\t" \
    "s_add_u32 %[a], %[sx], 131072\n\t" \
    "s_add_u32 %[b], %[oo], 0x1000\n\t" \
    "s_cmp_lg_u32 %[t], 1\n\t" \
    "s_cselect_b64 %[em], -1, 0\n\t" \
    "v_mfma_f32_16x16x32_bf16 v[196:199], v[132:135], v[20:23], v[196:199]\n\t" \
    "ds_read_b128 v[44:47], %[xp] offset:55296\n\t" \
    "v_mfma_f32_16x16x32_bf16 v[200:203], v[140:143], v[20:23], v[200:203]\n\t" \
    "ds_read_b128 v[48:51], %[xp] offset:56320\n\t" \
    "v_mfma_f32_16x16x32_bf16 v[204:207], v[132:135], v[24:27], v[204:207]\n\t" \
    "ds_read_b128 v[52:55], %[pa] offset:0\n\t" \
    "s_waitcnt lgkmcnt(5)\n\t" \
    "v_mfma_f32_16x16x32_bf16 v[208:211], v[140:143], v[24:27], v[208:211]\n\t" \
    "buffer_load_dwordx4 %[l16], %[rs], %[a] offen offset:0 sc1 lds\n\t" \
    "v_mfma_f32_16x16x32_bf16 v[196:199], v[136:139], v[28:31], v[196:199]\n\t" \
    "ds_read_b128 v[20:23], %[xp] offset:57344\n\t" \
    "v_mfma_f32_16x16x32_bf16 v[200:203], v[144:147], v[28:31], v[200:203]\n\t" \
    "ds_read_b128 v[24:27], %[xp] offset:58368\n\t" \
    "v_mfma_f32_16x16x32_bf16 v[204:207], v[136:139], v[32:35], v[204:207]\n\t" \
    "ds_read_b128 v[56:59], %[pa] offset:1024\n\t" \
    "s_waitcnt lgkmcnt(6)\n\t" \
    "v_mfma_f32_16x16x32_bf16 v[208:211], v[144:147], v[32:35], v[208:211]\n\t" \
    "buffer_load_dwordx4 %[l16], %[rs], %[a] offen offset:1024 sc1 lds\n\t" \
    "v_mfma_f32_16x16x32_bf16 v[196:199], v[148:151], v[36:39], v[196:199]\n\t" \
    "ds_read_b128 v[28:31], %[xp] offset:59392\n\t" \
    "v_mfma_f32_16x16x32_bf16 v[200:203], v[156:159], v[36:39], v[200:203]\n\t" \
    "ds_read_b128 v[32:35], %[xp] offset:60416\n\t" \
    "v_mfma_f32_16x16x32_bf16 v[204:207], v[148:151], v[40:43], v[204:207]\n\t" \
    "ds_read_b128 v[60:63], %[pa] offset:2048\n\t" \
    "s_waitcnt lgkmcnt(7)\n\t" \
    "v_mfma_f32_16x16x32_bf16 v[208:211], v[156:159], v[40:43], v[208:211]\n\t" \
    "buffer_load_dwordx4 %[l16], %[rs], %[a] offen offset:2048 sc1 lds\n\t" \
    "v_mfma_f32_16x16x32_bf16 v[196:199], v[152:155], v[44:47], v[196:199]\n\t" \
    "ds_read_b128 v[36:39], %[xp] offset:61440\n\t" \
    "s_waitcnt lgkmcnt(7)\n\t" \
    "v_add_f32 v0, v0, v52\n\t" \
    "v_mfma_f32_16x16x32_bf16 v[200:203], v[160:163], v[44:47], v[200:203]\n\t" \
    "ds_read_b128 v[40:43], %[xp] offset:62464\n\t" \
    "v_add_f32 v1, v1, v53\n\t" \
    "v_mfma_f32_16x16x32_bf16 v[204:207], v[152:155], v[48:51], v[204:207]\n\t" \
    "ds_read_b128 v[64:67], %[pa] offset:3072\n\t" \
    "s_waitcnt lgkmcnt(7)\n\t" \
    "v_mfma_f32_16x16x32_bf16 v[208:211], v[160:163], v[48:51], v[208:211]\n\t" \
    "buffer_load_dwordx4 %[l16], %[rs], %[a] offen offset:3072 sc1 lds\n\t" \
    "v_add_f32 v2, v2, v54\n\t" \
    "v_add_f32 v3, v3, v55\n\t" \
    "v_mfma_f32_16x16x32_bf16 v[196:199], v[164:167], v[20:23], v[196:199]\n\t" \
    "ds_read_b128 v[44:47], %[xp] offset:63488\n\t" \
    "s_waitcnt lgkmcnt(7)\n\t" \
    "v_add_f32 v4, v4, v56\n\t" \
    "v_mfma_f32_16x16x32_bf16 v[200:203], v[172:175], v[20:23], v[200:203]\n\t" \
    "ds_read_b128 v[48:51], %[xp] offset:64512\n\t" \
    "v_add_f32 v5, v5, v57\n\t" \
    "v_mfma_f32_16x16x32_bf16 v[204:207], v[164:167], v[24:27], v[204:207]\n\t" \
    "v_add_f32 v6, v6, v58\n\t" \
    "s_waitcnt lgkmcnt(6)\n\t" \
    "v_mfma_f32_16x16x32_bf16 v[208:211], v[172:175], v[24:27], v[208:211]\n\t" \
    "v_add_f32 v7, v7, v59\n\t" \
    "s_waitcnt lgkmcnt(5)\n\t" \
    "v_add_f32 v8, v8, v60\n\t" \
    "v_add_f32 v9, v9, v61\n\t" \
    "v_mfma_f32_16x16x32_bf16 v[196:199], v[168:171], v[28:31], v[196:199]\n\t" \
    "ds_read_b128 v[20:23], %[xp] offset:0\n\t" \
    "v_add_f32 v10, v10, v62\n\t" \
    "v_mfma_f32_16x16x32_bf16 v[200:203], v[176:179], v[28:31], v[200:203]\n\t" \
    "ds_read_b128 v[24:27], %[xp] offset:1024\n\t" \
    "v_add_f32 v11, v11, v63\n\t" \
    "v_mfma_f32_16x16x32_bf16 v[204:207], v[168:171], v[32:35], v[204:207]\n\t" \
    "s_waitcnt lgkmcnt(4)\n\t" \
    "v_add_f32 v12, v12, v64\n\t" \
    "v_mfma_f32_16x16x32_bf16 v[208:211], v[176:179], v[32:35], v[208:211]\n\t" \
    "v_add_f32 v13, v13, v65\n\t" \
    "v_add_f32 v14, v14, v66\n\t" \
    "v_add_f32 v15, v15, v67\n\t" \
    "v_mfma_f32_16x16x32_bf16 v[196:199], v[180:183], v[36:39], v[196:199]\n\t" \
    "ds_read_b128 v[28:31], %[xp] offset:2048\n\t" \
    "v_cvt_pk_bf16_f32 v52, v0, v4\n\t" \
    "v_mfma_f32_16x16x32_bf16 v[200:203], v[188:191], v[36:39], v[200:203]\n\t" \
    "ds_read_b128 v[32:35], %[xp] offset:3072\n\t" \
    "v_cvt_pk_bf16_f32 v53, v1, v5\n\t" \
    "v_mfma_f32_16x16x32_bf16 v[204:207], v[180:183], v[40:43], v[204:207]\n\t" \
    "v_cvt_pk_bf16_f32 v54, v2, v6\n\t" \
    "s_waitcnt lgkmcnt(4)\n\t" \
    "v_mfma_f32_16x16x32_bf16 v[208:211], v[188:191], v[40:43], v[208:211]\n\t" \
    "v_cvt_pk_bf16_f32 v55, v3, v7\n\t" \
    "v_cvt_pk_bf16_f32 v56, v8, v12\n\t" \
    "v_cvt_pk_bf16_f32 v57, v9, v13\n\t" \
    "v_mfma_f32_16x16x32_bf16 v[196:199], v[184:187], v[44:47], v[196:199]\n\t" \
    "ds_read_b128 v[36:39], %[xp] offset:4096\n\t" \
    "v_cvt_pk_bf16_f32 v58, v10, v14\n\t" \
    "v_mfma_f32_16x16x32_bf16 v[200:203], v[192:195], v[44:47], v[200:203]\n\t" \
    "ds_read_b128 v[40:43], %[xp] offset:5120\n\t" \
    "v_cvt_pk_bf16_f32 v59, v11, v15\n\t" \
    "v_mfma_f32_16x16x32_bf16 v[204:207], v[184:187], v[48:51], v[204:207]\n\t" \
    "s_waitcnt lgkmcnt(4)\n\t" \
    "v_mfma_f32_16x16x32_bf16 v[208:211], v[192:195], v[48:51], v[208:211]\n\t" \
    "s_mov_b64 exec, %[em]\n\t" \
    "buffer_store_dwordx4 v[52:55], %[l16], %[rs], %[oo] offen sc1\n\t" \
    "buffer_store_dwordx4 v[56:59], %[l16], %[rs], %[b] offen sc1\n\t" \
    "s_mov_b64 exec, -1\n\t" \
    "s_add_u32 %[sx], %[sx], 65536\n\t" \
    "s_add_u32 %[t], %[t], 1\n\t" \
    "s_add_u32 %[oo], %[oo], 262144\n\t" \
    "s_waitcnt vmcnt(6)\n\t" \
    "s_cmp_gt_u32 %[t], " STR(SQUICK) " + 1\n\t" \
    "s_cbranch_scc1 B1_%=\n\t" \
    "s_waitcnt vmcnt(0)\n\t" \
    "B1_%=:\n\t" \
    "s_barrier\n\t" \
    "s_cmp_lt_u32 %[t], %[te]\n\t" \
    "s_cbranch_scc1 L1_%=\n\t" \
    "s_cmp_eq_u32 %[fin], %[te]\n\t" \
    "s_cbranch_scc0 E_%=\n\t" \
    "s_nop 4\n\t" \
    "s_barrier\n\t" \
    "ds_read_b128 v[52:55], %[pa] offset:0\n\t" \
    "ds_read_b128 v[56:59], %[pa] offset:1024\n\t" \
    "ds_read_b128 v[60:63], %[pa] offset:2048\n\t" \
    "ds_read_b128 v[64:67], %[pa] offset:3072\n\t" \
    "s_add_u32 %[b], %[oo], 0x1000\n\t" \
    "s_waitcnt lgkmcnt(0)\n\t" \
    "v_add_f32 v196, v196, v52\n\t" \
    "v_add_f32 v197, v197, v53\n\t" \
    "v_add_f32 v198, v198, v54\n\t" \
    "v_add_f32 v199, v199, v55\n\t" \
    "v_add_f32 v200, v200, v56\n\t" \
    "v_add_f32 v201, v201, v57\n\t" \
    "v_add_f32 v202, v202, v58\n\t" \
    "v_add_f32 v203, v203, v59\n\t" \
    "v_add_f32 v204, v204, v60\n\t" \
    "v_add_f32 v205, v205, v61\n\t" \
    "v_add_f32 v206, v206, v62\n\t" \
    "v_add_f32 v207, v207, v63\n\t" \
    "v_add_f32 v208, v208, v64\n\t" \
    "v_add_f32 v209, v209, v65\n\t" \
    "v_add_f32 v210, v210, v66\n\t" \
    "v_add_f32 v211, v211, v67\n\t" \
    "v_cvt_pk_bf16_f32 v52, v196, v200\n\t" \
    "v_cvt_pk_bf16_f32 v53, v197, v201\n\t" \
    "v_cvt_pk_bf16_f32 v54, v198, v202\n\t" \
    "v_cvt_pk_bf16_f32 v55, v199, v203\n\t" \
    "v_cvt_pk_bf16_f32 v56, v204, v208\n\t" \
    "v_cvt_pk_bf16_f32 v57, v205, v209\n\t" \
    "v_cvt_pk_bf16_f32 v58, v206, v210\n\t" \
    "v_cvt_pk_bf16_f32 v59, v207, v211\n\t" \
    "buffer_store_dwordx4 v[52:55], %[l16], %[rs], %[oo] offen sc1\n\t" \
    "buffer_store_dwordx4 v[56:59], %[l16], %[rs], %[b] offen sc1\n\t" \
    "E_%=:\n\t" \
    "s_setprio 0\n\t" \
    "s_waitcnt lgkmcnt(0)\n\t" \
    "s_nop 7\n\t" \
    : [t] "+s"(t), [sx] "+s"(sx), [oo] "+s"(oo), [a] "=&s"(tmp), [b] "=&s"(tmp2), [em] "=&s"(em), "+{v[20:27]}"(xf[0]), "+{v[28:35]}"(xf[1]), "+{v[36:43]}"(xf[2]), "+{v[196:203]}"(ac[0]), "+{v[204:211]}"(ac[1]) \
    : [te] "s"(TE_), [fin] "s"(FIN_), [kh] "s"(kh), [sd] "s"(sd), [rs] "s"(rs), [l16] "v"(l16), [xp] "v"(xp), [pa] "v"(pa), [fm] "s"(fm), [pm] "s"(pm), [fo] "s"(fo), [po] "s"(po), [pd] "s"(pd), [tag] "s"(P.tag), "{v[68:75]}"(w[0][0]), "{v[76:83]}"(w[0][1]), "{v[84:91]}"(w[1][0]), "{v[92:99]}"(w[1][1]), "{v[100:107]}"(w[2][0]), "{v[108:115]}"(w[2][1]), "{v[116:123]}"(w[3][0]), "{v[124:131]}"(w[3][1]), "{v[132:139]}"(w[4][0]), "{v[140:147]}"(w[4][1]), "{v[148:155]}"(w[5][0]), "{v[156:163]}"(w[5][1]), "{v[164:171]}"(w[6][0]), "{v[172:179]}"(w[6][1]), "{v[180:187]}"(w[7][0]), "{v[188:195]}"(w[7][1]) \
    : "memory", "scc", "v0", "v1", "v2", "v3", "v4", "v5", "v6", "v7", "v8", "v9", "v10", "v11", "v12", "v13", "v14", "v15", "v44", "v45", "v46", "v47", "v48", "v49", "v50", "v51", "v52", "v53", "v54", "v55", "v56", "v57", "v58", "v59", "v60", "v61", "v62", "v63", "v64", "v65", "v66", "v67")

#define S_LOOP_V(TE_, FIN_) \
  asm volatile( \
    "s_cmp_eq_u32 %[kh], 0\n\t" \
    "s_cbranch_scc0 L1_%=\n\t" \
    "L0_%=:\n\t" \
    "s_setprio 0\n\t" \
    "s_nop 3\n\t" \
    "ds_write_b128 %[pa], v[196:199] offset:0\n\t" \
    "ds_write_b128 %[pa], v[200:203] offset:1024\n\t" \
    "ds_write_b128 %[pa], v[204:207] offset:2048\n\t" \
    "ds_write_b128 %[pa], v[208:211] offset:3072\n\t" \
    "s_sub_u32 %[a], %[t], 1\n\t" \
    "v_mov_b32 v53, %[tag]\n\t" \
    "v_mov_b32 v52, %[a]\n\t" \
    "s_sub_u32 %[b], %[t], 2\n\t" \
    "s_cmp_lt_u32 %[b], " STR(SQUICK) "\n\t" \
    "s_cselect_b64 exec, %[fm], 0\n\t" \
    "buffer_store_dwordx2 v[52:53], %[l16], %[rs], %[fo] offen sc1\n\t" \
    "s_mov_b64 exec, -1\n\t" \
    "s_add_u32 m0, %[sd], 49152\n\t" \
    "s_add_u32 %[a], %[sx], 81920\n\t" \
    "s_waitcnt lgkmcnt(0)\n\t" \
    "buffer_load_dwordx4 %[l16], %[rs], %[a] offen offset:0 sc1 lds\n\t" \
    "buffer_load_dwordx4 %[l16], %[rs], %[a] offen offset:1024 sc1 lds\n\t" \
    "buffer_load_dwordx4 %[l16], %[rs], %[a] offen offset:2048 sc1 lds\n\t" \
    "buffer_load_dwordx4 %[l16], %[rs], %[a] offen offset:3072 sc1 lds\n\t" \
    "v_mfma_f32_16x16x32_bf16 v[0:3], v[20:23], v[68:71], 0\n\t" \
    "ds_read_b128 v[44:47], %[xp] offset:6144\n\t" \
    "v_mfma_f32_16x16x32_bf16 v[4:7], v[20:23], v[76:79], 0\n\t" \
    "ds_read_b128 v[48:51], %[xp] offset:7168\n\t" \
    "v_mfma_f32_16x16x32_bf16 v[8:11], v[24:27], v[68:71], 0\n\t" \
    "v_mfma_f32_16x16x32_bf16 v[12:15], v[24:27], v[76:79], 0\n\t" \
    "v_mfma_f32_16x16x32_bf16 v[0:3], v[28:31], v[72:75], v[0:3]\n\t" \
    "ds_read_b128 v[20:23], %[xp] offset:8192\n\t" \
    "v_mfma_f32_16x16x32_bf16 v[4:7], v[28:31], v[80:83], v[4:7]\n\t" \
    "ds_read_b128 v[24:27], %[xp] offset:9216\n\t" \
    "v_mfma_f32_16x16x32_bf16 v[8:11], v[32:35], v[72:75], v[8:11]\n\t" \
    "v_mfma_f32_16x16x32_bf16 v[12:15], v[32:35], v[80:83], v[12:15]\n\t" \
    "v_mfma_f32_16x16x32_bf16 v[0:3], v[36:39], v[84:87], v[0:3]\n\t" \
    "ds_read_b128 v[28:31], %[xp] offset:10240\n\t" \
    "v_mfma_f32_16x16x32_bf16 v[4:7], v[36:39], v[92:95], v[4:7]\n\t" \
    "ds_read_b128 v[32:35], %[xp] offset:11264\n\t" \
    "v_mfma_f32_16x16x32_bf16 v[8:11], v[40:43], v[84:87], v[8:11]\n\t" \
    "s_waitcnt lgkmcnt(4)\n\t" \
    "v_mfma_f32_16x16x32_bf16 v[12:15], v[40:43], v[92:95], v[12:15]\n\t" \
    "v_mfma_f32_16x16x32_bf16 v[0:3], v[44:47], v[88:91], v[0:3]\n\t" \
    "ds_read_b128 v[36:39], %[xp] offset:12288\n\t" \
    "v_mfma_f32_16x16x32_bf16 v[4:7], v[44:47], v[96:99], v[4:7]\n\t" \
    "ds_read_b128 v[40:43], %[xp] offset:13312\n\t" \
    "v_mfma_f32_16x16x32_bf16 v[8:11], v[48:51], v[88:91], v[8:11]\n\t" \
    "s_waitcnt lgkmcnt(4)\n\t" \
    "v_mfma_f32_16x16x32_bf16 v[12:15], v[48:51], v[96:99], v[12:15]\n\t" \
    "v_mfma_f32_16x16x32_bf16 v[0:3], v[20:23], v[100:103], v[0:3]\n\t" \
    "ds_read_b128 v[44:47], %[xp] offset:14336\n\t" \
    "v_mfma_f32_16x16x32_bf16 v[4:7], v[20:23], v[108:111], v[4:7]\n\t" \
    "ds_read_b128 v[48:51], %[xp] offset:15360\n\t" \
    "v_mfma_f32_16x16x32_bf16 v[8:11], v[24:27], v[100:103], v[8:11]\n\t" \
    "s_waitcnt lgkmcnt(4)\n\t" \
    "v_mfma_f32_16x16x32_bf16 v[12:15], v[24:27], v[108:111], v[12:15]\n\t" \
    "v_mfma_f32_16x16x32_bf16 v[0:3], v[28:31], v[104:107], v[0:3]\n\t" \
    "ds_read_b128 v[20:23], %[xp] offset:16384\n\t" \
    "v_mfma_f32_16x16x32_bf16 v[4:7], v[28:31], v[112:115], v[4:7]\n\t" \
    "ds_read_b128 v[24:27], %[xp] offset:17408\n\t" \
    "v_mfma_f32_16x16x32_bf16 v[8:11], v[32:35], v[104:107], v[8:11]\n\t" \
    "s_waitcnt lgkmcnt(4)\n\t" \
    "v_mfma_f32_16x16x32_bf16 v[12:15], v[32:35], v[112:115], v[12:15]\n\t" \
    "v_mfma_f32_16x16x32_bf16 v[0:3], v[36:39], v[116:119], v[0:3]\n\t" \
    "ds_read_b128 v[28:31], %[xp] offset:18432\n\t" \
    "v_mfma_f32_16x16x32_bf16 v[4:7], v[36:39], v[124:127], v[4:7]\n\t" \
    "ds_read_b128 v[32:35], %[xp] offset:19456\n\t" \
    "v_mfma_f32_16x16x32_bf16 v[8:11], v[40:43], v[116:119], v[8:11]\n\t" \
    "s_waitcnt lgkmcnt(4)\n\t" \
    "v_mfma_f32_16x16x32_bf16 v[12:15], v[40:43], v[124:127], v[12:15]\n\t" \
    "v_mfma_f32_16x16x32_bf16 v[0:3], v[44:47], v[120:123], v[0:3]\n\t" \
    "ds_read_b128 v[36:39], %[xp] offset:20480\n\t" \
    "v_mfma_f32_16x16x32_bf16 v[4:7], v[44:47], v[128:131], v[4:7]\n\t" \
    "ds_read_b128 v[40:43], %[xp] offset:21504\n\t" \
    "v_mfma_f32_16x16x32_bf16 v[8:11], v[48:51], v[120:123], v[8:11]\n\t" \
    "s_waitcnt lgkmcnt(4)\n\t" \
    "v_mfma_f32_16x16x32_bf16 v[12:15], v[48:51], v[128:131], v[12:15]\n\t" \
    "s_waitcnt vmcnt(4)\n\t" \
    "s_barrier\n\t" \
    "s_setprio 0\n\t" \
    "s_sub_u32 %[a], %[t], 1\n\t" \
    "v_mov_b32 v53, %[tag]\n\t" \
    "v_mov_b32 v52, %[a]\n\t" \
    "s_cmp_gt_u32 %[t], 1\n\t" \
    "s_cselect_b64 exec, %[fm], 0\n\t" \
    "buffer_store_dwordx2 v[52:53], %[l16], %[rs], %[fo] offen sc1\n\t" \
    "s_mov_b64 exec, %[pm]\n\t" \
    "s_mov_b32 m0, %[pd]\n\t" \
    "s_add_u32 %[a], %[sx], %[po]\n\t" \
    "buffer_load_dwordx4 %[l16], %[rs], %[a] offen sc1 lds\n\t" \
    "s_mov_b64 exec, -1\n\t" \
    "s_add_u32 m0, %[sd], 0\n\t" \
    "s_add_u32 %[a], %[sx], 131072\n\t" \
    "buffer_load_dwordx4 %[l16], %[rs], %[a] offen offset:0 sc1 lds\n\t" \
    "buffer_load_dwordx4 %[l16], %[rs], %[a] offen offset:1024 sc1 lds\n\t" \
    "buffer_load_dwordx4 %[l16], %[rs], %[a] offen offset:2048 sc1 lds\n\t" \
    "buffer_load_dwordx4 %[l16], %[rs], %[a] offen offset:3072 sc1 lds\n\t" \
    "v_mfma_f32_16x16x32_bf16 v[0:3], v[20:23], v[132:135], v[0:3]\n\t" \
    "ds_read_b128 v[44:47], %[xp] offset:22528\n\t" \
    "v_mfma_f32_16x16x32_bf16 v[4:7], v[20:23], v[140:143], v[4:7]\n\t" \
    "ds_read_b128 v[48:51], %[xp] offset:23552\n\t" \
    "v_mfma_f32_16x16x32_bf16 v[8:11], v[24:27], v[132:135], v[8:11]\n\t" \
    "s_waitcnt lgkmcnt(4)\n\t" \
    "v_mfma_f32_16x16x32_bf16 v[12:15], v[24:27], v[140:143], v[12:15]\n\t" \
    "v_mfma_f32_16x16x32_bf16 v[0:3], v[28:31], v[136:139], v[0:3]\n\t" \
    "ds_read_b128 v[20:23], %[xp] offset:24576\n\t" \
    "v_mfma_f32_16x16x32_bf16 v[4:7], v[28:31], v[144:147], v[4:7]\n\t" \
    "ds_read_b128 v[24:27], %[xp] offset:25600\n\t" \
    "v_mfma_f32_16x16x32_bf16 v[8:11], v[32:35], v[136:139], v[8:11]\n\t" \
    "s_waitcnt lgkmcnt(4)\n\t" \
    "v_mfma_f32_16x16x32_bf16 v[12:15], v[32:35], v[144:147], v[12:15]\n\t" \
    "v_mfma_f32_16x16x32_bf16 v[0:3], v[36:39], v[148:151], v[0:3]\n\t" \
    "ds_read_b128 v[28:31], %[xp] offset:26624\n\t" \
    "v_mfma_f32_16x16x32_bf16 v[4:7], v[36:39], v[156:159], v[4:7]\n\t" \
    "ds_read_b128 v[32:35], %[xp] offset:27648\n\t" \
    "v_mfma_f32_16x16x32_bf16 v[8:11], v[40:43], v[148:151], v[8:11]\n\t" \
    "s_waitcnt lgkmcnt(4)\n\t" \
    "v_mfma_f32_16x16x32_bf16 v[12:15], v[40:43], v[156:159], v[12:15]\n\t" \
    "v_mfma_f32_16x16x32_bf16 v[0:3], v[44:47], v[152:155], v[0:3]\n\t" \
    "ds_read_b128 v[36:39], %[xp] offset:28672\n\t" \
    "v_mfma_f32_16x16x32_bf16 v[4:7], v[44:47], v[160:163], v[4:7]\n\t" \
    "ds_read_b128 v[40:43], %[xp] offset:29696\n\t" \
    "v_mfma_f32_16x16x32_bf16 v[8:11], v[48:51], v[152:155], v[8:11]\n\t" \
    "s_waitcnt lgkmcnt(4)\n\t" \
    "v_mfma_f32_16x16x32_bf16 v[12:15], v[48:51], v[160:163], v[12:15]\n\t" \
    "v_mfma_f32_16x16x32_bf16 v[0:3], v[20:23], v[164:167], v[0:3]\n\t" \
    "ds_read_b128 v[44:47], %[xp] offset:30720\n\t" \
    "v_mfma_f32_16x16x32_bf16 v[4:7], v[20:23], v[172:175], v[4:7]\n\t" \
    "ds_read_b128 v[48:51], %[xp] offset:31744\n\t" \
    "v_mfma_f32_16x16x32_bf16 v[8:11], v[24:27], v[164:167], v[8:11]\n\t" \
    "s_waitcnt lgkmcnt(4)\n\t" \
    "v_mfma_f32_16x16x32_bf16 v[12:15], v[24:27], v[172:175], v[12:15]\n\t" \
    "v_mfma_f32_16x16x32_bf16 v[0:3], v[28:31], v[168:171], v[0:3]\n\t" \
    "ds_read_b128 v[20:23], %[xp] offset:32768\n\t" \
    "v_mfma_f32_16x16x32_bf16 v[4:7], v[28:31], v[176:179], v[4:7]\n\t" \
    "ds_read_b128 v[24:27], %[xp] offset:33792\n\t" \
    "v_mfma_f32_16x16x32_bf16 v[8:11], v[32:35], v[168:171], v[8:11]\n\t" \
    "s_waitcnt lgkmcnt(4)\n\t" \
    "v_mfma_f32_16x16x32_bf16 v[12:15], v[32:35], v[176:179], v[12:15]\n\t" \
    "v_mfma_f32_16x16x32_bf16 v[0:3], v[36:39], v[180:183], v[0:3]\n\t" \
    "ds_read_b128 v[28:31], %[xp] offset:34816\n\t" \
    "v_mfma_f32_16x16x32_bf16 v[4:7], v[36:39], v[188:191], v[4:7]\n\t" \
    "ds_read_b128 v[32:35], %[xp] offset:35840\n\t" \
    "v_mfma_f32_16x16x32_bf16 v[8:11], v[40:43], v[180:183], v[8:11]\n\t" \
    "s_waitcnt lgkmcnt(4)\n\t" \
    "v_mfma_f32_16x16x32_bf16 v[12:15], v[40:43], v[188:191], v[12:15]\n\t" \
    "v_mfma_f32_16x16x32_bf16 v[0:3], v[44:47], v[184:187], v[0:3]\n\t" \
    "ds_read_b128 v[36:39], %[xp] offset:36864\n\t" \
    "v_mfma_f32_16x16x32_bf16 v[4:7], v[44:47], v[192:195], v[4:7]\n\t" \
    "ds_read_b128 v[40:43], %[xp] offset:37888\n\t" \
    "v_mfma_f32_16x16x32_bf16 v[8:11], v[48:51], v[184:187], v[8:11]\n\t" \
    "s_waitcnt lgkmcnt(4)\n\t" \
    "v_mfma_f32_16x16x32_bf16 v[12:15], v[48:51], v[192:195], v[12:15]\n\t" \
    "s_add_u32 %[sx], %[sx], 65536\n\t" \
    "s_add_u32 %[t], %[t], 1\n\t" \
    "s_waitcnt vmcnt(4)\n\t" \
    "s_barrier\n\t" \
    "s_setprio 0\n\t" \
    "s_nop 3\n\t" \
    "ds_write_b128 %[pa], v[0:3] offset:0\n\t" \
    "ds_write_b128 %[pa], v[4:7] offset:1024\n\t" \
    "ds_write_b128 %[pa], v[8:11] offset:2048\n\t" \
    "ds_write_b128 %[pa], v[12:15] offset:3072\n\t" \
    "s_sub_u32 %[a], %[t], 1\n\t" \
    "v_mov_b32 v53, %[tag]\n\t" \
    "v_mov_b32 v52, %[a]\n\t" \
    "s_sub_u32 %[b], %[t], 2\n\t" \
    "s_cmp_lt_u32 %[b], " STR(SQUICK) "\n\t" \
    "s_cselect_b64 exec, %[fm], 0\n\t" \
    "buffer_store_dwordx2 v[52:53], %[l16], %[rs], %[fo] offen sc1\n\t" \
    "s_mov_b64 exec, -1\n\t" \
    "s_add_u32 m0, %[sd], 16384\n\t" \
    "s_add_u32 %[a], %[sx], 81920\n\t" \
    "s_waitcnt lgkmcnt(0)\n\t" \
    "buffer_load_dwordx4 %[l16], %[rs], %[a] offen offset:0 sc1 lds\n\t" \
    "buffer_load_dwordx4 %[l16], %[rs], %[a] offen offset:1024 sc1 lds\n\t" \
    "buffer_load_dwordx4 %[l16], %[rs], %[a] offen offset:2048 sc1 lds\n\t" \
    "buffer_load_dwordx4 %[l16], %[rs], %[a] offen offset:3072 sc1 lds\n\t" \
    "v_mfma_f32_16x16x32_bf16 v[196:199], v[20:23], v[68:71], 0\n\t" \
    "ds_read_b128 v[44:47], %[xp] offset:38912\n\t" \
    "v_mfma_f32_16x16x32_bf16 v[200:203], v[20:23], v[76:79], 0\n\t" \
    "ds_read_b128 v[48:51], %[xp] offset:39936\n\t" \
    "v_mfma_f32_16x16x32_bf16 v[204:207], v[24:27], v[68:71], 0\n\t" \
    "v_mfma_f32_16x16x32_bf16 v[208:211], v[24:27], v[76:79], 0\n\t" \
    "v_mfma_f32_16x16x32_bf16 v[196:199], v[28:31], v[72:75], v[196:199]\n\t" \
    "ds_read_b128 v[20:23], %[xp] offset:40960\n\t" \
    "v_mfma_f32_16x16x32_bf16 v[200:203], v[28:31], v[80:83], v[200:203]\n\t" \
    "ds_read_b128 v[24:27], %[xp] offset:41984\n\t" \
    "v_mfma_f32_16x16x32_bf16 v[204:207], v[32:35], v[72:75], v[204:207]\n\t" \
    "v_mfma_f32_16x16x32_bf16 v[208:211], v[32:35], v[80:83], v[208:211]\n\t" \
    "v_mfma_f32_16x16x32_bf16 v[196:199], v[36:39], v[84:87], v[196:199]\n\t" \
    "ds_read_b128 v[28:31], %[xp] offset:43008\n\t" \
    "v_mfma_f32_16x16x32_bf16 v[200:203], v[36:39], v[92:95], v[200:203]\n\t" \
    "ds_read_b128 v[32:35], %[xp] offset:44032\n\t" \
    "v_mfma_f32_16x16x32_bf16 v[204:207], v[40:43], v[84:87], v[204:207]\n\t" \
    "s_waitcnt lgkmcnt(4)\n\t" \
    "v_mfma_f32_16x16x32_bf16 v[208:211], v[40:43], v[92:95], v[208:211]\n\t" \
    "v_mfma_f32_16x16x32_bf16 v[196:199], v[44:47], v[88:91], v[196:199]\n\t" \
    "ds_read_b128 v[36:39], %[xp] offset:45056\n\t" \
    "v_mfma_f32_16x16x32_bf16 v[200:203], v[44:47], v[96:99], v[200:203]\n\t" \
    "ds_read_b128 v[40:43], %[xp] offset:46080\n\t" \
    "v_mfma_f32_16x16x32_bf16 v[204:207], v[48:51], v[88:91], v[204:207]\n\t" \
    "s_waitcnt lgkmcnt(4)\n\t" \
    "v_mfma_f32_16x16x32_bf16 v[208:211], v[48:51], v[96:99], v[208:211]\n\t" \
    "v_mfma_f32_16x16x32_bf16 v[196:199], v[20:23], v[100:103], v[196:199]\n\t" \
    "ds_read_b128 v[44:47], %[xp] offset:47104\n\t" \
    "v_mfma_f32_16x16x32_bf16 v[200:203], v[20:23], v[108:111], v[200:203]\n\t" \
    "ds_read_b128 v[48:51], %[xp] offset:48128\n\t" \
    "v_mfma_f32_16x16x32_bf16 v[204:207], v[24:27], v[100:103], v[204:207]\n\t" \
    "s_waitcnt lgkmcnt(4)\n\t" \
    "v_mfma_f32_16x16x32_bf16 v[208:211], v[24:27], v[108:111], v[208:211]\n\t" \
    "v_mfma_f32_16x16x32_bf16 v[196:199], v[28:31], v[104:107], v[196:199]\n\t" \
    "ds_read_b128 v[20:23], %[xp] offset:49152\n\t" \
    "v_mfma_f32_16x16x32_bf16 v[200:203], v[28:31], v[112:115], v[200:203]\n\t" \
    "ds_read_b128 v[24:27], %[xp] offset:50176\n\t" \
    "v_mfma_f32_16x16x32_bf16 v[204:207], v[32:35], v[104:107], v[204:207]\n\t" \
    "s_waitcnt lgkmcnt(4)\n\t" \
    "v_mfma_f32_16x16x32_bf16 v[208:211], v[32:35], v[112:115], v[208:211]\n\t" \
    "v_mfma_f32_16x16x32_bf16 v[196:199], v[36:39], v[116:119], v[196:199]\n\t" \
    "ds_read_b128 v[28:31], %[xp] offset:51200\n\t" \
    "v_mfma_f32_16x16x32_bf16 v[200:203], v[36:39], v[124:127], v[200:203]\n\t" \
    "ds_read_b128 v[32:35], %[xp] offset:52224\n\t" \
    "v_mfma_f32_16x16x32_bf16 v[204:207], v[40:43], v[116:119], v[204:207]\n\t" \
    "s_waitcnt lgkmcnt(4)\n\t" \
    "v_mfma_f32_16x16x32_bf16 v[208:211], v[40:43], v[124:127], v[208:211]\n\t" \
    "v_mfma_f32_16x16x32_bf16 v[196:199], v[44:47], v[120:123], v[196:199]\n\t" \
    "ds_read_b128 v[36:39], %[xp] offset:53248\n\t" \
    "v_mfma_f32_16x16x32_bf16 v[200:203], v[44:47], v[128:131], v[200:203]\n\t" \
    "ds_read_b128 v[40:43], %[xp] offset:54272\n\t" \
    "v_mfma_f32_16x16x32_bf16 v[204:207], v[48:51], v[120:123], v[204:207]\n\t" \
    "s_waitcnt lgkmcnt(4)\n\t" \
    "v_mfma_f32_16x16x32_bf16 v[208:211], v[48:51], v[128:131], v[208:211]\n\t" \
    "s_waitcnt vmcnt(4)\n\t" \
    "s_barrier\n\t" \
    "s_setprio 0\n\t" \
    "s_sub_u32 %[a], %[t], 1\n\t" \
    "v_mov_b32 v53, %[tag]\n\t" \
    "v_mov_b32 v52, %[a]\n\t" \
    "s_cmp_gt_u32 %[t], 1\n\t" \
    "s_cselect_b64 exec, %[fm], 0\n\t" \
    "buffer_store_dwordx2 v[52:53], %[l16], %[rs], %[fo] offen sc1\n\t" \
    "s_mov_b64 exec, %[pm]\n\t" \
    "s_mov_b32 m0, %[pd]\n\t" \
    "s_add_u32 %[a], %[sx], %[po]\n\t" \
    "buffer_load_dwordx4 %[l16], %[rs], %[a] offen sc1 lds\n\t" \
    "s_mov_b64 exec, -1\n\t" \
    "s_add_u32 m0, %[sd], 32768\n\t" \
    "s_add_u32 %[a], %[sx], 131072\n\t" \
    "buffer_load_dwordx4 %[l16], %[rs], %[a] offen offset:0 sc1 lds\n\t" \
    "buffer_load_dwordx4 %[l16], %[rs], %[a] offen offset:1024 sc1 lds\n\t" \
    "buffer_load_dwordx4 %[l16], %[rs], %[a] offen offset:2048 sc1 lds\n\t" \
    "buffer_load_dwordx4 %[l16], %[rs], %[a] offen offset:3072 sc1 lds\n\t" \
    "v_mfma_f32_16x16x32_bf16 v[196:199], v[20:23], v[132:135], v[196:199]\n\t" \
    "ds_read_b128 v[44:47], %[xp] offset:55296\n\t" \
    "v_mfma_f32_16x16x32_bf16 v[200:203], v[20:23], v[140:143], v[200:203]\n\t" \
    "ds_read_b128 v[48:51], %[xp] offset:56320\n\t" \
    "v_mfma_f32_16x16x32_bf16 v[204:207], v[24:27], v[132:135], v[204:207]\n\t" \
    "s_waitcnt lgkmcnt(4)\n\t" \
    "v_mfma_f32_16x16x32_bf16 v[208:211], v[24:27], v[140:143], v[208:211]\n\t" \
    "v_mfma_f32_16x16x32_bf16 v[196:199], v[28:31], v[136:139], v[196:199]\n\t" \
    "ds_read_b128 v[20:23], %[xp] offset:57344\n\t" \
    "v_mfma_f32_16x16x32_bf16 v[200:203], v[28:31], v[144:147], v[200:203]\n\t" \
    "ds_read_b128 v[24:27], %[xp] offset:58368\n\t" \
    "v_mfma_f32_16x16x32_bf16 v[204:207], v[32:35], v[136:139], v[204:207]\n\t" \
    "s_waitcnt lgkmcnt(4)\n\t" \
    "v_mfma_f32_16x16x32_bf16 v[208:211], v[32:35], v[144:147], v[208:211]\n\t" \
    "v_mfma_f32_16x16x32_bf16 v[196:199], v[36:39], v[148:151], v[196:199]\n\t" \
    "ds_read_b128 v[28:31], %[xp] offset:59392\n\t" \
    "v_mfma_f32_16x16x32_bf16 v[200:203], v[36:39], v[156:159], v[200:203]\n\t" \
    "ds_read_b128 v[32:35], %[xp] offset:60416\n\t" \
    "v_mfma_f32_16x16x32_bf16 v[204:207], v[40:43], v[148:151], v[204:207]\n\t" \
    "s_waitcnt lgkmcnt(4)\n\t" \
    "v_mfma_f32_16x16x32_bf16 v[208:211], v[40:43], v[156:159], v[208:211]\n\t" \
    "v_mfma_f32_16x16x32_bf16 v[196:199], v[44:47], v[152:155], v[196:199]\n\t" \
    "ds_read_b128 v[36:39], %[xp] offset:61440\n\t" \
    "v_mfma_f32_16x16x32_bf16 v[200:203], v[44:47], v[160:163], v[200:203]\n\t" \
    "ds_read_b128 v[40:43], %[xp] offset:62464\n\t" \
    "v_mfma_f32_16x16x32_bf16 v[204:207], v[48:51], v[152:155], v[204:207]\n\t" \
    "s_waitcnt lgkmcnt(4)\n\t" \
    "v_mfma_f32_16x16x32_bf16 v[208:211], v[48:51], v[160:163], v[208:211]\n\t" \
    "v_mfma_f32_16x16x32_bf16 v[196:199], v[20:23], v[164:167], v[196:199]\n\t" \
    "ds_read_b128 v[44:47], %[xp] offset:63488\n\t" \
    "v_mfma_f32_16x16x32_bf16 v[200:203], v[20:23], v[172:175], v[200:203]\n\t" \
    "ds_read_b128 v[48:51], %[xp] offset:64512\n\t" \
    "v_mfma_f32_16x16x32_bf16 v[204:207], v[24:27], v[164:167], v[204:207]\n\t" \
    "s_waitcnt lgkmcnt(4)\n\t" \
    "v_mfma_f32_16x16x32_bf16 v[208:211], v[24:27], v[172:175], v[208:211]\n\t" \
    "v_mfma_f32_16x16x32_bf16 v[196:199], v[28:31], v[168:171], v[196:199]\n\t" \
    "ds_read_b128 v[20:23], %[xp] offset:0\n\t" \
    "v_mfma_f32_16x16x32_bf16 v[200:203], v[28:31], v[176:179], v[200:203]\n\t" \
    "ds_read_b128 v[24:27], %[xp] offset:1024\n\t" \
    "v_mfma_f32_16x16x32_bf16 v[204:207], v[32:35], v[168:171], v[204:207]\n\t" \
    "s_waitcnt lgkmcnt(4)\n\t" \
    "v_mfma_f32_16x16x32_bf16 v[208:211], v[32:35], v[176:179], v[208:211]\n\t" \
    "v_mfma_f32_16x16x32_bf16 v[196:199], v[36:39], v[180:183], v[196:199]\n\t" \
    "ds_read_b128 v[28:31], %[xp] offset:2048\n\t" \
    "v_mfma_f32_16x16x32_bf16 v[200:203], v[36:39], v[188:191], v[200:203]\n\t" \
    "ds_read_b128 v[32:35], %[xp] offset:3072\n\t" \
    "v_mfma_f32_16x16x32_bf16 v[204:207], v[40:43], v[180:183], v[204:207]\n\t" \
    "s_waitcnt lgkmcnt(4)\n\t" \
    "v_mfma_f32_16x16x32_bf16 v[208:211], v[40:43], v[188:191], v[208:211]\n\t" \
    "v_mfma_f32_16x16x32_bf16 v[196:199], v[44:47], v[184:187], v[196:199]\n\t" \
    "ds_read_b128 v[36:39], %[xp] offset:4096\n\t" \
    "v_mfma_f32_16x16x32_bf16 v[200:203], v[44:47], v[192:195], v[200:203]\n\t" \
    "ds_read_b128 v[40:43], %[xp] offset:5120\n\t" \
    "v_mfma_f32_16x16x32_bf16 v[204:207], v[48:51], v[184:187], v[204:207]\n\t" \
    "s_waitcnt lgkmcnt(4)\n\t" \
    "v_mfma_f32_16x16x32_bf16 v[208:211], v[48:51], v[192:195], v[208:211]\n\t" \
    "s_add_u32 %[sx], %[sx], 65536\n\t" \
    "s_add_u32 %[t], %[t], 1\n\t" \
    "s_waitcnt vmcnt(4)\n\t" \
    "s_barrier\n\t" \
    "s_cmp_lt_u32 %[t], %[te]\n\t" \
    "s_cbranch_scc1 L0_%=\n\t" \
    "s_cmp_eq_u32 %[fin], %[te]\n\t" \
    "s_cbranch_scc0 E_%=\n\t" \
    "s_nop 4\n\t" \
    "ds_write_b128 %[pa], v[196:199] offset:0\n\t" \
    "ds_write_b128 %[pa], v[200:203] offset:1024\n\t" \
    "ds_write_b128 %[pa], v[204:207] offset:2048\n\t" \
    "ds_write_b128 %[pa], v[208:211] offset:3072\n\t" \
    "s_waitcnt lgkmcnt(0)\n\t" \
    "s_barrier\n\t" \
    "s_branch E_%=\n\t" \
    "L1_%=:\n\t" \
    "s_setprio 1\n\t" \
    "s_add_u32 m0, %[sd], 49152\n\t" \
    "s_add_u32 %[a], %[sx], 81920\n\t" \
    "v_mfma_f32_16x16x32_bf16 v[0:3], v[20:23], v[68:71], 0\n\t" \
    "ds_read_b128 v[44:47], %[xp] offset:6144\n\t" \
    "v_mfma_f32_16x16x32_bf16 v[4:7], v[20:23], v[76:79], 0\n\t" \
    "ds_read_b128 v[48:51], %[xp] offset:7168\n\t" \
    "v_mfma_f32_16x16x32_bf16 v[8:11], v[24:27], v[68:71], 0\n\t" \
    "s_waitcnt lgkmcnt(4)\n\t" \
    "v_mfma_f32_16x16x32_bf16 v[12:15], v[24:27], v[76:79], 0\n\t" \
    "v_mfma_f32_16x16x32_bf16 v[0:3], v[28:31], v[72:75], v[0:3]\n\t" \
    "ds_read_b128 v[20:23], %[xp] offset:8192\n\t" \
    "v_mfma_f32_16x16x32_bf16 v[4:7], v[28:31], v[80:83], v[4:7]\n\t" \
    "ds_read_b128 v[24:27], %[xp] offset:9216\n\t" \
    "v_mfma_f32_16x16x32_bf16 v[8:11], v[32:35], v[72:75], v[8:11]\n\t" \
    "s_waitcnt lgkmcnt(4)\n\t" \
    "v_mfma_f32_16x16x32_bf16 v[12:15], v[32:35], v[80:83], v[12:15]\n\t" \
    "v_mfma_f32_16x16x32_bf16 v[0:3], v[36:39], v[84:87], v[0:3]\n\t" \
    "ds_read_b128 v[28:31], %[xp] offset:10240\n\t" \
    "v_mfma_f32_16x16x32_bf16 v[4:7], v[36:39], v[92:95], v[4:7]\n\t" \
    "ds_read_b128 v[32:35], %[xp] offset:11264\n\t" \
    "v_mfma_f32_16x16x32_bf16 v[8:11], v[40:43], v[84:87], v[8:11]\n\t" \
    "s_waitcnt lgkmcnt(4)\n\t" \
    "v_mfma_f32_16x16x32_bf16 v[12:15], v[40:43], v[92:95], v[12:15]\n\t" \
    "v_mfma_f32_16x16x32_bf16 v[0:3], v[44:47], v[88:91], v[0:3]\n\t" \
    "ds_read_b128 v[36:39], %[xp] offset:12288\n\t" \
    "v_mfma_f32_16x16x32_bf16 v[4:7], v[44:47], v[96:99], v[4:7]\n\t" \
    "ds_read_b128 v[40:43], %[xp] offset:13312\n\t" \
    "v_mfma_f32_16x16x32_bf16 v[8:11], v[48:51], v[88:91], v[8:11]\n\t" \
    "s_waitcnt lgkmcnt(4)\n\t" \
    "v_mfma_f32_16x16x32_bf16 v[12:15], v[48:51], v[96:99], v[12:15]\n\t" \
    "v_mfma_f32_16x16x32_bf16 v[0:3], v[20:23], v[100:103], v[0:3]\n\t" \
    "ds_read_b128 v[44:47], %[xp] offset:14336\n\t" \
    "v_mfma_f32_16x16x32_bf16 v[4:7], v[20:23], v[108:111], v[4:7]\n\t" \
    "ds_read_b128 v[48:51], %[xp] offset:15360\n\t" \
    "v_mfma_f32_16x16x32_bf16 v[8:11], v[24:27], v[100:103], v[8:11]\n\t" \
    "s_waitcnt lgkmcnt(4)\n\t" \
    "v_mfma_f32_16x16x32_bf16 v[12:15], v[24:27], v[108:111], v[12:15]\n\t" \
    "buffer_load_dwordx4 %[l16], %[rs], %[a] offen offset:0 sc1 lds\n\t" \
    "v_mfma_f32_16x16x32_bf16 v[0:3], v[28:31], v[104:107], v[0:3]\n\t" \
    "ds_read_b128 v[20:23], %[xp] offset:16384\n\t" \
    "v_mfma_f32_16x16x32_bf16 v[4:7], v[28:31], v[112:115], v[4:7]\n\t" \
    "ds_read_b128 v[24:27], %[xp] offset:17408\n\t" \
    "v_mfma_f32_16x16x32_bf16 v[8:11], v[32:35], v[104:107], v[8:11]\n\t" \
    "s_waitcnt lgkmcnt(4)\n\t" \
    "v_mfma_f32_16x16x32_bf16 v[12:15], v[32:35], v[112:115], v[12:15]\n\t" \
    "buffer_load_dwordx4 %[l16], %[rs], %[a] offen offset:1024 sc1 lds\n\t" \
    "v_mfma_f32_16x16x32_bf16 v[0:3], v[36:39], v[116:119], v[0:3]\n\t" \
    "ds_read_b128 v[28:31], %[xp] offset:18432\n\t" \
    "v_mfma_f32_16x16x32_bf16 v[4:7], v[36:39], v[124:127], v[4:7]\n\t" \
    "ds_read_b128 v[32:35], %[xp] offset:19456\n\t" \
    "v_mfma_f32_16x16x32_bf16 v[8:11], v[40:43], v[116:119], v[8:11]\n\t" \
    "s_waitcnt lgkmcnt(4)\n\t" \
    "v_mfma_f32_16x16x32_bf16 v[12:15], v[40:43], v[124:127], v[12:15]\n\t" \
    "buffer_load_dwordx4 %[l16], %[rs], %[a] offen offset:2048 sc1 lds\n\t" \
    "v_mfma_f32_16x16x32_bf16 v[0:3], v[44:47], v[120:123], v[0:3]\n\t" \
    "ds_read_b128 v[36:39], %[xp] offset:20480\n\t" \
    "v_mfma_f32_16x16x32_bf16 v[4:7], v[44:47], v[128:131], v[4:7]\n\t" \
    "ds_read_b128 v[40:43], %[xp] offset:21504\n\t" \
    "v_mfma_f32_16x16x32_bf16 v[8:11], v[48:51], v[120:123], v[8:11]\n\t" \
    "s_waitcnt lgkmcnt(4)\n\t" \
    "v_mfma_f32_16x16x32_bf16 v[12:15], v[48:51], v[128:131], v[12:15]\n\t" \
    "buffer_load_dwordx4 %[l16], %[rs], %[a] offen offset:3072 sc1 lds\n\t" \
    "s_waitcnt vmcnt(4)\n\t" \
    "s_barrier\n\t" \
    "s_setprio 1\n\t" \
    "s_add_u32 m0, %[sd], 0\n\t" \
    "s_add_u32 %[a], %[sx], 131072\n\t" \
    "s_add_u32 %[b], %[oo], 0x1000\n\t" \
    "s_cmp_lg_u32 %[t], 1\n\t" \
    "s_cselect_b64 %[em], -1, 0\n\t" \
    "v_mfma_f32_16x16x32_bf16 v[0:3], v[20:23], v[132:135], v[0:3]\n\t" \
    "ds_read_b128 v[44:47], %[xp] offset:22528\n\t" \
    "v_mfma_f32_16x16x32_bf16 v[4:7], v[20:23], v[140:143], v[4:7]\n\t" \
    "ds_read_b128 v[48:51], %[xp] offset:23552\n\t" \
    "v_mfma_f32_16x16x32_bf16 v[8:11], v[24:27], v[132:135], v[8:11]\n\t" \
    "ds_read_b128 v[52:55], %[pa] offset:0\n\t" \
    "s_waitcnt lgkmcnt(5)\n\t" \
    "v_mfma_f32_16x16x32_bf16 v[12:15], v[24:27], v[140:143], v[12:15]\n\t" \
    "buffer_load_dwordx4 %[l16], %[rs], %[a] offen offset:0 sc1 lds\n\t" \
    "v_mfma_f32_16x16x32_bf16 v[0:3], v[28:31], v[136:139], v[0:3]\n\t" \
    "ds_read_b128 v[20:23], %[xp] offset:24576\n\t" \
    "v_mfma_f32_16x16x32_bf16 v[4:7], v[28:31], v[144:147], v[4:7]\n\t" \
    "ds_read_b128 v[24:27], %[xp] offset:25600\n\t" \
    "v_mfma_f32_16x16x32_bf16 v[8:11], v[32:35], v[136:139], v[8:11]\n\t" \
    "ds_read_b128 v[56:59], %[pa] offset:1024\n\t" \
    "s_waitcnt lgkmcnt(6)\n\t" \
    "v_mfma_f32_16x16x32_bf16 v[12:15], v[32:35], v[144:147], v[12:15]\n\t" \
    "buffer_load_dwordx4 %[l16], %[rs], %[a] offen offset:1024 sc1 lds\n\t" \
    "v_mfma_f32_16x16x32_bf16 v[0:3], v[36:39], v[148:151], v[0:3]\n\t" \
    "ds_read_b128 v[28:31], %[xp] offset:26624\n\t" \
    "v_mfma_f32_16x16x32_bf16 v[4:7], v[36:39], v[156:159], v[4:7]\n\t" \
    "ds_read_b128 v[32:35], %[xp] offset:27648\n\t" \
    "v_mfma_f32_16x16x32_bf16 v[8:11], v[40:43], v[148:151], v[8:11]\n\t" \
    "ds_read_b128 v[60:63], %[pa] offset:2048\n\t" \
    "s_waitcnt lgkmcnt(7)\n\t" \
    "v_mfma_f32_16x16x32_bf16 v[12:15], v[40:43], v[156:159], v[12:15]\n\t" \
    "buffer_load_dwordx4 %[l16], %[rs], %[a] offen offset:2048 sc1 lds\n\t" \
    "v_mfma_f32_16x16x32_bf16 v[0:3], v[44:47], v[152:155], v[0:3]\n\t" \
    "ds_read_b128 v[36:39], %[xp] offset:28672\n\t" \
    "s_waitcnt lgkmcnt(7)\n\t" \
    "v_add_f32 v196, v196, v52\n\t" \
    "v_mfma_f32_16x16x32_bf16 v[4:7], v[44:47], v[160:163], v[4:7]\n\t" \
    "ds_read_b128 v[40:43], %[xp] offset:29696\n\t" \
    "v_add_f32 v197, v197, v53\n\t" \
    "v_mfma_f32_16x16x32_bf16 v[8:11], v[48:51], v[152:155], v[8:11]\n\t" \
    "ds_read_b128 v[64:67], %[pa] offset:3072\n\t" \
    "s_waitcnt lgkmcnt(7)\n\t" \
    "v_mfma_f32_16x16x32_bf16 v[12:15], v[48:51], v[160:163], v[12:15]\n\t" \
    "buffer_load_dwordx4 %[l16], %[rs], %[a] offen offset:3072 sc1 lds\n\t" \
    "v_add_f32 v198, v198, v54\n\t" \
    "v_add_f32 v199, v199, v55\n\t" \
    "v_mfma_f32_16x16x32_bf16 v[0:3], v[20:23], v[164:167], v[0:3]\n\t" \
    "ds_read_b128 v[44:47], %[xp] offset:30720\n\t" \
    "s_waitcnt lgkmcnt(7)\n\t" \
    "v_add_f32 v200, v200, v56\n\t" \
    "v_mfma_f32_16x16x32_bf16 v[4:7], v[20:23], v[172:175], v[4:7]\n\t" \
    "ds_read_b128 v[48:51], %[xp] offset:31744\n\t" \
    "v_add_f32 v201, v201, v57\n\t" \
    "v_mfma_f32_16x16x32_bf16 v[8:11], v[24:27], v[164:167], v[8:11]\n\t" \
    "v_add_f32 v202, v202, v58\n\t" \
    "s_waitcnt lgkmcnt(6)\n\t" \
    "v_mfma_f32_16x16x32_bf16 v[12:15], v[24:27], v[172:175], v[12:15]\n\t" \
    "v_add_f32 v203, v203, v59\n\t" \
    "s_waitcnt lgkmcnt(5)\n\t" \
    "v_add_f32 v204, v204, v60\n\t" \
    "v_add_f32 v205, v205, v61\n\t" \
    "v_mfma_f32_16x16x32_bf16 v[0:3], v[28:31], v[168:171], v[0:3]\n\t" \
    "ds_read_b128 v[20:23], %[xp] offset:32768\n\t" \
    "v_add_f32 v206, v206, v62\n\t" \
    "v_mfma_f32_16x16x32_bf16 v[4:7], v[28:31], v[176:179], v[4:7]\n\t" \
    "ds_read_b128 v[24:27], %[xp] offset:33792\n\t" \
    "v_add_f32 v207, v207, v63\n\t" \
    "v_mfma_f32_16x16x32_bf16 v[8:11], v[32:35], v[168:171], v[8:11]\n\t" \
    "s_waitcnt lgkmcnt(4)\n\t" \
    "v_add_f32 v208, v208, v64\n\t" \
    "v_mfma_f32_16x16x32_bf16 v[12:15], v[32:35], v[176:179], v[12:15]\n\t" \
    "v_add_f32 v209, v209, v65\n\t" \
    "v_add_f32 v210, v210, v66\n\t" \
    "v_add_f32 v211, v211, v67\n\t" \
    "v_mfma_f32_16x16x32_bf16 v[0:3], v[36:39], v[180:183], v[0:3]\n\t" \
    "ds_read_b128 v[28:31], %[xp] offset:34816\n\t" \
    "v_cvt_pk_bf16_f32 v52, v196, v197\n\t" \
    "v_mfma_f32_16x16x32_bf16 v[4:7], v[36:39], v[188:191], v[4:7]\n\t" \
    "ds_read_b128 v[32:35], %[xp] offset:35840\n\t" \
    "v_cvt_pk_bf16_f32 v53, v198, v199\n\t" \
    "v_mfma_f32_16x16x32_bf16 v[8:11], v[40:43], v[180:183], v[8:11]\n\t" \
    "v_cvt_pk_bf16_f32 v54, v204, v205\n\t" \
    "s_waitcnt lgkmcnt(4)\n\t" \
    "v_mfma_f32_16x16x32_bf16 v[12:15], v[40:43], v[188:191], v[12:15]\n\t" \
    "v_cvt_pk_bf16_f32 v55, v206, v207\n\t" \
    "v_cvt_pk_bf16_f32 v56, v200, v201\n\t" \
    "v_cvt_pk_bf16_f32 v57, v202, v203\n\t" \
    "v_mfma_f32_16x16x32_bf16 v[0:3], v[44:47], v[184:187], v[0:3]\n\t" \
    "ds_read_b128 v[36:39], %[xp] offset:36864\n\t" \
    "v_cvt_pk_bf16_f32 v58, v208, v209\n\t" \
    "v_mfma_f32_16x16x32_bf16 v[4:7], v[44:47], v[192:195], v[4:7]\n\t" \
    "ds_read_b128 v[40:43], %[xp] offset:37888\n\t" \
    "v_cvt_pk_bf16_f32 v59, v210, v211\n\t" \
    "v_mfma_f32_16x16x32_bf16 v[8:11], v[48:51], v[184:187], v[8:11]\n\t" \
    "s_waitcnt lgkmcnt(4)\n\t" \
    "v_mfma_f32_16x16x32_bf16 v[12:15], v[48:51], v[192:195], v[12:15]\n\t" \
    "s_mov_b64 exec, %[em]\n\t" \
    "buffer_store_dwordx4 v[52:55], %[l16], %[rs], %[oo] offen sc1\n\t" \
    "buffer_store_dwordx4 v[56:59], %[l16], %[rs], %[oo] offen offset:1024 sc1\n\t" \
    "s_mov_b64 exec, -1\n\t" \
    "s_add_u32 %[sx], %[sx], 65536\n\t" \
    "s_add_u32 %[t], %[t], 1\n\t" \
    "s_add_u32 %[oo], %[oo], 262144\n\t" \
    "s_waitcnt vmcnt(6)\n\t" \
    "s_cmp_gt_u32 %[t], " STR(SQUICK) " + 1\n\t" \
    "s_cbranch_scc1 B0_%=\n\t" \
    "s_waitcnt vmcnt(0)\n\t" \
    "B0_%=:\n\t" \
    "s_barrier\n\t" \
    "s_setprio 1\n\t" \
    "s_add_u32 m0, %[sd], 16384\n\t" \
    "s_add_u32 %[a], %[sx], 81920\n\t" \
    "v_mfma_f32_16x16x32_bf16 v[196:199], v[20:23], v[68:71], 0\n\t" \
    "ds_read_b128 v[44:47], %[xp] offset:38912\n\t" \
    "v_mfma_f32_16x16x32_bf16 v[200:203], v[20:23], v[76:79], 0\n\t" \
    "ds_read_b128 v[48:51], %[xp] offset:39936\n\t" \
    "v_mfma_f32_16x16x32_bf16 v[204:207], v[24:27], v[68:71], 0\n\t" \
    "s_waitcnt lgkmcnt(4)\n\t" \
    "v_mfma_f32_16x16x32_bf16 v[208:211], v[24:27], v[76:79], 0\n\t" \
    "v_mfma_f32_16x16x32_bf16 v[196:199], v[28:31], v[72:75], v[196:199]\n\t" \
    "ds_read_b128 v[20:23], %[xp] offset:40960\n\t" \
    "v_mfma_f32_16x16x32_bf16 v[200:203], v[28:31], v[80:83], v[200:203]\n\t" \
    "ds_read_b128 v[24:27], %[xp] offset:41984\n\t" \
    "v_mfma_f32_16x16x32_bf16 v[204:207], v[32:35], v[72:75], v[204:207]\n\t" \
    "s_waitcnt lgkmcnt(4)\n\t" \
    "v_mfma_f32_16x16x32_bf16 v[208:211], v[32:35], v[80:83], v[208:211]\n\t" \
    "v_mfma_f32_16x16x32_bf16 v[196:199], v[36:39], v[84:87], v[196:199]\n\t" \
    "ds_read_b128 v[28:31], %[xp] offset:43008\n\t" \
    "v_mfma_f32_16x16x32_bf16 v[200:203], v[36:39], v[92:95], v[200:203]\n\t" \
    "ds_read_b128 v[32:35], %[xp] offset:44032\n\t" \
    "v_mfma_f32_16x16x32_bf16 v[204:207], v[40:43], v[84:87], v[204:207]\n\t" \
    "s_waitcnt lgkmcnt(4)\n\t" \
    "v_mfma_f32_16x16x32_bf16 v[208:211], v[40:43], v[92:95], v[208:211]\n\t" \
    "v_mfma_f32_16x16x32_bf16 v[196:199], v[44:47], v[88:91], v[196:199]\n\t" \
    "ds_read_b128 v[36:39], %[xp] offset:45056\n\t" \
    "v_mfma_f32_16x16x32_bf16 v[200:203], v[44:47], v[96:99], v[200:203]\n\t" \
    "ds_read_b128 v[40:43], %[xp] offset:46080\n\t" \
    "v_mfma_f32_16x16x32_bf16 v[204:207], v[48:51], v[88:91], v[204:207]\n\t" \
    "s_waitcnt lgkmcnt(4)\n\t" \
    "v_mfma_f32_16x16x32_bf16 v[208:211], v[48:51], v[96:99], v[208:211]\n\t" \
    "v_mfma_f32_16x16x32_bf16 v[196:199], v[20:23], v[100:103], v[196:199]\n\t" \
    "ds_read_b128 v[44:47], %[xp] offset:47104\n\t" \
    "v_mfma_f32_16x16x32_bf16 v[200:203], v[20:23], v[108:111], v[200:203]\n\t" \
    "ds_read_b128 v[48:51], %[xp] offset:48128\n\t" \
    "v_mfma_f32_16x16x32_bf16 v[204:207], v[24:27], v[100:103], v[204:207]\n\t" \
    "s_waitcnt lgkmcnt(4)\n\t" \
    "v_mfma_f32_16x16x32_bf16 v[208:211], v[24:27], v[108:111], v[208:211]\n\t" \
    "buffer_load_dwordx4 %[l16], %[rs], %[a] offen offset:0 sc1 lds\n\t" \
    "v_mfma_f32_16x16x32_bf16 v[196:199], v[28:31], v[104:107], v[196:199]\n\t" \
    "ds_read_b128 v[20:23], %[xp] offset:49152\n\t" \
    "v_mfma_f32_16x16x32_bf16 v[200:203], v[28:31], v[112:115], v[200:203]\n\t" \
    "ds_read_b128 v[24:27], %[xp] offset:50176\n\t" \
    "v_mfma_f32_16x16x32_bf16 v[204:207], v[32:35], v[104:107], v[204:207]\n\t" \
    "s_waitcnt lgkmcnt(4)\n\t" \
    "v_mfma_f32_16x16x32_bf16 v[208:211], v[32:35], v[112:115], v[208:211]\n\t" \
    "buffer_load_dwordx4 %[l16], %[rs], %[a] offen offset:1024 sc1 lds\n\t" \
    "v_mfma_f32_16x16x32_bf16 v[196:199], v[36:39], v[116:119], v[196:199]\n\t" \
    "ds_read_b128 v[28:31], %[xp] offset:51200\n\t" \
    "v_mfma_f32_16x16x32_bf16 v[200:203], v[36:39], v[124:127], v[200:203]\n\t" \
    "ds_read_b128 v[32:35], %[xp] offset:52224\n\t" \
    "v_mfma_f32_16x16x32_bf16 v[204:207], v[40:43], v[116:119], v[204:207]\n\t" \
    "s_waitcnt lgkmcnt(4)\n\t" \
    "v_mfma_f32_16x16x32_bf16 v[208:211], v[40:43], v[124:127], v[208:211]\n\t" \
    "buffer_load_dwordx4 %[l16], %[rs], %[a] offen offset:2048 sc1 lds\n\t" \
    "v_mfma_f32_16x16x32_bf16 v[196:199], v[44:47], v[120:123], v[196:199]\n\t" \
    "ds_read_b128 v[36:39], %[xp] offset:53248\n\t" \
    "v_mfma_f32_16x16x32_bf16 v[200:203], v[44:47], v[128:131], v[200:203]\n\t" \
    "ds_read_b128 v[40:43], %[xp] offset:54272\n\t" \
    "v_mfma_f32_16x16x32_bf16 v[204:207], v[48:51], v[120:123], v[204:207]\n\t" \
    "s_waitcnt lgkmcnt(4)\n\t" \
    "v_mfma_f32_16x16x32_bf16 v[208:211], v[48:51], v[128:131], v[208:211]\n\t" \
    "buffer_load_dwordx4 %[l16], %[rs], %[a] offen offset:3072 sc1 lds\n\t" \
    "s_waitcnt vmcnt(4)\n\t" \
    "s_barrier\n\t" \
    "s_setprio 1\n\t" \
    "s_add_u32 m0, %[sd], 32768\n\t" \
    "s_add_u32 %[a], %[sx], 131072\n\t" \
    "s_add_u32 %[b], %[oo], 0x1000\n\t" \
    "s_cmp_lg_u32 %[t], 1\n\t" \
    "s_cselect_b64 %[em], -1, 0\n\t" \
    "v_mfma_f32_16x16x32_bf16 v[196:199], v[20:23], v[132:135], v[196:199]\n\t" \
    "ds_read_b128 v[44:47], %[xp] offset:55296\n\t" \
    "v_mfma_f32_16x16x32_bf16 v[200:203], v[20:23], v[140:143], v[200:203]\n\t" \
    "ds_read_b128 v[48:51], %[xp] offset:56320\n\t" \
    "v_mfma_f32_16x16x32_bf16 v[204:207], v[24:27], v[132:135], v[204:207]\n\t" \
    "ds_read_b128 v[52:55], %[pa] offset:0\n\t" \
    "s_waitcnt lgkmcnt(5)\n\t" \
    "v_mfma_f32_16x16x32_bf16 v[208:211], v[24:27], v[140:143], v[208:211]\n\t" \
    "buffer_load_dwordx4 %[l16], %[rs], %[a] offen offset:0 sc1 lds\n\t" \
    "v_mfma_f32_16x16x32_bf16 v[196:199], v[28:31], v[136:139], v[196:199]\n\t" \
    "ds_read_b128 v[20:23], %[xp] offset:57344\n\t" \
    "v_mfma_f32_16x16x32_bf16 v[200:203], v[28:31], v[144:147], v[200:203]\n\t" \
    "ds_read_b128 v[24:27], %[xp] offset:58368\n\t" \
    "v_mfma_f32_16x16x32_bf16 v[204:207], v[32:35], v[136:139], v[204:207]\n\t" \
    "ds_read_b128 v[56:59], %[pa] offset:1024\n\t" \
    "s_waitcnt lgkmcnt(6)\n\t" \
    "v_mfma_f32_16x16x32_bf16 v[208:211], v[32:35], v[144:147], v[208:211]\n\t" \
    "buffer_load_dwordx4 %[l16], %[rs], %[a] offen offset:1024 sc1 lds\n\t" \
    "v_mfma_f32_16x16x32_bf16 v[196:199], v[36:39], v[148:151], v[196:199]\n\t" \
    "ds_read_b128 v[28:31], %[xp] offset:59392\n\t" \
    "v_mfma_f32_16x16x32_bf16 v[200:203], v[36:39], v[156:159], v[200:203]\n\t" \
    "ds_read_b128 v[32:35], %[xp] offset:60416\n\t" \
    "v_mfma_f32_16x16x32_bf16 v[204:207], v[40:43], v[148:151], v[204:207]\n\t" \
    "ds_read_b128 v[60:63], %[pa] offset:2048\n\t" \
    "s_waitcnt lgkmcnt(7)\n\t" \
    "v_mfma_f32_16x16x32_bf16 v[208:211], v[40:43], v[156:159], v[208:211]\n\t" \
    "buffer_load_dwordx4 %[l16], %[rs], %[a] offen offset:2048 sc1 lds\n\t" \
    "v_mfma_f32_16x16x32_bf16 v[196:199], v[44:47], v[152:155], v[196:199]\n\t" \
    "ds_read_b128 v[36:39], %[xp] offset:61440\n\t" \
    "s_waitcnt lgkmcnt(7)\n\t" \
    "v_add_f32 v0, v0, v52\n\t" \
    "v_mfma_f32_16x16x32_bf16 v[200:203], v[44:47], v[160:163], v[200:203]\n\t" \
    "ds_read_b128 v[40:43], %[xp] offset:62464\n\t" \
    "v_add_f32 v1, v1, v53\n\t" \
    "v_mfma_f32_16x16x32_bf16 v[204:207], v[48:51], v[152:155], v[204:207]\n\t" \
    "ds_read_b128 v[64:67], %[pa] offset:3072\n\t" \
    "s_waitcnt lgkmcnt(7)\n\t" \
    "v_mfma_f32_16x16x32_bf16 v[208:211], v[48:51], v[160:163], v[208:211]\n\t" \
    "buffer_load_dwordx4 %[l16], %[rs], %[a] offen offset:3072 sc1 lds\n\t" \
    "v_add_f32 v2, v2, v54\n\t" \
    "v_add_f32 v3, v3, v55\n\t" \
    "v_mfma_f32_16x16x32_bf16 v[196:199], v[20:23], v[164:167], v[196:199]\n\t" \
    "ds_read_b128 v[44:47], %[xp] offset:63488\n\t" \
    "s_waitcnt lgkmcnt(7)\n\t" \
    "v_add_f32 v4, v4, v56\n\t" \
    "v_mfma_f32_16x16x32_bf16 v[200:203], v[20:23], v[172:175], v[200:203]\n\t" \
    "ds_read_b128 v[48:51], %[xp] offset:64512\n\t" \
    "v_add_f32 v5, v5, v57\n\t" \
    "v_mfma_f32_16x16x32_bf16 v[204:207], v[24:27], v[164:167], v[204:207]\n\t" \
    "v_add_f32 v6, v6, v58\n\t" \
    "s_waitcnt lgkmcnt(6)\n\t" \
    "v_mfma_f32_16x16x32_bf16 v[208:211], v[24:27], v[172:175], v[208:211]\n\t" \
    "v_add_f32 v7, v7, v59\n\t" \
    "s_waitcnt lgkmcnt(5)\n\t" \
    "v_add_f32 v8, v8, v60\n\t" \
    "v_add_f32 v9, v9, v61\n\t" \
    "v_mfma_f32_16x16x32_bf16 v[196:199], v[28:31], v[168:171], v[196:199]\n\t" \
    "ds_read_b128 v[20:23], %[xp] offset:0\n\t" \
    "v_add_f32 v10, v10, v62\n\t" \
    "v_mfma_f32_16x16x32_bf16 v[200:203], v[28:31], v[176:179], v[200:203]\n\t" \
    "ds_read_b128 v[24:27], %[xp] offset:1024\n\t" \
    "v_add_f32 v11, v11, v63\n\t" \
    "v_mfma_f32_16x16x32_bf16 v[204:207], v[32:35], v[168:171], v[204:207]\n\t" \
    "s_waitcnt lgkmcnt(4)\n\t" \
    "v_add_f32 v12, v12, v64\n\t" \
    "v_mfma_f32_16x16x32_bf16 v[208:211], v[32:35], v[176:179], v[208:211]\n\t" \
    "v_add_f32 v13, v13, v65\n\t" \
    "v_add_f32 v14, v14, v66\n\t" \
    "v_add_f32 v15, v15, v67\n\t" \
    "v_mfma_f32_16x16x32_bf16 v[196:199], v[36:39], v[180:183], v[196:199]\n\t" \
    "ds_read_b128 v[28:31], %[xp] offset:2048\n\t" \
    "v_cvt_pk_bf16_f32 v52, v0, v1\n\t" \
    "v_mfma_f32_16x16x32_bf16 v[200:203], v[36:39], v[188:191], v[200:203]\n\t" \
    "ds_read_b128 v[32:35], %[xp] offset:3072\n\t" \
    "v_cvt_pk_bf16_f32 v53, v2, v3\n\t" \
    "v_mfma_f32_16x16x32_bf16 v[204:207], v[40:43], v[180:183], v[204:207]\n\t" \
    "v_cvt_pk_bf16_f32 v54, v8, v9\n\t" \
    "s_waitcnt lgkmcnt(4)\n\t" \
    "v_mfma_f32_16x16x32_bf16 v[208:211], v[40:43], v[188:191], v[208:211]\n\t" \
    "v_cvt_pk_bf16_f32 v55, v10, v11\n\t" \
    "v_cvt_pk_bf16_f32 v56, v4, v5\n\t" \
    "v_cvt_pk_bf16_f32 v57, v6, v7\n\t" \
    "v_mfma_f32_16x16x32_bf16 v[196:199], v[44:47], v[184:187], v[196:199]\n\t" \
    "ds_read_b128 v[36:39], %[xp] offset:4096\n\t" \
    "v_cvt_pk_bf16_f32 v58, v12, v13\n\t" \
    "v_mfma_f32_16x16x32_bf16 v[200:203], v[44:47], v[192:195], v[200:203]\n\t" \
    "ds_read_b128 v[40:43], %[xp] offset:5120\n\t" \
    "v_cvt_pk_bf16_f32 v59, v14, v15\n\t" \
    "v_mfma_f32_16x16x32_bf16 v[204:207], v[48:51], v[184:187], v[204:207]\n\t" \
    "s_waitcnt lgkmcnt(4)\n\t" \
    "v_mfma_f32_16x16x32_bf16 v[208:211], v[48:51], v[192:195], v[208:211]\n\t" \
    "s_mov_b64 exec, %[em]\n\t" \
    "buffer_store_dwordx4 v[52:55], %[l16], %[rs], %[oo] offen sc1\n\t" \
    "buffer_store_dwordx4 v[56:59], %[l16], %[rs], %[oo] offen offset:1024 sc1\n\t" \
    "s_mov_b64 exec, -1\n\t" \
    "s_add_u32 %[sx], %[sx], 65536\n\t" \
    "s_add_u32 %[t], %[t], 1\n\t" \
    "s_add_u32 %[oo], %[oo], 262144\n\t" \
    "s_waitcnt vmcnt(6)\n\t" \
    "s_cmp_gt_u32 %[t], " STR(SQUICK) " + 1\n\t" \
    "s_cbranch_scc1 B1_%=\n\t" \
    "s_waitcnt vmcnt(0)\n\t" \
    "B1_%=:\n\t" \
    "s_barrier\n\t" \
    "s_cmp_lt_u32 %[t], %[te]\n\t" \
    "s_cbranch_scc1 L1_%=\n\t" \
    "s_cmp_eq_u32 %[fin], %[te]\n\t" \
    "s_cbranch_scc0 E_%=\n\t" \
    "s_nop 4\n\t" \
    "s_barrier\n\t" \
    "ds_read_b128 v[52:55], %[pa] offset:0\n\t" \
    "ds_read_b128 v[56:59], %[pa] offset:1024\n\t" \
    "ds_read_b128 v[60:63], %[pa] offset:2048\n\t" \
    "ds_read_b128 v[64:67], %[pa] offset:3072\n\t" \
    "s_add_u32 %[b], %[oo], 0x1000\n\t" \
    "s_waitcnt lgkmcnt(0)\n\t" \
    "v_add_f32 v196, v196, v52\n\t" \
    "v_add_f32 v197, v197, v53\n\t" \
    "v_add_f32 v198, v198, v54\n\t" \
    "v_add_f32 v199, v199, v55\n\t" \
    "v_add_f32 v200, v200, v56\n\t" \
    "v_add_f32 v201, v201, v57\n\t" \
    "v_add_f32 v202, v202, v58\n\t" \
    "v_add_f32 v203, v203, v59\n\t" \
    "v_add_f32 v204, v204, v60\n\t" \
    "v_add_f32 v205, v205, v61\n\t" \
    "v_add_f32 v206, v206, v62\n\t" \
    "v_add_f32 v207, v207, v63\n\t" \
    "v_add_f32 v208, v208, v64\n\t" \
    "v_add_f32 v209, v209, v65\n\t" \
    "v_add_f32 v210, v210, v66\n\t" \
    "v_add_f32 v211, v211, v67\n\t" \
    "v_cvt_pk_bf16_f32 v52, v196, v197\n\t" \
    "v_cvt_pk_bf16_f32 v53, v198, v199\n\t" \
    "v_cvt_pk_bf16_f32 v54, v204, v205\n\t" \
    "v_cvt_pk_bf16_f32 v55, v206, v207\n\t" \
    "v_cvt_pk_bf16_f32 v56, v200, v201\n\t" \
    "v_cvt_pk_bf16_f32 v57, v202, v203\n\t" \
    "v_cvt_pk_bf16_f32 v58, v208, v209\n\t" \
    "v_cvt_pk_bf16_f32 v59, v210, v211\n\t" \
    "buffer_store_dwordx4 v[52:55], %[l16], %[rs], %[oo] offen sc1\n\t" \
    "buffer_store_dwordx4 v[56:59], %[l16], %[rs], %[oo] offen offset:1024 sc1\n\t" \
    "E_%=:\n\t" \
    "s_setprio 0\n\t" \
    "s_waitcnt lgkmcnt(0)\n\t" \
    "s_nop 7\n\t" \
    : [t] "+s"(t), [sx] "+s"(sx), [oo] "+s"(oo), [a] "=&s"(tmp), [b] "=&s"(tmp2), [em] "=&s"(em), "+{v[20:27]}"(xf[0]), "+{v[28:35]}"(xf[1]), "+{v[36:43]}"(xf[2]), "+{v[196:203]}"(ac[0]), "+{v[204:211]}"(ac[1]) \
    : [te] "s"(TE_), [fin] "s"(FIN_), [kh] "s"(kh), [sd] "s"(sd), [rs] "s"(rs), [l16] "v"(l16), [xp] "v"(xp), [pa] "v"(pa), [fm] "s"(fm), [pm] "s"(pm), [fo] "s"(fo), [po] "s"(po), [pd] "s"(pd), [tag] "s"(P.tag), "{v[68:75]}"(w[0][0]), "{v[76:83]}"(w[0][1]), "{v[84:91]}"(w[1][0]), "{v[92:99]}"(w[1][1]), "{v[100:107]}"(w[2][0]), "{v[108:115]}"(w[2][1]), "{v[116:123]}"(w[3][0]), "{v[124:131]}"(w[3][1]), "{v[132:139]}"(w[4][0]), "{v[140:147]}"(w[4][1]), "{v[148:155]}"(w[5][0]), "{v[156:163]}"(w[5][1]), "{v[164:171]}"(w[6][0]), "{v[172:179]}"(w[6][1]), "{v[180:187]}"(w[7][0]), "{v[188:195]}"(w[7][1]) \
    : "memory", "scc", "v0", "v1", "v2", "v3", "v4", "v5", "v6", "v7", "v8", "v9", "v10", "v11", "v12", "v13", "v14", "v15", "v44", "v45", "v46", "v47", "v48", "v49", "v50", "v51", "v52", "v53", "v54", "v55", "v56", "v57", "v58", "v59", "v60", "v61", "v62", "v63", "v64", "v65", "v66", "v67")

#define S_LOOP_X(TE_, FIN_) \
  asm volatile( \
    "s_cmp_eq_u32 %[kh], 0\n\t" \
    "s_cbranch_scc0 L1_%=\n\t" \
    "L0_%=:\n\t" \
    "s_setprio 0\n\t" \
    "s_nop 3\n\t" \
    "ds_write_b128 %[pa], v[196:199] offset:0\n\t" \
    "ds_write_b128 %[pa], v[200:203] offset:1024\n\t" \
    "ds_write_b128 %[pa], v[204:207] offset:2048\n\t" \
    "ds_write_b128 %[pa], v[208:211] offset:3072\n\t" \
    "s_sub_u32 %[a], %[t], 1\n\t" \
    "v_mov_b32 v53, %[tag]\n\t" \
    "v_mov_b32 v52, %[a]\n\t" \
    "s_sub_u32 %[b], %[t], 2\n\t" \
    "s_cmp_lt_u32 %[b], " STR(SQUICK) "\n\t" \
    "s_cselect_b64 exec, %[fm], 0\n\t" \
    "buffer_store_dwordx2 v[52:53], %[l16], %[rs], %[fo] offen sc1\n\t" \
    "s_mov_b64 exec, -1\n\t" \
    "s_add_u32 m0, %[sd], 49152\n\t" \
    "s_add_u32 %[a], %[sx], 278528\n\t" \
    "s_waitcnt lgkmcnt(0)\n\t" \
    "buffer_load_dwordx4 %[l16], %[rs], %[a] offen offset:0 sc1 lds\n\t" \
    "buffer_load_dwordx4 %[l16], %[rs], %[a] offen offset:1024 sc1 lds\n\t" \
    "buffer_load_dwordx4 %[l16], %[rs], %[a] offen offset:2048 sc1 lds\n\t" \
    "buffer_load_dwordx4 %[l16], %[rs], %[a] offen offset:3072 sc1 lds\n\t" \
    "v_mfma_f32_16x16x32_bf16 v[0:3], v[68:71], v[20:23], 0\n\t" \
    "ds_read_b128 v[44:47], %[xp] offset:6144\n\t" \
    "v_mfma_f32_16x16x32_bf16 v[4:7], v[76:79], v[20:23], 0\n\t" \
    "ds_read_b128 v[48:51], %[xp] offset:7168\n\t" \
    "v_mfma_f32_16x16x32_bf16 v[8:11], v[68:71], v[24:27], 0\n\t" \
    "v_mfma_f32_16x16x32_bf16 v[12:15], v[76:79], v[24:27], 0\n\t" \
    "v_mfma_f32_16x16x32_bf16 v[0:3], v[72:75], v[28:31], v[0:3]\n\t" \
    "ds_read_b128 v[20:23], %[xp] offset:8192\n\t" \
    "v_mfma_f32_16x16x32_bf16 v[4:7], v[80:83], v[28:31], v[4:7]\n\t" \
    "ds_read_b128 v[24:27], %[xp] offset:9216\n\t" \
    "v_mfma_f32_16x16x32_bf16 v[8:11], v[72:75], v[32:35], v[8:11]\n\t" \
    "v_mfma_f32_16x16x32_bf16 v[12:15], v[80:83], v[32:35], v[12:15]\n\t" \
    "v_mfma_f32_16x16x32_bf16 v[0:3], v[84:87], v[36:39], v[0:3]\n\t" \
    "ds_read_b128 v[28:31], %[xp] offset:10240\n\t" \
    "v_mfma_f32_16x16x32_bf16 v[4:7], v[92:95], v[36:39], v[4:7]\n\t" \
    "ds_read_b128 v[32:35], %[xp] offset:11264\n\t" \
    "v_mfma_f32_16x16x32_bf16 v[8:11], v[84:87], v[40:43], v[8:11]\n\t" \
    "s_waitcnt lgkmcnt(4)\n\t" \
    "v_mfma_f32_16x16x32_bf16 v[12:15], v[92:95], v[40:43], v[12:15]\n\t" \
    "v_mfma_f32_16x16x32_bf16 v[0:3], v[88:91], v[44:47], v[0:3]\n\t" \
    "ds_read_b128 v[36:39], %[xp] offset:12288\n\t" \
    "v_mfma_f32_16x16x32_bf16 v[4:7], v[96:99], v[44:47], v[4:7]\n\t" \
    "ds_read_b128 v[40:43], %[xp] offset:13312\n\t" \
    "v_mfma_f32_16x16x32_bf16 v[8:11], v[88:91], v[48:51], v[8:11]\n\t" \
    "s_waitcnt lgkmcnt(4)\n\t" \
    "v_mfma_f32_16x16x32_bf16 v[12:15], v[96:99], v[48:51], v[12:15]\n\t" \
    "v_mfma_f32_16x16x32_bf16 v[0:3], v[100:103], v[20:23], v[0:3]\n\t" \
    "ds_read_b128 v[44:47], %[xp] offset:14336\n\t" \
    "v_mfma_f32_16x16x32_bf16 v[4:7], v[108:111], v[20:23], v[4:7]\n\t" \
    "ds_read_b128 v[48:51], %[xp] offset:15360\n\t" \
    "v_mfma_f32_16x16x32_bf16 v[8:11], v[100:103], v[24:27], v[8:11]\n\t" \
    "s_waitcnt lgkmcnt(4)\n\t" \
    "v_mfma_f32_16x16x32_bf16 v[12:15], v[108:111], v[24:27], v[12:15]\n\t" \
    "v_mfma_f32_16x16x32_bf16 v[0:3], v[104:107], v[28:31], v[0:3]\n\t" \
    "ds_read_b128 v[20:23], %[xp] offset:16384\n\t" \
    "v_mfma_f32_16x16x32_bf16 v[4:7], v[112:115], v[28:31], v[4:7]\n\t" \
    "ds_read_b128 v[24:27], %[xp] offset:17408\n\t" \
    "v_mfma_f32_16x16x32_bf16 v[8:11], v[104:107], v[32:35], v[8:11]\n\t" \
    "s_waitcnt lgkmcnt(4)\n\t" \
    "v_mfma_f32_16x16x32_bf16 v[12:15], v[112:115], v[32:35], v[12:15]\n\t" \
    "v_mfma_f32_16x16x32_bf16 v[0:3], v[116:119], v[36:39], v[0:3]\n\t" \
    "ds_read_b128 v[28:31], %[xp] offset:18432\n\t" \
    "v_mfma_f32_16x16x32_bf16 v[4:7], v[124:127], v[36:39], v[4:7]\n\t" \
    "ds_read_b128 v[32:35], %[xp] offset:19456\n\t" \
    "v_mfma_f32_16x16x32_bf16 v[8:11], v[116:119], v[40:43], v[8:11]\n\t" \
    "s_waitcnt lgkmcnt(4)\n\t" \
    "v_mfma_f32_16x16x32_bf16 v[12:15], v[124:127], v[40:43], v[12:15]\n\t" \
    "v_mfma_f32_16x16x32_bf16 v[0:3], v[120:123], v[44:47], v[0:3]\n\t" \
    "ds_read_b128 v[36:39], %[xp] offset:20480\n\t" \
    "v_mfma_f32_16x16x32_bf16 v[4:7], v[128:131], v[44:47], v[4:7]\n\t" \
    "ds_read_b128 v[40:43], %[xp] offset:21504\n\t" \
    "v_mfma_f32_16x16x32_bf16 v[8:11], v[120:123], v[48:51], v[8:11]\n\t" \
    "s_waitcnt lgkmcnt(4)\n\t" \
    "v_mfma_f32_16x16x32_bf16 v[12:15], v[128:131], v[48:51], v[12:15]\n\t" \
    "s_waitcnt vmcnt(4)\n\t" \
    "s_barrier\n\t" \
    "s_setprio 0\n\t" \
    "s_sub_u32 %[a], %[t], 1\n\t" \
    "v_mov_b32 v53, %[tag]\n\t" \
    "v_mov_b32 v52, %[a]\n\t" \
    "s_cmp_gt_u32 %[t], 1\n\t" \
    "s_cselect_b64 exec, %[fm], 0\n\t" \
    "buffer_store_dwordx2 v[52:53], %[l16], %[rs], %[fo] offen sc1\n\t" \
    "s_mov_b64 exec, %[pm]\n\t" \
    "s_mov_b32 m0, %[pd]\n\t" \
    "s_add_u32 %[a], %[sx], %[po]\n\t" \
    "buffer_load_dwordx4 %[l16], %[rs], %[a] offen sc1 lds\n\t" \
    "s_mov_b64 exec, -1\n\t" \
    "s_add_u32 m0, %[sd], 0\n\t" \
    "s_add_u32 %[a], %[sx], 524288\n\t" \
    "buffer_load_dwordx4 %[l16], %[rs], %[a] offen offset:0 sc1 lds\n\t" \
    "buffer_load_dwordx4 %[l16], %[rs], %[a] offen offset:1024 sc1 lds\n\t" \
    "buffer_load_dwordx4 %[l16], %[rs], %[a] offen offset:2048 sc1 lds\n\t" \
    "buffer_load_dwordx4 %[l16], %[rs], %[a] offen offset:3072 sc1 lds\n\t" \
    "v_mfma_f32_16x16x32_bf16 v[0:3], v[132:135], v[20:23], v[0:3]\n\t" \
    "ds_read_b128 v[44:47], %[xp] offset:22528\n\t" \
    "v_mfma_f32_16x16x32_bf16 v[4:7], v[140:143], v[20:23], v[4:7]\n\t" \
    "ds_read_b128 v[48:51], %[xp] offset:23552\n\t" \
    "v_mfma_f32_16x16x32_bf16 v[8:11], v[132:135], v[24:27], v[8:11]\n\t" \
    "s_waitcnt lgkmcnt(4)\n\t" \
    "v_mfma_f32_16x16x32_bf16 v[12:15], v[140:143], v[24:27], v[12:15]\n\t" \
    "v_mfma_f32_16x16x32_bf16 v[0:3], v[136:139], v[28:31], v[0:3]\n\t" \
    "ds_read_b128 v[20:23], %[xp] offset:24576\n\t" \
    "v_mfma_f32_16x16x32_bf16 v[4:7], v[144:147], v[28:31], v[4:7]\n\t" \
    "ds_read_b128 v[24:27], %[xp] offset:25600\n\t" \
    "v_mfma_f32_16x16x32_bf16 v[8:11], v[136:139], v[32:35], v[8:11]\n\t" \
    "s_waitcnt lgkmcnt(4)\n\t" \
    "v_mfma_f32_16x16x32_bf16 v[12:15], v[144:147], v[32:35], v[12:15]\n\t" \
    "v_mfma_f32_16x16x32_bf16 v[0:3], v[148:151], v[36:39], v[0:3]\n\t" \
    "ds_read_b128 v[28:31], %[xp] offset:26624\n\t" \
    "v_mfma_f32_16x16x32_bf16 v[4:7], v[156:159], v[36:39], v[4:7]\n\t" \
    "ds_read_b128 v[32:35], %[xp] offset:27648\n\t" \
    "v_mfma_f32_16x16x32_bf16 v[8:11], v[148:151], v[40:43], v[8:11]\n\t" \
    "s_waitcnt lgkmcnt(4)\n\t" \
    "v_mfma_f32_16x16x32_bf16 v[12:15], v[156:159], v[40:43], v[12:15]\n\t" \
    "v_mfma_f32_16x16x32_bf16 v[0:3], v[152:155], v[44:47], v[0:3]\n\t" \
    "ds_read_b128 v[36:39], %[xp] offset:28672\n\t" \
    "v_mfma_f32_16x16x32_bf16 v[4:7], v[160:163], v[44:47], v[4:7]\n\t" \
    "ds_read_b128 v[40:43], %[xp] offset:29696\n\t" \
    "v_mfma_f32_16x16x32_bf16 v[8:11], v[152:155], v[48:51], v[8:11]\n\t" \
    "s_waitcnt lgkmcnt(4)\n\t" \
    "v_mfma_f32_16x16x32_bf16 v[12:15], v[160:163], v[48:51], v[12:15]\n\t" \
    "v_mfma_f32_16x16x32_bf16 v[0:3], v[164:167], v[20:23], v[0:3]\n\t" \
    "ds_read_b128 v[44:47], %[xp] offset:30720\n\t" \
    "v_mfma_f32_16x16x32_bf16 v[4:7], v[172:175], v[20:23], v[4:7]\n\t" \
    "ds_read_b128 v[48:51], %[xp] offset:31744\n\t" \
    "v_mfma_f32_16x16x32_bf16 v[8:11], v[164:167], v[24:27], v[8:11]\n\t" \
    "s_waitcnt lgkmcnt(4)\n\t" \
    "v_mfma_f32_16x16x32_bf16 v[12:15], v[172:175], v[24:27], v[12:15]\n\t" \
    "v_mfma_f32_16x16x32_bf16 v[0:3], v[168:171], v[28:31], v[0:3]\n\t" \
    "ds_read_b128 v[20:23], %[xp] offset:32768\n\t" \
    "v_mfma_f32_16x16x32_bf16 v[4:7], v[176:179], v[28:31], v[4:7]\n\t" \
    "ds_read_b128 v[24:27], %[xp] offset:33792\n\t" \
    "v_mfma_f32_16x16x32_bf16 v[8:11], v[168:171], v[32:35], v[8:11]\n\t" \
    "s_waitcnt lgkmcnt(4)\n\t" \
    "v_mfma_f32_16x16x32_bf16 v[12:15], v[176:179], v[32:35], v[12:15]\n\t" \
    "v_mfma_f32_16x16x32_bf16 v[0:3], v[180:183], v[36:39], v[0:3]\n\t" \
    "ds_read_b128 v[28:31], %[xp] offset:34816\n\t" \
    "v_mfma_f32_16x16x32_bf16 v[4:7], v[188:191], v[36:39], v[4:7]\n\t" \
    "ds_read_b128 v[32:35], %[xp] offset:35840\n\t" \
    "v_mfma_f32_16x16x32_bf16 v[8:11], v[180:183], v[40:43], v[8:11]\n\t" \
    "s_waitcnt lgkmcnt(4)\n\t" \
    "v_mfma_f32_16x16x32_bf16 v[12:15], v[188:191], v[40:43], v[12:15]\n\t" \
    "v_mfma_f32_16x16x32_bf16 v[0:3], v[184:187], v[44:47], v[0:3]\n\t" \
    "ds_read_b128 v[36:39], %[xp] offset:36864\n\t" \
    "v_mfma_f32_16x16x32_bf16 v[4:7], v[192:195], v[44:47], v[4:7]\n\t" \
    "ds_read_b128 v[40:43], %[xp] offset:37888\n\t" \
    "v_mfma_f32_16x16x32_bf16 v[8:11], v[184:187], v[48:51], v[8:11]\n\t" \
    "s_waitcnt lgkmcnt(4)\n\t" \
    "v_mfma_f32_16x16x32_bf16 v[12:15], v[192:195], v[48:51], v[12:15]\n\t" \
    "s_add_u32 %[sx], %[sx], 262144\n\t" \
    "s_add_u32 %[t], %[t], 1\n\t" \
    "s_waitcnt vmcnt(4)\n\t" \
    "s_barrier\n\t" \
    "s_setprio 0\n\t" \
    "s_nop 3\n\t" \
    "ds_write_b128 %[pa], v[0:3] offset:0\n\t" \
    "ds_write_b128 %[pa], v[4:7] offset:1024\n\t" \
    "ds_write_b128 %[pa], v[8:11] offset:2048\n\t" \
    "ds_write_b128 %[pa], v[12:15] offset:3072\n\t" \
    "s_sub_u32 %[a], %[t], 1\n\t" \
    "v_mov_b32 v53, %[tag]\n\t" \
    "v_mov_b32 v52, %[a]\n\t" \
    "s_sub_u32 %[b], %[t], 2\n\t" \
    "s_cmp_lt_u32 %[b], " STR(SQUICK) "\n\t" \
    "s_cselect_b64 exec, %[fm], 0\n\t" \
    "buffer_store_dwordx2 v[52:53], %[l16], %[rs], %[fo] offen sc1\n\t" \
    "s_mov_b64 exec, -1\n\t" \
    "s_add_u32 m0, %[sd], 16384\n\t" \
    "s_add_u32 %[a], %[sx], 278528\n\t" \
    "s_waitcnt lgkmcnt(0)\n\t" \
    "buffer_load_dwordx4 %[l16], %[rs], %[a] offen offset:0 sc1 lds\n\t" \
    "buffer_load_dwordx4 %[l16], %[rs], %[a] offen offset:1024 sc1 lds\n\t" \
    "buffer_load_dwordx4 %[l16], %[rs], %[a] offen offset:2048 sc1 lds\n\t" \
    "buffer_load_dwordx4 %[l16], %[rs], %[a] offen offset:3072 sc1 lds\n\t" \
    "v_mfma_f32_16x16x32_bf16 v[196:199], v[68:71], v[20:23], 0\n\t" \
    "ds_read_b128 v[44:47], %[xp] offset:38912\n\t" \
    "v_mfma_f32_16x16x32_bf16 v[200:203], v[76:79], v[20:23], 0\n\t" \
    "ds_read_b128 v[48:51], %[xp] offset:39936\n\t" \
    "v_mfma_f32_16x16x32_bf16 v[204:207], v[68:71], v[24:27], 0\n\t" \
    "v_mfma_f32_16x16x32_bf16 v[208:211], v[76:79], v[24:27], 0\n\t" \
    "v_mfma_f32_16x16x32_bf16 v[196:199], v[72:75], v[28:31], v[196:199]\n\t" \
    "ds_read_b128 v[20:23], %[xp] offset:40960\n\t" \
    "v_mfma_f32_16x16x32_bf16 v[200:203], v[80:83], v[28:31], v[200:203]\n\t" \
    "ds_read_b128 v[24:27], %[xp] offset:41984\n\t" \
    "v_mfma_f32_16x16x32_bf16 v[204:207], v[72:75], v[32:35], v[204:207]\n\t" \
    "v_mfma_f32_16x16x32_bf16 v[208:211], v[80:83], v[32:35], v[208:211]\n\t" \
    "v_mfma_f32_16x16x32_bf16 v[196:199], v[84:87], v[36:39], v[196:199]\n\t" \
    "ds_read_b128 v[28:31], %[xp] offset:43008\n\t" \
    "v_mfma_f32_16x16x32_bf16 v[200:203], v[92:95], v[36:39], v[200:203]\n\t" \
    "ds_read_b128 v[32:35], %[xp] offset:44032\n\t" \
    "v_mfma_f32_16x16x32_bf16 v[204:207], v[84:87], v[40:43], v[204:207]\n\t" \
    "s_waitcnt lgkmcnt(4)\n\t" \
    "v_mfma_f32_16x16x32_bf16 v[208:211], v[92:95], v[40:43], v[208:211]\n\t" \
    "v_mfma_f32_16x16x32_bf16 v[196:199], v[88:91], v[44:47], v[196:199]\n\t" \
    "ds_read_b128 v[36:39], %[xp] offset:45056\n\t" \
    "v_mfma_f32_16x16x32_bf16 v[200:203], v[96:99], v[44:47], v[200:203]\n\t" \
    "ds_read_b128 v[40:43], %[xp] offset:46080\n\t" \
    "v_mfma_f32_16x16x32_bf16 v[204:207], v[88:91], v[48:51], v[204:207]\n\t" \
    "s_waitcnt lgkmcnt(4)\n\t" \
    "v_mfma_f32_16x16x32_bf16 v[208:211], v[96:99], v[48:51], v[208:211]\n\t" \
    "v_mfma_f32_16x16x32_bf16 v[196:199], v[100:103], v[20:23], v[196:199]\n\t" \
    "ds_read_b128 v[44:47], %[xp] offset:47104\n\t" \
    "v_mfma_f32_16x16x32_bf16 v[200:203], v[108:111], v[20:23], v[200:203]\n\t" \
    "ds_read_b128 v[48:51], %[xp] offset:48128\n\t" \
    "v_mfma_f32_16x16x32_bf16 v[204:207], v[100:103], v[24:27], v[204:207]\n\t" \
    "s_waitcnt lgkmcnt(4)\n\t" \
    "v_mfma_f32_16x16x32_bf16 v[208:211], v[108:111], v[24:27], v[208:211]\n\t" \
    "v_mfma_f32_16x16x32_bf16 v[196:199], v[104:107], v[28:31], v[196:199]\n\t" \
    "ds_read_b128 v[20:23], %[xp] offset:49152\n\t" \
    "v_mfma_f32_16x16x32_bf16 v[200:203], v[112:115], v[28:31], v[200:203]\n\t" \
    "ds_read_b128 v[24:27], %[xp] offset:50176\n\t" \
    "v_mfma_f32_16x16x32_bf16 v[204:207], v[104:107], v[32:35], v[204:207]\n\t" \
    "s_waitcnt lgkmcnt(4)\n\t" \
    "v_mfma_f32_16x16x32_bf16 v[208:211], v[112:115], v[32:35], v[208:211]\n\t" \
    "v_mfma_f32_16x16x32_bf16 v[196:199], v[116:119], v[36:39], v[196:199]\n\t" \
    "ds_read_b128 v[28:31], %[xp] offset:51200\n\t" \
    "v_mfma_f32_16x16x32_bf16 v[200:203], v[124:127], v[36:39], v[200:203]\n\t" \
    "ds_read_b128 v[32:35], %[xp] offset:52224\n\t" \
    "v_mfma_f32_16x16x32_bf16 v[204:207], v[116:119], v[40:43], v[204:207]\n\t" \
    "s_waitcnt lgkmcnt(4)\n\t" \
    "v_mfma_f32_16x16x32_bf16 v[208:211], v[124:127], v[40:43], v[208:211]\n\t" \
    "v_mfma_f32_16x16x32_bf16 v[196:199], v[120:123], v[44:47], v[196:199]\n\t" \
    "ds_read_b128 v[36:39], %[xp] offset:53248\n\t" \
    "v_mfma_f32_16x16x32_bf16 v[200:203], v[128:131], v[44:47], v[200:203]\n\t" \
    "ds_read_b128 v[40:43], %[xp] offset:54272\n\t" \
    "v_mfma_f32_16x16x32_bf16 v[204:207], v[120:123], v[48:51], v[204:207]\n\t" \
    "s_waitcnt lgkmcnt(4)\n\t" \
    "v_mfma_f32_16x16x32_bf16 v[208:211], v[128:131], v[48:51], v[208:211]\n\t" \
    "s_waitcnt vmcnt(4)\n\t" \
    "s_barrier\n\t" \
    "s_setprio 0\n\t" \
    "s_sub_u32 %[a], %[t], 1\n\t" \
    "v_mov_b32 v53, %[tag]\n\t" \
    "v_mov_b32 v52, %[a]\n\t" \
    "s_cmp_gt_u32 %[t], 1\n\t" \
    "s_cselect_b64 exec, %[fm], 0\n\t" \
    "buffer_store_dwordx2 v[52:53], %[l16], %[rs], %[fo] offen sc1\n\t" \
    "s_mov_b64 exec, %[pm]\n\t" \
    "s_mov_b32 m0, %[pd]\n\t" \
    "s_add_u32 %[a], %[sx], %[po]\n\t" \
    "buffer_load_dwordx4 %[l16], %[rs], %[a] offen sc1 lds\n\t" \
    "s_mov_b64 exec, -1\n\t" \
    "s_add_u32 m0, %[sd], 32768\n\t" \
    "s_add_u32 %[a], %[sx], 524288\n\t" \
    "buffer_load_dwordx4 %[l16], %[rs], %[a] offen offset:0 sc1 lds\n\t" \
    "buffer_load_dwordx4 %[l16], %[rs], %[a] offen offset:1024 sc1 lds\n\t" \
    "buffer_load_dwordx4 %[l16], %[rs], %[a] offen offset:2048 sc1 lds\n\t" \
    "buffer_load_dwordx4 %[l16], %[rs], %[a] offen offset:3072 sc1 lds\n\t" \
    "v_mfma_f32_16x16x32_bf16 v[196:199], v[132:135], v[20:23], v[196:199]\n\t" \
    "ds_read_b128 v[44:47], %[xp] offset:55296\n\t" \
    "v_mfma_f32_16x16x32_bf16 v[200:203], v[140:143], v[20:23], v[200:203]\n\t" \
    "ds_read_b128 v[48:51], %[xp] offset:56320\n\t" \
    "v_mfma_f32_16x16x32_bf16 v[204:207], v[132:135], v[24:27], v[204:207]\n\t" \
    "s_waitcnt lgkmcnt(4)\n\t" \
    "v_mfma_f32_16x16x32_bf16 v[208:211], v[140:143], v[24:27], v[208:211]\n\t" \
    "v_mfma_f32_16x16x32_bf16 v[196:199], v[136:139], v[28:31], v[196:199]\n\t" \
    "ds_read_b128 v[20:23], %[xp] offset:57344\n\t" \
    "v_mfma_f32_16x16x32_bf16 v[200:203], v[144:147], v[28:31], v[200:203]\n\t" \
    "ds_read_b128 v[24:27], %[xp] offset:58368\n\t" \
    "v_mfma_f32_16x16x32_bf16 v[204:207], v[136:139], v[32:35], v[204:207]\n\t" \
    "s_waitcnt lgkmcnt(4)\n\t" \
    "v_mfma_f32_16x16x32_bf16 v[208:211], v[144:147], v[32:35], v[208:211]\n\t" \
    "v_mfma_f32_16x16x32_bf16 v[196:199], v[148:151], v[36:39], v[196:199]\n\t" \
    "ds_read_b128 v[28:31], %[xp] offset:59392\n\t" \
    "v_mfma_f32_16x16x32_bf16 v[200:203], v[156:159], v[36:39], v[200:203]\n\t" \
    "ds_read_b128 v[32:35], %[xp] offset:60416\n\t" \
    "v_mfma_f32_16x16x32_bf16 v[204:207], v[148:151], v[40:43], v[204:207]\n\t" \
    "s_waitcnt lgkmcnt(4)\n\t" \
    "v_mfma_f32_16x16x32_bf16 v[208:211], v[156:159], v[40:43], v[208:211]\n\t" \
    "v_mfma_f32_16x16x32_bf16 v[196:199], v[152:155], v[44:47], v[196:199]\n\t" \
    "ds_read_b128 v[36:39], %[xp] offset:61440\n\t" \
    "v_mfma_f32_16x16x32_bf16 v[200:203], v[160:163], v[44:47], v[200:203]\n\t" \
    "ds_read_b128 v[40:43], %[xp] offset:62464\n\t" \
    "v_mfma_f32_16x16x32_bf16 v[204:207], v[152:155], v[48:51], v[204:207]\n\t" \
    "s_waitcnt lgkmcnt(4)\n\t" \
    "v_mfma_f32_16x16x32_bf16 v[208:211], v[160:163], v[48:51], v[208:211]\n\t" \
    "v_mfma_f32_16x16x32_bf16 v[196:199], v[164:167], v[20:23], v[196:199]\n\t" \
    "ds_read_b128 v[44:47], %[xp] offset:63488\n\t" \
    "v_mfma_f32_16x16x32_bf16 v[200:203], v[172:175], v[20:23], v[200:203]\n\t" \
    "ds_read_b128 v[48:51], %[xp] offset:64512\n\t" \
    "v_mfma_f32_16x16x32_bf16 v[204:207], v[164:167], v[24:27], v[204:207]\n\t" \
    "s_waitcnt lgkmcnt(4)\n\t" \
    "v_mfma_f32_16x16x32_bf16 v[208:211], v[172:175], v[24:27], v[208:211]\n\t" \
    "v_mfma_f32_16x16x32_bf16 v[196:199], v[168:171], v[28:31], v[196:199]\n\t" \
    "ds_read_b128 v[20:23], %[xp] offset:0\n\t" \
    "v_mfma_f32_16x16x32_bf16 v[200:203], v[176:179], v[28:31], v[200:203]\n\t" \
    "ds_read_b128 v[24:27], %[xp] offset:1024\n\t" \
    "v_mfma_f32_16x16x32_bf16 v[204:207], v[168:171], v[32:35], v[204:207]\n\t" \
    "s_waitcnt lgkmcnt(4)\n\t" \
    "v_mfma_f32_16x16x32_bf16 v[208:211], v[176:179], v[32:35], v[208:211]\n\t" \
    "v_mfma_f32_16x16x32_bf16 v[196:199], v[180:183], v[36:39], v[196:199]\n\t" \
    "ds_read_b128 v[28:31], %[xp] offset:2048\n\t" \
    "v_mfma_f32_16x16x32_bf16 v[200:203], v[188:191], v[36:39], v[200:203]\n\t" \
    "ds_read_b128 v[32:35], %[xp] offset:3072\n\t" \
    "v_mfma_f32_16x16x32_bf16 v[204:207], v[180:183], v[40:43], v[204:207]\n\t" \
    "s_waitcnt lgkmcnt(4)\n\t" \
    "v_mfma_f32_16x16x32_bf16 v[208:211], v[188:191], v[40:43], v[208:211]\n\t" \
    "v_mfma_f32_16x16x32_bf16 v[196:199], v[184:187], v[44:47], v[196:199]\n\t" \
    "ds_read_b128 v[36:39], %[xp] offset:4096\n\t" \
    "v_mfma_f32_16x16x32_bf16 v[200:203], v[192:195], v[44:47], v[200:203]\n\t" \
    "ds_read_b128 v[40:43], %[xp] offset:5120\n\t" \
    "v_mfma_f32_16x16x32_bf16 v[204:207], v[184:187], v[48:51], v[204:207]\n\t" \
    "s_waitcnt lgkmcnt(4)\n\t" \
    "v_mfma_f32_16x16x32_bf16 v[208:211], v[192:195], v[48:51], v[208:211]\n\t" \
    "s_add_u32 %[sx], %[sx], 262144\n\t" \
    "s_add_u32 %[t], %[t], 1\n\t" \
    "s_waitcnt vmcnt(4)\n\t" \
    "s_barrier\n\t" \
    "s_cmp_lt_u32 %[t], %[te]\n\t" \
    "s_cbranch_scc1 L0_%=\n\t" \
    "s_cmp_eq_u32 %[fin], %[te]\n\t" \
    "s_cbranch_scc0 E_%=\n\t" \
    "s_nop 4\n\t" \
    "ds_write_b128 %[pa], v[196:199] offset:0\n\t" \
    "ds_write_b128 %[pa], v[200:203] offset:1024\n\t" \
    "ds_write_b128 %[pa], v[204:207] offset:2048\n\t" \
    "ds_write_b128 %[pa], v[208:211] offset:3072\n\t" \
    "s_waitcnt lgkmcnt(0)\n\t" \
    "s_barrier\n\t" \
    "s_branch E_%=\n\t" \
    "L1_%=:\n\t" \
    "s_setprio 1\n\t" \
    "s_add_u32 m0, %[sd], 49152\n\t" \
    "s_add_u32 %[a], %[sx], 278528\n\t" \
    "v_mfma_f32_16x16x32_bf16 v[0:3], v[68:71], v[20:23], 0\n\t" \
    "ds_read_b128 v[44:47], %[xp] offset:6144\n\t" \
    "v_mfma_f32_16x16x32_bf16 v[4:7], v[76:79], v[20:23], 0\n\t" \
    "ds_read_b128 v[48:51], %[xp] offset:7168\n\t" \
    "v_mfma_f32_16x16x32_bf16 v[8:11], v[68:71], v[24:27], 0\n\t" \
    "s_waitcnt lgkmcnt(4)\n\t" \
    "v_mfma_f32_16x16x32_bf16 v[12:15], v[76:79], v[24:27], 0\n\t" \
    "v_mfma_f32_16x16x32_bf16 v[0:3], v[72:75], v[28:31], v[0:3]\n\t" \
    "ds_read_b128 v[20:23], %[xp] offset:8192\n\t" \
    "v_mfma_f32_16x16x32_bf16 v[4:7], v[80:83], v[28:31], v[4:7]\n\t" \
    "ds_read_b128 v[24:27], %[xp] offset:9216\n\t" \
    "v_mfma_f32_16x16x32_bf16 v[8:11], v[72:75], v[32:35], v[8:11]\n\t" \
    "s_waitcnt lgkmcnt(4)\n\t" \
    "v_mfma_f32_16x16x32_bf16 v[12:15], v[80:83], v[32:35], v[12:15]\n\t" \
    "v_mfma_f32_16x16x32_bf16 v[0:3], v[84:87], v[36:39], v[0:3]\n\t" \
    "ds_read_b128 v[28:31], %[xp] offset:10240\n\t" \
    "v_mfma_f32_16x16x32_bf16 v[4:7], v[92:95], v[36:39], v[4:7]\n\t" \
    "ds_read_b128 v[32:35], %[xp] offset:11264\n\t" \
    "v_mfma_f32_16x16x32_bf16 v[8:11], v[84:87], v[40:43], v[8:11]\n\t" \
    "s_waitcnt lgkmcnt(4)\n\t" \
    "v_mfma_f32_16x16x32_bf16 v[12:15], v[92:95], v[40:43], v[12:15]\n\t" \
    "v_mfma_f32_16x16x32_bf16 v[0:3], v[88:91], v[44:47], v[0:3]\n\t" \
    "ds_read_b128 v[36:39], %[xp] offset:12288\n\t" \
    "v_mfma_f32_16x16x32_bf16 v[4:7], v[96:99], v[44:47], v[4:7]\n\t" \
    "ds_read_b128 v[40:43], %[xp] offset:13312\n\t" \
    "v_mfma_f32_16x16x32_bf16 v[8:11], v[88:91], v[48:51], v[8:11]\n\t" \
    "s_waitcnt lgkmcnt(4)\n\t" \
    "v_mfma_f32_16x16x32_bf16 v[12:15], v[96:99], v[48:51], v[12:15]\n\t" \
    "v_mfma_f32_16x16x32_bf16 v[0:3], v[100:103], v[20:23], v[0:3]\n\t" \
    "ds_read_b128 v[44:47], %[xp] offset:14336\n\t" \
    "v_mfma_f32_16x16x32_bf16 v[4:7], v[108:111], v[20:23], v[4:7]\n\t" \
    "ds_read_b128 v[48:51], %[xp] offset:15360\n\t" \
    "v_mfma_f32_16x16x32_bf16 v[8:11], v[100:103], v[24:27], v[8:11]\n\t" \
    "s_waitcnt lgkmcnt(4)\n\t" \
    "v_mfma_f32_16x16x32_bf16 v[12:15], v[108:111], v[24:27], v[12:15]\n\t" \
    "buffer_load_dwordx4 %[l16], %[rs], %[a] offen offset:0 sc1 lds\n\t" \
    "v_mfma_f32_16x16x32_bf16 v[0:3], v[104:107], v[28:31], v[0:3]\n\t" \
    "ds_read_b128 v[20:23], %[xp] offset:16384\n\t" \
    "v_mfma_f32_16x16x32_bf16 v[4:7], v[112:115], v[28:31], v[4:7]\n\t" \
    "ds_read_b128 v[24:27], %[xp] offset:17408\n\t" \
    "v_mfma_f32_16x16x32_bf16 v[8:11], v[104:107], v[32:35], v[8:11]\n\t" \
    "s_waitcnt lgkmcnt(4)\n\t" \
    "v_mfma_f32_16x16x32_bf16 v[12:15], v[112:115], v[32:35], v[12:15]\n\t" \
    "buffer_load_dwordx4 %[l16], %[rs], %[a] offen offset:1024 sc1 lds\n\t" \
    "v_mfma_f32_16x16x32_bf16 v[0:3], v[116:119], v[36:39], v[0:3]\n\t" \
    "ds_read_b128 v[28:31], %[xp] offset:18432\n\t" \
    "v_mfma_f32_16x16x32_bf16 v[4:7], v[124:127], v[36:39], v[4:7]\n\t" \
    "ds_read_b128 v[32:35], %[xp] offset:19456\n\t" \
    "v_mfma_f32_16x16x32_bf16 v[8:11], v[116:119], v[40:43], v[8:11]\n\t" \
    "s_waitcnt lgkmcnt(4)\n\t" \
    "v_mfma_f32_16x16x32_bf16 v[12:15], v[124:127], v[40:43], v[12:15]\n\t" \
    "buffer_load_dwordx4 %[l16], %[rs], %[a] offen offset:2048 sc1 lds\n\t" \
    "v_mfma_f32_16x16x32_bf16 v[0:3], v[120:123], v[44:47], v[0:3]\n\t" \
    "ds_read_b128 v[36:39], %[xp] offset:20480\n\t" \
    "v_mfma_f32_16x16x32_bf16 v[4:7], v[128:131], v[44:47], v[4:7]\n\t" \
    "ds_read_b128 v[40:43], %[xp] offset:21504\n\t" \
    "v_mfma_f32_16x16x32_bf16 v[8:11], v[120:123], v[48:51], v[8:11]\n\t" \
    "s_waitcnt lgkmcnt(4)\n\t" \
    "v_mfma_f32_16x16x32_bf16 v[12:15], v[128:131], v[48:51], v[12:15]\n\t" \
    "buffer_load_dwordx4 %[l16], %[rs], %[a] offen offset:3072 sc1 lds\n\t" \
    "s_waitcnt vmcnt(4)\n\t" \
    "s_barrier\n\t" \
    "s_setprio 1\n\t" \
    "s_add_u32 m0, %[sd], 0\n\t" \
    "s_add_u32 %[a], %[sx], 524288\n\t" \
    "s_add_u32 %[b], %[oo], 0x1000\n\t" \
    "s_cmp_lg_u32 %[t], 0\n\t" \
    "s_cselect_b64 %[em], -1, 0\n\t" \
    "v_mfma_f32_16x16x32_bf16 v[0:3], v[132:135], v[20:23], v[0:3]\n\t" \
    "ds_read_b128 v[44:47], %[xp] offset:22528\n\t" \
    "v_mfma_f32_16x16x32_bf16 v[4:7], v[140:143], v[20:23], v[4:7]\n\t" \
    "ds_read_b128 v[48:51], %[xp] offset:23552\n\t" \
    "v_mfma_f32_16x16x32_bf16 v[8:11], v[132:135], v[24:27], v[8:11]\n\t" \
    "ds_read_b128 v[52:55], %[pa] offset:0\n\t" \
    "s_waitcnt lgkmcnt(5)\n\t" \
    "v_mfma_f32_16x16x32_bf16 v[12:15], v[140:143], v[24:27], v[12:15]\n\t" \
    "buffer_load_dwordx4 %[l16], %[rs], %[a] offen offset:0 sc1 lds\n\t" \
    "v_mfma_f32_16x16x32_bf16 v[0:3], v[136:139], v[28:31], v[0:3]\n\t" \
    "ds_read_b128 v[20:23], %[xp] offset:24576\n\t" \
    "v_mfma_f32_16x16x32_bf16 v[4:7], v[144:147], v[28:31], v[4:7]\n\t" \
    "ds_read_b128 v[24:27], %[xp] offset:25600\n\t" \
    "v_mfma_f32_16x16x32_bf16 v[8:11], v[136:139], v[32:35], v[8:11]\n\t" \
    "ds_read_b128 v[56:59], %[pa] offset:1024\n\t" \
    "s_waitcnt lgkmcnt(6)\n\t" \
    "v_mfma_f32_16x16x32_bf16 v[12:15], v[144:147], v[32:35], v[12:15]\n\t" \
    "buffer_load_dwordx4 %[l16], %[rs], %[a] offen offset:1024 sc1 lds\n\t" \
    "v_mfma_f32_16x16x32_bf16 v[0:3], v[148:151], v[36:39], v[0:3]\n\t" \
    "ds_read_b128 v[28:31], %[xp] offset:26624\n\t" \
    "v_mfma_f32_16x16x32_bf16 v[4:7], v[156:159], v[36:39], v[4:7]\n\t" \
    "ds_read_b128 v[32:35], %[xp] offset:27648\n\t" \
    "v_mfma_f32_16x16x32_bf16 v[8:11], v[148:151], v[40:43], v[8:11]\n\t" \
    "ds_read_b128 v[60:63], %[pa] offset:2048\n\t" \
    "s_waitcnt lgkmcnt(7)\n\t" \
    "v_mfma_f32_16x16x32_bf16 v[12:15], v[156:159], v[40:43], v[12:15]\n\t" \
    "buffer_load_dwordx4 %[l16], %[rs], %[a] offen offset:2048 sc1 lds\n\t" \
    "v_mfma_f32_16x16x32_bf16 v[0:3], v[152:155], v[44:47], v[0:3]\n\t" \
    "ds_read_b128 v[36:39], %[xp] offset:28672\n\t" \
    "s_waitcnt lgkmcnt(7)\n\t" \
    "v_add_f32 v196, v196, v52\n\t" \
    "v_mfma_f32_16x16x32_bf16 v[4:7], v[160:163], v[44:47], v[4:7]\n\t" \
    "ds_read_b128 v[40:43], %[xp] offset:29696\n\t" \
    "v_add_f32 v197, v197, v53\n\t" \
    "v_mfma_f32_16x16x32_bf16 v[8:11], v[152:155], v[48:51], v[8:11]\n\t" \
    "ds_read_b128 v[64:67], %[pa] offset:3072\n\t" \
    "s_waitcnt lgkmcnt(7)\n\t" \
    "v_mfma_f32_16x16x32_bf16 v[12:15], v[160:163], v[48:51], v[12:15]\n\t" \
    "buffer_load_dwordx4 %[l16], %[rs], %[a] offen offset:3072 sc1 lds\n\t" \
    "v_add_f32 v198, v198, v54\n\t" \
    "v_add_f32 v199, v199, v55\n\t" \
    "v_mfma_f32_16x16x32_bf16 v[0:3], v[164:167], v[20:23], v[0:3]\n\t" \
    "ds_read_b128 v[44:47], %[xp] offset:30720\n\t" \
    "s_waitcnt lgkmcnt(7)\n\t" \
    "v_add_f32 v200, v200, v56\n\t" \
    "v_mfma_f32_16x16x32_bf16 v[4:7], v[172:175], v[20:23], v[4:7]\n\t" \
    "ds_read_b128 v[48:51], %[xp] offset:31744\n\t" \
    "v_add_f32 v201, v201, v57\n\t" \
    "v_mfma_f32_16x16x32_bf16 v[8:11], v[164:167], v[24:27], v[8:11]\n\t" \
    "v_add_f32 v202, v202, v58\n\t" \
    "s_waitcnt lgkmcnt(6)\n\t" \
    "v_mfma_f32_16x16x32_bf16 v[12:15], v[172:175], v[24:27], v[12:15]\n\t" \
    "v_add_f32 v203, v203, v59\n\t" \
    "s_waitcnt lgkmcnt(5)\n\t" \
    "v_add_f32 v204, v204, v60\n\t" \
    "v_add_f32 v205, v205, v61\n\t" \
    "v_mfma_f32_16x16x32_bf16 v[0:3], v[168:171], v[28:31], v[0:3]\n\t" \
    "ds_read_b128 v[20:23], %[xp] offset:32768\n\t" \
    "v_add_f32 v206, v206, v62\n\t" \
    "v_mfma_f32_16x16x32_bf16 v[4:7], v[176:179], v[28:31], v[4:7]\n\t" \
    "ds_read_b128 v[24:27], %[xp] offset:33792\n\t" \
    "v_add_f32 v207, v207, v63\n\t" \
    "v_mfma_f32_16x16x32_bf16 v[8:11], v[168:171], v[32:35], v[8:11]\n\t" \
    "s_waitcnt lgkmcnt(4)\n\t" \
    "v_add_f32 v208, v208, v64\n\t" \
    "v_mfma_f32_16x16x32_bf16 v[12:15], v[176:179], v[32:35], v[12:15]\n\t" \
    "v_add_f32 v209, v209, v65\n\t" \
    "v_add_f32 v210, v210, v66\n\t" \
    "v_add_f32 v211, v211, v67\n\t" \
    "v_mfma_f32_16x16x32_bf16 v[0:3], v[180:183], v[36:39], v[0:3]\n\t" \
    "ds_read_b128 v[28:31], %[xp] offset:34816\n\t" \
    "v_mfma_f32_16x16x32_bf16 v[4:7], v[188:191], v[36:39], v[4:7]\n\t" \
    "ds_read_b128 v[32:35], %[xp] offset:35840\n\t" \
    "s_mov_b64 exec, %[em]\n\t" \
    "buffer_store_dwordx4 v[196:199], %[l16], %[rs], %[oo] offen offset:0 sc1\n\t" \
    "s_mov_b64 exec, -1\n\t" \
    "v_mfma_f32_16x16x32_bf16 v[8:11], v[180:183], v[40:43], v[8:11]\n\t" \
    "s_waitcnt lgkmcnt(4)\n\t" \
    "v_mfma_f32_16x16x32_bf16 v[12:15], v[188:191], v[40:43], v[12:15]\n\t" \
    "s_mov_b64 exec, %[em]\n\t" \
    "buffer_store_dwordx4 v[200:203], %[l16], %[rs], %[oo] offen offset:1024 sc1\n\t" \
    "s_mov_b64 exec, -1\n\t" \
    "v_mfma_f32_16x16x32_bf16 v[0:3], v[184:187], v[44:47], v[0:3]\n\t" \
    "ds_read_b128 v[36:39], %[xp] offset:36864\n\t" \
    "v_mfma_f32_16x16x32_bf16 v[4:7], v[192:195], v[44:47], v[4:7]\n\t" \
    "ds_read_b128 v[40:43], %[xp] offset:37888\n\t" \
    "s_mov_b64 exec, %[em]\n\t" \
    "buffer_store_dwordx4 v[204:207], %[l16], %[rs], %[oo] offen offset:2048 sc1\n\t" \
    "s_mov_b64 exec, -1\n\t" \
    "v_mfma_f32_16x16x32_bf16 v[8:11], v[184:187], v[48:51], v[8:11]\n\t" \
    "s_waitcnt lgkmcnt(4)\n\t" \
    "v_mfma_f32_16x16x32_bf16 v[12:15], v[192:195], v[48:51], v[12:15]\n\t" \
    "s_mov_b64 exec, %[em]\n\t" \
    "buffer_store_dwordx4 v[208:211], %[l16], %[rs], %[oo] offen offset:3072 sc1\n\t" \
    "s_mov_b64 exec, -1\n\t" \
    "s_add_u32 %[sx], %[sx], 262144\n\t" \
    "s_add_u32 %[t], %[t], 1\n\t" \
    "s_add_u32 %[oo], %[oo], 524288\n\t" \
    "s_waitcnt vmcnt(8)\n\t" \
    "s_cmp_gt_u32 %[t], " STR(SQUICK) " + 1\n\t" \
    "s_cbranch_scc1 B0_%=\n\t" \
    "s_waitcnt vmcnt(0)\n\t" \
    "B0_%=:\n\t" \
    "s_barrier\n\t" \
    "s_setprio 1\n\t" \
    "s_add_u32 m0, %[sd], 16384\n\t" \
    "s_add_u32 %[a], %[sx], 278528\n\t" \
    "v_mfma_f32_16x16x32_bf16 v[196:199], v[68:71], v[20:23], 0\n\t" \
    "ds_read_b128 v[44:47], %[xp] offset:38912\n\t" \
    "v_mfma_f32_16x16x32_bf16 v[200:203], v[76:79], v[20:23], 0\n\t" \
    "ds_read_b128 v[48:51], %[xp] offset:39936\n\t" \
    "v_mfma_f32_16x16x32_bf16 v[204:207], v[68:71], v[24:27], 0\n\t" \
    "s_waitcnt lgkmcnt(4)\n\t" \
    "v_mfma_f32_16x16x32_bf16 v[208:211], v[76:79], v[24:27], 0\n\t" \
    "v_mfma_f32_16x16x32_bf16 v[196:199], v[72:75], v[28:31], v[196:199]\n\t" \
    "ds_read_b128 v[20:23], %[xp] offset:40960\n\t" \
    "v_mfma_f32_16x16x32_bf16 v[200:203], v[80:83], v[28:31], v[200:203]\n\t" \
    "ds_read_b128 v[24:27], %[xp] offset:41984\n\t" \
    "v_mfma_f32_16x16x32_bf16 v[204:207], v[72:75], v[32:35], v[204:207]\n\t" \
    "s_waitcnt lgkmcnt(4)\n\t" \
    "v_mfma_f32_16x16x32_bf16 v[208:211], v[80:83], v[32:35], v[208:211]\n\t" \
    "v_mfma_f32_16x16x32_bf16 v[196:199], v[84:87], v[36:39], v[196:199]\n\t" \
    "ds_read_b128 v[28:31], %[xp] offset:43008\n\t" \
    "v_mfma_f32_16x16x32_bf16 v[200:203], v[92:95], v[36:39], v[200:203]\n\t" \
    "ds_read_b128 v[32:35], %[xp] offset:44032\n\t" \
    "v_mfma_f32_16x16x32_bf16 v[204:207], v[84:87], v[40:43], v[204:207]\n\t" \
    "s_waitcnt lgkmcnt(4)\n\t" \
    "v_mfma_f32_16x16x32_bf16 v[208:211], v[92:95], v[40:43], v[208:211]\n\t" \
    "v_mfma_f32_16x16x32_bf16 v[196:199], v[88:91], v[44:47], v[196:199]\n\t" \
    "ds_read_b128 v[36:39], %[xp] offset:45056\n\t" \
    "v_mfma_f32_16x16x32_bf16 v[200:203], v[96:99], v[44:47], v[200:203]\n\t" \
    "ds_read_b128 v[40:43], %[xp] offset:46080\n\t" \
    "v_mfma_f32_16x16x32_bf16 v[204:207], v[88:91], v[48:51], v[204:207]\n\t" \
    "s_waitcnt lgkmcnt(4)\n\t" \
    "v_mfma_f32_16x16x32_bf16 v[208:211], v[96:99], v[48:51], v[208:211]\n\t" \
    "v_mfma_f32_16x16x32_bf16 v[196:199], v[100:103], v[20:23], v[196:199]\n\t" \
    "ds_read_b128 v[44:47], %[xp] offset:47104\n\t" \
    "v_mfma_f32_16x16x32_bf16 v[200:203], v[108:111], v[20:23], v[200:203]\n\t" \
    "ds_read_b128 v[48:51], %[xp] offset:48128\n\t" \
    "v_mfma_f32_16x16x32_bf16 v[204:207], v[100:103], v[24:27], v[204:207]\n\t" \
    "s_waitcnt lgkmcnt(4)\n\t" \
    "v_mfma_f32_16x16x32_bf16 v[208:211], v[108:111], v[24:27], v[208:211]\n\t" \
    "buffer_load_dwordx4 %[l16], %[rs], %[a] offen offset:0 sc1 lds\n\t" \
    "v_mfma_f32_16x16x32_bf16 v[196:199], v[104:107], v[28:31], v[196:199]\n\t" \
    "ds_read_b128 v[20:23], %[xp] offset:49152\n\t" \
    "v_mfma_f32_16x16x32_bf16 v[200:203], v[112:115], v[28:31], v[200:203]\n\t" \
    "ds_read_b128 v[24:27], %[xp] offset:50176\n\t" \
    "v_mfma_f32_16x16x32_bf16 v[204:207], v[104:107], v[32:35], v[204:207]\n\t" \
    "s_waitcnt lgkmcnt(4)\n\t" \
    "v_mfma_f32_16x16x32_bf16 v[208:211], v[112:115], v[32:35], v[208:211]\n\t" \
    "buffer_load_dwordx4 %[l16], %[rs], %[a] offen offset:1024 sc1 lds\n\t" \
    "v_mfma_f32_16x16x32_bf16 v[196:199], v[116:119], v[36:39], v[196:199]\n\t" \
    "ds_read_b128 v[28:31], %[xp] offset:51200\n\t" \
    "v_mfma_f32_16x16x32_bf16 v[200:203], v[124:127], v[36:39], v[200:203]\n\t" \
    "ds_read_b128 v[32:35], %[xp] offset:52224\n\t" \
    "v_mfma_f32_16x16x32_bf16 v[204:207], v[116:119], v[40:43], v[204:207]\n\t" \
    "s_waitcnt lgkmcnt(4)\n\t" \
    "v_mfma_f32_16x16x32_bf16 v[208:211], v[124:127], v[40:43], v[208:211]\n\t" \
    "buffer_load_dwordx4 %[l16], %[rs], %[a] offen offset:2048 sc1 lds\n\t" \
    "v_mfma_f32_16x16x32_bf16 v[196:199], v[120:123], v[44:47], v[196:199]\n\t" \
    "ds_read_b128 v[36:39], %[xp] offset:53248\n\t" \
    "v_mfma_f32_16x16x32_bf16 v[200:203], v[128:131], v[44:47], v[200:203]\n\t" \
    "ds_read_b128 v[40:43], %[xp] offset:54272\n\t" \
    "v_mfma_f32_16x16x32_bf16 v[204:207], v[120:123], v[48:51], v[204:207]\n\t" \
    "s_waitcnt lgkmcnt(4)\n\t" \
    "v_mfma_f32_16x16x32_bf16 v[208:211], v[128:131], v[48:51], v[208:211]\n\t" \
    "buffer_load_dwordx4 %[l16], %[rs], %[a] offen offset:3072 sc1 lds\n\t" \
    "s_waitcnt vmcnt(4)\n\t" \
    "s_barrier\n\t" \
    "s_setprio 1\n\t" \
    "s_add_u32 m0, %[sd], 32768\n\t" \
    "s_add_u32 %[a], %[sx], 524288\n\t" \
    "s_add_u32 %[b], %[oo], 0x1000\n\t" \
    "s_cmp_lg_u32 %[t], 0\n\t" \
    "s_cselect_b64 %[em], -1, 0\n\t" \
    "v_mfma_f32_16x16x32_bf16 v[196:199], v[132:135], v[20:23], v[196:199]\n\t" \
    "ds_read_b128 v[44:47], %[xp] offset:55296\n\t" \
    "v_mfma_f32_16x16x32_bf16 v[200:203], v[140:143], v[20:23], v[200:203]\n\t" \
    "ds_read_b128 v[48:51], %[xp] offset:56320\n\t" \
    "v_mfma_f32_16x16x32_bf16 v[204:207], v[132:135], v[24:27], v[204:207]\n\t" \
    "ds_read_b128 v[52:55], %[pa] offset:0\n\t" \
    "s_waitcnt lgkmcnt(5)\n\t" \
    "v_mfma_f32_16x16x32_bf16 v[208:211], v[140:143], v[24:27], v[208:211]\n\t" \
    "buffer_load_dwordx4 %[l16], %[rs], %[a] offen offset:0 sc1 lds\n\t" \
    "v_mfma_f32_16x16x32_bf16 v[196:199], v[136:139], v[28:31], v[196:199]\n\t" \
    "ds_read_b128 v[20:23], %[xp] offset:57344\n\t" \
    "v_mfma_f32_16x16x32_bf16 v[200:203], v[144:147], v[28:31], v[200:203]\n\t" \
    "ds_read_b128 v[24:27], %[xp] offset:58368\n\t" \
    "v_mfma_f32_16x16x32_bf16 v[204:207], v[136:139], v[32:35], v[204:207]\n\t" \
    "ds_read_b128 v[56:59], %[pa] offset:1024\n\t" \
    "s_waitcnt lgkmcnt(6)\n\t" \
    "v_mfma_f32_16x16x32_bf16 v[208:211], v[144:147], v[32:35], v[208:211]\n\t" \
    "buffer_load_dwordx4 %[l16], %[rs], %[a] offen offset:1024 sc1 lds\n\t" \
    "v_mfma_f32_16x16x32_bf16 v[196:199], v[148:151], v[36:39], v[196:199]\n\t" \
    "ds_read_b128 v[28:31], %[xp] offset:59392\n\t" \
    "v_mfma_f32_16x16x32_bf16 v[200:203], v[156:159], v[36:39], v[200:203]\n\t" \
    "ds_read_b128 v[32:35], %[xp] offset:60416\n\t" \
    "v_mfma_f32_16x16x32_bf16 v[204:207], v[148:151], v[40:43], v[204:207]\n\t" \
    "ds_read_b128 v[60:63], %[pa] offset:2048\n\t" \
    "s_waitcnt lgkmcnt(7)\n\t" \
    "v_mfma_f32_16x16x32_bf16 v[208:211], v[156:159], v[40:43], v[208:211]\n\t" \
    "buffer_load_dwordx4 %[l16], %[rs], %[a] offen offset:2048 sc1 lds\n\t" \
    "v_mfma_f32_16x16x32_bf16 v[196:199], v[152:155], v[44:47], v[196:199]\n\t" \
    "ds_read_b128 v[36:39], %[xp] offset:61440\n\t" \
    "s_waitcnt lgkmcnt(7)\n\t" \
    "v_add_f32 v0, v0, v52\n\t" \
    "v_mfma_f32_16x16x32_bf16 v[200:203], v[160:163], v[44:47], v[200:203]\n\t" \
    "ds_read_b128 v[40:43], %[xp] offset:62464\n\t" \
    "v_add_f32 v1, v1, v53\n\t" \
    "v_mfma_f32_16x16x32_bf16 v[204:207], v[152:155], v[48:51], v[204:207]\n\t" \
    "ds_read_b128 v[64:67], %[pa] offset:3072\n\t" \
    "s_waitcnt lgkmcnt(7)\n\t" \
    "v_mfma_f32_16x16x32_bf16 v[208:211], v[160:163], v[48:51], v[208:211]\n\t" \
    "buffer_load_dwordx4 %[l16], %[rs], %[a] offen offset:3072 sc1 lds\n\t" \
    "v_add_f32 v2, v2, v54\n\t" \
    "v_add_f32 v3, v3, v55\n\t" \
    "v_mfma_f32_16x16x32_bf16 v[196:199], v[164:167], v[20:23], v[196:199]\n\t" \
    "ds_read_b128 v[44:47], %[xp] offset:63488\n\t" \
    "s_waitcnt lgkmcnt(7)\n\t" \
    "v_add_f32 v4, v4, v56\n\t" \
    "v_mfma_f32_16x16x32_bf16 v[200:203], v[172:175], v[20:23], v[200:203]\n\t" \
    "ds_read_b128 v[48:51], %[xp] offset:64512\n\t" \
    "v_add_f32 v5, v5, v57\n\t" \
    "v_mfma_f32_16x16x32_bf16 v[204:207], v[164:167], v[24:27], v[204:207]\n\t" \
    "v_add_f32 v6, v6, v58\n\t" \
    "s_waitcnt lgkmcnt(6)\n\t" \
    "v_mfma_f32_16x16x32_bf16 v[208:211], v[172:175], v[24:27], v[208:211]\n\t" \
    "v_add_f32 v7, v7, v59\n\t" \
    "s_waitcnt lgkmcnt(5)\n\t" \
    "v_add_f32 v8, v8, v60\n\t" \
    "v_add_f32 v9, v9, v61\n\t" \
    "v_mfma_f32_16x16x32_bf16 v[196:199], v[168:171], v[28:31], v[196:199]\n\t" \
    "ds_read_b128 v[20:23], %[xp] offset:0\n\t" \
    "v_add_f32 v10, v10, v62\n\t" \
    "v_mfma_f32_16x16x32_bf16 v[200:203], v[176:179], v[28:31], v[200:203]\n\t" \
    "ds_read_b128 v[24:27], %[xp] offset:1024\n\t" \
    "v_add_f32 v11, v11, v63\n\t" \
    "v_mfma_f32_16x16x32_bf16 v[204:207], v[168:171], v[32:35], v[204:207]\n\t" \
    "s_waitcnt lgkmcnt(4)\n\t" \
    "v_add_f32 v12, v12, v64\n\t" \
    "v_mfma_f32_16x16x32_bf16 v[208:211], v[176:179], v[32:35], v[208:211]\n\t" \
    "v_add_f32 v13, v13, v65\n\t" \
    "v_add_f32 v14, v14, v66\n\t" \
    "v_add_f32 v15, v15, v67\n\t" \
    "v_mfma_f32_16x16x32_bf16 v[196:199], v[180:183], v[36:39], v[196:199]\n\t" \
    "ds_read_b128 v[28:31], %[xp] offset:2048\n\t" \
    "v_mfma_f32_16x16x32_bf16 v[200:203], v[188:191], v[36:39], v[200:203]\n\t" \
    "ds_read_b128 v[32:35], %[xp] offset:3072\n\t" \
    "s_mov_b64 exec, %[em]\n\t" \
    "buffer_store_dwordx4 v[0:3], %[l16], %[rs], %[oo] offen offset:0 sc1\n\t" \
    "s_mov_b64 exec, -1\n\t" \
    "v_mfma_f32_16x16x32_bf16 v[204:207], v[180:183], v[40:43], v[204:207]\n\t" \
    "s_waitcnt lgkmcnt(4)\n\t" \
    "v_mfma_f32_16x16x32_bf16 v[208:211], v[188:191], v[40:43], v[208:211]\n\t" \
    "s_mov_b64 exec, %[em]\n\t" \
    "buffer_store_dwordx4 v[4:7], %[l16], %[rs], %[oo] offen offset:1024 sc1\n\t" \
    "s_mov_b64 exec, -1\n\t" \
    "v_mfma_f32_16x16x32_bf16 v[196:199], v[184:187], v[44:47], v[196:199]\n\t" \
    "ds_read_b128 v[36:39], %[xp] offset:4096\n\t" \
    "v_mfma_f32_16x16x32_bf16 v[200:203], v[192:195], v[44:47], v[200:203]\n\t" \
    "ds_read_b128 v[40:43], %[xp] offset:5120\n\t" \
    "s_mov_b64 exec, %[em]\n\t" \
    "buffer_store_dwordx4 v[8:11], %[l16], %[rs], %[oo] offen offset:2048 sc1\n\t" \
    "s_mov_b64 exec, -1\n\t" \
    "v_mfma_f32_16x16x32_bf16 v[204:207], v[184:187], v[48:51], v[204:207]\n\t" \
    "s_waitcnt lgkmcnt(4)\n\t" \
    "v_mfma_f32_16x16x32_bf16 v[208:211], v[192:195], v[48:51], v[208:211]\n\t" \
    "s_mov_b64 exec, %[em]\n\t" \
    "buffer_store_dwordx4 v[12:15], %[l16], %[rs], %[oo] offen offset:3072 sc1\n\t" \
    "s_mov_b64 exec, -1\n\t" \
    "s_add_u32 %[sx], %[sx], 262144\n\t" \
    "s_add_u32 %[t], %[t], 1\n\t" \
    "s_add_u32 %[oo], %[oo], 524288\n\t" \
    "s_waitcnt vmcnt(8)\n\t" \
    "s_cmp_gt_u32 %[t], " STR(SQUICK) " + 1\n\t" \
    "s_cbranch_scc1 B1_%=\n\t" \
    "s_waitcnt vmcnt(0)\n\t" \
    "B1_%=:\n\t" \
    "s_barrier\n\t" \
    "s_cmp_lt_u32 %[t], %[te]\n\t" \
    "s_cbranch_scc1 L1_%=\n\t" \
    "s_cmp_eq_u32 %[fin], %[te]\n\t" \
    "s_cbranch_scc0 E_%=\n\t" \
    "s_nop 4\n\t" \
    "s_barrier\n\t" \
    "ds_read_b128 v[52:55], %[pa] offset:0\n\t" \
    "ds_read_b128 v[56:59], %[pa] offset:1024\n\t" \
    "ds_read_b128 v[60:63], %[pa] offset:2048\n\t" \
    "ds_read_b128 v[64:67], %[pa] offset:3072\n\t" \
    "s_add_u32 %[b], %[oo], 0x1000\n\t" \
    "s_waitcnt lgkmcnt(0)\n\t" \
    "v_add_f32 v196, v196, v52\n\t" \
    "v_add_f32 v197, v197, v53\n\t" \
    "v_add_f32 v198, v198, v54\n\t" \
    "v_add_f32 v199, v199, v55\n\t" \
    "v_add_f32 v200, v200, v56\n\t" \
    "v_add_f32 v201, v201, v57\n\t" \
    "v_add_f32 v202, v202, v58\n\t" \
    "v_add_f32 v203, v203, v59\n\t" \
    "v_add_f32 v204, v204, v60\n\t" \
    "v_add_f32 v205, v205, v61\n\t" \
    "v_add_f32 v206, v206, v62\n\t" \
    "v_add_f32 v207, v207, v63\n\t" \
    "v_add_f32 v208, v208, v64\n\t" \
    "v_add_f32 v209, v209, v65\n\t" \
    "v_add_f32 v210, v210, v66\n\t" \
    "v_add_f32 v211, v211, v67\n\t" \
    "buffer_store_dwordx4 v[196:199], %[l16], %[rs], %[oo] offen offset:0 sc1\n\t" \
    "buffer_store_dwordx4 v[200:203], %[l16], %[rs], %[oo] offen offset:1024 sc1\n\t" \
    "buffer_store_dwordx4 v[204:207], %[l16], %[rs], %[oo] offen offset:2048 sc1\n\t" \
    "buffer_store_dwordx4 v[208:211], %[l16], %[rs], %[oo] offen offset:3072 sc1\n\t" \
    "E_%=:\n\t" \
    "s_setprio 0\n\t" \
    "s_waitcnt lgkmcnt(0)\n\t" \
    "s_nop 7\n\t" \
    : [t] "+s"(t), [sx] "+s"(sx), [oo] "+s"(oo), [a] "=&s"(tmp), [b] "=&s"(tmp2), [em] "=&s"(em), "+{v[20:27]}"(xf[0]), "+{v[28:35]}"(xf[1]), "+{v[36:43]}"(xf[2]), "+{v[196:203]}"(ac[0]), "+{v[204:211]}"(ac[1]) \
    : [te] "s"(TE_), [fin] "s"(FIN_), [kh] "s"(kh), [sd] "s"(sd), [rs] "s"(rs), [l16] "v"(l16), [xp] "v"(xp), [pa] "v"(pa), [fm] "s"(fm), [pm] "s"(pm), [fo] "s"(fo), [po] "s"(po), [pd] "s"(pd), [tag] "s"(P.tag), "{v[68:75]}"(w[0][0]), "{v[76:83]}"(w[0][1]), "{v[84:91]}"(w[1][0]), "{v[92:99]}"(w[1][1]), "{v[100:107]}"(w[2][0]), "{v[108:115]}"(w[2][1]), "{v[116:123]}"(w[3][0]), "{v[124:131]}"(w[3][1]), "{v[132:139]}"(w[4][0]), "{v[140:147]}"(w[4][1]), "{v[148:155]}"(w[5][0]), "{v[156:163]}"(w[5][1]), "{v[164:171]}"(w[6][0]), "{v[172:179]}"(w[6][1]), "{v[180:187]}"(w[7][0]), "{v[188:195]}"(w[7][1]) \
    : "memory", "scc", "v0", "v1", "v2", "v3", "v4", "v5", "v6", "v7", "v8", "v9", "v10", "v11", "v12", "v13", "v14", "v15", "v44", "v45", "v46", "v47", "v48", "v49", "v50", "v51", "v52", "v53", "v54", "v55", "v56", "v57", "v58", "v59", "v60", "v61", "v62", "v63", "v64", "v65", "v66", "v67")

#define ONE_X(XP_) \
  asm volatile( \
    "ds_read_b128 v[20:23], %[xp] offset:0\n\t" \
    "ds_read_b128 v[24:27], %[xp] offset:1024\n\t" \
    "ds_read_b128 v[28:31], %[xp] offset:2048\n\t" \
    "ds_read_b128 v[32:35], %[xp] offset:3072\n\t" \
    "ds_read_b128 v[36:39], %[xp] offset:4096\n\t" \
    "ds_read_b128 v[40:43], %[xp] offset:5120\n\t" \
    "s_waitcnt lgkmcnt(4)\n\t" \
    "v_mfma_f32_16x16x32_bf16 v[0:3], v[68:71], v[20:23], 0\n\t" \
    "ds_read_b128 v[44:47], %[xp] offset:6144\n\t" \
    "v_mfma_f32_16x16x32_bf16 v[4:7], v[76:79], v[20:23], 0\n\t" \
    "ds_read_b128 v[48:51], %[xp] offset:7168\n\t" \
    "v_mfma_f32_16x16x32_bf16 v[8:11], v[68:71], v[24:27], 0\n\t" \
    "s_waitcnt lgkmcnt(4)\n\t" \
    "v_mfma_f32_16x16x32_bf16 v[12:15], v[76:79], v[24:27], 0\n\t" \
    "v_mfma_f32_16x16x32_bf16 v[0:3], v[72:75], v[28:31], v[0:3]\n\t" \
    "ds_read_b128 v[20:23], %[xp] offset:8192\n\t" \
    "v_mfma_f32_16x16x32_bf16 v[4:7], v[80:83], v[28:31], v[4:7]\n\t" \
    "ds_read_b128 v[24:27], %[xp] offset:9216\n\t" \
    "v_mfma_f32_16x16x32_bf16 v[8:11], v[72:75], v[32:35], v[8:11]\n\t" \
    "s_waitcnt lgkmcnt(4)\n\t" \
    "v_mfma_f32_16x16x32_bf16 v[12:15], v[80:83], v[32:35], v[12:15]\n\t" \
    "v_mfma_f32_16x16x32_bf16 v[0:3], v[84:87], v[36:39], v[0:3]\n\t" \
    "ds_read_b128 v[28:31], %[xp] offset:10240\n\t" \
    "v_mfma_f32_16x16x32_bf16 v[4:7], v[92:95], v[36:39], v[4:7]\n\t" \
    "ds_read_b128 v[32:35], %[xp] offset:11264\n\t" \
    "v_mfma_f32_16x16x32_bf16 v[8:11], v[84:87], v[40:43], v[8:11]\n\t" \
    "s_waitcnt lgkmcnt(4)\n\t" \
    "v_mfma_f32_16x16x32_bf16 v[12:15], v[92:95], v[40:43], v[12:15]\n\t" \
    "v_mfma_f32_16x16x32_bf16 v[0:3], v[88:91], v[44:47], v[0:3]\n\t" \
    "ds_read_b128 v[36:39], %[xp] offset:12288\n\t" \
    "v_mfma_f32_16x16x32_bf16 v[4:7], v[96:99], v[44:47], v[4:7]\n\t" \
    "ds_read_b128 v[40:43], %[xp] offset:13312\n\t" \
    "v_mfma_f32_16x16x32_bf16 v[8:11], v[88:91], v[48:51], v[8:11]\n\t" \
    "s_waitcnt lgkmcnt(4)\n\t" \
    "v_mfma_f32_16x16x32_bf16 v[12:15], v[96:99], v[48:51], v[12:15]\n\t" \
    "v_mfma_f32_16x16x32_bf16 v[0:3], v[100:103], v[20:23], v[0:3]\n\t" \
    "ds_read_b128 v[44:47], %[xp] offset:14336\n\t" \
    "v_mfma_f32_16x16x32_bf16 v[4:7], v[108:111], v[20:23], v[4:7]\n\t" \
    "ds_read_b128 v[48:51], %[xp] offset:15360\n\t" \
    "v_mfma_f32_16x16x32_bf16 v[8:11], v[100:103], v[24:27], v[8:11]\n\t" \
    "s_waitcnt lgkmcnt(4)\n\t" \
    "v_mfma_f32_16x16x32_bf16 v[12:15], v[108:111], v[24:27], v[12:15]\n\t" \
    "s_waitcnt vmcnt(0)\n\t" \
    "s_barrier\n\t" \
    "v_mfma_f32_16x16x32_bf16 v[0:3], v[104:107], v[28:31], v[0:3]\n\t" \
    "ds_read_b128 v[20:23], %[xp] offset:16384\n\t" \
    "v_mfma_f32_16x16x32_bf16 v[4:7], v[112:115], v[28:31], v[4:7]\n\t" \
    "ds_read_b128 v[24:27], %[xp] offset:17408\n\t" \
    "v_mfma_f32_16x16x32_bf16 v[8:11], v[104:107], v[32:35], v[8:11]\n\t" \
    "s_waitcnt lgkmcnt(4)\n\t" \
    "v_mfma_f32_16x16x32_bf16 v[12:15], v[112:115], v[32:35], v[12:15]\n\t" \
    "buffer_load_dword v52, %[pv], %[rs], 0 offen sc1\n\t" \
    "v_mfma_f32_16x16x32_bf16 v[0:3], v[116:119], v[36:39], v[0:3]\n\t" \
    "ds_read_b128 v[28:31], %[xp] offset:18432\n\t" \
    "v_mfma_f32_16x16x32_bf16 v[4:7], v[124:127], v[36:39], v[4:7]\n\t" \
    "ds_read_b128 v[32:35], %[xp] offset:19456\n\t" \
    "v_mfma_f32_16x16x32_bf16 v[8:11], v[116:119], v[40:43], v[8:11]\n\t" \
    "s_waitcnt lgkmcnt(4)\n\t" \
    "v_mfma_f32_16x16x32_bf16 v[12:15], v[124:127], v[40:43], v[12:15]\n\t" \
    "v_mfma_f32_16x16x32_bf16 v[0:3], v[120:123], v[44:47], v[0:3]\n\t" \
    "ds_read_b128 v[36:39], %[xp] offset:20480\n\t" \
    "v_mfma_f32_16x16x32_bf16 v[4:7], v[128:131], v[44:47], v[4:7]\n\t" \
    "ds_read_b128 v[40:43], %[xp] offset:21504\n\t" \
    "v_mfma_f32_16x16x32_bf16 v[8:11], v[120:123], v[48:51], v[8:11]\n\t" \
    "s_waitcnt lgkmcnt(4)\n\t" \
    "v_mfma_f32_16x16x32_bf16 v[12:15], v[128:131], v[48:51], v[12:15]\n\t" \
    "v_mfma_f32_16x16x32_bf16 v[0:3], v[132:135], v[20:23], v[0:3]\n\t" \
    "ds_read_b128 v[44:47], %[xp] offset:22528\n\t" \
    "v_mfma_f32_16x16x32_bf16 v[4:7], v[140:143], v[20:23], v[4:7]\n\t" \
    "ds_read_b128 v[48:51], %[xp] offset:23552\n\t" \
    "v_mfma_f32_16x16x32_bf16 v[8:11], v[132:135], v[24:27], v[8:11]\n\t" \
    "s_waitcnt lgkmcnt(4)\n\t" \
    "v_mfma_f32_16x16x32_bf16 v[12:15], v[140:143], v[24:27], v[12:15]\n\t" \
    "v_mfma_f32_16x16x32_bf16 v[0:3], v[136:139], v[28:31], v[0:3]\n\t" \
    "ds_read_b128 v[20:23], %[xp] offset:24576\n\t" \
    "v_mfma_f32_16x16x32_bf16 v[4:7], v[144:147], v[28:31], v[4:7]\n\t" \
    "ds_read_b128 v[24:27], %[xp] offset:25600\n\t" \
    "v_mfma_f32_16x16x32_bf16 v[8:11], v[136:139], v[32:35], v[8:11]\n\t" \
    "s_waitcnt lgkmcnt(4)\n\t" \
    "v_mfma_f32_16x16x32_bf16 v[12:15], v[144:147], v[32:35], v[12:15]\n\t" \
    "v_mfma_f32_16x16x32_bf16 v[0:3], v[148:151], v[36:39], v[0:3]\n\t" \
    "ds_read_b128 v[28:31], %[xp] offset:26624\n\t" \
    "v_mfma_f32_16x16x32_bf16 v[4:7], v[156:159], v[36:39], v[4:7]\n\t" \
    "ds_read_b128 v[32:35], %[xp] offset:27648\n\t" \
    "v_mfma_f32_16x16x32_bf16 v[8:11], v[148:151], v[40:43], v[8:11]\n\t" \
    "s_waitcnt lgkmcnt(4)\n\t" \
    "v_mfma_f32_16x16x32_bf16 v[12:15], v[156:159], v[40:43], v[12:15]\n\t" \
    "v_mfma_f32_16x16x32_bf16 v[0:3], v[152:155], v[44:47], v[0:3]\n\t" \
    "ds_read_b128 v[36:39], %[xp] offset:28672\n\t" \
    "v_mfma_f32_16x16x32_bf16 v[4:7], v[160:163], v[44:47], v[4:7]\n\t" \
    "ds_read_b128 v[40:43], %[xp] offset:29696\n\t" \
    "v_mfma_f32_16x16x32_bf16 v[8:11], v[152:155], v[48:51], v[8:11]\n\t" \
    "s_waitcnt lgkmcnt(4)\n\t" \
    "v_mfma_f32_16x16x32_bf16 v[12:15], v[160:163], v[48:51], v[12:15]\n\t" \
    "v_mfma_f32_16x16x32_bf16 v[0:3], v[164:167], v[20:23], v[0:3]\n\t" \
    "ds_read_b128 v[44:47], %[xp] offset:30720\n\t" \
    "v_mfma_f32_16x16x32_bf16 v[4:7], v[172:175], v[20:23], v[4:7]\n\t" \
    "ds_read_b128 v[48:51], %[xp] offset:31744\n\t" \
    "v_mfma_f32_16x16x32_bf16 v[8:11], v[164:167], v[24:27], v[8:11]\n\t" \
    "s_waitcnt lgkmcnt(4)\n\t" \
    "v_mfma_f32_16x16x32_bf16 v[12:15], v[172:175], v[24:27], v[12:15]\n\t" \
    "v_mfma_f32_16x16x32_bf16 v[0:3], v[168:171], v[28:31], v[0:3]\n\t" \
    "v_mfma_f32_16x16x32_bf16 v[4:7], v[176:179], v[28:31], v[4:7]\n\t" \
    "v_mfma_f32_16x16x32_bf16 v[8:11], v[168:171], v[32:35], v[8:11]\n\t" \
    "s_waitcnt lgkmcnt(2)\n\t" \
    "v_mfma_f32_16x16x32_bf16 v[12:15], v[176:179], v[32:35], v[12:15]\n\t" \
    "v_mfma_f32_16x16x32_bf16 v[0:3], v[180:183], v[36:39], v[0:3]\n\t" \
    "v_mfma_f32_16x16x32_bf16 v[4:7], v[188:191], v[36:39], v[4:7]\n\t" \
    "v_mfma_f32_16x16x32_bf16 v[8:11], v[180:183], v[40:43], v[8:11]\n\t" \
    "s_waitcnt lgkmcnt(0)\n\t" \
    "v_mfma_f32_16x16x32_bf16 v[12:15], v[188:191], v[40:43], v[12:15]\n\t" \
    "v_mfma_f32_16x16x32_bf16 v[0:3], v[184:187], v[44:47], v[0:3]\n\t" \
    "v_mfma_f32_16x16x32_bf16 v[4:7], v[192:195], v[44:47], v[4:7]\n\t" \
    "v_mfma_f32_16x16x32_bf16 v[8:11], v[184:187], v[48:51], v[8:11]\n\t" \
    "v_mfma_f32_16x16x32_bf16 v[12:15], v[192:195], v[48:51], v[12:15]\n\t" \
    "s_waitcnt vmcnt(0)\n\t" \
    "s_nop 7\n\t" \
    : "=&{v52}"(pg), "=&{v[0:3]}"(a[0]), "=&{v[4:7]}"(a[1]), "=&{v[8:11]}"(a[2]), "=&{v[12:15]}"(a[3]) \
    : [xp] "v"(XP_), [pv] "v"(pv), [rs] "s"(rs), "{v[68:75]}"(w[0][0]), "{v[76:83]}"(w[0][1]), "{v[84:91]}"(w[1][0]), "{v[92:99]}"(w[1][1]), "{v[100:107]}"(w[2][0]), "{v[108:115]}"(w[2][1]), "{v[116:123]}"(w[3][0]), "{v[124:131]}"(w[3][1]), "{v[132:139]}"(w[4][0]), "{v[140:147]}"(w[4][1]), "{v[148:155]}"(w[5][0]), "{v[156:163]}"(w[5][1]), "{v[164:171]}"(w[6][0]), "{v[172:179]}"(w[6][1]), "{v[180:187]}"(w[7][0]), "{v[188:195]}"(w[7][1]) \
    : "memory", "v20", "v21", "v22", "v23", "v24", "v25", "v26", "v27", "v28", "v29", "v30", "v31", "v32", "v33", "v34", "v35", "v36", "v37", "v38", "v39", "v40", "v41", "v42", "v43", "v44", "v45", "v46", "v47", "v48", "v49", "v50", "v51")

#define ONE_K(XP_) \
  asm volatile( \
    "ds_read_b128 v[20:23], %[xp] offset:0\n\t" \
    "ds_read_b128 v[24:27], %[xp] offset:1024\n\t" \
    "ds_read_b128 v[28:31], %[xp] offset:2048\n\t" \
    "ds_read_b128 v[32:35], %[xp] offset:3072\n\t" \
    "ds_read_b128 v[36:39], %[xp] offset:4096\n\t" \
    "ds_read_b128 v[40:43], %[xp] offset:5120\n\t" \
    "s_waitcnt lgkmcnt(4)\n\t" \
    "v_mfma_f32_16x16x32_bf16 v[0:3], v[68:71], v[20:23], 0\n\t" \
    "ds_read_b128 v[44:47], %[xp] offset:6144\n\t" \
    "v_mfma_f32_16x16x32_bf16 v[4:7], v[76:79], v[20:23], 0\n\t" \
    "ds_read_b128 v[48:51], %[xp] offset:7168\n\t" \
    "v_mfma_f32_16x16x32_bf16 v[8:11], v[68:71], v[24:27], 0\n\t" \
    "s_waitcnt lgkmcnt(4)\n\t" \
    "v_mfma_f32_16x16x32_bf16 v[12:15], v[76:79], v[24:27], 0\n\t" \
    "v_mfma_f32_16x16x32_bf16 v[0:3], v[72:75], v[28:31], v[0:3]\n\t" \
    "ds_read_b128 v[20:23], %[xp] offset:8192\n\t" \
    "v_mfma_f32_16x16x32_bf16 v[4:7], v[80:83], v[28:31], v[4:7]\n\t" \
    "ds_read_b128 v[24:27], %[xp] offset:9216\n\t" \
    "v_mfma_f32_16x16x32_bf16 v[8:11], v[72:75], v[32:35], v[8:11]\n\t" \
    "s_waitcnt lgkmcnt(4)\n\t" \
    "v_mfma_f32_16x16x32_bf16 v[12:15], v[80:83], v[32:35], v[12:15]\n\t" \
    "v_mfma_f32_16x16x32_bf16 v[0:3], v[84:87], v[36:39], v[0:3]\n\t" \
    "ds_read_b128 v[28:31], %[xp] offset:10240\n\t" \
    "v_mfma_f32_16x16x32_bf16 v[4:7], v[92:95], v[36:39], v[4:7]\n\t" \
    "ds_read_b128 v[32:35], %[xp] offset:11264\n\t" \
    "v_mfma_f32_16x16x32_bf16 v[8:11], v[84:87], v[40:43], v[8:11]\n\t" \
    "s_waitcnt lgkmcnt(4)\n\t" \
    "v_mfma_f32_16x16x32_bf16 v[12:15], v[92:95], v[40:43], v[12:15]\n\t" \
    "v_mfma_f32_16x16x32_bf16 v[0:3], v[88:91], v[44:47], v[0:3]\n\t" \
    "ds_read_b128 v[36:39], %[xp] offset:12288\n\t" \
    "v_mfma_f32_16x16x32_bf16 v[4:7], v[96:99], v[44:47], v[4:7]\n\t" \
    "ds_read_b128 v[40:43], %[xp] offset:13312\n\t" \
    "v_mfma_f32_16x16x32_bf16 v[8:11], v[88:91], v[48:51], v[8:11]\n\t" \
    "s_waitcnt lgkmcnt(4)\n\t" \
    "v_mfma_f32_16x16x32_bf16 v[12:15], v[96:99], v[48:51], v[12:15]\n\t" \
    "v_mfma_f32_16x16x32_bf16 v[0:3], v[100:103], v[20:23], v[0:3]\n\t" \
    "ds_read_b128 v[44:47], %[xp] offset:14336\n\t" \
    "v_mfma_f32_16x16x32_bf16 v[4:7], v[108:111], v[20:23], v[4:7]\n\t" \
    "ds_read_b128 v[48:51], %[xp] offset:15360\n\t" \
    "v_mfma_f32_16x16x32_bf16 v[8:11], v[100:103], v[24:27], v[8:11]\n\t" \
    "s_waitcnt lgkmcnt(4)\n\t" \
    "v_mfma_f32_16x16x32_bf16 v[12:15], v[108:111], v[24:27], v[12:15]\n\t" \
    "s_waitcnt vmcnt(8)\n\t" \
    "s_barrier\n\t" \
    "v_mfma_f32_16x16x32_bf16 v[0:3], v[104:107], v[28:31], v[0:3]\n\t" \
    "ds_read_b128 v[20:23], %[xp] offset:16384\n\t" \
    "v_mfma_f32_16x16x32_bf16 v[4:7], v[112:115], v[28:31], v[4:7]\n\t" \
    "ds_read_b128 v[24:27], %[xp] offset:17408\n\t" \
    "v_mfma_f32_16x16x32_bf16 v[8:11], v[104:107], v[32:35], v[8:11]\n\t" \
    "s_waitcnt lgkmcnt(4)\n\t" \
    "v_mfma_f32_16x16x32_bf16 v[12:15], v[112:115], v[32:35], v[12:15]\n\t" \
    "v_mfma_f32_16x16x32_bf16 v[0:3], v[116:119], v[36:39], v[0:3]\n\t" \
    "ds_read_b128 v[28:31], %[xp] offset:18432\n\t" \
    "v_mfma_f32_16x16x32_bf16 v[4:7], v[124:127], v[36:39], v[4:7]\n\t" \
    "ds_read_b128 v[32:35], %[xp] offset:19456\n\t" \
    "v_mfma_f32_16x16x32_bf16 v[8:11], v[116:119], v[40:43], v[8:11]\n\t" \
    "s_waitcnt lgkmcnt(4)\n\t" \
    "v_mfma_f32_16x16x32_bf16 v[12:15], v[124:127], v[40:43], v[12:15]\n\t" \
    "v_mfma_f32_16x16x32_bf16 v[0:3], v[120:123], v[44:47], v[0:3]\n\t" \
    "ds_read_b128 v[36:39], %[xp] offset:20480\n\t" \
    "v_mfma_f32_16x16x32_bf16 v[4:7], v[128:131], v[44:47], v[4:7]\n\t" \
    "ds_read_b128 v[40:43], %[xp] offset:21504\n\t" \
    "v_mfma_f32_16x16x32_bf16 v[8:11], v[120:123], v[48:51], v[8:11]\n\t" \
    "s_waitcnt lgkmcnt(4)\n\t" \
    "v_mfma_f32_16x16x32_bf16 v[12:15], v[128:131], v[48:51], v[12:15]\n\t" \
    "v_mfma_f32_16x16x32_bf16 v[0:3], v[132:135], v[20:23], v[0:3]\n\t" \
    "ds_read_b128 v[44:47], %[xp] offset:22528\n\t" \
    "v_mfma_f32_16x16x32_bf16 v[4:7], v[140:143], v[20:23], v[4:7]\n\t" \
    "ds_read_b128 v[48:51], %[xp] offset:23552\n\t" \
    "v_mfma_f32_16x16x32_bf16 v[8:11], v[132:135], v[24:27], v[8:11]\n\t" \
    "s_waitcnt lgkmcnt(4)\n\t" \
    "v_mfma_f32_16x16x32_bf16 v[12:15], v[140:143], v[24:27], v[12:15]\n\t" \
    "v_mfma_f32_16x16x32_bf16 v[0:3], v[136:139], v[28:31], v[0:3]\n\t" \
    "ds_read_b128 v[20:23], %[xp] offset:24576\n\t" \
    "v_mfma_f32_16x16x32_bf16 v[4:7], v[144:147], v[28:31], v[4:7]\n\t" \
    "ds_read_b128 v[24:27], %[xp] offset:25600\n\t" \
    "v_mfma_f32_16x16x32_bf16 v[8:11], v[136:139], v[32:35], v[8:11]\n\t" \
    "s_waitcnt lgkmcnt(4)\n\t" \
    "v_mfma_f32_16x16x32_bf16 v[12:15], v[144:147], v[32:35], v[12:15]\n\t" \
    "v_mfma_f32_16x16x32_bf16 v[0:3], v[148:151], v[36:39], v[0:3]\n\t" \
    "ds_read_b128 v[28:31], %[xp] offset:26624\n\t" \
    "v_mfma_f32_16x16x32_bf16 v[4:7], v[156:159], v[36:39], v[4:7]\n\t" \
    "ds_read_b128 v[32:35], %[xp] offset:27648\n\t" \
    "v_mfma_f32_16x16x32_bf16 v[8:11], v[148:151], v[40:43], v[8:11]\n\t" \
    "s_waitcnt lgkmcnt(4)\n\t" \
    "v_mfma_f32_16x16x32_bf16 v[12:15], v[156:159], v[40:43], v[12:15]\n\t" \
    "v_mfma_f32_16x16x32_bf16 v[0:3], v[152:155], v[44:47], v[0:3]\n\t" \
    "ds_read_b128 v[36:39], %[xp] offset:28672\n\t" \
    "v_mfma_f32_16x16x32_bf16 v[4:7], v[160:163], v[44:47], v[4:7]\n\t" \
    "ds_read_b128 v[40:43], %[xp] offset:29696\n\t" \
    "v_mfma_f32_16x16x32_bf16 v[8:11], v[152:155], v[48:51], v[8:11]\n\t" \
    "s_waitcnt lgkmcnt(4)\n\t" \
    "v_mfma_f32_16x16x32_bf16 v[12:15], v[160:163], v[48:51], v[12:15]\n\t" \
    "v_mfma_f32_16x16x32_bf16 v[0:3], v[164:167], v[20:23], v[0:3]\n\t" \
    "ds_read_b128 v[44:47], %[xp] offset:30720\n\t" \
    "v_mfma_f32_16x16x32_bf16 v[4:7], v[172:175], v[20:23], v[4:7]\n\t" \
    "ds_read_b128 v[48:51], %[xp] offset:31744\n\t" \
    "v_mfma_f32_16x16x32_bf16 v[8:11], v[164:167], v[24:27], v[8:11]\n\t" \
    "s_waitcnt lgkmcnt(4)\n\t" \
    "v_mfma_f32_16x16x32_bf16 v[12:15], v[172:175], v[24:27], v[12:15]\n\t" \
    "v_mfma_f32_16x16x32_bf16 v[0:3], v[168:171], v[28:31], v[0:3]\n\t" \
    "v_mfma_f32_16x16x32_bf16 v[4:7], v[176:179], v[28:31], v[4:7]\n\t" \
    "v_mfma_f32_16x16x32_bf16 v[8:11], v[168:171], v[32:35], v[8:11]\n\t" \
    "s_waitcnt lgkmcnt(2)\n\t" \
    "v_mfma_f32_16x16x32_bf16 v[12:15], v[176:179], v[32:35], v[12:15]\n\t" \
    "v_mfma_f32_16x16x32_bf16 v[0:3], v[180:183], v[36:39], v[0:3]\n\t" \
    "v_mfma_f32_16x16x32_bf16 v[4:7], v[188:191], v[36:39], v[4:7]\n\t" \
    "v_mfma_f32_16x16x32_bf16 v[8:11], v[180:183], v[40:43], v[8:11]\n\t" \
    "s_waitcnt lgkmcnt(0)\n\t" \
    "v_mfma_f32_16x16x32_bf16 v[12:15], v[188:191], v[40:43], v[12:15]\n\t" \
    "v_mfma_f32_16x16x32_bf16 v[0:3], v[184:187], v[44:47], v[0:3]\n\t" \
    "v_mfma_f32_16x16x32_bf16 v[4:7], v[192:195], v[44:47], v[4:7]\n\t" \
    "v_mfma_f32_16x16x32_bf16 v[8:11], v[184:187], v[48:51], v[8:11]\n\t" \
    "v_mfma_f32_16x16x32_bf16 v[12:15], v[192:195], v[48:51], v[12:15]\n\t" \
    "s_nop 7\n\t" \
    : "=&{v[0:3]}"(a[0]), "=&{v[4:7]}"(a[1]), "=&{v[8:11]}"(a[2]), "=&{v[12:15]}"(a[3]) \
    : [xp] "v"(XP_), "{v[68:75]}"(w[0][0]), "{v[76:83]}"(w[0][1]), "{v[84:91]}"(w[1][0]), "{v[92:99]}"(w[1][1]), "{v[100:107]}"(w[2][0]), "{v[108:115]}"(w[2][1]), "{v[116:123]}"(w[3][0]), "{v[124:131]}"(w[3][1]), "{v[132:139]}"(w[4][0]), "{v[140:147]}"(w[4][1]), "{v[148:155]}"(w[5][0]), "{v[156:163]}"(w[5][1]), "{v[164:171]}"(w[6][0]), "{v[172:179]}"(w[6][1]), "{v[180:187]}"(w[7][0]), "{v[188:195]}"(w[7][1]) \
    : "memory", "v20", "v21", "v22", "v23", "v24", "v25", "v26", "v27", "v28", "v29", "v30", "v31", "v32", "v33", "v34", "v35", "v36", "v37", "v38", "v39", "v40", "v41", "v42", "v43", "v44", "v45", "v46", "v47", "v48", "v49", "v50", "v51")

#define ONE_V(XP_) \
  asm volatile( \
    "ds_read_b128 v[20:23], %[xp] offset:0\n\t" \
    "ds_read_b128 v[24:27], %[xp] offset:1024\n\t" \
    "ds_read_b128 v[28:31], %[xp] offset:2048\n\t" \
    "ds_read_b128 v[32:35], %[xp] offset:3072\n\t" \
    "ds_read_b128 v[36:39], %[xp] offset:4096\n\t" \
    "ds_read_b128 v[40:43], %[xp] offset:5120\n\t" \
    "s_waitcnt lgkmcnt(4)\n\t" \
    "v_mfma_f32_16x16x32_bf16 v[0:3], v[20:23], v[68:71], 0\n\t" \
    "ds_read_b128 v[44:47], %[xp] offset:6144\n\t" \
    "v_mfma_f32_16x16x32_bf16 v[4:7], v[20:23], v[76:79], 0\n\t" \
    "ds_read_b128 v[48:51], %[xp] offset:7168\n\t" \
    "v_mfma_f32_16x16x32_bf16 v[8:11], v[24:27], v[68:71], 0\n\t" \
    "s_waitcnt lgkmcnt(4)\n\t" \
    "v_mfma_f32_16x16x32_bf16 v[12:15], v[24:27], v[76:79], 0\n\t" \
    "v_mfma_f32_16x16x32_bf16 v[0:3], v[28:31], v[72:75], v[0:3]\n\t" \
    "ds_read_b128 v[20:23], %[xp] offset:8192\n\t" \
    "v_mfma_f32_16x16x32_bf16 v[4:7], v[28:31], v[80:83], v[4:7]\n\t" \
    "ds_read_b128 v[24:27], %[xp] offset:9216\n\t" \
    "v_mfma_f32_16x16x32_bf16 v[8:11], v[32:35], v[72:75], v[8:11]\n\t" \
    "s_waitcnt lgkmcnt(4)\n\t" \
    "v_mfma_f32_16x16x32_bf16 v[12:15], v[32:35], v[80:83], v[12:15]\n\t" \
    "v_mfma_f32_16x16x32_bf16 v[0:3], v[36:39], v[84:87], v[0:3]\n\t" \
    "ds_read_b128 v[28:31], %[xp] offset:10240\n\t" \
    "v_mfma_f32_16x16x32_bf16 v[4:7], v[36:39], v[92:95], v[4:7]\n\t" \
    "ds_read_b128 v[32:35], %[xp] offset:11264\n\t" \
    "v_mfma_f32_16x16x32_bf16 v[8:11], v[40:43], v[84:87], v[8:11]\n\t" \
    "s_waitcnt lgkmcnt(4)\n\t" \
    "v_mfma_f32_16x16x32_bf16 v[12:15], v[40:43], v[92:95], v[12:15]\n\t" \
    "v_mfma_f32_16x16x32_bf16 v[0:3], v[44:47], v[88:91], v[0:3]\n\t" \
    "ds_read_b128 v[36:39], %[xp] offset:12288\n\t" \
    "v_mfma_f32_16x16x32_bf16 v[4:7], v[44:47], v[96:99], v[4:7]\n\t" \
    "ds_read_b128 v[40:43], %[xp] offset:13312\n\t" \
    "v_mfma_f32_16x16x32_bf16 v[8:11], v[48:51], v[88:91], v[8:11]\n\t" \
    "s_waitcnt lgkmcnt(4)\n\t" \
    "v_mfma_f32_16x16x32_bf16 v[12:15], v[48:51], v[96:99], v[12:15]\n\t" \
    "v_mfma_f32_16x16x32_bf16 v[0:3], v[20:23], v[100:103], v[0:3]\n\t" \
    "ds_read_b128 v[44:47], %[xp] offset:14336\n\t" \
    "v_mfma_f32_16x16x32_bf16 v[4:7], v[20:23], v[108:111], v[4:7]\n\t" \
    "ds_read_b128 v[48:51], %[xp] offset:15360\n\t" \
    "v_mfma_f32_16x16x32_bf16 v[8:11], v[24:27], v[100:103], v[8:11]\n\t" \
    "s_waitcnt lgkmcnt(4)\n\t" \
    "v_mfma_f32_16x16x32_bf16 v[12:15], v[24:27], v[108:111], v[12:15]\n\t" \
    "s_waitcnt vmcnt(8)\n\t" \
    "s_barrier\n\t" \
    "v_mfma_f32_16x16x32_bf16 v[0:3], v[28:31], v[104:107], v[0:3]\n\t" \
    "ds_read_b128 v[20:23], %[xp] offset:16384\n\t" \
    "v_mfma_f32_16x16x32_bf16 v[4:7], v[28:31], v[112:115], v[4:7]\n\t" \
    "ds_read_b128 v[24:27], %[xp] offset:17408\n\t" \
    "v_mfma_f32_16x16x32_bf16 v[8:11], v[32:35], v[104:107], v[8:11]\n\t" \
    "s_waitcnt lgkmcnt(4)\n\t" \
    "v_mfma_f32_16x16x32_bf16 v[12:15], v[32:35], v[112:115], v[12:15]\n\t" \
    "v_mfma_f32_16x16x32_bf16 v[0:3], v[36:39], v[116:119], v[0:3]\n\t" \
    "ds_read_b128 v[28:31], %[xp] offset:18432\n\t" \
    "v_mfma_f32_16x16x32_bf16 v[4:7], v[36:39], v[124:127], v[4:7]\n\t" \
    "ds_read_b128 v[32:35], %[xp] offset:19456\n\t" \
    "v_mfma_f32_16x16x32_bf16 v[8:11], v[40:43], v[116:119], v[8:11]\n\t" \
    "s_waitcnt lgkmcnt(4)\n\t" \
    "v_mfma_f32_16x16x32_bf16 v[12:15], v[40:43], v[124:127], v[12:15]\n\t" \
    "v_mfma_f32_16x16x32_bf16 v[0:3], v[44:47], v[120:123], v[0:3]\n\t" \
    "ds_read_b128 v[36:39], %[xp] offset:20480\n\t" \
    "v_mfma_f32_16x16x32_bf16 v[4:7], v[44:47], v[128:131], v[4:7]\n\t" \
    "ds_read_b128 v[40:43], %[xp] offset:21504\n\t" \
    "v_mfma_f32_16x16x32_bf16 v[8:11], v[48:51], v[120:123], v[8:11]\n\t" \
    "s_waitcnt lgkmcnt(4)\n\t" \
    "v_mfma_f32_16x16x32_bf16 v[12:15], v[48:51], v[128:131], v[12:15]\n\t" \
    "v_mfma_f32_16x16x32_bf16 v[0:3], v[20:23], v[132:135], v[0:3]\n\t" \
    "ds_read_b128 v[44:47], %[xp] offset:22528\n\t" \
    "v_mfma_f32_16x16x32_bf16 v[4:7], v[20:23], v[140:143], v[4:7]\n\t" \
    "ds_read_b128 v[48:51], %[xp] offset:23552\n\t" \
    "v_mfma_f32_16x16x32_bf16 v[8:11], v[24:27], v[132:135], v[8:11]\n\t" \
    "s_waitcnt lgkmcnt(4)\n\t" \
    "v_mfma_f32_16x16x32_bf16 v[12:15], v[24:27], v[140:143], v[12:15]\n\t" \
    "v_mfma_f32_16x16x32_bf16 v[0:3], v[28:31], v[136:139], v[0:3]\n\t" \
    "ds_read_b128 v[20:23], %[xp] offset:24576\n\t" \
    "v_mfma_f32_16x16x32_bf16 v[4:7], v[28:31], v[144:147], v[4:7]\n\t" \
    "ds_read_b128 v[24:27], %[xp] offset:25600\n\t" \
    "v_mfma_f32_16x16x32_bf16 v[8:11], v[32:35], v[136:139], v[8:11]\n\t" \
    "s_waitcnt lgkmcnt(4)\n\t" \
    "v_mfma_f32_16x16x32_bf16 v[12:15], v[32:35], v[144:147], v[12:15]\n\t" \
    "v_mfma_f32_16x16x32_bf16 v[0:3], v[36:39], v[148:151], v[0:3]\n\t" \
    "ds_read_b128 v[28:31], %[xp] offset:26624\n\t" \
    "v_mfma_f32_16x16x32_bf16 v[4:7], v[36:39], v[156:159], v[4:7]\n\t" \
    "ds_read_b128 v[32:35], %[xp] offset:27648\n\t" \
    "v_mfma_f32_16x16x32_bf16 v[8:11], v[40:43], v[148:151], v[8:11]\n\t" \
    "s_waitcnt lgkmcnt(4)\n\t" \
    "v_mfma_f32_16x16x32_bf16 v[12:15], v[40:43], v[156:159], v[12:15]\n\t" \
    "v_mfma_f32_16x16x32_bf16 v[0:3], v[44:47], v[152:155], v[0:3]\n\t" \
    "ds_read_b128 v[36:39], %[xp] offset:28672\n\t" \
    "v_mfma_f32_16x16x32_bf16 v[4:7], v[44:47], v[160:163], v[4:7]\n\t" \
    "ds_read_b128 v[40:43], %[xp] offset:29696\n\t" \
    "v_mfma_f32_16x16x32_bf16 v[8:11], v[48:51], v[152:155], v[8:11]\n\t" \
    "s_waitcnt lgkmcnt(4)\n\t" \
    "v_mfma_f32_16x16x32_bf16 v[12:15], v[48:51], v[160:163], v[12:15]\n\t" \
    "v_mfma_f32_16x16x32_bf16 v[0:3], v[20:23], v[164:167], v[0:3]\n\t" \
    "ds_read_b128 v[44:47], %[xp] offset:30720\n\t" \
    "v_mfma_f32_16x16x32_bf16 v[4:7], v[20:23], v[172:175], v[4:7]\n\t" \
    "ds_read_b128 v[48:51], %[xp] offset:31744\n\t" \
    "v_mfma_f32_16x16x32_bf16 v[8:11], v[24:27], v[164:167], v[8:11]\n\t" \
    "s_waitcnt lgkmcnt(4)\n\t" \
    "v_mfma_f32_16x16x32_bf16 v[12:15], v[24:27], v[172:175], v[12:15]\n\t" \
    "v_mfma_f32_16x16x32_bf16 v[0:3], v[28:31], v[168:171], v[0:3]\n\t" \
    "v_mfma_f32_16x16x32_bf16 v[4:7], v[28:31], v[176:179], v[4:7]\n\t" \
    "v_mfma_f32_16x16x32_bf16 v[8:11], v[32:35], v[168:171], v[8:11]\n\t" \
    "s_waitcnt lgkmcnt(2)\n\t" \
    "v_mfma_f32_16x16x32_bf16 v[12:15], v[32:35], v[176:179], v[12:15]\n\t" \
    "v_mfma_f32_16x16x32_bf16 v[0:3], v[36:39], v[180:183], v[0:3]\n\t" \
    "v_mfma_f32_16x16x32_bf16 v[4:7], v[36:39], v[188:191], v[4:7]\n\t" \
    "v_mfma_f32_16x16x32_bf16 v[8:11], v[40:43], v[180:183], v[8:11]\n\t" \
    "s_waitcnt lgkmcnt(0)\n\t" \
    "v_mfma_f32_16x16x32_bf16 v[12:15], v[40:43], v[188:191], v[12:15]\n\t" \
    "v_mfma_f32_16x16x32_bf16 v[0:3], v[44:47], v[184:187], v[0:3]\n\t" \
    "v_mfma_f32_16x16x32_bf16 v[4:7], v[44:47], v[192:195], v[4:7]\n\t" \
    "v_mfma_f32_16x16x32_bf16 v[8:11], v[48:51], v[184:187], v[8:11]\n\t" \
    "v_mfma_f32_16x16x32_bf16 v[12:15], v[48:51], v[192:195], v[12:15]\n\t" \
    "s_nop 7\n\t" \
    : "=&{v[0:3]}"(a[0]), "=&{v[4:7]}"(a[1]), "=&{v[8:11]}"(a[2]), "=&{v[12:15]}"(a[3]) \
    : [xp] "v"(XP_), "{v[68:75]}"(w[0][0]), "{v[76:83]}"(w[0][1]), "{v[84:91]}"(w[1][0]), "{v[92:99]}"(w[1][1]), "{v[100:107]}"(w[2][0]), "{v[108:115]}"(w[2][1]), "{v[116:123]}"(w[3][0]), "{v[124:131]}"(w[3][1]), "{v[132:139]}"(w[4][0]), "{v[140:147]}"(w[4][1]), "{v[148:155]}"(w[5][0]), "{v[156:163]}"(w[5][1]), "{v[164:171]}"(w[6][0]), "{v[172:179]}"(w[6][1]), "{v[180:187]}"(w[7][0]), "{v[188:195]}"(w[7][1]) \
    : "memory", "v20", "v21", "v22", "v23", "v24", "v25", "v26", "v27", "v28", "v29", "v30", "v31", "v32", "v33", "v34", "v35", "v36", "v37", "v38", "v39", "v40", "v41", "v42", "v43", "v44", "v45", "v46", "v47", "v48", "v49", "v50", "v51")

#define H_LOOP1() \
  asm volatile( \
    "v_mov_b32 v20, 0\n\t" \
    "v_mov_b32 v21, 0\n\t" \
    "v_mov_b32 v22, 0\n\t" \
    "v_mov_b32 v23, 0\n\t" \
    "v_mov_b32 v24, 0\n\t" \
    "v_mov_b32 v25, 0\n\t" \
    "v_mov_b32 v26, 0\n\t" \
    "v_mov_b32 v27, 0\n\t" \
    "v_mov_b32 v28, 0\n\t" \
    "v_mov_b32 v29, 0\n\t" \
    "v_mov_b32 v30, 0\n\t" \
    "v_mov_b32 v31, 0\n\t" \
    "v_mov_b32 v32, 0\n\t" \
    "v_mov_b32 v33, 0\n\t" \
    "v_mov_b32 v34, 0\n\t" \
    "v_mov_b32 v35, 0\n\t" \
    "v_mov_b32 v36, 0\n\t" \
    "v_mov_b32 v37, 0\n\t" \
    "v_mov_b32 v38, 0\n\t" \
    "v_mov_b32 v39, 0\n\t" \
    "v_mov_b32 v40, 0\n\t" \
    "v_mov_b32 v41, 0\n\t" \
    "v_mov_b32 v42, 0\n\t" \
    "v_mov_b32 v43, 0\n\t" \
    "v_mov_b32 v44, 0\n\t" \
    "v_mov_b32 v45, 0\n\t" \
    "v_mov_b32 v46, 0\n\t" \
    "v_mov_b32 v47, 0\n\t" \
    "v_mov_b32 v48, 0\n\t" \
    "v_mov_b32 v49, 0\n\t" \
    "v_mov_b32 v50, 0\n\t" \
    "v_mov_b32 v51, 0\n\t" \
    "v_mov_b32 v52, 0\n\t" \
    "v_mov_b32 v53, 0\n\t" \
    "v_mov_b32 v54, 0\n\t" \
    "v_mov_b32 v55, 0\n\t" \
    "v_mov_b32 v56, 0\n\t" \
    "v_mov_b32 v57, 0\n\t" \
    "v_mov_b32 v58, 0\n\t" \
    "v_mov_b32 v59, 0\n\t" \
    "v_mov_b32 v60, 0\n\t" \
    "v_mov_b32 v61, 0\n\t" \
    "v_mov_b32 v62, 0\n\t" \
    "v_mov_b32 v63, 0\n\t" \
    "v_mov_b32 v64, 0\n\t" \
    "v_mov_b32 v65, 0\n\t" \
    "v_mov_b32 v66, 0\n\t" \
    "v_mov_b32 v67, 0\n\t" \
    "v_mov_b32 v68, 0\n\t" \
    "v_mov_b32 v69, 0\n\t" \
    "v_mov_b32 v70, 0\n\t" \
    "v_mov_b32 v71, 0\n\t" \
    "v_mov_b32 v72, 0\n\t" \
    "v_mov_b32 v73, 0\n\t" \
    "v_mov_b32 v74, 0\n\t" \
    "v_mov_b32 v75, 0\n\t" \
    "v_mov_b32 v76, 0\n\t" \
    "v_mov_b32 v77, 0\n\t" \
    "v_mov_b32 v78, 0\n\t" \
    "v_mov_b32 v79, 0\n\t" \
    "v_mov_b32 v80, 0\n\t" \
    "v_mov_b32 v81, 0\n\t" \
    "v_mov_b32 v82, 0\n\t" \
    "v_mov_b32 v83, 0\n\t" \
    "s_waitcnt vmcnt(8)\n\t" \
    "s_barrier\n\t" \
    "ds_read_b64_tr_b16 v[116:117], %[b0] offset:0\n\t" \
    "ds_read_b64_tr_b16 v[118:119], %[b0] offset:1024\n\t" \
    "ds_read_b64_tr_b16 v[124:125], %[b1] offset:0\n\t" \
    "ds_read_b64_tr_b16 v[126:127], %[b1] offset:1024\n\t" \
    "ds_read_b64_tr_b16 v[120:121], %[b0] offset:8192\n\t" \
    "ds_read_b64_tr_b16 v[122:123], %[b0] offset:9216\n\t" \
    "ds_read_b64_tr_b16 v[128:129], %[b1] offset:8192\n\t" \
    "ds_read_b64_tr_b16 v[130:131], %[b1] offset:9216\n\t" \
    "ds_read_b128 v[84:87], %[a] offset:0\n\t" \
    "ds_read_b128 v[88:91], %[a] offset:1024\n\t" \
    "ds_read_b128 v[92:95], %[a] offset:2048\n\t" \
    "ds_read_b128 v[96:99], %[a] offset:3072\n\t" \
    "ds_read_b128 v[100:103], %[a] offset:4096\n\t" \
    "ds_read_b128 v[104:107], %[a] offset:5120\n\t" \
    "ds_read_b128 v[108:111], %[a] offset:6144\n\t" \
    "ds_read_b128 v[112:115], %[a] offset:7168\n\t" \
    "L_%=:\n\t" \
    "s_add_u32 %[d], %[lw], %[c]\n\t" \
    "s_add_u32 %[y], %[c], 49152\n\t" \
    "s_cmp_eq_u32 %[y], 147456\n\t" \
    "s_cselect_b32 %[y], 0, %[y]\n\t" \
    "s_sub_u32 %[x], %[y], %[c]\n\t" \
    "s_mov_b32 %[c], %[y]\n\t" \
    "v_add_u32 %[a], %[x], %[a]\n\t" \
    "v_add_u32 %[b0], %[x], %[b0]\n\t" \
    "v_add_u32 %[b1], %[x], %[b1]\n\t" \
    "s_add_u32 %[x], %[t], 3\n\t" \
    "s_cmp_lt_u32 %[x], %[nt]\n\t" \
    "s_cselect_b32 %[x], %[x], 0\n\t" \
    "s_mul_i32 %[y], %[x], %[kb]\n\t" \
    "s_add_u32 %[y], %[y], %[cb]\n\t" \
    "s_lshl_b32 %[x], %[x], 11\n\t" \
    "s_add_u32 %[x], %[x], %[ca]\n\t" \
    "s_waitcnt vmcnt(4) lgkmcnt(0)\n\t" \
    "s_barrier\n\t" \
    "v_mfma_f32_16x16x32_bf16 v[20:23], v[116:119], v[84:87], v[20:23]\n\t" \
    "ds_read_b64_tr_b16 v[180:181], %[b0] offset:0\n\t" \
    "ds_read_b64_tr_b16 v[182:183], %[b0] offset:1024\n\t" \
    "s_add_u32 m0, %[d], 0\n\t" \
    "v_mfma_f32_16x16x32_bf16 v[24:27], v[124:127], v[84:87], v[24:27]\n\t" \
    "buffer_load_dwordx4 %[vb0], %[rb], %[y] offen lds\n\t" \
    "v_mfma_f32_16x16x32_bf16 v[20:23], v[120:123], v[88:91], v[20:23]\n\t" \
    "ds_read_b64_tr_b16 v[188:189], %[b1] offset:0\n\t" \
    "ds_read_b64_tr_b16 v[190:191], %[b1] offset:1024\n\t" \
    "v_mfma_f32_16x16x32_bf16 v[24:27], v[128:131], v[88:91], v[24:27]\n\t" \
    "ds_read_b64_tr_b16 v[184:185], %[b0] offset:8192\n\t" \
    "ds_read_b64_tr_b16 v[186:187], %[b0] offset:9216\n\t" \
    "v_mfma_f32_16x16x32_bf16 v[28:31], v[116:119], v[92:95], v[28:31]\n\t" \
    "ds_read_b64_tr_b16 v[192:193], %[b1] offset:8192\n\t" \
    "ds_read_b64_tr_b16 v[194:195], %[b1] offset:9216\n\t" \
    "s_add_u32 m0, %[d], 8192\n\t" \
    "v_mfma_f32_16x16x32_bf16 v[32:35], v[124:127], v[92:95], v[32:35]\n\t" \
    "buffer_load_dwordx4 %[vb1], %[rb], %[y] offen lds\n\t" \
    "v_mfma_f32_16x16x32_bf16 v[28:31], v[120:123], v[96:99], v[28:31]\n\t" \
    "ds_read_b128 v[148:151], %[a] offset:0\n\t" \
    "v_mfma_f32_16x16x32_bf16 v[32:35], v[128:131], v[96:99], v[32:35]\n\t" \
    "ds_read_b128 v[152:155], %[a] offset:1024\n\t" \
    "v_mfma_f32_16x16x32_bf16 v[36:39], v[116:119], v[100:103], v[36:39]\n\t" \
    "ds_read_b128 v[156:159], %[a] offset:2048\n\t" \
    "s_add_u32 m0, %[d], 32768\n\t" \
    "v_mfma_f32_16x16x32_bf16 v[40:43], v[124:127], v[100:103], v[40:43]\n\t" \
    "buffer_load_dwordx4 %[va0], %[ra], %[x] offen sc1 lds\n\t" \
    "v_mfma_f32_16x16x32_bf16 v[36:39], v[120:123], v[104:107], v[36:39]\n\t" \
    "ds_read_b128 v[160:163], %[a] offset:3072\n\t" \
    "v_mfma_f32_16x16x32_bf16 v[40:43], v[128:131], v[104:107], v[40:43]\n\t" \
    "ds_read_b128 v[164:167], %[a] offset:4096\n\t" \
    "v_mfma_f32_16x16x32_bf16 v[44:47], v[116:119], v[108:111], v[44:47]\n\t" \
    "ds_read_b128 v[168:171], %[a] offset:5120\n\t" \
    "s_add_u32 m0, %[d], 40960\n\t" \
    "v_mfma_f32_16x16x32_bf16 v[48:51], v[124:127], v[108:111], v[48:51]\n\t" \
    "buffer_load_dwordx4 %[va1], %[ra], %[x] offen sc1 lds\n\t" \
    "v_mfma_f32_16x16x32_bf16 v[44:47], v[120:123], v[112:115], v[44:47]\n\t" \
    "ds_read_b128 v[172:175], %[a] offset:6144\n\t" \
    "v_mfma_f32_16x16x32_bf16 v[48:51], v[128:131], v[112:115], v[48:51]\n\t" \
    "ds_read_b128 v[176:179], %[a] offset:7168\n\t" \
    "s_mov_b32 m0, %[pd]\n\t" \
    "s_cmp_lt_u32 %[ps], %[pe]\n\t" \
    "s_cselect_b64 exec, -1, 0\n\t" \
    "buffer_load_dword %[pv], %[rp], %[ps] offen sc1 lds\n\t" \
    "s_mov_b64 exec, -1\n\t" \
    "s_add_u32 %[ps], %[ps], %[pk]\n\t" \
    "s_add_u32 %[t], %[t], 1\n\t" \
    "s_add_u32 %[d], %[lw], %[c]\n\t" \
    "s_add_u32 %[y], %[c], 49152\n\t" \
    "s_cmp_eq_u32 %[y], 147456\n\t" \
    "s_cselect_b32 %[y], 0, %[y]\n\t" \
    "s_sub_u32 %[x], %[y], %[c]\n\t" \
    "s_mov_b32 %[c], %[y]\n\t" \
    "v_add_u32 %[a], %[x], %[a]\n\t" \
    "v_add_u32 %[b0], %[x], %[b0]\n\t" \
    "v_add_u32 %[b1], %[x], %[b1]\n\t" \
    "s_add_u32 %[x], %[t], 3\n\t" \
    "s_cmp_lt_u32 %[x], %[nt]\n\t" \
    "s_cselect_b32 %[x], %[x], 0\n\t" \
    "s_mul_i32 %[y], %[x], %[kb]\n\t" \
    "s_add_u32 %[y], %[y], %[cb]\n\t" \
    "s_lshl_b32 %[x], %[x], 11\n\t" \
    "s_add_u32 %[x], %[x], %[ca]\n\t" \
    "s_waitcnt vmcnt(4) lgkmcnt(0)\n\t" \
    "s_barrier\n\t" \
    "v_mfma_f32_16x16x32_bf16 v[20:23], v[180:183], v[148:151], v[20:23]\n\t" \
    "ds_read_b64_tr_b16 v[116:117], %[b0] offset:0\n\t" \
    "ds_read_b64_tr_b16 v[118:119], %[b0] offset:1024\n\t" \
    "s_add_u32 m0, %[d], 0\n\t" \
    "v_mfma_f32_16x16x32_bf16 v[24:27], v[188:191], v[148:151], v[24:27]\n\t" \
    "buffer_load_dwordx4 %[vb0], %[rb], %[y] offen lds\n\t" \
    "v_mfma_f32_16x16x32_bf16 v[20:23], v[184:187], v[152:155], v[20:23]\n\t" \
    "ds_read_b64_tr_b16 v[124:125], %[b1] offset:0\n\t" \
    "ds_read_b64_tr_b16 v[126:127], %[b1] offset:1024\n\t" \
    "v_mfma_f32_16x16x32_bf16 v[24:27], v[192:195], v[152:155], v[24:27]\n\t" \
    "ds_read_b64_tr_b16 v[120:121], %[b0] offset:8192\n\t" \
    "ds_read_b64_tr_b16 v[122:123], %[b0] offset:9216\n\t" \
    "v_mfma_f32_16x16x32_bf16 v[28:31], v[180:183], v[156:159], v[28:31]\n\t" \
    "ds_read_b64_tr_b16 v[128:129], %[b1] offset:8192\n\t" \
    "ds_read_b64_tr_b16 v[130:131], %[b1] offset:9216\n\t" \
    "s_add_u32 m0, %[d], 8192\n\t" \
    "v_mfma_f32_16x16x32_bf16 v[32:35], v[188:191], v[156:159], v[32:35]\n\t" \
    "buffer_load_dwordx4 %[vb1], %[rb], %[y] offen lds\n\t" \
    "v_mfma_f32_16x16x32_bf16 v[28:31], v[184:187], v[160:163], v[28:31]\n\t" \
    "ds_read_b128 v[84:87], %[a] offset:0\n\t" \
    "v_mfma_f32_16x16x32_bf16 v[32:35], v[192:195], v[160:163], v[32:35]\n\t" \
    "ds_read_b128 v[88:91], %[a] offset:1024\n\t" \
    "v_mfma_f32_16x16x32_bf16 v[36:39], v[180:183], v[164:167], v[36:39]\n\t" \
    "ds_read_b128 v[92:95], %[a] offset:2048\n\t" \
    "s_add_u32 m0, %[d], 32768\n\t" \
    "v_mfma_f32_16x16x32_bf16 v[40:43], v[188:191], v[164:167], v[40:43]\n\t" \
    "buffer_load_dwordx4 %[va0], %[ra], %[x] offen sc1 lds\n\t" \
    "v_mfma_f32_16x16x32_bf16 v[36:39], v[184:187], v[168:171], v[36:39]\n\t" \
    "ds_read_b128 v[96:99], %[a] offset:3072\n\t" \
    "v_mfma_f32_16x16x32_bf16 v[40:43], v[192:195], v[168:171], v[40:43]\n\t" \
    "ds_read_b128 v[100:103], %[a] offset:4096\n\t" \
    "v_mfma_f32_16x16x32_bf16 v[44:47], v[180:183], v[172:175], v[44:47]\n\t" \
    "ds_read_b128 v[104:107], %[a] offset:5120\n\t" \
    "s_add_u32 m0, %[d], 40960\n\t" \
    "v_mfma_f32_16x16x32_bf16 v[48:51], v[188:191], v[172:175], v[48:51]\n\t" \
    "buffer_load_dwordx4 %[va1], %[ra], %[x] offen sc1 lds\n\t" \
    "v_mfma_f32_16x16x32_bf16 v[44:47], v[184:187], v[176:179], v[44:47]\n\t" \
    "ds_read_b128 v[108:111], %[a] offset:6144\n\t" \
    "v_mfma_f32_16x16x32_bf16 v[48:51], v[192:195], v[176:179], v[48:51]\n\t" \
    "ds_read_b128 v[112:115], %[a] offset:7168\n\t" \
    "s_mov_b32 m0, %[pd]\n\t" \
    "s_cmp_lt_u32 %[ps], %[pe]\n\t" \
    "s_cselect_b64 exec, -1, 0\n\t" \
    "buffer_load_dword %[pv], %[rp], %[ps] offen sc1 lds\n\t" \
    "s_mov_b64 exec, -1\n\t" \
    "s_add_u32 %[ps], %[ps], %[pk]\n\t" \
    "s_add_u32 %[t], %[t], 1\n\t" \
    "s_cmp_lt_u32 %[t], %[nt]\n\t" \
    "s_cbranch_scc1 L_%=\n\t" \
    "s_waitcnt lgkmcnt(0)\n\t" \
    "s_nop 7\n\t" \
    : "=&{v[20:23]}"(acc[0][0][0][0]), "=&{v[24:27]}"(acc[0][0][0][1]), "=&{v[28:31]}"(acc[0][0][1][0]), "=&{v[32:35]}"(acc[0][0][1][1]), "=&{v[36:39]}"(acc[0][0][2][0]), "=&{v[40:43]}"(acc[0][0][2][1]), "=&{v[44:47]}"(acc[0][0][3][0]), "=&{v[48:51]}"(acc[0][0][3][1]), "=&{v[52:55]}"(acc[0][1][0][0]), "=&{v[56:59]}"(acc[0][1][0][1]), "=&{v[60:63]}"(acc[0][1][1][0]), "=&{v[64:67]}"(acc[0][1][1][1]), "=&{v[68:71]}"(acc[0][1][2][0]), "=&{v[72:75]}"(acc[0][1][2][1]), "=&{v[76:79]}"(acc[0][1][3][0]), "=&{v[80:83]}"(acc[0][1][3][1]), [a] "+v"(aoff), [b0] "+v"(boff0), [b1] "+v"(boff1), [c] "+s"(cur), [t] "+s"(t), [ps] "+s"(ps), [d] "=&s"(sd), [x] "=&s"(sx), [y] "=&s"(sy) \
    : [va0] "v"(voffA[0]), [va1] "v"(voffA[1]), [vb0] "v"(voffB[0]), [vb1] "v"(voffB[1]), [pv] "v"(pf.v), [ra] "s"(ra), [rb] "s"(rb), [rp] "s"(rp), [lw] "s"(ldsw), [nt] "s"(nt), [kb] "s"(kB), [ca] "s"(cA), [cb] "s"(cB), [pd] "s"(pd), [pe] "s"(pe), [pk] "s"(pf.step) \
    : "memory", "scc", "v84", "v85", "v86", "v87", "v88", "v89", "v90", "v91", "v92", "v93", "v94", "v95", "v96", "v97", "v98", "v99", "v100", "v101", "v102", "v103", "v104", "v105", "v106", "v107", "v108", "v109", "v110", "v111", "v112", "v113", "v114", "v115", "v116", "v117", "v118", "v119", "v120", "v121", "v122", "v123", "v124", "v125", "v126", "v127", "v128", "v129", "v130", "v131", "v132", "v133", "v134", "v135", "v136", "v137", "v138", "v139", "v140", "v141", "v142", "v143", "v144", "v145", "v146", "v147", "v148", "v149", "v150", "v151", "v152", "v153", "v154", "v155", "v156", "v157", "v158", "v159", "v160", "v161", "v162", "v163", "v164", "v165", "v166", "v167", "v168", "v169", "v170", "v171", "v172", "v173", "v174", "v175", "v176", "v177", "v178", "v179", "v180", "v181", "v182", "v183", "v184", "v185", "v186", "v187", "v188", "v189", "v190", "v191", "v192", "v193", "v194", "v195", "v196", "v197", "v198", "v199", "v200", "v201", "v202", "v203", "v204", "v205", "v206", "v207", "v208", "v209", "v210", "v211")

#define H_LOOP2() \
  asm volatile( \
    "v_mov_b32 v20, 0\n\t" \
    "v_mov_b32 v21, 0\n\t" \
    "v_mov_b32 v22, 0\n\t" \
    "v_mov_b32 v23, 0\n\t" \
    "v_mov_b32 v24, 0\n\t" \
    "v_mov_b32 v25, 0\n\t" \
    "v_mov_b32 v26, 0\n\t" \
    "v_mov_b32 v27, 0\n\t" \
    "v_mov_b32 v28, 0\n\t" \
    "v_mov_b32 v29, 0\n\t" \
    "v_mov_b32 v30, 0\n\t" \
    "v_mov_b32 v31, 0\n\t" \
    "v_mov_b32 v32, 0\n\t" \
    "v_mov_b32 v33, 0\n\t" \
    "v_mov_b32 v34, 0\n\t" \
    "v_mov_b32 v35, 0\n\t" \
    "v_mov_b32 v36, 0\n\t" \
    "v_mov_b32 v37, 0\n\t" \
    "v_mov_b32 v38, 0\n\t" \
    "v_mov_b32 v39, 0\n\t" \
    "v_mov_b32 v40, 0\n\t" \
    "v_mov_b32 v41, 0\n\t" \
    "v_mov_b32 v42, 0\n\t" \
    "v_mov_b32 v43, 0\n\t" \
    "v_mov_b32 v44, 0\n\t" \
    "v_mov_b32 v45, 0\n\t" \
    "v_mov_b32 v46, 0\n\t" \
    "v_mov_b32 v47, 0\n\t" \
    "v_mov_b32 v48, 0\n\t" \
    "v_mov_b32 v49, 0\n\t" \
    "v_mov_b32 v50, 0\n\t" \
    "v_mov_b32 v51, 0\n\t" \
    "v_mov_b32 v52, 0\n\t" \
    "v_mov_b32 v53, 0\n\t" \
    "v_mov_b32 v54, 0\n\t" \
    "v_mov_b32 v55, 0\n\t" \
    "v_mov_b32 v56, 0\n\t" \
    "v_mov_b32 v57, 0\n\t" \
    "v_mov_b32 v58, 0\n\t" \
    "v_mov_b32 v59, 0\n\t" \
    "v_mov_b32 v60, 0\n\t" \
    "v_mov_b32 v61, 0\n\t" \
    "v_mov_b32 v62, 0\n\t" \
    "v_mov_b32 v63, 0\n\t" \
    "v_mov_b32 v64, 0\n\t" \
    "v_mov_b32 v65, 0\n\t" \
    "v_mov_b32 v66, 0\n\t" \
    "v_mov_b32 v67, 0\n\t" \
    "v_mov_b32 v68, 0\n\t" \
    "v_mov_b32 v69, 0\n\t" \
    "v_mov_b32 v70, 0\n\t" \
    "v_mov_b32 v71, 0\n\t" \
    "v_mov_b32 v72, 0\n\t" \
    "v_mov_b32 v73, 0\n\t" \
    "v_mov_b32 v74, 0\n\t" \
    "v_mov_b32 v75, 0\n\t" \
    "v_mov_b32 v76, 0\n\t" \
    "v_mov_b32 v77, 0\n\t" \
    "v_mov_b32 v78, 0\n\t" \
    "v_mov_b32 v79, 0\n\t" \
    "v_mov_b32 v80, 0\n\t" \
    "v_mov_b32 v81, 0\n\t" \
    "v_mov_b32 v82, 0\n\t" \
    "v_mov_b32 v83, 0\n\t" \
    "s_waitcnt vmcnt(12)\n\t" \
    "s_barrier\n\t" \
    "ds_read_b64_tr_b16 v[116:117], %[b0] offset:0\n\t" \
    "ds_read_b64_tr_b16 v[118:119], %[b0] offset:1024\n\t" \
    "ds_read_b64_tr_b16 v[124:125], %[b1] offset:0\n\t" \
    "ds_read_b64_tr_b16 v[126:127], %[b1] offset:1024\n\t" \
    "ds_read_b64_tr_b16 v[120:121], %[b0] offset:8192\n\t" \
    "ds_read_b64_tr_b16 v[122:123], %[b0] offset:9216\n\t" \
    "ds_read_b64_tr_b16 v[128:129], %[b1] offset:8192\n\t" \
    "ds_read_b64_tr_b16 v[130:131], %[b1] offset:9216\n\t" \
    "ds_read_b128 v[84:87], %[a] offset:0\n\t" \
    "ds_read_b128 v[88:91], %[a] offset:1024\n\t" \
    "ds_read_b128 v[92:95], %[a] offset:2048\n\t" \
    "ds_read_b128 v[96:99], %[a] offset:3072\n\t" \
    "ds_read_b128 v[100:103], %[a] offset:4096\n\t" \
    "ds_read_b128 v[104:107], %[a] offset:5120\n\t" \
    "ds_read_b128 v[108:111], %[a] offset:6144\n\t" \
    "ds_read_b128 v[112:115], %[a] offset:7168\n\t" \
    "ds_read_b64_tr_b16 v[132:133], %[b0] offset:16384\n\t" \
    "ds_read_b64_tr_b16 v[134:135], %[b0] offset:17408\n\t" \
    "ds_read_b64_tr_b16 v[140:141], %[b1] offset:16384\n\t" \
    "ds_read_b64_tr_b16 v[142:143], %[b1] offset:17408\n\t" \
    "ds_read_b64_tr_b16 v[136:137], %[b0] offset:24576\n\t" \
    "ds_read_b64_tr_b16 v[138:139], %[b0] offset:25600\n\t" \
    "ds_read_b64_tr_b16 v[144:145], %[b1] offset:24576\n\t" \
    "ds_read_b64_tr_b16 v[146:147], %[b1] offset:25600\n\t" \
    "L_%=:\n\t" \
    "s_add_u32 %[d], %[lw], %[c]\n\t" \
    "s_add_u32 %[y], %[c], 49152\n\t" \
    "s_cmp_eq_u32 %[y], 147456\n\t" \
    "s_cselect_b32 %[y], 0, %[y]\n\t" \
    "s_sub_u32 %[x], %[y], %[c]\n\t" \
    "s_mov_b32 %[c], %[y]\n\t" \
    "v_add_u32 %[a], %[x], %[a]\n\t" \
    "v_add_u32 %[b0], %[x], %[b0]\n\t" \
    "v_add_u32 %[b1], %[x], %[b1]\n\t" \
    "s_add_u32 %[x], %[t], 3\n\t" \
    "s_cmp_lt_u32 %[x], %[nt]\n\t" \
    "s_cselect_b32 %[x], %[x], 0\n\t" \
    "s_mul_i32 %[y], %[x], %[kb]\n\t" \
    "s_add_u32 %[y], %[y], %[cb]\n\t" \
    "s_lshl_b32 %[x], %[x], 11\n\t" \
    "s_add_u32 %[x], %[x], %[ca]\n\t" \
    "s_waitcnt vmcnt(6) lgkmcnt(0)\n\t" \
    "s_barrier\n\t" \
    "v_mfma_f32_16x16x32_bf16 v[20:23], v[116:119], v[84:87], v[20:23]\n\t" \
    "ds_read_b64_tr_b16 v[180:181], %[b0] offset:0\n\t" \
    "s_add_u32 m0, %[d], 0\n\t" \
    "v_mfma_f32_16x16x32_bf16 v[24:27], v[124:127], v[84:87], v[24:27]\n\t" \
    "buffer_load_dwordx4 %[vb0], %[rb], %[y] offen lds\n\t" \
    "v_mfma_f32_16x16x32_bf16 v[20:23], v[120:123], v[88:91], v[20:23]\n\t" \
    "ds_read_b64_tr_b16 v[182:183], %[b0] offset:1024\n\t" \
    "v_mfma_f32_16x16x32_bf16 v[24:27], v[128:131], v[88:91], v[24:27]\n\t" \
    "ds_read_b64_tr_b16 v[188:189], %[b1] offset:0\n\t" \
    "v_mfma_f32_16x16x32_bf16 v[28:31], v[116:119], v[92:95], v[28:31]\n\t" \
    "ds_read_b64_tr_b16 v[190:191], %[b1] offset:1024\n\t" \
    "v_mfma_f32_16x16x32_bf16 v[32:35], v[124:127], v[92:95], v[32:35]\n\t" \
    "ds_read_b64_tr_b16 v[184:185], %[b0] offset:8192\n\t" \
    "s_add_u32 m0, %[d], 8192\n\t" \
    "v_mfma_f32_16x16x32_bf16 v[28:31], v[120:123], v[96:99], v[28:31]\n\t" \
    "buffer_load_dwordx4 %[vb1], %[rb], %[y] offen lds\n\t" \
    "v_mfma_f32_16x16x32_bf16 v[32:35], v[128:131], v[96:99], v[32:35]\n\t" \
    "ds_read_b64_tr_b16 v[186:187], %[b0] offset:9216\n\t" \
    "v_mfma_f32_16x16x32_bf16 v[36:39], v[116:119], v[100:103], v[36:39]\n\t" \
    "ds_read_b64_tr_b16 v[192:193], %[b1] offset:8192\n\t" \
    "v_mfma_f32_16x16x32_bf16 v[40:43], v[124:127], v[100:103], v[40:43]\n\t" \
    "ds_read_b64_tr_b16 v[194:195], %[b1] offset:9216\n\t" \
    "v_mfma_f32_16x16x32_bf16 v[36:39], v[120:123], v[104:107], v[36:39]\n\t" \
    "ds_read_b128 v[148:151], %[a] offset:0\n\t" \
    "s_add_u32 m0, %[d], 32768\n\t" \
    "v_mfma_f32_16x16x32_bf16 v[40:43], v[128:131], v[104:107], v[40:43]\n\t" \
    "buffer_load_dwordx4 %[va0], %[ra], %[x] offen sc1 lds\n\t" \
    "v_mfma_f32_16x16x32_bf16 v[44:47], v[116:119], v[108:111], v[44:47]\n\t" \
    "ds_read_b128 v[152:155], %[a] offset:1024\n\t" \
    "v_mfma_f32_16x16x32_bf16 v[48:51], v[124:127], v[108:111], v[48:51]\n\t" \
    "ds_read_b128 v[156:159], %[a] offset:2048\n\t" \
    "v_mfma_f32_16x16x32_bf16 v[44:47], v[120:123], v[112:115], v[44:47]\n\t" \
    "ds_read_b128 v[160:163], %[a] offset:3072\n\t" \
    "v_mfma_f32_16x16x32_bf16 v[48:51], v[128:131], v[112:115], v[48:51]\n\t" \
    "ds_read_b128 v[164:167], %[a] offset:4096\n\t" \
    "s_add_u32 m0, %[d], 40960\n\t" \
    "v_mfma_f32_16x16x32_bf16 v[52:55], v[132:135], v[84:87], v[52:55]\n\t" \
    "buffer_load_dwordx4 %[va1], %[ra], %[x] offen sc1 lds\n\t" \
    "v_mfma_f32_16x16x32_bf16 v[56:59], v[140:143], v[84:87], v[56:59]\n\t" \
    "ds_read_b128 v[168:171], %[a] offset:5120\n\t" \
    "v_mfma_f32_16x16x32_bf16 v[52:55], v[136:139], v[88:91], v[52:55]\n\t" \
    "ds_read_b128 v[172:175], %[a] offset:6144\n\t" \
    "v_mfma_f32_16x16x32_bf16 v[56:59], v[144:147], v[88:91], v[56:59]\n\t" \
    "ds_read_b128 v[176:179], %[a] offset:7168\n\t" \
    "v_mfma_f32_16x16x32_bf16 v[60:63], v[132:135], v[92:95], v[60:63]\n\t" \
    "ds_read_b64_tr_b16 v[196:197], %[b0] offset:16384\n\t" \
    "s_add_u32 m0, %[d], 16128\n\t" \
    "v_mfma_f32_16x16x32_bf16 v[64:67], v[140:143], v[92:95], v[64:67]\n\t" \
    "buffer_load_dwordx4 %[vb0], %[rb], %[y] offen offset:256 lds\n\t" \
    "v_mfma_f32_16x16x32_bf16 v[60:63], v[136:139], v[96:99], v[60:63]\n\t" \
    "ds_read_b64_tr_b16 v[198:199], %[b0] offset:17408\n\t" \
    "v_mfma_f32_16x16x32_bf16 v[64:67], v[144:147], v[96:99], v[64:67]\n\t" \
    "ds_read_b64_tr_b16 v[204:205], %[b1] offset:16384\n\t" \
    "v_mfma_f32_16x16x32_bf16 v[68:71], v[132:135], v[100:103], v[68:71]\n\t" \
    "ds_read_b64_tr_b16 v[206:207], %[b1] offset:17408\n\t" \
    "v_mfma_f32_16x16x32_bf16 v[72:75], v[140:143], v[100:103], v[72:75]\n\t" \
    "ds_read_b64_tr_b16 v[200:201], %[b0] offset:24576\n\t" \
    "s_add_u32 m0, %[d], 24320\n\t" \
    "v_mfma_f32_16x16x32_bf16 v[68:71], v[136:139], v[104:107], v[68:71]\n\t" \
    "buffer_load_dwordx4 %[vb1], %[rb], %[y] offen offset:256 lds\n\t" \
    "v_mfma_f32_16x16x32_bf16 v[72:75], v[144:147], v[104:107], v[72:75]\n\t" \
    "ds_read_b64_tr_b16 v[202:203], %[b0] offset:25600\n\t" \
    "v_mfma_f32_16x16x32_bf16 v[76:79], v[132:135], v[108:111], v[76:79]\n\t" \
    "ds_read_b64_tr_b16 v[208:209], %[b1] offset:24576\n\t" \
    "v_mfma_f32_16x16x32_bf16 v[80:83], v[140:143], v[108:111], v[80:83]\n\t" \
    "ds_read_b64_tr_b16 v[210:211], %[b1] offset:25600\n\t" \
    "v_mfma_f32_16x16x32_bf16 v[76:79], v[136:139], v[112:115], v[76:79]\n\t" \
    "v_mfma_f32_16x16x32_bf16 v[80:83], v[144:147], v[112:115], v[80:83]\n\t" \
    "s_mov_b32 m0, %[pd]\n\t" \
    "s_cmp_lt_u32 %[ps], %[pe]\n\t" \
    "s_cselect_b64 exec, -1, 0\n\t" \
    "buffer_load_dword %[pv], %[rp], %[ps] offen sc1 lds\n\t" \
    "s_mov_b64 exec, -1\n\t" \
    "s_add_u32 %[ps], %[ps], %[pk]\n\t" \
    "s_add_u32 %[t], %[t], 1\n\t" \
    "s_add_u32 %[d], %[lw], %[c]\n\t" \
    "s_add_u32 %[y], %[c], 49152\n\t" \
    "s_cmp_eq_u32 %[y], 147456\n\t" \
    "s_cselect_b32 %[y], 0, %[y]\n\t" \
    "s_sub_u32 %[x], %[y], %[c]\n\t" \
    "s_mov_b32 %[c], %[y]\n\t" \
    "v_add_u32 %[a], %[x], %[a]\n\t" \
    "v_add_u32 %[b0], %[x], %[b0]\n\t" \
    "v_add_u32 %[b1], %[x], %[b1]\n\t" \
    "s_add_u32 %[x], %[t], 3\n\t" \
    "s_cmp_lt_u32 %[x], %[nt]\n\t" \
    "s_cselect_b32 %[x], %[x], 0\n\t" \
    "s_mul_i32 %[y], %[x], %[kb]\n\t" \
    "s_add_u32 %[y], %[y], %[cb]\n\t" \
    "s_lshl_b32 %[x], %[x], 11\n\t" \
    "s_add_u32 %[x], %[x], %[ca]\n\t" \
    "s_waitcnt vmcnt(6) lgkmcnt(0)\n\t" \
    "s_barrier\n\t" \
    "v_mfma_f32_16x16x32_bf16 v[20:23], v[180:183], v[148:151], v[20:23]\n\t" \
    "ds_read_b64_tr_b16 v[116:117], %[b0] offset:0\n\t" \
    "s_add_u32 m0, %[d], 0\n\t" \
    "v_mfma_f32_16x16x32_bf16 v[24:27], v[188:191], v[148:151], v[24:27]\n\t" \
    "buffer_load_dwordx4 %[vb0], %[rb], %[y] offen lds\n\t" \
    "v_mfma_f32_16x16x32_bf16 v[20:23], v[184:187], v[152:155], v[20:23]\n\t" \
    "ds_read_b64_tr_b16 v[118:119], %[b0] offset:1024\n\t" \
    "v_mfma_f32_16x16x32_bf16 v[24:27], v[192:195], v[152:155], v[24:27]\n\t" \
    "ds_read_b64_tr_b16 v[124:125], %[b1] offset:0\n\t" \
    "v_mfma_f32_16x16x32_bf16 v[28:31], v[180:183], v[156:159], v[28:31]\n\t" \
    "ds_read_b64_tr_b16 v[126:127], %[b1] offset:1024\n\t" \
    "v_mfma_f32_16x16x32_bf16 v[32:35], v[188:191], v[156:159], v[32:35]\n\t" \
    "ds_read_b64_tr_b16 v[120:121], %[b0] offset:8192\n\t" \
    "s_add_u32 m0, %[d], 8192\n\t" \
    "v_mfma_f32_16x16x32_bf16 v[28:31], v[184:187], v[160:163], v[28:31]\n\t" \
    "buffer_load_dwordx4 %[vb1], %[rb], %[y] offen lds\n\t" \
    "v_mfma_f32_16x16x32_bf16 v[32:35], v[192:195], v[160:163], v[32:35]\n\t" \
    "ds_read_b64_tr_b16 v[122:123], %[b0] offset:9216\n\t" \
    "v_mfma_f32_16x16x32_bf16 v[36:39], v[180:183], v[164:167], v[36:39]\n\t" \
    "ds_read_b64_tr_b16 v[128:129], %[b1] offset:8192\n\t" \
    "v_mfma_f32_16x16x32_bf16 v[40:43], v[188:191], v[164:167], v[40:43]\n\t" \
    "ds_read_b64_tr_b16 v[130:131], %[b1] offset:9216\n\t" \
    "v_mfma_f32_16x16x32_bf16 v[36:39], v[184:187], v[168:171], v[36:39]\n\t" \
    "ds_read_b128 v[84:87], %[a] offset:0\n\t" \
    "s_add_u32 m0, %[d], 32768\n\t" \
    "v_mfma_f32_16x16x32_bf16 v[40:43], v[192:195], v[168:171], v[40:43]\n\t" \
    "buffer_load_dwordx4 %[va0], %[ra], %[x] offen sc1 lds\n\t" \
    "v_mfma_f32_16x16x32_bf16 v[44:47], v[180:183], v[172:175], v[44:47]\n\t" \
    "ds_read_b128 v[88:91], %[a] offset:1024\n\t" \
    "v_mfma_f32_16x16x32_bf16 v[48:51], v[188:191], v[172:175], v[48:51]\n\t" \
    "ds_read_b128 v[92:95], %[a] offset:2048\n\t" \
    "v_mfma_f32_16x16x32_bf16 v[44:47], v[184:187], v[176:179], v[44:47]\n\t" \
    "ds_read_b128 v[96:99], %[a] offset:3072\n\t" \
    "v_mfma_f32_16x16x32_bf16 v[48:51], v[192:195], v[176:179], v[48:51]\n\t" \
    "ds_read_b128 v[100:103], %[a] offset:4096\n\t" \
    "s_add_u32 m0, %[d], 40960\n\t" \
    "v_mfma_f32_16x16x32_bf16 v[52:55], v[196:199], v[148:151], v[52:55]\n\t" \
    "buffer_load_dwordx4 %[va1], %[ra], %[x] offen sc1 lds\n\t" \
    "v_mfma_f32_16x16x32_bf16 v[56:59], v[204:207], v[148:151], v[56:59]\n\t" \
    "ds_read_b128 v[104:107], %[a] offset:5120\n\t" \
    "v_mfma_f32_16x16x32_bf16 v[52:55], v[200:203], v[152:155], v[52:55]\n\t" \
    "ds_read_b128 v[108:111], %[a] offset:6144\n\t" \
    "v_mfma_f32_16x16x32_bf16 v[56:59], v[208:211], v[152:155], v[56:59]\n\t" \
    "ds_read_b128 v[112:115], %[a] offset:7168\n\t" \
    "v_mfma_f32_16x16x32_bf16 v[60:63], v[196:199], v[156:159], v[60:63]\n\t" \
    "ds_read_b64_tr_b16 v[132:133], %[b0] offset:16384\n\t" \
    "s_add_u32 m0, %[d], 16128\n\t" \
    "v_mfma_f32_16x16x32_bf16 v[64:67], v[204:207], v[156:159], v[64:67]\n\t" \
    "buffer_load_dwordx4 %[vb0], %[rb], %[y] offen offset:256 lds\n\t" \
    "v_mfma_f32_16x16x32_bf16 v[60:63], v[200:203], v[160:163], v[60:63]\n\t" \
    "ds_read_b64_tr_b16 v[134:135], %[b0] offset:17408\n\t" \
    "v_mfma_f32_16x16x32_bf16 v[64:67], v[208:211], v[160:163], v[64:67]\n\t" \
    "ds_read_b64_tr_b16 v[140:141], %[b1] offset:16384\n\t" \
    "v_mfma_f32_16x16x32_bf16 v[68:71], v[196:199], v[164:167], v[68:71]\n\t" \
    "ds_read_b64_tr_b16 v[142:143], %[b1] offset:17408\n\t" \
    "v_mfma_f32_16x16x32_bf16 v[72:75], v[204:207], v[164:167], v[72:75]\n\t" \
    "ds_read_b64_tr_b16 v[136:137], %[b0] offset:24576\n\t" \
    "s_add_u32 m0, %[d], 24320\n\t" \
    "v_mfma_f32_16x16x32_bf16 v[68:71], v[200:203], v[168:171], v[68:71]\n\t" \
    "buffer_load_dwordx4 %[vb1], %[rb], %[y] offen offset:256 lds\n\t" \
    "v_mfma_f32_16x16x32_bf16 v[72:75], v[208:211], v[168:171], v[72:75]\n\t" \
    "ds_read_b64_tr_b16 v[138:139], %[b0] offset:25600\n\t" \
    "v_mfma_f32_16x16x32_bf16 v[76:79], v[196:199], v[172:175], v[76:79]\n\t" \
    "ds_read_b64_tr_b16 v[144:145], %[b1] offset:24576\n\t" \
    "v_mfma_f32_16x16x32_bf16 v[80:83], v[204:207], v[172:175], v[80:83]\n\t" \
    "ds_read_b64_tr_b16 v[146:147], %[b1] offset:25600\n\t" \
    "v_mfma_f32_16x16x32_bf16 v[76:79], v[200:203], v[176:179], v[76:79]\n\t" \
    "v_mfma_f32_16x16x32_bf16 v[80:83], v[208:211], v[176:179], v[80:83]\n\t" \
    "s_mov_b32 m0, %[pd]\n\t" \
    "s_cmp_lt_u32 %[ps], %[pe]\n\t" \
    "s_cselect_b64 exec, -1, 0\n\t" \
    "buffer_load_dword %[pv], %[rp], %[ps] offen sc1 lds\n\t" \
    "s_mov_b64 exec, -1\n\t" \
    "s_add_u32 %[ps], %[ps], %[pk]\n\t" \
    "s_add_u32 %[t], %[t], 1\n\t" \
    "s_cmp_lt_u32 %[t], %[nt]\n\t" \
    "s_cbranch_scc1 L_%=\n\t" \
    "s_waitcnt lgkmcnt(0)\n\t" \
    "s_nop 7\n\t" \
    : "=&{v[20:23]}"(acc[0][0][0][0]), "=&{v[24:27]}"(acc[0][0][0][1]), "=&{v[28:31]}"(acc[0][0][1][0]), "=&{v[32:35]}"(acc[0][0][1][1]), "=&{v[36:39]}"(acc[0][0][2][0]), "=&{v[40:43]}"(acc[0][0][2][1]), "=&{v[44:47]}"(acc[0][0][3][0]), "=&{v[48:51]}"(acc[0][0][3][1]), "=&{v[52:55]}"(acc[0][1][0][0]), "=&{v[56:59]}"(acc[0][1][0][1]), "=&{v[60:63]}"(acc[0][1][1][0]), "=&{v[64:67]}"(acc[0][1][1][1]), "=&{v[68:71]}"(acc[0][1][2][0]), "=&{v[72:75]}"(acc[0][1][2][1]), "=&{v[76:79]}"(acc[0][1][3][0]), "=&{v[80:83]}"(acc[0][1][3][1]), [a] "+v"(aoff), [b0] "+v"(boff0), [b1] "+v"(boff1), [c] "+s"(cur), [t] "+s"(t), [ps] "+s"(ps), [d] "=&s"(sd), [x] "=&s"(sx), [y] "=&s"(sy) \
    : [va0] "v"(voffA[0]), [va1] "v"(voffA[1]), [vb0] "v"(voffB[0]), [vb1] "v"(voffB[1]), [pv] "v"(pf.v), [ra] "s"(ra), [rb] "s"(rb), [rp] "s"(rp), [lw] "s"(ldsw), [nt] "s"(nt), [kb] "s"(kB), [ca] "s"(cA), [cb] "s"(cB), [pd] "s"(pd), [pe] "s"(pe), [pk] "s"(pf.step) \
    : "memory", "scc", "v84", "v85", "v86", "v87", "v88", "v89", "v90", "v91", "v92", "v93", "v94", "v95", "v96", "v97", "v98", "v99", "v100", "v101", "v102", "v103", "v104", "v105", "v106", "v107", "v108", "v109", "v110", "v111", "v112", "v113", "v114", "v115", "v116", "v117", "v118", "v119", "v120", "v121", "v122", "v123", "v124", "v125", "v126", "v127", "v128", "v129", "v130", "v131", "v132", "v133", "v134", "v135", "v136", "v137", "v138", "v139", "v140", "v141", "v142", "v143", "v144", "v145", "v146", "v147", "v148", "v149", "v150", "v151", "v152", "v153", "v154", "v155", "v156", "v157", "v158", "v159", "v160", "v161", "v162", "v163", "v164", "v165", "v166", "v167", "v168", "v169", "v170", "v171", "v172", "v173", "v174", "v175", "v176", "v177", "v178", "v179", "v180", "v181", "v182", "v183", "v184", "v185", "v186", "v187", "v188", "v189", "v190", "v191", "v192", "v193", "v194", "v195", "v196", "v197", "v198", "v199", "v200", "v201", "v202", "v203", "v204", "v205", "v206", "v207", "v208", "v209", "v210", "v211")
#define AQ_PIN0() asm volatile("" : "+{v[28:35]}"(wr[0][0]), "+{v[36:43]}"(wr[0][1]))
#define AQ_PIN1() asm volatile("" : "+{v[44:51]}"(wr[1][0]), "+{v[52:59]}"(wr[1][1]))
#define AQ_PIN2() asm volatile("" : "+{v[60:67]}"(wr[2][0]), "+{v[68:75]}"(wr[2][1]))
#define AQ_PIN3() asm volatile("" : "+{v[76:83]}"(wr[3][0]), "+{v[84:91]}"(wr[3][1]))
#define AQ_PIN4() asm volatile("" : "+{v[92:99]}"(wr[4][0]), "+{v[100:107]}"(wr[4][1]))
#define AQ_PIN5() asm volatile("" : "+{v[108:115]}"(wr[5][0]), "+{v[116:123]}"(wr[5][1]))
#define AQ_PIN6() asm volatile("" : "+{v[124:131]}"(wr[6][0]), "+{v[132:139]}"(wr[6][1]))
#define AQ_PIN7() asm volatile("" : "+{v[140:147]}"(wr[7][0]), "+{v[148:155]}"(wr[7][1]))
#define AQ_PIN8() asm volatile("" : "+{v[156:163]}"(wr[8][0]), "+{v[164:171]}"(wr[8][1]))
#define AQ_PIN9() asm volatile("" : "+{v[172:179]}"(wr[9][0]), "+{v[180:187]}"(wr[9][1]))
#define AQ_PIN10() asm volatile("" : "+{v[188:195]}"(wr[10][0]), "+{v[196:203]}"(wr[10][1]))
#define AQ_PIN11() asm volatile("" : "+{v[204:211]}"(wr[11][0]), "+{v[212:219]}"(wr[11][1]))
#define AK_PIN0() asm volatile("" : "+{v[20:27]}"(wr[0][0]), "+{v[28:35]}"(wr[0][1]))
#define AK_PIN1() asm volatile("" : "+{v[36:43]}"(wr[1][0]), "+{v[44:51]}"(wr[1][1]))
#define AK_PIN2() asm volatile("" : "+{v[52:59]}"(wr[2][0]), "+{v[60:67]}"(wr[2][1]))
#define AK_PIN3() asm volatile("" : "+{v[68:75]}"(wr[3][0]), "+{v[76:83]}"(wr[3][1]))
#define AK_PIN4() asm volatile("" : "+{v[84:91]}"(wr[4][0]), "+{v[92:99]}"(wr[4][1]))
#define AK_PIN5() asm volatile("" : "+{v[100:107]}"(wr[5][0]), "+{v[108:115]}"(wr[5][1]))
#define AK_PIN6() asm volatile("" : "+{v[116:123]}"(wr[6][0]), "+{v[124:131]}"(wr[6][1]))
#define AK_PIN7() asm volatile("" : "+{v[132:139]}"(wr[7][0]), "+{v[140:147]}"(wr[7][1]))
#define AK_PIN8() asm volatile("" : "+{v[148:155]}"(wr[8][0]), "+{v[156:163]}"(wr[8][1]))
#define AK_PIN9() asm volatile("" : "+{v[164:171]}"(wr[9][0]), "+{v[172:179]}"(wr[9][1]))
#define AK_PIN10() asm volatile("" : "+{v[180:187]}"(wr[10][0]), "+{v[188:195]}"(wr[10][1]))
#define AK_PIN11() asm volatile("" : "+{v[196:203]}"(wr[11][0]), "+{v[204:211]}"(wr[11][1]))
#define V_PIN() asm volatile("" : "+{v[28:35]}"(wv[0]), "+{v[36:43]}"(wv[1]), "+{v[44:51]}"(wv[2]), "+{v[52:59]}"(wv[3]), "+{v[60:67]}"(wv[4]), "+{v[68:75]}"(wv[5]), "+{v[76:83]}"(wv[6]), "+{v[84:91]}"(wv[7]), "+{v[92:99]}"(wv[8]), "+{v[100:107]}"(wv[9]), "+{v[108:115]}"(wv[10]), "+{v[116:123]}"(wv[11]), "+{v[124:131]}"(wv[12]), "+{v[132:139]}"(wv[13]), "+{v[140:147]}"(wv[14]), "+{v[148:155]}"(wv[15]))
#define WOV_PIN0() asm volatile("" : "+{v[156:163]}"(wo[0][0][0]), "+{v[164:171]}"(wo[0][0][1]), "+{v[172:179]}"(wo[0][1][0]), "+{v[180:187]}"(wo[0][1][1]))
#define WOV_PIN1() asm volatile("" : "+{v[188:195]}"(wo[1][0][0]), "+{v[196:203]}"(wo[1][0][1]), "+{v[204:211]}"(wo[1][1][0]), "+{v[212:219]}"(wo[1][1][1]))
#define WOW_PIN0() asm volatile("" : "+{v[28:35]}"(wo[0][0][0]), "+{v[36:43]}"(wo[0][0][1]), "+{v[44:51]}"(wo[0][1][0]), "+{v[52:59]}"(wo[0][1][1]))
#define WOW_PIN1() asm volatile("" : "+{v[60:67]}"(wo[1][0][0]), "+{v[68:75]}"(wo[1][0][1]), "+{v[76:83]}"(wo[1][1][0]), "+{v[84:91]}"(wo[1][1][1]))
#define WOW_PIN2() asm volatile("" : "+{v[92:99]}"(wo[2][0][0]), "+{v[100:107]}"(wo[2][0][1]), "+{v[108:115]}"(wo[2][1][0]), "+{v[116:123]}"(wo[2][1][1]))
#define WOW_PIN3() asm volatile("" : "+{v[124:131]}"(wo[3][0][0]), "+{v[132:139]}"(wo[3][0][1]), "+{v[140:147]}"(wo[3][1][0]), "+{v[148:155]}"(wo[3][1][1]))
#define WOW_PIN4() asm volatile("" : "+{v[156:163]}"(wo[4][0][0]), "+{v[164:171]}"(wo[4][0][1]), "+{v[172:179]}"(wo[4][1][0]), "+{v[180:187]}"(wo[4][1][1]))
#define WOW_PIN5() asm volatile("" : "+{v[188:195]}"(wo[5][0][0]), "+{v[196:203]}"(wo[5][0][1]), "+{v[204:211]}"(wo[5][1][0]), "+{v[212:219]}"(wo[5][1][1]))
#define A_PIN(QW, i) do { switch (i) { case 0: if (QW) AQ_PIN0(); else AK_PIN0(); break; case 1: if (QW) AQ_PIN1(); else AK_PIN1(); break; case 2: if (QW) AQ_PIN2(); else AK_PIN2(); break; case 3: if (QW) AQ_PIN3(); else AK_PIN3(); break; case 4: if (QW) AQ_PIN4(); else AK_PIN4(); break; case 5: if (QW) AQ_PIN5(); else AK_PIN5(); break; case 6: if (QW) AQ_PIN6(); else AK_PIN6(); break; case 7: if (QW) AQ_PIN7(); else AK_PIN7(); break; case 8: if (QW) AQ_PIN8(); else AK_PIN8(); break; case 9: if (QW) AQ_PIN9(); else AK_PIN9(); break; case 10: if (QW) AQ_PIN10(); else AK_PIN10(); break; case 11: if (QW) AQ_PIN11(); else AK_PIN11(); break; } } while (0)
#define WO_PIN(VW, i) do { switch (i) { case 0: if (VW) WOV_PIN0(); else WOW_PIN0(); break; case 1: if (VW) WOV_PIN1(); else WOW_PIN1(); break; case 2: if (!VW) WOW_PIN2(); break; case 3: if (!VW) WOW_PIN3(); break; case 4: if (!VW) WOW_PIN4(); break; case 5: if (!VW) WOW_PIN5(); break; } } while (0)

__device__ __forceinline__ int lds_byte(int r, int c) {
  const int st = (r >> 4) * 2 + (c >> 5), rr = r & 15, cc = c & 31, ob = rr * 64 + cc * 2;
  return st * 1024 + (ob ^ (((ob >> 9) & 1) << 5));
}
#define PFNT ""
#define G_DvoffB dmaw
#define PFH 5
struct Pf { unsigned v, step; bool on, a; };
#define G_PF(t) do { if (pf.on && (t) < nt) asm volatile("s_mov_b32 m0, %3\n\ts_nop 0\n\tbuffer_load_dword %0, %1, %2 offen sc1 " PFNT "lds" \
    :: "v"(pf.v), "s"(pf.a ? ra : rb), "s"((t) * pf.step), "s"((unsigned)(size_t)lds + 148480) : "memory"); } while (0)

#define G_ST1(bufoff, r, so, voff, i) G_D##voff(r, (voff)[i], so, ldsw + (bufoff) + (i) * 8192)
#define G_DvoffA dmas
#define G_STAGE(bufoff, so, voff) do { G_ST1(bufoff, G_R##voff, so, voff, 0); G_ST1(bufoff, G_R##voff, so, voff, 1); } while (0)
#define G_RvoffA ra
#define G_RvoffB rb
#define G_WAIT_V(n) asm volatile("s_waitcnt vmcnt(%0)" :: "n"(n) : "memory")
__device__ __forceinline__ void gemm_h(LAS unsigned char* lds, rsrc_t ra, rsrc_t rb, unsigned cA, unsigned cB, int lda, int ldb, int nt,
                                       const Pf& pf, bool wide, f32x4 (&acc)[1][2][4][2]) {
  const int wid = __builtin_amdgcn_readfirstlane(threadIdx.x >> 6), lane = opq(0), tid = wid * 64 + lane;
  const int wr = wid >> 2, wc = wid & 3, fr = lane & 15, fq = lane >> 4;
  unsigned voffA[2], voffB[2];
  #pragma unroll
  for (int i = 0; i < 2; ++i) {
    voffA[i] = (unsigned)(((wid >> 1) + 4 * i) * 16 * lda + (wid & 1) * 1024 + lane * 16);
    const int b = tid * 16 + i * 8192, r = b >> 8, pos = (b >> 4) & 15;
    const int c = pos ^ (((r & 3) << 1) | (((r >> 3) & 1) << 3));
    voffB[i] = (unsigned)(r * ldb + c * 16);
  }
  const unsigned kB = 64 * ldb;
  const unsigned ldsw = (unsigned)(size_t)lds + (unsigned)wid * 1024u;
  const int q = (lane & 15) >> 2, p = lane & 3;
  unsigned aoff = (unsigned)(size_t)lds + 32768 + lds_byte(wr * 64 + fr, fq * 8);
  unsigned boff0 = (unsigned)(size_t)lds + (8 * fq + q) * 256 + (((wc * 4 + (p >> 1)) ^ ((q << 1) | ((fq & 1) << 3))) << 4) + 8 * (p & 1), boff1 = boff0 ^ 32;
#define H_STAGE(so, t) do { G_STAGE(so, cB + (t) * kB, voffB); G_STAGE((so) + 32768, cA + (t) * 2048, voffA); if (wide) G_STAGE((so) + 16384, cB + (t) * kB + 256, voffB); } while (0)
  #pragma unroll
  for (int t = 3; t < PFH; ++t) G_PF(t);
  H_STAGE(0, 0); H_STAGE(49152, 1); H_STAGE(98304, 2);
  const rsrc_t rp = pf.a ? ra : rb;
  const unsigned pd = (unsigned)(size_t)lds + 148480, pe = pf.on ? nt * pf.step : 0u;
  unsigned cur = 0, t = 0, ps = PFH * pf.step, sd, sx, sy;
  if (wide) H_LOOP2(); else H_LOOP1();
}

__device__ __forceinline__ u32x4 epi_pack(const f32x4 a0, const f32x4 a1) {
  const unsigned x0 = pack2(a0[0], a0[1]), x1 = pack2(a0[2], a0[3]);
  const unsigned y0 = pack2(a1[0], a1[1]), y1 = pack2(a1[2], a1[3]);
  auto s0 = __builtin_amdgcn_permlane16_swap(x0, y0, false, false);
  auto s1 = __builtin_amdgcn_permlane16_swap(x1, y1, false, false);
  return (u32x4){s0[0], s1[0], s0[1], s1[1]};
}
__device__ __forceinline__ unsigned gelu2(unsigned w) {
  const f32x2 x = {lo16(w), hi16(w)};
  const float c0 = -2.f * 1.4426950408889634f * 0.7978845608028654f, c1 = c0 * 0.044715f;
  const f32x2 a = x * (x * x * c1 + c0);
  const f32x2 d = (f32x2){__builtin_amdgcn_exp2f(a[0]), __builtin_amdgcn_exp2f(a[1])} + 1.f;
  const f32x2 y = x * (f32x2){__builtin_amdgcn_rcpf(d[0]), __builtin_amdgcn_rcpf(d[1])};
  return pack2(y[0], y[1]);
}
__device__ __forceinline__ float silu_f(float b) { return b * __builtin_amdgcn_rcpf(1.f + __expf(-b)); }

__device__ __forceinline__ unsigned blk_in(int r, int c) { const int ob = r * 64 + c * 2; return ob ^ (((ob >> 9) & 1) << 5); }
__device__ __forceinline__ unsigned xn_blk(int rb, int cb) { return (unsigned)(((rb >> 1) * 32 + cb) * 2 + (rb & 1)) * 1024u; }

template <int EPI>
__device__ __forceinline__ void epi_rows(const f32x4 (&acc)[1][2][4][2], rsrc_t rs, unsigned obase, int ldc, int pm2, int pn2, int sh, bool wide, int wr, int wc, int ln) {
  const int fq = ln >> 4, cb = 16 * (fq & 1) + 8 * (fq >> 1), fr = (ln & 15) + sh;
  const unsigned lo = (unsigned)((fr >> 4) * 16 * ldc) + blk_in(fr & 15, cb);
  const unsigned ob = (unsigned)(pm2 * 128 + 64 * wr) * (unsigned)ldc + (unsigned)(pn2 * 4 + wc) * 1024u + lo;
  u32x4 r[8];
  if (EPI >= 2) {
    #pragma unroll
    for (int i = 0; i < 8; ++i)
      r[i] = ld16(rs, EPI == 2 ? WS_XN + xn_blk(pm2 * 8 + 4 * wr + (i & 3), pn2 * 4 + 4 * (i >> 2) + wc) + lo : WS_X1 + ob + (i & 3) * 32768 + (i >> 2) * 4096);
  }
  #pragma unroll
  for (int i = 0; i < 8; ++i) {
    if (i == 4 && !wide) break;
    u32x4 w = epi_pack(acc[0][i >> 2][i & 3][0], acc[0][i >> 2][i & 3][1]);
    #pragma unroll
    for (int k = 0; k < 4; ++k) {
      w[k] = EPI < 2 ? gelu2(w[k]) : pack2(lo16(r[i][k]) + lo16(w[k]), hi16(r[i][k]) + hi16(w[k]));
      if (EPI == 3) w[k] = pack2(silu_f(lo16(w[k])), silu_f(hi16(w[k])));
    }
    st16(rs, obase + ob + (unsigned)(16 * (i & 3) * ldc) + (i >> 2) * 4096, w);
  }
}

__device__ __forceinline__ float row_sum16(float v) {
  v += __uint_as_float(__builtin_amdgcn_update_dpp(0, (int)__float_as_uint(v), 0x128, 0xf, 0xf, false));
  v += __uint_as_float(__builtin_amdgcn_update_dpp(0, (int)__float_as_uint(v), 0x124, 0xf, 0xf, false));
  v += __uint_as_float(__builtin_amdgcn_update_dpp(0, (int)__float_as_uint(v), 0x122, 0xf, 0xf, false));
  v += __uint_as_float(__builtin_amdgcn_update_dpp(0, (int)__float_as_uint(v), 0x121, 0xf, 0xf, false));
  return v;
}
__device__ __forceinline__ void ln_rows(u32x4 (&a)[8]) {
  f32x2 v[32];
  #pragma unroll
  for (int i = 0; i < 32; ++i) v[i] = (f32x2){lo16(a[i >> 2][i & 3]), hi16(a[i >> 2][i & 3])};
  f32x2 s[4] = {v[0], v[1], v[2], v[3]};
  #pragma unroll
  for (int i = 4; i < 32; ++i) s[i & 3] += v[i];
  s[0] = (s[0] + s[1]) + (s[2] + s[3]);
  const float mean = row_sum16(s[0][0] + s[0][1]) * (1.f / DM);
  #pragma unroll
  for (int i = 0; i < 32; ++i) { const f32x2 d = v[i] - mean; s[i & 3] = i < 4 ? d * d : s[i & 3] + d * d; }
  s[0] = (s[0] + s[1]) + (s[2] + s[3]);
  const float var = row_sum16(s[0][0] + s[0][1]) * (1.f / DM);
  const float mb = rb1(mean), vb = rb1(var);
  const float rden = __builtin_amdgcn_rcpf(rb1(__builtin_amdgcn_sqrtf(rb1(vb + 1e-5f))));
  #pragma unroll
  for (int i = 0; i < 32; ++i) {
    const f32x2 d = v[i] - mb;
    const unsigned w = pack2(d[0], d[1]);
    const f32x2 y = (f32x2){lo16(w), hi16(w)} * rden;
    a[i >> 2][i & 3] = pack2(y[0], y[1]);
  }
}

template <int N>
__device__ __forceinline__ void fin_sum(rsrc_t rs, unsigned po, u32x4* v) {
  u32x4 pv[N][8];
  #pragma unroll
  for (int i = 0; i < 8 * N; ++i) pv[i >> 3][i & 7] = ld16(rs, po + (i >> 3) * 16384 + ((i >> 1) & 3) * 131072 + (i & 1) * 1024);
#define PF4(i) __builtin_bit_cast(f32x4, pv[c][i])
  #pragma unroll
  for (int c = 0; c < N; ++c) v[c] = epi_pack((PF4(0) + PF4(2)) + (PF4(4) + PF4(6)), (PF4(1) + PF4(3)) + (PF4(5) + PF4(7)));
}
__device__ __forceinline__ void fin_out(const Ptrs& P, unsigned eo, u32x4 v, u32x4 gv, u32x4 xv) {
  #pragma unroll
  for (int k = 0; k < 4; ++k) {
    v[k] = pack2(lo16(v[k]) * lo16(gv[k]), hi16(v[k]) * hi16(gv[k]));
    v[k] = pack2(lo16(xv[k]) + lo16(v[k]), hi16(xv[k]) + hi16(v[k]));
  }
  *(u32x4*)((char*)P.out + eo) = v;
}

__device__ __forceinline__ void mlp_role(LAS unsigned char* lds, const Ptrs& P, int hf, int slot, int wid) {
  const int wr = wid >> 2, wc = wid & 3;
  const int p2 = hf * 8 + (slot >> 2), pn = slot & 3;
  char* ws = P.ws;
  const rsrc_t rs = WS_RSRC(P);
  const u16* const p1 = P.w1; const u16* const p2w = P.w2; const u16* const p3 = P.w3; const u16* const p4 = P.w4;
#define M_DONE(c) do { VMWAIT(); BARRIER(); if (wid == 0) cnt_add(ws, c, opq(0)); } while (0)
  #pragma unroll 1
  for (int code = 0; code < 7; ++code) {
    int ln = opq(0);
    if (wid == 0) {
      if (code == 0) flag_wait(P, F_LN1(p2 * 4 + (ln & 3)), 0, ln);
      else if (code < 3 || code == 6) {
        const unsigned co = CNT_OFF + (code == 1 ? C_W1(min(max(p2 - 1 + min(ln, 2), 0), 15)) : code == 2 ? C_LN(p2) : C_W3(p2)) * 128;
        for (unsigned spins = 0; __any(ld4(rs, co) < 4u);) {
          __builtin_amdgcn_s_sleep(2);
          if (spin_fail(spins, P, ln)) break;
        }
      }
    }
    BARRIER();
    TLW(3 * code);
    const bool w3 = code >= 2 && code < 6, wide = code != 5;
    const int lda = code == 6 ? 7168 : 2048, ldb = w3 ? 7168 : 2048, c2 = w3 ? (code - 2) * 8 + (wide ? 2 * pn : pn) : 2 * pn;
    const rsrc_t rb = __builtin_amdgcn_make_buffer_rsrc((void*)(code == 0 ? p1 : code == 1 ? p2w : w3 ? p3 : p4), 0, code < 2 ? DM * DM * 2 : DM * FF * 2, RSRC_W3);
    const unsigned cA = (code == 0 ? WS_XN2 : code == 1 ? WS_H1 : w3 ? WS_X1N : WS_H3) + p2 * 128 * lda, cB = c2 * 256;
    Pf pf;
    pf.on = wc == 0 && (!wr || code < 3 || code == 6); pf.a = wr;
    ln &= 31;
    pf.v = wr ? cA + (pn * 2 + (ln >> 4)) * 16 * lda + (ln & 15) * 128 : cB + ((slot >> 2) * 8 + (ln >> 2)) * ldb + (ln & (wide ? 3 : 1)) * 128;
    pf.step = wr ? 2048 : 64 * ldb;
    f32x4 acq[1][2][4][2];
    gemm_h(lds, rs, rb, cA, cB, lda, ldb, code == 6 ? 56 : 16, pf, wide, acq);
    TLW(3 * code + 1);
    ln = opq(0);
    if (code == 1) {
      epi_rows<2>(acq, rs, WS_X1, 2048, p2, c2, 0, true, wr, wc, ln);
      M_DONE(C_W2(p2));
      if (wid == 0) cnt_wait(P, C_W2(p2), 4, ln);
      BARRIER();
      const unsigned ro = (unsigned)(p2 * 8 + pn * 2 + wr) * 32768u + ((ln >> 2) & 3) * 1024 + blk_in(wc * 4 + (ln >> 4), (ln & 3) * 8);
      u32x4 v[8];
      #pragma unroll
      for (int i = 0; i < 8; ++i) v[i] = ld16(rs, WS_X1 + ro + i * 4096);
      ln_rows(v);
      #pragma unroll
      for (int i = 0; i < 8; ++i) st16(rs, WS_X1N + ro + i * 4096, v[i]);
      M_DONE(C_LN(p2));
    } else if (code == 6) {
      epi_rows<3>(acq, rs, WS_G, 2048, p2, c2, 0, true, wr, wc, ln);
      M_DONE(C_W4(p2));
    } else {
      VMWAIT();
      if (code == 0 && wid < 4) touch(p2w, DM * DM * 2, ((hf * 32 + slot) * 256 + wid * 64 + ln) * 128, lds);
      epi_rows<1>(acq, rs, code ? WS_H3 : WS_H1, ldb, p2, c2, code ? 0 : (pn & 1) ? -1 - (pn >> 1) : 1 + (pn >> 1), wide, wr, wc, ln);
      if (code == 0 && wid == 0) setup_wait(P, ln);
      if (code == 0 || code == 5) M_DONE(code ? C_W3(p2) : C_W1(p2));
    }
    TLW(3 * code + 2);
  }
}

#define FINE 52
__device__ __forceinline__ void fin_role(LAS unsigned char* lds, const Ptrs& P, int wid) {
  const int wr = wid >> 2, wc = wid & 3;
  const rsrc_t rs = WS_RSRC(P);
  volatile LAS int* tk = (volatile LAS int*)(lds + 147456);
  unsigned tks = 0;
#define POP() do { if (wid == 0 && opq(0) == 0) tks = __hip_atomic_fetch_add((gu32*)(P.ws + CNT_OFF + C_HEAD * 128), 1u, RLX_AGENT); } while (0)
  POP();
  for (int round = 0;; ++round) {
    if (wid == 0 && opq(0) == 0) tk[round & 1] = tks;
    LBARRIER();
    const int u = __builtin_amdgcn_readfirstlane(tk[round & 1]);
    if (u >= 2 * FINE + 8 * (XS3 - FINE)) break;
    const bool big = u < 2 * FINE;
    const int sg = big ? u >> 1 : FINE + ((u - 2 * FINE) >> 3), cc0 = big ? (u & 1) * 4 : (u - 2 * FINE) & 7;
    const int ln = opq(0), fq = ln >> 4, cb = 16 * (fq & 1) + 8 * (fq >> 1);
    const unsigned eo = (unsigned)(sg * 32 + wr * 16 + (ln & 15)) * 2048u + (unsigned)(cc0 * 128 + wc * 32 + cb) * 2u;
    const unsigned go = WS_G + (unsigned)(sg * 2 + wr) * 32768u + (cc0 * 4 + wc) * 1024 + blk_in(ln & 15, cb);
    const unsigned po = WS_PT + (unsigned)sg * 524288u + cc0 * 16384 + wc * 4096 + wr * 2048 + ln * 16;
    const int xg = (sg >= XS1) + (sg >= XS2), want = sg + 1 - (xg == 2 ? XS2 : xg * XS1);
    u32x4 v[4], gv[4], xv[4];
    if (big) {
      if (wid == 2) flag_wait(P, F_PRG(5 + xg, ln & 31), want, ln);
      BARRIER();
      fin_sum<2>(rs, po, v);
      asm volatile("" : "+v"(v[0]), "+v"(v[1]) :: "memory");
      #pragma unroll
      for (int c = 0; c < 4; ++c) xv[c] = __builtin_nontemporal_load((const u32x4*)((const char*)P.x + eo + c * 256));
      fin_sum<2>(rs, po + 32768, v + 2);
      if (wid == 1) cnt_wait(P, C_W4(sg >> 2), 4, ln);
      BARRIER();
      #pragma unroll
      for (int c = 0; c < 4; ++c) gv[c] = ld16(rs, go + c * 4096);
      #pragma unroll
      for (int c = 0; c < 4; ++c) fin_out(P, eo + c * 256, v[c], gv[c], xv[c]);
    } else {
      if (wid == 1) cnt_wait(P, C_W4(sg >> 2), 4, ln);
      BARRIER();
      gv[0] = ld16(rs, go);
      xv[0] = __builtin_nontemporal_load((const u32x4*)((const char*)P.x + eo));
      if (wid == 2) flag_wait<3>(P, F_PRG(5 + xg, (ln & 3) * 8 + cc0), want, ln);
      BARRIER();
      fin_sum<1>(rs, po, v);
      fin_out(P, eo, v[0], gv[0], xv[0]);
    }
    POP();
  }
}

__device__ __forceinline__ u32x4 ldsv(LAS unsigned char* p) { return *(volatile LAS u32x4*)p; }
#define DMA_A dman
#define DMA_V dman
#define DMA_O dmaw
#define TOUCH 0
#define TAUX 0
#define POISON4 ((u32x4){~0u, ~0u, ~0u, ~0u})
#define DLYM 0
#define DLYA 8
#define DLYK (DLYA + 8)
#define DLYO 8
#define FLA 0
#define FLB 0
#define CADM 1
#define CADO 1

#define RTC(id)
#define DCA 2
#define DCB 12
#define DL1 -3
#define DL2 9
#define DLB 15
#define ADL 0
#define SN0 150
#define MAP 0
#define HELP 256
#define BORD 0
#define QT 0
__device__ __forceinline__ void track(int& est, int d, int gain) { const int e = d - (est >> 4); est += e > 44 ? DCB * 2 : e * gain; }
__device__ __forceinline__ unsigned rfl(unsigned v) { unsigned r; asm volatile("s_nop 0\n\tv_readfirstlane_b32 %0, %1\n\ts_nop 4" : "=s"(r) : "v"(v)); return r; }
__device__ __forceinline__ void plan_wait(rsrc_t rs, unsigned fo, int s, int& lim, unsigned& tm, unsigned& d) {
  while ((d & 255u) != (unsigned)s && (lim -= DEC1) > 0) {
    __builtin_amdgcn_s_sleep(1);
    const u32x2 v = __builtin_amdgcn_raw_buffer_load_b64(rs, 0, fo, AUX_SC1);
    tm = rfl(v.x); d = rfl(v.y);
  }
  const unsigned now = (unsigned)__builtin_amdgcn_s_memrealtime() * 4u;
  tm = now + min((int)(tm - now), 2000);
  d = min(d >> 8, 1000u);
}
#define SPIN_BUDGET (1 << 21)

__device__ __forceinline__ unsigned rec_go(LAS unsigned char* w, const Ptrs& P, u32x2 f, int wid, int lane) {
  if (wid >= 6) {
    if (__any(f.y != P.tag)) f.x = flag_wait(P, wid == 6 ? F_LN0(lane) : F_CAL(lane), 0, lane);
    if (wid == 7) { const unsigned m = near_half(f.x); if (lane == 0) *(volatile LAS unsigned*)w = m; }
  }
  LBARRIER();
  return __builtin_amdgcn_readfirstlane(*(volatile LAS unsigned*)w);
}

#define SLOT(i) ((i) * 16384)
#define LA_MEM SLOT(8)
#define LA_Q SLOT(9)
#define LA_K (SLOT(9) + 2048)
#define LA_P (SLOT(9) + 4096)
#define LA_KS (SLOT(9) + 6144)
#define LA_VM LA_MEM
#define LA_KX (SLOT(9) + 8192)

template <bool QW>
__device__ __forceinline__ void a_wave(LAS unsigned char* lds, Ptrs& P, int h, int wid) {
  char* ws = P.ws;
  const int lane = opq(0), tid = wid * 64 + lane;
  const int j = wid & 3, g = lane >> 4, q4 = (lane & 15) >> 2, p4 = lane & 3;
  const rsrc_t rs = WS_RSRC(P);
  const rsrc_t rq = __builtin_amdgcn_make_buffer_rsrc((void*)P.Wq, 0, HD * DM * 2, RSRC_W3);
  const rsrc_t rk = __builtin_amdgcn_make_buffer_rsrc((void*)P.Wk, 0, HD * DM * 2, RSRC_W3);
  const unsigned lbase = (unsigned)(size_t)lds;
  int lim = SPIN_BUDGET;
  u32x8 wr[12][2];
  const int trow = 16 * (g >> 1) + 4 * (g & 1) + 8 * (q4 >> 1) + (q4 & 1);
  const int tsw = ((q4 & 1) | ((g & 1) << 1) | ((q4 >> 1) << 2)) << 1;
  const int toff = trow * 256 + (((4 * j + (p4 >> 1)) ^ tsw) << 4) + 8 * (p4 & 1);
  {
    unsigned dv[2];
    #pragma unroll
    for (int x = 0; x < 2; ++x) {
      const int row = 8 * wid + 4 * x + (lane >> 4);
      const int sw = ((row & 1) | (((row >> 2) & 1) << 1) | (((row >> 3) & 1) << 2)) << 1;
      dv[x] = (unsigned)(row * (HD * 2) + h * 256 + (((lane & 15) ^ sw) << 4));
    }
#define A_GRP(n) do { _Pragma("unroll") for (int i = 0; i < 8; ++i) \
      DMA_A(((n) & 1) ? rk : rq, dv[i & 1], (4 * (((n) & 1) ? ((n) >> 1) ^ ((n) >> 2) : (((n) >> 1) + 1) & 3) + (i >> 1)) * 64 * HD * 2, lbase + SLOT(4 * ((n) & 1) + (i >> 1)) + wid * 2048 + (i & 1) * 1024); } while (0)
    A_GRP(0); A_GRP(1);
    #pragma unroll
    for (int n = 0; n < 6; ++n) {
      VMW(8);
      BARRIER();
      if ((n & 1) == (QW ? 0 : 1)) {
        #pragma unroll
        for (int b = 0; b < 4; ++b) {
          LAS unsigned char* t = lds + SLOT(QW ? 0 : 4) + ((b ^ j) << 14);
          wr[4 * (n >> 1) + b][0] = rd_frag<8192, 512>(t + toff);
          wr[4 * (n >> 1) + b][1] = rd_frag<8192, 512>(t + (toff ^ 32));
        }
        #pragma unroll
        for (int b = 0; b < 4; ++b) A_PIN(QW, 4 * (n >> 1) + b);
      }
      LBARRIER();
      A_GRP(n + 2);
    }
  }
  const unsigned cb = comm_base(ws);
  u32x2 f0 = {0u, 0u};
  #pragma unroll
  for (int rd = 0; rd < 2; ++rd) {
    if (rd == 0) VMW(8);
    else {
      if (wid >= 6) f0 = ld8(rs, FLG_OFF + (wid == 6 ? F_LN0(lane) : F_CAL(lane)) * 128);
      const unsigned fv = wid == 3 ? FLG_OFF + F_PRG(min((lane >> 1) & 3, 2), h) * 128 + (lane & 1) * 4 : WS_QF + j * (16u << 20) + h * 8192 + lane * 128;
      if (QW) asm volatile("s_mov_b32 m0, %2\n\ts_nop 0\n\tbuffer_load_dword %0, %1, 0 offen sc1 lds" :: "v"(fv), "s"(rs), "s"(lbase + LA_P + (wid == 3 ? 640 : 1024)) : "memory");
      get_tag(P);
      VMWAIT();
    }
    BARRIER();
    u32x8 f[2][2];
    #pragma unroll
    for (int i = 0; i < 2; ++i) {
      f[i][0] = rd_frag<8192, 512>(lds + SLOT((QW ? 0 : 2) + 4 * rd + i) + toff);
      f[i][1] = rd_frag<8192, 512>(lds + SLOT((QW ? 0 : 2) + 4 * rd + i) + (toff ^ 32));
    }
    asm volatile("" : "+v"(f[0][0]), "+v"(f[0][1]), "+v"(f[1][0]), "+v"(f[1][1]));
    LBARRIER();
    #pragma unroll
    for (int i = 0; i < 2; ++i)
      #pragma unroll
      for (int t = 0; t < 2; ++t) {
        LAS unsigned char* p = lds + SLOT((QW ? 0 : 2) + 4 * rd + i) + j * 4096 + t * 2048 + lane * 16;
        *(LAS u32x4*)p = (u32x4){f[i][t][0], f[i][t][1], f[i][t][2], f[i][t][3]};
        *(LAS u32x4*)(p + 1024) = (u32x4){f[i][t][4], f[i][t][5], f[i][t][6], f[i][t][7]};
      }
  }
  const unsigned mask = rec_go(lds + LA_P + 128, P, f0, wid, lane);
  const int idx = (lane & 1) ? 0xEEEE : 0x4444;
  const unsigned ab_a = lbase + LA_MEM + lane * 16, am_a = ab_a + j * 1024;
  const unsigned t0_a = lbase + SLOT((QW ? 0 : 4) + j) + j * 4096 + lane * 16;
  const unsigned iw_a = lbase + (QW ? LA_Q : LA_K) + j * 512 + q4 * 128 + g * 32 + p4 * 4;
  const unsigned sw_a = lbase + LA_KS + (j >> 1) * 1024 + ((j & 1) * 2 + (q4 >> 1)) * 256 + (4 * g + (p4 & 1)) * 16 + ((q4 & 1) * 2 + (p4 >> 1)) * 4;
  const unsigned qr_a = lbase + LA_Q + g * 128 + (lane & 7) * 16;
  const unsigned kr_a = lbase + LA_K + g * 128 + (2 * q4 + (lane & 1)) * 16;
  const unsigned fr_a = lbase + LA_P + p4 * 4;
  if (tid < 12) *(LAS unsigned*)(lds + LA_P + tid * 4) = 0u;
  const unsigned vo_v = VB_AT(h) + j * 512 + lane * 4, md = lbase + LA_VM - VB_AT(h);
  const unsigned oo_v = OB_AT((lane >> 2) & 1, h) + (j * 16 + (g >> 1) * 8 + p4 * 2 + (g & 1)) * 16;
  const unsigned long long onej = 1ull << j;
  const unsigned lkx = lbase + LA_KX + j * 2048;
  u32x8 oxr = {};
  const unsigned xo_v = (lane >> 5) * 2048 + (lane & 31) * 16, vl_a = lbase + LA_VM + (j & 1) * 1024 + lane * 4;
  const int dq = 16 * (j >> 1) + (lane & 15) - 4 * g;
  const unsigned fb = step_base(cb, mask, 0);
  unsigned tgt = (unsigned)__builtin_amdgcn_s_memrealtime() * 4u;
  unsigned long long tp = 0;
  unsigned pt = 0, pd = 0;
  const unsigned long long fmask = __builtin_amdgcn_readfirstlane((16u >> wid) & 1u);
  int known = 0;
  __builtin_amdgcn_s_setprio(QW ? 2 : 1);
  LBARRIER();
  report(P, wid);
  TL(72);
  for (int s = 0; s < NSEG; ++s) {
    const unsigned sb = step_base(cb, mask, s);
    const unsigned sbm = sb - (lbase + LA_MEM);
    const unsigned fo = (unsigned)(s * NH + h) * 8192u + j * 2048 - (lbase + LA_MEM);
    GSDECL;
    RTC(h);
    lim = __builtin_amdgcn_readfirstlane(lim);
    if (QW) {
      if (wid == 3 && s > 0) {
        if (opq(0) == 0) __builtin_amdgcn_raw_buffer_store_b32((unsigned)tp * 4u - tgt, rs, 0, fb + FL_O(h), AUX_SC1);
        plan_wait(rs, fb + FL_T(s), s, lim, pt, pd);
        tgt = pt;
      }
      if (s >= known) {
        const u32x2 v = *(volatile LAS u32x2*)(lds + LA_P + 640 + (opq(0) & 3) * 8);
        if (!__any(v.y != P.tag)) known = rfl(min(min(__builtin_amdgcn_readlane(v.x, 0), __builtin_amdgcn_readlane(v.x, 1)), __builtin_amdgcn_readlane(v.x, 2)));
      }
      kv_wait(P, s, h, known, opq(0));
      const unsigned kxo = WS_KF + fo, vxo = WS_VF + fo;
      if (wid < 3) for (int i = 0; i < ADL; ++i) __builtin_amdgcn_s_sleep(1);
      {
      dmas(rs, ab_a, kxo, lkx);
      dmas(rs, ab_a, kxo + 1024, lkx + 1024);
      dmas(rs, ab_a, WS_QF + fo, lbase + LA_P + 1024);
      dmas(rs, ab_a, WS_QF + fo + 1024, lbase + LA_P + 1024);
      }
      if (wid == 2 && s + 1 >= known) {
        const int ln = opq(0);
        const unsigned fv = FLG_OFF + F_PRG(min((ln >> 1) & 3, 2), h) * 128 + (ln & 1) * 4;
        asm volatile("s_mov_b32 m0, %2\n\ts_nop 0\n\tbuffer_load_dword %0, %1, 0 offen sc1 lds" :: "v"(fv), "s"(rs), "s"(lbase + LA_P + 640) : "memory");
      }
      unsigned rg = 0x3c000000u;
      A_MAIN_Q();
      TL(s);
      GSFLUSH(0, 24 + wid); GSFLUSH(1, 4 + wid); GSFLUSH(2, 12 + j); GSFLUSH(3, 16 + j); GSFLUSH(4, 20 + j);
      if (s + PD < NSEG && !(opq(0) & 8)) __builtin_amdgcn_raw_buffer_store_b128(poison4(), rs, oo_v, step_base(cb, mask, s + PD), AUX_SC1);
    } else {
      const unsigned qxo = WS_QF + fo - (j & 1) * 2048, oxo = WS_OX + (unsigned)((s == 0 || s == NSEG - 1 ? s : s - 1) * NH + h) * 8192u + (j & 1) * 4096 + (j >> 1) * 1024;
      A_MAIN_K();
      if (s >= NSEG - 2) {
        __builtin_amdgcn_raw_buffer_store_b128((u32x4){oxr[0], oxr[1], oxr[2], oxr[3]}, rs, xo_v, oxo + (NSEG - 1 - s) * NH * 8192, AUX_SC1);
        __builtin_amdgcn_raw_buffer_store_b128((u32x4){oxr[4], oxr[5], oxr[6], oxr[7]}, rs, xo_v + 512, oxo + (NSEG - 1 - s) * NH * 8192, AUX_SC1);
      }
      if (wid == 7) GSFLUSH(0, 28);
      GSFLUSH(1, 4 + wid);
    }
  }
  VMWAIT();
  BARRIER();
  if (wid == 4) flag_set(P, F_PRG(4, h), NSEG, opq(0));
}

#define TSL 4
#define LB_MEM 0
#define LB_RED 16384
template <bool VW>
__device__ __forceinline__ void b_wave(LAS unsigned char* lds, Ptrs& P, int r, int c, int wid, int bs, int xg) {
  constexpr int NHD = VW ? 2 : 6;
  char* ws = P.ws;
  const int lane = opq(0), tid = wid * 64 + lane;
  const int j = wid & 3, g = lane >> 4, q4 = (lane & 15) >> 2, p4 = lane & 3;
  const int b = r * 32 + c;
  const rsrc_t rs = WS_RSRC(P);
  const rsrc_t rv = __builtin_amdgcn_make_buffer_rsrc((void*)P.Wv, 0, HD * DM * 2, RSRC_W3);
  const rsrc_t ro = __builtin_amdgcn_make_buffer_rsrc((void*)P.Wo, 0, HD * DM * 2, RSRC_W3);
  const unsigned lbase = (unsigned)(size_t)lds;
  int lim = SPIN_BUDGET;
  const int h0 = ((VW ? 2 * j : 8 + 6 * j) + 5 * c) & 31;
  u32x8 wv[16], wo[6][2][2];
  {
    if ((wid & 3) < 2 + MAP && xg < 2) {
      const unsigned nk = 8 + 2 * xg, M = (bs * (2 + MAP) + (wid & 3)) * 64 + lane, row = M / nk, k = M - row * nk;
      if (row < HELP) touch(VW ? P.Wq : P.Wk, HD * DM * 2, (row < 256 ? row + (VW ? 512 : 256) : row + 512) * (HD * 2) + min(xg * 4096 + (k >> 1) * 1024 + 256, 7936u) + (k & 1) * 128, lds);
    }
    const int nl = MAP ? (xg == 2 ? 8 : 4) : xg == 2 ? 6 : 5;
    #pragma unroll
    for (int i = 0; i < 2; ++i) {
      const unsigned L = bs * 1024 + wid * 128 + i * 64 + lane, row = L / nl;
      touch(P.Wo, HD * DM * 2, row * (DM * 2) + (c - (bs >> 1)) * 64 + (L - row * nl) * 128, lds);
    }
    VMWAIT();
    BARRIER();
    const int pos = lane >> 2;
    const int prow = (pos & 9) | ((pos & 2) << 1) | ((pos & 4) >> 1);
    const unsigned dv0 = (unsigned)(prow * (DM * 2) + c * 64 + ((lane & 3) << 4));
    const unsigned dv1 = (unsigned)(prow * (DM * 2) + c * 64 + (((lane & 3) ^ 2) << 4));
    const int toff = wid * 16384 + (g * 16 + q4) * 64 + (((p4 >> 1) ^ ((g & 1) << 1)) << 4) + 8 * (p4 & 1);
    #pragma unroll
    for (int rd = 0; rd < NHD / 2; ++rd) {
      #pragma unroll
      for (int hh = 0; hh < 2; ++hh) {
        const int h = (h0 + 2 * rd + hh) & 31;
        #pragma unroll
        for (int i = 0; i < 8; ++i)
          DMA_O(ro, (i & 1) ? dv1 : dv0, (unsigned)(h * 128 + i * 16) * (DM * 2), lbase + wid * 16384 + hh * 8192 + i * 1024);
      }
      VMWAIT();
      #pragma unroll
      for (int hh = 0; hh < 2; ++hh)
        #pragma unroll
        for (int ks = 0; ks < 2; ++ks)
          #pragma unroll
          for (int t = 0; t < 2; ++t)
            wo[2 * rd + hh][ks][t] = rd_frag<4096, 256>(lds + (toff ^ (t * 32)) + hh * 8192 + ks * 512);
      #pragma unroll
      for (int hh = 0; hh < 2; ++hh)
        WO_PIN(VW, 2 * rd + hh);
      asm volatile("s_waitcnt lgkmcnt(0)" ::: "memory");
    }
    const int lr = lane >> 3;
    #pragma unroll
    for (int i = 0; i < 16; ++i) {
      const int sw = (((lr >> 2) & 1) | ((i & 1) << 1)) << 1;
      const unsigned dv = (unsigned)(lr * (HD * 2) + b * 128 + (((lane & 7) ^ sw) << 4));
      DMA_V(rv, dv, (unsigned)(wid * 128 + i * 8) * (HD * 2), lbase + wid * 16384 + i * 1024);
    }
    VMWAIT();
    BARRIER();
    if (VW) {
      const int trow = 16 * (g >> 1) + 4 * (g & 1) + 8 * (q4 >> 1) + (q4 & 1);
      const int tsw = ((g & 1) | ((q4 >> 1) << 1)) << 1;
      LAS unsigned char* tv = lds + trow * 128 + (((2 * j + (p4 >> 1)) ^ tsw) << 4) + 8 * (p4 & 1);
      #pragma unroll
      for (int ks = 0; ks < 16; ++ks) wv[ks] = rd_frag<4096, 256>(tv + ks * 8192);
      V_PIN();
    }
    LBARRIER();
  }
  const unsigned cb = comm_base(ws);
  get_tag(P);
  if (wid == 0) touch(P.x, SEQ * DM * 2, (SEQ - 64 + lane) * 2048 + c * 64, lds);
  const unsigned mask = rec_go(lds + LB_RED + 4096 + 64, P, (u32x2){0u, P.tag - 1u}, wid, lane);
  const int idx = (lane & 1) ? 0xEEEE : 0x4444;
  const unsigned am_a = lbase + LB_MEM + lane * 16;
  const unsigned fb = step_base(cb, mask, 0);
  volatile LAS unsigned* tsl = (volatile LAS unsigned*)(lds + LB_RED + 4096);
  if (tid < 8) tsl[tid] = tid ? 0u : (unsigned)__builtin_amdgcn_s_memrealtime() * 4u;
  unsigned dn = 600;
  int da = 560 * 16, gb = 350 * 16;
  if (VW) __builtin_amdgcn_s_setprio(2);
  LBARRIER();
  report(P, wid);
  TL(72);
  for (int s = 0; s < NSEG; ++s) {
    const unsigned sb = step_base(cb, mask, s);
    const int ln = opq(lane), gg = ln >> 4;
    GSDECL;
    RTC(32 + b);
    const int l16 = ln * 16;
    const int xc = 32 * (c & 1) + 2 * (ln & 15);
    const unsigned mo_v = MF_AT(c >> 1) + ((xc >> 4) * 16 + (4 * r + gg) * 2 + ((xc >> 3) & 1)) * 16 + (xc & 7) * 2;
    if (!VW) {
      const unsigned tm = __builtin_amdgcn_readfirstlane(*tsl), tgt = tm + DLB, la = lbase + LB_MEM + j * 2048 + l16, mo = sb + j * 2048;
      B_POLL();
      if (wid == 7) {
        const int late = (int)((unsigned)__builtin_amdgcn_s_memrealtime() * 4u - tgt) - SN0;
        const unsigned to = tm + dn + (late > 80 ? late : 0);
        if (ln == 0) tsl[1] = to;
        if (b == 0) {
          dn = (da >> 4) + DL1;
          if (ln == 0) { *tsl = to + (gb >> 4) + DL2; st8(rs, fb + FL_T(s + 1), (u32x2){to + (gb >> 4) + DL2, (dn << 8) | (s + 1)}); }
          const unsigned va = ld4(rs, fb + FL_O(ln & 31)), vb = ld4(rs, fb + FL_B(ln));
          if (s > 2) {
            track(da, rfl(wmax(va)), s < 8 ? 5 : DCA);
            track(gb, rfl(wmax(vb)), s < 8 ? 5 : DCA);
          }
        }
      }
      if (wid == 7 && s > 0 && s + 2 < NSEG) __builtin_amdgcn_raw_buffer_store_b32(~0u, rs, mo_v, step_base(cb, mask, s + 2), AUX_SC1);
    } else {
      GS(3);
      const unsigned vo = VB_AT(b >> 1) + ((b & 1) * 4 + j) * 256;
      V_PROJ();
      GS(0); GSFLUSH(0, 4 + j);
      if (s + PD < NSEG && ln < 16) st16(rs, step_base(cb, mask, s + PD) + vo + l16, poison4());
    }
    GSFLUSH(3, 24 + wid);
    if (s + 1 == NSEG) break;
    unsigned so[6];
    #pragma unroll
    for (int i = 0; i < NHD; ++i) { const int h = (h0 + i) & 31; so[i] = __builtin_amdgcn_readfirstlane(sb + OB_AT(r, h)); }
    const unsigned sn = __builtin_amdgcn_readfirstlane(step_base(cb, mask, s + 1));
    const unsigned rw_a = lbase + LB_RED + (wid * 128 + (gg & 1) * 64 + (gg >> 1) * 16 + (ln & 15)) * 4;
    const unsigned rr_a = lbase + LB_RED + ln * 8;
    if (wid == 7) {
      if (b != 0) {
        __builtin_amdgcn_s_sleep(4);
        unsigned tm = 0;
        dn = 0;
        plan_wait(rs, fb + FL_T(s + 1), s + 1, lim, tm, dn);
        if (ln == 0) *tsl = tm;
      }
      const unsigned tgt = rfl(tsl[1]);
      T_WAIT();
    }
    lim = __builtin_amdgcn_readfirstlane(lim);
    unsigned long long t1;
    if (VW) WO_V(); else WO_W();
    if (wid == 0) {
      if (ln == 0) st4(rs, fb + FL_B(b), (unsigned)t1 * 4u - tsl[1]);
    }
    if (wid == 0) {
      GSFLUSH(2, 16);
      TL(s);
    }
    GSFLUSH(1, 8 + wid);
    if (wid == 0 || wid == 4) { GSFLUSH(4, 17 + (wid >> 2)); GSFLUSH(0, 19 + (wid >> 2)); }
  }
  __builtin_amdgcn_s_setprio(0);
  const int ln = opq(0);
  TL(64);
  volatile LAS int* rdy = (volatile LAS int*)(lds + 98304 + 3 * 16384);
  if (tid == 0) *rdy = 61;
  LBARRIER();
#define T_SRC(sg, i) (WS_OX + (unsigned)((sg) * NH + ((h0 + (i)) & 31)) * 8192u + r * 1024)
#define T_MMA(i, k, f) do { const u32x8 w0 = wo[i][(k) & 1][0], w1 = wo[i][(k) & 1][1]; const int e = ((k) >> 1) * 4; \
    a0 = MFMA32(((u32x4){w0[e], w0[e + 1], w0[e + 2], w0[e + 3]}), f, a0); a1 = MFMA32(((u32x4){w1[e], w1[e + 1], w1[e + 2], w1[e + 3]}), f, a1); } while (0)
#define T_DMA(sg, i) do { _Pragma("unroll") for (int k = 0; k < 4; ++k) dmas(rs, ln * 16, T_SRC(sg, i) + k * 2048, lbase + j * 24576 + (i) * 4096 + k * 1024); } while (0)
  const int cb2 = 16 * ((ln >> 4) & 1) + 8 * (ln >> 5);
  const unsigned eo = (unsigned)((NSEG - 1 - j) * 32 + r * 16 + (ln & 15)) * 2048u + (unsigned)(c * 32 + cb2) * 2u;
  u32x4 gv, xv;
  if (VW && wid < NSEG - XS3) {
    xv = *(const u32x4*)((const char*)P.x + eo);
    cnt_wait(P, C_W4(15), 4, ln);
    gv = ld16(rs, WS_G + (unsigned)((NSEG - 1 - j) * 2 + r) * 32768u + c * 1024 + blk_in(ln & 15, cb2));
  }
  bool ahead = false;
  f32x4 k0, k1;
  #pragma unroll 1
  for (int sg = XS3; sg < NSEG; ++sg) {
    f32x4 a0 = zero4(), a1 = zero4();
    if (VW) {
      if (wid == 3) {
        for (int w = __builtin_amdgcn_readfirstlane(*rdy); w <= sg;) {
          w = max(w + 1, -wmax(-(int)flag_wait<TSL>(P, F_PRG(4, ln & 31), w + 1, ln)));
          if (ln == 0) *rdy = w;
        }
      } else while (__builtin_amdgcn_readfirstlane(*rdy) <= sg) __builtin_amdgcn_s_sleep(1);
      u32x4 of[8];
      #pragma unroll
      for (int i = 0; i < 8; ++i) of[i] = __builtin_amdgcn_raw_buffer_load_b128(rs, ln * 16 + (i & 3) * 2048, T_SRC(sg, i >> 2), AUX_SC1);
      #pragma unroll
      for (int i = 0; i < 8; ++i) T_MMA(i >> 2, i & 3, of[i]);
    } else {
      if (!ahead) {
        while (__builtin_amdgcn_readfirstlane(*rdy) <= sg) __builtin_amdgcn_s_sleep(1);
        #pragma unroll
        for (int i = 0; i < NHD; ++i) T_DMA(sg, i);
      }
      VMW(20);
      ahead = __builtin_amdgcn_readfirstlane(*rdy) > sg + 1;
      u32x4 of[2][4];
#define T_RD(i) do { _Pragma("unroll") for (int k = 0; k < 4; ++k) of[(i) & 1][k] = *(const LAS u32x4*)(lds + j * 24576 + (i) * 4096 + k * 1024 + ln * 16); } while (0)
      T_RD(0);
      #pragma unroll
      for (int i = 0; i < NHD; ++i) {
        asm volatile("s_waitcnt lgkmcnt(0)" : "+v"(of[i & 1][0]), "+v"(of[i & 1][1]), "+v"(of[i & 1][2]), "+v"(of[i & 1][3]) :: "memory");
        if (i + 1 < NHD) {
          if (ahead) VMW(16); else G_WAIT_V(16 - 4 * (i < 4 ? i : 4));
          T_RD(i + 1);
        }
        #pragma unroll
        for (int k = 0; k < 4; ++k) {
          if (ahead) dmas(rs, ln * 16, T_SRC(sg + 1, i) + k * 2048, lbase + j * 24576 + i * 4096 + k * 1024);
          T_MMA(i, k, of[i & 1][k]);
          __builtin_amdgcn_sched_barrier(0);
        }
      }
    }
    LAS f32x4* pw = (LAS f32x4*)(lds + 98304 + ((sg & 3) * 8 + wid) * 2048) + ln;
    if (wid == 0 && sg == NSEG - 1) { k0 = a0; k1 = a1; } else { pw[0] = a0; pw[64] = a1; }
  }
  if (VW && wid < NSEG - XS3) {
    LBARRIER();
    const LAS f32x4* pr = (const LAS f32x4*)(lds + 98304 + (3 - j) * 16384) + ln;
    f32x4 t[2];
    #pragma unroll
    for (int k = 0; k < 2; ++k)
      t[k] = (((wid ? pr[k * 64] : k ? k1 : k0) + pr[128 + k * 64]) + (pr[256 + k * 64] + pr[384 + k * 64])) + ((pr[512 + k * 64] + pr[640 + k * 64]) + (pr[768 + k * 64] + pr[896 + k * 64]));
    fin_out(P, eo, epi_pack(t[0], t[1]), gv, xv);
  } else LBARRIER();
}

#define DMA_S dman
#define XDL 4
#define XPC 0
#define PW3 11
#define PW4 25
#define SDL 0
#define LS_P 131072
template <int MODE>
__device__ __forceinline__ void s_role(LAS unsigned char* lds, Ptrs& P, const u16* W, int h, unsigned obase, int nseg, int fl, int wid, int s0) {
  constexpr bool XM = MODE >= 2;
  constexpr unsigned RS = XM ? DM * 2 : HD * 2, SS = XM ? 262144u : 65536u;
  const int lane = opq(0);
  const int kh = wid >> 2, tp = wid & 3;
  const rsrc_t rs = WS_RSRC(P);
  const rsrc_t rw = __builtin_amdgcn_make_buffer_rsrc((void*)W, 0, HD * DM * 2, RSRC_W3);
  const unsigned lbase = (unsigned)(size_t)lds;
  u32x8 w[8][2];
  unsigned f0 = 0;
  if (!XM) for (int i = 0; i < SDL; ++i) __builtin_amdgcn_s_sleep(32);
  if (XM && XDL) {
    if (wid == 0) flag_wait(P, F_PRG(4, h), max(s0 - XDL, 0), lane);
    BARRIER();
  }
  {
    const int g = lane >> 4, q4 = (lane & 15) >> 2, p4 = lane & 3;
    const int trow = XM ? 16 * g + 4 * (q4 >> 1) + (q4 & 1) : 8 * g + q4;
#define S_SW(row) ((((row) & 1) | ((((row) >> (XM ? 2 : 1)) & 1) << 1) | ((((row) >> (XM ? 4 : 3)) & 1) << 2)) << 1)
    const int toff = trow * 256 + (((4 * tp + (p4 >> 1)) ^ S_SW(trow)) << 4) + 8 * (p4 & 1);
    unsigned dv[2];
    #pragma unroll
    for (int x = 0; x < 2; ++x) {
      const int row = 8 * wid + 4 * x + (lane >> 4);
      dv[x] = (unsigned)row * RS + (XM ? (h >> 3) * 1024 * RS + (h & 7) * 256 : h * 256) + (((lane & 15) ^ S_SW(row)) << 4);
    }
#define S_IMG(i) do { DMA_S(rw, dv[0], (i) * 64 * RS, lbase + ((i) & 7) * 16384 + wid * 2048); \
                      DMA_S(rw, dv[1], (i) * 64 * RS, lbase + ((i) & 7) * 16384 + wid * 2048 + 1024); if (XM && XPC) __builtin_amdgcn_s_sleep(XPC); } while (0)
    #pragma unroll
    for (int i = 0; i < 8; ++i) S_IMG(i);
    #pragma unroll
    for (int b = 0; b < 4; ++b) {
      if (b == 3 && !XM) { f0 = ld4(rs, FLG_OFF + F_LN0(lane & 7) * 128 + 4); get_tag(P); }
      if (b < 3) VMW(8); else VMWAIT();
      BARRIER();
      if (kh == (b >> 1)) {
        #pragma unroll
        for (int i = 4 * (b & 1); i < 4 * (b & 1) + 4; ++i) {
          w[i][0] = rd_frag<XM ? 2048 : 8192, XM ? 512 : 1024>(lds + toff + i * 16384);
          w[i][1] = rd_frag<XM ? 2048 : 8192, XM ? 512 : 1024>(lds + (toff ^ 32) + i * 16384);
        }
      }
      LBARRIER();
      if (b < 2) {
        #pragma unroll
        for (int i = 4 * b + 8; i < 4 * b + 12; ++i) S_IMG(i);
      }
    }
  }
  TL(XM ? 74 : 72);
  const unsigned l16 = lane * 16;
  volatile LAS int* rd = (volatile LAS int*)(lds + LS_P + 20480);
#define X_READY(want) do { \
    if (wid == 0) { \
      const int n_ = -wmax(-(int)flag_wait(P, F_PRG(4, (h & 24) + (lane & 7)), s0 + (want), lane)) - s0; \
      if (lane == 0) *rd = max(n_, want); \
    } \
    LBARRIER(); } while (0)
  if (MODE == 2) X_READY(2);
  else if (!XM) {
    if (__any(f0 != P.tag)) flag_wait(P, F_LN0(lane & 7), 0, lane);
  }
  unsigned sx = (XM ? WS_OX + s0 * SS + (h >> 3) * 65536 : WS_XN) + kh * 32768 + tp * 4096;
  const unsigned sd = lbase + kh * 65536 + tp * 4096;
  const unsigned xl = kh * 65536 + (XM ? l16 : blk_in(lane & 15, (lane >> 4) * 8));
  const unsigned xp = lbase + xl, pa = lbase + LS_P + tp * 4096 + l16;
  LAS f32x4* const pq = (LAS f32x4*)(lds + LS_P + tp * 4096 + l16);
  if (MODE == 3) {
#define X_GET(t) do { _Pragma("unroll") for (int i = 0; i < 16; ++i) \
      dmas(rs, l16, sx + (t) * SS + ((i >> 2) & 1) * 32768 + (i >> 3) * 16384 + (i & 3) * 1024, sd + ((i >> 2) & 1) * 65536 + ((t) & 1) * 32768 + (i >> 3) * 16384 + (i & 3) * 1024); } while (0)
    bool have = false;
    const unsigned pv = FLG_OFF + F_PRG(kh ? 7 : 4, kh ? h : (h & 24) + (lane & 7)) * 128;
    if (!kh) __builtin_amdgcn_s_setprio(1);
    #pragma unroll 1
    for (int t = 0; t < nseg; ++t) {
      if (!have && !kh) {
        flag_wait<2>(P, F_PRG(4, (h & 24) + (lane & 7)), s0 + t + 1, lane);
        X_GET(t);
        VMW(8);
      }
      BARRIER();
      f32x4 a[4];
      unsigned pg;
      ONE_X(xp + (t & 1) * 32768);
      if (wid == 5) flag_set(P, fl, t, lane);
      have = t + 1 < nseg && !__any(pg < (unsigned)(s0 + t + 2));
      if (!kh && lane == 0) rd[tp] = have;
      pq[kh * 128] = kh ? a[0] : a[2]; pq[kh * 128 + 64] = kh ? a[1] : a[3];
      if (have && !kh) X_GET(t + 1);
      LBARRIER();
      const unsigned oo = obase + (unsigned)(s0 + t) * 0x80000u + (unsigned)h * 16384u + tp * 4096 + kh * 2048 + l16;
      st16(rs, oo, __builtin_bit_cast(u32x4, (kh ? a[2] : a[0]) + pq[128 - kh * 128]));
      st16(rs, oo + 1024, __builtin_bit_cast(u32x4, (kh ? a[3] : a[1]) + pq[192 - kh * 128]));
      if (__any(rd[lane & 3] == 0)) {
        VMWAIT();
        BARRIER();
        if (wid == 5) flag_set(P, fl, t + 1, lane);
      } else if (!kh) VMW(10);
      asm volatile("s_waitcnt lgkmcnt(0)" ::: "memory");
      TL(t);
    }
    __builtin_amdgcn_s_setprio(0);
    TL(65);
    return;
  }
  if (XM) {
    #pragma unroll
    for (int i = 0; i < 12; ++i) dmas(rs, l16, sx + (i >> 3) * SS + ((i >> 2) & 1) * 16384 + (i & 3) * 1024, sd + (i >> 2) * 16384 + (i & 3) * 1024);
    VMWAIT();
    BARRIER();
  } else {
    #pragma unroll
    for (int i = 0; i < 16; ++i) dmas(rs, l16, sx + (i >> 3) * SS + ((i >> 2) & 1) * 16384 + (i & 3) * 1024, sd + ((i >> 2) ^ 2) * 16384 + (i & 3) * 1024);
    VMW(12);
    BARRIER();
    f32x4 a[4];
    if (MODE) ONE_V(xp + 32768); else ONE_K(xp + 32768);
    pq[kh * 128] = kh ? a[0] : a[MODE ? 1 : 2]; pq[kh * 128 + 64] = kh ? a[MODE ? 2 : 1] : a[3];
    LBARRIER();
    const f32x4 a0 = (kh ? a[MODE ? 1 : 2] : a[0]) + pq[128 - kh * 128], a1 = (kh ? a[3] : a[MODE ? 2 : 1]) + pq[192 - kh * 128];
    if (MODE) st16(rs, obase + h * 8192 + (2 * tp + kh) * 1024 + l16, (u32x4){pack2(a0[0], a0[1]), pack2(a0[2], a0[3]), pack2(a1[0], a1[1]), pack2(a1[2], a1[3])});
    else st16(rs, obase + h * 8192 + kh * 4096 + tp * 1024 + l16, (u32x4){pack2(a0[0], a1[0]), pack2(a0[1], a1[1]), pack2(a0[2], a1[2]), pack2(a0[3], a1[3])});
    sx += SS;
    #pragma unroll
    for (int i = 0; i < 4; ++i) dmas(rs, l16, sx + SS + i * 1024, sd + 32768 + i * 1024);
    VMW(4);
    BARRIER();
    if (wid == 1) flag_set(P, fl, 1, lane);
    TL(62);
  }
  u32x8 xf[3], ac[2] = {};
  #pragma unroll
  for (int i = 0; i < 3; ++i) {
    const u32x4 a = *(const LAS u32x4*)(lds + xl + i * 2048), b = *(const LAS u32x4*)(lds + xl + i * 2048 + 1024);
    xf[i] = (u32x8){a[0], a[1], a[2], a[3], b[0], b[1], b[2], b[3]};
  }
  unsigned long long fm, pm;
  asm volatile("s_cmp_eq_u32 %2, 1\n\ts_cselect_b64 %0, 1, 0\n\ts_cmp_eq_u32 %3, 1\n\ts_cselect_b64 %1, -1, 0" : "=&s"(fm), "=&s"(pm) : "s"(wid), "s"(XM ? 0 : wid >> 1) : "scc");
  const unsigned fo = FLG_OFF + fl * 128, pd = lbase + LS_P + 16384;
  const unsigned po = 4 * 65536 + h * 2048 + (wid & 1) * 1024 - tp * 4096 - kh * 32768;
  unsigned oo = XM ? obase + (unsigned)h * 16384u + tp * 4096 + (s0 - 1) * 0x80000u : obase + (unsigned)h * 8192u + tp * (MODE ? 2048 : 1024), tmp, tmp2;
  unsigned long long em;
  int t = XM ? 0 : 1;
  if (MODE < 2) {
    #pragma unroll 1
    for (int part = 0; part < 4; ++part) {
      if (part == 1) { TL(63); if (wid == 0) setup_wait(P, lane); }
      if (MODE == 1 && part >= 2) {
        #pragma unroll
        for (int i = 0; i < 4; ++i) {
          const unsigned to = ((i * 32 + h) * 512 + wid * 64 + opq(0)) * 128;
          if (part == 2) touch(P.w3, DM * FF * 2, to, lds); else touch(P.w4, DM * FF * 2, to, lds);
        }
      }
      const int te = part == 0 ? 3 : part == 1 ? PW3 : part == 2 ? PW4 : nseg + 1;
      if (MODE) S_LOOP_V(te, 0); else S_LOOP_K(te, 0);
    }
  } else {
    #pragma unroll 1
    while (t < nseg) {
      X_READY(min(t + 4, nseg));
      const int n = __builtin_amdgcn_readfirstlane(*rd), te = n >= nseg ? nseg : (n - 2) & ~1;
      TL(t);
      S_LOOP_X(te, nseg);
    }
  }
  TL(XM ? 65 : 64);
  VMWAIT();
  BARRIER();
  if (wid == 1) flag_set(P, fl, nseg, lane);
}

#define LND 1
#define CALN 6
__device__ __forceinline__ void setup_role(LAS unsigned char* lds, Ptrs& P, int cu, int wid) {
  const int lane = opq(0), tid = wid * 64 + lane;
  const rsrc_t rs = WS_RSRC(P);
  u32x4 v[8];
  const u32x4* px = (const u32x4*)(P.x + (size_t)(cu * 32 + wid * 4 + (lane >> 4)) * DM) + (lane & 15);
  #pragma unroll
  for (int i = 0; i < 8; ++i) v[i] = __builtin_nontemporal_load(px + i * 16);
  const unsigned epo = __hip_atomic_load((gu32*)(P.ws + EPO_OFF), RLX_AGENT);
  const unsigned cb = comm_base(P.ws);
  const u32x4 z = {0u, 0u, 0u, 0u};
  const int cr = 63 - cu;
  if (wid == 0 && cr < 16) st16(rs, CNT_OFF + cr * 1024 + lane * 16, z);
  if (cr < 4) st16(rs, cb + FL_B(0) + cr * 8192 + tid * 16, z);
  if (cr == 4 && tid < 256) {
    const int g4 = tid >> 6, k = (tid >> 5) & 1 & (g4 >> 1);
    st16(rs, WS_H1 + ((g4 & 1) ? 127u * 32768u : 0u) + (g4 * 8 + ((tid >> 2) & 7)) * 1024 + blk_in((g4 & 1) ? 15 - k : k, (tid & 3) * 8), z);
  }
  #pragma unroll
  for (int i = 0; i < 2; ++i) {
    const int f = i * 54 + cr;
    if (cr < PD * NCHUNK) {
      u32x4 m = {~0u, ~0u, ~0u, ~0u};
      if (f < 4) {
        const int ks = (f >> 1) * 8 + wid;
        m = *(const u32x4*)(P.mem0 + ((lane >> 1) & 7) * DM + 64 * ks + 16 * (lane >> 4) + 8 * (lane & 1));
        m.x = canon1(m.x); m.y = canon1(m.y); m.z = canon1(m.z); m.w = canon1(m.w);
      }
      st16(rs, cb + (unsigned)(f / (NCHUNK * 2)) * CB_STEP + (f % (NCHUNK * 2)) * 8192u + tid * 16, m);
    }
  }
  const unsigned bi = blk_in((wid & 3) * 4 + (lane >> 4), (lane & 3) * 8);
  const int c0 = (lane >> 2) & 3;
  ln_rows(v);
  TL(69);
  if (cu >= 8) for (int i = 0; i < LND; ++i) __builtin_amdgcn_s_sleep(32);
  #pragma unroll
  for (int i = 0; i < 8; ++i) st16(rs, WS_XN + xn_blk(cu * 2 + (wid >> 2), 4 * i + c0) + bi, v[i]);
  P.tag = __builtin_amdgcn_readfirstlane(epo) + 1u;
  ln_rows(v);
  if (cu < 8) {
    VMWAIT();
    BARRIER();
    if (wid == 0) flag_set(P, F_LN0(cu), 1, lane);
    TL(66);
  }
  asm volatile("" ::: "memory");
  #pragma unroll
  for (int i = 0; i < 8; ++i) st16(rs, WS_XN2 + (unsigned)(cu * 2 + (wid >> 2)) * 32768u + (4 * i + c0) * 1024 + bi, v[i]);
  touch(P.w1, DM * DM * 2, ((cu & 31) * 512 + tid) * 128, lds);
  VMW(9);
  BARRIER();
  if (wid == 0) flag_set(P, F_LN0(cu), 1, lane);
  TL(70);
  if (wid == 0) VMW(2); else VMW(1);
  BARRIER();
  if (wid == 0) flag_set(P, F_LN1(cu), 1, lane);
  TL(71);
  if (cu < 32 && (wid >> 1) == 1) {
    const int id = cu * 2 + (wid & 1);
    unsigned best[2] = {65535u, 65535u};
    #pragma unroll
    for (int it = 0; it < CALN; ++it) {
      const unsigned long long c0 = __builtin_amdgcn_s_memtime();
      asm volatile("s_waitcnt lgkmcnt(0)" ::: "memory");
      if (lane == 0) st4(rs, cb + (id & 3) * CB_STEP + (30 + ((id >> 2) & 1)) * 16384u + (it & 1) * 8192u + (id >> 3) * 1024u, 0u);
      VMWAIT();
      const unsigned d = (unsigned)(__builtin_amdgcn_s_memtime() - c0);
      if (it >= 2) best[it & 1] = min(best[it & 1], d);
    }
    flag_set(P, F_CAL(id), (best[0] << 16) | best[1], lane);
  }
}

#define TLBP 0
#define CPF 1
__global__ __launch_bounds__(512, 2) void mega_kernel(const u16* x, const u16* mem0, const u16* Wq, const u16* Wk, const u16* Wv, const u16* Wo,
                                                     const u16* w1, const u16* w2, const u16* w3, const u16* w4, u16* out, char* ws) {
  __shared__ __attribute__((aligned(16))) unsigned char smem[163840];
  LAS unsigned char* lds = (LAS unsigned char*)smem;
  Ptrs P;
  P.x = x; P.mem0 = mem0; P.Wq = Wq; P.Wk = Wk; P.Wv = Wv; P.Wo = Wo; P.w1 = w1; P.w2 = w2; P.w3 = w3; P.w4 = w4; P.out = out; P.ws = ws;
  const int xcd = blockIdx.x & 7, slot = blockIdx.x >> 3;
  const int wid = __builtin_amdgcn_readfirstlane(threadIdx.x >> 6);
  TL(79);
  P.tag = 0;
  if (wid == 7) {
    unsigned long long pc;
    unsigned len;
    asm volatile("s_getpc_b64 %0\nMK_BEG:\n\ts_mov_b32 %1, MK_END-MK_BEG" : "=s"(pc), "=s"(len));
    touch((const void*)pc, len, (slot * 64 + opq(0)) * 128, lds);
  }
  const int na = xcd == 2 ? 8 : 12;
  const bool isa = xcd < 3 && slot < na;
  const int ah = xcd == 2 ? slot * 4 + 2 : xcd * 16 + (slot & 3) * 4 + (slot >> 2) + (slot >> 3), bs = slot - na, br = bs & 1, bc = (xcd == 1 ? 22 : xcd * 5) + (bs >> 1);
  if (isa) {
    if (wid < 4) a_wave<true>(lds, P, ah, wid); else a_wave<false>(lds, P, ah, wid);
  } else if (xcd < 3) {
    if (wid < 4) b_wave<true>(lds, P, br, bc, wid, bs, xcd); else b_wave<false>(lds, P, br, bc, wid, bs, xcd);
  } else {
    if ((xcd & 3) == 3) {
      setup_role(lds, P, (xcd >> 2) * 32 + slot, wid);
    } else if (xcd == 6) s_role<1>(lds, P, Wv, slot, WS_VF, NSEG, F_PRG(2, slot), wid, 0);
    else s_role<0>(lds, P, xcd == 4 ? Wk : Wq, slot, xcd == 4 ? WS_KF : WS_QF, NSEG, F_PRG(xcd - 4, slot), wid, 0);
    if ((xcd & 3) == 3) mlp_role(lds, P, xcd >> 2, slot, wid);
    else if (xcd == 6) s_role<3>(lds, P, Wo, slot, WS_PT, XS3 - XS2, F_PRG(7, slot), wid, XS2);
    else s_role<2>(lds, P, Wo, slot, WS_PT, xcd == 4 ? XS1 : XS2 - XS1, F_PRG(xcd + 1, slot), wid, xcd == 4 ? 0 : XS1);
    report(P, wid);
    fin_role(lds, P, wid);
    if (blockIdx.x == 3 && wid == 0) {
      cnt_wait(P, C_ARR, 256, threadIdx.x);
      if (threadIdx.x == 0) __hip_atomic_store((gu32*)(ws + EPO_OFF), P.tag, RLX_AGENT);
    }
  }
  asm volatile("MK_END:");
  TL(78);
}

extern "C" void kernel_launch(void* const* d_in, const int* in_sizes, int n_in, void* d_out, int out_size,
                              void* d_ws, size_t ws_size, hipStream_t stream) {
  mega_kernel<<<256, 512, 0, stream>>>((const u16*)d_in[0], (const u16*)d_in[1], (const u16*)d_in[2], (const u16*)d_in[3], (const u16*)d_in[4],
                                       (const u16*)d_in[5], (const u16*)d_in[8], (const u16*)d_in[9], (const u16*)d_in[10], (const u16*)d_in[11],
                                       (u16*)d_out, (char*)d_ws);
}
```

```cpp
#include <hip/hip_runtime.h>
#include <hip/hip_bf16.h>
#include <stdint.h>

#define SEQ 2048
#define DM 1024
#define NH 32
#define DH 128
#define HD (NH * DH)
#define MM 8
#define SEG 32
#define NSEG (SEQ / SEG)
#define FF 3584

typedef unsigned short u16;
typedef __attribute__((ext_vector_type(8))) short bf16x8;
typedef __attribute__((ext_vector_type(4))) short bf16x4;
typedef __attribute__((ext_vector_type(4))) float f32x4;
typedef __attribute__((ext_vector_type(4))) unsigned u32x4;
typedef __attribute__((ext_vector_type(2))) unsigned u32x2;
typedef __attribute__((ext_vector_type(2))) float f32x2;
typedef __attribute__((ext_vector_type(2))) __bf16 bf16v2;
typedef __attribute__((ext_vector_type(8))) unsigned u32x8;
#define LAS __attribute__((address_space(3)))
#define GAS __attribute__((address_space(1)))

__device__ __forceinline__ unsigned pack2(float lo, float hi) {
  return __builtin_bit_cast(unsigned, __builtin_convertvector((f32x2){lo, hi}, bf16v2));
}
__device__ __forceinline__ float rb1(float x) { return __uint_as_float(pack2(0.f, x)); }

typedef GAS unsigned gu32;
#define RLX_AGENT __ATOMIC_RELAXED, __HIP_MEMORY_SCOPE_AGENT
#define SPIN_CAP (1u << 20)
#define AUX_SC1 16
#define RSRC_W3 0x00020000
typedef __amdgpu_buffer_rsrc_t rsrc_t;

__device__ __forceinline__ u32x4 ld16(rsrc_t rs, unsigned off) { return __builtin_amdgcn_raw_buffer_load_b128(rs, off, 0, AUX_SC1); }
__device__ __forceinline__ u32x2 ld8(rsrc_t rs, unsigned off) { return __builtin_amdgcn_raw_buffer_load_b64(rs, off, 0, AUX_SC1); }
__device__ __forceinline__ unsigned ld4(rsrc_t rs, unsigned off) { return __builtin_amdgcn_raw_buffer_load_b32(rs, off, 0, AUX_SC1); }
__device__ __forceinline__ void st16(rsrc_t rs, unsigned off, u32x4 v) { __builtin_amdgcn_raw_buffer_store_b128(v, rs, off, 0, AUX_SC1); }
__device__ __forceinline__ void st8(rsrc_t rs, unsigned off, u32x2 v) { __builtin_amdgcn_raw_buffer_store_b64(v, rs, off, 0, AUX_SC1); }
__device__ __forceinline__ void st4(rsrc_t rs, unsigned off, unsigned v) { __builtin_amdgcn_raw_buffer_store_b32(v, rs, off, 0, AUX_SC1); }
__device__ __forceinline__ float lo16(unsigned w) { return __uint_as_float(w << 16); }
__device__ __forceinline__ float hi16(unsigned w) { return __uint_as_float(w & 0xffff0000u); }

__device__ __forceinline__ float xsum16_32(float v) {
  unsigned u = __float_as_uint(v);
  auto a = __builtin_amdgcn_permlane16_swap(u, u, false, false);
  v = __uint_as_float(a[0]) + __uint_as_float(a[1]);
  u = __float_as_uint(v);
  auto b = __builtin_amdgcn_permlane32_swap(u, u, false, false);
  return __uint_as_float(b[0]) + __uint_as_float(b[1]);
}

__device__ __forceinline__ unsigned canon1(unsigned w) { return min(w, 0xfffffffeu); }
__device__ __forceinline__ unsigned max4(u32x4 v) { return max(max(v.x, v.y), max(v.z, v.w)); }
__device__ __forceinline__ int opq(int) { int v; asm volatile("v_mbcnt_lo_u32_b32 %0, -1, 0\n\tv_mbcnt_hi_u32_b32 %0, -1, %0" : "=v"(v)); return v; }
__device__ __forceinline__ u32x4 poison4() { unsigned m = ~0u; asm volatile("" : "+v"(m)); return (u32x4){m, m, m, m}; }
__device__ __forceinline__ f32x4 zero4() { float m = 0.f; asm volatile("" : "+v"(m)); return (f32x4){m, m, m, m}; }
#define MFMA32(a, b, c) __builtin_amdgcn_mfma_f32_16x16x32_bf16(__builtin_bit_cast(bf16x8, a), __builtin_bit_cast(bf16x8, b), c, 0, 0, 0)
#define MFMA16(a, b, c) __builtin_amdgcn_mfma_f32_16x16x16bf16_1k(__builtin_bit_cast(bf16x4, a), __builtin_bit_cast(bf16x4, b), c, 0, 0, 0)
#define LBARRIER() do { asm volatile("s_waitcnt lgkmcnt(0)" ::: "memory"); __builtin_amdgcn_s_barrier(); asm volatile("" ::: "memory"); } while (0)
#define BARRIER() do { asm volatile("" ::: "memory"); __builtin_amdgcn_s_barrier(); asm volatile("" ::: "memory"); } while (0)
#define VMWAIT() asm volatile("s_waitcnt vmcnt(0)" ::: "memory")
#define VMW(n) asm volatile("s_waitcnt vmcnt(" #n ")" ::: "memory")
#define DELAY(n) do { if ((n) > 0) __builtin_amdgcn_s_sleep(n); } while (0)

#define EPO_OFF 0
#define TMO_OFF 128
#define FLG_OFF 256
#define F_LN0(i) (i)
#define F_LN1(i) (64 + (i))
#define F_CAL(i) (128 + (i))
#define F_PRG(t, h) (192 + (t) * 32 + (h))
#define CNT_OFF 65536
#define C_W1(p2) (p2)
#define C_W2(p2) (16 + (p2))
#define C_LN(p2) (32 + (p2))
#define C_W3(p2) (48 + (p2))
#define C_W4(p2) (64 + (p2))
#define C_HEAD 80
#define C_ARR 81
#define NCNT 128
#define WS_XN (1u << 20)
#define WS_XN2 (WS_XN + (4u << 20))
#define WS_QF (WS_XN2 + (4u << 20))
#define WS_KF (WS_QF + (16u << 20))
#define WS_VF (WS_KF + (16u << 20))
#define WS_DUM (WS_VF + (16u << 20))
#define WS_H1 (WS_DUM + (1u << 20))
#define WS_X1 (WS_H1 + (4u << 20) + 65536u)
#define WS_H3 (WS_X1 + (4u << 20))
#define WS_OX WS_QF
#define WS_X1N WS_H1
#define WS_G WS_XN2
#define PT_AT(kq) (WS_KF + ((kq) >> 1) * (16u << 20) + ((kq) & 1) * 131072u)
#define WS_COMM (WS_H3 + (14u << 20))
#define WS_END (WS_COMM + (4u << 20))
#define CB_STEP 524288u
#define PD 3
__device__ __forceinline__ unsigned comm_base(const char* ws) { return WS_COMM + ((0u - ((unsigned)(size_t)ws + WS_COMM)) & 0x1fffffu); }
__device__ __forceinline__ unsigned step_base(unsigned cb, unsigned mask, int s) { return cb + (unsigned)(s & 3) * CB_STEP + mask; }
#define MF_AT(ks) (((ks) >> 3) * 16384u + ((ks) & 7) * 1024u)
#define OB_AT(r, h) ((2u + (r) * 4u + ((h) >> 3)) * 16384u + ((h) & 7) * 1024u)
#define VB_AT(h) ((10u + ((h) >> 2)) * 16384u + ((h) & 3) * 2048u)
#define XS1 24
#define XS2 46
#define XS3 62
#define CSL 8
#define FSL 6
#define SQUICK 3
#define NCHUNK 18
#define FL_O(h) (29u * 16384u + (h) * 128u)
#define FL_B(b) (28u * 16384u + (b) * 128u)
#define FL_T(s) FL_O(32 + ((s) & 3))

struct Ptrs { const u16 *x, *mem0, *Wq, *Wk, *Wv, *Wo, *w1, *w2, *w3, *w4; u16* out; char* ws; unsigned tag; };
#define WS_RSRC(P) __builtin_amdgcn_make_buffer_rsrc((void*)(P).ws, 0, WS_END, RSRC_W3)

__device__ __forceinline__ bool spin_fail(unsigned& spins, const Ptrs& P, int lane) {
  asm volatile("" ::: "memory");
  if (++spins > SPIN_CAP) {
    unsigned tv;
    asm volatile("v_mov_b32 %0, %1" : "=v"(tv) : "s"(P.tag));
    if (lane == 0) __builtin_amdgcn_raw_buffer_store_b32(tv, WS_RSRC(P), 0, TMO_OFF, AUX_SC1);
    return true;
  }
  return (spins & 255u) == 0u && (unsigned)__builtin_amdgcn_readfirstlane(__builtin_amdgcn_raw_buffer_load_b32(WS_RSRC(P), 0, TMO_OFF, AUX_SC1)) == P.tag;
}
__device__ __forceinline__ unsigned cnt_load(char* ws, int idx) {
  return __builtin_amdgcn_readfirstlane(__hip_atomic_load((gu32*)(ws + CNT_OFF + idx * 128), RLX_AGENT));
}
__device__ __forceinline__ void cnt_add(char* ws, int idx, int lane) {
  asm volatile("s_waitcnt vmcnt(0)" ::: "memory");
  if (lane == 0) __hip_atomic_fetch_add((gu32*)(ws + CNT_OFF + idx * 128), 1u, RLX_AGENT);
}
__device__ __forceinline__ void cnt_wait(const Ptrs& P, int idx, unsigned want, int lane) {
  for (unsigned spins = 0;;) {
    if (cnt_load(P.ws, idx) >= want) break;
    __builtin_amdgcn_s_sleep(CSL);
    if (spin_fail(spins, P, lane)) break;
  }
}
template <int SL = FSL>
__device__ __forceinline__ unsigned flag_wait(const Ptrs& P, int f, unsigned want, int lane) {
  const rsrc_t rs = WS_RSRC(P);
  u32x2 v;
  for (unsigned spins = 0;;) {
    v = ld8(rs, FLG_OFF + f * 128);
    if (!__any(v.y != P.tag || v.x < want)) break;
    __builtin_amdgcn_s_sleep(SL);
    if (spin_fail(spins, P, lane)) break;
  }
  asm volatile("" ::: "memory");
  return v.x;
}
__device__ __forceinline__ void flag_set(const Ptrs& P, int f, unsigned val, int lane) {
  if (lane == 0) st8(WS_RSRC(P), FLG_OFF + f * 128, (u32x2){val, P.tag});
}
__device__ __forceinline__ void get_tag(Ptrs& P) { P.tag = __builtin_amdgcn_readfirstlane(__hip_atomic_load((gu32*)(P.ws + EPO_OFF), RLX_AGENT)) + 1u; }
__device__ __forceinline__ void setup_wait(const Ptrs& P, int lane) { flag_wait(P, F_LN0(lane), 0, lane); }
__device__ __forceinline__ void clock_wait(unsigned tgt) {
  while ((int)(tgt - (unsigned)__builtin_amdgcn_s_memrealtime()) > 0) {}
}
__device__ __forceinline__ void report(const Ptrs& P, int wid) {
  if (wid == 0 && opq(0) == 0) __hip_atomic_fetch_add((gu32*)(P.ws + CNT_OFF + C_ARR * 128), 1u, RLX_AGENT);
}
__device__ __forceinline__ void kv_wait(const Ptrs& P, int s, int h, int& known, int lane) {
  if (s < known) return;
  const unsigned v = flag_wait(P, F_PRG(min(lane, 2), h), s + 1, lane);
  known = __builtin_amdgcn_readfirstlane(min(min(__builtin_amdgcn_readlane(v, 0), __builtin_amdgcn_readlane(v, 1)), __builtin_amdgcn_readlane(v, 2)));
}
__device__ __forceinline__ int wmax(int v) {
  v = max(v, __builtin_amdgcn_update_dpp(0, v, 0x128, 0xf, 0xf, false));
  v = max(v, __builtin_amdgcn_update_dpp(0, v, 0x124, 0xf, 0xf, false));
  v = max(v, __builtin_amdgcn_update_dpp(0, v, 0x122, 0xf, 0xf, false));
  v = max(v, __builtin_amdgcn_update_dpp(0, v, 0x121, 0xf, 0xf, false));
  return max(max(__builtin_amdgcn_readlane(v, 0), __builtin_amdgcn_readlane(v, 16)), max(__builtin_amdgcn_readlane(v, 32), __builtin_amdgcn_readlane(v, 48)));
}
__device__ __forceinline__ unsigned near_half(unsigned v) { return wmax(-(int)(v & 65535u)) > wmax(-(int)(v >> 16)) ? 8192u : 0u; }
__device__ __forceinline__ void dmaw(rsrc_t rs, unsigned voff, unsigned soff, unsigned ldsaddr) {
  asm volatile("s_mov_b32 m0, %3\n\ts_nop 0\n\tbuffer_load_dwordx4 %0, %1, %2 offen lds" :: "v"(voff), "s"(rs), "s"(soff), "s"(ldsaddr) : "memory");
}
__device__ __forceinline__ void dman(rsrc_t rs, unsigned voff, unsigned soff, unsigned ldsaddr) {
  asm volatile("s_mov_b32 m0, %3\n\ts_nop 0\n\tbuffer_load_dwordx4 %0, %1, %2 offen nt lds" :: "v"(voff), "s"(rs), "s"(soff), "s"(ldsaddr) : "memory");
}
__device__ __forceinline__ void dmas(rsrc_t rs, unsigned voff, unsigned soff, unsigned ldsaddr) {
  asm volatile("s_mov_b32 m0, %3\n\ts_nop 0\n\tbuffer_load_dwordx4 %0, %1, %2 offen sc1 lds" :: "v"(voff), "s"(rs), "s"(soff), "s"(ldsaddr) : "memory");
}
__device__ __forceinline__ void touch(const void* w, unsigned bytes, unsigned voff, LAS unsigned char* lds) {
  asm volatile("s_mov_b32 m0, %2\n\ts_nop 0\n\tbuffer_load_dword %0, %1, 0 offen lds"
               :: "v"(voff), "s"(__builtin_amdgcn_make_buffer_rsrc((void*)w, 0, bytes, RSRC_W3)), "s"((unsigned)(size_t)lds + 148480) : "memory");
}
__device__ __forceinline__ u32x2 trd(LAS unsigned char* p) {
  return __builtin_bit_cast(u32x2, __builtin_amdgcn_ds_read_tr16_b64_v4i16((LAS bf16x4*)p));
}
template <int BO, int UO>
__device__ __forceinline__ u32x8 rd_frag(LAS unsigned char* p) {
  const u32x2 a = trd(p), b = trd(p + UO), c = trd(p + BO), d = trd(p + BO + UO);
  return (u32x8){a.x, a.y, b.x, b.y, c.x, c.y, d.x, d.y};
}

#define TL(i) do {} while (0)
#define TLW(i) do {} while (0)
#define GSDECL
#define GS(i) do {} while (0)
#define GSFLUSH(k, i) do {} while (0)

#define QPRIO 0
#define VPOS 3
#define VLDI "buffer_load_dword v254, v17, %[rs], %[sb] offen sc1\n\tbuffer_load_dword v255, v17, %[rs], %[sb] offen offset:256 sc1\n\t"
#define VLD(p) VLD##p
#define VLD0
#define VLD1
#define VLD2
#define VLD3 VLDI
#define VLD4
#define DEC1 1
#define DEC2 1
#define DGA
#define DGB
#define DGC
#define DGE
#define DGO
#define DGP
#define DGQ
#define PACE "s_sleep 1\n\t"
#define STR_(x) #x
#define STR(x) STR_(x)
#define GSW
#define GSA(k)
#define GSO(k)
#define A_MAIN_Q() \
  asm volatile( \
    "v_mov_b32 v253, %[tag]\n\t" \
    "buffer_load_dwordx4 v[20:23], v12, %[rs], %[vx] offen sc1\n\t" \
    "buffer_load_dwordx4 v[24:27], v12, %[rs], %[vx] offen offset:1024 sc1\n\t" \
    "ds_read_b128 v[236:239], v9 offset:0\n\t" \
    "ds_read_b128 v[240:243], v9 offset:1024\n\t" \
    "ds_read_b128 v[244:247], v9 offset:2048\n\t" \
    "ds_read_b128 v[248:251], v9 offset:3072\n\t" \
    "v_mov_b32 v0, 0\n\t" \
    "v_mov_b32 v1, 0\n\t" \
    "v_mov_b32 v2, 0\n\t" \
    "v_mov_b32 v3, 0\n\t" \
    "v_mov_b32 v4, 0\n\t" \
    "v_mov_b32 v5, 0\n\t" \
    "v_mov_b32 v6, 0\n\t" \
    "v_mov_b32 v7, 0\n\t" \
    "s_cmp_lg_u32 %[sc], 3\n\t" \
    "s_cbranch_scc1 PB_%=\n\t" \
    "s_getpc_b64 s[92:93]\n\t" \
    "TPA_%=:\n\t" \
    "s_add_u32 s92, s92, TEA_%=-TPA_%=\n\t" \
    "s_addc_u32 s93, s93, 0\n\t" \
    "s_lshr_b32 s91, %[tgt], 2\n\t" \
    "s_sub_u32 s91, s91, 3\n\t" \
    "TWA_%=:\n\t" \
    "s_memrealtime s[88:89]\n\t" \
    DGE \
    "s_waitcnt lgkmcnt(0)\n\t" \
    "s_sub_u32 s89, s91, s88\n\t" \
    "s_lshl_b32 s89, s89, 2\n\t" \
    "s_cmp_gt_i32 s89, 0\n\t" \
    "s_cbranch_scc1 TWA_%=\n\t" \
    "s_lshl_b32 s88, s88, 2\n\t" \
    "s_sub_u32 s89, %[tgt], s88\n\t" \
    "s_max_i32 s89, s89, 0\n\t" \
    "s_mul_i32 s89, s89, 11\n\t" \
    "s_lshr_b32 s89, s89, 1\n\t" \
    DGB \
    "s_and_b32 s89, s89, 0x7c\n\t" \
    "s_sub_u32 s92, s92, s89\n\t" \
    "s_subb_u32 s93, s93, 0\n\t" \
    "s_setpc_b64 s[92:93]\n\t" \
    "s_nop 0\n\t" \
    "s_nop 0\n\t" \
    "s_nop 0\n\t" \
    "s_nop 0\n\t" \
    "s_nop 0\n\t" \
    "s_nop 0\n\t" \
    "s_nop 0\n\t" \
    "s_nop 0\n\t" \
    "s_nop 0\n\t" \
    "s_nop 0\n\t" \
    "s_nop 0\n\t" \
    "s_nop 0\n\t" \
    "s_nop 0\n\t" \
    "s_nop 0\n\t" \
    "s_nop 0\n\t" \
    "s_nop 0\n\t" \
    "s_nop 0\n\t" \
    "s_nop 0\n\t" \
    "s_nop 0\n\t" \
    "s_nop 0\n\t" \
    "s_nop 0\n\t" \
    "s_nop 0\n\t" \
    "TEA_%=:\n\t" \
    "PB_%=:\n\t" \
    "s_waitcnt lgkmcnt(0)\n\t" \
    "s_barrier\n\t" \
    "buffer_load_dwordx4 v[220:223], v8, %[rs], %[m0] offen sc1\n\t" \
    "buffer_load_dwordx4 v[224:227], v8, %[rs], %[m1] offen sc1\n\t" \
    "PM_%=:\n\t" \
    "s_waitcnt vmcnt(1)\n\t" \
    "ds_write_b128 v8, v[220:223] offset:0\n\t" \
    "v_max3_u32 v252, v220, v221, v222\n\t" \
    "s_waitcnt vmcnt(0)\n\t" \
    "ds_write_b128 v8, v[224:227] offset:4096\n\t" \
    "v_max3_u32 v252, v252, v223, v224\n\t" \
    "v_max3_u32 v252, v252, v225, v226\n\t" \
    "v_max_u32 v252, v252, v227\n\t" \
    "v_cmp_eq_u32 vcc, -1, v252\n\t" \
    "s_cbranch_vccz PD_%=\n\t" \
    "s_sub_u32 %[lim], %[lim], 1\n\t" \
    "s_cmp_gt_i32 %[lim], 0\n\t" \
    "s_cbranch_scc0 PD_%=\n\t" \
    "s_sleep " STR(CADM) "\n\t" \
    "buffer_load_dwordx4 v[220:223], v8, %[rs], %[m0] offen sc1\n\t" \
    "buffer_load_dwordx4 v[224:227], v8, %[rs], %[m1] offen sc1\n\t" \
    "s_branch PM_%=\n\t" \
    "PD_%=:\n\t" \
    GSA(0) \
    "v_smfmac_f32_16x16x64_bf16 v[0:3], v[220:223], v[236:243], v10\n\t" \
    "v_xor_b32 v9, 16384, v9\n\t" \
    "ds_read_b128 v[236:239], v9 offset:0\n\t" \
    "ds_read_b128 v[240:243], v9 offset:1024\n\t" \
    "v_smfmac_f32_16x16x64_bf16 v[4:7], v[220:223], v[244:251], v10\n\t" \
    "ds_read_b128 v[244:247], v9 offset:2048\n\t" \
    "ds_read_b128 v[248:251], v9 offset:3072\n\t" \
    "s_waitcnt lgkmcnt(4)\n\t" \
    "s_barrier\n\t" \
    "ds_read_b128 v[228:231], v8 offset:8192\n\t" \
    "ds_read_b128 v[232:235], v8 offset:12288\n\t" \
    "v_xor_b32 v8, 1024, v8\n\t" \
    "ds_read_b128 v[220:223], v8 offset:4096\n\t" \
    "v_smfmac_f32_16x16x64_bf16 v[0:3], v[224:227], v[28:35], v10\n\t" \
    "v_smfmac_f32_16x16x64_bf16 v[4:7], v[224:227], v[36:43], v10\n\t" \
    "ds_read_b128 v[224:227], v8 offset:0\n\t" \
    "s_waitcnt lgkmcnt(3)\n\t" \
    "v_smfmac_f32_16x16x64_bf16 v[0:3], v[228:231], v[92:99], v10\n\t" \
    "v_smfmac_f32_16x16x64_bf16 v[4:7], v[228:231], v[100:107], v10\n\t" \
    "ds_read_b128 v[228:231], v8 offset:8192\n\t" \
    "s_waitcnt lgkmcnt(3)\n\t" \
    "v_smfmac_f32_16x16x64_bf16 v[0:3], v[232:235], v[156:163], v10\n\t" \
    "v_smfmac_f32_16x16x64_bf16 v[4:7], v[232:235], v[164:171], v10\n\t" \
    "ds_read_b128 v[232:235], v8 offset:12288\n\t" \
    "s_waitcnt lgkmcnt(3)\n\t" \
    "v_smfmac_f32_16x16x64_bf16 v[0:3], v[220:223], v[44:51], v10\n\t" \
    "v_smfmac_f32_16x16x64_bf16 v[4:7], v[220:223], v[52:59], v10\n\t" \
    "v_xor_b32 v8, 3072, v8\n\t" \
    "ds_read_b128 v[220:223], v8 offset:4096\n\t" \
    "s_waitcnt lgkmcnt(3)\n\t" \
    "v_smfmac_f32_16x16x64_bf16 v[0:3], v[224:227], v[236:243], v10\n\t" \
    "v_xor_b32 v9, 49152, v9\n\t" \
    "ds_read_b128 v[236:239], v9 offset:0\n\t" \
    "v_smfmac_f32_16x16x64_bf16 v[4:7], v[224:227], v[244:251], v10\n\t" \
    "ds_read_b128 v[240:243], v9 offset:1024\n\t" \
    "s_waitcnt lgkmcnt(4)\n\t" \
    "v_smfmac_f32_16x16x64_bf16 v[0:3], v[228:231], v[108:115], v10\n\t" \
    "ds_read_b128 v[224:227], v8 offset:8192\n\t" \
    "v_smfmac_f32_16x16x64_bf16 v[4:7], v[228:231], v[116:123], v10\n\t" \
    "ds_read_b128 v[244:247], v9 offset:2048\n\t" \
    "s_waitcnt lgkmcnt(5)\n\t" \
    "v_smfmac_f32_16x16x64_bf16 v[0:3], v[232:235], v[172:179], v10\n\t" \
    "ds_read_b128 v[228:231], v8 offset:0\n\t" \
    "v_smfmac_f32_16x16x64_bf16 v[4:7], v[232:235], v[180:187], v10\n\t" \
    "ds_read_b128 v[248:251], v9 offset:3072\n\t" \
    "s_waitcnt lgkmcnt(6)\n\t" \
    "v_smfmac_f32_16x16x64_bf16 v[0:3], v[220:223], v[60:67], v10\n\t" \
    "ds_read_b128 v[232:235], v8 offset:12288\n\t" \
    "v_smfmac_f32_16x16x64_bf16 v[4:7], v[220:223], v[68:75], v10\n\t" \
    "v_xor_b32 v8, 1024, v8\n\t" \
    "ds_read_b128 v[220:223], v8 offset:4096\n\t" \
    "s_waitcnt lgkmcnt(5)\n\t" \
    "v_smfmac_f32_16x16x64_bf16 v[0:3], v[224:227], v[124:131], v10\n\t" \
    "v_smfmac_f32_16x16x64_bf16 v[4:7], v[224:227], v[132:139], v10\n\t" \
    "ds_read_b128 v[224:227], v8 offset:8192\n\t" \
    "s_waitcnt lgkmcnt(3)\n\t" \
    "v_smfmac_f32_16x16x64_bf16 v[0:3], v[228:231], v[236:243], v10\n\t" \
    "v_xor_b32 v9, 16384, v9\n\t" \
    "ds_read_b128 v[236:239], v9 offset:0\n\t" \
    "v_smfmac_f32_16x16x64_bf16 v[4:7], v[228:231], v[244:251], v10\n\t" \
    "ds_read_b128 v[240:243], v9 offset:1024\n\t" \
    "s_waitcnt lgkmcnt(4)\n\t" \
    "v_smfmac_f32_16x16x64_bf16 v[0:3], v[232:235], v[188:195], v10\n\t" \
    "ds_read_b128 v[228:231], v8 offset:12288\n\t" \
    "v_smfmac_f32_16x16x64_bf16 v[4:7], v[232:235], v[196:203], v10\n\t" \
    "ds_read_b128 v[244:247], v9 offset:2048\n\t" \
    "s_waitcnt lgkmcnt(5)\n\t" \
    "v_smfmac_f32_16x16x64_bf16 v[0:3], v[220:223], v[76:83], v10\n\t" \
    "ds_read_b128 v[232:235], v8 offset:0\n\t" \
    "v_xor_b32 v8, 3072, v8\n\t" \
    "v_smfmac_f32_16x16x64_bf16 v[4:7], v[220:223], v[84:91], v10\n\t" \
    "ds_read_b128 v[248:251], v9 offset:3072\n\t" \
    "s_waitcnt lgkmcnt(6)\n\t" \
    "v_smfmac_f32_16x16x64_bf16 v[0:3], v[224:227], v[140:147], v10\n\t" \
    "ds_read_b128 v[220:223], v12 offset:24576\n\t" \
    "v_smfmac_f32_16x16x64_bf16 v[4:7], v[224:227], v[148:155], v10\n\t" \
    "ds_read_b128 v[224:227], v12 offset:28672\n\t" \
    "s_waitcnt lgkmcnt(5)\n\t" \
    "v_smfmac_f32_16x16x64_bf16 v[0:3], v[228:231], v[204:211], v10\n\t" \
    "v_smfmac_f32_16x16x64_bf16 v[4:7], v[228:231], v[212:219], v10\n\t" \
    "ds_read_b128 v[228:231], v12 offset:25600\n\t" \
    "s_waitcnt lgkmcnt(3)\n\t" \
    "v_smfmac_f32_16x16x64_bf16 v[0:3], v[232:235], v[236:243], v10\n\t" \
    "v_xor_b32 v9, 49152, v9\n\t" \
    "v_smfmac_f32_16x16x64_bf16 v[4:7], v[232:235], v[244:251], v10\n\t" \
    VLD(4) \
    "s_nop 5\n\t" \
    "v_add_f32 v0, v0, v1\n\t" \
    "v_add_f32 v2, v2, v3\n\t" \
    "v_add_f32 v4, v4, v5\n\t" \
    "v_add_f32 v6, v6, v7\n\t" \
    "v_mul_f32 v0, %[sl], v0\n\t" \
    "v_mul_f32 v2, %[sl], v2\n\t" \
    "v_mul_f32 v4, %[sl], v4\n\t" \
    "v_mul_f32 v6, %[sl], v6\n\t" \
    "v_cvt_pk_bf16_f32 v0, v0, v4\n\t" \
    "v_cvt_pk_bf16_f32 v2, v2, v6\n\t" \
    "ds_write2_b32 v11, v0, v2 offset0:0 offset1:4\n\t" \
    "s_mov_b64 exec, %[onej]\n\t" \
    "ds_write_b32 v16, v253\n\t" \
    "s_mov_b64 exec, -1\n\t" \
    GSA(1) \
    "s_setprio 0\n\t" \
    "ds_read_b128 v[232:235], v12 offset:29696\n\t" \
    "s_nop 1\n\t" \
    "ds_read_b128 v[0:3], v12 offset:26624\n\t" \
    "s_nop 1\n\t" \
    "ds_read_b128 v[4:7], v12 offset:27648\n\t" \
    "s_nop 1\n\t" \
    "ds_read_b32 v252, v16\n\t" \
    "ds_read_b128 v[236:239], v14 offset:0\n\t" \
    "s_nop 1\n\t" \
    "ds_read_b128 v[240:243], v14 offset:512\n\t" \
    "s_nop 1\n\t" \
    "ds_read_b128 v[244:247], v14 offset:1024\n\t" \
    "s_nop 1\n\t" \
    "s_setprio 2\n\t" \
    VLD(3) \
    "s_setprio 0\n\t" \
    "ds_read_b128 v[248:251], v14 offset:1536\n\t" \
    "s_nop 1\n\t" \
    "s_setprio 2\n\t" \
    "s_waitcnt lgkmcnt(4)\n\t" \
    "v_cmp_ne_u32 vcc, v253, v252\n\t" \
    "s_cbranch_vccnz RP_%=\n\t" \
    "CP_%=:\n\t" \
    "s_setprio 2\n\t" \
    VLD(0) \
    "s_waitcnt lgkmcnt(2)\n\t" \
    "v_mfma_f32_16x16x32_bf16 v[220:223], v[220:223], v[236:239], 0\n\t" \
    "v_mfma_f32_16x16x32_bf16 v[224:227], v[224:227], v[236:239], 0\n\t" \
    "v_mfma_f32_16x16x32_bf16 v[220:223], v[228:231], v[240:243], v[220:223]\n\t" \
    "ds_read_b128 v[228:231], v12 offset:30720\n\t" \
    "v_mfma_f32_16x16x32_bf16 v[224:227], v[232:235], v[240:243], v[224:227]\n\t" \
    "ds_read_b128 v[232:235], v12 offset:31744\n\t" \
    "s_setprio " STR(QPRIO) "\n\t" \
    VLD(1) \
    "s_waitcnt lgkmcnt(2)\n\t" \
    "v_mfma_f32_16x16x32_bf16 v[220:223], v[0:3], v[244:247], v[220:223]\n\t" \
    "v_mfma_f32_16x16x32_bf16 v[220:223], v[4:7], v[248:251], v[220:223]\n\t" \
    "s_waitcnt lgkmcnt(1)\n\t" \
    "v_mfma_f32_16x16x32_bf16 v[224:227], v[228:231], v[244:247], v[224:227]\n\t" \
    "s_waitcnt lgkmcnt(0)\n\t" \
    "v_mfma_f32_16x16x32_bf16 v[224:227], v[232:235], v[248:251], v[224:227]\n\t" \
    GSA(2) \
    "s_setprio 2\n\t" \
    "s_nop 2\n\t" \
    "v_exp_f32 v220, v220\n\t" \
    "v_exp_f32 v221, v221\n\t" \
    "v_exp_f32 v222, v222\n\t" \
    "v_exp_f32 v223, v223\n\t" \
    "v_exp_f32 v224, v224\n\t" \
    "v_exp_f32 v225, v225\n\t" \
    "v_exp_f32 v226, v226\n\t" \
    "v_exp_f32 v227, v227\n\t" \
    "ds_read_b32 v252, v16 offset:16\n\t" \
    "ds_read_b128 v[0:3], v12 offset:22528\n\t" \
    "ds_read_b128 v[4:7], v12 offset:23552\n\t" \
    VLD(2) \
    "v_cvt_pk_bf16_f32 v220, v220, v221\n\t" \
    "v_cvt_pk_bf16_f32 v221, v222, v223\n\t" \
    "v_cvt_pk_bf16_f32 v222, v224, v225\n\t" \
    "v_cvt_pk_bf16_f32 v223, v226, v227\n\t" \
    "v_mov_b32 v224, 0\n\t" \
    "v_mov_b32 v225, 0\n\t" \
    "v_mfma_f32_16x16x32_bf16 v[228:231], v[20:23], v[220:223], 0\n\t" \
    "v_mov_b32 v226, 0\n\t" \
    "v_mov_b32 v227, 0\n\t" \
    "v_mfma_f32_16x16x32_bf16 v[232:235], v[24:27], v[220:223], 0\n\t" \
    "s_waitcnt lgkmcnt(2)\n\t" \
    "v_cmp_ne_u32 vcc, %[tag], v252\n\t" \
    "s_cbranch_vccnz RK_%=\n\t" \
    "CK_%=:\n\t" \
    "s_waitcnt lgkmcnt(1)\n\t" \
    "v_smfmac_f32_16x16x64_bf16 v[224:227], v[0:3], v[236:243], v10\n\t" \
    "s_waitcnt lgkmcnt(0)\n\t" \
    "v_smfmac_f32_16x16x64_bf16 v[224:227], v[4:7], v[244:251], v10\n\t" \
    "v_mov_b32 v236, %[one]\n\t" \
    "v_mov_b32 v237, %[one]\n\t" \
    "v_mov_b32 v238, %[one]\n\t" \
    "v_mov_b32 v239, %[one]\n\t" \
    "v_mov_b32 v1, 0\n\t" \
    "v_mov_b32 v2, 0\n\t" \
    "v_mfma_f32_16x16x32_bf16 v[240:243], v[236:239], v[220:223], 0\n\t" \
    "s_nop 0\n\t" \
    "v_add_f32 v4, v224, v225\n\t" \
    "v_add_f32 v5, v226, v227\n\t" \
    "v_exp_f32 v4, v4\n\t" \
    "v_exp_f32 v5, v5\n\t" \
    "s_nop 0\n\t" \
    "v_cvt_pk_bf16_f32 v0, v4, v5\n\t" \
    "v_cvt_pk_bf16_f32 v3, v4, v5\n\t" \
    GSA(3) \
    "CM_%=:\n\t" \
    "s_nop 0\n\t" \
    "v_mfma_f32_16x16x16_bf16 v[240:243], v[236:237], v[0:1], v[240:243]\n\t" \
    "s_waitcnt vmcnt(0)\n\t" \
    "v_mfma_f32_16x16x16_bf16 v[228:231], v[254:255], v[0:1], v[228:231]\n\t" \
    "v_max_u32 v7, v254, v255\n\t" \
    "v_mfma_f32_16x16x16_bf16 v[232:235], v[254:255], v[2:3], v[232:235]\n\t" \
    "v_cmp_eq_u32 s[88:89], -1, v7\n\t" \
    "s_not_b64 exec, %[hi]\n\t" \
    "s_nop 1\n\t" \
    "v_rcp_f32 v252, v240\n\t" \
    "v_subrev_u32 v253, 0x21800000, v240\n\t" \
    "v_cmp_gt_u32 vcc, v253, %[rg]\n\t" \
    "v_pk_mul_f32 v[228:229], v[228:229], v[252:253] op_sel_hi:[1,0]\n\t" \
    "v_pk_mul_f32 v[230:231], v[230:231], v[252:253] op_sel_hi:[1,0]\n\t" \
    "v_pk_mul_f32 v[232:233], v[232:233], v[252:253] op_sel_hi:[1,0]\n\t" \
    "v_pk_mul_f32 v[234:235], v[234:235], v[252:253] op_sel_hi:[1,0]\n\t" \
    "s_or_b64 vcc, vcc, s[88:89]\n\t" \
    "v_cvt_pk_bf16_f32 v4, v228, v229\n\t" \
    "v_cvt_pk_bf16_f32 v5, v230, v231\n\t" \
    "v_cvt_pk_bf16_f32 v6, v232, v233\n\t" \
    "v_cvt_pk_bf16_f32 v7, v234, v235\n\t" \
    "s_cbranch_scc1 FX_%=\n\t" \
    "buffer_store_dwordx4 v[4:7], v18, %[rs], %[sb] offen sc1\n\t" \
    "s_memrealtime %[tp]\n\t" \
    DGA \
    GSA(4) \
    "v_max3_u32 v252, v4, v5, v6\n\t" \
    "v_max_u32 v252, v252, v7\n\t" \
    "v_cmp_eq_u32 vcc, -1, v252\n\t" \
    "s_cbranch_vccnz RC_%=\n\t" \
    "CC_%=:\n\t" \
    "s_mov_b64 exec, -1\n\t" \
    "v_add_u32 v252, %[vd], v12\n\t" \
    "v_add_u32 v253, %[md], v17\n\t" \
    "ds_write_b128 v252, v[20:23]\n\t" \
    "ds_write_b128 v252, v[24:27] offset:1024\n\t" \
    "ds_write2st64_b32 v253, v254, v255 offset0:0 offset1:1\n\t" \
    "s_waitcnt lgkmcnt(0)\n\t" \
    "s_cmp_lg_u32 %[sc], 3\n\t" \
    "s_cbranch_scc1 NH_%=\n\t" \
    "buffer_load_dwordx2 v[0:1], off, %[rs], %[fl] sc1\n\t" \
    "s_barrier\n\t" \
    "s_waitcnt vmcnt(0)\n\t" \
    "v_readfirstlane_b32 %[pt], v0\n\t" \
    "v_readfirstlane_b32 %[pd], v1\n\t" \
    "s_nop 3\n\t" \
    "s_branch E_%=\n\t" \
    "NH_%=:\n\t" \
    "s_barrier\n\t" \
    "s_branch E_%=\n\t" \
    "RP_%=:\n\t" \
    "s_waitcnt lgkmcnt(0)\n\t" \
    "ds_read_b32 v252, v16\n\t" \
    "ds_read_b128 v[236:239], v14 offset:0\n\t" \
    "ds_read_b128 v[240:243], v14 offset:512\n\t" \
    "ds_read_b128 v[244:247], v14 offset:1024\n\t" \
    "ds_read_b128 v[248:251], v14 offset:1536\n\t" \
    "s_sub_u32 %[lim], %[lim], " STR(DEC1) "\n\t" \
    "s_cmp_gt_i32 %[lim], 0\n\t" \
    "s_waitcnt lgkmcnt(4)\n\t" \
    "s_cbranch_scc0 CP_%=\n\t" \
    "v_cmp_ne_u32 vcc, %[tag], v252\n\t" \
    "s_cbranch_vccnz RP_%=\n\t" \
    "s_branch CP_%=\n\t" \
    "RK_%=:\n\t" \
    "s_waitcnt lgkmcnt(0)\n\t" \
    "ds_read_b32 v252, v16 offset:16\n\t" \
    "ds_read_b128 v[0:3], v12 offset:22528\n\t" \
    "ds_read_b128 v[4:7], v12 offset:23552\n\t" \
    "s_sub_u32 %[lim], %[lim], " STR(DEC1) "\n\t" \
    "s_cmp_gt_i32 %[lim], 0\n\t" \
    "s_waitcnt lgkmcnt(2)\n\t" \
    "s_cbranch_scc0 CK_%=\n\t" \
    "v_cmp_ne_u32 vcc, %[tag], v252\n\t" \
    "s_cbranch_vccnz RK_%=\n\t" \
    "s_branch CK_%=\n\t" \
    "RC_%=:\n\t" \
    "v_min_u32 v4, -2, v4\n\t" \
    "v_min_u32 v5, -2, v5\n\t" \
    "v_min_u32 v6, -2, v6\n\t" \
    "v_min_u32 v7, -2, v7\n\t" \
    "buffer_store_dwordx4 v[4:7], v18, %[rs], %[sb] offen sc1\n\t" \
    "s_branch CC_%=\n\t" \
    "FX_%=:\n\t" \
    "s_mov_b64 exec, -1\n\t" \
    "s_cmp_eq_u64 s[88:89], 0\n\t" \
    "s_cbranch_scc1 RM_%=\n\t" \
    "buffer_load_dword v254, v17, %[rs], %[sb] offen sc1\n\t" \
    "buffer_load_dword v255, v17, %[rs], %[sb] offen offset:256 sc1\n\t" \
    "v_mfma_f32_16x16x32_bf16 v[228:231], v[20:23], v[220:223], 0\n\t" \
    "v_mfma_f32_16x16x32_bf16 v[232:235], v[24:27], v[220:223], 0\n\t" \
    "v_mfma_f32_16x16x32_bf16 v[240:243], v[236:239], v[220:223], 0\n\t" \
    "s_sub_u32 %[lim], %[lim], " STR(DEC2) "\n\t" \
    "s_cmp_gt_i32 %[lim], 0\n\t" \
    "s_cbranch_scc1 CM_%=\n\t" \
    "s_waitcnt vmcnt(0)\n\t" \
    "v_mov_b32 v254, 0\n\t" \
    "v_mov_b32 v255, 0\n\t" \
    "s_branch CM_%=\n\t" \
    "RM_%=:\n\t" \
    "s_mov_b32 %[rg], -1\n\t" \
    "s_nop 7\n\t" \
    "v_add_f32 v4, v224, v225\n\t" \
    "v_add_f32 v5, v226, v227\n\t" \
    "ds_read_b128 v[236:239], v14 offset:0\n\t" \
    "ds_read_b128 v[240:243], v14 offset:512\n\t" \
    "ds_read_b128 v[244:247], v14 offset:1024\n\t" \
    "ds_read_b128 v[248:251], v14 offset:1536\n\t" \
    "ds_read_b128 v[232:235], v12 offset:24576\n\t" \
    "s_waitcnt lgkmcnt(0)\n\t" \
    "v_mfma_f32_16x16x32_bf16 v[220:223], v[232:235], v[236:239], 0\n\t" \
    "ds_read_b128 v[232:235], v12 offset:25600\n\t" \
    "s_waitcnt lgkmcnt(0)\n\t" \
    "v_mfma_f32_16x16x32_bf16 v[220:223], v[232:235], v[240:243], v[220:223]\n\t" \
    "ds_read_b128 v[232:235], v12 offset:26624\n\t" \
    "s_waitcnt lgkmcnt(0)\n\t" \
    "v_mfma_f32_16x16x32_bf16 v[220:223], v[232:235], v[244:247], v[220:223]\n\t" \
    "ds_read_b128 v[232:235], v12 offset:27648\n\t" \
    "s_waitcnt lgkmcnt(0)\n\t" \
    "v_mfma_f32_16x16x32_bf16 v[220:223], v[232:235], v[248:251], v[220:223]\n\t" \
    "ds_read_b128 v[232:235], v12 offset:28672\n\t" \
    "s_waitcnt lgkmcnt(0)\n\t" \
    "v_mfma_f32_16x16x32_bf16 v[224:227], v[232:235], v[236:239], 0\n\t" \
    "ds_read_b128 v[232:235], v12 offset:29696\n\t" \
    "s_waitcnt lgkmcnt(0)\n\t" \
    "v_mfma_f32_16x16x32_bf16 v[224:227], v[232:235], v[240:243], v[224:227]\n\t" \
    "ds_read_b128 v[232:235], v12 offset:30720\n\t" \
    "s_waitcnt lgkmcnt(0)\n\t" \
    "v_mfma_f32_16x16x32_bf16 v[224:227], v[232:235], v[244:247], v[224:227]\n\t" \
    "ds_read_b128 v[232:235], v12 offset:31744\n\t" \
    "s_waitcnt lgkmcnt(0)\n\t" \
    "v_mfma_f32_16x16x32_bf16 v[224:227], v[232:235], v[248:251], v[224:227]\n\t" \
    "v_max3_f32 v252, v220, v221, v222\n\t" \
    "s_nop 6\n\t" \
    "v_max3_f32 v253, v223, v224, v225\n\t" \
    "v_max3_f32 v252, v252, v226, v227\n\t" \
    "v_max3_f32 v253, v253, v4, v5\n\t" \
    "v_max_f32 v252, v252, v253\n\t" \
    "v_mov_b32 v253, v252\n\t" \
    "v_mov_b32 v6, v252\n\t" \
    "s_nop 1\n\t" \
    "v_permlane16_swap_b32 v253, v6\n\t" \
    "v_max_f32 v252, v253, v6\n\t" \
    "v_max_f32 v253, v253, v6\n\t" \
    "s_nop 1\n\t" \
    "v_permlane32_swap_b32 v252, v253\n\t" \
    "v_max_f32 v252, v252, v253\n\t" \
    "v_sub_f32 v220, v220, v252\n\t" \
    "v_sub_f32 v221, v221, v252\n\t" \
    "v_sub_f32 v222, v222, v252\n\t" \
    "v_sub_f32 v223, v223, v252\n\t" \
    "v_sub_f32 v224, v224, v252\n\t" \
    "v_sub_f32 v225, v225, v252\n\t" \
    "v_sub_f32 v226, v226, v252\n\t" \
    "v_sub_f32 v227, v227, v252\n\t" \
    "v_sub_f32 v4, v4, v252\n\t" \
    "v_sub_f32 v5, v5, v252\n\t" \
    "v_exp_f32 v220, v220\n\t" \
    "v_exp_f32 v221, v221\n\t" \
    "v_exp_f32 v222, v222\n\t" \
    "v_exp_f32 v223, v223\n\t" \
    "v_exp_f32 v224, v224\n\t" \
    "v_exp_f32 v225, v225\n\t" \
    "v_exp_f32 v226, v226\n\t" \
    "v_exp_f32 v227, v227\n\t" \
    "v_cvt_pk_bf16_f32 v220, v220, v221\n\t" \
    "v_cvt_pk_bf16_f32 v221, v222, v223\n\t" \
    "v_cvt_pk_bf16_f32 v222, v224, v225\n\t" \
    "v_cvt_pk_bf16_f32 v223, v226, v227\n\t" \
    "v_mov_b32 v236, %[one]\n\t" \
    "v_mov_b32 v237, %[one]\n\t" \
    "v_mov_b32 v238, %[one]\n\t" \
    "v_mov_b32 v239, %[one]\n\t" \
    "v_mfma_f32_16x16x32_bf16 v[228:231], v[20:23], v[220:223], 0\n\t" \
    "v_mfma_f32_16x16x32_bf16 v[232:235], v[24:27], v[220:223], 0\n\t" \
    "v_mfma_f32_16x16x32_bf16 v[240:243], v[236:239], v[220:223], 0\n\t" \
    "v_exp_f32 v4, v4\n\t" \
    "v_exp_f32 v5, v5\n\t" \
    "s_nop 0\n\t" \
    "v_cvt_pk_bf16_f32 v0, v4, v5\n\t" \
    "v_cvt_pk_bf16_f32 v3, v4, v5\n\t" \
    "s_nop 1\n\t" \
    "s_branch CM_%=\n\t" \
    "E_%=:\n\t" \
    GSW \
    "s_nop 1\n\t" \
    : [lim] "+s"(lim), [rg] "+s"(rg), [tp] "=&s"(tp), [pt] "=&s"(pt), [pd] "=&s"(pd) DGO DGP GSO(0) GSO(1) GSO(2) GSO(3) GSO(4) \
    : "{v8}"(am_a), "{v9}"(t0_a), "{v10}"(idx), "{v11}"(iw_a), "{v12}"(ab_a), "{v14}"(qr_a), "{v16}"(fr_a), "{v17}"(vo_v), "{v18}"(oo_v), [rs] "s"(rs), [sb] "s"(sb), [tag] "s"(0xffffff81u + s), [hi] "s"(0xff00ff00ff00ff00ull), [tgt] "s"(tgt), [sc] "s"(wid), [onej] "s"(onej), [sl] "s"(0x3e0293ee), [one] "s"(0x3f803f80), [m0] "s"(sbm), [m1] "s"(sbm + 4096u), [vx] "s"(vxo), [fl] "s"(fb + FL_T(s + 1)), [vd] "s"(8192 + j * 2048), [md] "s"(md), "{v[28:35]}"(wr[0][0]), "{v[36:43]}"(wr[0][1]), "{v[44:51]}"(wr[1][0]), "{v[52:59]}"(wr[1][1]), "{v[60:67]}"(wr[2][0]), "{v[68:75]}"(wr[2][1]), "{v[76:83]}"(wr[3][0]), "{v[84:91]}"(wr[3][1]), "{v[92:99]}"(wr[4][0]), "{v[100:107]}"(wr[4][1]), "{v[108:115]}"(wr[5][0]), "{v[116:123]}"(wr[5][1]), "{v[124:131]}"(wr[6][0]), "{v[132:139]}"(wr[6][1]), "{v[140:147]}"(wr[7][0]), "{v[148:155]}"(wr[7][1]), "{v[156:163]}"(wr[8][0]), "{v[164:171]}"(wr[8][1]), "{v[172:179]}"(wr[9][0]), "{v[180:187]}"(wr[9][1]), "{v[188:195]}"(wr[10][0]), "{v[196:203]}"(wr[10][1]), "{v[204:211]}"(wr[11][0]), "{v[212:219]}"(wr[11][1]) \
    : "memory", "vcc", "scc", "s88", "s89", "s90", "s91", "s92", "s93", "s94", "s95", "v0", "v1", "v2", "v3", "v4", "v5", "v6", "v7", "v20", "v21", "v22", "v23", "v24", "v25", "v26", "v27", "v220", "v221", "v222", "v223", "v224", "v225", "v226", "v227", "v228", "v229", "v230", "v231", "v232", "v233", "v234", "v235", "v236", "v237", "v238", "v239", "v240", "v241", "v242", "v243", "v244", "v245", "v246", "v247", "v248", "v249", "v250", "v251", "v252", "v253", "v254", "v255")

#define A_MAIN_K() \
  asm volatile( \
    "ds_read_b128 v[236:239], v9 offset:0\n\t" \
    "ds_read_b128 v[240:243], v9 offset:1024\n\t" \
    "ds_read_b128 v[244:247], v9 offset:2048\n\t" \
    "ds_read_b128 v[248:251], v9 offset:3072\n\t" \
    "v_mov_b32 v0, 0\n\t" \
    "v_mov_b32 v1, 0\n\t" \
    "v_mov_b32 v2, 0\n\t" \
    "v_mov_b32 v3, 0\n\t" \
    "v_mov_b32 v4, 0\n\t" \
    "v_mov_b32 v5, 0\n\t" \
    "v_mov_b32 v6, 0\n\t" \
    "v_mov_b32 v7, 0\n\t" \
    "s_waitcnt lgkmcnt(0)\n\t" \
    "s_barrier\n\t" \
    "buffer_load_dwordx4 v[220:223], v8, %[rs], %[m0] offen sc1\n\t" \
    "buffer_load_dwordx4 v[224:227], v8, %[rs], %[m1] offen sc1\n\t" \
    GSA(3) \
    "v_mov_b32 v252, v252\n\t" \
    "s_nop 2\n\t" \
    "v_mov_b32 v252, v252\n\t" \
    "s_nop 2\n\t" \
    "v_mov_b32 v252, v252\n\t" \
    "s_nop 2\n\t" \
    "v_mov_b32 v252, v252\n\t" \
    "s_nop 2\n\t" \
    "v_mov_b32 v252, v252\n\t" \
    "s_nop 2\n\t" \
    "v_mov_b32 v252, v252\n\t" \
    "s_nop 2\n\t" \
    "v_mov_b32 v252, v252\n\t" \
    "s_nop 2\n\t" \
    "v_mov_b32 v252, v252\n\t" \
    "s_nop 2\n\t" \
    "v_mov_b32 v252, v252\n\t" \
    "s_nop 2\n\t" \
    "v_mov_b32 v252, v252\n\t" \
    "s_nop 2\n\t" \
    "v_mov_b32 v252, v252\n\t" \
    "s_nop 2\n\t" \
    "v_mov_b32 v252, v252\n\t" \
    "s_nop 2\n\t" \
    "v_mov_b32 v252, v252\n\t" \
    "s_nop 2\n\t" \
    "v_mov_b32 v252, v252\n\t" \
    "s_nop 2\n\t" \
    "v_mov_b32 v252, v252\n\t" \
    "s_nop 2\n\t" \
    "v_mov_b32 v252, v252\n\t" \
    "s_nop 2\n\t" \
    "v_mov_b32 v252, v252\n\t" \
    "s_nop 2\n\t" \
    "v_mov_b32 v252, v252\n\t" \
    "s_nop 2\n\t" \
    "v_mov_b32 v252, v252\n\t" \
    "s_nop 2\n\t" \
    "v_mov_b32 v252, v252\n\t" \
    "s_nop 2\n\t" \
    "v_mov_b32 v252, v252\n\t" \
    "s_nop 2\n\t" \
    "v_mov_b32 v252, v252\n\t" \
    "s_nop 2\n\t" \
    "v_mov_b32 v252, v252\n\t" \
    "s_nop 2\n\t" \
    "v_mov_b32 v252, v252\n\t" \
    "s_nop 2\n\t" \
    "v_mov_b32 v252, v252\n\t" \
    "s_nop 2\n\t" \
    "v_mov_b32 v252, v252\n\t" \
    "s_nop 2\n\t" \
    "v_mov_b32 v252, v252\n\t" \
    "s_nop 2\n\t" \
    "v_mov_b32 v252, v252\n\t" \
    "s_nop 2\n\t" \
    "v_mov_b32 v252, v252\n\t" \
    "s_nop 2\n\t" \
    "v_mov_b32 v252, v252\n\t" \
    "s_nop 2\n\t" \
    "PM_%=:\n\t" \
    "s_waitcnt vmcnt(1)\n\t" \
    "ds_write_b128 v8, v[220:223] offset:8192\n\t" \
    "v_max3_u32 v252, v220, v221, v222\n\t" \
    "s_waitcnt vmcnt(0)\n\t" \
    "ds_write_b128 v8, v[224:227] offset:12288\n\t" \
    "v_max3_u32 v252, v252, v223, v224\n\t" \
    "v_max3_u32 v252, v252, v225, v226\n\t" \
    "v_max_u32 v252, v252, v227\n\t" \
    "v_cmp_eq_u32 vcc, -1, v252\n\t" \
    "s_cbranch_vccz PD_%=\n\t" \
    "s_sub_u32 %[lim], %[lim], 1\n\t" \
    "s_cmp_gt_i32 %[lim], 0\n\t" \
    "s_cbranch_scc0 PD_%=\n\t" \
    "s_sleep " STR(CADM) "\n\t" \
    "buffer_load_dwordx4 v[220:223], v8, %[rs], %[m0] offen sc1\n\t" \
    "buffer_load_dwordx4 v[224:227], v8, %[rs], %[m1] offen sc1\n\t" \
    "s_branch PM_%=\n\t" \
    "PD_%=:\n\t" \
    GSA(0) \
    "v_smfmac_f32_16x16x64_bf16 v[0:3], v[220:223], v[236:243], v10\n\t" \
    "v_xor_b32 v9, 16384, v9\n\t" \
    "v_smfmac_f32_16x16x64_bf16 v[4:7], v[220:223], v[244:251], v10\n\t" \
    "s_waitcnt lgkmcnt(0)\n\t" \
    "s_barrier\n\t" \
    DGA \
    "s_cmp_lg_u32 %[st], 63\n\t" \
    "s_cbranch_scc1 NL_%=\n\t" \
    "s_mov_b64 exec, %[fm]\n\t" \
    "v_mov_b32 v252, %[st]\n\t" \
    "v_mov_b32 v253, %[tag]\n\t" \
    "v_mov_b32 v254, 0\n\t" \
    "buffer_store_dwordx2 v[252:253], v254, %[rs], %[po] offen sc1\n\t" \
    "s_mov_b64 exec, -1\n\t" \
    "NL_%=:\n\t" \
    "ds_read_b128 v[236:239], v9 offset:0\n\t" \
    "ds_read_b128 v[240:243], v9 offset:1024\n\t" \
    "ds_read_b128 v[244:247], v9 offset:2048\n\t" \
    "ds_read_b128 v[248:251], v9 offset:3072\n\t" \
    "ds_read_b128 v[228:231], v8 offset:0\n\t" \
    "ds_read_b128 v[232:235], v8 offset:4096\n\t" \
    "v_smfmac_f32_16x16x64_bf16 v[0:3], v[224:227], v[148:155], v10\n\t" \
    "v_xor_b32 v8, 1024, v8\n\t" \
    "ds_read_b128 v[220:223], v8 offset:8192\n\t" \
    "v_smfmac_f32_16x16x64_bf16 v[4:7], v[224:227], v[156:163], v10\n\t" \
    "ds_read_b128 v[224:227], v8 offset:12288\n\t" \
    "buffer_store_dwordx4 v[212:215], v14, %[rs], %[ox] offen offset:0 sc1\n\t" \
    "buffer_store_dwordx4 v[216:219], v14, %[rs], %[ox] offen offset:512 sc1\n\t" \
    "s_waitcnt lgkmcnt(3)\n\t" \
    "v_smfmac_f32_16x16x64_bf16 v[0:3], v[228:231], v[20:27], v10\n\t" \
    "v_smfmac_f32_16x16x64_bf16 v[4:7], v[228:231], v[28:35], v10\n\t" \
    "ds_read_b128 v[228:231], v8 offset:0\n\t" \
    "s_waitcnt lgkmcnt(3)\n\t" \
    "v_smfmac_f32_16x16x64_bf16 v[0:3], v[232:235], v[84:91], v10\n\t" \
    "v_smfmac_f32_16x16x64_bf16 v[4:7], v[232:235], v[92:99], v10\n\t" \
    "ds_read_b128 v[232:235], v8 offset:4096\n\t" \
    "s_waitcnt lgkmcnt(3)\n\t" \
    "v_smfmac_f32_16x16x64_bf16 v[0:3], v[220:223], v[236:243], v10\n\t" \
    "v_xor_b32 v9, 49152, v9\n\t" \
    "ds_read_b128 v[236:239], v9 offset:0\n\t" \
    "v_smfmac_f32_16x16x64_bf16 v[4:7], v[220:223], v[244:251], v10\n\t" \
    "ds_read_b128 v[240:243], v9 offset:1024\n\t" \
    "s_waitcnt lgkmcnt(4)\n\t" \
    "v_smfmac_f32_16x16x64_bf16 v[0:3], v[224:227], v[164:171], v10\n\t" \
    "v_xor_b32 v8, 3072, v8\n\t" \
    "ds_read_b128 v[220:223], v8 offset:12288\n\t" \
    "v_smfmac_f32_16x16x64_bf16 v[4:7], v[224:227], v[172:179], v10\n\t" \
    "ds_read_b128 v[244:247], v9 offset:2048\n\t" \
    "s_waitcnt lgkmcnt(5)\n\t" \
    "v_smfmac_f32_16x16x64_bf16 v[0:3], v[228:231], v[36:43], v10\n\t" \
    "ds_read_b128 v[224:227], v8 offset:0\n\t" \
    "v_smfmac_f32_16x16x64_bf16 v[4:7], v[228:231], v[44:51], v10\n\t" \
    "ds_read_b128 v[248:251], v9 offset:3072\n\t" \
    "s_waitcnt lgkmcnt(6)\n\t" \
    "v_smfmac_f32_16x16x64_bf16 v[0:3], v[232:235], v[100:107], v10\n\t" \
    "ds_read_b128 v[228:231], v8 offset:8192\n\t" \
    "v_smfmac_f32_16x16x64_bf16 v[4:7], v[232:235], v[108:115], v10\n\t" \
    "ds_read_b128 v[232:235], v8 offset:4096\n\t" \
    "s_waitcnt lgkmcnt(5)\n\t" \
    "v_smfmac_f32_16x16x64_bf16 v[0:3], v[220:223], v[180:187], v10\n\t" \
    "v_smfmac_f32_16x16x64_bf16 v[4:7], v[220:223], v[188:195], v10\n\t" \
    "v_xor_b32 v8, 1024, v8\n\t" \
    "ds_read_b128 v[220:223], v8 offset:12288\n\t" \
    "s_waitcnt lgkmcnt(4)\n\t" \
    "v_smfmac_f32_16x16x64_bf16 v[0:3], v[224:227], v[52:59], v10\n\t" \
    "v_smfmac_f32_16x16x64_bf16 v[4:7], v[224:227], v[60:67], v10\n\t" \
    "ds_read_b128 v[224:227], v8 offset:0\n\t" \
    "s_waitcnt lgkmcnt(3)\n\t" \
    "v_smfmac_f32_16x16x64_bf16 v[0:3], v[228:231], v[236:243], v10\n\t" \
    "v_xor_b32 v9, 16384, v9\n\t" \
    "ds_read_b128 v[236:239], v9 offset:0\n\t" \
    "v_smfmac_f32_16x16x64_bf16 v[4:7], v[228:231], v[244:251], v10\n\t" \
    "ds_read_b128 v[240:243], v9 offset:1024\n\t" \
    "s_waitcnt lgkmcnt(4)\n\t" \
    "v_smfmac_f32_16x16x64_bf16 v[0:3], v[232:235], v[116:123], v10\n\t" \
    "ds_read_b128 v[228:231], v8 offset:4096\n\t" \
    "v_smfmac_f32_16x16x64_bf16 v[4:7], v[232:235], v[124:131], v10\n\t" \
    "ds_read_b128 v[244:247], v9 offset:2048\n\t" \
    "s_waitcnt lgkmcnt(5)\n\t" \
    "v_smfmac_f32_16x16x64_bf16 v[0:3], v[220:223], v[196:203], v10\n\t" \
    "ds_read_b128 v[232:235], v8 offset:8192\n\t" \
    "v_xor_b32 v8, 3072, v8\n\t" \
    "v_smfmac_f32_16x16x64_bf16 v[4:7], v[220:223], v[204:211], v10\n\t" \
    "ds_read_b128 v[248:251], v9 offset:3072\n\t" \
    "s_waitcnt lgkmcnt(6)\n\t" \
    "v_smfmac_f32_16x16x64_bf16 v[0:3], v[224:227], v[68:75], v10\n\t" \
    "v_smfmac_f32_16x16x64_bf16 v[4:7], v[224:227], v[76:83], v10\n\t" \
    "s_waitcnt lgkmcnt(3)\n\t" \
    "v_smfmac_f32_16x16x64_bf16 v[0:3], v[228:231], v[132:139], v10\n\t" \
    "v_smfmac_f32_16x16x64_bf16 v[4:7], v[228:231], v[140:147], v10\n\t" \
    "s_waitcnt lgkmcnt(0)\n\t" \
    "v_smfmac_f32_16x16x64_bf16 v[0:3], v[232:235], v[236:243], v10\n\t" \
    "v_xor_b32 v9, 49152, v9\n\t" \
    "v_smfmac_f32_16x16x64_bf16 v[4:7], v[232:235], v[244:251], v10\n\t" \
    "s_setprio 3\n\t" \
    "v_mov_b32 v253, %[stag]\n\t" \
    "s_nop 3\n\t" \
    "v_add_f32 v0, v0, v1\n\t" \
    "v_add_f32 v2, v2, v3\n\t" \
    "v_add_f32 v4, v4, v5\n\t" \
    "v_add_f32 v6, v6, v7\n\t" \
    "v_cvt_pk_bf16_f32 v0, v0, v4\n\t" \
    "v_cvt_pk_bf16_f32 v2, v2, v6\n\t" \
    "ds_write2_b32 v13, v0, v2 offset0:0 offset1:8\n\t" \
    "s_mov_b64 exec, %[onej]\n\t" \
    "ds_write_b32 v18, v253 offset:16\n\t" \
    "s_mov_b64 exec, -1\n\t" \
    "s_setprio 0\n\t" \
    "ds_write2_b32 v11, v0, v2 offset0:0 offset1:4\n\t" \
    "s_mov_b64 exec, %[onej]\n\t" \
    "ds_write_b32 v18, v253 offset:32\n\t" \
    "s_mov_b64 exec, -1\n\t" \
    GSA(1) \
    "s_setprio 0\n\t" \
    "buffer_load_dwordx4 v[236:239], v12, %[rs], %[qx] offen offset:0 sc1\n\t" \
    "buffer_load_dwordx4 v[240:243], v12, %[rs], %[qx] offen offset:1024 sc1\n\t" \
    "buffer_load_dwordx4 v[244:247], v12, %[rs], %[qx] offen offset:2048 sc1\n\t" \
    "buffer_load_dwordx4 v[248:251], v12, %[rs], %[qx] offen offset:3072 sc1\n\t" \
    "v_cmp_le_i32 s[72:73], 0, v16\n\t" \
    "v_cmp_le_i32 s[74:75], 1, v16\n\t" \
    "v_cmp_le_i32 s[76:77], 2, v16\n\t" \
    "v_cmp_le_i32 s[78:79], 3, v16\n\t" \
    "v_cmp_le_i32 s[80:81], 16, v16\n\t" \
    "v_cmp_le_i32 s[82:83], 17, v16\n\t" \
    "v_cmp_le_i32 s[84:85], 18, v16\n\t" \
    "v_cmp_le_i32 s[86:87], 19, v16\n\t" \
    "v_mov_b32 v254, v254\n\t" \
    "s_nop 2\n\t" \
    "v_mov_b32 v254, v254\n\t" \
    "s_nop 2\n\t" \
    "v_mov_b32 v254, v254\n\t" \
    "s_nop 2\n\t" \
    "v_mov_b32 v254, v254\n\t" \
    "s_nop 2\n\t" \
    "v_mov_b32 v254, v254\n\t" \
    "s_nop 2\n\t" \
    "v_mov_b32 v254, v254\n\t" \
    "s_nop 2\n\t" \
    "v_mov_b32 v254, v254\n\t" \
    "s_nop 2\n\t" \
    "v_mov_b32 v254, v254\n\t" \
    "s_nop 2\n\t" \
    "v_mov_b32 v254, v254\n\t" \
    "s_nop 2\n\t" \
    "v_mov_b32 v254, v254\n\t" \
    "s_nop 2\n\t" \
    "v_mov_b32 v254, v254\n\t" \
    "s_nop 2\n\t" \
    "v_mov_b32 v254, v254\n\t" \
    "s_nop 2\n\t" \
    "v_mov_b32 v254, v254\n\t" \
    "s_nop 2\n\t" \
    "v_mov_b32 v254, v254\n\t" \
    "s_nop 2\n\t" \
    "v_mov_b32 v254, v254\n\t" \
    "s_nop 2\n\t" \
    "v_mov_b32 v254, v254\n\t" \
    "s_nop 2\n\t" \
    "v_mov_b32 v254, v254\n\t" \
    "s_nop 2\n\t" \
    "v_mov_b32 v254, v254\n\t" \
    "s_nop 2\n\t" \
    "v_mov_b32 v254, v254\n\t" \
    "s_nop 2\n\t" \
    "v_mov_b32 v254, v254\n\t" \
    "s_nop 2\n\t" \
    "LW_%=:\n\t" \
    "ds_read_b32 v0, v18 offset:32\n\t" \
    "s_sub_u32 %[lim], %[lim], " STR(DEC1) "\n\t" \
    "s_cmp_gt_i32 %[lim], 0\n\t" \
    "v_mov_b32 v254, v254\n\t" \
    "s_nop 2\n\t" \
    "v_mov_b32 v254, v254\n\t" \
    "s_nop 2\n\t" \
    "v_mov_b32 v254, v254\n\t" \
    "s_nop 2\n\t" \
    "v_mov_b32 v254, v254\n\t" \
    "s_nop 2\n\t" \
    "v_mov_b32 v254, v254\n\t" \
    "s_nop 2\n\t" \
    "s_waitcnt lgkmcnt(0)\n\t" \
    "s_cbranch_scc0 CW_%=\n\t" \
    "v_cmp_ne_u32 vcc, v0, v253\n\t" \
    "s_cbranch_vccnz LW_%=\n\t" \
    "CW_%=:\n\t" \
    "ds_read_b128 v[212:215], v12 offset:24576\n\t" \
    "ds_read_b128 v[216:219], v12 offset:28672\n\t" \
    "ds_read_b128 v[220:223], v15 offset:0\n\t" \
    "ds_read_b128 v[224:227], v12 offset:25600\n\t" \
    "ds_read_b128 v[228:231], v12 offset:29696\n\t" \
    "v_add_u32 v253, %[se], v12\n\t" \
    "v_mov_b32 v254, v254\n\t" \
    "s_nop 2\n\t" \
    "v_mov_b32 v254, v254\n\t" \
    "s_nop 2\n\t" \
    "v_mov_b32 v254, v254\n\t" \
    "s_nop 2\n\t" \
    "v_mov_b32 v254, v254\n\t" \
    "s_nop 2\n\t" \
    "s_waitcnt vmcnt(0)\n\t" \
    "s_waitcnt lgkmcnt(4)\n\t" \
    "v_mfma_f32_16x16x32_bf16 v[0:3], v[212:215], v[236:239], 0\n\t" \
    "ds_read_b128 v[212:215], v15 offset:512\n\t" \
    "s_waitcnt lgkmcnt(4)\n\t" \
    "v_mfma_f32_16x16x32_bf16 v[4:7], v[216:219], v[236:239], 0\n\t" \
    "ds_read_b128 v[216:219], v12 offset:26624\n\t" \
    "s_waitcnt lgkmcnt(4)\n\t" \
    "v_mfma_f32_16x16x32_bf16 v[232:235], v[220:223], v[236:239], 0\n\t" \
    "ds_read_b128 v[220:223], v12 offset:30720\n\t" \
    "s_waitcnt lgkmcnt(4)\n\t" \
    "v_mfma_f32_16x16x32_bf16 v[0:3], v[224:227], v[240:243], v[0:3]\n\t" \
    "ds_read_b128 v[224:227], v15 offset:1024\n\t" \
    "s_waitcnt lgkmcnt(4)\n\t" \
    "v_mfma_f32_16x16x32_bf16 v[4:7], v[228:231], v[240:243], v[4:7]\n\t" \
    "ds_read_b128 v[228:231], v12 offset:27648\n\t" \
    "s_waitcnt lgkmcnt(4)\n\t" \
    "v_mfma_f32_16x16x32_bf16 v[232:235], v[212:215], v[240:243], v[232:235]\n\t" \
    "ds_read_b128 v[212:215], v12 offset:31744\n\t" \
    "s_waitcnt lgkmcnt(4)\n\t" \
    "v_mfma_f32_16x16x32_bf16 v[0:3], v[216:219], v[244:247], v[0:3]\n\t" \
    "ds_read_b128 v[216:219], v15 offset:1536\n\t" \
    "s_waitcnt lgkmcnt(4)\n\t" \
    "v_mfma_f32_16x16x32_bf16 v[4:7], v[220:223], v[244:247], v[4:7]\n\t" \
    "s_waitcnt lgkmcnt(3)\n\t" \
    "v_mfma_f32_16x16x32_bf16 v[232:235], v[224:227], v[244:247], v[232:235]\n\t" \
    "s_waitcnt lgkmcnt(2)\n\t" \
    "v_mfma_f32_16x16x32_bf16 v[0:3], v[228:231], v[248:251], v[0:3]\n\t" \
    "s_waitcnt lgkmcnt(1)\n\t" \
    "v_mfma_f32_16x16x32_bf16 v[4:7], v[212:215], v[248:251], v[4:7]\n\t" \
    "s_waitcnt lgkmcnt(0)\n\t" \
    "v_mfma_f32_16x16x32_bf16 v[232:235], v[216:219], v[248:251], v[232:235]\n\t" \
    "s_barrier\n\t" \
    GSA(4) \
    "ds_read_b128 v[236:239], v253 offset:8192\n\t" \
    "ds_read_b128 v[240:243], v253 offset:9216\n\t" \
    "ds_read_b128 v[244:247], v253 offset:10240\n\t" \
    "ds_read_b128 v[248:251], v253 offset:11264\n\t" \
    "ds_read2st64_b32 v[228:229], v17 offset0:0 offset1:1\n\t" \
    "ds_read2st64_b32 v[230:231], v17 offset0:2 offset1:3\n\t" \
    "s_mov_b64 exec, %[fm]\n\t" \
    "v_mov_b32 v220, %[st]\n\t" \
    "v_mov_b32 v221, %[tag]\n\t" \
    "v_mov_b32 v222, 0\n\t" \
    "buffer_store_dwordx2 v[220:221], v222, %[rs], %[po] offen sc1\n\t" \
    "s_mov_b64 exec, -1\n\t" \
    "v_mov_b32 v254, 0xff800000\n\t" \
    "v_cvt_pk_bf16_f32 v0, 0, v0\n\t" \
    "v_cvt_pk_bf16_f32 v1, 0, v1\n\t" \
    "v_cvt_pk_bf16_f32 v2, 0, v2\n\t" \
    "v_cvt_pk_bf16_f32 v3, 0, v3\n\t" \
    "v_cvt_pk_bf16_f32 v4, 0, v4\n\t" \
    "v_cvt_pk_bf16_f32 v5, 0, v5\n\t" \
    "v_cvt_pk_bf16_f32 v6, 0, v6\n\t" \
    "v_cvt_pk_bf16_f32 v7, 0, v7\n\t" \
    "v_cvt_pk_bf16_f32 v232, 0, v232\n\t" \
    "v_cvt_pk_bf16_f32 v233, 0, v233\n\t" \
    "v_mul_f32 v0, 0x3db504f3, v0\n\t" \
    "v_mul_f32 v1, 0x3db504f3, v1\n\t" \
    "v_mul_f32 v2, 0x3db504f3, v2\n\t" \
    "v_mul_f32 v3, 0x3db504f3, v3\n\t" \
    "v_mul_f32 v4, 0x3db504f3, v4\n\t" \
    "v_mul_f32 v5, 0x3db504f3, v5\n\t" \
    "v_mul_f32 v6, 0x3db504f3, v6\n\t" \
    "v_mul_f32 v7, 0x3db504f3, v7\n\t" \
    "v_mul_f32 v232, 0x3db504f3, v232\n\t" \
    "v_mul_f32 v233, 0x3db504f3, v233\n\t" \
    "v_cvt_pk_bf16_f32 v0, 0, v0\n\t" \
    "v_cvt_pk_bf16_f32 v1, 0, v1\n\t" \
    "v_cvt_pk_bf16_f32 v2, 0, v2\n\t" \
    "v_cvt_pk_bf16_f32 v3, 0, v3\n\t" \
    "v_cvt_pk_bf16_f32 v4, 0, v4\n\t" \
    "v_cvt_pk_bf16_f32 v5, 0, v5\n\t" \
    "v_cvt_pk_bf16_f32 v6, 0, v6\n\t" \
    "v_cvt_pk_bf16_f32 v7, 0, v7\n\t" \
    "v_cvt_pk_bf16_f32 v232, 0, v232\n\t" \
    "v_cvt_pk_bf16_f32 v233, 0, v233\n\t" \
    "v_cndmask_b32 v0, v254, v0, s[72:73]\n\t" \
    "v_cndmask_b32 v1, v254, v1, s[74:75]\n\t" \
    "v_cndmask_b32 v2, v254, v2, s[76:77]\n\t" \
    "v_cndmask_b32 v3, v254, v3, s[78:79]\n\t" \
    "v_cndmask_b32 v4, v254, v4, s[80:81]\n\t" \
    "v_cndmask_b32 v5, v254, v5, s[82:83]\n\t" \
    "v_cndmask_b32 v6, v254, v6, s[84:85]\n\t" \
    "v_cndmask_b32 v7, v254, v7, s[86:87]\n\t" \
    "v_max3_f32 v252, v0, v1, v2\n\t" \
    "v_max3_f32 v255, v3, v4, v5\n\t" \
    "v_max3_f32 v254, v6, v7, v232\n\t" \
    "v_max3_f32 v252, v252, v255, v254\n\t" \
    "v_max_f32 v252, v252, v233\n\t" \
    "v_mov_b32 v255, v252\n\t" \
    "v_mov_b32 v254, v252\n\t" \
    "s_nop 1\n\t" \
    "v_permlane16_swap_b32 v255, v254\n\t" \
    "v_max_f32 v252, v255, v254\n\t" \
    "v_max_f32 v255, v255, v254\n\t" \
    "s_nop 1\n\t" \
    "v_permlane32_swap_b32 v252, v255\n\t" \
    "v_max_f32 v252, v252, v255\n\t" \
    "v_mul_f32 v252, 0xbfb8aa3b, v252\n\t" \
    "v_fma_f32 v0, v0, %[l2e], v252\n\t" \
    "v_fma_f32 v1, v1, %[l2e], v252\n\t" \
    "v_fma_f32 v2, v2, %[l2e], v252\n\t" \
    "v_fma_f32 v3, v3, %[l2e], v252\n\t" \
    "v_fma_f32 v4, v4, %[l2e], v252\n\t" \
    "v_fma_f32 v5, v5, %[l2e], v252\n\t" \
    "v_fma_f32 v6, v6, %[l2e], v252\n\t" \
    "v_fma_f32 v7, v7, %[l2e], v252\n\t" \
    "v_fma_f32 v232, v232, %[l2e], v252\n\t" \
    "v_fma_f32 v233, v233, %[l2e], v252\n\t" \
    "v_exp_f32 v0, v0\n\t" \
    "v_exp_f32 v1, v1\n\t" \
    "v_exp_f32 v2, v2\n\t" \
    "v_exp_f32 v3, v3\n\t" \
    "v_exp_f32 v4, v4\n\t" \
    "v_exp_f32 v5, v5\n\t" \
    "v_exp_f32 v6, v6\n\t" \
    "v_exp_f32 v7, v7\n\t" \
    "v_exp_f32 v232, v232\n\t" \
    "v_exp_f32 v233, v233\n\t" \
    "v_add_f32 v252, v0, v1\n\t" \
    "v_add_f32 v235, v2, v3\n\t" \
    "v_add_f32 v254, v4, v5\n\t" \
    "v_add_f32 v255, v6, v7\n\t" \
    "v_add_f32 v234, v232, v233\n\t" \
    "v_add_f32 v252, v252, v235\n\t" \
    "v_add_f32 v254, v254, v255\n\t" \
    "v_add_f32 v252, v252, v254\n\t" \
    "v_add_f32 v252, v252, v234\n\t" \
    "v_mov_b32 v255, v252\n\t" \
    "v_mov_b32 v254, v252\n\t" \
    "s_nop 1\n\t" \
    "v_permlane16_swap_b32 v255, v254\n\t" \
    "v_add_f32 v252, v255, v254\n\t" \
    "v_add_f32 v255, v255, v254\n\t" \
    "s_nop 1\n\t" \
    "v_permlane32_swap_b32 v252, v255\n\t" \
    "v_add_f32 v252, v252, v255\n\t" \
    "v_rcp_f32 v252, v252\n\t" \
    "s_nop 0\n\t" \
    "v_mul_f32 v0, v0, v252\n\t" \
    "v_mul_f32 v1, v1, v252\n\t" \
    "v_mul_f32 v2, v2, v252\n\t" \
    "v_mul_f32 v3, v3, v252\n\t" \
    "v_mul_f32 v4, v4, v252\n\t" \
    "v_mul_f32 v5, v5, v252\n\t" \
    "v_mul_f32 v6, v6, v252\n\t" \
    "v_mul_f32 v7, v7, v252\n\t" \
    "v_mul_f32 v232, v232, v252\n\t" \
    "v_mul_f32 v233, v233, v252\n\t" \
    "v_cvt_pk_bf16_f32 v0, v0, v1\n\t" \
    "v_cvt_pk_bf16_f32 v1, v2, v3\n\t" \
    "v_cvt_pk_bf16_f32 v2, v4, v5\n\t" \
    "v_cvt_pk_bf16_f32 v3, v6, v7\n\t" \
    "v_cvt_pk_bf16_f32 v4, v232, v233\n\t" \
    "v_mov_b32 v5, 0\n\t" \
    "v_mov_b32 v6, 0\n\t" \
    "v_cvt_pk_bf16_f32 v7, v232, v233\n\t" \
    "s_waitcnt lgkmcnt(0)\n\t" \
    "v_mfma_f32_16x16x32_bf16 v[236:239], v[236:239], v[0:3], 0\n\t" \
    "v_mfma_f32_16x16x32_bf16 v[240:243], v[240:243], v[0:3], 0\n\t" \
    "v_mfma_f32_16x16x32_bf16 v[244:247], v[244:247], v[0:3], 0\n\t" \
    "v_mfma_f32_16x16x32_bf16 v[248:251], v[248:251], v[0:3], 0\n\t" \
    "v_mfma_f32_16x16x16_bf16 v[236:239], v[228:229], v[4:5], v[236:239]\n\t" \
    "v_mfma_f32_16x16x16_bf16 v[240:243], v[228:229], v[6:7], v[240:243]\n\t" \
    "v_mfma_f32_16x16x16_bf16 v[244:247], v[230:231], v[4:5], v[244:247]\n\t" \
    "v_mfma_f32_16x16x16_bf16 v[248:251], v[230:231], v[6:7], v[248:251]\n\t" \
    "s_nop 4\n\t" \
    "v_cvt_pk_bf16_f32 v212, v236, v237\n\t" \
    "v_cvt_pk_bf16_f32 v213, v240, v241\n\t" \
    "v_cvt_pk_bf16_f32 v214, v238, v239\n\t" \
    "v_cvt_pk_bf16_f32 v215, v242, v243\n\t" \
    "v_permlane16_swap_b32 v212, v213\n\t" \
    "s_nop 0\n\t" \
    "v_permlane16_swap_b32 v214, v215\n\t" \
    "v_cvt_pk_bf16_f32 v216, v244, v245\n\t" \
    "v_cvt_pk_bf16_f32 v217, v248, v249\n\t" \
    "v_cvt_pk_bf16_f32 v218, v246, v247\n\t" \
    "v_cvt_pk_bf16_f32 v219, v250, v251\n\t" \
    "v_permlane16_swap_b32 v216, v217\n\t" \
    "s_nop 0\n\t" \
    "v_permlane16_swap_b32 v218, v219\n\t" \
    "s_setprio 1\n\t" \
    GSA(2) \
    GSW \
    "s_nop 1\n\t" \
    : [lim] "+s"(lim), "+{v[212:219]}"(oxr) DGO GSO(0) GSO(1) GSO(2) GSO(3) GSO(4) \
    : "{v8}"(am_a), "{v9}"(t0_a), "{v10}"(idx), "{v11}"(iw_a), "{v12}"(ab_a), "{v13}"(sw_a), "{v14}"(xo_v), "{v15}"(kr_a), "{v16}"(dq), "{v17}"(vl_a), "{v18}"(fr_a), [stag] "s"(0xffffff81u + s), [onej] "s"(onej), [rs] "s"(rs), [m0] "s"(sbm + 16384u), [m1] "s"(sbm + 20480u), [st] "s"(s), [tag] "s"(P.tag), [po] "s"(FLG_OFF + F_PRG(4, h) * 128), [fm] "s"(fmask), [qx] "s"(qxo), [qd] "s"(lbase + LA_VM), [qh] "s"((j & 1) * 2048), [ox] "s"(oxo), [se] "s"((j & 1) * 4096), [l2e] "s"(0x3fb8aa3b), "{v[20:27]}"(wr[0][0]), "{v[28:35]}"(wr[0][1]), "{v[36:43]}"(wr[1][0]), "{v[44:51]}"(wr[1][1]), "{v[52:59]}"(wr[2][0]), "{v[60:67]}"(wr[2][1]), "{v[68:75]}"(wr[3][0]), "{v[76:83]}"(wr[3][1]), "{v[84:91]}"(wr[4][0]), "{v[92:99]}"(wr[4][1]), "{v[100:107]}"(wr[5][0]), "{v[108:115]}"(wr[5][1]), "{v[116:123]}"(wr[6][0]), "{v[124:131]}"(wr[6][1]), "{v[132:139]}"(wr[7][0]), "{v[140:147]}"(wr[7][1]), "{v[148:155]}"(wr[8][0]), "{v[156:163]}"(wr[8][1]), "{v[164:171]}"(wr[9][0]), "{v[172:179]}"(wr[9][1]), "{v[180:187]}"(wr[10][0]), "{v[188:195]}"(wr[10][1]), "{v[196:203]}"(wr[11][0]), "{v[204:211]}"(wr[11][1]) \
    : "memory", "scc", "vcc", "s90", "s91", "s72", "s73", "s74", "s75", "s76", "s77", "s78", "s79", "s80", "s81", "s82", "s83", "s84", "s85", "s86", "s87", "v0", "v1", "v2", "v3", "v4", "v5", "v6", "v7", "v220", "v221", "v222", "v223", "v224", "v225", "v226", "v227", "v228", "v229", "v230", "v231", "v232", "v233", "v234", "v235", "v236", "v237", "v238", "v239", "v240", "v241", "v242", "v243", "v244", "v245", "v246", "v247", "v248", "v249", "v250", "v251", "v252", "v253", "v254", "v255")

#define B_POLL() \
  asm volatile( \
    "s_getpc_b64 s[92:93]\n\t" \
    "TPB_%=:\n\t" \
    "s_add_u32 s92, s92, TEB_%=-TPB_%=\n\t" \
    "s_addc_u32 s93, s93, 0\n\t" \
    "s_lshr_b32 s91, %[tgt], 2\n\t" \
    "s_sub_u32 s91, s91, 3\n\t" \
    "TWB_%=:\n\t" \
    "s_memrealtime s[88:89]\n\t" \
    DGE \
    "s_waitcnt lgkmcnt(0)\n\t" \
    "s_sub_u32 s89, s91, s88\n\t" \
    "s_lshl_b32 s89, s89, 2\n\t" \
    "s_cmp_gt_i32 s89, 0\n\t" \
    "s_cbranch_scc1 TWB_%=\n\t" \
    "s_lshl_b32 s88, s88, 2\n\t" \
    "s_sub_u32 s89, %[tgt], s88\n\t" \
    "s_max_i32 s89, s89, 0\n\t" \
    "s_mul_i32 s89, s89, 11\n\t" \
    "s_lshr_b32 s89, s89, 1\n\t" \
    "s_and_b32 s89, s89, 0x7c\n\t" \
    "s_sub_u32 s92, s92, s89\n\t" \
    "s_subb_u32 s93, s93, 0\n\t" \
    "s_setpc_b64 s[92:93]\n\t" \
    "s_nop 0\n\t" \
    "s_nop 0\n\t" \
    "s_nop 0\n\t" \
    "s_nop 0\n\t" \
    "s_nop 0\n\t" \
    "s_nop 0\n\t" \
    "s_nop 0\n\t" \
    "s_nop 0\n\t" \
    "s_nop 0\n\t" \
    "s_nop 0\n\t" \
    "s_nop 0\n\t" \
    "s_nop 0\n\t" \
    "s_nop 0\n\t" \
    "s_nop 0\n\t" \
    "s_nop 0\n\t" \
    "s_nop 0\n\t" \
    "s_nop 0\n\t" \
    "s_nop 0\n\t" \
    "s_nop 0\n\t" \
    "s_nop 0\n\t" \
    "s_nop 0\n\t" \
    "s_nop 0\n\t" \
    "TEB_%=:\n\t" \
    "buffer_load_dwordx4 v[220:223], %[l16], %[rs], %[m0] offen offset:0 sc1\n\t" \
    "buffer_load_dwordx4 v[224:227], %[l16], %[rs], %[m0] offen offset:1024 sc1\n\t" \
    "buffer_load_dwordx4 v[228:231], %[l16], %[rs], %[m1] offen offset:0 sc1\n\t" \
    "buffer_load_dwordx4 v[232:235], %[l16], %[rs], %[m1] offen offset:1024 sc1\n\t" \
    "v_mov_b32 v252, v252\n\t" \
    "s_nop 2\n\t" \
    "v_mov_b32 v252, v252\n\t" \
    "s_nop 2\n\t" \
    "v_mov_b32 v252, v252\n\t" \
    "s_nop 2\n\t" \
    "v_mov_b32 v252, v252\n\t" \
    "s_nop 2\n\t" \
    "v_mov_b32 v252, v252\n\t" \
    "s_nop 2\n\t" \
    "v_mov_b32 v252, v252\n\t" \
    "s_nop 2\n\t" \
    "v_mov_b32 v252, v252\n\t" \
    "s_nop 2\n\t" \
    "v_mov_b32 v252, v252\n\t" \
    "s_nop 2\n\t" \
    "v_mov_b32 v252, v252\n\t" \
    "s_nop 2\n\t" \
    "v_mov_b32 v252, v252\n\t" \
    "s_nop 2\n\t" \
    "v_mov_b32 v252, v252\n\t" \
    "s_nop 2\n\t" \
    "v_mov_b32 v252, v252\n\t" \
    "s_nop 2\n\t" \
    "v_mov_b32 v252, v252\n\t" \
    "s_nop 2\n\t" \
    "v_mov_b32 v252, v252\n\t" \
    "s_nop 2\n\t" \
    "v_mov_b32 v252, v252\n\t" \
    "s_nop 2\n\t" \
    "v_mov_b32 v252, v252\n\t" \
    "s_nop 2\n\t" \
    "v_mov_b32 v252, v252\n\t" \
    "s_nop 2\n\t" \
    "v_mov_b32 v252, v252\n\t" \
    "s_nop 2\n\t" \
    "v_mov_b32 v252, v252\n\t" \
    "s_nop 2\n\t" \
    "v_mov_b32 v252, v252\n\t" \
    "s_nop 2\n\t" \
    "v_mov_b32 v252, v252\n\t" \
    "s_nop 2\n\t" \
    "v_mov_b32 v252, v252\n\t" \
    "s_nop 2\n\t" \
    "v_mov_b32 v252, v252\n\t" \
    "s_nop 2\n\t" \
    "v_mov_b32 v252, v252\n\t" \
    "s_nop 2\n\t" \
    "PM_%=:\n\t" \
    "s_waitcnt vmcnt(3)\n\t" \
    "ds_write_b128 %[la], v[220:223] offset:0\n\t" \
    "v_max3_u32 v252, v220, v220, v221\n\t" \
    "v_max3_u32 v252, v252, v222, v223\n\t" \
    "s_waitcnt vmcnt(2)\n\t" \
    "ds_write_b128 %[la], v[224:227] offset:1024\n\t" \
    "v_max3_u32 v252, v252, v224, v225\n\t" \
    "v_max3_u32 v252, v252, v226, v227\n\t" \
    "s_waitcnt vmcnt(1)\n\t" \
    "ds_write_b128 %[la], v[228:231] offset:8192\n\t" \
    "v_max3_u32 v252, v252, v228, v229\n\t" \
    "v_max3_u32 v252, v252, v230, v231\n\t" \
    "s_waitcnt vmcnt(0)\n\t" \
    "ds_write_b128 %[la], v[232:235] offset:9216\n\t" \
    "v_max3_u32 v252, v252, v232, v233\n\t" \
    "v_max3_u32 v252, v252, v234, v235\n\t" \
    "v_cmp_eq_u32 vcc, -1, v252\n\t" \
    "s_cbranch_vccz PD_%=\n\t" \
    "s_sub_u32 %[lim], %[lim], 1\n\t" \
    "s_cmp_gt_i32 %[lim], 0\n\t" \
    "s_cbranch_scc0 PD_%=\n\t" \
    "s_sleep " STR(CADM) "\n\t" \
    "buffer_load_dwordx4 v[220:223], %[l16], %[rs], %[m0] offen offset:0 sc1\n\t" \
    "buffer_load_dwordx4 v[224:227], %[l16], %[rs], %[m0] offen offset:1024 sc1\n\t" \
    "buffer_load_dwordx4 v[228:231], %[l16], %[rs], %[m1] offen offset:0 sc1\n\t" \
    "buffer_load_dwordx4 v[232:235], %[l16], %[rs], %[m1] offen offset:1024 sc1\n\t" \
    "s_branch PM_%=\n\t" \
    "PD_%=:\n\t" \
    GSA(3) \
    "s_waitcnt lgkmcnt(0)\n\t" \
    "s_barrier\n\t" \
    : [lim] "+s"(lim) GSO(3) \
    : [l16] "v"(l16), [la] "v"(la), [rs] "s"(rs), [m0] "s"(mo), [m1] "s"(mo + 16384u), [tgt] "s"(tgt) \
    : "memory", "vcc", "scc", "v252", "s88", "s89", "s90", "s91", "s92", "s93", "s94", "s95", "v220", "v221", "v222", "v223", "v224", "v225", "v226", "v227", "v228", "v229", "v230", "v231", "v232", "v233", "v234", "v235")

#define T_WAIT() \
  asm volatile( \
    "s_getpc_b64 s[92:93]\n\t" \
    "TPC_%=:\n\t" \
    "s_add_u32 s92, s92, TEC_%=-TPC_%=\n\t" \
    "s_addc_u32 s93, s93, 0\n\t" \
    "s_lshr_b32 s91, %[tgt], 2\n\t" \
    "s_sub_u32 s91, s91, 3\n\t" \
    "TWC_%=:\n\t" \
    "s_memrealtime s[88:89]\n\t" \
    DGE \
    "s_waitcnt lgkmcnt(0)\n\t" \
    "s_sub_u32 s89, s91, s88\n\t" \
    "s_lshl_b32 s89, s89, 2\n\t" \
    "s_cmp_gt_i32 s89, 0\n\t" \
    "s_cbranch_scc1 TWC_%=\n\t" \
    "s_lshl_b32 s88, s88, 2\n\t" \
    "s_sub_u32 s89, %[tgt], s88\n\t" \
    "s_max_i32 s89, s89, 0\n\t" \
    "s_mul_i32 s89, s89, 11\n\t" \
    "s_lshr_b32 s89, s89, 1\n\t" \
    "s_and_b32 s89, s89, 0x7c\n\t" \
    "s_sub_u32 s92, s92, s89\n\t" \
    "s_subb_u32 s93, s93, 0\n\t" \
    "s_setpc_b64 s[92:93]\n\t" \
    "s_nop 0\n\t" \
    "s_nop 0\n\t" \
    "s_nop 0\n\t" \
    "s_nop 0\n\t" \
    "s_nop 0\n\t" \
    "s_nop 0\n\t" \
    "s_nop 0\n\t" \
    "s_nop 0\n\t" \
    "s_nop 0\n\t" \
    "s_nop 0\n\t" \
    "s_nop 0\n\t" \
    "s_nop 0\n\t" \
    "s_nop 0\n\t" \
    "s_nop 0\n\t" \
    "s_nop 0\n\t" \
    "s_nop 0\n\t" \
    "s_nop 0\n\t" \
    "s_nop 0\n\t" \
    "s_nop 0\n\t" \
    "s_nop 0\n\t" \
    "s_nop 0\n\t" \
    "s_nop 0\n\t" \
    "TEC_%=:\n\t" \
    :  \
    : [tgt] "s"(tgt) \
    : "s88", "s89", "s90", "s91", "s92", "s93", "s94", "s95", "memory", "scc")

#define V_PROJ() \
  asm volatile( \
    "v_mov_b32 v0, 0\n\t" \
    "v_mov_b32 v1, 0\n\t" \
    "v_mov_b32 v2, 0\n\t" \
    "v_mov_b32 v3, 0\n\t" \
    "v_mov_b32 v4, 0\n\t" \
    "v_mov_b32 v5, 0\n\t" \
    "v_mov_b32 v6, 0\n\t" \
    "v_mov_b32 v7, 0\n\t" \
    "s_barrier\n\t" \
    "ds_read_b128 v[220:223], %[am] offset:0\n\t" \
    "ds_read_b128 v[224:227], %[am] offset:1024\n\t" \
    "ds_read_b128 v[228:231], %[am] offset:2048\n\t" \
    "ds_read_b128 v[232:235], %[am] offset:3072\n\t" \
    "ds_read_b128 v[236:239], %[am] offset:4096\n\t" \
    "ds_read_b128 v[240:243], %[am] offset:5120\n\t" \
    "ds_read_b128 v[244:247], %[am] offset:6144\n\t" \
    "ds_read_b128 v[248:251], %[am] offset:7168\n\t" \
    "s_waitcnt lgkmcnt(7)\n\t" \
    "v_smfmac_f32_16x16x64_bf16 v[0:3], v[220:223], v[28:35], %[idx]\n\t" \
    "ds_read_b128 v[220:223], %[am] offset:8192\n\t" \
    "s_waitcnt lgkmcnt(7)\n\t" \
    "v_smfmac_f32_16x16x64_bf16 v[4:7], v[224:227], v[36:43], %[idx]\n\t" \
    "ds_read_b128 v[224:227], %[am] offset:9216\n\t" \
    "s_waitcnt lgkmcnt(7)\n\t" \
    "v_smfmac_f32_16x16x64_bf16 v[0:3], v[228:231], v[44:51], %[idx]\n\t" \
    "ds_read_b128 v[228:231], %[am] offset:10240\n\t" \
    "s_waitcnt lgkmcnt(7)\n\t" \
    "v_smfmac_f32_16x16x64_bf16 v[4:7], v[232:235], v[52:59], %[idx]\n\t" \
    "ds_read_b128 v[232:235], %[am] offset:11264\n\t" \
    "s_waitcnt lgkmcnt(7)\n\t" \
    "v_smfmac_f32_16x16x64_bf16 v[0:3], v[236:239], v[60:67], %[idx]\n\t" \
    "ds_read_b128 v[236:239], %[am] offset:12288\n\t" \
    "s_waitcnt lgkmcnt(7)\n\t" \
    "v_smfmac_f32_16x16x64_bf16 v[4:7], v[240:243], v[68:75], %[idx]\n\t" \
    "ds_read_b128 v[240:243], %[am] offset:13312\n\t" \
    "s_waitcnt lgkmcnt(7)\n\t" \
    "v_smfmac_f32_16x16x64_bf16 v[0:3], v[244:247], v[76:83], %[idx]\n\t" \
    "ds_read_b128 v[244:247], %[am] offset:14336\n\t" \
    "s_waitcnt lgkmcnt(7)\n\t" \
    "v_smfmac_f32_16x16x64_bf16 v[4:7], v[248:251], v[84:91], %[idx]\n\t" \
    "ds_read_b128 v[248:251], %[am] offset:15360\n\t" \
    "s_waitcnt lgkmcnt(7)\n\t" \
    "v_smfmac_f32_16x16x64_bf16 v[0:3], v[220:223], v[92:99], %[idx]\n\t" \
    "s_waitcnt lgkmcnt(6)\n\t" \
    "v_smfmac_f32_16x16x64_bf16 v[4:7], v[224:227], v[100:107], %[idx]\n\t" \
    "s_waitcnt lgkmcnt(5)\n\t" \
    "v_smfmac_f32_16x16x64_bf16 v[0:3], v[228:231], v[108:115], %[idx]\n\t" \
    "s_waitcnt lgkmcnt(4)\n\t" \
    "v_smfmac_f32_16x16x64_bf16 v[4:7], v[232:235], v[116:123], %[idx]\n\t" \
    "s_waitcnt lgkmcnt(3)\n\t" \
    "v_smfmac_f32_16x16x64_bf16 v[0:3], v[236:239], v[124:131], %[idx]\n\t" \
    "s_waitcnt lgkmcnt(2)\n\t" \
    "v_smfmac_f32_16x16x64_bf16 v[4:7], v[240:243], v[132:139], %[idx]\n\t" \
    "s_waitcnt lgkmcnt(1)\n\t" \
    "v_smfmac_f32_16x16x64_bf16 v[0:3], v[244:247], v[140:147], %[idx]\n\t" \
    "s_waitcnt lgkmcnt(0)\n\t" \
    "v_smfmac_f32_16x16x64_bf16 v[4:7], v[248:251], v[148:155], %[idx]\n\t" \
    "s_nop 5\n\t" \
    "v_add_f32 v0, v0, v1\n\t" \
    "v_add_f32 v2, v2, v3\n\t" \
    "v_add_f32 v4, v4, v5\n\t" \
    "v_add_f32 v6, v6, v7\n\t" \
    "v_add_f32 v0, v0, v4\n\t" \
    "v_add_f32 v2, v2, v6\n\t" \
    "v_cvt_pk_bf16_f32 v0, v0, v2\n\t" \
    "v_min_u32 v0, -2, v0\n\t" \
    "s_nop 1\n\t" \
    "v_mov_b32_dpp v1, v0 quad_perm:[1,1,1,1] row_mask:0xf bank_mask:0xf\n\t" \
    "v_mov_b32_dpp v2, v0 quad_perm:[2,2,2,2] row_mask:0xf bank_mask:0xf\n\t" \
    "v_mov_b32_dpp v3, v0 quad_perm:[3,3,3,3] row_mask:0xf bank_mask:0xf\n\t" \
    "s_mov_b64 exec, %[qm]\n\t" \
    "buffer_store_dwordx4 v[0:3], %[l4], %[rs], %[vo] offen sc1\n\t" \
    "s_mov_b64 exec, -1\n\t" \
    "s_nop 1\n\t" \
    :  \
    : [am] "v"(am_a), [idx] "v"(idx), [l4] "v"(ln * 4), [rs] "s"(rs), [vo] "s"(sb + vo), [qm] "s"(0x1111111111111111ull), "{v[28:35]}"(wv[0]), "{v[36:43]}"(wv[1]), "{v[44:51]}"(wv[2]), "{v[52:59]}"(wv[3]), "{v[60:67]}"(wv[4]), "{v[68:75]}"(wv[5]), "{v[76:83]}"(wv[6]), "{v[84:91]}"(wv[7]), "{v[92:99]}"(wv[8]), "{v[100:107]}"(wv[9]), "{v[108:115]}"(wv[10]), "{v[116:123]}"(wv[11]), "{v[124:131]}"(wv[12]), "{v[132:139]}"(wv[13]), "{v[140:147]}"(wv[14]), "{v[148:155]}"(wv[15]) \
    : "memory", "v0", "v1", "v2", "v3", "v4", "v5", "v6", "v7", "v220", "v221", "v222", "v223", "v224", "v225", "v226", "v227", "v228", "v229", "v230", "v231", "v232", "v233", "v234", "v235", "v236", "v237", "v238", "v239", "v240", "v241", "v242", "v243", "v244", "v245", "v246", "v247", "v248", "v249", "v250", "v251")

#define WO_V() \
  asm volatile( \
    "v_mov_b32 v0, 0\n\t" \
    "v_mov_b32 v1, 0\n\t" \
    "v_mov_b32 v2, 0\n\t" \
    "v_mov_b32 v3, 0\n\t" \
    "v_mov_b32 v4, 0\n\t" \
    "v_mov_b32 v5, 0\n\t" \
    "v_mov_b32 v6, 0\n\t" \
    "v_mov_b32 v7, 0\n\t" \
    "v_mov_b32 v8, 0\n\t" \
    "v_mov_b32 v9, 0\n\t" \
    "v_mov_b32 v10, 0\n\t" \
    "v_mov_b32 v11, 0\n\t" \
    "v_mov_b32 v12, 0\n\t" \
    "v_mov_b32 v13, 0\n\t" \
    "v_mov_b32 v14, 0\n\t" \
    "v_mov_b32 v15, 0\n\t" \
    "v_mov_b32 v252, 0\n\t" \
    "s_barrier\n\t" \
    GSA(4) \
    DGC \
    "buffer_load_dwordx4 v[220:223], %[l16], %[rs], %[s0] offen sc1\n\t" \
    "s_nop 7\n\t" \
    "buffer_load_dwordx4 v[224:227], %[l16], %[rs], %[s1] offen sc1\n\t" \
    "A_%=:\n\t" \
    "s_waitcnt vmcnt(1)\n\t" \
    "v_smfmac_f32_16x16x64_bf16 v[0:3], v[220:223], v[156:163], %[idx]\n\t" \
    "v_max3_u32 v252, v252, v220, v221\n\t" \
    "v_smfmac_f32_16x16x64_bf16 v[4:7], v[220:223], v[164:171], %[idx]\n\t" \
    "v_max3_u32 v252, v252, v222, v223\n\t" \
    "v_smfmac_f32_16x16x64_bf16 v[8:11], v[220:223], v[172:179], %[idx]\n\t" \
    "v_smfmac_f32_16x16x64_bf16 v[12:15], v[220:223], v[180:187], %[idx]\n\t" \
    "s_waitcnt vmcnt(0)\n\t" \
    "v_smfmac_f32_16x16x64_bf16 v[0:3], v[224:227], v[188:195], %[idx]\n\t" \
    "v_max3_u32 v252, v252, v224, v225\n\t" \
    "v_smfmac_f32_16x16x64_bf16 v[4:7], v[224:227], v[196:203], %[idx]\n\t" \
    "v_max3_u32 v252, v252, v226, v227\n\t" \
    "v_smfmac_f32_16x16x64_bf16 v[8:11], v[224:227], v[204:211], %[idx]\n\t" \
    "v_smfmac_f32_16x16x64_bf16 v[12:15], v[224:227], v[212:219], %[idx]\n\t" \
    "s_setprio 0\n\t" \
    "v_cmp_eq_u32 vcc, -1, v252\n\t" \
    "s_cbranch_vccnz SL_%=\n\t" \
    "D_%=:\n\t" \
    GSA(1) \
    "s_nop 2\n\t" \
    "v_add_f32 v0, v0, v1\n\t" \
    "v_add_f32 v2, v2, v3\n\t" \
    "v_add_f32 v4, v4, v5\n\t" \
    "v_add_f32 v6, v6, v7\n\t" \
    "v_add_f32 v8, v8, v9\n\t" \
    "v_add_f32 v10, v10, v11\n\t" \
    "v_add_f32 v12, v12, v13\n\t" \
    "v_add_f32 v14, v14, v15\n\t" \
    "v_permlane32_swap_b32 v0, v4\n\t" \
    "v_permlane32_swap_b32 v2, v6\n\t" \
    "v_permlane32_swap_b32 v8, v12\n\t" \
    "v_permlane32_swap_b32 v10, v14\n\t" \
    "v_add_f32 v0, v0, v12\n\t" \
    "v_add_f32 v2, v2, v14\n\t" \
    "ds_write2_b32 %[rw], v0, v2 offset0:0 offset1:32\n\t" \
    "s_setprio 2\n\t" \
    "s_waitcnt lgkmcnt(0)\n\t" \
    "s_barrier\n\t" \
    "s_cmp_eq_u32 %[red], 0\n\t" \
    "s_cbranch_scc0 E_%=\n\t" \
    "ds_read2st64_b64 v[0:3], %[rr] offset0:0 offset1:1\n\t" \
    "ds_read2st64_b64 v[4:7], %[rr] offset0:2 offset1:3\n\t" \
    "ds_read2st64_b64 v[8:11], %[rr] offset0:4 offset1:5\n\t" \
    "ds_read2st64_b64 v[12:15], %[rr] offset0:6 offset1:7\n\t" \
    "s_waitcnt lgkmcnt(3)\n\t" \
    "v_pk_add_f32 v[0:1], v[0:1], v[2:3]\n\t" \
    "s_waitcnt lgkmcnt(2)\n\t" \
    "v_pk_add_f32 v[4:5], v[4:5], v[6:7]\n\t" \
    "s_waitcnt lgkmcnt(1)\n\t" \
    "v_pk_add_f32 v[8:9], v[8:9], v[10:11]\n\t" \
    "v_pk_add_f32 v[0:1], v[0:1], v[4:5]\n\t" \
    "s_waitcnt lgkmcnt(0)\n\t" \
    "v_pk_add_f32 v[12:13], v[12:13], v[14:15]\n\t" \
    "s_nop 0\n\t" \
    "v_pk_add_f32 v[8:9], v[8:9], v[12:13]\n\t" \
    "s_nop 0\n\t" \
    "v_pk_add_f32 v[0:1], v[0:1], v[8:9]\n\t" \
    "s_nop 0\n\t" \
    "v_cvt_pk_bf16_f32 v0, v0, v1\n\t" \
    "v_min_u32 v0, -2, v0\n\t" \
    "buffer_store_dword v0, %[mo], %[rs], %[sn] offen sc1\n\t" \
    "s_memrealtime %[t1]\n\t" \
    DGA \
    "s_waitcnt lgkmcnt(0)\n\t" \
    GSA(2) \
    "s_branch E_%=\n\t" \
    "SL_%=:\n\t" \
    "s_sleep 1\n\t" \
    "buffer_load_dwordx4 v[220:223], %[l16], %[rs], %[s0] offen sc1\n\t" \
    "buffer_load_dwordx4 v[224:227], %[l16], %[rs], %[s1] offen sc1\n\t" \
    "s_sub_u32 %[lim], %[lim], " STR(DEC2) "\n\t" \
    "s_cmp_gt_i32 %[lim], 0\n\t" \
    "s_cbranch_scc0 G_%=\n\t" \
    "s_nop 3\n\t" \
    "v_mov_b32 v0, 0\n\t" \
    "v_mov_b32 v1, 0\n\t" \
    "v_mov_b32 v2, 0\n\t" \
    "v_mov_b32 v3, 0\n\t" \
    "v_mov_b32 v4, 0\n\t" \
    "v_mov_b32 v5, 0\n\t" \
    "v_mov_b32 v6, 0\n\t" \
    "v_mov_b32 v7, 0\n\t" \
    "v_mov_b32 v8, 0\n\t" \
    "v_mov_b32 v9, 0\n\t" \
    "v_mov_b32 v10, 0\n\t" \
    "v_mov_b32 v11, 0\n\t" \
    "v_mov_b32 v12, 0\n\t" \
    "v_mov_b32 v13, 0\n\t" \
    "v_mov_b32 v14, 0\n\t" \
    "v_mov_b32 v15, 0\n\t" \
    "v_mov_b32 v252, 0\n\t" \
    "s_branch A_%=\n\t" \
    "G_%=:\n\t" \
    "s_waitcnt vmcnt(0)\n\t" \
    "s_branch D_%=\n\t" \
    "E_%=:\n\t" \
    GSW \
    : [lim] "+s"(lim) GSO(1) GSO(4), [t1] "=&s"(t1) DGO DGQ GSO(2) \
    : [l16] "v"(l16), [rs] "s"(rs), [idx] "v"(idx), [rw] "v"(rw_a), [s0] "s"(so[0]), [s1] "s"(so[1]), [rr] "v"(rr_a), [mo] "v"(mo_v), [sn] "s"(sn), [red] "s"(wid), "{v[156:163]}"(wo[0][0][0]), "{v[164:171]}"(wo[0][0][1]), "{v[172:179]}"(wo[0][1][0]), "{v[180:187]}"(wo[0][1][1]), "{v[188:195]}"(wo[1][0][0]), "{v[196:203]}"(wo[1][0][1]), "{v[204:211]}"(wo[1][1][0]), "{v[212:219]}"(wo[1][1][1]) \
    : "memory", "vcc", "scc", "v252", "v0", "v1", "v2", "v3", "v4", "v5", "v6", "v7", "v8", "v9", "v10", "v11", "v12", "v13", "v14", "v15", "v220", "v221", "v222", "v223", "v224", "v225", "v226", "v227")

#define WO_W() \
  asm volatile( \
    "v_mov_b32 v0, 0\n\t" \
    "v_mov_b32 v1, 0\n\t" \
    "v_mov_b32 v2, 0\n\t" \
    "v_mov_b32 v3, 0\n\t" \
    "v_mov_b32 v4, 0\n\t" \
    "v_mov_b32 v5, 0\n\t" \
    "v_mov_b32 v6, 0\n\t" \
    "v_mov_b32 v7, 0\n\t" \
    "v_mov_b32 v8, 0\n\t" \
    "v_mov_b32 v9, 0\n\t" \
    "v_mov_b32 v10, 0\n\t" \
    "v_mov_b32 v11, 0\n\t" \
    "v_mov_b32 v12, 0\n\t" \
    "v_mov_b32 v13, 0\n\t" \
    "v_mov_b32 v14, 0\n\t" \
    "v_mov_b32 v15, 0\n\t" \
    "v_mov_b32 v252, 0\n\t" \
    "s_setprio 1\n\t" \
    "s_barrier\n\t" \
    GSA(4) \
    "buffer_load_dwordx4 v[220:223], %[l16], %[rs], %[s0] offen sc1\n\t" \
    "s_nop 15\n\t" \
    "buffer_load_dwordx4 v[224:227], %[l16], %[rs], %[s1] offen sc1\n\t" \
    "s_nop 15\n\t" \
    "buffer_load_dwordx4 v[228:231], %[l16], %[rs], %[s2] offen sc1\n\t" \
    "s_nop 15\n\t" \
    "buffer_load_dwordx4 v[232:235], %[l16], %[rs], %[s3] offen sc1\n\t" \
    "s_nop 15\n\t" \
    "buffer_load_dwordx4 v[236:239], %[l16], %[rs], %[s4] offen sc1\n\t" \
    "s_nop 15\n\t" \
    "buffer_load_dwordx4 v[240:243], %[l16], %[rs], %[s5] offen sc1\n\t" \
    "A_%=:\n\t" \
    "s_waitcnt vmcnt(5)\n\t" \
    "v_smfmac_f32_16x16x64_bf16 v[0:3], v[220:223], v[28:35], %[idx]\n\t" \
    "v_max3_u32 v252, v252, v220, v221\n\t" \
    "v_smfmac_f32_16x16x64_bf16 v[4:7], v[220:223], v[36:43], %[idx]\n\t" \
    "v_max3_u32 v252, v252, v222, v223\n\t" \
    "v_smfmac_f32_16x16x64_bf16 v[8:11], v[220:223], v[44:51], %[idx]\n\t" \
    "v_smfmac_f32_16x16x64_bf16 v[12:15], v[220:223], v[52:59], %[idx]\n\t" \
    "s_waitcnt vmcnt(4)\n\t" \
    "v_smfmac_f32_16x16x64_bf16 v[0:3], v[224:227], v[60:67], %[idx]\n\t" \
    "v_max3_u32 v252, v252, v224, v225\n\t" \
    "v_smfmac_f32_16x16x64_bf16 v[4:7], v[224:227], v[68:75], %[idx]\n\t" \
    "v_max3_u32 v252, v252, v226, v227\n\t" \
    "v_smfmac_f32_16x16x64_bf16 v[8:11], v[224:227], v[76:83], %[idx]\n\t" \
    "v_smfmac_f32_16x16x64_bf16 v[12:15], v[224:227], v[84:91], %[idx]\n\t" \
    "s_waitcnt vmcnt(3)\n\t" \
    "v_smfmac_f32_16x16x64_bf16 v[0:3], v[228:231], v[92:99], %[idx]\n\t" \
    "v_max3_u32 v252, v252, v228, v229\n\t" \
    "v_smfmac_f32_16x16x64_bf16 v[4:7], v[228:231], v[100:107], %[idx]\n\t" \
    "v_max3_u32 v252, v252, v230, v231\n\t" \
    "v_smfmac_f32_16x16x64_bf16 v[8:11], v[228:231], v[108:115], %[idx]\n\t" \
    "v_smfmac_f32_16x16x64_bf16 v[12:15], v[228:231], v[116:123], %[idx]\n\t" \
    "s_waitcnt vmcnt(2)\n\t" \
    "v_smfmac_f32_16x16x64_bf16 v[0:3], v[232:235], v[124:131], %[idx]\n\t" \
    "v_max3_u32 v252, v252, v232, v233\n\t" \
    "v_smfmac_f32_16x16x64_bf16 v[4:7], v[232:235], v[132:139], %[idx]\n\t" \
    "v_max3_u32 v252, v252, v234, v235\n\t" \
    "v_smfmac_f32_16x16x64_bf16 v[8:11], v[232:235], v[140:147], %[idx]\n\t" \
    "v_smfmac_f32_16x16x64_bf16 v[12:15], v[232:235], v[148:155], %[idx]\n\t" \
    "s_waitcnt vmcnt(1)\n\t" \
    "v_smfmac_f32_16x16x64_bf16 v[0:3], v[236:239], v[156:163], %[idx]\n\t" \
    "v_max3_u32 v252, v252, v236, v237\n\t" \
    "v_smfmac_f32_16x16x64_bf16 v[4:7], v[236:239], v[164:171], %[idx]\n\t" \
    "v_max3_u32 v252, v252, v238, v239\n\t" \
    "v_smfmac_f32_16x16x64_bf16 v[8:11], v[236:239], v[172:179], %[idx]\n\t" \
    "v_smfmac_f32_16x16x64_bf16 v[12:15], v[236:239], v[180:187], %[idx]\n\t" \
    "s_waitcnt vmcnt(0)\n\t" \
    "v_smfmac_f32_16x16x64_bf16 v[0:3], v[240:243], v[188:195], %[idx]\n\t" \
    "v_max3_u32 v252, v252, v240, v241\n\t" \
    "v_smfmac_f32_16x16x64_bf16 v[4:7], v[240:243], v[196:203], %[idx]\n\t" \
    "v_max3_u32 v252, v252, v242, v243\n\t" \
    "v_smfmac_f32_16x16x64_bf16 v[8:11], v[240:243], v[204:211], %[idx]\n\t" \
    "v_smfmac_f32_16x16x64_bf16 v[12:15], v[240:243], v[212:219], %[idx]\n\t" \
    "v_cmp_eq_u32 vcc, -1, v252\n\t" \
    "s_cbranch_vccnz SL_%=\n\t" \
    "D_%=:\n\t" \
    GSA(1) \
    "s_nop 2\n\t" \
    "v_add_f32 v0, v0, v1\n\t" \
    "v_add_f32 v2, v2, v3\n\t" \
    "v_add_f32 v4, v4, v5\n\t" \
    "v_add_f32 v6, v6, v7\n\t" \
    "v_add_f32 v8, v8, v9\n\t" \
    "v_add_f32 v10, v10, v11\n\t" \
    "v_add_f32 v12, v12, v13\n\t" \
    "v_add_f32 v14, v14, v15\n\t" \
    "v_permlane32_swap_b32 v0, v4\n\t" \
    "v_permlane32_swap_b32 v2, v6\n\t" \
    "v_permlane32_swap_b32 v8, v12\n\t" \
    "v_permlane32_swap_b32 v10, v14\n\t" \
    "v_add_f32 v0, v0, v12\n\t" \
    "v_add_f32 v2, v2, v14\n\t" \
    "ds_write2_b32 %[rw], v0, v2 offset0:0 offset1:32\n\t" \
    "s_waitcnt lgkmcnt(0)\n\t" \
    "s_barrier\n\t" \
    "s_branch E_%=\n\t" \
    "SL_%=:\n\t" \
    "s_sleep 1\n\t" \
    "buffer_load_dwordx4 v[220:223], %[l16], %[rs], %[s0] offen sc1\n\t" \
    "buffer_load_dwordx4 v[224:227], %[l16], %[rs], %[s1] offen sc1\n\t" \
    "buffer_load_dwordx4 v[228:231], %[l16], %[rs], %[s2] offen sc1\n\t" \
    "buffer_load_dwordx4 v[232:235], %[l16], %[rs], %[s3] offen sc1\n\t" \
    "buffer_load_dwordx4 v[236:239], %[l16], %[rs], %[s4] offen sc1\n\t" \
    "buffer_load_dwordx4 v[240:243], %[l16], %[rs], %[s5] offen sc1\n\t" \
    "s_sub_u32 %[lim], %[lim], " STR(DEC2) "\n\t" \
    "s_cmp_gt_i32 %[lim], 0\n\t" \
    "s_cbranch_scc0 G_%=\n\t" \
    "s_nop 3\n\t" \
    "v_mov_b32 v0, 0\n\t" \
    "v_mov_b32 v1, 0\n\t" \
    "v_mov_b32 v2, 0\n\t" \
    "v_mov_b32 v3, 0\n\t" \
    "v_mov_b32 v4, 0\n\t" \
    "v_mov_b32 v5, 0\n\t" \
    "v_mov_b32 v6, 0\n\t" \
    "v_mov_b32 v7, 0\n\t" \
    "v_mov_b32 v8, 0\n\t" \
    "v_mov_b32 v9, 0\n\t" \
    "v_mov_b32 v10, 0\n\t" \
    "v_mov_b32 v11, 0\n\t" \
    "v_mov_b32 v12, 0\n\t" \
    "v_mov_b32 v13, 0\n\t" \
    "v_mov_b32 v14, 0\n\t" \
    "v_mov_b32 v15, 0\n\t" \
    "v_mov_b32 v252, 0\n\t" \
    "s_branch A_%=\n\t" \
    "G_%=:\n\t" \
    "s_waitcnt vmcnt(0)\n\t" \
    "s_branch D_%=\n\t" \
    "E_%=:\n\t" \
    GSW \
    : [lim] "+s"(lim) GSO(1) GSO(4) \
    : [l16] "v"(l16), [rs] "s"(rs), [idx] "v"(idx), [rw] "v"(rw_a), [s0] "s"(so[0]), [s1] "s"(so[1]), [s2] "s"(so[2]), [s3] "s"(so[3]), [s4] "s"(so[4]), [s5] "s"(so[5]), "{v[28:35]}"(wo[0][0][0]), "{v[36:43]}"(wo[0][0][1]), "{v[44:51]}"(wo[0][1][0]), "{v[52:59]}"(wo[0][1][1]), "{v[60:67]}"(wo[1][0][0]), "{v[68:75]}"(wo[1][0][1]), "{v[76:83]}"(wo[1][1][0]), "{v[84:91]}"(wo[1][1][1]), "{v[92:99]}"(wo[2][0][0]), "{v[100:107]}"(wo[2][0][1]), "{v[108:115]}"(wo[2][1][0]), "{v[116:123]}"(wo[2][1][1]), "{v[124:131]}"(wo[3][0][0]), "{v[132:139]}"(wo[3][0][1]), "{v[140:147]}"(wo[3][1][0]), "{v[148:155]}"(wo[3][1][1]), "{v[156:163]}"(wo[4][0][0]), "{v[164:171]}"(wo[4][0][1]), "{v[172:179]}"(wo[4][1][0]), "{v[180:187]}"(wo[4][1][1]), "{v[188:195]}"(wo[5][0][0]), "{v[196:203]}"(wo[5][0][1]), "{v[204:211]}"(wo[5][1][0]), "{v[212:219]}"(wo[5][1][1]) \
    : "memory", "vcc", "scc", "v252", "v0", "v1", "v2", "v3", "v4", "v5", "v6", "v7", "v8", "v9", "v10", "v11", "v12", "v13", "v14", "v15", "v220", "v221", "v222", "v223", "v224", "v225", "v226", "v227", "v228", "v229", "v230", "v231", "v232", "v233", "v234", "v235", "v236", "v237", "v238", "v239", "v240", "v241", "v242", "v243")

#define S_LOOP_K(TE_, FIN_) \
  asm volatile( \
    "s_cmp_eq_u32 %[kh], 0\n\t" \
    "s_cbranch_scc0 L1_%=\n\t" \
    "L0_%=:\n\t" \
    "s_setprio 0\n\t" \
    "s_nop 3\n\t" \
    "ds_write_b128 %[pa], v[196:199] offset:0\n\t" \
    "ds_write_b128 %[pa], v[200:203] offset:1024\n\t" \
    "ds_write_b128 %[pa], v[204:207] offset:2048\n\t" \
    "ds_write_b128 %[pa], v[208:211] offset:3072\n\t" \
    "s_sub_u32 %[a], %[t], 1\n\t" \
    "v_mov_b32 v53, %[tag]\n\t" \
    "v_mov_b32 v52, %[a]\n\t" \
    "s_sub_u32 %[b], %[t], 2\n\t" \
    "s_cmp_lt_u32 %[b], " STR(SQUICK) "\n\t" \
    "s_cselect_b64 exec, %[fm], 0\n\t" \
    "buffer_store_dwordx2 v[52:53], %[l16], %[rs], %[fo] offen sc1\n\t" \
    "s_mov_b64 exec, -1\n\t" \
    "s_add_u32 m0, %[sd], 49152\n\t" \
    "s_add_u32 %[a], %[sx], 81920\n\t" \
    "s_waitcnt lgkmcnt(0)\n\t" \
    "buffer_load_dwordx4 %[l16], %[rs], %[a] offen offset:0 sc1 lds\n\t" \
    "buffer_load_dwordx4 %[l16], %[rs], %[a] offen offset:1024 sc1 lds\n\t" \
    "buffer_load_dwordx4 %[l16], %[rs], %[a] offen offset:2048 sc1 lds\n\t" \
    "buffer_load_dwordx4 %[l16], %[rs], %[a] offen offset:3072 sc1 lds\n\t" \
    "v_mfma_f32_16x16x32_bf16 v[0:3], v[68:71], v[20:23], 0\n\t" \
    "ds_read_b128 v[44:47], %[xp] offset:6144\n\t" \
    "v_mfma_f32_16x16x32_bf16 v[4:7], v[76:79], v[20:23], 0\n\t" \
    "ds_read_b128 v[48:51], %[xp] offset:7168\n\t" \
    "v_mfma_f32_16x16x32_bf16 v[8:11], v[68:71], v[24:27], 0\n\t" \
    "v_mfma_f32_16x16x32_bf16 v[12:15], v[76:79], v[24:27], 0\n\t" \
    "v_mfma_f32_16x16x32_bf16 v[0:3], v[72:75], v[28:31], v[0:3]\n\t" \
    "ds_read_b128 v[20:23], %[xp] offset:8192\n\t" \
    "v_mfma_f32_16x16x32_bf16 v[4:7], v[80:83], v[28:31], v[4:7]\n\t" \
    "ds_read_b128 v[24:27], %[xp] offset:9216\n\t" \
    "v_mfma_f32_16x16x32_bf16 v[8:11], v[72:75], v[32:35], v[8:11]\n\t" \
    "v_mfma_f32_16x16x32_bf16 v[12:15], v[80:83], v[32:35], v[12:15]\n\t" \
    "v_mfma_f32_16x16x32_bf16 v[0:3], v[84:87], v[36:39], v[0:3]\n\t" \
    "ds_read_b128 v[28:31], %[xp] offset:10240\n\t" \
    "v_mfma_f32_16x16x32_bf16 v[4:7], v[92:95], v[36:39], v[4:7]\n\t" \
    "ds_read_b128 v[32:35], %[xp] offset:11264\n\t" \
    "v_mfma_f32_16x16x32_bf16 v[8:11], v[84:87], v[40:43], v[8:11]\n\t" \
    "s_waitcnt lgkmcnt(4)\n\t" \
    "v_mfma_f32_16x16x32_bf16 v[12:15], v[92:95], v[40:43], v[12:15]\n\t" \
    "v_mfma_f32_16x16x32_bf16 v[0:3], v[88:91], v[44:47], v[0:3]\n\t" \
    "ds_read_b128 v[36:39], %[xp] offset:12288\n\t" \
    "v_mfma_f32_16x16x32_bf16 v[4:7], v[96:99], v[44:47], v[4:7]\n\t" \
    "ds_read_b128 v[40:43], %[xp] offset:13312\n\t" \
    "v_mfma_f32_16x16x32_bf16 v[8:11], v[88:91], v[48:51], v[8:11]\n\t" \
    "s_waitcnt lgkmcnt(4)\n\t" \
    "v_mfma_f32_16x16x32_bf16 v[12:15], v[96:99], v[48:51], v[12:15]\n\t" \
    "v_mfma_f32_16x16x32_bf16 v[0:3], v[100:103], v[20:23], v[0:3]\n\t" \
    "ds_read_b128 v[44:47], %[xp] offset:14336\n\t" \
    "v_mfma_f32_16x16x32_bf16 v[4:7], v[108:111], v[20:23], v[4:7]\n\t" \
    "ds_read_b128 v[48:51], %[xp] offset:15360\n\t" \
    "v_mfma_f32_16x16x32_bf16 v[8:11], v[100:103], v[24:27], v[8:11]\n\t" \
    "s_waitcnt lgkmcnt(4)\n\t" \
    "v_mfma_f32_16x16x32_bf16 v[12:15], v[108:111], v[24:27], v[12:15]\n\t" \
    "v_mfma_f32_16x16x32_bf16 v[0:3], v[104:107], v[28:31], v[0:3]\n\t" \
    "ds_read_b128 v[20:23], %[xp] offset:16384\n\t" \
    "v_mfma_f32_16x16x32_bf16 v[4:7], v[112:115], v[28:31], v[4:7]\n\t" \
    "ds_read_b128 v[24:27], %[xp] offset:17408\n\t" \
    "v_mfma_f32_16x16x32_bf16 v[8:11], v[104:107], v[32:35], v[8:11]\n\t" \
    "s_waitcnt lgkmcnt(4)\n\t" \
    "v_mfma_f32_16x16x32_bf16 v[12:15], v[112:115], v[32:35], v[12:15]\n\t" \
    "v_mfma_f32_16x16x32_bf16 v[0:3], v[116:119], v[36:39], v[0:3]\n\t" \
    "ds_read_b128 v[28:31], %[xp] offset:18432\n\t" \
    "v_mfma_f32_16x16x32_bf16 v[4:7], v[124:127], v[36:39], v[4:7]\n\t" \
    "ds_read_b128 v[32:35], %[xp] offset:19456\n\t" \
    "v_mfma_f32_16x16x32_bf16 v[8:11], v[116:119], v[40:43], v[8:11]\n\t" \
    "s_waitcnt lgkmcnt(4)\n\t" \
    "v_mfma_f32_16x16x32_bf16 v[12:15], v[124:127], v[40:43], v[12:15]\n\t" \
    "v_mfma_f32_16x16x32_bf16 v[0:3], v[120:123], v[44:47], v[0:3]\n\t" \
    "ds_read_b128 v[36:39], %[xp] offset:20480\n\t" \
    "v_mfma_f32_16x16x32_bf16 v[4:7], v[128:131], v[44:47], v[4:7]\n\t" \
    "ds_read_b128 v[40:43], %[xp] offset:21504\n\t" \
    "v_mfma_f32_16x16x32_bf16 v[8:11], v[120:123], v[48:51], v[8:11]\n\t" \
    "s_waitcnt lgkmcnt(4)\n\t" \
    "v_mfma_f32_16x16x32_bf16 v[12:15], v[128:131], v[48:51], v[12:15]\n\t" \
    "s_waitcnt vmcnt(4)\n\t" \
    "s_barrier\n\t" \
    "s_setprio 0\n\t" \
    "s_sub_u32 %[a], %[t], 1\n\t" \
    "v_mov_b32 v53, %[tag]\n\t" \
    "v_mov_b32 v52, %[a]\n\t" \
    "s_cmp_gt_u32 %[t], 1\n\t" \
    "s_cselect_b64 exec, %[fm], 0\n\t" \
    "buffer_store_dwordx2 v[52:53], %[l16], %[rs], %[fo] offen sc1\n\t" \
    "s_mov_b64 exec, %[pm]\n\t" \
    "s_mov_b32 m0, %[pd]\n\t" \
    "s_add_u32 %[a], %[sx], %[po]\n\t" \
    "buffer_load_dwordx4 %[l16], %[rs], %[a] offen sc1 lds\n\t" \
    "s_mov_b64 exec, -1\n\t" \
    "s_add_u32 m0, %[sd], 0\n\t" \
    "s_add_u32 %[a], %[sx], 131072\n\t" \
    "buffer_load_dwordx4 %[l16], %[rs], %[a] offen offset:0 sc1 lds\n\t" \
    "buffer_load_dwordx4 %[l16], %[rs], %[a] offen offset:1024 sc1 lds\n\t" \
    "buffer_load_dwordx4 %[l16], %[rs], %[a] offen offset:2048 sc1 lds\n\t" \
    "buffer_load_dwordx4 %[l16], %[rs], %[a] offen offset:3072 sc1 lds\n\t" \
    "v_mfma_f32_16x16x32_bf16 v[0:3], v[132:135], v[20:23], v[0:3]\n\t" \
    "ds_read_b128 v[44:47], %[xp] offset:22528\n\t" \
    "v_mfma_f32_16x16x32_bf16 v[4:7], v[140:143], v[20:23], v[4:7]\n\t" \
    "ds_read_b128 v[48:51], %[xp] offset:23552\n\t" \
    "v_mfma_f32_16x16x32_bf16 v[8:11], v[132:135], v[24:27], v[8:11]\n\t" \
    "s_waitcnt lgkmcnt(4)\n\t" \
    "v_mfma_f32_16x16x32_bf16 v[12:15], v[140:143], v[24:27], v[12:15]\n\t" \
    "v_mfma_f32_16x16x32_bf16 v[0:3], v[136:139], v[28:31], v[0:3]\n\t" \
    "ds_read_b128 v[20:23], %[xp] offset:24576\n\t" \
    "v_mfma_f32_16x16x32_bf16 v[4:7], v[144:147], v[28:31], v[4:7]\n\t" \
    "ds_read_b128 v[24:27], %[xp] offset:25600\n\t" \
    "v_mfma_f32_16x16x32_bf16 v[8:11], v[136:139], v[32:35], v[8:11]\n\t" \
    "s_waitcnt lgkmcnt(4)\n\t" \
    "v_mfma_f32_16x16x32_bf16 v[12:15], v[144:147], v[32:35], v[12:15]\n\t" \
    "v_mfma_f32_16x16x32_bf16 v[0:3], v[148:151], v[36:39], v[0:3]\n\t" \
    "ds_read_b128 v[28:31], %[xp] offset:26624\n\t" \
    "v_mfma_f32_16x16x32_bf16 v[4:7], v[156:159], v[36:39], v[4:7]\n\t" \
    "ds_read_b128 v[32:35], %[xp] offset:27648\n\t" \
    "v_mfma_f32_16x16x32_bf16 v[8:11], v[148:151], v[40:43], v[8:11]\n\t" \
    "s_waitcnt lgkmcnt(4)\n\t" \
    "v_mfma_f32_16x16x32_bf16 v[12:15], v[156:159], v[40:43], v[12:15]\n\t" \
    "v_mfma_f32_16x16x32_bf16 v[0:3], v[152:155], v[44:47], v[0:3]\n\t" \
    "ds_read_b128 v[36:39], %[xp] offset:28672\n\t" \
    "v_mfma_f32_16x16x32_bf16 v[4:7], v[160:163], v[44:47], v[4:7]\n\t" \
    "ds_read_b128 v[40:43], %[xp] offset:29696\n\t" \
    "v_mfma_f32_16x16x32_bf16 v[8:11], v[152:155], v[48:51], v[8:11]\n\t" \
    "s_waitcnt lgkmcnt(4)\n\t" \
    "v_mfma_f32_16x16x32_bf16 v[12:15], v[160:163], v[48:51], v[12:15]\n\t" \
    "v_mfma_f32_16x16x32_bf16 v[0:3], v[164:167], v[20:23], v[0:3]\n\t" \
    "ds_read_b128 v[44:47], %[xp] offset:30720\n\t" \
    "v_mfma_f32_16x16x32_bf16 v[4:7], v[172:175], v[20:23], v[4:7]\n\t" \
    "ds_read_b128 v[48:51], %[xp] offset:31744\n\t" \
    "v_mfma_f32_16x16x32_bf16 v[8:11], v[164:167], v[24:27], v[8:11]\n\t" \
    "s_waitcnt lgkmcnt(4)\n\t" \
    "v_mfma_f32_16x16x32_bf16 v[12:15], v[172:175], v[24:27], v[12:15]\n\t" \
    "v_mfma_f32_16x16x32_bf16 v[0:3], v[168:171], v[28:31], v[0:3]\n\t" \
    "ds_read_b128 v[20:23], %[xp] offset:32768\n\t" \
    "v_mfma_f32_16x16x32_bf16 v[4:7], v[176:179], v[28:31], v[4:7]\n\t" \
    "ds_read_b128 v[24:27], %[xp] offset:33792\n\t" \
    "v_mfma_f32_16x16x32_bf16 v[8:11], v[168:171], v[32:35], v[8:11]\n\t" \
    "s_waitcnt lgkmcnt(4)\n\t" \
    "v_mfma_f32_16x16x32_bf16 v[12:15], v[176:179], v[32:35], v[12:15]\n\t" \
    "v_mfma_f32_16x16x32_bf16 v[0:3], v[180:183], v[36:39], v[0:3]\n\t" \
    "ds_read_b128 v[28:31], %[xp] offset:34816\n\t" \
    "v_mfma_f32_16x16x32_bf16 v[4:7], v[188:191], v[36:39], v[4:7]\n\t" \
    "ds_read_b128 v[32:35], %[xp] offset:35840\n\t" \
    "v_mfma_f32_16x16x32_bf16 v[8:11], v[180:183], v[40:43], v[8:11]\n\t" \
    "s_waitcnt lgkmcnt(4)\n\t" \
    "v_mfma_f32_16x16x32_bf16 v[12:15], v[188:191], v[40:43], v[12:15]\n\t" \
    "v_mfma_f32_16x16x32_bf16 v[0:3], v[184:187], v[44:47], v[0:3]\n\t" \
    "ds_read_b128 v[36:39], %[xp] offset:36864\n\t" \
    "v_mfma_f32_16x16x32_bf16 v[4:7], v[192:195], v[44:47], v[4:7]\n\t" \
    "ds_read_b128 v[40:43], %[xp] offset:37888\n\t" \
    "v_mfma_f32_16x16x32_bf16 v[8:11], v[184:187], v[48:51], v[8:11]\n\t" \
    "s_waitcnt lgkmcnt(4)\n\t" \
    "v_mfma_f32_16x16x32_bf16 v[12:15], v[192:195], v[48:51], v[12:15]\n\t" \
    "s_add_u32 %[sx], %[sx], 65536\n\t" \
    "s_add_u32 %[t], %[t], 1\n\t" \
    "s_waitcnt vmcnt(4)\n\t" \
    "s_barrier\n\t" \
    "s_setprio 0\n\t" \
    "s_nop 3\n\t" \
    "ds_write_b128 %[pa], v[0:3] offset:0\n\t" \
    "ds_write_b128 %[pa], v[4:7] offset:1024\n\t" \
    "ds_write_b128 %[pa], v[8:11] offset:2048\n\t" \
    "ds_write_b128 %[pa], v[12:15] offset:3072\n\t" \
    "s_sub_u32 %[a], %[t], 1\n\t" \
    "v_mov_b32 v53, %[tag]\n\t" \
    "v_mov_b32 v52, %[a]\n\t" \
    "s_sub_u32 %[b], %[t], 2\n\t" \
    "s_cmp_lt_u32 %[b], " STR(SQUICK) "\n\t" \
    "s_cselect_b64 exec, %[fm], 0\n\t" \
    "buffer_store_dwordx2 v[52:53], %[l16], %[rs], %[fo] offen sc1\n\t" \
    "s_mov_b64 exec, -1\n\t" \
    "s_add_u32 m0, %[sd], 16384\n\t" \
    "s_add_u32 %[a], %[sx], 81920\n\t" \
    "s_waitcnt lgkmcnt(0)\n\t" \
    "buffer_load_dwordx4 %[l16], %[rs], %[a] offen offset:0 sc1 lds\n\t" \
    "buffer_load_dwordx4 %[l16], %[rs], %[a] offen offset:1024 sc1 lds\n\t" \
    "buffer_load_dwordx4 %[l16], %[rs], %[a] offen offset:2048 sc1 lds\n\t" \
    "buffer_load_dwordx4 %[l16], %[rs], %[a] offen offset:3072 sc1 lds\n\t" \
    "v_mfma_f32_16x16x32_bf16 v[196:199], v[68:71], v[20:23], 0\n\t" \
    "ds_read_b128 v[44:47], %[xp] offset:38912\n\t" \
    "v_mfma_f32_16x16x32_bf16 v[200:203], v[76:79], v[20:23], 0\n\t" \
    "ds_read_b128 v[48:51], %[xp] offset:39936\n\t" \
    "v_mfma_f32_16x16x32_bf16 v[204:207], v[68:71], v[24:27], 0\n\t" \
    "v_mfma_f32_16x16x32_bf16 v[208:211], v[76:79], v[24:27], 0\n\t" \
    "v_mfma_f32_16x16x32_bf16 v[196:199], v[72:75], v[28:31], v[196:199]\n\t" \
    "ds_read_b128 v[20:23], %[xp] offset:40960\n\t" \
    "v_mfma_f32_16x16x32_bf16 v[200:203], v[80:83], v[28:31], v[200:203]\n\t" \
    "ds_read_b128 v[24:27], %[xp] offset:41984\n\t" \
    "v_mfma_f32_16x16x32_bf16 v[204:207], v[72:75], v[32:35], v[204:207]\n\t" \
    "v_mfma_f32_16x16x32_bf16 v[208:211], v[80:83], v[32:35], v[208:211]\n\t" \
    "v_mfma_f32_16x16x32_bf16 v[196:199], v[84:87], v[36:39], v[196:199]\n\t" \
    "ds_read_b128 v[28:31], %[xp] offset:43008\n\t" \
    "v_mfma_f32_16x16x32_bf16 v[200:203], v[92:95], v[36:39], v[200:203]\n\t" \
    "ds_read_b128 v[32:35], %[xp] offset:44032\n\t" \
    "v_mfma_f32_16x16x32_bf16 v[204:207], v[84:87], v[40:43], v[204:207]\n\t" \
    "s_waitcnt lgkmcnt(4)\n\t" \
    "v_mfma_f32_16x16x32_bf16 v[208:211], v[92:95], v[40:43], v[208:211]\n\t" \
    "v_mfma_f32_16x16x32_bf16 v[196:199], v[88:91], v[44:47], v[196:199]\n\t" \
    "ds_read_b128 v[36:39], %[xp] offset:45056\n\t" \
    "v_mfma_f32_16x16x32_bf16 v[200:203], v[96:99], v[44:47], v[200:203]\n\t" \
    "ds_read_b128 v[40:43], %[xp] offset:46080\n\t" \
    "v_mfma_f32_16x16x32_bf16 v[204:207], v[88:91], v[48:51], v[204:207]\n\t" \
    "s_waitcnt lgkmcnt(4)\n\t" \
    "v_mfma_f32_16x16x32_bf16 v[208:211], v[96:99], v[48:51], v[208:211]\n\t" \
    "v_mfma_f32_16x16x32_bf16 v[196:199], v[100:103], v[20:23], v[196:199]\n\t" \
    "ds_read_b128 v[44:47], %[xp] offset:47104\n\t" \
    "v_mfma_f32_16x16x32_bf16 v[200:203], v[108:111], v[20:23], v[200:203]\n\t" \
    "ds_read_b128 v[48:51], %[xp] offset:48128\n\t" \
    "v_mfma_f32_16x16x32_bf16 v[204:207], v[100:103], v[24:27], v[204:207]\n\t" \
    "s_waitcnt lgkmcnt(4)\n\t" \
    "v_mfma_f32_16x16x32_bf16 v[208:211], v[108:111], v[24:27], v[208:211]\n\t" \
    "v_mfma_f32_16x16x32_bf16 v[196:199], v[104:107], v[28:31], v[196:199]\n\t" \
    "ds_read_b128 v[20:23], %[xp] offset:49152\n\t" \
    "v_mfma_f32_16x16x32_bf16 v[200:203], v[112:115], v[28:31], v[200:203]\n\t" \
    "ds_read_b128 v[24:27], %[xp] offset:50176\n\t" \
    "v_mfma_f32_16x16x32_bf16 v[204:207], v[104:107], v[32:35], v[204:207]\n\t" \
    "s_waitcnt lgkmcnt(4)\n\t" \
    "v_mfma_f32_16x16x32_bf16 v[208:211], v[112:115], v[32:35], v[208:211]\n\t" \
    "v_mfma_f32_16x16x32_bf16 v[196:199], v[116:119], v[36:39], v[196:199]\n\t" \
    "ds_read_b128 v[28:31], %[xp] offset:51200\n\t" \
    "v_mfma_f32_16x16x32_bf16 v[200:203], v[124:127], v[36:39], v[200:203]\n\t" \
    "ds_read_b128 v[32:35], %[xp] offset:52224\n\t" \
    "v_mfma_f32_16x16x32_bf16 v[204:207], v[116:119], v[40:43], v[204:207]\n\t" \
    "s_waitcnt lgkmcnt(4)\n\t" \
    "v_mfma_f32_16x16x32_bf16 v[208:211], v[124:127], v[40:43], v[208:211]\n\t" \
    "v_mfma_f32_16x16x32_bf16 v[196:199], v[120:123], v[44:47], v[196:199]\n\t" \
    "ds_read_b128 v[36:39], %[xp] offset:53248\n\t" \
    "v_mfma_f32_16x16x32_bf16 v[200:203], v[128:131], v[44:47], v[200:203]\n\t" \
    "ds_read_b128 v[40:43], %[xp] offset:54272\n\t" \
    "v_mfma_f32_16x16x32_bf16 v[204:207], v[120:123], v[48:51], v[204:207]\n\t" \
    "s_waitcnt lgkmcnt(4)\n\t" \
    "v_mfma_f32_16x16x32_bf16 v[208:211], v[128:131], v[48:51], v[208:211]\n\t" \
    "s_waitcnt vmcnt(4)\n\t" \
    "s_barrier\n\t" \
    "s_setprio 0\n\t" \
    "s_sub_u32 %[a], %[t], 1\n\t" \
    "v_mov_b32 v53, %[tag]\n\t" \
    "v_mov_b32 v52, %[a]\n\t" \
    "s_cmp_gt_u32 %[t], 1\n\t" \
    "s_cselect_b64 exec, %[fm], 0\n\t" \
    "buffer_store_dwordx2 v[52:53], %[l16], %[rs], %[fo] offen sc1\n\t" \
    "s_mov_b64 exec, %[pm]\n\t" \
    "s_mov_b32 m0, %[pd]\n\t" \
    "s_add_u32 %[a], %[sx], %[po]\n\t" \
    "buffer_load_dwordx4 %[l16], %[rs], %[a] offen sc1 lds\n\t" \
    "s_mov_b64 exec, -1\n\t" \
    "s_add_u32 m0, %[sd], 32768\n\t" \
    "s_add_u32 %[a], %[sx], 131072\n\t" \
    "buffer_load_dwordx4 %[l16], %[rs], %[a] offen offset:0 sc1 lds\n\t" \
    "buffer_load_dwordx4 %[l16], %[rs], %[a] offen offset:1024 sc1 lds\n\t" \
    "buffer_load_dwordx4 %[l16], %[rs], %[a] offen offset:2048 sc1 lds\n\t" \
    "buffer_load_dwordx4 %[l16], %[rs], %[a] offen offset:3072 sc1 lds\n\t" \
    "v_mfma_f32_16x16x32_bf16 v[196:199], v[132:135], v[20:23], v[196:199]\n\t" \
    "ds_read_b128 v[44:47], %[xp] offset:55296\n\t" \
    "v_mfma_f32_16x16x32_bf16 v[200:203], v[140:143], v[20:23], v[200:203]\n\t" \
    "ds_read_b128 v[48:51], %[xp] offset:56320\n\t" \
    "v_mfma_f32_16x16x32_bf16 v[204:207], v[132:135], v[24:27], v[204:207]\n\t" \
    "s_waitcnt lgkmcnt(4)\n\t" \
    "v_mfma_f32_16x16x32_bf16 v[208:211], v[140:143], v[24:27], v[208:211]\n\t" \
    "v_mfma_f32_16x16x32_bf16 v[196:199], v[136:139], v[28:31], v[196:199]\n\t" \
    "ds_read_b128 v[20:23], %[xp] offset:57344\n\t" \
    "v_mfma_f32_16x16x32_bf16 v[200:203], v[144:147], v[28:31], v[200:203]\n\t" \
    "ds_read_b128 v[24:27], %[xp] offset:58368\n\t" \
    "v_mfma_f32_16x16x32_bf16 v[204:207], v[136:139], v[32:35], v[204:207]\n\t" \
    "s_waitcnt lgkmcnt(4)\n\t" \
    "v_mfma_f32_16x16x32_bf16 v[208:211], v[144:147], v[32:35], v[208:211]\n\t" \
    "v_mfma_f32_16x16x32_bf16 v[196:199], v[148:151], v[36:39], v[196:199]\n\t" \
    "ds_read_b128 v[28:31], %[xp] offset:59392\n\t" \
    "v_mfma_f32_16x16x32_bf16 v[200:203], v[156:159], v[36:39], v[200:203]\n\t" \
    "ds_read_b128 v[32:35], %[xp] offset:60416\n\t" \
    "v_mfma_f32_16x16x32_bf16 v[204:207], v[148:151], v[40:43], v[204:207]\n\t" \
    "s_waitcnt lgkmcnt(4)\n\t" \
    "v_mfma_f32_16x16x32_bf16 v[208:211], v[156:159], v[40:43], v[208:211]\n\t" \
    "v_mfma_f32_16x16x32_bf16 v[196:199], v[152:155], v[44:47], v[196:199]\n\t" \
    "ds_read_b128 v[36:39], %[xp] offset:61440\n\t" \
    "v_mfma_f32_16x16x32_bf16 v[200:203], v[160:163], v[44:47], v[200:203]\n\t" \
    "ds_read_b128 v[40:43], %[xp] offset:62464\n\t" \
    "v_mfma_f32_16x16x32_bf16 v[204:207], v[152:155], v[48:51], v[204:207]\n\t" \
    "s_waitcnt lgkmcnt(4)\n\t" \
    "v_mfma_f32_16x16x32_bf16 v[208:211], v[160:163], v[48:51], v[208:211]\n\t" \
    "v_mfma_f32_16x16x32_bf16 v[196:199], v[164:167], v[20:23], v[196:199]\n\t" \
    "ds_read_b128 v[44:47], %[xp] offset:63488\n\t" \
    "v_mfma_f32_16x16x32_bf16 v[200:203], v[172:175], v[20:23], v[200:203]\n\t" \
    "ds_read_b128 v[48:51], %[xp] offset:64512\n\t" \
    "v_mfma_f32_16x16x32_bf16 v[204:207], v[164:167], v[24:27], v[204:207]\n\t" \
    "s_waitcnt lgkmcnt(4)\n\t" \
    "v_mfma_f32_16x16x32_bf16 v[208:211], v[172:175], v[24:27], v[208:211]\n\t" \
    "v_mfma_f32_16x16x32_bf16 v[196:199], v[168:171], v[28:31], v[196:199]\n\t" \
    "ds_read_b128 v[20:23], %[xp] offset:0\n\t" \
    "v_mfma_f32_16x16x32_bf16 v[200:203], v[176:179], v[28:31], v[200:203]\n\t" \
    "ds_read_b128 v[24:27], %[xp] offset:1024\n\t" \
    "v_mfma_f32_16x16x32_bf16 v[204:207], v[168:171], v[32:35], v[204:207]\n\t" \
    "s_waitcnt lgkmcnt(4)\n\t" \
    "v_mfma_f32_16x16x32_bf16 v[208:211], v[176:179], v[32:35], v[208:211]\n\t" \
    "v_mfma_f32_16x16x32_bf16 v[196:199], v[180:183], v[36:39], v[196:199]\n\t" \
    "ds_read_b128 v[28:31], %[xp] offset:2048\n\t" \
    "v_mfma_f32_16x16x32_bf16 v[200:203], v[188:191], v[36:39], v[200:203]\n\t" \
    "ds_read_b128 v[32:35], %[xp] offset:3072\n\t" \
    "v_mfma_f32_16x16x32_bf16 v[204:207], v[180:183], v[40:43], v[204:207]\n\t" \
    "s_waitcnt lgkmcnt(4)\n\t" \
    "v_mfma_f32_16x16x32_bf16 v[208:211], v[188:191], v[40:43], v[208:211]\n\t" \
    "v_mfma_f32_16x16x32_bf16 v[196:199], v[184:187], v[44:47], v[196:199]\n\t" \
    "ds_read_b128 v[36:39], %[xp] offset:4096\n\t" \
    "v_mfma_f32_16x16x32_bf16 v[200:203], v[192:195], v[44:47], v[200:203]\n\t" \
    "ds_read_b128 v[40:43], %[xp] offset:5120\n\t" \
    "v_mfma_f32_16x16x32_bf16 v[204:207], v[184:187], v[48:51], v[204:207]\n\t" \
    "s_waitcnt lgkmcnt(4)\n\t" \
    "v_mfma_f32_16x16x32_bf16 v[208:211], v[192:195], v[48:51], v[208:211]\n\t" \
    "s_add_u32 %[sx], %[sx], 65536\n\t" \
    "s_add_u32 %[t], %[t], 1\n\t" \
    "s_waitcnt vmcnt(4)\n\t" \
    "s_barrier\n\t" \
    "s_cmp_lt_u32 %[t], %[te]\n\t" \
    "s_cbranch_scc1 L0_%=\n\t" \
    "s_cmp_eq_u32 %[fin], %[te]\n\t" \
    "s_cbranch_scc0 E_%=\n\t" \
    "s_nop 4\n\t" \
    "ds_write_b128 %[pa], v[196:199] offset:0\n\t" \
    "ds_write_b128 %[pa], v[200:203] offset:1024\n\t" \
    "ds_write_b128 %[pa], v[204:207] offset:2048\n\t" \
    "ds_write_b128 %[pa], v[208:211] offset:3072\n\t" \
    "s_waitcnt lgkmcnt(0)\n\t" \
    "s_barrier\n\t" \
    "s_branch E_%=\n\t" \
    "L1_%=:\n\t" \
    "s_setprio 1\n\t" \
    "s_add_u32 m0, %[sd], 49152\n\t" \
    "s_add_u32 %[a], %[sx], 81920\n\t" \
    "v_mfma_f32_16x16x32_bf16 v[0:3], v[68:71], v[20:23], 0\n\t" \
    "ds_read_b128 v[44:47], %[xp] offset:6144\n\t" \
    "v_mfma_f32_16x16x32_bf16 v[4:7], v[76:79], v[20:23], 0\n\t" \
    "ds_read_b128 v[48:51], %[xp] offset:7168\n\t" \
    "v_mfma_f32_16x16x32_bf16 v[8:11], v[68:71], v[24:27], 0\n\t" \
    "s_waitcnt lgkmcnt(4)\n\t" \
    "v_mfma_f32_16x16x32_bf16 v[12:15], v[76:79], v[24:27], 0\n\t" \
    "v_mfma_f32_16x16x32_bf16 v[0:3], v[72:75], v[28:31], v[0:3]\n\t" \
    "ds_read_b128 v[20:23], %[xp] offset:8192\n\t" \
    "v_mfma_f32_16x16x32_bf16 v[4:7], v[80:83], v[28:31], v[4:7]\n\t" \
    "ds_read_b128 v[24:27], %[xp] offset:9216\n\t" \
    "v_mfma_f32_16x16x32_bf16 v[8:11], v[72:75], v[32:35], v[8:11]\n\t" \
    "s_waitcnt lgkmcnt(4)\n\t" \
    "v_mfma_f32_16x16x32_bf16 v[12:15], v[80:83], v[32:35], v[12:15]\n\t" \
    "v_mfma_f32_16x16x32_bf16 v[0:3], v[84:87], v[36:39], v[0:3]\n\t" \
    "ds_read_b128 v[28:31], %[xp] offset:10240\n\t" \
    "v_mfma_f32_16x16x32_bf16 v[4:7], v[92:95], v[36:39], v[4:7]\n\t" \
    "ds_read_b128 v[32:35], %[xp] offset:11264\n\t" \
    "v_mfma_f32_16x16x32_bf16 v[8:11], v[84:87], v[40:43], v[8:11]\n\t" \
    "s_waitcnt lgkmcnt(4)\n\t" \
    "v_mfma_f32_16x16x32_bf16 v[12:15], v[92:95], v[40:43], v[12:15]\n\t" \
    "v_mfma_f32_16x16x32_bf16 v[0:3], v[88:91], v[44:47], v[0:3]\n\t" \
    "ds_read_b128 v[36:39], %[xp] offset:12288\n\t" \
    "v_mfma_f32_16x16x32_bf16 v[4:7], v[96:99], v[44:47], v[4:7]\n\t" \
    "ds_read_b128 v[40:43], %[xp] offset:13312\n\t" \
    "v_mfma_f32_16x16x32_bf16 v[8:11], v[88:91], v[48:51], v[8:11]\n\t" \
    "s_waitcnt lgkmcnt(4)\n\t" \
    "v_mfma_f32_16x16x32_bf16 v[12:15], v[96:99], v[48:51], v[12:15]\n\t" \
    "v_mfma_f32_16x16x32_bf16 v[0:3], v[100:103], v[20:23], v[0:3]\n\t" \
    "ds_read_b128 v[44:47], %[xp] offset:14336\n\t" \
    "v_mfma_f32_16x16x32_bf16 v[4:7], v[108:111], v[20:23], v[4:7]\n\t" \
    "ds_read_b128 v[48:51], %[xp] offset:15360\n\t" \
    "v_mfma_f32_16x16x32_bf16 v[8:11], v[100:103], v[24:27], v[8:11]\n\t" \
    "s_waitcnt lgkmcnt(4)\n\t" \
    "v_mfma_f32_16x16x32_bf16 v[12:15], v[108:111], v[24:27], v[12:15]\n\t" \
    "buffer_load_dwordx4 %[l16], %[rs], %[a] offen offset:0 sc1 lds\n\t" \
    "v_mfma_f32_16x16x32_bf16 v[0:3], v[104:107], v[28:31], v[0:3]\n\t" \
    "ds_read_b128 v[20:23], %[xp] offset:16384\n\t" \
    "v_mfma_f32_16x16x32_bf16 v[4:7], v[112:115], v[28:31], v[4:7]\n\t" \
    "ds_read_b128 v[24:27], %[xp] offset:17408\n\t" \
    "v_mfma_f32_16x16x32_bf16 v[8:11], v[104:107], v[32:35], v[8:11]\n\t" \
    "s_waitcnt lgkmcnt(4)\n\t" \
    "v_mfma_f32_16x16x32_bf16 v[12:15], v[112:115], v[32:35], v[12:15]\n\t" \
    "buffer_load_dwordx4 %[l16], %[rs], %[a] offen offset:1024 sc1 lds\n\t" \
    "v_mfma_f32_16x16x32_bf16 v[0:3], v[116:119], v[36:39], v[0:3]\n\t" \
    "ds_read_b128 v[28:31], %[xp] offset:18432\n\t" \
    "v_mfma_f32_16x16x32_bf16 v[4:7], v[124:127], v[36:39], v[4:7]\n\t" \
    "ds_read_b128 v[32:35], %[xp] offset:19456\n\t" \
    "v_mfma_f32_16x16x32_bf16 v[8:11], v[116:119], v[40:43], v[8:11]\n\t" \
    "s_waitcnt lgkmcnt(4)\n\t" \
    "v_mfma_f32_16x16x32_bf16 v[12:15], v[124:127], v[40:43], v[12:15]\n\t" \
    "buffer_load_dwordx4 %[l16], %[rs], %[a] offen offset:2048 sc1 lds\n\t" \
    "v_mfma_f32_16x16x32_bf16 v[0:3], v[120:123], v[44:47], v[0:3]\n\t" \
    "ds_read_b128 v[36:39], %[xp] offset:20480\n\t" \
    "v_mfma_f32_16x16x32_bf16 v[4:7], v[128:131], v[44:47], v[4:7]\n\t" \
    "ds_read_b128 v[40:43], %[xp] offset:21504\n\t" \
    "v_mfma_f32_16x16x32_bf16 v[8:11], v[120:123], v[48:51], v[8:11]\n\t" \
    "s_waitcnt lgkmcnt(4)\n\t" \
    "v_mfma_f32_16x16x32_bf16 v[12:15], v[128:131], v[48:51], v[12:15]\n\t" \
    "buffer_load_dwordx4 %[l16], %[rs], %[a] offen offset:3072 sc1 lds\n\t" \
    "s_waitcnt vmcnt(4)\n\t" \
    "s_barrier\n\t" \
    "s_setprio 1\n\t" \
    "s_add_u32 m0, %[sd], 0\n\t" \
    "s_add_u32 %[a], %[sx], 131072\n\t" \
    "s_add_u32 %[b], %[oo], 0x1000\n\t" \
    "s_cmp_lg_u32 %[t], 1\n\t" \
    "s_cselect_b64 %[em], -1, 0\n\t" \
    "v_mfma_f32_16x16x32_bf16 v[0:3], v[132:135], v[20:23], v[0:3]\n\t" \
    "ds_read_b128 v[44:47], %[xp] offset:22528\n\t" \
    "v_mfma_f32_16x16x32_bf16 v[4:7], v[140:143], v[20:23], v[4:7]\n\t" \
    "ds_read_b128 v[48:51], %[xp] offset:23552\n\t" \
    "v_mfma_f32_16x16x32_bf16 v[8:11], v[132:135], v[24:27], v[8:11]\n\t" \
    "ds_read_b128 v[52:55], %[pa] offset:0\n\t" \
    "s_waitcnt lgkmcnt(5)\n\t" \
    "v_mfma_f32_16x16x32_bf16 v[12:15], v[140:143], v[24:27], v[12:15]\n\t" \
    "buffer_load_dwordx4 %[l16], %[rs], %[a] offen offset:0 sc1 lds\n\t" \
    "v_mfma_f32_16x16x32_bf16 v[0:3], v[136:139], v[28:31], v[0:3]\n\t" \
    "ds_read_b128 v[20:23], %[xp] offset:24576\n\t" \
    "v_mfma_f32_16x16x32_bf16 v[4:7], v[144:147], v[28:31], v[4:7]\n\t" \
    "ds_read_b128 v[24:27], %[xp] offset:25600\n\t" \
    "v_mfma_f32_16x16x32_bf16 v[8:11], v[136:139], v[32:35], v[8:11]\n\t" \
    "ds_read_b128 v[56:59], %[pa] offset:1024\n\t" \
    "s_waitcnt lgkmcnt(6)\n\t" \
    "v_mfma_f32_16x16x32_bf16 v[12:15], v[144:147], v[32:35], v[12:15]\n\t" \
    "buffer_load_dwordx4 %[l16], %[rs], %[a] offen offset:1024 sc1 lds\n\t" \
    "v_mfma_f32_16x16x32_bf16 v[0:3], v[148:151], v[36:39], v[0:3]\n\t" \
    "ds_read_b128 v[28:31], %[xp] offset:26624\n\t" \
    "v_mfma_f32_16x16x32_bf16 v[4:7], v[156:159], v[36:39], v[4:7]\n\t" \
    "ds_read_b128 v[32:35], %[xp] offset:27648\n\t" \
    "v_mfma_f32_16x16x32_bf16 v[8:11], v[148:151], v[40:43], v[8:11]\n\t" \
    "ds_read_b128 v[60:63], %[pa] offset:2048\n\t" \
    "s_waitcnt lgkmcnt(7)\n\t" \
    "v_mfma_f32_16x16x32_bf16 v[12:15], v[156:159], v[40:43], v[12:15]\n\t" \
    "buffer_load_dwordx4 %[l16], %[rs], %[a] offen offset:2048 sc1 lds\n\t" \
    "v_mfma_f32_16x16x32_bf16 v[0:3], v[152:155], v[44:47], v[0:3]\n\t" \
    "ds_read_b128 v[36:39], %[xp] offset:28672\n\t" \
    "s_waitcnt lgkmcnt(7)\n\t" \
    "v_add_f32 v196, v196, v52\n\t" \
    "v_mfma_f32_16x16x32_bf16 v[4:7], v[160:163], v[44:47], v[4:7]\n\t" \
    "ds_read_b128 v[40:43], %[xp] offset:29696\n\t" \
    "v_add_f32 v197, v197, v53\n\t" \
    "v_mfma_f32_16x16x32_bf16 v[8:11], v[152:155], v[48:51], v[8:11]\n\t" \
    "ds_read_b128 v[64:67], %[pa] offset:3072\n\t" \
    "s_waitcnt lgkmcnt(7)\n\t" \
    "v_mfma_f32_16x16x32_bf16 v[12:15], v[160:163], v[48:51], v[12:15]\n\t" \
    "buffer_load_dwordx4 %[l16], %[rs], %[a] offen offset:3072 sc1 lds\n\t" \
    "v_add_f32 v198, v198, v54\n\t" \
    "v_add_f32 v199, v199, v55\n\t" \
    "v_mfma_f32_16x16x32_bf16 v[0:3], v[164:167], v[20:23], v[0:3]\n\t" \
    "ds_read_b128 v[44:47], %[xp] offset:30720\n\t" \
    "s_waitcnt lgkmcnt(7)\n\t" \
    "v_add_f32 v200, v200, v56\n\t" \
    "v_mfma_f32_16x16x32_bf16 v[4:7], v[172:175], v[20:23], v[4:7]\n\t" \
    "ds_read_b128 v[48:51], %[xp] offset:31744\n\t" \
    "v_add_f32 v201, v201, v57\n\t" \
    "v_mfma_f32_16x16x32_bf16 v[8:11], v[164:167], v[24:27], v[8:11]\n\t" \
    "v_add_f32 v202, v202, v58\n\t" \
    "s_waitcnt lgkmcnt(6)\n\t" \
    "v_mfma_f32_16x16x32_bf16 v[12:15], v[172:175], v[24:27], v[12:15]\n\t" \
    "v_add_f32 v203, v203, v59\n\t" \
    "s_waitcnt lgkmcnt(5)\n\t" \
    "v_add_f32 v204, v204, v60\n\t" \
    "v_add_f32 v205, v205, v61\n\t" \
    "v_mfma_f32_16x16x32_bf16 v[0:3], v[168:171], v[28:31], v[0:3]\n\t" \
    "ds_read_b128 v[20:23], %[xp] offset:32768\n\t" \
    "v_add_f32 v206, v206, v62\n\t" \
    "v_mfma_f32_16x16x32_bf16 v[4:7], v[176:179], v[28:31], v[4:7]\n\t" \
    "ds_read_b128 v[24:27], %[xp] offset:33792\n\t" \
    "v_add_f32 v207, v207, v63\n\t" \
    "v_mfma_f32_16x16x32_bf16 v[8:11], v[168:171], v[32:35], v[8:11]\n\t" \
    "s_waitcnt lgkmcnt(4)\n\t" \
    "v_add_f32 v208, v208, v64\n\t" \
    "v_mfma_f32_16x16x32_bf16 v[12:15], v[176:179], v[32:35], v[12:15]\n\t" \
    "v_add_f32 v209, v209, v65\n\t" \
    "v_add_f32 v210, v210, v66\n\t" \
    "v_add_f32 v211, v211, v67\n\t" \
    "v_mfma_f32_16x16x32_bf16 v[0:3], v[180:183], v[36:39], v[0:3]\n\t" \
    "ds_read_b128 v[28:31], %[xp] offset:34816\n\t" \
    "v_cvt_pk_bf16_f32 v52, v196, v200\n\t" \
    "v_mfma_f32_16x16x32_bf16 v[4:7], v[188:191], v[36:39], v[4:7]\n\t" \
    "ds_read_b128 v[32:35], %[xp] offset:35840\n\t" \
    "v_cvt_pk_bf16_f32 v53, v197, v201\n\t" \
    "v_mfma_f32_16x16x32_bf16 v[8:11], v[180:183], v[40:43], v[8:11]\n\t" \
    "v_cvt_pk_bf16_f32 v54, v198, v202\n\t" \
    "s_waitcnt lgkmcnt(4)\n\t" \
    "v_mfma_f32_16x16x32_bf16 v[12:15], v[188:191], v[40:43], v[12:15]\n\t" \
    "v_cvt_pk_bf16_f32 v55, v199, v203\n\t" \
    "v_cvt_pk_bf16_f32 v56, v204, v208\n\t" \
    "v_cvt_pk_bf16_f32 v57, v205, v209\n\t" \
    "v_mfma_f32_16x16x32_bf16 v[0:3], v[184:187], v[44:47], v[0:3]\n\t" \
    "ds_read_b128 v[36:39], %[xp] offset:36864\n\t" \
    "v_cvt_pk_bf16_f32 v58, v206, v210\n\t" \
    "v_mfma_f32_16x16x32_bf16 v[4:7], v[192:195], v[44:47], v[4:7]\n\t" \
    "ds_read_b128 v[40:43], %[xp] offset:37888\n\t" \
    "v_cvt_pk_bf16_f32 v59, v207, v211\n\t" \
    "v_mfma_f32_16x16x32_bf16 v[8:11], v[184:187], v[48:51], v[8:11]\n\t" \
    "s_waitcnt lgkmcnt(4)\n\t" \
    "v_mfma_f32_16x16x32_bf16 v[12:15], v[192:195], v[48:51], v[12:15]\n\t" \
    "s_mov_b64 exec, %[em]\n\t" \
    "buffer_store_dwordx4 v[52:55], %[l16], %[rs], %[oo] offen sc1\n\t" \
    "buffer_store_dwordx4 v[56:59], %[l16], %[rs], %[b] offen sc1\n\t" \
    "s_mov_b64 exec, -1\n\t" \
    "s_add_u32 %[sx], %[sx], 65536\n\t" \
    "s_add_u32 %[t], %[t], 1\n\t" \
    "s_add_u32 %[oo], %[oo], 262144\n\t" \
    "s_waitcnt vmcnt(6)\n\t" \
    "s_cmp_gt_u32 %[t], " STR(SQUICK) " + 1\n\t" \
    "s_cbranch_scc1 B0_%=\n\t" \
    "s_waitcnt vmcnt(0)\n\t" \
    "B0_%=:\n\t" \
    "s_barrier\n\t" \
    "s_setprio 1\n\t" \
    "s_add_u32 m0, %[sd], 16384\n\t" \
    "s_add_u32 %[a], %[sx], 81920\n\t" \
    "v_mfma_f32_16x16x32_bf16 v[196:199], v[68:71], v[20:23], 0\n\t" \
    "ds_read_b128 v[44:47], %[xp] offset:38912\n\t" \
    "v_mfma_f32_16x16x32_bf16 v[200:203], v[76:79], v[20:23], 0\n\t" \
    "ds_read_b128 v[48:51], %[xp] offset:39936\n\t" \
    "v_mfma_f32_16x16x32_bf16 v[204:207], v[68:71], v[24:27], 0\n\t" \
    "s_waitcnt lgkmcnt(4)\n\t" \
    "v_mfma_f32_16x16x32_bf16 v[208:211], v[76:79], v[24:27], 0\n\t" \
    "v_mfma_f32_16x16x32_bf16 v[196:199], v[72:75], v[28:31], v[196:199]\n\t" \
    "ds_read_b128 v[20:23], %[xp] offset:40960\n\t" \
    "v_mfma_f32_16x16x32_bf16 v[200:203], v[80:83], v[28:31], v[200:203]\n\t" \
    "ds_read_b128 v[24:27], %[xp] offset:41984\n\t" \
    "v_mfma_f32_16x16x32_bf16 v[204:207], v[72:75], v[32:35], v[204:207]\n\t" \
    "s_waitcnt lgkmcnt(4)\n\t" \
    "v_mfma_f32_16x16x32_bf16 v[208:211], v[80:83], v[32:35], v[208:211]\n\t" \
    "v_mfma_f32_16x16x32_bf16 v[196:199], v[84:87], v[36:39], v[196:199]\n\t" \
    "ds_read_b128 v[28:31], %[xp] offset:43008\n\t" \
    "v_mfma_f32_16x16x32_bf16 v[200:203], v[92:95], v[36:39], v[200:203]\n\t" \
    "ds_read_b128 v[32:35], %[xp] offset:44032\n\t" \
    "v_mfma_f32_16x16x32_bf16 v[204:207], v[84:87], v[40:43], v[204:207]\n\t" \
    "s_waitcnt lgkmcnt(4)\n\t" \
    "v_mfma_f32_16x16x32_bf16 v[208:211], v[92:95], v[40:43], v[208:211]\n\t" \
    "v_mfma_f32_16x16x32_bf16 v[196:199], v[88:91], v[44:47], v[196:199]\n\t" \
    "ds_read_b128 v[36:39], %[xp] offset:45056\n\t" \
    "v_mfma_f32_16x16x32_bf16 v[200:203], v[96:99], v[44:47], v[200:203]\n\t" \
    "ds_read_b128 v[40:43], %[xp] offset:46080\n\t" \
    "v_mfma_f32_16x16x32_bf16 v[204:207], v[88:91], v[48:51], v[204:207]\n\t" \
    "s_waitcnt lgkmcnt(4)\n\t" \
    "v_mfma_f32_16x16x32_bf16 v[208:211], v[96:99], v[48:51], v[208:211]\n\t" \
    "v_mfma_f32_16x16x32_bf16 v[196:199], v[100:103], v[20:23], v[196:199]\n\t" \
    "ds_read_b128 v[44:47], %[xp] offset:47104\n\t" \
    "v_mfma_f32_16x16x32_bf16 v[200:203], v[108:111], v[20:23], v[200:203]\n\t" \
    "ds_read_b128 v[48:51], %[xp] offset:48128\n\t" \
    "v_mfma_f32_16x16x32_bf16 v[204:207], v[100:103], v[24:27], v[204:207]\n\t" \
    "s_waitcnt lgkmcnt(4)\n\t" \
    "v_mfma_f32_16x16x32_bf16 v[208:211], v[108:111], v[24:27], v[208:211]\n\t" \
    "buffer_load_dwordx4 %[l16], %[rs], %[a] offen offset:0 sc1 lds\n\t" \
    "v_mfma_f32_16x16x32_bf16 v[196:199], v[104:107], v[28:31], v[196:199]\n\t" \
    "ds_read_b128 v[20:23], %[xp] offset:49152\n\t" \
    "v_mfma_f32_16x16x32_bf16 v[200:203], v[112:115], v[28:31], v[200:203]\n\t" \
    "ds_read_b128 v[24:27], %[xp] offset:50176\n\t" \
    "v_mfma_f32_16x16x32_bf16 v[204:207], v[104:107], v[32:35], v[204:207]\n\t" \
    "s_waitcnt lgkmcnt(4)\n\t" \
    "v_mfma_f32_16x16x32_bf16 v[208:211], v[112:115], v[32:35], v[208:211]\n\t" \
    "buffer_load_dwordx4 %[l16], %[rs], %[a] offen offset:1024 sc1 lds\n\t" \
    "v_mfma_f32_16x16x32_bf16 v[196:199], v[116:119], v[36:39], v[196:199]\n\t" \
    "ds_read_b128 v[28:31], %[xp] offset:51200\n\t" \
    "v_mfma_f32_16x16x32_bf16 v[200:203], v[124:127], v[36:39], v[200:203]\n\t" \
    "ds_read_b128 v[32:35], %[xp] offset:52224\n\t" \
    "v_mfma_f32_16x16x32_bf16 v[204:207], v[116:119], v[40:43], v[204:207]\n\t" \
    "s_waitcnt lgkmcnt(4)\n\t" \
    "v_mfma_f32_16x16x32_bf16 v[208:211], v[124:127], v[40:43], v[208:211]\n\t" \
    "buffer_load_dwordx4 %[l16], %[rs], %[a] offen offset:2048 sc1 lds\n\t" \
    "v_mfma_f32_16x16x32_bf16 v[196:199], v[120:123], v[44:47], v[196:199]\n\t" \
    "ds_read_b128 v[36:39], %[xp] offset:53248\n\t" \
    "v_mfma_f32_16x16x32_bf16 v[200:203], v[128:131], v[44:47], v[200:203]\n\t" \
    "ds_read_b128 v[40:43], %[xp] offset:54272\n\t" \
    "v_mfma_f32_16x16x32_bf16 v[204:207], v[120:123], v[48:51], v[204:207]\n\t" \
    "s_waitcnt lgkmcnt(4)\n\t" \
    "v_mfma_f32_16x16x32_bf16 v[208:211], v[128:131], v[48:51], v[208:211]\n\t" \
    "buffer_load_dwordx4 %[l16], %[rs], %[a] offen offset:3072 sc1 lds\n\t" \
    "s_waitcnt vmcnt(4)\n\t" \
    "s_barrier\n\t" \
    "s_setprio 1\n\t" \
    "s_add_u32 m0, %[sd], 32768\n\t" \
    "s_add_u32 %[a], %[sx], 131072\n\t" \
    "s_add_u32 %[b], %[oo], 0x1000\n\t" \
    "s_cmp_lg_u32 %[t], 1\n\t" \
    "s_cselect_b64 %[em], -1, 0\n\t" \
    "v_mfma_f32_16x16x32_bf16 v[196:199], v[132:135], v[20:23], v[196:199]\n\t" \
    "ds_read_b128 v[44:47], %[xp] offset:55296\n\t" \
    "v_mfma_f32_16x16x32_bf16 v[200:203], v[140:143], v[20:23], v[200:203]\n\t" \
    "ds_read_b128 v[48:51], %[xp] offset:56320\n\t" \
    "v_mfma_f32_16x16x32_bf16 v[204:207], v[132:135], v[24:27], v[204:207]\n\t" \
    "ds_read_b128 v[52:55], %[pa] offset:0\n\t" \
    "s_waitcnt lgkmcnt(5)\n\t" \
    "v_mfma_f32_16x16x32_bf16 v[208:211], v[140:143], v[24:27], v[208:211]\n\t" \
    "buffer_load_dwordx4 %[l16], %[rs], %[a] offen offset:0 sc1 lds\n\t" \
    "v_mfma_f32_16x16x32_bf16 v[196:199], v[136:139], v[28:31], v[196:199]\n\t" \
    "ds_read_b128 v[20:23], %[xp] offset:57344\n\t" \
    "v_mfma_f32_16x16x32_bf16 v[200:203], v[144:147], v[28:31], v[200:203]\n\t" \
    "ds_read_b128 v[24:27], %[xp] offset:58368\n\t" \
    "v_mfma_f32_16x16x32_bf16 v[204:207], v[136:139], v[32:35], v[204:207]\n\t" \
    "ds_read_b128 v[56:59], %[pa] offset:1024\n\t" \
    "s_waitcnt lgkmcnt(6)\n\t" \
    "v_mfma_f32_16x16x32_bf16 v[208:211], v[144:147], v[32:35], v[208:211]\n\t" \
    "buffer_load_dwordx4 %[l16], %[rs], %[a] offen offset:1024 sc1 lds\n\t" \
    "v_mfma_f32_16x16x32_bf16 v[196:199], v[148:151], v[36:39], v[196:199]\n\t" \
    "ds_read_b128 v[28:31], %[xp] offset:59392\n\t" \
    "v_mfma_f32_16x16x32_bf16 v[200:203], v[156:159], v[36:39], v[200:203]\n\t" \
    "ds_read_b128 v[32:35], %[xp] offset:60416\n\t" \
    "v_mfma_f32_16x16x32_bf16 v[204:207], v[148:151], v[40:43], v[204:207]\n\t" \
    "ds_read_b128 v[60:63], %[pa] offset:2048\n\t" \
    "s_waitcnt lgkmcnt(7)\n\t" \
    "v_mfma_f32_16x16x32_bf16 v[208:211], v[156:159], v[40:43], v[208:211]\n\t" \
    "buffer_load_dwordx4 %[l16], %[rs], %[a] offen offset:2048 sc1 lds\n\t" \
    "v_mfma_f32_16x16x32_bf16 v[196:199], v[152:155], v[44:47], v[196:199]\n\t" \
    "ds_read_b128 v[36:39], %[xp] offset:61440\n\t" \
    "s_waitcnt lgkmcnt(7)\n\t" \
    "v_add_f32 v0, v0, v52\n\t" \
    "v_mfma_f32_16x16x32_bf16 v[200:203], v[160:163], v[44:47], v[200:203]\n\t" \
    "ds_read_b128 v[40:43], %[xp] offset:62464\n\t" \
    "v_add_f32 v1, v1, v53\n\t" \
    "v_mfma_f32_16x16x32_bf16 v[204:207], v[152:155], v[48:51], v[204:207]\n\t" \
    "ds_read_b128 v[64:67], %[pa] offset:3072\n\t" \
    "s_waitcnt lgkmcnt(7)\n\t" \
    "v_mfma_f32_16x16x32_bf16 v[208:211], v[160:163], v[48:51], v[208:211]\n\t" \
    "buffer_load_dwordx4 %[l16], %[rs], %[a] offen offset:3072 sc1 lds\n\t" \
    "v_add_f32 v2, v2, v54\n\t" \
    "v_add_f32 v3, v3, v55\n\t" \
    "v_mfma_f32_16x16x32_bf16 v[196:199], v[164:167], v[20:23], v[196:199]\n\t" \
    "ds_read_b128 v[44:47], %[xp] offset:63488\n\t" \
    "s_waitcnt lgkmcnt(7)\n\t" \
    "v_add_f32 v4, v4, v56\n\t" \
    "v_mfma_f32_16x16x32_bf16 v[200:203], v[172:175], v[20:23], v[200:203]\n\t" \
    "ds_read_b128 v[48:51], %[xp] offset:64512\n\t" \
    "v_add_f32 v5, v5, v57\n\t" \
    "v_mfma_f32_16x16x32_bf16 v[204:207], v[164:167], v[24:27], v[204:207]\n\t" \
    "v_add_f32 v6, v6, v58\n\t" \
    "s_waitcnt lgkmcnt(6)\n\t" \
    "v_mfma_f32_16x16x32_bf16 v[208:211], v[172:175], v[24:27], v[208:211]\n\t" \
    "v_add_f32 v7, v7, v59\n\t" \
    "s_waitcnt lgkmcnt(5)\n\t" \
    "v_add_f32 v8, v8, v60\n\t" \
    "v_add_f32 v9, v9, v61\n\t" \
    "v_mfma_f32_16x16x32_bf16 v[196:199], v[168:171], v[28:31], v[196:199]\n\t" \
    "ds_read_b128 v[20:23], %[xp] offset:0\n\t" \
    "v_add_f32 v10, v10, v62\n\t" \
    "v_mfma_f32_16x16x32_bf16 v[200:203], v[176:179], v[28:31], v[200:203]\n\t" \
    "ds_read_b128 v[24:27], %[xp] offset:1024\n\t" \
    "v_add_f32 v11, v11, v63\n\t" \
    "v_mfma_f32_16x16x32_bf16 v[204:207], v[168:171], v[32:35], v[204:207]\n\t" \
    "s_waitcnt lgkmcnt(4)\n\t" \
    "v_add_f32 v12, v12, v64\n\t" \
    "v_mfma_f32_16x16x32_bf16 v[208:211], v[176:179], v[32:35], v[208:211]\n\t" \
    "v_add_f32 v13, v13, v65\n\t" \
    "v_add_f32 v14, v14, v66\n\t" \
    "v_add_f32 v15, v15, v67\n\t" \
    "v_mfma_f32_16x16x32_bf16 v[196:199], v[180:183], v[36:39], v[196:199]\n\t" \
    "ds_read_b128 v[28:31], %[xp] offset:2048\n\t" \
    "v_cvt_pk_bf16_f32 v52, v0, v4\n\t" \
    "v_mfma_f32_16x16x32_bf16 v[200:203], v[188:191], v[36:39], v[200:203]\n\t" \
    "ds_read_b128 v[32:35], %[xp] offset:3072\n\t" \
    "v_cvt_pk_bf16_f32 v53, v1, v5\n\t" \
    "v_mfma_f32_16x16x32_bf16 v[204:207], v[180:183], v[40:43], v[204:207]\n\t" \
    "v_cvt_pk_bf16_f32 v54, v2, v6\n\t" \
    "s_waitcnt lgkmcnt(4)\n\t" \
    "v_mfma_f32_16x16x32_bf16 v[208:211], v[188:191], v[40:43], v[208:211]\n\t" \
    "v_cvt_pk_bf16_f32 v55, v3, v7\n\t" \
    "v_cvt_pk_bf16_f32 v56, v8, v12\n\t" \
    "v_cvt_pk_bf16_f32 v57, v9, v13\n\t" \
    "v_mfma_f32_16x16x32_bf16 v[196:199], v[184:187], v[44:47], v[196:199]\n\t" \
    "ds_read_b128 v[36:39], %[xp] offset:4096\n\t" \
    "v_cvt_pk_bf16_f32 v58, v10, v14\n\t" \
    "v_mfma_f32_16x16x32_bf16 v[200:203], v[192:195], v[44:47], v[200:203]\n\t" \
    "ds_read_b128 v[40:43], %[xp] offset:5120\n\t" \
    "v_cvt_pk_bf16_f32 v59, v11, v15\n\t" \
    "v_mfma_f32_16x16x32_bf16 v[204:207], v[184:187], v[48:51], v[204:207]\n\t" \
    "s_waitcnt lgkmcnt(4)\n\t" \
    "v_mfma_f32_16x16x32_bf16 v[208:211], v[192:195], v[48:51], v[208:211]\n\t" \
    "s_mov_b64 exec, %[em]\n\t" \
    "buffer_store_dwordx4 v[52:55], %[l16], %[rs], %[oo] offen sc1\n\t" \
    "buffer_store_dwordx4 v[56:59], %[l16], %[rs], %[b] offen sc1\n\t" \
    "s_mov_b64 exec, -1\n\t" \
    "s_add_u32 %[sx], %[sx], 65536\n\t" \
    "s_add_u32 %[t], %[t], 1\n\t" \
    "s_add_u32 %[oo], %[oo], 262144\n\t" \
    "s_waitcnt vmcnt(6)\n\t" \
    "s_cmp_gt_u32 %[t], " STR(SQUICK) " + 1\n\t" \
    "s_cbranch_scc1 B1_%=\n\t" \
    "s_waitcnt vmcnt(0)\n\t" \
    "B1_%=:\n\t" \
    "s_barrier\n\t" \
    "s_cmp_lt_u32 %[t], %[te]\n\t" \
    "s_cbranch_scc1 L1_%=\n\t" \
    "s_cmp_eq_u32 %[fin], %[te]\n\t" \
    "s_cbranch_scc0 E_%=\n\t" \
    "s_nop 4\n\t" \
    "s_barrier\n\t" \
    "ds_read_b128 v[52:55], %[pa] offset:0\n\t" \
    "ds_read_b128 v[56:59], %[pa] offset:1024\n\t" \
    "ds_read_b128 v[60:63], %[pa] offset:2048\n\t" \
    "ds_read_b128 v[64:67], %[pa] offset:3072\n\t" \
    "s_add_u32 %[b], %[oo], 0x1000\n\t" \
    "s_waitcnt lgkmcnt(0)\n\t" \
    "v_add_f32 v196, v196, v52\n\t" \
    "v_add_f32 v197, v197, v53\n\t" \
    "v_add_f32 v198, v198, v54\n\t" \
    "v_add_f32 v199, v199, v55\n\t" \
    "v_add_f32 v200, v200, v56\n\t" \
    "v_add_f32 v201, v201, v57\n\t" \
    "v_add_f32 v202, v202, v58\n\t" \
    "v_add_f32 v203, v203, v59\n\t" \
    "v_add_f32 v204, v204, v60\n\t" \
    "v_add_f32 v205, v205, v61\n\t" \
    "v_add_f32 v206, v206, v62\n\t" \
    "v_add_f32 v207, v207, v63\n\t" \
    "v_add_f32 v208, v208, v64\n\t" \
    "v_add_f32 v209, v209, v65\n\t" \
    "v_add_f32 v210, v210, v66\n\t" \
    "v_add_f32 v211, v211, v67\n\t" \
    "v_cvt_pk_bf16_f32 v52, v196, v200\n\t" \
    "v_cvt_pk_bf16_f32 v53, v197, v201\n\t" \
    "v_cvt_pk_bf16_f32 v54, v198, v202\n\t" \
    "v_cvt_pk_bf16_f32 v55, v199, v203\n\t" \
    "v_cvt_pk_bf16_f32 v56, v204, v208\n\t" \
    "v_cvt_pk_bf16_f32 v57, v205, v209\n\t" \
    "v_cvt_pk_bf16_f32 v58, v206, v210\n\t" \
    "v_cvt_pk_bf16_f32 v59, v207, v211\n\t" \
    "buffer_store_dwordx4 v[52:55], %[l16], %[rs], %[oo] offen sc1\n\t" \
    "buffer_store_dwordx4 v[56:59], %[l16], %[rs], %[b] offen sc1\n\t" \
    "E_%=:\n\t" \
    "s_setprio 0\n\t" \
    "s_waitcnt lgkmcnt(0)\n\t" \
    "s_nop 7\n\t" \
    : [t] "+s"(t), [sx] "+s"(sx), [oo] "+s"(oo), [a] "=&s"(tmp), [b] "=&s"(tmp2), [em] "=&s"(em), "+{v[20:27]}"(xf[0]), "+{v[28:35]}"(xf[1]), "+{v[36:43]}"(xf[2]), "+{v[196:203]}"(ac[0]), "+{v[204:211]}"(ac[1]) \
    : [te] "s"(TE_), [fin] "s"(FIN_), [kh] "s"(kh), [sd] "s"(sd), [rs] "s"(rs), [l16] "v"(l16), [xp] "v"(xp), [pa] "v"(pa), [fm] "s"(fm), [pm] "s"(pm), [fo] "s"(fo), [po] "s"(po), [pd] "s"(pd), [tag] "s"(P.tag), "{v[68:75]}"(w[0][0]), "{v[76:83]}"(w[0][1]), "{v[84:91]}"(w[1][0]), "{v[92:99]}"(w[1][1]), "{v[100:107]}"(w[2][0]), "{v[108:115]}"(w[2][1]), "{v[116:123]}"(w[3][0]), "{v[124:131]}"(w[3][1]), "{v[132:139]}"(w[4][0]), "{v[140:147]}"(w[4][1]), "{v[148:155]}"(w[5][0]), "{v[156:163]}"(w[5][1]), "{v[164:171]}"(w[6][0]), "{v[172:179]}"(w[6][1]), "{v[180:187]}"(w[7][0]), "{v[188:195]}"(w[7][1]) \
    : "memory", "scc", "v0", "v1", "v2", "v3", "v4", "v5", "v6", "v7", "v8", "v9", "v10", "v11", "v12", "v13", "v14", "v15", "v44", "v45", "v46", "v47", "v48", "v49", "v50", "v51", "v52", "v53", "v54", "v55", "v56", "v57", "v58", "v59", "v60", "v61", "v62", "v63", "v64", "v65", "v66", "v67")

#define S_LOOP_V(TE_, FIN_) \
  asm volatile( \
    "s_cmp_eq_u32 %[kh], 0\n\t" \
    "s_cbranch_scc0 L1_%=\n\t" \
    "L0_%=:\n\t" \
    "s_setprio 0\n\t" \
    "s_nop 3\n\t" \
    "ds_write_b128 %[pa], v[196:199] offset:0\n\t" \
    "ds_write_b128 %[pa], v[200:203] offset:1024\n\t" \
    "ds_write_b128 %[pa], v[204:207] offset:2048\n\t" \
    "ds_write_b128 %[pa], v[208:211] offset:3072\n\t" \
    "s_sub_u32 %[a], %[t], 1\n\t" \
    "v_mov_b32 v53, %[tag]\n\t" \
    "v_mov_b32 v52, %[a]\n\t" \
    "s_sub_u32 %[b], %[t], 2\n\t" \
    "s_cmp_lt_u32 %[b], " STR(SQUICK) "\n\t" \
    "s_cselect_b64 exec, %[fm], 0\n\t" \
    "buffer_store_dwordx2 v[52:53], %[l16], %[rs], %[fo] offen sc1\n\t" \
    "s_mov_b64 exec, -1\n\t" \
    "s_add_u32 m0, %[sd], 49152\n\t" \
    "s_add_u32 %[a], %[sx], 81920\n\t" \
    "s_waitcnt lgkmcnt(0)\n\t" \
    "buffer_load_dwordx4 %[l16], %[rs], %[a] offen offset:0 sc1 lds\n\t" \
    "buffer_load_dwordx4 %[l16], %[rs], %[a] offen offset:1024 sc1 lds\n\t" \
    "buffer_load_dwordx4 %[l16], %[rs], %[a] offen offset:2048 sc1 lds\n\t" \
    "buffer_load_dwordx4 %[l16], %[rs], %[a] offen offset:3072 sc1 lds\n\t" \
    "v_mfma_f32_16x16x32_bf16 v[0:3], v[20:23], v[68:71], 0\n\t" \
    "ds_read_b128 v[44:47], %[xp] offset:6144\n\t" \
    "v_mfma_f32_16x16x32_bf16 v[4:7], v[20:23], v[76:79], 0\n\t" \
    "ds_read_b128 v[48:51], %[xp] offset:7168\n\t" \
    "v_mfma_f32_16x16x32_bf16 v[8:11], v[24:27], v[68:71], 0\n\t" \
    "v_mfma_f32_16x16x32_bf16 v[12:15], v[24:27], v[76:79], 0\n\t" \
    "v_mfma_f32_16x16x32_bf16 v[0:3], v[28:31], v[72:75], v[0:3]\n\t" \
    "ds_read_b128 v[20:23], %[xp] offset:8192\n\t" \
    "v_mfma_f32_16x16x32_bf16 v[4:7], v[28:31], v[80:83], v[4:7]\n\t" \
    "ds_read_b128 v[24:27], %[xp] offset:9216\n\t" \
    "v_mfma_f32_16x16x32_bf16 v[8:11], v[32:35], v[72:75], v[8:11]\n\t" \
    "v_mfma_f32_16x16x32_bf16 v[12:15], v[32:35], v[80:83], v[12:15]\n\t" \
    "v_mfma_f32_16x16x32_bf16 v[0:3], v[36:39], v[84:87], v[0:3]\n\t" \
    "ds_read_b128 v[28:31], %[xp] offset:10240\n\t" \
    "v_mfma_f32_16x16x32_bf16 v[4:7], v[36:39], v[92:95], v[4:7]\n\t" \
    "ds_read_b128 v[32:35], %[xp] offset:11264\n\t" \
    "v_mfma_f32_16x16x32_bf16 v[8:11], v[40:43], v[84:87], v[8:11]\n\t" \
    "s_waitcnt lgkmcnt(4)\n\t" \
    "v_mfma_f32_16x16x32_bf16 v[12:15], v[40:43], v[92:95], v[12:15]\n\t" \
    "v_mfma_f32_16x16x32_bf16 v[0:3], v[44:47], v[88:91], v[0:3]\n\t" \
    "ds_read_b128 v[36:39], %[xp] offset:12288\n\t" \
    "v_mfma_f32_16x16x32_bf16 v[4:7], v[44:47], v[96:99], v[4:7]\n\t" \
    "ds_read_b128 v[40:43], %[xp] offset:13312\n\t" \
    "v_mfma_f32_16x16x32_bf16 v[8:11], v[48:51], v[88:91], v[8:11]\n\t" \
    "s_waitcnt lgkmcnt(4)\n\t" \
    "v_mfma_f32_16x16x32_bf16 v[12:15], v[48:51], v[96:99], v[12:15]\n\t" \
    "v_mfma_f32_16x16x32_bf16 v[0:3], v[20:23], v[100:103], v[0:3]\n\t" \
    "ds_read_b128 v[44:47], %[xp] offset:14336\n\t" \
    "v_mfma_f32_16x16x32_bf16 v[4:7], v[20:23], v[108:111], v[4:7]\n\t" \
    "ds_read_b128 v[48:51], %[xp] offset:15360\n\t" \
    "v_mfma_f32_16x16x32_bf16 v[8:11], v[24:27], v[100:103], v[8:11]\n\t" \
    "s_waitcnt lgkmcnt(4)\n\t" \
    "v_mfma_f32_16x16x32_bf16 v[12:15], v[24:27], v[108:111], v[12:15]\n\t" \
    "v_mfma_f32_16x16x32_bf16 v[0:3], v[28:31], v[104:107], v[0:3]\n\t" \
    "ds_read_b128 v[20:23], %[xp] offset:16384\n\t" \
    "v_mfma_f32_16x16x32_bf16 v[4:7], v[28:31], v[112:115], v[4:7]\n\t" \
    "ds_read_b128 v[24:27], %[xp] offset:17408\n\t" \
    "v_mfma_f32_16x16x32_bf16 v[8:11], v[32:35], v[104:107], v[8:11]\n\t" \
    "s_waitcnt lgkmcnt(4)\n\t" \
    "v_mfma_f32_16x16x32_bf16 v[12:15], v[32:35], v[112:115], v[12:15]\n\t" \
    "v_mfma_f32_16x16x32_bf16 v[0:3], v[36:39], v[116:119], v[0:3]\n\t" \
    "ds_read_b128 v[28:31], %[xp] offset:18432\n\t" \
    "v_mfma_f32_16x16x32_bf16 v[4:7], v[36:39], v[124:127], v[4:7]\n\t" \
    "ds_read_b128 v[32:35], %[xp] offset:19456\n\t" \
    "v_mfma_f32_16x16x32_bf16 v[8:11], v[40:43], v[116:119], v[8:11]\n\t" \
    "s_waitcnt lgkmcnt(4)\n\t" \
    "v_mfma_f32_16x16x32_bf16 v[12:15], v[40:43], v[124:127], v[12:15]\n\t" \
    "v_mfma_f32_16x16x32_bf16 v[0:3], v[44:47], v[120:123], v[0:3]\n\t" \
    "ds_read_b128 v[36:39], %[xp] offset:20480\n\t" \
    "v_mfma_f32_16x16x32_bf16 v[4:7], v[44:47], v[128:131], v[4:7]\n\t" \
    "ds_read_b128 v[40:43], %[xp] offset:21504\n\t" \
    "v_mfma_f32_16x16x32_bf16 v[8:11], v[48:51], v[120:123], v[8:11]\n\t" \
    "s_waitcnt lgkmcnt(4)\n\t" \
    "v_mfma_f32_16x16x32_bf16 v[12:15], v[48:51], v[128:131], v[12:15]\n\t" \
    "s_waitcnt vmcnt(4)\n\t" \
    "s_barrier\n\t" \
    "s_setprio 0\n\t" \
    "s_sub_u32 %[a], %[t], 1\n\t" \
    "v_mov_b32 v53, %[tag]\n\t" \
    "v_mov_b32 v52, %[a]\n\t" \
    "s_cmp_gt_u32 %[t], 1\n\t" \
    "s_cselect_b64 exec, %[fm], 0\n\t" \
    "buffer_store_dwordx2 v[52:53], %[l16], %[rs], %[fo] offen sc1\n\t" \
    "s_mov_b64 exec, %[pm]\n\t" \
    "s_mov_b32 m0, %[pd]\n\t" \
    "s_add_u32 %[a], %[sx], %[po]\n\t" \
    "buffer_load_dwordx4 %[l16], %[rs], %[a] offen sc1 lds\n\t" \
    "s_mov_b64 exec, -1\n\t" \
    "s_add_u32 m0, %[sd], 0\n\t" \
    "s_add_u32 %[a], %[sx], 131072\n\t" \
    "buffer_load_dwordx4 %[l16], %[rs], %[a] offen offset:0 sc1 lds\n\t" \
    "buffer_load_dwordx4 %[l16], %[rs], %[a] offen offset:1024 sc1 lds\n\t" \
    "buffer_load_dwordx4 %[l16], %[rs], %[a] offen offset:2048 sc1 lds\n\t" \
    "buffer_load_dwordx4 %[l16], %[rs], %[a] offen offset:3072 sc1 lds\n\t" \
    "v_mfma_f32_16x16x32_bf16 v[0:3], v[20:23], v[132:135], v[0:3]\n\t" \
    "ds_read_b128 v[44:47], %[xp] offset:22528\n\t" \
    "v_mfma_f32_16x16x32_bf16 v[4:7], v[20:23], v[140:143], v[4:7]\n\t" \
    "ds_read_b128 v[48:51], %[xp] offset:23552\n\t" \
    "v_mfma_f32_16x16x32_bf16 v[8:11], v[24:27], v[132:135], v[8:11]\n\t" \
    "s_waitcnt lgkmcnt(4)\n\t" \
    "v_mfma_f32_16x16x32_bf16 v[12:15], v[24:27], v[140:143], v[12:15]\n\t" \
    "v_mfma_f32_16x16x32_bf16 v[0:3], v[28:31], v[136:139], v[0:3]\n\t" \
    "ds_read_b128 v[20:23], %[xp] offset:24576\n\t" \
    "v_mfma_f32_16x16x32_bf16 v[4:7], v[28:31], v[144:147], v[4:7]\n\t" \
    "ds_read_b128 v[24:27], %[xp] offset:25600\n\t" \
    "v_mfma_f32_16x16x32_bf16 v[8:11], v[32:35], v[136:139], v[8:11]\n\t" \
    "s_waitcnt lgkmcnt(4)\n\t" \
    "v_mfma_f32_16x16x32_bf16 v[12:15], v[32:35], v[144:147], v[12:15]\n\t" \
    "v_mfma_f32_16x16x32_bf16 v[0:3], v[36:39], v[148:151], v[0:3]\n\t" \
    "ds_read_b128 v[28:31], %[xp] offset:26624\n\t" \
    "v_mfma_f32_16x16x32_bf16 v[4:7], v[36:39], v[156:159], v[4:7]\n\t" \
    "ds_read_b128 v[32:35], %[xp] offset:27648\n\t" \
    "v_mfma_f32_16x16x32_bf16 v[8:11], v[40:43], v[148:151], v[8:11]\n\t" \
    "s_waitcnt lgkmcnt(4)\n\t" \
    "v_mfma_f32_16x16x32_bf16 v[12:15], v[40:43], v[156:159], v[12:15]\n\t" \
    "v_mfma_f32_16x16x32_bf16 v[0:3], v[44:47], v[152:155], v[0:3]\n\t" \
    "ds_read_b128 v[36:39], %[xp] offset:28672\n\t" \
    "v_mfma_f32_16x16x32_bf16 v[4:7], v[44:47], v[160:163], v[4:7]\n\t" \
    "ds_read_b128 v[40:43], %[xp] offset:29696\n\t" \
    "v_mfma_f32_16x16x32_bf16 v[8:11], v[48:51], v[152:155], v[8:11]\n\t" \
    "s_waitcnt lgkmcnt(4)\n\t" \
    "v_mfma_f32_16x16x32_bf16 v[12:15], v[48:51], v[160:163], v[12:15]\n\t" \
    "v_mfma_f32_16x16x32_bf16 v[0:3], v[20:23], v[164:167], v[0:3]\n\t" \
    "ds_read_b128 v[44:47], %[xp] offset:30720\n\t" \
    "v_mfma_f32_16x16x32_bf16 v[4:7], v[20:23], v[172:175], v[4:7]\n\t" \
    "ds_read_b128 v[48:51], %[xp] offset:31744\n\t" \
    "v_mfma_f32_16x16x32_bf16 v[8:11], v[24:27], v[164:167], v[8:11]\n\t" \
    "s_waitcnt lgkmcnt(4)\n\t" \
    "v_mfma_f32_16x16x32_bf16 v[12:15], v[24:27], v[172:175], v[12:15]\n\t" \
    "v_mfma_f32_16x16x32_bf16 v[0:3], v[28:31], v[168:171], v[0:3]\n\t" \
    "ds_read_b128 v[20:23], %[xp] offset:32768\n\t" \
    "v_mfma_f32_16x16x32_bf16 v[4:7], v[28:31], v[176:179], v[4:7]\n\t" \
    "ds_read_b128 v[24:27], %[xp] offset:33792\n\t" \
    "v_mfma_f32_16x16x32_bf16 v[8:11], v[32:35], v[168:171], v[8:11]\n\t" \
    "s_waitcnt lgkmcnt(4)\n\t" \
    "v_mfma_f32_16x16x32_bf16 v[12:15], v[32:35], v[176:179], v[12:15]\n\t" \
    "v_mfma_f32_16x16x32_bf16 v[0:3], v[36:39], v[180:183], v[0:3]\n\t" \
    "ds_read_b128 v[28:31], %[xp] offset:34816\n\t" \
    "v_mfma_f32_16x16x32_bf16 v[4:7], v[36:39], v[188:191], v[4:7]\n\t" \
    "ds_read_b128 v[32:35], %[xp] offset:35840\n\t" \
    "v_mfma_f32_16x16x32_bf16 v[8:11], v[40:43], v[180:183], v[8:11]\n\t" \
    "s_waitcnt lgkmcnt(4)\n\t" \
    "v_mfma_f32_16x16x32_bf16 v[12:15], v[40:43], v[188:191], v[12:15]\n\t" \
    "v_mfma_f32_16x16x32_bf16 v[0:3], v[44:47], v[184:187], v[0:3]\n\t" \
    "ds_read_b128 v[36:39], %[xp] offset:36864\n\t" \
    "v_mfma_f32_16x16x32_bf16 v[4:7], v[44:47], v[192:195], v[4:7]\n\t" \
    "ds_read_b128 v[40:43], %[xp] offset:37888\n\t" \
    "v_mfma_f32_16x16x32_bf16 v[8:11], v[48:51], v[184:187], v[8:11]\n\t" \
    "s_waitcnt lgkmcnt(4)\n\t" \
    "v_mfma_f32_16x16x32_bf16 v[12:15], v[48:51], v[192:195], v[12:15]\n\t" \
    "s_add_u32 %[sx], %[sx], 65536\n\t" \
    "s_add_u32 %[t], %[t], 1\n\t" \
    "s_waitcnt vmcnt(4)\n\t" \
    "s_barrier\n\t" \
    "s_setprio 0\n\t" \
    "s_nop 3\n\t" \
    "ds_write_b128 %[pa], v[0:3] offset:0\n\t" \
    "ds_write_b128 %[pa], v[4:7] offset:1024\n\t" \
    "ds_write_b128 %[pa], v[8:11] offset:2048\n\t" \
    "ds_write_b128 %[pa], v[12:15] offset:3072\n\t" \
    "s_sub_u32 %[a], %[t], 1\n\t" \
    "v_mov_b32 v53, %[tag]\n\t" \
    "v_mov_b32 v52, %[a]\n\t" \
    "s_sub_u32 %[b], %[t], 2\n\t" \
    "s_cmp_lt_u32 %[b], " STR(SQUICK) "\n\t" \
    "s_cselect_b64 exec, %[fm], 0\n\t" \
    "buffer_store_dwordx2 v[52:53], %[l16], %[rs], %[fo] offen sc1\n\t" \
    "s_mov_b64 exec, -1\n\t" \
    "s_add_u32 m0, %[sd], 16384\n\t" \
    "s_add_u32 %[a], %[sx], 81920\n\t" \
    "s_waitcnt lgkmcnt(0)\n\t" \
    "buffer_load_dwordx4 %[l16], %[rs], %[a] offen offset:0 sc1 lds\n\t" \
    "buffer_load_dwordx4 %[l16], %[rs], %[a] offen offset:1024 sc1 lds\n\t" \
    "buffer_load_dwordx4 %[l16], %[rs], %[a] offen offset:2048 sc1 lds\n\t" \
    "buffer_load_dwordx4 %[l16], %[rs], %[a] offen offset:3072 sc1 lds\n\t" \
    "v_mfma_f32_16x16x32_bf16 v[196:199], v[20:23], v[68:71], 0\n\t" \
    "ds_read_b128 v[44:47], %[xp] offset:38912\n\t" \
    "v_mfma_f32_16x16x32_bf16 v[200:203], v[20:23], v[76:79], 0\n\t" \
    "ds_read_b128 v[48:51], %[xp] offset:39936\n\t" \
    "v_mfma_f32_16x16x32_bf16 v[204:207], v[24:27], v[68:71], 0\n\t" \
    "v_mfma_f32_16x16x32_bf16 v[208:211], v[24:27], v[76:79], 0\n\t" \
    "v_mfma_f32_16x16x32_bf16 v[196:199], v[28:31], v[72:75], v[196:199]\n\t" \
    "ds_read_b128 v[20:23], %[xp] offset:40960\n\t" \
    "v_mfma_f32_16x16x32_bf16 v[200:203], v[28:31], v[80:83], v[200:203]\n\t" \
    "ds_read_b128 v[24:27], %[xp] offset:41984\n\t" \
    "v_mfma_f32_16x16x32_bf16 v[204:207], v[32:35], v[72:75], v[204:207]\n\t" \
    "v_mfma_f32_16x16x32_bf16 v[208:211], v[32:35], v[80:83], v[208:211]\n\t" \
    "v_mfma_f32_16x16x32_bf16 v[196:199], v[36:39], v[84:87], v[196:199]\n\t" \
    "ds_read_b128 v[28:31], %[xp] offset:43008\n\t" \
    "v_mfma_f32_16x16x32_bf16 v[200:203], v[36:39], v[92:95], v[200:203]\n\t" \
    "ds_read_b128 v[32:35], %[xp] offset:44032\n\t" \
    "v_mfma_f32_16x16x32_bf16 v[204:207], v[40:43], v[84:87], v[204:207]\n\t" \
    "s_waitcnt lgkmcnt(4)\n\t" \
    "v_mfma_f32_16x16x32_bf16 v[208:211], v[40:43], v[92:95], v[208:211]\n\t" \
    "v_mfma_f32_16x16x32_bf16 v[196:199], v[44:47], v[88:91], v[196:199]\n\t" \
    "ds_read_b128 v[36:39], %[xp] offset:45056\n\t" \
    "v_mfma_f32_16x16x32_bf16 v[200:203], v[44:47], v[96:99], v[200:203]\n\t" \
    "ds_read_b128 v[40:43], %[xp] offset:46080\n\t" \
    "v_mfma_f32_16x16x32_bf16 v[204:207], v[48:51], v[88:91], v[204:207]\n\t" \
    "s_waitcnt lgkmcnt(4)\n\t" \
    "v_mfma_f32_16x16x32_bf16 v[208:211], v[48:51], v[96:99], v[208:211]\n\t" \
    "v_mfma_f32_16x16x32_bf16 v[196:199], v[20:23], v[100:103], v[196:199]\n\t" \
    "ds_read_b128 v[44:47], %[xp] offset:47104\n\t" \
    "v_mfma_f32_16x16x32_bf16 v[200:203], v[20:23], v[108:111], v[200:203]\n\t" \
    "ds_read_b128 v[48:51], %[xp] offset:48128\n\t" \
    "v_mfma_f32_16x16x32_bf16 v[204:207], v[24:27], v[100:103], v[204:207]\n\t" \
    "s_waitcnt lgkmcnt(4)\n\t" \
    "v_mfma_f32_16x16x32_bf16 v[208:211], v[24:27], v[108:111], v[208:211]\n\t" \
    "v_mfma_f32_16x16x32_bf16 v[196:199], v[28:31], v[104:107], v[196:199]\n\t" \
    "ds_read_b128 v[20:23], %[xp] offset:49152\n\t" \
    "v_mfma_f32_16x16x32_bf16 v[200:203], v[28:31], v[112:115], v[200:203]\n\t" \
    "ds_read_b128 v[24:27], %[xp] offset:50176\n\t" \
    "v_mfma_f32_16x16x32_bf16 v[204:207], v[32:35], v[104:107], v[204:207]\n\t" \
    "s_waitcnt lgkmcnt(4)\n\t" \
    "v_mfma_f32_16x16x32_bf16 v[208:211], v[32:35], v[112:115], v[208:211]\n\t" \
    "v_mfma_f32_16x16x32_bf16 v[196:199], v[36:39], v[116:119], v[196:199]\n\t" \
    "ds_read_b128 v[28:31], %[xp] offset:51200\n\t" \
    "v_mfma_f32_16x16x32_bf16 v[200:203], v[36:39], v[124:127], v[200:203]\n\t" \
    "ds_read_b128 v[32:35], %[xp] offset:52224\n\t" \
    "v_mfma_f32_16x16x32_bf16 v[204:207], v[40:43], v[116:119], v[204:207]\n\t" \
    "s_waitcnt lgkmcnt(4)\n\t" \
    "v_mfma_f32_16x16x32_bf16 v[208:211], v[40:43], v[124:127], v[208:211]\n\t" \
    "v_mfma_f32_16x16x32_bf16 v[196:199], v[44:47], v[120:123], v[196:199]\n\t" \
    "ds_read_b128 v[36:39], %[xp] offset:53248\n\t" \
    "v_mfma_f32_16x16x32_bf16 v[200:203], v[44:47], v[128:131], v[200:203]\n\t" \
    "ds_read_b128 v[40:43], %[xp] offset:54272\n\t" \
    "v_mfma_f32_16x16x32_bf16 v[204:207], v[48:51], v[120:123], v[204:207]\n\t" \
    "s_waitcnt lgkmcnt(4)\n\t" \
    "v_mfma_f32_16x16x32_bf16 v[208:211], v[48:51], v[128:131], v[208:211]\n\t" \
    "s_waitcnt vmcnt(4)\n\t" \
    "s_barrier\n\t" \
    "s_setprio 0\n\t" \
    "s_sub_u32 %[a], %[t], 1\n\t" \
    "v_mov_b32 v53, %[tag]\n\t" \
    "v_mov_b32 v52, %[a]\n\t" \
    "s_cmp_gt_u32 %[t], 1\n\t" \
    "s_cselect_b64 exec, %[fm], 0\n\t" \
    "buffer_store_dwordx2 v[52:53], %[l16], %[rs], %[fo] offen sc1\n\t" \
    "s_mov_b64 exec, %[pm]\n\t" \
    "s_mov_b32 m0, %[pd]\n\t" \
    "s_add_u32 %[a], %[sx], %[po]\n\t" \
    "buffer_load_dwordx4 %[l16], %[rs], %[a] offen sc1 lds\n\t" \
    "s_mov_b64 exec, -1\n\t" \
    "s_add_u32 m0, %[sd], 32768\n\t" \
    "s_add_u32 %[a], %[sx], 131072\n\t" \
    "buffer_load_dwordx4 %[l16], %[rs], %[a] offen offset:0 sc1 lds\n\t" \
    "buffer_load_dwordx4 %[l16], %[rs], %[a] offen offset:1024 sc1 lds\n\t" \
    "buffer_load_dwordx4 %[l16], %[rs], %[a] offen offset:2048 sc1 lds\n\t" \
    "buffer_load_dwordx4 %[l16], %[rs], %[a] offen offset:3072 sc1 lds\n\t" \
    "v_mfma_f32_16x16x32_bf16 v[196:199], v[20:23], v[132:135], v[196:199]\n\t" \
    "ds_read_b128 v[44:47], %[xp] offset:55296\n\t" \
    "v_mfma_f32_16x16x32_bf16 v[200:203], v[20:23], v[140:143], v[200:203]\n\t" \
    "ds_read_b128 v[48:51], %[xp] offset:56320\n\t" \
    "v_mfma_f32_16x16x32_bf16 v[204:207], v[24:27], v[132:135], v[204:207]\n\t" \
    "s_waitcnt lgkmcnt(4)\n\t" \
    "v_mfma_f32_16x16x32_bf16 v[208:211], v[24:27], v[140:143], v[208:211]\n\t" \
    "v_mfma_f32_16x16x32_bf16 v[196:199], v[28:31], v[136:139], v[196:199]\n\t" \
    "ds_read_b128 v[20:23], %[xp] offset:57344\n\t" \
    "v_mfma_f32_16x16x32_bf16 v[200:203], v[28:31], v[144:147], v[200:203]\n\t" \
    "ds_read_b128 v[24:27], %[xp] offset:58368\n\t" \
    "v_mfma_f32_16x16x32_bf16 v[204:207], v[32:35], v[136:139], v[204:207]\n\t" \
    "s_waitcnt lgkmcnt(4)\n\t" \
    "v_mfma_f32_16x16x32_bf16 v[208:211], v[32:35], v[144:147], v[208:211]\n\t" \
    "v_mfma_f32_16x16x32_bf16 v[196:199], v[36:39], v[148:151], v[196:199]\n\t" \
    "ds_read_b128 v[28:31], %[xp] offset:59392\n\t" \
    "v_mfma_f32_16x16x32_bf16 v[200:203], v[36:39], v[156:159], v[200:203]\n\t" \
    "ds_read_b128 v[32:35], %[xp] offset:60416\n\t" \
    "v_mfma_f32_16x16x32_bf16 v[204:207], v[40:43], v[148:151], v[204:207]\n\t" \
    "s_waitcnt lgkmcnt(4)\n\t" \
    "v_mfma_f32_16x16x32_bf16 v[208:211], v[40:43], v[156:159], v[208:211]\n\t" \
    "v_mfma_f32_16x16x32_bf16 v[196:199], v[44:47], v[152:155], v[196:199]\n\t" \
    "ds_read_b128 v[36:39], %[xp] offset:61440\n\t" \
    "v_mfma_f32_16x16x32_bf16 v[200:203], v[44:47], v[160:163], v[200:203]\n\t" \
    "ds_read_b128 v[40:43], %[xp] offset:62464\n\t" \
    "v_mfma_f32_16x16x32_bf16 v[204:207], v[48:51], v[152:155], v[204:207]\n\t" \
    "s_waitcnt lgkmcnt(4)\n\t" \
    "v_mfma_f32_16x16x32_bf16 v[208:211], v[48:51], v[160:163], v[208:211]\n\t" \
    "v_mfma_f32_16x16x32_bf16 v[196:199], v[20:23], v[164:167], v[196:199]\n\t" \
    "ds_read_b128 v[44:47], %[xp] offset:63488\n\t" \
    "v_mfma_f32_16x16x32_bf16 v[200:203], v[20:23], v[172:175], v[200:203]\n\t" \
    "ds_read_b128 v[48:51], %[xp] offset:64512\n\t" \
    "v_mfma_f32_16x16x32_bf16 v[204:207], v[24:27], v[164:167], v[204:207]\n\t" \
    "s_waitcnt lgkmcnt(4)\n\t" \
    "v_mfma_f32_16x16x32_bf16 v[208:211], v[24:27], v[172:175], v[208:211]\n\t" \
    "v_mfma_f32_16x16x32_bf16 v[196:199], v[28:31], v[168:171], v[196:199]\n\t" \
    "ds_read_b128 v[20:23], %[xp] offset:0\n\t" \
    "v_mfma_f32_16x16x32_bf16 v[200:203], v[28:31], v[176:179], v[200:203]\n\t" \
    "ds_read_b128 v[24:27], %[xp] offset:1024\n\t" \
    "v_mfma_f32_16x16x32_bf16 v[204:207], v[32:35], v[168:171], v[204:207]\n\t" \
    "s_waitcnt lgkmcnt(4)\n\t" \
    "v_mfma_f32_16x16x32_bf16 v[208:211], v[32:35], v[176:179], v[208:211]\n\t" \
    "v_mfma_f32_16x16x32_bf16 v[196:199], v[36:39], v[180:183], v[196:199]\n\t" \
    "ds_read_b128 v[28:31], %[xp] offset:2048\n\t" \
    "v_mfma_f32_16x16x32_bf16 v[200:203], v[36:39], v[188:191], v[200:203]\n\t" \
    "ds_read_b128 v[32:35], %[xp] offset:3072\n\t" \
    "v_mfma_f32_16x16x32_bf16 v[204:207], v[40:43], v[180:183], v[204:207]\n\t" \
    "s_waitcnt lgkmcnt(4)\n\t" \
    "v_mfma_f32_16x16x32_bf16 v[208:211], v[40:43], v[188:191], v[208:211]\n\t" \
    "v_mfma_f32_16x16x32_bf16 v[196:199], v[44:47], v[184:187], v[196:199]\n\t" \
    "ds_read_b128 v[36:39], %[xp] offset:4096\n\t" \
    "v_mfma_f32_16x16x32_bf16 v[200:203], v[44:47], v[192:195], v[200:203]\n\t" \
    "ds_read_b128 v[40:43], %[xp] offset:5120\n\t" \
    "v_mfma_f32_16x16x32_bf16 v[204:207], v[48:51], v[184:187], v[204:207]\n\t" \
    "s_waitcnt lgkmcnt(4)\n\t" \
    "v_mfma_f32_16x16x32_bf16 v[208:211], v[48:51], v[192:195], v[208:211]\n\t" \
    "s_add_u32 %[sx], %[sx], 65536\n\t" \
    "s_add_u32 %[t], %[t], 1\n\t" \
    "s_waitcnt vmcnt(4)\n\t" \
    "s_barrier\n\t" \
    "s_cmp_lt_u32 %[t], %[te]\n\t" \
    "s_cbranch_scc1 L0_%=\n\t" \
    "s_cmp_eq_u32 %[fin], %[te]\n\t" \
    "s_cbranch_scc0 E_%=\n\t" \
    "s_nop 4\n\t" \
    "ds_write_b128 %[pa], v[196:199] offset:0\n\t" \
    "ds_write_b128 %[pa], v[200:203] offset:1024\n\t" \
    "ds_write_b128 %[pa], v[204:207] offset:2048\n\t" \
    "ds_write_b128 %[pa], v[208:211] offset:3072\n\t" \
    "s_waitcnt lgkmcnt(0)\n\t" \
    "s_barrier\n\t" \
    "s_branch E_%=\n\t" \
    "L1_%=:\n\t" \
    "s_setprio 1\n\t" \
    "s_add_u32 m0, %[sd], 49152\n\t" \
    "s_add_u32 %[a], %[sx], 81920\n\t" \
    "v_mfma_f32_16x16x32_bf16 v[0:3], v[20:23], v[68:71], 0\n\t" \
    "ds_read_b128 v[44:47], %[xp] offset:6144\n\t" \
    "v_mfma_f32_16x16x32_bf16 v[4:7], v[20:23], v[76:79], 0\n\t" \
    "ds_read_b128 v[48:51], %[xp] offset:7168\n\t" \
    "v_mfma_f32_16x16x32_bf16 v[8:11], v[24:27], v[68:71], 0\n\t" \
    "s_waitcnt lgkmcnt(4)\n\t" \
    "v_mfma_f32_16x16x32_bf16 v[12:15], v[24:27], v[76:79], 0\n\t" \
    "v_mfma_f32_16x16x32_bf16 v[0:3], v[28:31], v[72:75], v[0:3]\n\t" \
    "ds_read_b128 v[20:23], %[xp] offset:8192\n\t" \
    "v_mfma_f32_16x16x32_bf16 v[4:7], v[28:31], v[80:83], v[4:7]\n\t" \
    "ds_read_b128 v[24:27], %[xp] offset:9216\n\t" \
    "v_mfma_f32_16x16x32_bf16 v[8:11], v[32:35], v[72:75], v[8:11]\n\t" \
    "s_waitcnt lgkmcnt(4)\n\t" \
    "v_mfma_f32_16x16x32_bf16 v[12:15], v[32:35], v[80:83], v[12:15]\n\t" \
    "v_mfma_f32_16x16x32_bf16 v[0:3], v[36:39], v[84:87], v[0:3]\n\t" \
    "ds_read_b128 v[28:31], %[xp] offset:10240\n\t" \
    "v_mfma_f32_16x16x32_bf16 v[4:7], v[36:39], v[92:95], v[4:7]\n\t" \
    "ds_read_b128 v[32:35], %[xp] offset:11264\n\t" \
    "v_mfma_f32_16x16x32_bf16 v[8:11], v[40:43], v[84:87], v[8:11]\n\t" \
    "s_waitcnt lgkmcnt(4)\n\t" \
    "v_mfma_f32_16x16x32_bf16 v[12:15], v[40:43], v[92:95], v[12:15]\n\t" \
    "v_mfma_f32_16x16x32_bf16 v[0:3], v[44:47], v[88:91], v[0:3]\n\t" \
    "ds_read_b128 v[36:39], %[xp] offset:12288\n\t" \
    "v_mfma_f32_16x16x32_bf16 v[4:7], v[44:47], v[96:99], v[4:7]\n\t" \
    "ds_read_b128 v[40:43], %[xp] offset:13312\n\t" \
    "v_mfma_f32_16x16x32_bf16 v[8:11], v[48:51], v[88:91], v[8:11]\n\t" \
    "s_waitcnt lgkmcnt(4)\n\t" \
    "v_mfma_f32_16x16x32_bf16 v[12:15], v[48:51], v[96:99], v[12:15]\n\t" \
    "v_mfma_f32_16x16x32_bf16 v[0:3], v[20:23], v[100:103], v[0:3]\n\t" \
    "ds_read_b128 v[44:47], %[xp] offset:14336\n\t" \
    "v_mfma_f32_16x16x32_bf16 v[4:7], v[20:23], v[108:111], v[4:7]\n\t" \
    "ds_read_b128 v[48:51], %[xp] offset:15360\n\t" \
    "v_mfma_f32_16x16x32_bf16 v[8:11], v[24:27], v[100:103], v[8:11]\n\t" \
    "s_waitcnt lgkmcnt(4)\n\t" \
    "v_mfma_f32_16x16x32_bf16 v[12:15], v[24:27], v[108:111], v[12:15]\n\t" \
    "buffer_load_dwordx4 %[l16], %[rs], %[a] offen offset:0 sc1 lds\n\t" \
    "v_mfma_f32_16x16x32_bf16 v[0:3], v[28:31], v[104:107], v[0:3]\n\t" \
    "ds_read_b128 v[20:23], %[xp] offset:16384\n\t" \
    "v_mfma_f32_16x16x32_bf16 v[4:7], v[28:31], v[112:115], v[4:7]\n\t" \
    "ds_read_b128 v[24:27], %[xp] offset:17408\n\t" \
    "v_mfma_f32_16x16x32_bf16 v[8:11], v[32:35], v[104:107], v[8:11]\n\t" \
    "s_waitcnt lgkmcnt(4)\n\t" \
    "v_mfma_f32_16x16x32_bf16 v[12:15], v[32:35], v[112:115], v[12:15]\n\t" \
    "buffer_load_dwordx4 %[l16], %[rs], %[a] offen offset:1024 sc1 lds\n\t" \
    "v_mfma_f32_16x16x32_bf16 v[0:3], v[36:39], v[116:119], v[0:3]\n\t" \
    "ds_read_b128 v[28:31], %[xp] offset:18432\n\t" \
    "v_mfma_f32_16x16x32_bf16 v[4:7], v[36:39], v[124:127], v[4:7]\n\t" \
    "ds_read_b128 v[32:35], %[xp] offset:19456\n\t" \
    "v_mfma_f32_16x16x32_bf16 v[8:11], v[40:43], v[116:119], v[8:11]\n\t" \
    "s_waitcnt lgkmcnt(4)\n\t" \
    "v_mfma_f32_16x16x32_bf16 v[12:15], v[40:43], v[124:127], v[12:15]\n\t" \
    "buffer_load_dwordx4 %[l16], %[rs], %[a] offen offset:2048 sc1 lds\n\t" \
    "v_mfma_f32_16x16x32_bf16 v[0:3], v[44:47], v[120:123], v[0:3]\n\t" \
    "ds_read_b128 v[36:39], %[xp] offset:20480\n\t" \
    "v_mfma_f32_16x16x32_bf16 v[4:7], v[44:47], v[128:131], v[4:7]\n\t" \
    "ds_read_b128 v[40:43], %[xp] offset:21504\n\t" \
    "v_mfma_f32_16x16x32_bf16 v[8:11], v[48:51], v[120:123], v[8:11]\n\t" \
    "s_waitcnt lgkmcnt(4)\n\t" \
    "v_mfma_f32_16x16x32_bf16 v[12:15], v[48:51], v[128:131], v[12:15]\n\t" \
    "buffer_load_dwordx4 %[l16], %[rs], %[a] offen offset:3072 sc1 lds\n\t" \
    "s_waitcnt vmcnt(4)\n\t" \
    "s_barrier\n\t" \
    "s_setprio 1\n\t" \
    "s_add_u32 m0, %[sd], 0\n\t" \
    "s_add_u32 %[a], %[sx], 131072\n\t" \
    "s_add_u32 %[b], %[oo], 0x1000\n\t" \
    "s_cmp_lg_u32 %[t], 1\n\t" \
    "s_cselect_b64 %[em], -1, 0\n\t" \
    "v_mfma_f32_16x16x32_bf16 v[0:3], v[20:23], v[132:135], v[0:3]\n\t" \
    "ds_read_b128 v[44:47], %[xp] offset:22528\n\t" \
    "v_mfma_f32_16x16x32_bf16 v[4:7], v[20:23], v[140:143], v[4:7]\n\t" \
    "ds_read_b128 v[48:51], %[xp] offset:23552\n\t" \
    "v_mfma_f32_16x16x32_bf16 v[8:11], v[24:27], v[132:135], v[8:11]\n\t" \
    "ds_read_b128 v[52:55], %[pa] offset:0\n\t" \
    "s_waitcnt lgkmcnt(5)\n\t" \
    "v_mfma_f32_16x16x32_bf16 v[12:15], v[24:27], v[140:143], v[12:15]\n\t" \
    "buffer_load_dwordx4 %[l16], %[rs], %[a] offen offset:0 sc1 lds\n\t" \
    "v_mfma_f32_16x16x32_bf16 v[0:3], v[28:31], v[136:139], v[0:3]\n\t" \
    "ds_read_b128 v[20:23], %[xp] offset:24576\n\t" \
    "v_mfma_f32_16x16x32_bf16 v[4:7], v[28:31], v[144:147], v[4:7]\n\t" \
    "ds_read_b128 v[24:27], %[xp] offset:25600\n\t" \
    "v_mfma_f32_16x16x32_bf16 v[8:11], v[32:35], v[136:139], v[8:11]\n\t" \
    "ds_read_b128 v[56:59], %[pa] offset:1024\n\t" \
    "s_waitcnt lgkmcnt(6)\n\t" \
    "v_mfma_f32_16x16x32_bf16 v[12:15], v[32:35], v[144:147], v[12:15]\n\t" \
    "buffer_load_dwordx4 %[l16], %[rs], %[a] offen offset:1024 sc1 lds\n\t" \
    "v_mfma_f32_16x16x32_bf16 v[0:3], v[36:39], v[148:151], v[0:3]\n\t" \
    "ds_read_b128 v[28:31], %[xp] offset:26624\n\t" \
    "v_mfma_f32_16x16x32_bf16 v[4:7], v[36:39], v[156:159], v[4:7]\n\t" \
    "ds_read_b128 v[32:35], %[xp] offset:27648\n\t" \
    "v_mfma_f32_16x16x32_bf16 v[8:11], v[40:43], v[148:151], v[8:11]\n\t" \
    "ds_read_b128 v[60:63], %[pa] offset:2048\n\t" \
    "s_waitcnt lgkmcnt(7)\n\t" \
    "v_mfma_f32_16x16x32_bf16 v[12:15], v[40:43], v[156:159], v[12:15]\n\t" \
    "buffer_load_dwordx4 %[l16], %[rs], %[a] offen offset:2048 sc1 lds\n\t" \
    "v_mfma_f32_16x16x32_bf16 v[0:3], v[44:47], v[152:155], v[0:3]\n\t" \
    "ds_read_b128 v[36:39], %[xp] offset:28672\n\t" \
    "s_waitcnt lgkmcnt(7)\n\t" \
    "v_add_f32 v196, v196, v52\n\t" \
    "v_mfma_f32_16x16x32_bf16 v[4:7], v[44:47], v[160:163], v[4:7]\n\t" \
    "ds_read_b128 v[40:43], %[xp] offset:29696\n\t" \
    "v_add_f32 v197, v197, v53\n\t" \
    "v_mfma_f32_16x16x32_bf16 v[8:11], v[48:51], v[152:155], v[8:11]\n\t" \
    "ds_read_b128 v[64:67], %[pa] offset:3072\n\t" \
    "s_waitcnt lgkmcnt(7)\n\t" \
    "v_mfma_f32_16x16x32_bf16 v[12:15], v[48:51], v[160:163], v[12:15]\n\t" \
    "buffer_load_dwordx4 %[l16], %[rs], %[a] offen offset:3072 sc1 lds\n\t" \
    "v_add_f32 v198, v198, v54\n\t" \
    "v_add_f32 v199, v199, v55\n\t" \
    "v_mfma_f32_16x16x32_bf16 v[0:3], v[20:23], v[164:167], v[0:3]\n\t" \
    "ds_read_b128 v[44:47], %[xp] offset:30720\n\t" \
    "s_waitcnt lgkmcnt(7)\n\t" \
    "v_add_f32 v200, v200, v56\n\t" \
    "v_mfma_f32_16x16x32_bf16 v[4:7], v[20:23], v[172:175], v[4:7]\n\t" \
    "ds_read_b128 v[48:51], %[xp] offset:31744\n\t" \
    "v_add_f32 v201, v201, v57\n\t" \
    "v_mfma_f32_16x16x32_bf16 v[8:11], v[24:27], v[164:167], v[8:11]\n\t" \
    "v_add_f32 v202, v202, v58\n\t" \
    "s_waitcnt lgkmcnt(6)\n\t" \
    "v_mfma_f32_16x16x32_bf16 v[12:15], v[24:27], v[172:175], v[12:15]\n\t" \
    "v_add_f32 v203, v203, v59\n\t" \
    "s_waitcnt lgkmcnt(5)\n\t" \
    "v_add_f32 v204, v204, v60\n\t" \
    "v_add_f32 v205, v205, v61\n\t" \
    "v_mfma_f32_16x16x32_bf16 v[0:3], v[28:31], v[168:171], v[0:3]\n\t" \
    "ds_read_b128 v[20:23], %[xp] offset:32768\n\t" \
    "v_add_f32 v206, v206, v62\n\t" \
    "v_mfma_f32_16x16x32_bf16 v[4:7], v[28:31], v[176:179], v[4:7]\n\t" \
    "ds_read_b128 v[24:27], %[xp] offset:33792\n\t" \
    "v_add_f32 v207, v207, v63\n\t" \
    "v_mfma_f32_16x16x32_bf16 v[8:11], v[32:35], v[168:171], v[8:11]\n\t" \
    "s_waitcnt lgkmcnt(4)\n\t" \
    "v_add_f32 v208, v208, v64\n\t" \
    "v_mfma_f32_16x16x32_bf16 v[12:15], v[32:35], v[176:179], v[12:15]\n\t" \
    "v_add_f32 v209, v209, v65\n\t" \
    "v_add_f32 v210, v210, v66\n\t" \
    "v_add_f32 v211, v211, v67\n\t" \
    "v_mfma_f32_16x16x32_bf16 v[0:3], v[36:39], v[180:183], v[0:3]\n\t" \
    "ds_read_b128 v[28:31], %[xp] offset:34816\n\t" \
    "v_cvt_pk_bf16_f32 v52, v196, v197\n\t" \
    "v_mfma_f32_16x16x32_bf16 v[4:7], v[36:39], v[188:191], v[4:7]\n\t" \
    "ds_read_b128 v[32:35], %[xp] offset:35840\n\t" \
    "v_cvt_pk_bf16_f32 v53, v198, v199\n\t" \
    "v_mfma_f32_16x16x32_bf16 v[8:11], v[40:43], v[180:183], v[8:11]\n\t" \
    "v_cvt_pk_bf16_f32 v54, v204, v205\n\t" \
    "s_waitcnt lgkmcnt(4)\n\t" \
    "v_mfma_f32_16x16x32_bf16 v[12:15], v[40:43], v[188:191], v[12:15]\n\t" \
    "v_cvt_pk_bf16_f32 v55, v206, v207\n\t" \
    "v_cvt_pk_bf16_f32 v56, v200, v201\n\t" \
    "v_cvt_pk_bf16_f32 v57, v202, v203\n\t" \
    "v_mfma_f32_16x16x32_bf16 v[0:3], v[44:47], v[184:187], v[0:3]\n\t" \
    "ds_read_b128 v[36:39], %[xp] offset:36864\n\t" \
    "v_cvt_pk_bf16_f32 v58, v208, v209\n\t" \
    "v_mfma_f32_16x16x32_bf16 v[4:7], v[44:47], v[192:195], v[4:7]\n\t" \
    "ds_read_b128 v[40:43], %[xp] offset:37888\n\t" \
    "v_cvt_pk_bf16_f32 v59, v210, v211\n\t" \
    "v_mfma_f32_16x16x32_bf16 v[8:11], v[48:51], v[184:187], v[8:11]\n\t" \
    "s_waitcnt lgkmcnt(4)\n\t" \
    "v_mfma_f32_16x16x32_bf16 v[12:15], v[48:51], v[192:195], v[12:15]\n\t" \
    "s_mov_b64 exec, %[em]\n\t" \
    "buffer_store_dwordx4 v[52:55], %[l16], %[rs], %[oo] offen sc1\n\t" \
    "buffer_store_dwordx4 v[56:59], %[l16], %[rs], %[oo] offen offset:1024 sc1\n\t" \
    "s_mov_b64 exec, -1\n\t" \
    "s_add_u32 %[sx], %[sx], 65536\n\t" \
    "s_add_u32 %[t], %[t], 1\n\t" \
    "s_add_u32 %[oo], %[oo], 262144\n\t" \
    "s_waitcnt vmcnt(6)\n\t" \
    "s_cmp_gt_u32 %[t], " STR(SQUICK) " + 1\n\t" \
    "s_cbranch_scc1 B0_%=\n\t" \
    "s_waitcnt vmcnt(0)\n\t" \
    "B0_%=:\n\t" \
    "s_barrier\n\t" \
    "s_setprio 1\n\t" \
    "s_add_u32 m0, %[sd], 16384\n\t" \
    "s_add_u32 %[a], %[sx], 81920\n\t" \
    "v_mfma_f32_16x16x32_bf16 v[196:199], v[20:23], v[68:71], 0\n\t" \
    "ds_read_b128 v[44:47], %[xp] offset:38912\n\t" \
    "v_mfma_f32_16x16x32_bf16 v[200:203], v[20:23], v[76:79], 0\n\t" \
    "ds_read_b128 v[48:51], %[xp] offset:39936\n\t" \
    "v_mfma_f32_16x16x32_bf16 v[204:207], v[24:27], v[68:71], 0\n\t" \
    "s_waitcnt lgkmcnt(4)\n\t" \
    "v_mfma_f32_16x16x32_bf16 v[208:211], v[24:27], v[76:79], 0\n\t" \
    "v_mfma_f32_16x16x32_bf16 v[196:199], v[28:31], v[72:75], v[196:199]\n\t" \
    "ds_read_b128 v[20:23], %[xp] offset:40960\n\t" \
    "v_mfma_f32_16x16x32_bf16 v[200:203], v[28:31], v[80:83], v[200:203]\n\t" \
    "ds_read_b128 v[24:27], %[xp] offset:41984\n\t" \
    "v_mfma_f32_16x16x32_bf16 v[204:207], v[32:35], v[72:75], v[204:207]\n\t" \
    "s_waitcnt lgkmcnt(4)\n\t" \
    "v_mfma_f32_16x16x32_bf16 v[208:211], v[32:35], v[80:83], v[208:211]\n\t" \
    "v_mfma_f32_16x16x32_bf16 v[196:199], v[36:39], v[84:87], v[196:199]\n\t" \
    "ds_read_b128 v[28:31], %[xp] offset:43008\n\t" \
    "v_mfma_f32_16x16x32_bf16 v[200:203], v[36:39], v[92:95], v[200:203]\n\t" \
    "ds_read_b128 v[32:35], %[xp] offset:44032\n\t" \
    "v_mfma_f32_16x16x32_bf16 v[204:207], v[40:43], v[84:87], v[204:207]\n\t" \
    "s_waitcnt lgkmcnt(4)\n\t" \
    "v_mfma_f32_16x16x32_bf16 v[208:211], v[40:43], v[92:95], v[208:211]\n\t" \
    "v_mfma_f32_16x16x32_bf16 v[196:199], v[44:47], v[88:91], v[196:199]\n\t" \
    "ds_read_b128 v[36:39], %[xp] offset:45056\n\t" \
    "v_mfma_f32_16x16x32_bf16 v[200:203], v[44:47], v[96:99], v[200:203]\n\t" \
    "ds_read_b128 v[40:43], %[xp] offset:46080\n\t" \
    "v_mfma_f32_16x16x32_bf16 v[204:207], v[48:51], v[88:91], v[204:207]\n\t" \
    "s_waitcnt lgkmcnt(4)\n\t" \
    "v_mfma_f32_16x16x32_bf16 v[208:211], v[48:51], v[96:99], v[208:211]\n\t" \
    "v_mfma_f32_16x16x32_bf16 v[196:199], v[20:23], v[100:103], v[196:199]\n\t" \
    "ds_read_b128 v[44:47], %[xp] offset:47104\n\t" \
    "v_mfma_f32_16x16x32_bf16 v[200:203], v[20:23], v[108:111], v[200:203]\n\t" \
    "ds_read_b128 v[48:51], %[xp] offset:48128\n\t" \
    "v_mfma_f32_16x16x32_bf16 v[204:207], v[24:27], v[100:103], v[204:207]\n\t" \
    "s_waitcnt lgkmcnt(4)\n\t" \
    "v_mfma_f32_16x16x32_bf16 v[208:211], v[24:27], v[108:111], v[208:211]\n\t" \
    "buffer_load_dwordx4 %[l16], %[rs], %[a] offen offset:0 sc1 lds\n\t" \
    "v_mfma_f32_16x16x32_bf16 v[196:199], v[28:31], v[104:107], v[196:199]\n\t" \
    "ds_read_b128 v[20:23], %[xp] offset:49152\n\t" \
    "v_mfma_f32_16x16x32_bf16 v[200:203], v[28:31], v[112:115], v[200:203]\n\t" \
    "ds_read_b128 v[24:27], %[xp] offset:50176\n\t" \
    "v_mfma_f32_16x16x32_bf16 v[204:207], v[32:35], v[104:107], v[204:207]\n\t" \
    "s_waitcnt lgkmcnt(4)\n\t" \
    "v_mfma_f32_16x16x32_bf16 v[208:211], v[32:35], v[112:115], v[208:211]\n\t" \
    "buffer_load_dwordx4 %[l16], %[rs], %[a] offen offset:1024 sc1 lds\n\t" \
    "v_mfma_f32_16x16x32_bf16 v[196:199], v[36:39], v[116:119], v[196:199]\n\t" \
    "ds_read_b128 v[28:31], %[xp] offset:51200\n\t" \
    "v_mfma_f32_16x16x32_bf16 v[200:203], v[36:39], v[124:127], v[200:203]\n\t" \
    "ds_read_b128 v[32:35], %[xp] offset:52224\n\t" \
    "v_mfma_f32_16x16x32_bf16 v[204:207], v[40:43], v[116:119], v[204:207]\n\t" \
    "s_waitcnt lgkmcnt(4)\n\t" \
    "v_mfma_f32_16x16x32_bf16 v[208:211], v[40:43], v[124:127], v[208:211]\n\t" \
    "buffer_load_dwordx4 %[l16], %[rs], %[a] offen offset:2048 sc1 lds\n\t" \
    "v_mfma_f32_16x16x32_bf16 v[196:199], v[44:47], v[120:123], v[196:199]\n\t" \
    "ds_read_b128 v[36:39], %[xp] offset:53248\n\t" \
    "v_mfma_f32_16x16x32_bf16 v[200:203], v[44:47], v[128:131], v[200:203]\n\t" \
    "ds_read_b128 v[40:43], %[xp] offset:54272\n\t" \
    "v_mfma_f32_16x16x32_bf16 v[204:207], v[48:51], v[120:123], v[204:207]\n\t" \
    "s_waitcnt lgkmcnt(4)\n\t" \
    "v_mfma_f32_16x16x32_bf16 v[208:211], v[48:51], v[128:131], v[208:211]\n\t" \
    "buffer_load_dwordx4 %[l16], %[rs], %[a] offen offset:3072 sc1 lds\n\t" \
    "s_waitcnt vmcnt(4)\n\t" \
    "s_barrier\n\t" \
    "s_setprio 1\n\t" \
    "s_add_u32 m0, %[sd], 32768\n\t" \
    "s_add_u32 %[a], %[sx], 131072\n\t" \
    "s_add_u32 %[b], %[oo], 0x1000\n\t" \
    "s_cmp_lg_u32 %[t], 1\n\t" \
    "s_cselect_b64 %[em], -1, 0\n\t" \
    "v_mfma_f32_16x16x32_bf16 v[196:199], v[20:23], v[132:135], v[196:199]\n\t" \
    "ds_read_b128 v[44:47], %[xp] offset:55296\n\t" \
    "v_mfma_f32_16x16x32_bf16 v[200:203], v[20:23], v[140:143], v[200:203]\n\t" \
    "ds_read_b128 v[48:51], %[xp] offset:56320\n\t" \
    "v_mfma_f32_16x16x32_bf16 v[204:207], v[24:27], v[132:135], v[204:207]\n\t" \
    "ds_read_b128 v[52:55], %[pa] offset:0\n\t" \
    "s_waitcnt lgkmcnt(5)\n\t" \
    "v_mfma_f32_16x16x32_bf16 v[208:211], v[24:27], v[140:143], v[208:211]\n\t" \
    "buffer_load_dwordx4 %[l16], %[rs], %[a] offen offset:0 sc1 lds\n\t" \
    "v_mfma_f32_16x16x32_bf16 v[196:199], v[28:31], v[136:139], v[196:199]\n\t" \
    "ds_read_b128 v[20:23], %[xp] offset:57344\n\t" \
    "v_mfma_f32_16x16x32_bf16 v[200:203], v[28:31], v[144:147], v[200:203]\n\t" \
    "ds_read_b128 v[24:27], %[xp] offset:58368\n\t" \
    "v_mfma_f32_16x16x32_bf16 v[204:207], v[32:35], v[136:139], v[204:207]\n\t" \
    "ds_read_b128 v[56:59], %[pa] offset:1024\n\t" \
    "s_waitcnt lgkmcnt(6)\n\t" \
    "v_mfma_f32_16x16x32_bf16 v[208:211], v[32:35], v[144:147], v[208:211]\n\t" \
    "buffer_load_dwordx4 %[l16], %[rs], %[a] offen offset:1024 sc1 lds\n\t" \
    "v_mfma_f32_16x16x32_bf16 v[196:199], v[36:39], v[148:151], v[196:199]\n\t" \
    "ds_read_b128 v[28:31], %[xp] offset:59392\n\t" \
    "v_mfma_f32_16x16x32_bf16 v[200:203], v[36:39], v[156:159], v[200:203]\n\t" \
    "ds_read_b128 v[32:35], %[xp] offset:60416\n\t" \
    "v_mfma_f32_16x16x32_bf16 v[204:207], v[40:43], v[148:151], v[204:207]\n\t" \
    "ds_read_b128 v[60:63], %[pa] offset:2048\n\t" \
    "s_waitcnt lgkmcnt(7)\n\t" \
    "v_mfma_f32_16x16x32_bf16 v[208:211], v[40:43], v[156:159], v[208:211]\n\t" \
    "buffer_load_dwordx4 %[l16], %[rs], %[a] offen offset:2048 sc1 lds\n\t" \
    "v_mfma_f32_16x16x32_bf16 v[196:199], v[44:47], v[152:155], v[196:199]\n\t" \
    "ds_read_b128 v[36:39], %[xp] offset:61440\n\t" \
    "s_waitcnt lgkmcnt(7)\n\t" \
    "v_add_f32 v0, v0, v52\n\t" \
    "v_mfma_f32_16x16x32_bf16 v[200:203], v[44:47], v[160:163], v[200:203]\n\t" \
    "ds_read_b128 v[40:43], %[xp] offset:62464\n\t" \
    "v_add_f32 v1, v1, v53\n\t" \
    "v_mfma_f32_16x16x32_bf16 v[204:207], v[48:51], v[152:155], v[204:207]\n\t" \
    "ds_read_b128 v[64:67], %[pa] offset:3072\n\t" \
    "s_waitcnt lgkmcnt(7)\n\t" \
    "v_mfma_f32_16x16x32_bf16 v[208:211], v[48:51], v[160:163], v[208:211]\n\t" \
    "buffer_load_dwordx4 %[l16], %[rs], %[a] offen offset:3072 sc1 lds\n\t" \
    "v_add_f32 v2, v2, v54\n\t" \
    "v_add_f32 v3, v3, v55\n\t" \
    "v_mfma_f32_16x16x32_bf16 v[196:199], v[20:23], v[164:167], v[196:199]\n\t" \
    "ds_read_b128 v[44:47], %[xp] offset:63488\n\t" \
    "s_waitcnt lgkmcnt(7)\n\t" \
    "v_add_f32 v4, v4, v56\n\t" \
    "v_mfma_f32_16x16x32_bf16 v[200:203], v[20:23], v[172:175], v[200:203]\n\t" \
    "ds_read_b128 v[48:51], %[xp] offset:64512\n\t" \
    "v_add_f32 v5, v5, v57\n\t" \
    "v_mfma_f32_16x16x32_bf16 v[204:207], v[24:27], v[164:167], v[204:207]\n\t" \
    "v_add_f32 v6, v6, v58\n\t" \
    "s_waitcnt lgkmcnt(6)\n\t" \
    "v_mfma_f32_16x16x32_bf16 v[208:211], v[24:27], v[172:175], v[208:211]\n\t" \
    "v_add_f32 v7, v7, v59\n\t" \
    "s_waitcnt lgkmcnt(5)\n\t" \
    "v_add_f32 v8, v8, v60\n\t" \
    "v_add_f32 v9, v9, v61\n\t" \
    "v_mfma_f32_16x16x32_bf16 v[196:199], v[28:31], v[168:171], v[196:199]\n\t" \
    "ds_read_b128 v[20:23], %[xp] offset:0\n\t" \
    "v_add_f32 v10, v10, v62\n\t" \
    "v_mfma_f32_16x16x32_bf16 v[200:203], v[28:31], v[176:179], v[200:203]\n\t" \
    "ds_read_b128 v[24:27], %[xp] offset:1024\n\t" \
    "v_add_f32 v11, v11, v63\n\t" \
    "v_mfma_f32_16x16x32_bf16 v[204:207], v[32:35], v[168:171], v[204:207]\n\t" \
    "s_waitcnt lgkmcnt(4)\n\t" \
    "v_add_f32 v12, v12, v64\n\t" \
    "v_mfma_f32_16x16x32_bf16 v[208:211], v[32:35], v[176:179], v[208:211]\n\t" \
    "v_add_f32 v13, v13, v65\n\t" \
    "v_add_f32 v14, v14, v66\n\t" \
    "v_add_f32 v15, v15, v67\n\t" \
    "v_mfma_f32_16x16x32_bf16 v[196:199], v[36:39], v[180:183], v[196:199]\n\t" \
    "ds_read_b128 v[28:31], %[xp] offset:2048\n\t" \
    "v_cvt_pk_bf16_f32 v52, v0, v1\n\t" \
    "v_mfma_f32_16x16x32_bf16 v[200:203], v[36:39], v[188:191], v[200:203]\n\t" \
    "ds_read_b128 v[32:35], %[xp] offset:3072\n\t" \
    "v_cvt_pk_bf16_f32 v53, v2, v3\n\t" \
    "v_mfma_f32_16x16x32_bf16 v[204:207], v[40:43], v[180:183], v[204:207]\n\t" \
    "v_cvt_pk_bf16_f32 v54, v8, v9\n\t" \
    "s_waitcnt lgkmcnt(4)\n\t" \
    "v_mfma_f32_16x16x32_bf16 v[208:211], v[40:43], v[188:191], v[208:211]\n\t" \
    "v_cvt_pk_bf16_f32 v55, v10, v11\n\t" \
    "v_cvt_pk_bf16_f32 v56, v4, v5\n\t" \
    "v_cvt_pk_bf16_f32 v57, v6, v7\n\t" \
    "v_mfma_f32_16x16x32_bf16 v[196:199], v[44:47], v[184:187], v[196:199]\n\t" \
    "ds_read_b128 v[36:39], %[xp] offset:4096\n\t" \
    "v_cvt_pk_bf16_f32 v58, v12, v13\n\t" \
    "v_mfma_f32_16x16x32_bf16 v[200:203], v[44:47], v[192:195], v[200:203]\n\t" \
    "ds_read_b128 v[40:43], %[xp] offset:5120\n\t" \
    "v_cvt_pk_bf16_f32 v59, v14, v15\n\t" \
    "v_mfma_f32_16x16x32_bf16 v[204:207], v[48:51], v[184:187], v[204:207]\n\t" \
    "s_waitcnt lgkmcnt(4)\n\t" \
    "v_mfma_f32_16x16x32_bf16 v[208:211], v[48:51], v[192:195], v[208:211]\n\t" \
    "s_mov_b64 exec, %[em]\n\t" \
    "buffer_store_dwordx4 v[52:55], %[l16], %[rs], %[oo] offen sc1\n\t" \
    "buffer_store_dwordx4 v[56:59], %[l16], %[rs], %[oo] offen offset:1024 sc1\n\t" \
    "s_mov_b64 exec, -1\n\t" \
    "s_add_u32 %[sx], %[sx], 65536\n\t" \
    "s_add_u32 %[t], %[t], 1\n\t" \
    "s_add_u32 %[oo], %[oo], 262144\n\t" \
    "s_waitcnt vmcnt(6)\n\t" \
    "s_cmp_gt_u32 %[t], " STR(SQUICK) " + 1\n\t" \
    "s_cbranch_scc1 B1_%=\n\t" \
    "s_waitcnt vmcnt(0)\n\t" \
    "B1_%=:\n\t" \
    "s_barrier\n\t" \
    "s_cmp_lt_u32 %[t], %[te]\n\t" \
    "s_cbranch_scc1 L1_%=\n\t" \
    "s_cmp_eq_u32 %[fin], %[te]\n\t" \
    "s_cbranch_scc0 E_%=\n\t" \
    "s_nop 4\n\t" \
    "s_barrier\n\t" \
    "ds_read_b128 v[52:55], %[pa] offset:0\n\t" \
    "ds_read_b128 v[56:59], %[pa] offset:1024\n\t" \
    "ds_read_b128 v[60:63], %[pa] offset:2048\n\t" \
    "ds_read_b128 v[64:67], %[pa] offset:3072\n\t" \
    "s_add_u32 %[b], %[oo], 0x1000\n\t" \
    "s_waitcnt lgkmcnt(0)\n\t" \
    "v_add_f32 v196, v196, v52\n\t" \
    "v_add_f32 v197, v197, v53\n\t" \
    "v_add_f32 v198, v198, v54\n\t" \
    "v_add_f32 v199, v199, v55\n\t" \
    "v_add_f32 v200, v200, v56\n\t" \
    "v_add_f32 v201, v201, v57\n\t" \
    "v_add_f32 v202, v202, v58\n\t" \
    "v_add_f32 v203, v203, v59\n\t" \
    "v_add_f32 v204, v204, v60\n\t" \
    "v_add_f32 v205, v205, v61\n\t" \
    "v_add_f32 v206, v206, v62\n\t" \
    "v_add_f32 v207, v207, v63\n\t" \
    "v_add_f32 v208, v208, v64\n\t" \
    "v_add_f32 v209, v209, v65\n\t" \
    "v_add_f32 v210, v210, v66\n\t" \
    "v_add_f32 v211, v211, v67\n\t" \
    "v_cvt_pk_bf16_f32 v52, v196, v197\n\t" \
    "v_cvt_pk_bf16_f32 v53, v198, v199\n\t" \
    "v_cvt_pk_bf16_f32 v54, v204, v205\n\t" \
    "v_cvt_pk_bf16_f32 v55, v206, v207\n\t" \
    "v_cvt_pk_bf16_f32 v56, v200, v201\n\t" \
    "v_cvt_pk_bf16_f32 v57, v202, v203\n\t" \
    "v_cvt_pk_bf16_f32 v58, v208, v209\n\t" \
    "v_cvt_pk_bf16_f32 v59, v210, v211\n\t" \
    "buffer_store_dwordx4 v[52:55], %[l16], %[rs], %[oo] offen sc1\n\t" \
    "buffer_store_dwordx4 v[56:59], %[l16], %[rs], %[oo] offen offset:1024 sc1\n\t" \
    "E_%=:\n\t" \
    "s_setprio 0\n\t" \
    "s_waitcnt lgkmcnt(0)\n\t" \
    "s_nop 7\n\t" \
    : [t] "+s"(t), [sx] "+s"(sx), [oo] "+s"(oo), [a] "=&s"(tmp), [b] "=&s"(tmp2), [em] "=&s"(em), "+{v[20:27]}"(xf[0]), "+{v[28:35]}"(xf[1]), "+{v[36:43]}"(xf[2]), "+{v[196:203]}"(ac[0]), "+{v[204:211]}"(ac[1]) \
    : [te] "s"(TE_), [fin] "s"(FIN_), [kh] "s"(kh), [sd] "s"(sd), [rs] "s"(rs), [l16] "v"(l16), [xp] "v"(xp), [pa] "v"(pa), [fm] "s"(fm), [pm] "s"(pm), [fo] "s"(fo), [po] "s"(po), [pd] "s"(pd), [tag] "s"(P.tag), "{v[68:75]}"(w[0][0]), "{v[76:83]}"(w[0][1]), "{v[84:91]}"(w[1][0]), "{v[92:99]}"(w[1][1]), "{v[100:107]}"(w[2][0]), "{v[108:115]}"(w[2][1]), "{v[116:123]}"(w[3][0]), "{v[124:131]}"(w[3][1]), "{v[132:139]}"(w[4][0]), "{v[140:147]}"(w[4][1]), "{v[148:155]}"(w[5][0]), "{v[156:163]}"(w[5][1]), "{v[164:171]}"(w[6][0]), "{v[172:179]}"(w[6][1]), "{v[180:187]}"(w[7][0]), "{v[188:195]}"(w[7][1]) \
    : "memory", "scc", "v0", "v1", "v2", "v3", "v4", "v5", "v6", "v7", "v8", "v9", "v10", "v11", "v12", "v13", "v14", "v15", "v44", "v45", "v46", "v47", "v48", "v49", "v50", "v51", "v52", "v53", "v54", "v55", "v56", "v57", "v58", "v59", "v60", "v61", "v62", "v63", "v64", "v65", "v66", "v67")

#define S_LOOP_X(TE_, FIN_) \
  asm volatile( \
    "s_cmp_eq_u32 %[kh], 0\n\t" \
    "s_cbranch_scc0 L1_%=\n\t" \
    "L0_%=:\n\t" \
    "s_setprio 0\n\t" \
    "s_nop 3\n\t" \
    "ds_write_b128 %[pa], v[196:199] offset:0\n\t" \
    "ds_write_b128 %[pa], v[200:203] offset:1024\n\t" \
    "ds_write_b128 %[pa], v[204:207] offset:2048\n\t" \
    "ds_write_b128 %[pa], v[208:211] offset:3072\n\t" \
    "s_sub_u32 %[a], %[t], 1\n\t" \
    "v_mov_b32 v53, %[tag]\n\t" \
    "v_mov_b32 v52, %[a]\n\t" \
    "s_sub_u32 %[b], %[t], 2\n\t" \
    "s_cmp_lt_u32 %[b], " STR(SQUICK) "\n\t" \
    "s_cselect_b64 exec, %[fm], 0\n\t" \
    "buffer_store_dwordx2 v[52:53], %[l16], %[rs], %[fo] offen sc1\n\t" \
    "s_mov_b64 exec, -1\n\t" \
    "s_add_u32 m0, %[sd], 49152\n\t" \
    "s_add_u32 %[a], %[sx], 278528\n\t" \
    "s_waitcnt lgkmcnt(0)\n\t" \
    "buffer_load_dwordx4 %[l16], %[rs], %[a] offen offset:0 sc1 lds\n\t" \
    "buffer_load_dwordx4 %[l16], %[rs], %[a] offen offset:1024 sc1 lds\n\t" \
    "buffer_load_dwordx4 %[l16], %[rs], %[a] offen offset:2048 sc1 lds\n\t" \
    "buffer_load_dwordx4 %[l16], %[rs], %[a] offen offset:3072 sc1 lds\n\t" \
    "v_mfma_f32_16x16x32_bf16 v[0:3], v[68:71], v[20:23], 0\n\t" \
    "ds_read_b128 v[44:47], %[xp] offset:6144\n\t" \
    "v_mfma_f32_16x16x32_bf16 v[4:7], v[76:79], v[20:23], 0\n\t" \
    "ds_read_b128 v[48:51], %[xp] offset:7168\n\t" \
    "v_mfma_f32_16x16x32_bf16 v[8:11], v[68:71], v[24:27], 0\n\t" \
    "v_mfma_f32_16x16x32_bf16 v[12:15], v[76:79], v[24:27], 0\n\t" \
    "v_mfma_f32_16x16x32_bf16 v[0:3], v[72:75], v[28:31], v[0:3]\n\t" \
    "ds_read_b128 v[20:23], %[xp] offset:8192\n\t" \
    "v_mfma_f32_16x16x32_bf16 v[4:7], v[80:83], v[28:31], v[4:7]\n\t" \
    "ds_read_b128 v[24:27], %[xp] offset:9216\n\t" \
    "v_mfma_f32_16x16x32_bf16 v[8:11], v[72:75], v[32:35], v[8:11]\n\t" \
    "v_mfma_f32_16x16x32_bf16 v[12:15], v[80:83], v[32:35], v[12:15]\n\t" \
    "v_mfma_f32_16x16x32_bf16 v[0:3], v[84:87], v[36:39], v[0:3]\n\t" \
    "ds_read_b128 v[28:31], %[xp] offset:10240\n\t" \
    "v_mfma_f32_16x16x32_bf16 v[4:7], v[92:95], v[36:39], v[4:7]\n\t" \
    "ds_read_b128 v[32:35], %[xp] offset:11264\n\t" \
    "v_mfma_f32_16x16x32_bf16 v[8:11], v[84:87], v[40:43], v[8:11]\n\t" \
    "s_waitcnt lgkmcnt(4)\n\t" \
    "v_mfma_f32_16x16x32_bf16 v[12:15], v[92:95], v[40:43], v[12:15]\n\t" \
    "v_mfma_f32_16x16x32_bf16 v[0:3], v[88:91], v[44:47], v[0:3]\n\t" \
    "ds_read_b128 v[36:39], %[xp] offset:12288\n\t" \
    "v_mfma_f32_16x16x32_bf16 v[4:7], v[96:99], v[44:47], v[4:7]\n\t" \
    "ds_read_b128 v[40:43], %[xp] offset:13312\n\t" \
    "v_mfma_f32_16x16x32_bf16 v[8:11], v[88:91], v[48:51], v[8:11]\n\t" \
    "s_waitcnt lgkmcnt(4)\n\t" \
    "v_mfma_f32_16x16x32_bf16 v[12:15], v[96:99], v[48:51], v[12:15]\n\t" \
    "v_mfma_f32_16x16x32_bf16 v[0:3], v[100:103], v[20:23], v[0:3]\n\t" \
    "ds_read_b128 v[44:47], %[xp] offset:14336\n\t" \
    "v_mfma_f32_16x16x32_bf16 v[4:7], v[108:111], v[20:23], v[4:7]\n\t" \
    "ds_read_b128 v[48:51], %[xp] offset:15360\n\t" \
    "v_mfma_f32_16x16x32_bf16 v[8:11], v[100:103], v[24:27], v[8:11]\n\t" \
    "s_waitcnt lgkmcnt(4)\n\t" \
    "v_mfma_f32_16x16x32_bf16 v[12:15], v[108:111], v[24:27], v[12:15]\n\t" \
    "v_mfma_f32_16x16x32_bf16 v[0:3], v[104:107], v[28:31], v[0:3]\n\t" \
    "ds_read_b128 v[20:23], %[xp] offset:16384\n\t" \
    "v_mfma_f32_16x16x32_bf16 v[4:7], v[112:115], v[28:31], v[4:7]\n\t" \
    "ds_read_b128 v[24:27], %[xp] offset:17408\n\t" \
    "v_mfma_f32_16x16x32_bf16 v[8:11], v[104:107], v[32:35], v[8:11]\n\t" \
    "s_waitcnt lgkmcnt(4)\n\t" \
    "v_mfma_f32_16x16x32_bf16 v[12:15], v[112:115], v[32:35], v[12:15]\n\t" \
    "v_mfma_f32_16x16x32_bf16 v[0:3], v[116:119], v[36:39], v[0:3]\n\t" \
    "ds_read_b128 v[28:31], %[xp] offset:18432\n\t" \
    "v_mfma_f32_16x16x32_bf16 v[4:7], v[124:127], v[36:39], v[4:7]\n\t" \
    "ds_read_b128 v[32:35], %[xp] offset:19456\n\t" \
    "v_mfma_f32_16x16x32_bf16 v[8:11], v[116:119], v[40:43], v[8:11]\n\t" \
    "s_waitcnt lgkmcnt(4)\n\t" \
    "v_mfma_f32_16x16x32_bf16 v[12:15], v[124:127], v[40:43], v[12:15]\n\t" \
    "v_mfma_f32_16x16x32_bf16 v[0:3], v[120:123], v[44:47], v[0:3]\n\t" \
    "ds_read_b128 v[36:39], %[xp] offset:20480\n\t" \
    "v_mfma_f32_16x16x32_bf16 v[4:7], v[128:131], v[44:47], v[4:7]\n\t" \
    "ds_read_b128 v[40:43], %[xp] offset:21504\n\t" \
    "v_mfma_f32_16x16x32_bf16 v[8:11], v[120:123], v[48:51], v[8:11]\n\t" \
    "s_waitcnt lgkmcnt(4)\n\t" \
    "v_mfma_f32_16x16x32_bf16 v[12:15], v[128:131], v[48:51], v[12:15]\n\t" \
    "s_waitcnt vmcnt(4)\n\t" \
    "s_barrier\n\t" \
    "s_setprio 0\n\t" \
    "s_sub_u32 %[a], %[t], 1\n\t" \
    "v_mov_b32 v53, %[tag]\n\t" \
    "v_mov_b32 v52, %[a]\n\t" \
    "s_cmp_gt_u32 %[t], 1\n\t" \
    "s_cselect_b64 exec, %[fm], 0\n\t" \
    "buffer_store_dwordx2 v[52:53], %[l16], %[rs], %[fo] offen sc1\n\t" \
    "s_mov_b64 exec, %[pm]\n\t" \
    "s_mov_b32 m0, %[pd]\n\t" \
    "s_add_u32 %[a], %[sx], %[po]\n\t" \
    "buffer_load_dwordx4 %[l16], %[rs], %[a] offen sc1 lds\n\t" \
    "s_mov_b64 exec, -1\n\t" \
    "s_add_u32 m0, %[sd], 0\n\t" \
    "s_add_u32 %[a], %[sx], 524288\n\t" \
    "buffer_load_dwordx4 %[l16], %[rs], %[a] offen offset:0 sc1 lds\n\t" \
    "buffer_load_dwordx4 %[l16], %[rs], %[a] offen offset:1024 sc1 lds\n\t" \
    "buffer_load_dwordx4 %[l16], %[rs], %[a] offen offset:2048 sc1 lds\n\t" \
    "buffer_load_dwordx4 %[l16], %[rs], %[a] offen offset:3072 sc1 lds\n\t" \
    "v_mfma_f32_16x16x32_bf16 v[0:3], v[132:135], v[20:23], v[0:3]\n\t" \
    "ds_read_b128 v[44:47], %[xp] offset:22528\n\t" \
    "v_mfma_f32_16x16x32_bf16 v[4:7], v[140:143], v[20:23], v[4:7]\n\t" \
    "ds_read_b128 v[48:51], %[xp] offset:23552\n\t" \
    "v_mfma_f32_16x16x32_bf16 v[8:11], v[132:135], v[24:27], v[8:11]\n\t" \
    "s_waitcnt lgkmcnt(4)\n\t" \
    "v_mfma_f32_16x16x32_bf16 v[12:15], v[140:143], v[24:27], v[12:15]\n\t" \
    "v_mfma_f32_16x16x32_bf16 v[0:3], v[136:139], v[28:31], v[0:3]\n\t" \
    "ds_read_b128 v[20:23], %[xp] offset:24576\n\t" \
    "v_mfma_f32_16x16x32_bf16 v[4:7], v[144:147], v[28:31], v[4:7]\n\t" \
    "ds_read_b128 v[24:27], %[xp] offset:25600\n\t" \
    "v_mfma_f32_16x16x32_bf16 v[8:11], v[136:139], v[32:35], v[8:11]\n\t" \
    "s_waitcnt lgkmcnt(4)\n\t" \
    "v_mfma_f32_16x16x32_bf16 v[12:15], v[144:147], v[32:35], v[12:15]\n\t" \
    "v_mfma_f32_16x16x32_bf16 v[0:3], v[148:151], v[36:39], v[0:3]\n\t" \
    "ds_read_b128 v[28:31], %[xp] offset:26624\n\t" \
    "v_mfma_f32_16x16x32_bf16 v[4:7], v[156:159], v[36:39], v[4:7]\n\t" \
    "ds_read_b128 v[32:35], %[xp] offset:27648\n\t" \
    "v_mfma_f32_16x16x32_bf16 v[8:11], v[148:151], v[40:43], v[8:11]\n\t" \
    "s_waitcnt lgkmcnt(4)\n\t" \
    "v_mfma_f32_16x16x32_bf16 v[12:15], v[156:159], v[40:43], v[12:15]\n\t" \
    "v_mfma_f32_16x16x32_bf16 v[0:3], v[152:155], v[44:47], v[0:3]\n\t" \
    "ds_read_b128 v[36:39], %[xp] offset:28672\n\t" \
    "v_mfma_f32_16x16x32_bf16 v[4:7], v[160:163], v[44:47], v[4:7]\n\t" \
    "ds_read_b128 v[40:43], %[xp] offset:29696\n\t" \
    "v_mfma_f32_16x16x32_bf16 v[8:11], v[152:155], v[48:51], v[8:11]\n\t" \
    "s_waitcnt lgkmcnt(4)\n\t" \
    "v_mfma_f32_16x16x32_bf16 v[12:15], v[160:163], v[48:51], v[12:15]\n\t" \
    "v_mfma_f32_16x16x32_bf16 v[0:3], v[164:167], v[20:23], v[0:3]\n\t" \
    "ds_read_b128 v[44:47], %[xp] offset:30720\n\t" \
    "v_mfma_f32_16x16x32_bf16 v[4:7], v[172:175], v[20:23], v[4:7]\n\t" \
    "ds_read_b128 v[48:51], %[xp] offset:31744\n\t" \
    "v_mfma_f32_16x16x32_bf16 v[8:11], v[164:167], v[24:27], v[8:11]\n\t" \
    "s_waitcnt lgkmcnt(4)\n\t" \
    "v_mfma_f32_16x16x32_bf16 v[12:15], v[172:175], v[24:27], v[12:15]\n\t" \
    "v_mfma_f32_16x16x32_bf16 v[0:3], v[168:171], v[28:31], v[0:3]\n\t" \
    "ds_read_b128 v[20:23], %[xp] offset:32768\n\t" \
    "v_mfma_f32_16x16x32_bf16 v[4:7], v[176:179], v[28:31], v[4:7]\n\t" \
    "ds_read_b128 v[24:27], %[xp] offset:33792\n\t" \
    "v_mfma_f32_16x16x32_bf16 v[8:11], v[168:171], v[32:35], v[8:11]\n\t" \
    "s_waitcnt lgkmcnt(4)\n\t" \
    "v_mfma_f32_16x16x32_bf16 v[12:15], v[176:179], v[32:35], v[12:15]\n\t" \
    "v_mfma_f32_16x16x32_bf16 v[0:3], v[180:183], v[36:39], v[0:3]\n\t" \
    "ds_read_b128 v[28:31], %[xp] offset:34816\n\t" \
    "v_mfma_f32_16x16x32_bf16 v[4:7], v[188:191], v[36:39], v[4:7]\n\t" \
    "ds_read_b128 v[32:35], %[xp] offset:35840\n\t" \
    "v_mfma_f32_16x16x32_bf16 v[8:11], v[180:183], v[40:43], v[8:11]\n\t" \
    "s_waitcnt lgkmcnt(4)\n\t" \
    "v_mfma_f32_16x16x32_bf16 v[12:15], v[188:191], v[40:43], v[12:15]\n\t" \
    "v_mfma_f32_16x16x32_bf16 v[0:3], v[184:187], v[44:47], v[0:3]\n\t" \
    "ds_read_b128 v[36:39], %[xp] offset:36864\n\t" \
    "v_mfma_f32_16x16x32_bf16 v[4:7], v[192:195], v[44:47], v[4:7]\n\t" \
    "ds_read_b128 v[40:43], %[xp] offset:37888\n\t" \
    "v_mfma_f32_16x16x32_bf16 v[8:11], v[184:187], v[48:51], v[8:11]\n\t" \
    "s_waitcnt lgkmcnt(4)\n\t" \
    "v_mfma_f32_16x16x32_bf16 v[12:15], v[192:195], v[48:51], v[12:15]\n\t" \
    "s_add_u32 %[sx], %[sx], 262144\n\t" \
    "s_add_u32 %[t], %[t], 1\n\t" \
    "s_waitcnt vmcnt(4)\n\t" \
    "s_barrier\n\t" \
    "s_setprio 0\n\t" \
    "s_nop 3\n\t" \
    "ds_write_b128 %[pa], v[0:3] offset:0\n\t" \
    "ds_write_b128 %[pa], v[4:7] offset:1024\n\t" \
    "ds_write_b128 %[pa], v[8:11] offset:2048\n\t" \
    "ds_write_b128 %[pa], v[12:15] offset:3072\n\t" \
    "s_sub_u32 %[a], %[t], 1\n\t" \
    "v_mov_b32 v53, %[tag]\n\t" \
    "v_mov_b32 v52, %[a]\n\t" \
    "s_sub_u32 %[b], %[t], 2\n\t" \
    "s_cmp_lt_u32 %[b], " STR(SQUICK) "\n\t" \
    "s_cselect_b64 exec, %[fm], 0\n\t" \
    "buffer_store_dwordx2 v[52:53], %[l16], %[rs], %[fo] offen sc1\n\t" \
    "s_mov_b64 exec, -1\n\t" \
    "s_add_u32 m0, %[sd], 16384\n\t" \
    "s_add_u32 %[a], %[sx], 278528\n\t" \
    "s_waitcnt lgkmcnt(0)\n\t" \
    "buffer_load_dwordx4 %[l16], %[rs], %[a] offen offset:0 sc1 lds\n\t" \
    "buffer_load_dwordx4 %[l16], %[rs], %[a] offen offset:1024 sc1 lds\n\t" \
    "buffer_load_dwordx4 %[l16], %[rs], %[a] offen offset:2048 sc1 lds\n\t" \
    "buffer_load_dwordx4 %[l16], %[rs], %[a] offen offset:3072 sc1 lds\n\t" \
    "v_mfma_f32_16x16x32_bf16 v[196:199], v[68:71], v[20:23], 0\n\t" \
    "ds_read_b128 v[44:47], %[xp] offset:38912\n\t" \
    "v_mfma_f32_16x16x32_bf16 v[200:203], v[76:79], v[20:23], 0\n\t" \
    "ds_read_b128 v[48:51], %[xp] offset:39936\n\t" \
    "v_mfma_f32_16x16x32_bf16 v[204:207], v[68:71], v[24:27], 0\n\t" \
    "v_mfma_f32_16x16x32_bf16 v[208:211], v[76:79], v[24:27], 0\n\t" \
    "v_mfma_f32_16x16x32_bf16 v[196:199], v[72:75], v[28:31], v[196:199]\n\t" \
    "ds_read_b128 v[20:23], %[xp] offset:40960\n\t" \
    "v_mfma_f32_16x16x32_bf16 v[200:203], v[80:83], v[28:31], v[200:203]\n\t" \
    "ds_read_b128 v[24:27], %[xp] offset:41984\n\t" \
    "v_mfma_f32_16x16x32_bf16 v[204:207], v[72:75], v[32:35], v[204:207]\n\t" \
    "v_mfma_f32_16x16x32_bf16 v[208:211], v[80:83], v[32:35], v[208:211]\n\t" \
    "v_mfma_f32_16x16x32_bf16 v[196:199], v[84:87], v[36:39], v[196:199]\n\t" \
    "ds_read_b128 v[28:31], %[xp] offset:43008\n\t" \
    "v_mfma_f32_16x16x32_bf16 v[200:203], v[92:95], v[36:39], v[200:203]\n\t" \
    "ds_read_b128 v[32:35], %[xp] offset:44032\n\t" \
    "v_mfma_f32_16x16x32_bf16 v[204:207], v[84:87], v[40:43], v[204:207]\n\t" \
    "s_waitcnt lgkmcnt(4)\n\t" \
    "v_mfma_f32_16x16x32_bf16 v[208:211], v[92:95], v[40:43], v[208:211]\n\t" \
    "v_mfma_f32_16x16x32_bf16 v[196:199], v[88:91], v[44:47], v[196:199]\n\t" \
    "ds_read_b128 v[36:39], %[xp] offset:45056\n\t" \
    "v_mfma_f32_16x16x32_bf16 v[200:203], v[96:99], v[44:47], v[200:203]\n\t" \
    "ds_read_b128 v[40:43], %[xp] offset:46080\n\t" \
    "v_mfma_f32_16x16x32_bf16 v[204:207], v[88:91], v[48:51], v[204:207]\n\t" \
    "s_waitcnt lgkmcnt(4)\n\t" \
    "v_mfma_f32_16x16x32_bf16 v[208:211], v[96:99], v[48:51], v[208:211]\n\t" \
    "v_mfma_f32_16x16x32_bf16 v[196:199], v[100:103], v[20:23], v[196:199]\n\t" \
    "ds_read_b128 v[44:47], %[xp] offset:47104\n\t" \
    "v_mfma_f32_16x16x32_bf16 v[200:203], v[108:111], v[20:23], v[200:203]\n\t" \
    "ds_read_b128 v[48:51], %[xp] offset:48128\n\t" \
    "v_mfma_f32_16x16x32_bf16 v[204:207], v[100:103], v[24:27], v[204:207]\n\t" \
    "s_waitcnt lgkmcnt(4)\n\t" \
    "v_mfma_f32_16x16x32_bf16 v[208:211], v[108:111], v[24:27], v[208:211]\n\t" \
    "v_mfma_f32_16x16x32_bf16 v[196:199], v[104:107], v[28:31], v[196:199]\n\t" \
    "ds_read_b128 v[20:23], %[xp] offset:49152\n\t" \
    "v_mfma_f32_16x16x32_bf16 v[200:203], v[112:115], v[28:31], v[200:203]\n\t" \
    "ds_read_b128 v[24:27], %[xp] offset:50176\n\t" \
    "v_mfma_f32_16x16x32_bf16 v[204:207], v[104:107], v[32:35], v[204:207]\n\t" \
    "s_waitcnt lgkmcnt(4)\n\t" \
    "v_mfma_f32_16x16x32_bf16 v[208:211], v[112:115], v[32:35], v[208:211]\n\t" \
    "v_mfma_f32_16x16x32_bf16 v[196:199], v[116:119], v[36:39], v[196:199]\n\t" \
    "ds_read_b128 v[28:31], %[xp] offset:51200\n\t" \
    "v_mfma_f32_16x16x32_bf16 v[200:203], v[124:127], v[36:39], v[200:203]\n\t" \
    "ds_read_b128 v[32:35], %[xp] offset:52224\n\t" \
    "v_mfma_f32_16x16x32_bf16 v[204:207], v[116:119], v[40:43], v[204:207]\n\t" \
    "s_waitcnt lgkmcnt(4)\n\t" \
    "v_mfma_f32_16x16x32_bf16 v[208:211], v[124:127], v[40:43], v[208:211]\n\t" \
    "v_mfma_f32_16x16x32_bf16 v[196:199], v[120:123], v[44:47], v[196:199]\n\t" \
    "ds_read_b128 v[36:39], %[xp] offset:53248\n\t" \
    "v_mfma_f32_16x16x32_bf16 v[200:203], v[128:131], v[44:47], v[200:203]\n\t" \
    "ds_read_b128 v[40:43], %[xp] offset:54272\n\t" \
    "v_mfma_f32_16x16x32_bf16 v[204:207], v[120:123], v[48:51], v[204:207]\n\t" \
    "s_waitcnt lgkmcnt(4)\n\t" \
    "v_mfma_f32_16x16x32_bf16 v[208:211], v[128:131], v[48:51], v[208:211]\n\t" \
    "s_waitcnt vmcnt(4)\n\t" \
    "s_barrier\n\t" \
    "s_setprio 0\n\t" \
    "s_sub_u32 %[a], %[t], 1\n\t" \
    "v_mov_b32 v53, %[tag]\n\t" \
    "v_mov_b32 v52, %[a]\n\t" \
    "s_cmp_gt_u32 %[t], 1\n\t" \
    "s_cselect_b64 exec, %[fm], 0\n\t" \
    "buffer_store_dwordx2 v[52:53], %[l16], %[rs], %[fo] offen sc1\n\t" \
    "s_mov_b64 exec, %[pm]\n\t" \
    "s_mov_b32 m0, %[pd]\n\t" \
    "s_add_u32 %[a], %[sx], %[po]\n\t" \
    "buffer_load_dwordx4 %[l16], %[rs], %[a] offen sc1 lds\n\t" \
    "s_mov_b64 exec, -1\n\t" \
    "s_add_u32 m0, %[sd], 32768\n\t" \
    "s_add_u32 %[a], %[sx], 524288\n\t" \
    "buffer_load_dwordx4 %[l16], %[rs], %[a] offen offset:0 sc1 lds\n\t" \
    "buffer_load_dwordx4 %[l16], %[rs], %[a] offen offset:1024 sc1 lds\n\t" \
    "buffer_load_dwordx4 %[l16], %[rs], %[a] offen offset:2048 sc1 lds\n\t" \
    "buffer_load_dwordx4 %[l16], %[rs], %[a] offen offset:3072 sc1 lds\n\t" \
    "v_mfma_f32_16x16x32_bf16 v[196:199], v[132:135], v[20:23], v[196:199]\n\t" \
    "ds_read_b128 v[44:47], %[xp] offset:55296\n\t" \
    "v_mfma_f32_16x16x32_bf16 v[200:203], v[140:143], v[20:23], v[200:203]\n\t" \
    "ds_read_b128 v[48:51], %[xp] offset:56320\n\t" \
    "v_mfma_f32_16x16x32_bf16 v[204:207], v[132:135], v[24:27], v[204:207]\n\t" \
    "s_waitcnt lgkmcnt(4)\n\t" \
    "v_mfma_f32_16x16x32_bf16 v[208:211], v[140:143], v[24:27], v[208:211]\n\t" \
    "v_mfma_f32_16x16x32_bf16 v[196:199], v[136:139], v[28:31], v[196:199]\n\t" \
    "ds_read_b128 v[20:23], %[xp] offset:57344\n\t" \
    "v_mfma_f32_16x16x32_bf16 v[200:203], v[144:147], v[28:31], v[200:203]\n\t" \
    "ds_read_b128 v[24:27], %[xp] offset:58368\n\t" \
    "v_mfma_f32_16x16x32_bf16 v[204:207], v[136:139], v[32:35], v[204:207]\n\t" \
    "s_waitcnt lgkmcnt(4)\n\t" \
    "v_mfma_f32_16x16x32_bf16 v[208:211], v[144:147], v[32:35], v[208:211]\n\t" \
    "v_mfma_f32_16x16x32_bf16 v[196:199], v[148:151], v[36:39], v[196:199]\n\t" \
    "ds_read_b128 v[28:31], %[xp] offset:59392\n\t" \
    "v_mfma_f32_16x16x32_bf16 v[200:203], v[156:159], v[36:39], v[200:203]\n\t" \
    "ds_read_b128 v[32:35], %[xp] offset:60416\n\t" \
    "v_mfma_f32_16x16x32_bf16 v[204:207], v[148:151], v[40:43], v[204:207]\n\t" \
    "s_waitcnt lgkmcnt(4)\n\t" \
    "v_mfma_f32_16x16x32_bf16 v[208:211], v[156:159], v[40:43], v[208:211]\n\t" \
    "v_mfma_f32_16x16x32_bf16 v[196:199], v[152:155], v[44:47], v[196:199]\n\t" \
    "ds_read_b128 v[36:39], %[xp] offset:61440\n\t" \
    "v_mfma_f32_16x16x32_bf16 v[200:203], v[160:163], v[44:47], v[200:203]\n\t" \
    "ds_read_b128 v[40:43], %[xp] offset:62464\n\t" \
    "v_mfma_f32_16x16x32_bf16 v[204:207], v[152:155], v[48:51], v[204:207]\n\t" \
    "s_waitcnt lgkmcnt(4)\n\t" \
    "v_mfma_f32_16x16x32_bf16 v[208:211], v[160:163], v[48:51], v[208:211]\n\t" \
    "v_mfma_f32_16x16x32_bf16 v[196:199], v[164:167], v[20:23], v[196:199]\n\t" \
    "ds_read_b128 v[44:47], %[xp] offset:63488\n\t" \
    "v_mfma_f32_16x16x32_bf16 v[200:203], v[172:175], v[20:23], v[200:203]\n\t" \
    "ds_read_b128 v[48:51], %[xp] offset:64512\n\t" \
    "v_mfma_f32_16x16x32_bf16 v[204:207], v[164:167], v[24:27], v[204:207]\n\t" \
    "s_waitcnt lgkmcnt(4)\n\t" \
    "v_mfma_f32_16x16x32_bf16 v[208:211], v[172:175], v[24:27], v[208:211]\n\t" \
    "v_mfma_f32_16x16x32_bf16 v[196:199], v[168:171], v[28:31], v[196:199]\n\t" \
    "ds_read_b128 v[20:23], %[xp] offset:0\n\t" \
    "v_mfma_f32_16x16x32_bf16 v[200:203], v[176:179], v[28:31], v[200:203]\n\t" \
    "ds_read_b128 v[24:27], %[xp] offset:1024\n\t" \
    "v_mfma_f32_16x16x32_bf16 v[204:207], v[168:171], v[32:35], v[204:207]\n\t" \
    "s_waitcnt lgkmcnt(4)\n\t" \
    "v_mfma_f32_16x16x32_bf16 v[208:211], v[176:179], v[32:35], v[208:211]\n\t" \
    "v_mfma_f32_16x16x32_bf16 v[196:199], v[180:183], v[36:39], v[196:199]\n\t" \
    "ds_read_b128 v[28:31], %[xp] offset:2048\n\t" \
    "v_mfma_f32_16x16x32_bf16 v[200:203], v[188:191], v[36:39], v[200:203]\n\t" \
    "ds_read_b128 v[32:35], %[xp] offset:3072\n\t" \
    "v_mfma_f32_16x16x32_bf16 v[204:207], v[180:183], v[40:43], v[204:207]\n\t" \
    "s_waitcnt lgkmcnt(4)\n\t" \
    "v_mfma_f32_16x16x32_bf16 v[208:211], v[188:191], v[40:43], v[208:211]\n\t" \
    "v_mfma_f32_16x16x32_bf16 v[196:199], v[184:187], v[44:47], v[196:199]\n\t" \
    "ds_read_b128 v[36:39], %[xp] offset:4096\n\t" \
    "v_mfma_f32_16x16x32_bf16 v[200:203], v[192:195], v[44:47], v[200:203]\n\t" \
    "ds_read_b128 v[40:43], %[xp] offset:5120\n\t" \
    "v_mfma_f32_16x16x32_bf16 v[204:207], v[184:187], v[48:51], v[204:207]\n\t" \
    "s_waitcnt lgkmcnt(4)\n\t" \
    "v_mfma_f32_16x16x32_bf16 v[208:211], v[192:195], v[48:51], v[208:211]\n\t" \
    "s_add_u32 %[sx], %[sx], 262144\n\t" \
    "s_add_u32 %[t], %[t], 1\n\t" \
    "s_waitcnt vmcnt(4)\n\t" \
    "s_barrier\n\t" \
    "s_cmp_lt_u32 %[t], %[te]\n\t" \
    "s_cbranch_scc1 L0_%=\n\t" \
    "s_cmp_eq_u32 %[fin], %[te]\n\t" \
    "s_cbranch_scc0 E_%=\n\t" \
    "s_nop 4\n\t" \
    "ds_write_b128 %[pa], v[196:199] offset:0\n\t" \
    "ds_write_b128 %[pa], v[200:203] offset:1024\n\t" \
    "ds_write_b128 %[pa], v[204:207] offset:2048\n\t" \
    "ds_write_b128 %[pa], v[208:211] offset:3072\n\t" \
    "s_waitcnt lgkmcnt(0)\n\t" \
    "s_barrier\n\t" \
    "s_branch E_%=\n\t" \
    "L1_%=:\n\t" \
    "s_setprio 1\n\t" \
    "s_add_u32 m0, %[sd], 49152\n\t" \
    "s_add_u32 %[a], %[sx], 278528\n\t" \
    "v_mfma_f32_16x16x32_bf16 v[0:3], v[68:71], v[20:23], 0\n\t" \
    "ds_read_b128 v[44:47], %[xp] offset:6144\n\t" \
    "v_mfma_f32_16x16x32_bf16 v[4:7], v[76:79], v[20:23], 0\n\t" \
    "ds_read_b128 v[48:51], %[xp] offset:7168\n\t" \
    "v_mfma_f32_16x16x32_bf16 v[8:11], v[68:71], v[24:27], 0\n\t" \
    "s_waitcnt lgkmcnt(4)\n\t" \
    "v_mfma_f32_16x16x32_bf16 v[12:15], v[76:79], v[24:27], 0\n\t" \
    "v_mfma_f32_16x16x32_bf16 v[0:3], v[72:75], v[28:31], v[0:3]\n\t" \
    "ds_read_b128 v[20:23], %[xp] offset:8192\n\t" \
    "v_mfma_f32_16x16x32_bf16 v[4:7], v[80:83], v[28:31], v[4:7]\n\t" \
    "ds_read_b128 v[24:27], %[xp] offset:9216\n\t" \
    "v_mfma_f32_16x16x32_bf16 v[8:11], v[72:75], v[32:35], v[8:11]\n\t" \
    "s_waitcnt lgkmcnt(4)\n\t" \
    "v_mfma_f32_16x16x32_bf16 v[12:15], v[80:83], v[32:35], v[12:15]\n\t" \
    "v_mfma_f32_16x16x32_bf16 v[0:3], v[84:87], v[36:39], v[0:3]\n\t" \
    "ds_read_b128 v[28:31], %[xp] offset:10240\n\t" \
    "v_mfma_f32_16x16x32_bf16 v[4:7], v[92:95], v[36:39], v[4:7]\n\t" \
    "ds_read_b128 v[32:35], %[xp] offset:11264\n\t" \
    "v_mfma_f32_16x16x32_bf16 v[8:11], v[84:87], v[40:43], v[8:11]\n\t" \
    "s_waitcnt lgkmcnt(4)\n\t" \
    "v_mfma_f32_16x16x32_bf16 v[12:15], v[92:95], v[40:43], v[12:15]\n\t" \
    "v_mfma_f32_16x16x32_bf16 v[0:3], v[88:91], v[44:47], v[0:3]\n\t" \
    "ds_read_b128 v[36:39], %[xp] offset:12288\n\t" \
    "v_mfma_f32_16x16x32_bf16 v[4:7], v[96:99], v[44:47], v[4:7]\n\t" \
    "ds_read_b128 v[40:43], %[xp] offset:13312\n\t" \
    "v_mfma_f32_16x16x32_bf16 v[8:11], v[88:91], v[48:51], v[8:11]\n\t" \
    "s_waitcnt lgkmcnt(4)\n\t" \
    "v_mfma_f32_16x16x32_bf16 v[12:15], v[96:99], v[48:51], v[12:15]\n\t" \
    "v_mfma_f32_16x16x32_bf16 v[0:3], v[100:103], v[20:23], v[0:3]\n\t" \
    "ds_read_b128 v[44:47], %[xp] offset:14336\n\t" \
    "v_mfma_f32_16x16x32_bf16 v[4:7], v[108:111], v[20:23], v[4:7]\n\t" \
    "ds_read_b128 v[48:51], %[xp] offset:15360\n\t" \
    "v_mfma_f32_16x16x32_bf16 v[8:11], v[100:103], v[24:27], v[8:11]\n\t" \
    "s_waitcnt lgkmcnt(4)\n\t" \
    "v_mfma_f32_16x16x32_bf16 v[12:15], v[108:111], v[24:27], v[12:15]\n\t" \
    "buffer_load_dwordx4 %[l16], %[rs], %[a] offen offset:0 sc1 lds\n\t" \
    "v_mfma_f32_16x16x32_bf16 v[0:3], v[104:107], v[28:31], v[0:3]\n\t" \
    "ds_read_b128 v[20:23], %[xp] offset:16384\n\t" \
    "v_mfma_f32_16x16x32_bf16 v[4:7], v[112:115], v[28:31], v[4:7]\n\t" \
    "ds_read_b128 v[24:27], %[xp] offset:17408\n\t" \
    "v_mfma_f32_16x16x32_bf16 v[8:11], v[104:107], v[32:35], v[8:11]\n\t" \
    "s_waitcnt lgkmcnt(4)\n\t" \
    "v_mfma_f32_16x16x32_bf16 v[12:15], v[112:115], v[32:35], v[12:15]\n\t" \
    "buffer_load_dwordx4 %[l16], %[rs], %[a] offen offset:1024 sc1 lds\n\t" \
    "v_mfma_f32_16x16x32_bf16 v[0:3], v[116:119], v[36:39], v[0:3]\n\t" \
    "ds_read_b128 v[28:31], %[xp] offset:18432\n\t" \
    "v_mfma_f32_16x16x32_bf16 v[4:7], v[124:127], v[36:39], v[4:7]\n\t" \
    "ds_read_b128 v[32:35], %[xp] offset:19456\n\t" \
    "v_mfma_f32_16x16x32_bf16 v[8:11], v[116:119], v[40:43], v[8:11]\n\t" \
    "s_waitcnt lgkmcnt(4)\n\t" \
    "v_mfma_f32_16x16x32_bf16 v[12:15], v[124:127], v[40:43], v[12:15]\n\t" \
    "buffer_load_dwordx4 %[l16], %[rs], %[a] offen offset:2048 sc1 lds\n\t" \
    "v_mfma_f32_16x16x32_bf16 v[0:3], v[120:123], v[44:47], v[0:3]\n\t" \
    "ds_read_b128 v[36:39], %[xp] offset:20480\n\t" \
    "v_mfma_f32_16x16x32_bf16 v[4:7], v[128:131], v[44:47], v[4:7]\n\t" \
    "ds_read_b128 v[40:43], %[xp] offset:21504\n\t" \
    "v_mfma_f32_16x16x32_bf16 v[8:11], v[120:123], v[48:51], v[8:11]\n\t" \
    "s_waitcnt lgkmcnt(4)\n\t" \
    "v_mfma_f32_16x16x32_bf16 v[12:15], v[128:131], v[48:51], v[12:15]\n\t" \
    "buffer_load_dwordx4 %[l16], %[rs], %[a] offen offset:3072 sc1 lds\n\t" \
    "s_waitcnt vmcnt(4)\n\t" \
    "s_barrier\n\t" \
    "s_setprio 1\n\t" \
    "s_add_u32 m0, %[sd], 0\n\t" \
    "s_add_u32 %[a], %[sx], 524288\n\t" \
    "s_add_u32 %[b], %[oo], 0x1000\n\t" \
    "s_cmp_lg_u32 %[t], 0\n\t" \
    "s_cselect_b64 %[em], -1, 0\n\t" \
    "v_mfma_f32_16x16x32_bf16 v[0:3], v[132:135], v[20:23], v[0:3]\n\t" \
    "ds_read_b128 v[44:47], %[xp] offset:22528\n\t" \
    "v_mfma_f32_16x16x32_bf16 v[4:7], v[140:143], v[20:23], v[4:7]\n\t" \
    "ds_read_b128 v[48:51], %[xp] offset:23552\n\t" \
    "v_mfma_f32_16x16x32_bf16 v[8:11], v[132:135], v[24:27], v[8:11]\n\t" \
    "ds_read_b128 v[52:55], %[pa] offset:0\n\t" \
    "s_waitcnt lgkmcnt(5)\n\t" \
    "v_mfma_f32_16x16x32_bf16 v[12:15], v[140:143], v[24:27], v[12:15]\n\t" \
    "buffer_load_dwordx4 %[l16], %[rs], %[a] offen offset:0 sc1 lds\n\t" \
    "v_mfma_f32_16x16x32_bf16 v[0:3], v[136:139], v[28:31], v[0:3]\n\t" \
    "ds_read_b128 v[20:23], %[xp] offset:24576\n\t" \
    "v_mfma_f32_16x16x32_bf16 v[4:7], v[144:147], v[28:31], v[4:7]\n\t" \
    "ds_read_b128 v[24:27], %[xp] offset:25600\n\t" \
    "v_mfma_f32_16x16x32_bf16 v[8:11], v[136:139], v[32:35], v[8:11]\n\t" \
    "ds_read_b128 v[56:59], %[pa] offset:1024\n\t" \
    "s_waitcnt lgkmcnt(6)\n\t" \
    "v_mfma_f32_16x16x32_bf16 v[12:15], v[144:147], v[32:35], v[12:15]\n\t" \
    "buffer_load_dwordx4 %[l16], %[rs], %[a] offen offset:1024 sc1 lds\n\t" \
    "v_mfma_f32_16x16x32_bf16 v[0:3], v[148:151], v[36:39], v[0:3]\n\t" \
    "ds_read_b128 v[28:31], %[xp] offset:26624\n\t" \
    "v_mfma_f32_16x16x32_bf16 v[4:7], v[156:159], v[36:39], v[4:7]\n\t" \
    "ds_read_b128 v[32:35], %[xp] offset:27648\n\t" \
    "v_mfma_f32_16x16x32_bf16 v[8:11], v[148:151], v[40:43], v[8:11]\n\t" \
    "ds_read_b128 v[60:63], %[pa] offset:2048\n\t" \
    "s_waitcnt lgkmcnt(7)\n\t" \
    "v_mfma_f32_16x16x32_bf16 v[12:15], v[156:159], v[40:43], v[12:15]\n\t" \
    "buffer_load_dwordx4 %[l16], %[rs], %[a] offen offset:2048 sc1 lds\n\t" \
    "v_mfma_f32_16x16x32_bf16 v[0:3], v[152:155], v[44:47], v[0:3]\n\t" \
    "ds_read_b128 v[36:39], %[xp] offset:28672\n\t" \
    "s_waitcnt lgkmcnt(7)\n\t" \
    "v_add_f32 v196, v196, v52\n\t" \
    "v_mfma_f32_16x16x32_bf16 v[4:7], v[160:163], v[44:47], v[4:7]\n\t" \
    "ds_read_b128 v[40:43], %[xp] offset:29696\n\t" \
    "v_add_f32 v197, v197, v53\n\t" \
    "v_mfma_f32_16x16x32_bf16 v[8:11], v[152:155], v[48:51], v[8:11]\n\t" \
    "ds_read_b128 v[64:67], %[pa] offset:3072\n\t" \
    "s_waitcnt lgkmcnt(7)\n\t" \
    "v_mfma_f32_16x16x32_bf16 v[12:15], v[160:163], v[48:51], v[12:15]\n\t" \
    "buffer_load_dwordx4 %[l16], %[rs], %[a] offen offset:3072 sc1 lds\n\t" \
    "v_add_f32 v198, v198, v54\n\t" \
    "v_add_f32 v199, v199, v55\n\t" \
    "v_mfma_f32_16x16x32_bf16 v[0:3], v[164:167], v[20:23], v[0:3]\n\t" \
    "ds_read_b128 v[44:47], %[xp] offset:30720\n\t" \
    "s_waitcnt lgkmcnt(7)\n\t" \
    "v_add_f32 v200, v200, v56\n\t" \
    "v_mfma_f32_16x16x32_bf16 v[4:7], v[172:175], v[20:23], v[4:7]\n\t" \
    "ds_read_b128 v[48:51], %[xp] offset:31744\n\t" \
    "v_add_f32 v201, v201, v57\n\t" \
    "v_mfma_f32_16x16x32_bf16 v[8:11], v[164:167], v[24:27], v[8:11]\n\t" \
    "v_add_f32 v202, v202, v58\n\t" \
    "s_waitcnt lgkmcnt(6)\n\t" \
    "v_mfma_f32_16x16x32_bf16 v[12:15], v[172:175], v[24:27], v[12:15]\n\t" \
    "v_add_f32 v203, v203, v59\n\t" \
    "s_waitcnt lgkmcnt(5)\n\t" \
    "v_add_f32 v204, v204, v60\n\t" \
    "v_add_f32 v205, v205, v61\n\t" \
    "v_mfma_f32_16x16x32_bf16 v[0:3], v[168:171], v[28:31], v[0:3]\n\t" \
    "ds_read_b128 v[20:23], %[xp] offset:32768\n\t" \
    "v_add_f32 v206, v206, v62\n\t" \
    "v_mfma_f32_16x16x32_bf16 v[4:7], v[176:179], v[28:31], v[4:7]\n\t" \
    "ds_read_b128 v[24:27], %[xp] offset:33792\n\t" \
    "v_add_f32 v207, v207, v63\n\t" \
    "v_mfma_f32_16x16x32_bf16 v[8:11], v[168:171], v[32:35], v[8:11]\n\t" \
    "s_waitcnt lgkmcnt(4)\n\t" \
    "v_add_f32 v208, v208, v64\n\t" \
    "v_mfma_f32_16x16x32_bf16 v[12:15], v[176:179], v[32:35], v[12:15]\n\t" \
    "v_add_f32 v209, v209, v65\n\t" \
    "v_add_f32 v210, v210, v66\n\t" \
    "v_add_f32 v211, v211, v67\n\t" \
    "v_mfma_f32_16x16x32_bf16 v[0:3], v[180:183], v[36:39], v[0:3]\n\t" \
    "ds_read_b128 v[28:31], %[xp] offset:34816\n\t" \
    "v_mfma_f32_16x16x32_bf16 v[4:7], v[188:191], v[36:39], v[4:7]\n\t" \
    "ds_read_b128 v[32:35], %[xp] offset:35840\n\t" \
    "s_mov_b64 exec, %[em]\n\t" \
    "buffer_store_dwordx4 v[196:199], %[l16], %[rs], %[oo] offen offset:0 sc1\n\t" \
    "s_mov_b64 exec, -1\n\t" \
    "v_mfma_f32_16x16x32_bf16 v[8:11], v[180:183], v[40:43], v[8:11]\n\t" \
    "s_waitcnt lgkmcnt(4)\n\t" \
    "v_mfma_f32_16x16x32_bf16 v[12:15], v[188:191], v[40:43], v[12:15]\n\t" \
    "s_mov_b64 exec, %[em]\n\t" \
    "buffer_store_dwordx4 v[200:203], %[l16], %[rs], %[oo] offen offset:1024 sc1\n\t" \
    "s_mov_b64 exec, -1\n\t" \
    "v_mfma_f32_16x16x32_bf16 v[0:3], v[184:187], v[44:47], v[0:3]\n\t" \
    "ds_read_b128 v[36:39], %[xp] offset:36864\n\t" \
    "v_mfma_f32_16x16x32_bf16 v[4:7], v[192:195], v[44:47], v[4:7]\n\t" \
    "ds_read_b128 v[40:43], %[xp] offset:37888\n\t" \
    "s_mov_b64 exec, %[em]\n\t" \
    "buffer_store_dwordx4 v[204:207], %[l16], %[rs], %[oo] offen offset:2048 sc1\n\t" \
    "s_mov_b64 exec, -1\n\t" \
    "v_mfma_f32_16x16x32_bf16 v[8:11], v[184:187], v[48:51], v[8:11]\n\t" \
    "s_waitcnt lgkmcnt(4)\n\t" \
    "v_mfma_f32_16x16x32_bf16 v[12:15], v[192:195], v[48:51], v[12:15]\n\t" \
    "s_mov_b64 exec, %[em]\n\t" \
    "buffer_store_dwordx4 v[208:211], %[l16], %[rs], %[oo] offen offset:3072 sc1\n\t" \
    "s_mov_b64 exec, -1\n\t" \
    "s_add_u32 %[sx], %[sx], 262144\n\t" \
    "s_add_u32 %[t], %[t], 1\n\t" \
    "s_add_u32 %[oo], %[oo], 262144\n\t" \
    "s_waitcnt vmcnt(8)\n\t" \
    "s_cmp_gt_u32 %[t], " STR(SQUICK) " + 1\n\t" \
    "s_cbranch_scc1 B0_%=\n\t" \
    "s_waitcnt vmcnt(0)\n\t" \
    "B0_%=:\n\t" \
    "s_barrier\n\t" \
    "s_setprio 1\n\t" \
    "s_add_u32 m0, %[sd], 16384\n\t" \
    "s_add_u32 %[a], %[sx], 278528\n\t" \
    "v_mfma_f32_16x16x32_bf16 v[196:199], v[68:71], v[20:23], 0\n\t" \
    "ds_read_b128 v[44:47], %[xp] offset:38912\n\t" \
    "v_mfma_f32_16x16x32_bf16 v[200:203], v[76:79], v[20:23], 0\n\t" \
    "ds_read_b128 v[48:51], %[xp] offset:39936\n\t" \
    "v_mfma_f32_16x16x32_bf16 v[204:207], v[68:71], v[24:27], 0\n\t" \
    "s_waitcnt lgkmcnt(4)\n\t" \
    "v_mfma_f32_16x16x32_bf16 v[208:211], v[76:79], v[24:27], 0\n\t" \
    "v_mfma_f32_16x16x32_bf16 v[196:199], v[72:75], v[28:31], v[196:199]\n\t" \
    "ds_read_b128 v[20:23], %[xp] offset:40960\n\t" \
    "v_mfma_f32_16x16x32_bf16 v[200:203], v[80:83], v[28:31], v[200:203]\n\t" \
    "ds_read_b128 v[24:27], %[xp] offset:41984\n\t" \
    "v_mfma_f32_16x16x32_bf16 v[204:207], v[72:75], v[32:35], v[204:207]\n\t" \
    "s_waitcnt lgkmcnt(4)\n\t" \
    "v_mfma_f32_16x16x32_bf16 v[208:211], v[80:83], v[32:35], v[208:211]\n\t" \
    "v_mfma_f32_16x16x32_bf16 v[196:199], v[84:87], v[36:39], v[196:199]\n\t" \
    "ds_read_b128 v[28:31], %[xp] offset:43008\n\t" \
    "v_mfma_f32_16x16x32_bf16 v[200:203], v[92:95], v[36:39], v[200:203]\n\t" \
    "ds_read_b128 v[32:35], %[xp] offset:44032\n\t" \
    "v_mfma_f32_16x16x32_bf16 v[204:207], v[84:87], v[40:43], v[204:207]\n\t" \
    "s_waitcnt lgkmcnt(4)\n\t" \
    "v_mfma_f32_16x16x32_bf16 v[208:211], v[92:95], v[40:43], v[208:211]\n\t" \
    "v_mfma_f32_16x16x32_bf16 v[196:199], v[88:91], v[44:47], v[196:199]\n\t" \
    "ds_read_b128 v[36:39], %[xp] offset:45056\n\t" \
    "v_mfma_f32_16x16x32_bf16 v[200:203], v[96:99], v[44:47], v[200:203]\n\t" \
    "ds_read_b128 v[40:43], %[xp] offset:46080\n\t" \
    "v_mfma_f32_16x16x32_bf16 v[204:207], v[88:91], v[48:51], v[204:207]\n\t" \
    "s_waitcnt lgkmcnt(4)\n\t" \
    "v_mfma_f32_16x16x32_bf16 v[208:211], v[96:99], v[48:51], v[208:211]\n\t" \
    "v_mfma_f32_16x16x32_bf16 v[196:199], v[100:103], v[20:23], v[196:199]\n\t" \
    "ds_read_b128 v[44:47], %[xp] offset:47104\n\t" \
    "v_mfma_f32_16x16x32_bf16 v[200:203], v[108:111], v[20:23], v[200:203]\n\t" \
    "ds_read_b128 v[48:51], %[xp] offset:48128\n\t" \
    "v_mfma_f32_16x16x32_bf16 v[204:207], v[100:103], v[24:27], v[204:207]\n\t" \
    "s_waitcnt lgkmcnt(4)\n\t" \
    "v_mfma_f32_16x16x32_bf16 v[208:211], v[108:111], v[24:27], v[208:211]\n\t" \
    "buffer_load_dwordx4 %[l16], %[rs], %[a] offen offset:0 sc1 lds\n\t" \
    "v_mfma_f32_16x16x32_bf16 v[196:199], v[104:107], v[28:31], v[196:199]\n\t" \
    "ds_read_b128 v[20:23], %[xp] offset:49152\n\t" \
    "v_mfma_f32_16x16x32_bf16 v[200:203], v[112:115], v[28:31], v[200:203]\n\t" \
    "ds_read_b128 v[24:27], %[xp] offset:50176\n\t" \
    "v_mfma_f32_16x16x32_bf16 v[204:207], v[104:107], v[32:35], v[204:207]\n\t" \
    "s_waitcnt lgkmcnt(4)\n\t" \
    "v_mfma_f32_16x16x32_bf16 v[208:211], v[112:115], v[32:35], v[208:211]\n\t" \
    "buffer_load_dwordx4 %[l16], %[rs], %[a] offen offset:1024 sc1 lds\n\t" \
    "v_mfma_f32_16x16x32_bf16 v[196:199], v[116:119], v[36:39], v[196:199]\n\t" \
    "ds_read_b128 v[28:31], %[xp] offset:51200\n\t" \
    "v_mfma_f32_16x16x32_bf16 v[200:203], v[124:127], v[36:39], v[200:203]\n\t" \
    "ds_read_b128 v[32:35], %[xp] offset:52224\n\t" \
    "v_mfma_f32_16x16x32_bf16 v[204:207], v[116:119], v[40:43], v[204:207]\n\t" \
    "s_waitcnt lgkmcnt(4)\n\t" \
    "v_mfma_f32_16x16x32_bf16 v[208:211], v[124:127], v[40:43], v[208:211]\n\t" \
    "buffer_load_dwordx4 %[l16], %[rs], %[a] offen offset:2048 sc1 lds\n\t" \
    "v_mfma_f32_16x16x32_bf16 v[196:199], v[120:123], v[44:47], v[196:199]\n\t" \
    "ds_read_b128 v[36:39], %[xp] offset:53248\n\t" \
    "v_mfma_f32_16x16x32_bf16 v[200:203], v[128:131], v[44:47], v[200:203]\n\t" \
    "ds_read_b128 v[40:43], %[xp] offset:54272\n\t" \
    "v_mfma_f32_16x16x32_bf16 v[204:207], v[120:123], v[48:51], v[204:207]\n\t" \
    "s_waitcnt lgkmcnt(4)\n\t" \
    "v_mfma_f32_16x16x32_bf16 v[208:211], v[128:131], v[48:51], v[208:211]\n\t" \
    "buffer_load_dwordx4 %[l16], %[rs], %[a] offen offset:3072 sc1 lds\n\t" \
    "s_waitcnt vmcnt(4)\n\t" \
    "s_barrier\n\t" \
    "s_setprio 1\n\t" \
    "s_add_u32 m0, %[sd], 32768\n\t" \
    "s_add_u32 %[a], %[sx], 524288\n\t" \
    "s_add_u32 %[b], %[oo], 0x1000\n\t" \
    "s_cmp_lg_u32 %[t], 0\n\t" \
    "s_cselect_b64 %[em], -1, 0\n\t" \
    "v_mfma_f32_16x16x32_bf16 v[196:199], v[132:135], v[20:23], v[196:199]\n\t" \
    "ds_read_b128 v[44:47], %[xp] offset:55296\n\t" \
    "v_mfma_f32_16x16x32_bf16 v[200:203], v[140:143], v[20:23], v[200:203]\n\t" \
    "ds_read_b128 v[48:51], %[xp] offset:56320\n\t" \
    "v_mfma_f32_16x16x32_bf16 v[204:207], v[132:135], v[24:27], v[204:207]\n\t" \
    "ds_read_b128 v[52:55], %[pa] offset:0\n\t" \
    "s_waitcnt lgkmcnt(5)\n\t" \
    "v_mfma_f32_16x16x32_bf16 v[208:211], v[140:143], v[24:27], v[208:211]\n\t" \
    "buffer_load_dwordx4 %[l16], %[rs], %[a] offen offset:0 sc1 lds\n\t" \
    "v_mfma_f32_16x16x32_bf16 v[196:199], v[136:139], v[28:31], v[196:199]\n\t" \
    "ds_read_b128 v[20:23], %[xp] offset:57344\n\t" \
    "v_mfma_f32_16x16x32_bf16 v[200:203], v[144:147], v[28:31], v[200:203]\n\t" \
    "ds_read_b128 v[24:27], %[xp] offset:58368\n\t" \
    "v_mfma_f32_16x16x32_bf16 v[204:207], v[136:139], v[32:35], v[204:207]\n\t" \
    "ds_read_b128 v[56:59], %[pa] offset:1024\n\t" \
    "s_waitcnt lgkmcnt(6)\n\t" \
    "v_mfma_f32_16x16x32_bf16 v[208:211], v[144:147], v[32:35], v[208:211]\n\t" \
    "buffer_load_dwordx4 %[l16], %[rs], %[a] offen offset:1024 sc1 lds\n\t" \
    "v_mfma_f32_16x16x32_bf16 v[196:199], v[148:151], v[36:39], v[196:199]\n\t" \
    "ds_read_b128 v[28:31], %[xp] offset:59392\n\t" \
    "v_mfma_f32_16x16x32_bf16 v[200:203], v[156:159], v[36:39], v[200:203]\n\t" \
    "ds_read_b128 v[32:35], %[xp] offset:60416\n\t" \
    "v_mfma_f32_16x16x32_bf16 v[204:207], v[148:151], v[40:43], v[204:207]\n\t" \
    "ds_read_b128 v[60:63], %[pa] offset:2048\n\t" \
    "s_waitcnt lgkmcnt(7)\n\t" \
    "v_mfma_f32_16x16x32_bf16 v[208:211], v[156:159], v[40:43], v[208:211]\n\t" \
    "buffer_load_dwordx4 %[l16], %[rs], %[a] offen offset:2048 sc1 lds\n\t" \
    "v_mfma_f32_16x16x32_bf16 v[196:199], v[152:155], v[44:47], v[196:199]\n\t" \
    "ds_read_b128 v[36:39], %[xp] offset:61440\n\t" \
    "s_waitcnt lgkmcnt(7)\n\t" \
    "v_add_f32 v0, v0, v52\n\t" \
    "v_mfma_f32_16x16x32_bf16 v[200:203], v[160:163], v[44:47], v[200:203]\n\t" \
    "ds_read_b128 v[40:43], %[xp] offset:62464\n\t" \
    "v_add_f32 v1, v1, v53\n\t" \
    "v_mfma_f32_16x16x32_bf16 v[204:207], v[152:155], v[48:51], v[204:207]\n\t" \
    "ds_read_b128 v[64:67], %[pa] offset:3072\n\t" \
    "s_waitcnt lgkmcnt(7)\n\t" \
    "v_mfma_f32_16x16x32_bf16 v[208:211], v[160:163], v[48:51], v[208:211]\n\t" \
    "buffer_load_dwordx4 %[l16], %[rs], %[a] offen offset:3072 sc1 lds\n\t" \
    "v_add_f32 v2, v2, v54\n\t" \
    "v_add_f32 v3, v3, v55\n\t" \
    "v_mfma_f32_16x16x32_bf16 v[196:199], v[164:167], v[20:23], v[196:199]\n\t" \
    "ds_read_b128 v[44:47], %[xp] offset:63488\n\t" \
    "s_waitcnt lgkmcnt(7)\n\t" \
    "v_add_f32 v4, v4, v56\n\t" \
    "v_mfma_f32_16x16x32_bf16 v[200:203], v[172:175], v[20:23], v[200:203]\n\t" \
    "ds_read_b128 v[48:51], %[xp] offset:64512\n\t" \
    "v_add_f32 v5, v5, v57\n\t" \
    "v_mfma_f32_16x16x32_bf16 v[204:207], v[164:167], v[24:27], v[204:207]\n\t" \
    "v_add_f32 v6, v6, v58\n\t" \
    "s_waitcnt lgkmcnt(6)\n\t" \
    "v_mfma_f32_16x16x32_bf16 v[208:211], v[172:175], v[24:27], v[208:211]\n\t" \
    "v_add_f32 v7, v7, v59\n\t" \
    "s_waitcnt lgkmcnt(5)\n\t" \
    "v_add_f32 v8, v8, v60\n\t" \
    "v_add_f32 v9, v9, v61\n\t" \
    "v_mfma_f32_16x16x32_bf16 v[196:199], v[168:171], v[28:31], v[196:199]\n\t" \
    "ds_read_b128 v[20:23], %[xp] offset:0\n\t" \
    "v_add_f32 v10, v10, v62\n\t" \
    "v_mfma_f32_16x16x32_bf16 v[200:203], v[176:179], v[28:31], v[200:203]\n\t" \
    "ds_read_b128 v[24:27], %[xp] offset:1024\n\t" \
    "v_add_f32 v11, v11, v63\n\t" \
    "v_mfma_f32_16x16x32_bf16 v[204:207], v[168:171], v[32:35], v[204:207]\n\t" \
    "s_waitcnt lgkmcnt(4)\n\t" \
    "v_add_f32 v12, v12, v64\n\t" \
    "v_mfma_f32_16x16x32_bf16 v[208:211], v[176:179], v[32:35], v[208:211]\n\t" \
    "v_add_f32 v13, v13, v65\n\t" \
    "v_add_f32 v14, v14, v66\n\t" \
    "v_add_f32 v15, v15, v67\n\t" \
    "v_mfma_f32_16x16x32_bf16 v[196:199], v[180:183], v[36:39], v[196:199]\n\t" \
    "ds_read_b128 v[28:31], %[xp] offset:2048\n\t" \
    "v_mfma_f32_16x16x32_bf16 v[200:203], v[188:191], v[36:39], v[200:203]\n\t" \
    "ds_read_b128 v[32:35], %[xp] offset:3072\n\t" \
    "s_mov_b64 exec, %[em]\n\t" \
    "buffer_store_dwordx4 v[0:3], %[l16], %[rs], %[oo] offen offset:0 sc1\n\t" \
    "s_mov_b64 exec, -1\n\t" \
    "v_mfma_f32_16x16x32_bf16 v[204:207], v[180:183], v[40:43], v[204:207]\n\t" \
    "s_waitcnt lgkmcnt(4)\n\t" \
    "v_mfma_f32_16x16x32_bf16 v[208:211], v[188:191], v[40:43], v[208:211]\n\t" \
    "s_mov_b64 exec, %[em]\n\t" \
    "buffer_store_dwordx4 v[4:7], %[l16], %[rs], %[oo] offen offset:1024 sc1\n\t" \
    "s_mov_b64 exec, -1\n\t" \
    "v_mfma_f32_16x16x32_bf16 v[196:199], v[184:187], v[44:47], v[196:199]\n\t" \
    "ds_read_b128 v[36:39], %[xp] offset:4096\n\t" \
    "v_mfma_f32_16x16x32_bf16 v[200:203], v[192:195], v[44:47], v[200:203]\n\t" \
    "ds_read_b128 v[40:43], %[xp] offset:5120\n\t" \
    "s_mov_b64 exec, %[em]\n\t" \
    "buffer_store_dwordx4 v[8:11], %[l16], %[rs], %[oo] offen offset:2048 sc1\n\t" \
    "s_mov_b64 exec, -1\n\t" \
    "v_mfma_f32_16x16x32_bf16 v[204:207], v[184:187], v[48:51], v[204:207]\n\t" \
    "s_waitcnt lgkmcnt(4)\n\t" \
    "v_mfma_f32_16x16x32_bf16 v[208:211], v[192:195], v[48:51], v[208:211]\n\t" \
    "s_mov_b64 exec, %[em]\n\t" \
    "buffer_store_dwordx4 v[12:15], %[l16], %[rs], %[oo] offen offset:3072 sc1\n\t" \
    "s_mov_b64 exec, -1\n\t" \
    "s_add_u32 %[sx], %[sx], 262144\n\t" \
    "s_add_u32 %[t], %[t], 1\n\t" \
    "s_add_u32 %[oo], %[oo], 262144\n\t" \
    "s_waitcnt vmcnt(8)\n\t" \
    "s_cmp_gt_u32 %[t], " STR(SQUICK) " + 1\n\t" \
    "s_cbranch_scc1 B1_%=\n\t" \
    "s_waitcnt vmcnt(0)\n\t" \
    "B1_%=:\n\t" \
    "s_barrier\n\t" \
    "s_cmp_lt_u32 %[t], %[te]\n\t" \
    "s_cbranch_scc1 L1_%=\n\t" \
    "s_cmp_eq_u32 %[fin], %[te]\n\t" \
    "s_cbranch_scc0 E_%=\n\t" \
    "s_nop 4\n\t" \
    "s_barrier\n\t" \
    "ds_read_b128 v[52:55], %[pa] offset:0\n\t" \
    "ds_read_b128 v[56:59], %[pa] offset:1024\n\t" \
    "ds_read_b128 v[60:63], %[pa] offset:2048\n\t" \
    "ds_read_b128 v[64:67], %[pa] offset:3072\n\t" \
    "s_add_u32 %[b], %[oo], 0x1000\n\t" \
    "s_waitcnt lgkmcnt(0)\n\t" \
    "v_add_f32 v196, v196, v52\n\t" \
    "v_add_f32 v197, v197, v53\n\t" \
    "v_add_f32 v198, v198, v54\n\t" \
    "v_add_f32 v199, v199, v55\n\t" \
    "v_add_f32 v200, v200, v56\n\t" \
    "v_add_f32 v201, v201, v57\n\t" \
    "v_add_f32 v202, v202, v58\n\t" \
    "v_add_f32 v203, v203, v59\n\t" \
    "v_add_f32 v204, v204, v60\n\t" \
    "v_add_f32 v205, v205, v61\n\t" \
    "v_add_f32 v206, v206, v62\n\t" \
    "v_add_f32 v207, v207, v63\n\t" \
    "v_add_f32 v208, v208, v64\n\t" \
    "v_add_f32 v209, v209, v65\n\t" \
    "v_add_f32 v210, v210, v66\n\t" \
    "v_add_f32 v211, v211, v67\n\t" \
    "buffer_store_dwordx4 v[196:199], %[l16], %[rs], %[oo] offen offset:0 sc1\n\t" \
    "buffer_store_dwordx4 v[200:203], %[l16], %[rs], %[oo] offen offset:1024 sc1\n\t" \
    "buffer_store_dwordx4 v[204:207], %[l16], %[rs], %[oo] offen offset:2048 sc1\n\t" \
    "buffer_store_dwordx4 v[208:211], %[l16], %[rs], %[oo] offen offset:3072 sc1\n\t" \
    "E_%=:\n\t" \
    "s_setprio 0\n\t" \
    "s_waitcnt lgkmcnt(0)\n\t" \
    "s_nop 7\n\t" \
    : [t] "+s"(t), [sx] "+s"(sx), [oo] "+s"(oo), [a] "=&s"(tmp), [b] "=&s"(tmp2), [em] "=&s"(em), "+{v[20:27]}"(xf[0]), "+{v[28:35]}"(xf[1]), "+{v[36:43]}"(xf[2]), "+{v[196:203]}"(ac[0]), "+{v[204:211]}"(ac[1]) \
    : [te] "s"(TE_), [fin] "s"(FIN_), [kh] "s"(kh), [sd] "s"(sd), [rs] "s"(rs), [l16] "v"(l16), [xp] "v"(xp), [pa] "v"(pa), [fm] "s"(fm), [pm] "s"(pm), [fo] "s"(fo), [po] "s"(po), [pd] "s"(pd), [tag] "s"(P.tag), "{v[68:75]}"(w[0][0]), "{v[76:83]}"(w[0][1]), "{v[84:91]}"(w[1][0]), "{v[92:99]}"(w[1][1]), "{v[100:107]}"(w[2][0]), "{v[108:115]}"(w[2][1]), "{v[116:123]}"(w[3][0]), "{v[124:131]}"(w[3][1]), "{v[132:139]}"(w[4][0]), "{v[140:147]}"(w[4][1]), "{v[148:155]}"(w[5][0]), "{v[156:163]}"(w[5][1]), "{v[164:171]}"(w[6][0]), "{v[172:179]}"(w[6][1]), "{v[180:187]}"(w[7][0]), "{v[188:195]}"(w[7][1]) \
    : "memory", "scc", "v0", "v1", "v2", "v3", "v4", "v5", "v6", "v7", "v8", "v9", "v10", "v11", "v12", "v13", "v14", "v15", "v44", "v45", "v46", "v47", "v48", "v49", "v50", "v51", "v52", "v53", "v54", "v55", "v56", "v57", "v58", "v59", "v60", "v61", "v62", "v63", "v64", "v65", "v66", "v67")

#define ONE_X(XP_) \
  asm volatile( \
    "ds_read_b128 v[20:23], %[xp] offset:0\n\t" \
    "ds_read_b128 v[24:27], %[xp] offset:1024\n\t" \
    "ds_read_b128 v[28:31], %[xp] offset:2048\n\t" \
    "ds_read_b128 v[32:35], %[xp] offset:3072\n\t" \
    "ds_read_b128 v[36:39], %[xp] offset:4096\n\t" \
    "ds_read_b128 v[40:43], %[xp] offset:5120\n\t" \
    "s_waitcnt lgkmcnt(4)\n\t" \
    "v_mfma_f32_16x16x32_bf16 v[0:3], v[68:71], v[20:23], 0\n\t" \
    "ds_read_b128 v[44:47], %[xp] offset:6144\n\t" \
    "v_mfma_f32_16x16x32_bf16 v[4:7], v[76:79], v[20:23], 0\n\t" \
    "ds_read_b128 v[48:51], %[xp] offset:7168\n\t" \
    "v_mfma_f32_16x16x32_bf16 v[8:11], v[68:71], v[24:27], 0\n\t" \
    "s_waitcnt lgkmcnt(4)\n\t" \
    "v_mfma_f32_16x16x32_bf16 v[12:15], v[76:79], v[24:27], 0\n\t" \
    "v_mfma_f32_16x16x32_bf16 v[0:3], v[72:75], v[28:31], v[0:3]\n\t" \
    "ds_read_b128 v[20:23], %[xp] offset:8192\n\t" \
    "v_mfma_f32_16x16x32_bf16 v[4:7], v[80:83], v[28:31], v[4:7]\n\t" \
    "ds_read_b128 v[24:27], %[xp] offset:9216\n\t" \
    "v_mfma_f32_16x16x32_bf16 v[8:11], v[72:75], v[32:35], v[8:11]\n\t" \
    "s_waitcnt lgkmcnt(4)\n\t" \
    "v_mfma_f32_16x16x32_bf16 v[12:15], v[80:83], v[32:35], v[12:15]\n\t" \
    "v_mfma_f32_16x16x32_bf16 v[0:3], v[84:87], v[36:39], v[0:3]\n\t" \
    "ds_read_b128 v[28:31], %[xp] offset:10240\n\t" \
    "v_mfma_f32_16x16x32_bf16 v[4:7], v[92:95], v[36:39], v[4:7]\n\t" \
    "ds_read_b128 v[32:35], %[xp] offset:11264\n\t" \
    "v_mfma_f32_16x16x32_bf16 v[8:11], v[84:87], v[40:43], v[8:11]\n\t" \
    "s_waitcnt lgkmcnt(4)\n\t" \
    "v_mfma_f32_16x16x32_bf16 v[12:15], v[92:95], v[40:43], v[12:15]\n\t" \
    "v_mfma_f32_16x16x32_bf16 v[0:3], v[88:91], v[44:47], v[0:3]\n\t" \
    "ds_read_b128 v[36:39], %[xp] offset:12288\n\t" \
    "v_mfma_f32_16x16x32_bf16 v[4:7], v[96:99], v[44:47], v[4:7]\n\t" \
    "ds_read_b128 v[40:43], %[xp] offset:13312\n\t" \
    "v_mfma_f32_16x16x32_bf16 v[8:11], v[88:91], v[48:51], v[8:11]\n\t" \
    "s_waitcnt lgkmcnt(4)\n\t" \
    "v_mfma_f32_16x16x32_bf16 v[12:15], v[96:99], v[48:51], v[12:15]\n\t" \
    "v_mfma_f32_16x16x32_bf16 v[0:3], v[100:103], v[20:23], v[0:3]\n\t" \
    "ds_read_b128 v[44:47], %[xp] offset:14336\n\t" \
    "v_mfma_f32_16x16x32_bf16 v[4:7], v[108:111], v[20:23], v[4:7]\n\t" \
    "ds_read_b128 v[48:51], %[xp] offset:15360\n\t" \
    "v_mfma_f32_16x16x32_bf16 v[8:11], v[100:103], v[24:27], v[8:11]\n\t" \
    "s_waitcnt lgkmcnt(4)\n\t" \
    "v_mfma_f32_16x16x32_bf16 v[12:15], v[108:111], v[24:27], v[12:15]\n\t" \
    "s_waitcnt vmcnt(0)\n\t" \
    "s_barrier\n\t" \
    "v_mfma_f32_16x16x32_bf16 v[0:3], v[104:107], v[28:31], v[0:3]\n\t" \
    "ds_read_b128 v[20:23], %[xp] offset:16384\n\t" \
    "v_mfma_f32_16x16x32_bf16 v[4:7], v[112:115], v[28:31], v[4:7]\n\t" \
    "ds_read_b128 v[24:27], %[xp] offset:17408\n\t" \
    "v_mfma_f32_16x16x32_bf16 v[8:11], v[104:107], v[32:35], v[8:11]\n\t" \
    "s_waitcnt lgkmcnt(4)\n\t" \
    "v_mfma_f32_16x16x32_bf16 v[12:15], v[112:115], v[32:35], v[12:15]\n\t" \
    "buffer_load_dword v52, %[pv], %[rs], 0 offen sc1\n\t" \
    "v_mfma_f32_16x16x32_bf16 v[0:3], v[116:119], v[36:39], v[0:3]\n\t" \
    "ds_read_b128 v[28:31], %[xp] offset:18432\n\t" \
    "v_mfma_f32_16x16x32_bf16 v[4:7], v[124:127], v[36:39], v[4:7]\n\t" \
    "ds_read_b128 v[32:35], %[xp] offset:19456\n\t" \
    "v_mfma_f32_16x16x32_bf16 v[8:11], v[116:119], v[40:43], v[8:11]\n\t" \
    "s_waitcnt lgkmcnt(4)\n\t" \
    "v_mfma_f32_16x16x32_bf16 v[12:15], v[124:127], v[40:43], v[12:15]\n\t" \
    "v_mfma_f32_16x16x32_bf16 v[0:3], v[120:123], v[44:47], v[0:3]\n\t" \
    "ds_read_b128 v[36:39], %[xp] offset:20480\n\t" \
    "v_mfma_f32_16x16x32_bf16 v[4:7], v[128:131], v[44:47], v[4:7]\n\t" \
    "ds_read_b128 v[40:43], %[xp] offset:21504\n\t" \
    "v_mfma_f32_16x16x32_bf16 v[8:11], v[120:123], v[48:51], v[8:11]\n\t" \
    "s_waitcnt lgkmcnt(4)\n\t" \
    "v_mfma_f32_16x16x32_bf16 v[12:15], v[128:131], v[48:51], v[12:15]\n\t" \
    "v_mfma_f32_16x16x32_bf16 v[0:3], v[132:135], v[20:23], v[0:3]\n\t" \
    "ds_read_b128 v[44:47], %[xp] offset:22528\n\t" \
    "v_mfma_f32_16x16x32_bf16 v[4:7], v[140:143], v[20:23], v[4:7]\n\t" \
    "ds_read_b128 v[48:51], %[xp] offset:23552\n\t" \
    "v_mfma_f32_16x16x32_bf16 v[8:11], v[132:135], v[24:27], v[8:11]\n\t" \
    "s_waitcnt lgkmcnt(4)\n\t" \
    "v_mfma_f32_16x16x32_bf16 v[12:15], v[140:143], v[24:27], v[12:15]\n\t" \
    "v_mfma_f32_16x16x32_bf16 v[0:3], v[136:139], v[28:31], v[0:3]\n\t" \
    "ds_read_b128 v[20:23], %[xp] offset:24576\n\t" \
    "v_mfma_f32_16x16x32_bf16 v[4:7], v[144:147], v[28:31], v[4:7]\n\t" \
    "ds_read_b128 v[24:27], %[xp] offset:25600\n\t" \
    "v_mfma_f32_16x16x32_bf16 v[8:11], v[136:139], v[32:35], v[8:11]\n\t" \
    "s_waitcnt lgkmcnt(4)\n\t" \
    "v_mfma_f32_16x16x32_bf16 v[12:15], v[144:147], v[32:35], v[12:15]\n\t" \
    "v_mfma_f32_16x16x32_bf16 v[0:3], v[148:151], v[36:39], v[0:3]\n\t" \
    "ds_read_b128 v[28:31], %[xp] offset:26624\n\t" \
    "v_mfma_f32_16x16x32_bf16 v[4:7], v[156:159], v[36:39], v[4:7]\n\t" \
    "ds_read_b128 v[32:35], %[xp] offset:27648\n\t" \
    "v_mfma_f32_16x16x32_bf16 v[8:11], v[148:151], v[40:43], v[8:11]\n\t" \
    "s_waitcnt lgkmcnt(4)\n\t" \
    "v_mfma_f32_16x16x32_bf16 v[12:15], v[156:159], v[40:43], v[12:15]\n\t" \
    "v_mfma_f32_16x16x32_bf16 v[0:3], v[152:155], v[44:47], v[0:3]\n\t" \
    "ds_read_b128 v[36:39], %[xp] offset:28672\n\t" \
    "v_mfma_f32_16x16x32_bf16 v[4:7], v[160:163], v[44:47], v[4:7]\n\t" \
    "ds_read_b128 v[40:43], %[xp] offset:29696\n\t" \
    "v_mfma_f32_16x16x32_bf16 v[8:11], v[152:155], v[48:51], v[8:11]\n\t" \
    "s_waitcnt lgkmcnt(4)\n\t" \
    "v_mfma_f32_16x16x32_bf16 v[12:15], v[160:163], v[48:51], v[12:15]\n\t" \
    "v_mfma_f32_16x16x32_bf16 v[0:3], v[164:167], v[20:23], v[0:3]\n\t" \
    "ds_read_b128 v[44:47], %[xp] offset:30720\n\t" \
    "v_mfma_f32_16x16x32_bf16 v[4:7], v[172:175], v[20:23], v[4:7]\n\t" \
    "ds_read_b128 v[48:51], %[xp] offset:31744\n\t" \
    "v_mfma_f32_16x16x32_bf16 v[8:11], v[164:167], v[24:27], v[8:11]\n\t" \
    "s_waitcnt lgkmcnt(4)\n\t" \
    "v_mfma_f32_16x16x32_bf16 v[12:15], v[172:175], v[24:27], v[12:15]\n\t" \
    "v_mfma_f32_16x16x32_bf16 v[0:3], v[168:171], v[28:31], v[0:3]\n\t" \
    "v_mfma_f32_16x16x32_bf16 v[4:7], v[176:179], v[28:31], v[4:7]\n\t" \
    "v_mfma_f32_16x16x32_bf16 v[8:11], v[168:171], v[32:35], v[8:11]\n\t" \
    "s_waitcnt lgkmcnt(2)\n\t" \
    "v_mfma_f32_16x16x32_bf16 v[12:15], v[176:179], v[32:35], v[12:15]\n\t" \
    "v_mfma_f32_16x16x32_bf16 v[0:3], v[180:183], v[36:39], v[0:3]\n\t" \
    "v_mfma_f32_16x16x32_bf16 v[4:7], v[188:191], v[36:39], v[4:7]\n\t" \
    "v_mfma_f32_16x16x32_bf16 v[8:11], v[180:183], v[40:43], v[8:11]\n\t" \
    "s_waitcnt lgkmcnt(0)\n\t" \
    "v_mfma_f32_16x16x32_bf16 v[12:15], v[188:191], v[40:43], v[12:15]\n\t" \
    "v_mfma_f32_16x16x32_bf16 v[0:3], v[184:187], v[44:47], v[0:3]\n\t" \
    "v_mfma_f32_16x16x32_bf16 v[4:7], v[192:195], v[44:47], v[4:7]\n\t" \
    "v_mfma_f32_16x16x32_bf16 v[8:11], v[184:187], v[48:51], v[8:11]\n\t" \
    "v_mfma_f32_16x16x32_bf16 v[12:15], v[192:195], v[48:51], v[12:15]\n\t" \
    "s_waitcnt vmcnt(0)\n\t" \
    "s_nop 7\n\t" \
    : "=&{v52}"(pg), "=&{v[0:3]}"(a[0]), "=&{v[4:7]}"(a[1]), "=&{v[8:11]}"(a[2]), "=&{v[12:15]}"(a[3]) \
    : [xp] "v"(XP_), [pv] "v"(pv), [rs] "s"(rs), "{v[68:75]}"(w[0][0]), "{v[76:83]}"(w[0][1]), "{v[84:91]}"(w[1][0]), "{v[92:99]}"(w[1][1]), "{v[100:107]}"(w[2][0]), "{v[108:115]}"(w[2][1]), "{v[116:123]}"(w[3][0]), "{v[124:131]}"(w[3][1]), "{v[132:139]}"(w[4][0]), "{v[140:147]}"(w[4][1]), "{v[148:155]}"(w[5][0]), "{v[156:163]}"(w[5][1]), "{v[164:171]}"(w[6][0]), "{v[172:179]}"(w[6][1]), "{v[180:187]}"(w[7][0]), "{v[188:195]}"(w[7][1]) \
    : "memory", "v20", "v21", "v22", "v23", "v24", "v25", "v26", "v27", "v28", "v29", "v30", "v31", "v32", "v33", "v34", "v35", "v36", "v37", "v38", "v39", "v40", "v41", "v42", "v43", "v44", "v45", "v46", "v47", "v48", "v49", "v50", "v51")

#define ONE_K(XP_) \
  asm volatile( \
    "ds_read_b128 v[20:23], %[xp] offset:0\n\t" \
    "ds_read_b128 v[24:27], %[xp] offset:1024\n\t" \
    "ds_read_b128 v[28:31], %[xp] offset:2048\n\t" \
    "ds_read_b128 v[32:35], %[xp] offset:3072\n\t" \
    "ds_read_b128 v[36:39], %[xp] offset:4096\n\t" \
    "ds_read_b128 v[40:43], %[xp] offset:5120\n\t" \
    "s_waitcnt lgkmcnt(4)\n\t" \
    "v_mfma_f32_16x16x32_bf16 v[0:3], v[68:71], v[20:23], 0\n\t" \
    "ds_read_b128 v[44:47], %[xp] offset:6144\n\t" \
    "v_mfma_f32_16x16x32_bf16 v[4:7], v[76:79], v[20:23], 0\n\t" \
    "ds_read_b128 v[48:51], %[xp] offset:7168\n\t" \
    "v_mfma_f32_16x16x32_bf16 v[8:11], v[68:71], v[24:27], 0\n\t" \
    "s_waitcnt lgkmcnt(4)\n\t" \
    "v_mfma_f32_16x16x32_bf16 v[12:15], v[76:79], v[24:27], 0\n\t" \
    "v_mfma_f32_16x16x32_bf16 v[0:3], v[72:75], v[28:31], v[0:3]\n\t" \
    "ds_read_b128 v[20:23], %[xp] offset:8192\n\t" \
    "v_mfma_f32_16x16x32_bf16 v[4:7], v[80:83], v[28:31], v[4:7]\n\t" \
    "ds_read_b128 v[24:27], %[xp] offset:9216\n\t" \
    "v_mfma_f32_16x16x32_bf16 v[8:11], v[72:75], v[32:35], v[8:11]\n\t" \
    "s_waitcnt lgkmcnt(4)\n\t" \
    "v_mfma_f32_16x16x32_bf16 v[12:15], v[80:83], v[32:35], v[12:15]\n\t" \
    "v_mfma_f32_16x16x32_bf16 v[0:3], v[84:87], v[36:39], v[0:3]\n\t" \
    "ds_read_b128 v[28:31], %[xp] offset:10240\n\t" \
    "v_mfma_f32_16x16x32_bf16 v[4:7], v[92:95], v[36:39], v[4:7]\n\t" \
    "ds_read_b128 v[32:35], %[xp] offset:11264\n\t" \
    "v_mfma_f32_16x16x32_bf16 v[8:11], v[84:87], v[40:43], v[8:11]\n\t" \
    "s_waitcnt lgkmcnt(4)\n\t" \
    "v_mfma_f32_16x16x32_bf16 v[12:15], v[92:95], v[40:43], v[12:15]\n\t" \
    "v_mfma_f32_16x16x32_bf16 v[0:3], v[88:91], v[44:47], v[0:3]\n\t" \
    "ds_read_b128 v[36:39], %[xp] offset:12288\n\t" \
    "v_mfma_f32_16x16x32_bf16 v[4:7], v[96:99], v[44:47], v[4:7]\n\t" \
    "ds_read_b128 v[40:43], %[xp] offset:13312\n\t" \
    "v_mfma_f32_16x16x32_bf16 v[8:11], v[88:91], v[48:51], v[8:11]\n\t" \
    "s_waitcnt lgkmcnt(4)\n\t" \
    "v_mfma_f32_16x16x32_bf16 v[12:15], v[96:99], v[48:51], v[12:15]\n\t" \
    "v_mfma_f32_16x16x32_bf16 v[0:3], v[100:103], v[20:23], v[0:3]\n\t" \
    "ds_read_b128 v[44:47], %[xp] offset:14336\n\t" \
    "v_mfma_f32_16x16x32_bf16 v[4:7], v[108:111], v[20:23], v[4:7]\n\t" \
    "ds_read_b128 v[48:51], %[xp] offset:15360\n\t" \
    "v_mfma_f32_16x16x32_bf16 v[8:11], v[100:103], v[24:27], v[8:11]\n\t" \
    "s_waitcnt lgkmcnt(4)\n\t" \
    "v_mfma_f32_16x16x32_bf16 v[12:15], v[108:111], v[24:27], v[12:15]\n\t" \
    "s_waitcnt vmcnt(8)\n\t" \
    "s_barrier\n\t" \
    "v_mfma_f32_16x16x32_bf16 v[0:3], v[104:107], v[28:31], v[0:3]\n\t" \
    "ds_read_b128 v[20:23], %[xp] offset:16384\n\t" \
    "v_mfma_f32_16x16x32_bf16 v[4:7], v[112:115], v[28:31], v[4:7]\n\t" \
    "ds_read_b128 v[24:27], %[xp] offset:17408\n\t" \
    "v_mfma_f32_16x16x32_bf16 v[8:11], v[104:107], v[32:35], v[8:11]\n\t" \
    "s_waitcnt lgkmcnt(4)\n\t" \
    "v_mfma_f32_16x16x32_bf16 v[12:15], v[112:115], v[32:35], v[12:15]\n\t" \
    "v_mfma_f32_16x16x32_bf16 v[0:3], v[116:119], v[36:39], v[0:3]\n\t" \
    "ds_read_b128 v[28:31], %[xp] offset:18432\n\t" \
    "v_mfma_f32_16x16x32_bf16 v[4:7], v[124:127], v[36:39], v[4:7]\n\t" \
    "ds_read_b128 v[32:35], %[xp] offset:19456\n\t" \
    "v_mfma_f32_16x16x32_bf16 v[8:11], v[116:119], v[40:43], v[8:11]\n\t" \
    "s_waitcnt lgkmcnt(4)\n\t" \
    "v_mfma_f32_16x16x32_bf16 v[12:15], v[124:127], v[40:43], v[12:15]\n\t" \
    "v_mfma_f32_16x16x32_bf16 v[0:3], v[120:123], v[44:47], v[0:3]\n\t" \
    "ds_read_b128 v[36:39], %[xp] offset:20480\n\t" \
    "v_mfma_f32_16x16x32_bf16 v[4:7], v[128:131], v[44:47], v[4:7]\n\t" \
    "ds_read_b128 v[40:43], %[xp] offset:21504\n\t" \
    "v_mfma_f32_16x16x32_bf16 v[8:11], v[120:123], v[48:51], v[8:11]\n\t" \
    "s_waitcnt lgkmcnt(4)\n\t" \
    "v_mfma_f32_16x16x32_bf16 v[12:15], v[128:131], v[48:51], v[12:15]\n\t" \
    "v_mfma_f32_16x16x32_bf16 v[0:3], v[132:135], v[20:23], v[0:3]\n\t" \
    "ds_read_b128 v[44:47], %[xp] offset:22528\n\t" \
    "v_mfma_f32_16x16x32_bf16 v[4:7], v[140:143], v[20:23], v[4:7]\n\t" \
    "ds_read_b128 v[48:51], %[xp] offset:23552\n\t" \
    "v_mfma_f32_16x16x32_bf16 v[8:11], v[132:135], v[24:27], v[8:11]\n\t" \
    "s_waitcnt lgkmcnt(4)\n\t" \
    "v_mfma_f32_16x16x32_bf16 v[12:15], v[140:143], v[24:27], v[12:15]\n\t" \
    "v_mfma_f32_16x16x32_bf16 v[0:3], v[136:139], v[28:31], v[0:3]\n\t" \
    "ds_read_b128 v[20:23], %[xp] offset:24576\n\t" \
    "v_mfma_f32_16x16x32_bf16 v[4:7], v[144:147], v[28:31], v[4:7]\n\t" \
    "ds_read_b128 v[24:27], %[xp] offset:25600\n\t" \
    "v_mfma_f32_16x16x32_bf16 v[8:11], v[136:139], v[32:35], v[8:11]\n\t" \
    "s_waitcnt lgkmcnt(4)\n\t" \
    "v_mfma_f32_16x16x32_bf16 v[12:15], v[144:147], v[32:35], v[12:15]\n\t" \
    "v_mfma_f32_16x16x32_bf16 v[0:3], v[148:151], v[36:39], v[0:3]\n\t" \
    "ds_read_b128 v[28:31], %[xp] offset:26624\n\t" \
    "v_mfma_f32_16x16x32_bf16 v[4:7], v[156:159], v[36:39], v[4:7]\n\t" \
    "ds_read_b128 v[32:35], %[xp] offset:27648\n\t" \
    "v_mfma_f32_16x16x32_bf16 v[8:11], v[148:151], v[40:43], v[8:11]\n\t" \
    "s_waitcnt lgkmcnt(4)\n\t" \
    "v_mfma_f32_16x16x32_bf16 v[12:15], v[156:159], v[40:43], v[12:15]\n\t" \
    "v_mfma_f32_16x16x32_bf16 v[0:3], v[152:155], v[44:47], v[0:3]\n\t" \
    "ds_read_b128 v[36:39], %[xp] offset:28672\n\t" \
    "v_mfma_f32_16x16x32_bf16 v[4:7], v[160:163], v[44:47], v[4:7]\n\t" \
    "ds_read_b128 v[40:43], %[xp] offset:29696\n\t" \
    "v_mfma_f32_16x16x32_bf16 v[8:11], v[152:155], v[48:51], v[8:11]\n\t" \
    "s_waitcnt lgkmcnt(4)\n\t" \
    "v_mfma_f32_16x16x32_bf16 v[12:15], v[160:163], v[48:51], v[12:15]\n\t" \
    "v_mfma_f32_16x16x32_bf16 v[0:3], v[164:167], v[20:23], v[0:3]\n\t" \
    "ds_read_b128 v[44:47], %[xp] offset:30720\n\t" \
    "v_mfma_f32_16x16x32_bf16 v[4:7], v[172:175], v[20:23], v[4:7]\n\t" \
    "ds_read_b128 v[48:51], %[xp] offset:31744\n\t" \
    "v_mfma_f32_16x16x32_bf16 v[8:11], v[164:167], v[24:27], v[8:11]\n\t" \
    "s_waitcnt lgkmcnt(4)\n\t" \
    "v_mfma_f32_16x16x32_bf16 v[12:15], v[172:175], v[24:27], v[12:15]\n\t" \
    "v_mfma_f32_16x16x32_bf16 v[0:3], v[168:171], v[28:31], v[0:3]\n\t" \
    "v_mfma_f32_16x16x32_bf16 v[4:7], v[176:179], v[28:31], v[4:7]\n\t" \
    "v_mfma_f32_16x16x32_bf16 v[8:11], v[168:171], v[32:35], v[8:11]\n\t" \
    "s_waitcnt lgkmcnt(2)\n\t" \
    "v_mfma_f32_16x16x32_bf16 v[12:15], v[176:179], v[32:35], v[12:15]\n\t" \
    "v_mfma_f32_16x16x32_bf16 v[0:3], v[180:183], v[36:39], v[0:3]\n\t" \
    "v_mfma_f32_16x16x32_bf16 v[4:7], v[188:191], v[36:39], v[4:7]\n\t" \
    "v_mfma_f32_16x16x32_bf16 v[8:11], v[180:183], v[40:43], v[8:11]\n\t" \
    "s_waitcnt lgkmcnt(0)\n\t" \
    "v_mfma_f32_16x16x32_bf16 v[12:15], v[188:191], v[40:43], v[12:15]\n\t" \
    "v_mfma_f32_16x16x32_bf16 v[0:3], v[184:187], v[44:47], v[0:3]\n\t" \
    "v_mfma_f32_16x16x32_bf16 v[4:7], v[192:195], v[44:47], v[4:7]\n\t" \
    "v_mfma_f32_16x16x32_bf16 v[8:11], v[184:187], v[48:51], v[8:11]\n\t" \
    "v_mfma_f32_16x16x32_bf16 v[12:15], v[192:195], v[48:51], v[12:15]\n\t" \
    "s_nop 7\n\t" \
    : "=&{v[0:3]}"(a[0]), "=&{v[4:7]}"(a[1]), "=&{v[8:11]}"(a[2]), "=&{v[12:15]}"(a[3]) \
    : [xp] "v"(XP_), "{v[68:75]}"(w[0][0]), "{v[76:83]}"(w[0][1]), "{v[84:91]}"(w[1][0]), "{v[92:99]}"(w[1][1]), "{v[100:107]}"(w[2][0]), "{v[108:115]}"(w[2][1]), "{v[116:123]}"(w[3][0]), "{v[124:131]}"(w[3][1]), "{v[132:139]}"(w[4][0]), "{v[140:147]}"(w[4][1]), "{v[148:155]}"(w[5][0]), "{v[156:163]}"(w[5][1]), "{v[164:171]}"(w[6][0]), "{v[172:179]}"(w[6][1]), "{v[180:187]}"(w[7][0]), "{v[188:195]}"(w[7][1]) \
    : "memory", "v20", "v21", "v22", "v23", "v24", "v25", "v26", "v27", "v28", "v29", "v30", "v31", "v32", "v33", "v34", "v35", "v36", "v37", "v38", "v39", "v40", "v41", "v42", "v43", "v44", "v45", "v46", "v47", "v48", "v49", "v50", "v51")

#define ONE_V(XP_) \
  asm volatile( \
    "ds_read_b128 v[20:23], %[xp] offset:0\n\t" \
    "ds_read_b128 v[24:27], %[xp] offset:1024\n\t" \
    "ds_read_b128 v[28:31], %[xp] offset:2048\n\t" \
    "ds_read_b128 v[32:35], %[xp] offset:3072\n\t" \
    "ds_read_b128 v[36:39], %[xp] offset:4096\n\t" \
    "ds_read_b128 v[40:43], %[xp] offset:5120\n\t" \
    "s_waitcnt lgkmcnt(4)\n\t" \
    "v_mfma_f32_16x16x32_bf16 v[0:3], v[20:23], v[68:71], 0\n\t" \
    "ds_read_b128 v[44:47], %[xp] offset:6144\n\t" \
    "v_mfma_f32_16x16x32_bf16 v[4:7], v[20:23], v[76:79], 0\n\t" \
    "ds_read_b128 v[48:51], %[xp] offset:7168\n\t" \
    "v_mfma_f32_16x16x32_bf16 v[8:11], v[24:27], v[68:71], 0\n\t" \
    "s_waitcnt lgkmcnt(4)\n\t" \
    "v_mfma_f32_16x16x32_bf16 v[12:15], v[24:27], v[76:79], 0\n\t" \
    "v_mfma_f32_16x16x32_bf16 v[0:3], v[28:31], v[72:75], v[0:3]\n\t" \
    "ds_read_b128 v[20:23], %[xp] offset:8192\n\t" \
    "v_mfma_f32_16x16x32_bf16 v[4:7], v[28:31], v[80:83], v[4:7]\n\t" \
    "ds_read_b128 v[24:27], %[xp] offset:9216\n\t" \
    "v_mfma_f32_16x16x32_bf16 v[8:11], v[32:35], v[72:75], v[8:11]\n\t" \
    "s_waitcnt lgkmcnt(4)\n\t" \
    "v_mfma_f32_16x16x32_bf16 v[12:15], v[32:35], v[80:83], v[12:15]\n\t" \
    "v_mfma_f32_16x16x32_bf16 v[0:3], v[36:39], v[84:87], v[0:3]\n\t" \
    "ds_read_b128 v[28:31], %[xp] offset:10240\n\t" \
    "v_mfma_f32_16x16x32_bf16 v[4:7], v[36:39], v[92:95], v[4:7]\n\t" \
    "ds_read_b128 v[32:35], %[xp] offset:11264\n\t" \
    "v_mfma_f32_16x16x32_bf16 v[8:11], v[40:43], v[84:87], v[8:11]\n\t" \
    "s_waitcnt lgkmcnt(4)\n\t" \
    "v_mfma_f32_16x16x32_bf16 v[12:15], v[40:43], v[92:95], v[12:15]\n\t" \
    "v_mfma_f32_16x16x32_bf16 v[0:3], v[44:47], v[88:91], v[0:3]\n\t" \
    "ds_read_b128 v[36:39], %[xp] offset:12288\n\t" \
    "v_mfma_f32_16x16x32_bf16 v[4:7], v[44:47], v[96:99], v[4:7]\n\t" \
    "ds_read_b128 v[40:43], %[xp] offset:13312\n\t" \
    "v_mfma_f32_16x16x32_bf16 v[8:11], v[48:51], v[88:91], v[8:11]\n\t" \
    "s_waitcnt lgkmcnt(4)\n\t" \
    "v_mfma_f32_16x16x32_bf16 v[12:15], v[48:51], v[96:99], v[12:15]\n\t" \
    "v_mfma_f32_16x16x32_bf16 v[0:3], v[20:23], v[100:103], v[0:3]\n\t" \
    "ds_read_b128 v[44:47], %[xp] offset:14336\n\t" \
    "v_mfma_f32_16x16x32_bf16 v[4:7], v[20:23], v[108:111], v[4:7]\n\t" \
    "ds_read_b128 v[48:51], %[xp] offset:15360\n\t" \
    "v_mfma_f32_16x16x32_bf16 v[8:11], v[24:27], v[100:103], v[8:11]\n\t" \
    "s_waitcnt lgkmcnt(4)\n\t" \
    "v_mfma_f32_16x16x32_bf16 v[12:15], v[24:27], v[108:111], v[12:15]\n\t" \
    "s_waitcnt vmcnt(8)\n\t" \
    "s_barrier\n\t" \
    "v_mfma_f32_16x16x32_bf16 v[0:3], v[28:31], v[104:107], v[0:3]\n\t" \
    "ds_read_b128 v[20:23], %[xp] offset:16384\n\t" \
    "v_mfma_f32_16x16x32_bf16 v[4:7], v[28:31], v[112:115], v[4:7]\n\t" \
    "ds_read_b128 v[24:27], %[xp] offset:17408\n\t" \
    "v_mfma_f32_16x16x32_bf16 v[8:11], v[32:35], v[104:107], v[8:11]\n\t" \
    "s_waitcnt lgkmcnt(4)\n\t" \
    "v_mfma_f32_16x16x32_bf16 v[12:15], v[32:35], v[112:115], v[12:15]\n\t" \
    "v_mfma_f32_16x16x32_bf16 v[0:3], v[36:39], v[116:119], v[0:3]\n\t" \
    "ds_read_b128 v[28:31], %[xp] offset:18432\n\t" \
    "v_mfma_f32_16x16x32_bf16 v[4:7], v[36:39], v[124:127], v[4:7]\n\t" \
    "ds_read_b128 v[32:35], %[xp] offset:19456\n\t" \
    "v_mfma_f32_16x16x32_bf16 v[8:11], v[40:43], v[116:119], v[8:11]\n\t" \
    "s_waitcnt lgkmcnt(4)\n\t" \
    "v_mfma_f32_16x16x32_bf16 v[12:15], v[40:43], v[124:127], v[12:15]\n\t" \
    "v_mfma_f32_16x16x32_bf16 v[0:3], v[44:47], v[120:123], v[0:3]\n\t" \
    "ds_read_b128 v[36:39], %[xp] offset:20480\n\t" \
    "v_mfma_f32_16x16x32_bf16 v[4:7], v[44:47], v[128:131], v[4:7]\n\t" \
    "ds_read_b128 v[40:43], %[xp] offset:21504\n\t" \
    "v_mfma_f32_16x16x32_bf16 v[8:11], v[48:51], v[120:123], v[8:11]\n\t" \
    "s_waitcnt lgkmcnt(4)\n\t" \
    "v_mfma_f32_16x16x32_bf16 v[12:15], v[48:51], v[128:131], v[12:15]\n\t" \
    "v_mfma_f32_16x16x32_bf16 v[0:3], v[20:23], v[132:135], v[0:3]\n\t" \
    "ds_read_b128 v[44:47], %[xp] offset:22528\n\t" \
    "v_mfma_f32_16x16x32_bf16 v[4:7], v[20:23], v[140:143], v[4:7]\n\t" \
    "ds_read_b128 v[48:51], %[xp] offset:23552\n\t" \
    "v_mfma_f32_16x16x32_bf16 v[8:11], v[24:27], v[132:135], v[8:11]\n\t" \
    "s_waitcnt lgkmcnt(4)\n\t" \
    "v_mfma_f32_16x16x32_bf16 v[12:15], v[24:27], v[140:143], v[12:15]\n\t" \
    "v_mfma_f32_16x16x32_bf16 v[0:3], v[28:31], v[136:139], v[0:3]\n\t" \
    "ds_read_b128 v[20:23], %[xp] offset:24576\n\t" \
    "v_mfma_f32_16x16x32_bf16 v[4:7], v[28:31], v[144:147], v[4:7]\n\t" \
    "ds_read_b128 v[24:27], %[xp] offset:25600\n\t" \
    "v_mfma_f32_16x16x32_bf16 v[8:11], v[32:35], v[136:139], v[8:11]\n\t" \
    "s_waitcnt lgkmcnt(4)\n\t" \
    "v_mfma_f32_16x16x32_bf16 v[12:15], v[32:35], v[144:147], v[12:15]\n\t" \
    "v_mfma_f32_16x16x32_bf16 v[0:3], v[36:39], v[148:151], v[0:3]\n\t" \
    "ds_read_b128 v[28:31], %[xp] offset:26624\n\t" \
    "v_mfma_f32_16x16x32_bf16 v[4:7], v[36:39], v[156:159], v[4:7]\n\t" \
    "ds_read_b128 v[32:35], %[xp] offset:27648\n\t" \
    "v_mfma_f32_16x16x32_bf16 v[8:11], v[40:43], v[148:151], v[8:11]\n\t" \
    "s_waitcnt lgkmcnt(4)\n\t" \
    "v_mfma_f32_16x16x32_bf16 v[12:15], v[40:43], v[156:159], v[12:15]\n\t" \
    "v_mfma_f32_16x16x32_bf16 v[0:3], v[44:47], v[152:155], v[0:3]\n\t" \
    "ds_read_b128 v[36:39], %[xp] offset:28672\n\t" \
    "v_mfma_f32_16x16x32_bf16 v[4:7], v[44:47], v[160:163], v[4:7]\n\t" \
    "ds_read_b128 v[40:43], %[xp] offset:29696\n\t" \
    "v_mfma_f32_16x16x32_bf16 v[8:11], v[48:51], v[152:155], v[8:11]\n\t" \
    "s_waitcnt lgkmcnt(4)\n\t" \
    "v_mfma_f32_16x16x32_bf16 v[12:15], v[48:51], v[160:163], v[12:15]\n\t" \
    "v_mfma_f32_16x16x32_bf16 v[0:3], v[20:23], v[164:167], v[0:3]\n\t" \
    "ds_read_b128 v[44:47], %[xp] offset:30720\n\t" \
    "v_mfma_f32_16x16x32_bf16 v[4:7], v[20:23], v[172:175], v[4:7]\n\t" \
    "ds_read_b128 v[48:51], %[xp] offset:31744\n\t" \
    "v_mfma_f32_16x16x32_bf16 v[8:11], v[24:27], v[164:167], v[8:11]\n\t" \
    "s_waitcnt lgkmcnt(4)\n\t" \
    "v_mfma_f32_16x16x32_bf16 v[12:15], v[24:27], v[172:175], v[12:15]\n\t" \
    "v_mfma_f32_16x16x32_bf16 v[0:3], v[28:31], v[168:171], v[0:3]\n\t" \
    "v_mfma_f32_16x16x32_bf16 v[4:7], v[28:31], v[176:179], v[4:7]\n\t" \
    "v_mfma_f32_16x16x32_bf16 v[8:11], v[32:35], v[168:171], v[8:11]\n\t" \
    "s_waitcnt lgkmcnt(2)\n\t" \
    "v_mfma_f32_16x16x32_bf16 v[12:15], v[32:35], v[176:179], v[12:15]\n\t" \
    "v_mfma_f32_16x16x32_bf16 v[0:3], v[36:39], v[180:183], v[0:3]\n\t" \
    "v_mfma_f32_16x16x32_bf16 v[4:7], v[36:39], v[188:191], v[4:7]\n\t" \
    "v_mfma_f32_16x16x32_bf16 v[8:11], v[40:43], v[180:183], v[8:11]\n\t" \
    "s_waitcnt lgkmcnt(0)\n\t" \
    "v_mfma_f32_16x16x32_bf16 v[12:15], v[40:43], v[188:191], v[12:15]\n\t" \
    "v_mfma_f32_16x16x32_bf16 v[0:3], v[44:47], v[184:187], v[0:3]\n\t" \
    "v_mfma_f32_16x16x32_bf16 v[4:7], v[44:47], v[192:195], v[4:7]\n\t" \
    "v_mfma_f32_16x16x32_bf16 v[8:11], v[48:51], v[184:187], v[8:11]\n\t" \
    "v_mfma_f32_16x16x32_bf16 v[12:15], v[48:51], v[192:195], v[12:15]\n\t" \
    "s_nop 7\n\t" \
    : "=&{v[0:3]}"(a[0]), "=&{v[4:7]}"(a[1]), "=&{v[8:11]}"(a[2]), "=&{v[12:15]}"(a[3]) \
    : [xp] "v"(XP_), "{v[68:75]}"(w[0][0]), "{v[76:83]}"(w[0][1]), "{v[84:91]}"(w[1][0]), "{v[92:99]}"(w[1][1]), "{v[100:107]}"(w[2][0]), "{v[108:115]}"(w[2][1]), "{v[116:123]}"(w[3][0]), "{v[124:131]}"(w[3][1]), "{v[132:139]}"(w[4][0]), "{v[140:147]}"(w[4][1]), "{v[148:155]}"(w[5][0]), "{v[156:163]}"(w[5][1]), "{v[164:171]}"(w[6][0]), "{v[172:179]}"(w[6][1]), "{v[180:187]}"(w[7][0]), "{v[188:195]}"(w[7][1]) \
    : "memory", "v20", "v21", "v22", "v23", "v24", "v25", "v26", "v27", "v28", "v29", "v30", "v31", "v32", "v33", "v34", "v35", "v36", "v37", "v38", "v39", "v40", "v41", "v42", "v43", "v44", "v45", "v46", "v47", "v48", "v49", "v50", "v51")

#define H_LOOP1() \
  asm volatile( \
    "v_mov_b32 v20, 0\n\t" \
    "v_mov_b32 v21, 0\n\t" \
    "v_mov_b32 v22, 0\n\t" \
    "v_mov_b32 v23, 0\n\t" \
    "v_mov_b32 v24, 0\n\t" \
    "v_mov_b32 v25, 0\n\t" \
    "v_mov_b32 v26, 0\n\t" \
    "v_mov_b32 v27, 0\n\t" \
    "v_mov_b32 v28, 0\n\t" \
    "v_mov_b32 v29, 0\n\t" \
    "v_mov_b32 v30, 0\n\t" \
    "v_mov_b32 v31, 0\n\t" \
    "v_mov_b32 v32, 0\n\t" \
    "v_mov_b32 v33, 0\n\t" \
    "v_mov_b32 v34, 0\n\t" \
    "v_mov_b32 v35, 0\n\t" \
    "v_mov_b32 v36, 0\n\t" \
    "v_mov_b32 v37, 0\n\t" \
    "v_mov_b32 v38, 0\n\t" \
    "v_mov_b32 v39, 0\n\t" \
    "v_mov_b32 v40, 0\n\t" \
    "v_mov_b32 v41, 0\n\t" \
    "v_mov_b32 v42, 0\n\t" \
    "v_mov_b32 v43, 0\n\t" \
    "v_mov_b32 v44, 0\n\t" \
    "v_mov_b32 v45, 0\n\t" \
    "v_mov_b32 v46, 0\n\t" \
    "v_mov_b32 v47, 0\n\t" \
    "v_mov_b32 v48, 0\n\t" \
    "v_mov_b32 v49, 0\n\t" \
    "v_mov_b32 v50, 0\n\t" \
    "v_mov_b32 v51, 0\n\t" \
    "v_mov_b32 v52, 0\n\t" \
    "v_mov_b32 v53, 0\n\t" \
    "v_mov_b32 v54, 0\n\t" \
    "v_mov_b32 v55, 0\n\t" \
    "v_mov_b32 v56, 0\n\t" \
    "v_mov_b32 v57, 0\n\t" \
    "v_mov_b32 v58, 0\n\t" \
    "v_mov_b32 v59, 0\n\t" \
    "v_mov_b32 v60, 0\n\t" \
    "v_mov_b32 v61, 0\n\t" \
    "v_mov_b32 v62, 0\n\t" \
    "v_mov_b32 v63, 0\n\t" \
    "v_mov_b32 v64, 0\n\t" \
    "v_mov_b32 v65, 0\n\t" \
    "v_mov_b32 v66, 0\n\t" \
    "v_mov_b32 v67, 0\n\t" \
    "v_mov_b32 v68, 0\n\t" \
    "v_mov_b32 v69, 0\n\t" \
    "v_mov_b32 v70, 0\n\t" \
    "v_mov_b32 v71, 0\n\t" \
    "v_mov_b32 v72, 0\n\t" \
    "v_mov_b32 v73, 0\n\t" \
    "v_mov_b32 v74, 0\n\t" \
    "v_mov_b32 v75, 0\n\t" \
    "v_mov_b32 v76, 0\n\t" \
    "v_mov_b32 v77, 0\n\t" \
    "v_mov_b32 v78, 0\n\t" \
    "v_mov_b32 v79, 0\n\t" \
    "v_mov_b32 v80, 0\n\t" \
    "v_mov_b32 v81, 0\n\t" \
    "v_mov_b32 v82, 0\n\t" \
    "v_mov_b32 v83, 0\n\t" \
    "s_waitcnt vmcnt(8)\n\t" \
    "s_barrier\n\t" \
    "ds_read_b64_tr_b16 v[116:117], %[b0] offset:0\n\t" \
    "ds_read_b64_tr_b16 v[118:119], %[b0] offset:1024\n\t" \
    "ds_read_b64_tr_b16 v[124:125], %[b1] offset:0\n\t" \
    "ds_read_b64_tr_b16 v[126:127], %[b1] offset:1024\n\t" \
    "ds_read_b64_tr_b16 v[120:121], %[b0] offset:8192\n\t" \
    "ds_read_b64_tr_b16 v[122:123], %[b0] offset:9216\n\t" \
    "ds_read_b64_tr_b16 v[128:129], %[b1] offset:8192\n\t" \
    "ds_read_b64_tr_b16 v[130:131], %[b1] offset:9216\n\t" \
    "ds_read_b128 v[84:87], %[a] offset:0\n\t" \
    "ds_read_b128 v[88:91], %[a] offset:1024\n\t" \
    "ds_read_b128 v[92:95], %[a] offset:2048\n\t" \
    "ds_read_b128 v[96:99], %[a] offset:3072\n\t" \
    "ds_read_b128 v[100:103], %[a] offset:4096\n\t" \
    "ds_read_b128 v[104:107], %[a] offset:5120\n\t" \
    "ds_read_b128 v[108:111], %[a] offset:6144\n\t" \
    "ds_read_b128 v[112:115], %[a] offset:7168\n\t" \
    "L_%=:\n\t" \
    "s_add_u32 %[d], %[lw], %[c]\n\t" \
    "s_add_u32 %[y], %[c], 49152\n\t" \
    "s_cmp_eq_u32 %[y], 147456\n\t" \
    "s_cselect_b32 %[y], 0, %[y]\n\t" \
    "s_sub_u32 %[x], %[y], %[c]\n\t" \
    "s_mov_b32 %[c], %[y]\n\t" \
    "v_add_u32 %[a], %[x], %[a]\n\t" \
    "v_add_u32 %[b0], %[x], %[b0]\n\t" \
    "v_add_u32 %[b1], %[x], %[b1]\n\t" \
    "s_add_u32 %[x], %[t], 3\n\t" \
    "s_sub_u32 %[y], %[x], %[nt]\n\t" \
    "s_cmp_lt_u32 %[x], %[nt]\n\t" \
    "s_cselect_b32 %[x], %[x], %[y]\n\t" \
    "s_cselect_b32 m0, %[cb], %[cn]\n\t" \
    "s_mul_i32 %[y], %[x], %[kb]\n\t" \
    "s_add_u32 %[y], %[y], m0\n\t" \
    "s_lshl_b32 %[x], %[x], 11\n\t" \
    "s_add_u32 %[x], %[x], %[ca]\n\t" \
    "s_waitcnt vmcnt(4) lgkmcnt(0)\n\t" \
    "s_barrier\n\t" \
    "v_mfma_f32_16x16x32_bf16 v[20:23], v[116:119], v[84:87], v[20:23]\n\t" \
    "ds_read_b64_tr_b16 v[180:181], %[b0] offset:0\n\t" \
    "ds_read_b64_tr_b16 v[182:183], %[b0] offset:1024\n\t" \
    "s_add_u32 m0, %[d], 0\n\t" \
    "v_mfma_f32_16x16x32_bf16 v[24:27], v[124:127], v[84:87], v[24:27]\n\t" \
    "buffer_load_dwordx4 %[vb0], %[rb], %[y] offen lds\n\t" \
    "v_mfma_f32_16x16x32_bf16 v[20:23], v[120:123], v[88:91], v[20:23]\n\t" \
    "ds_read_b64_tr_b16 v[188:189], %[b1] offset:0\n\t" \
    "ds_read_b64_tr_b16 v[190:191], %[b1] offset:1024\n\t" \
    "v_mfma_f32_16x16x32_bf16 v[24:27], v[128:131], v[88:91], v[24:27]\n\t" \
    "ds_read_b64_tr_b16 v[184:185], %[b0] offset:8192\n\t" \
    "ds_read_b64_tr_b16 v[186:187], %[b0] offset:9216\n\t" \
    "v_mfma_f32_16x16x32_bf16 v[28:31], v[116:119], v[92:95], v[28:31]\n\t" \
    "ds_read_b64_tr_b16 v[192:193], %[b1] offset:8192\n\t" \
    "ds_read_b64_tr_b16 v[194:195], %[b1] offset:9216\n\t" \
    "s_add_u32 m0, %[d], 8192\n\t" \
    "v_mfma_f32_16x16x32_bf16 v[32:35], v[124:127], v[92:95], v[32:35]\n\t" \
    "buffer_load_dwordx4 %[vb1], %[rb], %[y] offen lds\n\t" \
    "v_mfma_f32_16x16x32_bf16 v[28:31], v[120:123], v[96:99], v[28:31]\n\t" \
    "ds_read_b128 v[148:151], %[a] offset:0\n\t" \
    "v_mfma_f32_16x16x32_bf16 v[32:35], v[128:131], v[96:99], v[32:35]\n\t" \
    "ds_read_b128 v[152:155], %[a] offset:1024\n\t" \
    "v_mfma_f32_16x16x32_bf16 v[36:39], v[116:119], v[100:103], v[36:39]\n\t" \
    "ds_read_b128 v[156:159], %[a] offset:2048\n\t" \
    "s_add_u32 m0, %[d], 32768\n\t" \
    "v_mfma_f32_16x16x32_bf16 v[40:43], v[124:127], v[100:103], v[40:43]\n\t" \
    "buffer_load_dwordx4 %[va0], %[ra], %[x] offen sc1 lds\n\t" \
    "v_mfma_f32_16x16x32_bf16 v[36:39], v[120:123], v[104:107], v[36:39]\n\t" \
    "ds_read_b128 v[160:163], %[a] offset:3072\n\t" \
    "v_mfma_f32_16x16x32_bf16 v[40:43], v[128:131], v[104:107], v[40:43]\n\t" \
    "ds_read_b128 v[164:167], %[a] offset:4096\n\t" \
    "v_mfma_f32_16x16x32_bf16 v[44:47], v[116:119], v[108:111], v[44:47]\n\t" \
    "ds_read_b128 v[168:171], %[a] offset:5120\n\t" \
    "s_add_u32 m0, %[d], 40960\n\t" \
    "v_mfma_f32_16x16x32_bf16 v[48:51], v[124:127], v[108:111], v[48:51]\n\t" \
    "buffer_load_dwordx4 %[va1], %[ra], %[x] offen sc1 lds\n\t" \
    "v_mfma_f32_16x16x32_bf16 v[44:47], v[120:123], v[112:115], v[44:47]\n\t" \
    "ds_read_b128 v[172:175], %[a] offset:6144\n\t" \
    "v_mfma_f32_16x16x32_bf16 v[48:51], v[128:131], v[112:115], v[48:51]\n\t" \
    "ds_read_b128 v[176:179], %[a] offset:7168\n\t" \
    "s_mov_b32 m0, %[pd]\n\t" \
    "s_cmp_lt_u32 %[ps], %[pe]\n\t" \
    "s_cselect_b64 exec, -1, 0\n\t" \
    "buffer_load_dword %[pv], %[rp], %[ps] offen sc1 lds\n\t" \
    "s_mov_b64 exec, -1\n\t" \
    "s_add_u32 %[ps], %[ps], %[pk]\n\t" \
    "s_add_u32 %[t], %[t], 1\n\t" \
    "s_add_u32 %[d], %[lw], %[c]\n\t" \
    "s_add_u32 %[y], %[c], 49152\n\t" \
    "s_cmp_eq_u32 %[y], 147456\n\t" \
    "s_cselect_b32 %[y], 0, %[y]\n\t" \
    "s_sub_u32 %[x], %[y], %[c]\n\t" \
    "s_mov_b32 %[c], %[y]\n\t" \
    "v_add_u32 %[a], %[x], %[a]\n\t" \
    "v_add_u32 %[b0], %[x], %[b0]\n\t" \
    "v_add_u32 %[b1], %[x], %[b1]\n\t" \
    "s_add_u32 %[x], %[t], 3\n\t" \
    "s_sub_u32 %[y], %[x], %[nt]\n\t" \
    "s_cmp_lt_u32 %[x], %[nt]\n\t" \
    "s_cselect_b32 %[x], %[x], %[y]\n\t" \
    "s_cselect_b32 m0, %[cb], %[cn]\n\t" \
    "s_mul_i32 %[y], %[x], %[kb]\n\t" \
    "s_add_u32 %[y], %[y], m0\n\t" \
    "s_lshl_b32 %[x], %[x], 11\n\t" \
    "s_add_u32 %[x], %[x], %[ca]\n\t" \
    "s_waitcnt vmcnt(4) lgkmcnt(0)\n\t" \
    "s_barrier\n\t" \
    "v_mfma_f32_16x16x32_bf16 v[20:23], v[180:183], v[148:151], v[20:23]\n\t" \
    "ds_read_b64_tr_b16 v[116:117], %[b0] offset:0\n\t" \
    "ds_read_b64_tr_b16 v[118:119], %[b0] offset:1024\n\t" \
    "s_add_u32 m0, %[d], 0\n\t" \
    "v_mfma_f32_16x16x32_bf16 v[24:27], v[188:191], v[148:151], v[24:27]\n\t" \
    "buffer_load_dwordx4 %[vb0], %[rb], %[y] offen lds\n\t" \
    "v_mfma_f32_16x16x32_bf16 v[20:23], v[184:187], v[152:155], v[20:23]\n\t" \
    "ds_read_b64_tr_b16 v[124:125], %[b1] offset:0\n\t" \
    "ds_read_b64_tr_b16 v[126:127], %[b1] offset:1024\n\t" \
    "v_mfma_f32_16x16x32_bf16 v[24:27], v[192:195], v[152:155], v[24:27]\n\t" \
    "ds_read_b64_tr_b16 v[120:121], %[b0] offset:8192\n\t" \
    "ds_read_b64_tr_b16 v[122:123], %[b0] offset:9216\n\t" \
    "v_mfma_f32_16x16x32_bf16 v[28:31], v[180:183], v[156:159], v[28:31]\n\t" \
    "ds_read_b64_tr_b16 v[128:129], %[b1] offset:8192\n\t" \
    "ds_read_b64_tr_b16 v[130:131], %[b1] offset:9216\n\t" \
    "s_add_u32 m0, %[d], 8192\n\t" \
    "v_mfma_f32_16x16x32_bf16 v[32:35], v[188:191], v[156:159], v[32:35]\n\t" \
    "buffer_load_dwordx4 %[vb1], %[rb], %[y] offen lds\n\t" \
    "v_mfma_f32_16x16x32_bf16 v[28:31], v[184:187], v[160:163], v[28:31]\n\t" \
    "ds_read_b128 v[84:87], %[a] offset:0\n\t" \
    "v_mfma_f32_16x16x32_bf16 v[32:35], v[192:195], v[160:163], v[32:35]\n\t" \
    "ds_read_b128 v[88:91], %[a] offset:1024\n\t" \
    "v_mfma_f32_16x16x32_bf16 v[36:39], v[180:183], v[164:167], v[36:39]\n\t" \
    "ds_read_b128 v[92:95], %[a] offset:2048\n\t" \
    "s_add_u32 m0, %[d], 32768\n\t" \
    "v_mfma_f32_16x16x32_bf16 v[40:43], v[188:191], v[164:167], v[40:43]\n\t" \
    "buffer_load_dwordx4 %[va0], %[ra], %[x] offen sc1 lds\n\t" \
    "v_mfma_f32_16x16x32_bf16 v[36:39], v[184:187], v[168:171], v[36:39]\n\t" \
    "ds_read_b128 v[96:99], %[a] offset:3072\n\t" \
    "v_mfma_f32_16x16x32_bf16 v[40:43], v[192:195], v[168:171], v[40:43]\n\t" \
    "ds_read_b128 v[100:103], %[a] offset:4096\n\t" \
    "v_mfma_f32_16x16x32_bf16 v[44:47], v[180:183], v[172:175], v[44:47]\n\t" \
    "ds_read_b128 v[104:107], %[a] offset:5120\n\t" \
    "s_add_u32 m0, %[d], 40960\n\t" \
    "v_mfma_f32_16x16x32_bf16 v[48:51], v[188:191], v[172:175], v[48:51]\n\t" \
    "buffer_load_dwordx4 %[va1], %[ra], %[x] offen sc1 lds\n\t" \
    "v_mfma_f32_16x16x32_bf16 v[44:47], v[184:187], v[176:179], v[44:47]\n\t" \
    "ds_read_b128 v[108:111], %[a] offset:6144\n\t" \
    "v_mfma_f32_16x16x32_bf16 v[48:51], v[192:195], v[176:179], v[48:51]\n\t" \
    "ds_read_b128 v[112:115], %[a] offset:7168\n\t" \
    "s_mov_b32 m0, %[pd]\n\t" \
    "s_cmp_lt_u32 %[ps], %[pe]\n\t" \
    "s_cselect_b64 exec, -1, 0\n\t" \
    "buffer_load_dword %[pv], %[rp], %[ps] offen sc1 lds\n\t" \
    "s_mov_b64 exec, -1\n\t" \
    "s_add_u32 %[ps], %[ps], %[pk]\n\t" \
    "s_add_u32 %[t], %[t], 1\n\t" \
    "s_cmp_lt_u32 %[t], %[nt]\n\t" \
    "s_cbranch_scc1 L_%=\n\t" \
    "s_waitcnt lgkmcnt(0)\n\t" \
    "s_nop 7\n\t" \
    : "=&{v[20:23]}"(acc[0][0][0][0]), "=&{v[24:27]}"(acc[0][0][0][1]), "=&{v[28:31]}"(acc[0][0][1][0]), "=&{v[32:35]}"(acc[0][0][1][1]), "=&{v[36:39]}"(acc[0][0][2][0]), "=&{v[40:43]}"(acc[0][0][2][1]), "=&{v[44:47]}"(acc[0][0][3][0]), "=&{v[48:51]}"(acc[0][0][3][1]), "=&{v[52:55]}"(acc[0][1][0][0]), "=&{v[56:59]}"(acc[0][1][0][1]), "=&{v[60:63]}"(acc[0][1][1][0]), "=&{v[64:67]}"(acc[0][1][1][1]), "=&{v[68:71]}"(acc[0][1][2][0]), "=&{v[72:75]}"(acc[0][1][2][1]), "=&{v[76:79]}"(acc[0][1][3][0]), "=&{v[80:83]}"(acc[0][1][3][1]), [a] "+v"(aoff), [b0] "+v"(boff0), [b1] "+v"(boff1), [c] "+s"(cur), [t] "+s"(t), [ps] "+s"(ps), [d] "=&s"(sd), [x] "=&s"(sx), [y] "=&s"(sy) \
    : [va0] "v"(voffA[0]), [va1] "v"(voffA[1]), [vb0] "v"(voffB[0]), [vb1] "v"(voffB[1]), [pv] "v"(pf.v), [ra] "s"(ra), [rb] "s"(rb), [rp] "s"(rp), [lw] "s"(ldsw), [nt] "s"(nt), [kb] "s"(kB), [ca] "s"(cA), [cb] "s"(cB), [cn] "s"(cN), [pd] "s"(pd), [pe] "s"(pe), [pk] "s"(pf.step) \
    : "memory", "scc", "v84", "v85", "v86", "v87", "v88", "v89", "v90", "v91", "v92", "v93", "v94", "v95", "v96", "v97", "v98", "v99", "v100", "v101", "v102", "v103", "v104", "v105", "v106", "v107", "v108", "v109", "v110", "v111", "v112", "v113", "v114", "v115", "v116", "v117", "v118", "v119", "v120", "v121", "v122", "v123", "v124", "v125", "v126", "v127", "v128", "v129", "v130", "v131", "v132", "v133", "v134", "v135", "v136", "v137", "v138", "v139", "v140", "v141", "v142", "v143", "v144", "v145", "v146", "v147", "v148", "v149", "v150", "v151", "v152", "v153", "v154", "v155", "v156", "v157", "v158", "v159", "v160", "v161", "v162", "v163", "v164", "v165", "v166", "v167", "v168", "v169", "v170", "v171", "v172", "v173", "v174", "v175", "v176", "v177", "v178", "v179", "v180", "v181", "v182", "v183", "v184", "v185", "v186", "v187", "v188", "v189", "v190", "v191", "v192", "v193", "v194", "v195", "v196", "v197", "v198", "v199", "v200", "v201", "v202", "v203", "v204", "v205", "v206", "v207", "v208", "v209", "v210", "v211")

#define H_LOOP2() \
  asm volatile( \
    "v_mov_b32 v20, 0\n\t" \
    "v_mov_b32 v21, 0\n\t" \
    "v_mov_b32 v22, 0\n\t" \
    "v_mov_b32 v23, 0\n\t" \
    "v_mov_b32 v24, 0\n\t" \
    "v_mov_b32 v25, 0\n\t" \
    "v_mov_b32 v26, 0\n\t" \
    "v_mov_b32 v27, 0\n\t" \
    "v_mov_b32 v28, 0\n\t" \
    "v_mov_b32 v29, 0\n\t" \
    "v_mov_b32 v30, 0\n\t" \
    "v_mov_b32 v31, 0\n\t" \
    "v_mov_b32 v32, 0\n\t" \
    "v_mov_b32 v33, 0\n\t" \
    "v_mov_b32 v34, 0\n\t" \
    "v_mov_b32 v35, 0\n\t" \
    "v_mov_b32 v36, 0\n\t" \
    "v_mov_b32 v37, 0\n\t" \
    "v_mov_b32 v38, 0\n\t" \
    "v_mov_b32 v39, 0\n\t" \
    "v_mov_b32 v40, 0\n\t" \
    "v_mov_b32 v41, 0\n\t" \
    "v_mov_b32 v42, 0\n\t" \
    "v_mov_b32 v43, 0\n\t" \
    "v_mov_b32 v44, 0\n\t" \
    "v_mov_b32 v45, 0\n\t" \
    "v_mov_b32 v46, 0\n\t" \
    "v_mov_b32 v47, 0\n\t" \
    "v_mov_b32 v48, 0\n\t" \
    "v_mov_b32 v49, 0\n\t" \
    "v_mov_b32 v50, 0\n\t" \
    "v_mov_b32 v51, 0\n\t" \
    "v_mov_b32 v52, 0\n\t" \
    "v_mov_b32 v53, 0\n\t" \
    "v_mov_b32 v54, 0\n\t" \
    "v_mov_b32 v55, 0\n\t" \
    "v_mov_b32 v56, 0\n\t" \
    "v_mov_b32 v57, 0\n\t" \
    "v_mov_b32 v58, 0\n\t" \
    "v_mov_b32 v59, 0\n\t" \
    "v_mov_b32 v60, 0\n\t" \
    "v_mov_b32 v61, 0\n\t" \
    "v_mov_b32 v62, 0\n\t" \
    "v_mov_b32 v63, 0\n\t" \
    "v_mov_b32 v64, 0\n\t" \
    "v_mov_b32 v65, 0\n\t" \
    "v_mov_b32 v66, 0\n\t" \
    "v_mov_b32 v67, 0\n\t" \
    "v_mov_b32 v68, 0\n\t" \
    "v_mov_b32 v69, 0\n\t" \
    "v_mov_b32 v70, 0\n\t" \
    "v_mov_b32 v71, 0\n\t" \
    "v_mov_b32 v72, 0\n\t" \
    "v_mov_b32 v73, 0\n\t" \
    "v_mov_b32 v74, 0\n\t" \
    "v_mov_b32 v75, 0\n\t" \
    "v_mov_b32 v76, 0\n\t" \
    "v_mov_b32 v77, 0\n\t" \
    "v_mov_b32 v78, 0\n\t" \
    "v_mov_b32 v79, 0\n\t" \
    "v_mov_b32 v80, 0\n\t" \
    "v_mov_b32 v81, 0\n\t" \
    "v_mov_b32 v82, 0\n\t" \
    "v_mov_b32 v83, 0\n\t" \
    "s_waitcnt vmcnt(12)\n\t" \
    "s_barrier\n\t" \
    "ds_read_b64_tr_b16 v[116:117], %[b0] offset:0\n\t" \
    "ds_read_b64_tr_b16 v[118:119], %[b0] offset:1024\n\t" \
    "ds_read_b64_tr_b16 v[124:125], %[b1] offset:0\n\t" \
    "ds_read_b64_tr_b16 v[126:127], %[b1] offset:1024\n\t" \
    "ds_read_b64_tr_b16 v[120:121], %[b0] offset:8192\n\t" \
    "ds_read_b64_tr_b16 v[122:123], %[b0] offset:9216\n\t" \
    "ds_read_b64_tr_b16 v[128:129], %[b1] offset:8192\n\t" \
    "ds_read_b64_tr_b16 v[130:131], %[b1] offset:9216\n\t" \
    "ds_read_b128 v[84:87], %[a] offset:0\n\t" \
    "ds_read_b128 v[88:91], %[a] offset:1024\n\t" \
    "ds_read_b128 v[92:95], %[a] offset:2048\n\t" \
    "ds_read_b128 v[96:99], %[a] offset:3072\n\t" \
    "ds_read_b128 v[100:103], %[a] offset:4096\n\t" \
    "ds_read_b128 v[104:107], %[a] offset:5120\n\t" \
    "ds_read_b128 v[108:111], %[a] offset:6144\n\t" \
    "ds_read_b128 v[112:115], %[a] offset:7168\n\t" \
    "ds_read_b64_tr_b16 v[132:133], %[b0] offset:16384\n\t" \
    "ds_read_b64_tr_b16 v[134:135], %[b0] offset:17408\n\t" \
    "ds_read_b64_tr_b16 v[140:141], %[b1] offset:16384\n\t" \
    "ds_read_b64_tr_b16 v[142:143], %[b1] offset:17408\n\t" \
    "ds_read_b64_tr_b16 v[136:137], %[b0] offset:24576\n\t" \
    "ds_read_b64_tr_b16 v[138:139], %[b0] offset:25600\n\t" \
    "ds_read_b64_tr_b16 v[144:145], %[b1] offset:24576\n\t" \
    "ds_read_b64_tr_b16 v[146:147], %[b1] offset:25600\n\t" \
    "L_%=:\n\t" \
    "s_add_u32 %[d], %[lw], %[c]\n\t" \
    "s_add_u32 %[y], %[c], 49152\n\t" \
    "s_cmp_eq_u32 %[y], 147456\n\t" \
    "s_cselect_b32 %[y], 0, %[y]\n\t" \
    "s_sub_u32 %[x], %[y], %[c]\n\t" \
    "s_mov_b32 %[c], %[y]\n\t" \
    "v_add_u32 %[a], %[x], %[a]\n\t" \
    "v_add_u32 %[b0], %[x], %[b0]\n\t" \
    "v_add_u32 %[b1], %[x], %[b1]\n\t" \
    "s_add_u32 %[x], %[t], 3\n\t" \
    "s_sub_u32 %[y], %[x], %[nt]\n\t" \
    "s_cmp_lt_u32 %[x], %[nt]\n\t" \
    "s_cselect_b32 %[x], %[x], %[y]\n\t" \
    "s_cselect_b32 m0, %[cb], %[cn]\n\t" \
    "s_mul_i32 %[y], %[x], %[kb]\n\t" \
    "s_add_u32 %[y], %[y], m0\n\t" \
    "s_lshl_b32 %[x], %[x], 11\n\t" \
    "s_add_u32 %[x], %[x], %[ca]\n\t" \
    "s_waitcnt vmcnt(6) lgkmcnt(0)\n\t" \
    "s_barrier\n\t" \
    "v_mfma_f32_16x16x32_bf16 v[20:23], v[116:119], v[84:87], v[20:23]\n\t" \
    "ds_read_b64_tr_b16 v[180:181], %[b0] offset:0\n\t" \
    "s_add_u32 m0, %[d], 0\n\t" \
    "v_mfma_f32_16x16x32_bf16 v[24:27], v[124:127], v[84:87], v[24:27]\n\t" \
    "buffer_load_dwordx4 %[vb0], %[rb], %[y] offen lds\n\t" \
    "v_mfma_f32_16x16x32_bf16 v[20:23], v[120:123], v[88:91], v[20:23]\n\t" \
    "ds_read_b64_tr_b16 v[182:183], %[b0] offset:1024\n\t" \
    "v_mfma_f32_16x16x32_bf16 v[24:27], v[128:131], v[88:91], v[24:27]\n\t" \
    "ds_read_b64_tr_b16 v[188:189], %[b1] offset:0\n\t" \
    "v_mfma_f32_16x16x32_bf16 v[28:31], v[116:119], v[92:95], v[28:31]\n\t" \
    "ds_read_b64_tr_b16 v[190:191], %[b1] offset:1024\n\t" \
    "v_mfma_f32_16x16x32_bf16 v[32:35], v[124:127], v[92:95], v[32:35]\n\t" \
    "ds_read_b64_tr_b16 v[184:185], %[b0] offset:8192\n\t" \
    "s_add_u32 m0, %[d], 8192\n\t" \
    "v_mfma_f32_16x16x32_bf16 v[28:31], v[120:123], v[96:99], v[28:31]\n\t" \
    "buffer_load_dwordx4 %[vb1], %[rb], %[y] offen lds\n\t" \
    "v_mfma_f32_16x16x32_bf16 v[32:35], v[128:131], v[96:99], v[32:35]\n\t" \
    "ds_read_b64_tr_b16 v[186:187], %[b0] offset:9216\n\t" \
    "v_mfma_f32_16x16x32_bf16 v[36:39], v[116:119], v[100:103], v[36:39]\n\t" \
    "ds_read_b64_tr_b16 v[192:193], %[b1] offset:8192\n\t" \
    "v_mfma_f32_16x16x32_bf16 v[40:43], v[124:127], v[100:103], v[40:43]\n\t" \
    "ds_read_b64_tr_b16 v[194:195], %[b1] offset:9216\n\t" \
    "v_mfma_f32_16x16x32_bf16 v[36:39], v[120:123], v[104:107], v[36:39]\n\t" \
    "ds_read_b128 v[148:151], %[a] offset:0\n\t" \
    "s_add_u32 m0, %[d], 32768\n\t" \
    "v_mfma_f32_16x16x32_bf16 v[40:43], v[128:131], v[104:107], v[40:43]\n\t" \
    "buffer_load_dwordx4 %[va0], %[ra], %[x] offen sc1 lds\n\t" \
    "v_mfma_f32_16x16x32_bf16 v[44:47], v[116:119], v[108:111], v[44:47]\n\t" \
    "ds_read_b128 v[152:155], %[a] offset:1024\n\t" \
    "v_mfma_f32_16x16x32_bf16 v[48:51], v[124:127], v[108:111], v[48:51]\n\t" \
    "ds_read_b128 v[156:159], %[a] offset:2048\n\t" \
    "v_mfma_f32_16x16x32_bf16 v[44:47], v[120:123], v[112:115], v[44:47]\n\t" \
    "ds_read_b128 v[160:163], %[a] offset:3072\n\t" \
    "v_mfma_f32_16x16x32_bf16 v[48:51], v[128:131], v[112:115], v[48:51]\n\t" \
    "ds_read_b128 v[164:167], %[a] offset:4096\n\t" \
    "s_add_u32 m0, %[d], 40960\n\t" \
    "v_mfma_f32_16x16x32_bf16 v[52:55], v[132:135], v[84:87], v[52:55]\n\t" \
    "buffer_load_dwordx4 %[va1], %[ra], %[x] offen sc1 lds\n\t" \
    "v_mfma_f32_16x16x32_bf16 v[56:59], v[140:143], v[84:87], v[56:59]\n\t" \
    "ds_read_b128 v[168:171], %[a] offset:5120\n\t" \
    "v_mfma_f32_16x16x32_bf16 v[52:55], v[136:139], v[88:91], v[52:55]\n\t" \
    "ds_read_b128 v[172:175], %[a] offset:6144\n\t" \
    "v_mfma_f32_16x16x32_bf16 v[56:59], v[144:147], v[88:91], v[56:59]\n\t" \
    "ds_read_b128 v[176:179], %[a] offset:7168\n\t" \
    "v_mfma_f32_16x16x32_bf16 v[60:63], v[132:135], v[92:95], v[60:63]\n\t" \
    "ds_read_b64_tr_b16 v[196:197], %[b0] offset:16384\n\t" \
    "s_add_u32 m0, %[d], 16128\n\t" \
    "v_mfma_f32_16x16x32_bf16 v[64:67], v[140:143], v[92:95], v[64:67]\n\t" \
    "buffer_load_dwordx4 %[vb0], %[rb], %[y] offen offset:256 lds\n\t" \
    "v_mfma_f32_16x16x32_bf16 v[60:63], v[136:139], v[96:99], v[60:63]\n\t" \
    "ds_read_b64_tr_b16 v[198:199], %[b0] offset:17408\n\t" \
    "v_mfma_f32_16x16x32_bf16 v[64:67], v[144:147], v[96:99], v[64:67]\n\t" \
    "ds_read_b64_tr_b16 v[204:205], %[b1] offset:16384\n\t" \
    "v_mfma_f32_16x16x32_bf16 v[68:71], v[132:135], v[100:103], v[68:71]\n\t" \
    "ds_read_b64_tr_b16 v[206:207], %[b1] offset:17408\n\t" \
    "v_mfma_f32_16x16x32_bf16 v[72:75], v[140:143], v[100:103], v[72:75]\n\t" \
    "ds_read_b64_tr_b16 v[200:201], %[b0] offset:24576\n\t" \
    "s_add_u32 m0, %[d], 24320\n\t" \
    "v_mfma_f32_16x16x32_bf16 v[68:71], v[136:139], v[104:107], v[68:71]\n\t" \
    "buffer_load_dwordx4 %[vb1], %[rb], %[y] offen offset:256 lds\n\t" \
    "v_mfma_f32_16x16x32_bf16 v[72:75], v[144:147], v[104:107], v[72:75]\n\t" \
    "ds_read_b64_tr_b16 v[202:203], %[b0] offset:25600\n\t" \
    "v_mfma_f32_16x16x32_bf16 v[76:79], v[132:135], v[108:111], v[76:79]\n\t" \
    "ds_read_b64_tr_b16 v[208:209], %[b1] offset:24576\n\t" \
    "v_mfma_f32_16x16x32_bf16 v[80:83], v[140:143], v[108:111], v[80:83]\n\t" \
    "ds_read_b64_tr_b16 v[210:211], %[b1] offset:25600\n\t" \
    "v_mfma_f32_16x16x32_bf16 v[76:79], v[136:139], v[112:115], v[76:79]\n\t" \
    "v_mfma_f32_16x16x32_bf16 v[80:83], v[144:147], v[112:115], v[80:83]\n\t" \
    "s_mov_b32 m0, %[pd]\n\t" \
    "s_cmp_lt_u32 %[ps], %[pe]\n\t" \
    "s_cselect_b64 exec, -1, 0\n\t" \
    "buffer_load_dword %[pv], %[rp], %[ps] offen sc1 lds\n\t" \
    "s_mov_b64 exec, -1\n\t" \
    "s_add_u32 %[ps], %[ps], %[pk]\n\t" \
    "s_add_u32 %[t], %[t], 1\n\t" \
    "s_add_u32 %[d], %[lw], %[c]\n\t" \
    "s_add_u32 %[y], %[c], 49152\n\t" \
    "s_cmp_eq_u32 %[y], 147456\n\t" \
    "s_cselect_b32 %[y], 0, %[y]\n\t" \
    "s_sub_u32 %[x], %[y], %[c]\n\t" \
    "s_mov_b32 %[c], %[y]\n\t" \
    "v_add_u32 %[a], %[x], %[a]\n\t" \
    "v_add_u32 %[b0], %[x], %[b0]\n\t" \
    "v_add_u32 %[b1], %[x], %[b1]\n\t" \
    "s_add_u32 %[x], %[t], 3\n\t" \
    "s_sub_u32 %[y], %[x], %[nt]\n\t" \
    "s_cmp_lt_u32 %[x], %[nt]\n\t" \
    "s_cselect_b32 %[x], %[x], %[y]\n\t" \
    "s_cselect_b32 m0, %[cb], %[cn]\n\t" \
    "s_mul_i32 %[y], %[x], %[kb]\n\t" \
    "s_add_u32 %[y], %[y], m0\n\t" \
    "s_lshl_b32 %[x], %[x], 11\n\t" \
    "s_add_u32 %[x], %[x], %[ca]\n\t" \
    "s_waitcnt vmcnt(6) lgkmcnt(0)\n\t" \
    "s_barrier\n\t" \
    "v_mfma_f32_16x16x32_bf16 v[20:23], v[180:183], v[148:151], v[20:23]\n\t" \
    "ds_read_b64_tr_b16 v[116:117], %[b0] offset:0\n\t" \
    "s_add_u32 m0, %[d], 0\n\t" \
    "v_mfma_f32_16x16x32_bf16 v[24:27], v[188:191], v[148:151], v[24:27]\n\t" \
    "buffer_load_dwordx4 %[vb0], %[rb], %[y] offen lds\n\t" \
    "v_mfma_f32_16x16x32_bf16 v[20:23], v[184:187], v[152:155], v[20:23]\n\t" \
    "ds_read_b64_tr_b16 v[118:119], %[b0] offset:1024\n\t" \
    "v_mfma_f32_16x16x32_bf16 v[24:27], v[192:195], v[152:155], v[24:27]\n\t" \
    "ds_read_b64_tr_b16 v[124:125], %[b1] offset:0\n\t" \
    "v_mfma_f32_16x16x32_bf16 v[28:31], v[180:183], v[156:159], v[28:31]\n\t" \
    "ds_read_b64_tr_b16 v[126:127], %[b1] offset:1024\n\t" \
    "v_mfma_f32_16x16x32_bf16 v[32:35], v[188:191], v[156:159], v[32:35]\n\t" \
    "ds_read_b64_tr_b16 v[120:121], %[b0] offset:8192\n\t" \
    "s_add_u32 m0, %[d], 8192\n\t" \
    "v_mfma_f32_16x16x32_bf16 v[28:31], v[184:187], v[160:163], v[28:31]\n\t" \
    "buffer_load_dwordx4 %[vb1], %[rb], %[y] offen lds\n\t" \
    "v_mfma_f32_16x16x32_bf16 v[32:35], v[192:195], v[160:163], v[32:35]\n\t" \
    "ds_read_b64_tr_b16 v[122:123], %[b0] offset:9216\n\t" \
    "v_mfma_f32_16x16x32_bf16 v[36:39], v[180:183], v[164:167], v[36:39]\n\t" \
    "ds_read_b64_tr_b16 v[128:129], %[b1] offset:8192\n\t" \
    "v_mfma_f32_16x16x32_bf16 v[40:43], v[188:191], v[164:167], v[40:43]\n\t" \
    "ds_read_b64_tr_b16 v[130:131], %[b1] offset:9216\n\t" \
    "v_mfma_f32_16x16x32_bf16 v[36:39], v[184:187], v[168:171], v[36:39]\n\t" \
    "ds_read_b128 v[84:87], %[a] offset:0\n\t" \
    "s_add_u32 m0, %[d], 32768\n\t" \
    "v_mfma_f32_16x16x32_bf16 v[40:43], v[192:195], v[168:171], v[40:43]\n\t" \
    "buffer_load_dwordx4 %[va0], %[ra], %[x] offen sc1 lds\n\t" \
    "v_mfma_f32_16x16x32_bf16 v[44:47], v[180:183], v[172:175], v[44:47]\n\t" \
    "ds_read_b128 v[88:91], %[a] offset:1024\n\t" \
    "v_mfma_f32_16x16x32_bf16 v[48:51], v[188:191], v[172:175], v[48:51]\n\t" \
    "ds_read_b128 v[92:95], %[a] offset:2048\n\t" \
    "v_mfma_f32_16x16x32_bf16 v[44:47], v[184:187], v[176:179], v[44:47]\n\t" \
    "ds_read_b128 v[96:99], %[a] offset:3072\n\t" \
    "v_mfma_f32_16x16x32_bf16 v[48:51], v[192:195], v[176:179], v[48:51]\n\t" \
    "ds_read_b128 v[100:103], %[a] offset:4096\n\t" \
    "s_add_u32 m0, %[d], 40960\n\t" \
    "v_mfma_f32_16x16x32_bf16 v[52:55], v[196:199], v[148:151], v[52:55]\n\t" \
    "buffer_load_dwordx4 %[va1], %[ra], %[x] offen sc1 lds\n\t" \
    "v_mfma_f32_16x16x32_bf16 v[56:59], v[204:207], v[148:151], v[56:59]\n\t" \
    "ds_read_b128 v[104:107], %[a] offset:5120\n\t" \
    "v_mfma_f32_16x16x32_bf16 v[52:55], v[200:203], v[152:155], v[52:55]\n\t" \
    "ds_read_b128 v[108:111], %[a] offset:6144\n\t" \
    "v_mfma_f32_16x16x32_bf16 v[56:59], v[208:211], v[152:155], v[56:59]\n\t" \
    "ds_read_b128 v[112:115], %[a] offset:7168\n\t" \
    "v_mfma_f32_16x16x32_bf16 v[60:63], v[196:199], v[156:159], v[60:63]\n\t" \
    "ds_read_b64_tr_b16 v[132:133], %[b0] offset:16384\n\t" \
    "s_add_u32 m0, %[d], 16128\n\t" \
    "v_mfma_f32_16x16x32_bf16 v[64:67], v[204:207], v[156:159], v[64:67]\n\t" \
    "buffer_load_dwordx4 %[vb0], %[rb], %[y] offen offset:256 lds\n\t" \
    "v_mfma_f32_16x16x32_bf16 v[60:63], v[200:203], v[160:163], v[60:63]\n\t" \
    "ds_read_b64_tr_b16 v[134:135], %[b0] offset:17408\n\t" \
    "v_mfma_f32_16x16x32_bf16 v[64:67], v[208:211], v[160:163], v[64:67]\n\t" \
    "ds_read_b64_tr_b16 v[140:141], %[b1] offset:16384\n\t" \
    "v_mfma_f32_16x16x32_bf16 v[68:71], v[196:199], v[164:167], v[68:71]\n\t" \
    "ds_read_b64_tr_b16 v[142:143], %[b1] offset:17408\n\t" \
    "v_mfma_f32_16x16x32_bf16 v[72:75], v[204:207], v[164:167], v[72:75]\n\t" \
    "ds_read_b64_tr_b16 v[136:137], %[b0] offset:24576\n\t" \
    "s_add_u32 m0, %[d], 24320\n\t" \
    "v_mfma_f32_16x16x32_bf16 v[68:71], v[200:203], v[168:171], v[68:71]\n\t" \
    "buffer_load_dwordx4 %[vb1], %[rb], %[y] offen offset:256 lds\n\t" \
    "v_mfma_f32_16x16x32_bf16 v[72:75], v[208:211], v[168:171], v[72:75]\n\t" \
    "ds_read_b64_tr_b16 v[138:139], %[b0] offset:25600\n\t" \
    "v_mfma_f32_16x16x32_bf16 v[76:79], v[196:199], v[172:175], v[76:79]\n\t" \
    "ds_read_b64_tr_b16 v[144:145], %[b1] offset:24576\n\t" \
    "v_mfma_f32_16x16x32_bf16 v[80:83], v[204:207], v[172:175], v[80:83]\n\t" \
    "ds_read_b64_tr_b16 v[146:147], %[b1] offset:25600\n\t" \
    "v_mfma_f32_16x16x32_bf16 v[76:79], v[200:203], v[176:179], v[76:79]\n\t" \
    "v_mfma_f32_16x16x32_bf16 v[80:83], v[208:211], v[176:179], v[80:83]\n\t" \
    "s_mov_b32 m0, %[pd]\n\t" \
    "s_cmp_lt_u32 %[ps], %[pe]\n\t" \
    "s_cselect_b64 exec, -1, 0\n\t" \
    "buffer_load_dword %[pv], %[rp], %[ps] offen sc1 lds\n\t" \
    "s_mov_b64 exec, -1\n\t" \
    "s_add_u32 %[ps], %[ps], %[pk]\n\t" \
    "s_add_u32 %[t], %[t], 1\n\t" \
    "s_cmp_lt_u32 %[t], %[nt]\n\t" \
    "s_cbranch_scc1 L_%=\n\t" \
    "s_waitcnt lgkmcnt(0)\n\t" \
    "s_nop 7\n\t" \
    : "=&{v[20:23]}"(acc[0][0][0][0]), "=&{v[24:27]}"(acc[0][0][0][1]), "=&{v[28:31]}"(acc[0][0][1][0]), "=&{v[32:35]}"(acc[0][0][1][1]), "=&{v[36:39]}"(acc[0][0][2][0]), "=&{v[40:43]}"(acc[0][0][2][1]), "=&{v[44:47]}"(acc[0][0][3][0]), "=&{v[48:51]}"(acc[0][0][3][1]), "=&{v[52:55]}"(acc[0][1][0][0]), "=&{v[56:59]}"(acc[0][1][0][1]), "=&{v[60:63]}"(acc[0][1][1][0]), "=&{v[64:67]}"(acc[0][1][1][1]), "=&{v[68:71]}"(acc[0][1][2][0]), "=&{v[72:75]}"(acc[0][1][2][1]), "=&{v[76:79]}"(acc[0][1][3][0]), "=&{v[80:83]}"(acc[0][1][3][1]), [a] "+v"(aoff), [b0] "+v"(boff0), [b1] "+v"(boff1), [c] "+s"(cur), [t] "+s"(t), [ps] "+s"(ps), [d] "=&s"(sd), [x] "=&s"(sx), [y] "=&s"(sy) \
    : [va0] "v"(voffA[0]), [va1] "v"(voffA[1]), [vb0] "v"(voffB[0]), [vb1] "v"(voffB[1]), [pv] "v"(pf.v), [ra] "s"(ra), [rb] "s"(rb), [rp] "s"(rp), [lw] "s"(ldsw), [nt] "s"(nt), [kb] "s"(kB), [ca] "s"(cA), [cb] "s"(cB), [cn] "s"(cN), [pd] "s"(pd), [pe] "s"(pe), [pk] "s"(pf.step) \
    : "memory", "scc", "v84", "v85", "v86", "v87", "v88", "v89", "v90", "v91", "v92", "v93", "v94", "v95", "v96", "v97", "v98", "v99", "v100", "v101", "v102", "v103", "v104", "v105", "v106", "v107", "v108", "v109", "v110", "v111", "v112", "v113", "v114", "v115", "v116", "v117", "v118", "v119", "v120", "v121", "v122", "v123", "v124", "v125", "v126", "v127", "v128", "v129", "v130", "v131", "v132", "v133", "v134", "v135", "v136", "v137", "v138", "v139", "v140", "v141", "v142", "v143", "v144", "v145", "v146", "v147", "v148", "v149", "v150", "v151", "v152", "v153", "v154", "v155", "v156", "v157", "v158", "v159", "v160", "v161", "v162", "v163", "v164", "v165", "v166", "v167", "v168", "v169", "v170", "v171", "v172", "v173", "v174", "v175", "v176", "v177", "v178", "v179", "v180", "v181", "v182", "v183", "v184", "v185", "v186", "v187", "v188", "v189", "v190", "v191", "v192", "v193", "v194", "v195", "v196", "v197", "v198", "v199", "v200", "v201", "v202", "v203", "v204", "v205", "v206", "v207", "v208", "v209", "v210", "v211")
#define AQ_PIN0() asm volatile("" : "+{v[28:35]}"(wr[0][0]), "+{v[36:43]}"(wr[0][1]))
#define AQ_PIN1() asm volatile("" : "+{v[44:51]}"(wr[1][0]), "+{v[52:59]}"(wr[1][1]))
#define AQ_PIN2() asm volatile("" : "+{v[60:67]}"(wr[2][0]), "+{v[68:75]}"(wr[2][1]))
#define AQ_PIN3() asm volatile("" : "+{v[76:83]}"(wr[3][0]), "+{v[84:91]}"(wr[3][1]))
#define AQ_PIN4() asm volatile("" : "+{v[92:99]}"(wr[4][0]), "+{v[100:107]}"(wr[4][1]))
#define AQ_PIN5() asm volatile("" : "+{v[108:115]}"(wr[5][0]), "+{v[116:123]}"(wr[5][1]))
#define AQ_PIN6() asm volatile("" : "+{v[124:131]}"(wr[6][0]), "+{v[132:139]}"(wr[6][1]))
#define AQ_PIN7() asm volatile("" : "+{v[140:147]}"(wr[7][0]), "+{v[148:155]}"(wr[7][1]))
#define AQ_PIN8() asm volatile("" : "+{v[156:163]}"(wr[8][0]), "+{v[164:171]}"(wr[8][1]))
#define AQ_PIN9() asm volatile("" : "+{v[172:179]}"(wr[9][0]), "+{v[180:187]}"(wr[9][1]))
#define AQ_PIN10() asm volatile("" : "+{v[188:195]}"(wr[10][0]), "+{v[196:203]}"(wr[10][1]))
#define AQ_PIN11() asm volatile("" : "+{v[204:211]}"(wr[11][0]), "+{v[212:219]}"(wr[11][1]))
#define AK_PIN0() asm volatile("" : "+{v[20:27]}"(wr[0][0]), "+{v[28:35]}"(wr[0][1]))
#define AK_PIN1() asm volatile("" : "+{v[36:43]}"(wr[1][0]), "+{v[44:51]}"(wr[1][1]))
#define AK_PIN2() asm volatile("" : "+{v[52:59]}"(wr[2][0]), "+{v[60:67]}"(wr[2][1]))
#define AK_PIN3() asm volatile("" : "+{v[68:75]}"(wr[3][0]), "+{v[76:83]}"(wr[3][1]))
#define AK_PIN4() asm volatile("" : "+{v[84:91]}"(wr[4][0]), "+{v[92:99]}"(wr[4][1]))
#define AK_PIN5() asm volatile("" : "+{v[100:107]}"(wr[5][0]), "+{v[108:115]}"(wr[5][1]))
#define AK_PIN6() asm volatile("" : "+{v[116:123]}"(wr[6][0]), "+{v[124:131]}"(wr[6][1]))
#define AK_PIN7() asm volatile("" : "+{v[132:139]}"(wr[7][0]), "+{v[140:147]}"(wr[7][1]))
#define AK_PIN8() asm volatile("" : "+{v[148:155]}"(wr[8][0]), "+{v[156:163]}"(wr[8][1]))
#define AK_PIN9() asm volatile("" : "+{v[164:171]}"(wr[9][0]), "+{v[172:179]}"(wr[9][1]))
#define AK_PIN10() asm volatile("" : "+{v[180:187]}"(wr[10][0]), "+{v[188:195]}"(wr[10][1]))
#define AK_PIN11() asm volatile("" : "+{v[196:203]}"(wr[11][0]), "+{v[204:211]}"(wr[11][1]))
#define V_PIN() asm volatile("" : "+{v[28:35]}"(wv[0]), "+{v[36:43]}"(wv[1]), "+{v[44:51]}"(wv[2]), "+{v[52:59]}"(wv[3]), "+{v[60:67]}"(wv[4]), "+{v[68:75]}"(wv[5]), "+{v[76:83]}"(wv[6]), "+{v[84:91]}"(wv[7]), "+{v[92:99]}"(wv[8]), "+{v[100:107]}"(wv[9]), "+{v[108:115]}"(wv[10]), "+{v[116:123]}"(wv[11]), "+{v[124:131]}"(wv[12]), "+{v[132:139]}"(wv[13]), "+{v[140:147]}"(wv[14]), "+{v[148:155]}"(wv[15]))
#define WOV_PIN0() asm volatile("" : "+{v[156:163]}"(wo[0][0][0]), "+{v[164:171]}"(wo[0][0][1]), "+{v[172:179]}"(wo[0][1][0]), "+{v[180:187]}"(wo[0][1][1]))
#define WOV_PIN1() asm volatile("" : "+{v[188:195]}"(wo[1][0][0]), "+{v[196:203]}"(wo[1][0][1]), "+{v[204:211]}"(wo[1][1][0]), "+{v[212:219]}"(wo[1][1][1]))
#define WOW_PIN0() asm volatile("" : "+{v[28:35]}"(wo[0][0][0]), "+{v[36:43]}"(wo[0][0][1]), "+{v[44:51]}"(wo[0][1][0]), "+{v[52:59]}"(wo[0][1][1]))
#define WOW_PIN1() asm volatile("" : "+{v[60:67]}"(wo[1][0][0]), "+{v[68:75]}"(wo[1][0][1]), "+{v[76:83]}"(wo[1][1][0]), "+{v[84:91]}"(wo[1][1][1]))
#define WOW_PIN2() asm volatile("" : "+{v[92:99]}"(wo[2][0][0]), "+{v[100:107]}"(wo[2][0][1]), "+{v[108:115]}"(wo[2][1][0]), "+{v[116:123]}"(wo[2][1][1]))
#define WOW_PIN3() asm volatile("" : "+{v[124:131]}"(wo[3][0][0]), "+{v[132:139]}"(wo[3][0][1]), "+{v[140:147]}"(wo[3][1][0]), "+{v[148:155]}"(wo[3][1][1]))
#define WOW_PIN4() asm volatile("" : "+{v[156:163]}"(wo[4][0][0]), "+{v[164:171]}"(wo[4][0][1]), "+{v[172:179]}"(wo[4][1][0]), "+{v[180:187]}"(wo[4][1][1]))
#define WOW_PIN5() asm volatile("" : "+{v[188:195]}"(wo[5][0][0]), "+{v[196:203]}"(wo[5][0][1]), "+{v[204:211]}"(wo[5][1][0]), "+{v[212:219]}"(wo[5][1][1]))
#define A_PIN(QW, i) do { switch (i) { case 0: if (QW) AQ_PIN0(); else AK_PIN0(); break; case 1: if (QW) AQ_PIN1(); else AK_PIN1(); break; case 2: if (QW) AQ_PIN2(); else AK_PIN2(); break; case 3: if (QW) AQ_PIN3(); else AK_PIN3(); break; case 4: if (QW) AQ_PIN4(); else AK_PIN4(); break; case 5: if (QW) AQ_PIN5(); else AK_PIN5(); break; case 6: if (QW) AQ_PIN6(); else AK_PIN6(); break; case 7: if (QW) AQ_PIN7(); else AK_PIN7(); break; case 8: if (QW) AQ_PIN8(); else AK_PIN8(); break; case 9: if (QW) AQ_PIN9(); else AK_PIN9(); break; case 10: if (QW) AQ_PIN10(); else AK_PIN10(); break; case 11: if (QW) AQ_PIN11(); else AK_PIN11(); break; } } while (0)
#define WO_PIN(VW, i) do { switch (i) { case 0: if (VW) WOV_PIN0(); else WOW_PIN0(); break; case 1: if (VW) WOV_PIN1(); else WOW_PIN1(); break; case 2: if (!VW) WOW_PIN2(); break; case 3: if (!VW) WOW_PIN3(); break; case 4: if (!VW) WOW_PIN4(); break; case 5: if (!VW) WOW_PIN5(); break; } } while (0)

__device__ __forceinline__ int lds_byte(int r, int c) {
  const int st = (r >> 4) * 2 + (c >> 5), rr = r & 15, cc = c & 31, ob = rr * 64 + cc * 2;
  return st * 1024 + (ob ^ (((ob >> 9) & 1) << 5));
}
#define PFNT ""
#define G_DvoffB dmaw
#define PFH 5
struct Pf { unsigned v, step; bool on, a; };
#define G_PF(t) do { if (pf.on && (t) < nt) asm volatile("s_mov_b32 m0, %3\n\ts_nop 0\n\tbuffer_load_dword %0, %1, %2 offen sc1 " PFNT "lds" \
    :: "v"(pf.v), "s"(pf.a ? ra : rb), "s"((t) * pf.step), "s"((unsigned)(size_t)lds + 148480) : "memory"); } while (0)

#define G_ST1(bufoff, r, so, voff, i) G_D##voff(r, (voff)[i], so, ldsw + (bufoff) + (i) * 8192)
#define G_DvoffA dmas
#define G_STAGE(bufoff, so, voff) do { G_ST1(bufoff, G_R##voff, so, voff, 0); G_ST1(bufoff, G_R##voff, so, voff, 1); } while (0)
#define G_RvoffA ra
#define G_RvoffB rb
#define G_WAIT_V(n) asm volatile("s_waitcnt vmcnt(%0)" :: "n"(n) : "memory")
__device__ __forceinline__ void gemm_h(LAS unsigned char* lds, rsrc_t ra, rsrc_t rb, unsigned cA, unsigned cB, int lda, int ldb, int nt,
                                       const Pf& pf, bool wide, unsigned cN, bool cont, unsigned cur, f32x4 (&acc)[1][2][4][2]) {
  const int wid = __builtin_amdgcn_readfirstlane(threadIdx.x >> 6), lane = opq(0), tid = wid * 64 + lane;
  const int wr = wid >> 2, wc = wid & 3, fr = lane & 15, fq = lane >> 4;
  unsigned voffA[2], voffB[2];
  #pragma unroll
  for (int i = 0; i < 2; ++i) {
    voffA[i] = (unsigned)(((wid >> 1) + 4 * i) * 16 * lda + (wid & 1) * 1024 + lane * 16);
    const int b = tid * 16 + i * 8192, r = b >> 8, pos = (b >> 4) & 15;
    const int c = pos ^ (((r & 3) << 1) | (((r >> 3) & 1) << 3));
    voffB[i] = (unsigned)(r * ldb + c * 16);
  }
  const unsigned kB = 64 * ldb;
  const unsigned ldsw = (unsigned)(size_t)lds + (unsigned)wid * 1024u;
  const int q = (lane & 15) >> 2, p = lane & 3;
  unsigned aoff = (unsigned)(size_t)lds + cur + 32768 + lds_byte(wr * 64 + fr, fq * 8);
  unsigned boff0 = (unsigned)(size_t)lds + cur + (8 * fq + q) * 256 + (((wc * 4 + (p >> 1)) ^ ((q << 1) | ((fq & 1) << 3))) << 4) + 8 * (p & 1), boff1 = boff0 ^ 32;
#define H_STAGE(so, t) do { G_STAGE(so, cB + (t) * kB, voffB); G_STAGE((so) + 32768, cA + (t) * 2048, voffA); if (wide) G_STAGE((so) + 16384, cB + (t) * kB + 256, voffB); } while (0)
  #pragma unroll
  for (int t = 3; t < PFH; ++t) G_PF(t);
  if (!cont) { H_STAGE(0, 0); H_STAGE(49152, 1); H_STAGE(98304, 2); }
  const rsrc_t rp = pf.a ? ra : rb;
  const unsigned pd = (unsigned)(size_t)lds + 148480, pe = pf.on ? nt * pf.step : 0u;
  unsigned t = 0, ps = PFH * pf.step, sd, sx, sy;
  if (wide) H_LOOP2(); else H_LOOP1();
}

__device__ __forceinline__ u32x4 epi_pack(const f32x4 a0, const f32x4 a1) {
  const unsigned x0 = pack2(a0[0], a0[1]), x1 = pack2(a0[2], a0[3]);
  const unsigned y0 = pack2(a1[0], a1[1]), y1 = pack2(a1[2], a1[3]);
  auto s0 = __builtin_amdgcn_permlane16_swap(x0, y0, false, false);
  auto s1 = __builtin_amdgcn_permlane16_swap(x1, y1, false, false);
  return (u32x4){s0[0], s1[0], s0[1], s1[1]};
}
__device__ __forceinline__ unsigned gelu2(unsigned w) {
  const f32x2 x = {lo16(w), hi16(w)};
  const float c0 = -2.f * 1.4426950408889634f * 0.7978845608028654f, c1 = c0 * 0.044715f;
  const f32x2 a = x * (x * x * c1 + c0);
  const f32x2 d = (f32x2){__builtin_amdgcn_exp2f(a[0]), __builtin_amdgcn_exp2f(a[1])} + 1.f;
  const f32x2 y = x * (f32x2){__builtin_amdgcn_rcpf(d[0]), __builtin_amdgcn_rcpf(d[1])};
  return pack2(y[0], y[1]);
}
__device__ __forceinline__ float silu_f(float b) { return b * __builtin_amdgcn_rcpf(1.f + __expf(-b)); }

__device__ __forceinline__ unsigned blk_in(int r, int c) { const int ob = r * 64 + c * 2; return ob ^ (((ob >> 9) & 1) << 5); }
__device__ __forceinline__ unsigned xn_blk(int rb, int cb) { return (unsigned)(((rb >> 1) * 32 + cb) * 2 + (rb & 1)) * 1024u; }

template <int EPI>
__device__ __forceinline__ void epi_rows(const f32x4 (&acc)[1][2][4][2], rsrc_t rs, unsigned obase, int ldc, int pm2, int pn2, int sh, bool wide, int wr, int wc, int ln) {
  const int fq = ln >> 4, cb = 16 * (fq & 1) + 8 * (fq >> 1), fr = (ln & 15) + sh;
  const unsigned lo = (unsigned)((fr >> 4) * 16 * ldc) + blk_in(fr & 15, cb);
  const unsigned ob = (unsigned)(pm2 * 128 + 64 * wr) * (unsigned)ldc + (unsigned)(pn2 * 4 + wc) * 1024u + lo;
  u32x4 r[8];
  if (EPI >= 2) {
    #pragma unroll
    for (int i = 0; i < 8; ++i)
      r[i] = ld16(rs, EPI == 2 ? WS_XN + xn_blk(pm2 * 8 + 4 * wr + (i & 3), pn2 * 4 + 4 * (i >> 2) + wc) + lo : WS_X1 + ob + (i & 3) * 32768 + (i >> 2) * 4096);
  }
  #pragma unroll
  for (int i = 0; i < 8; ++i) {
    if (i == 4 && !wide) break;
    u32x4 w = epi_pack(acc[0][i >> 2][i & 3][0], acc[0][i >> 2][i & 3][1]);
    #pragma unroll
    for (int k = 0; k < 4; ++k) {
      w[k] = EPI < 2 ? gelu2(w[k]) : pack2(lo16(r[i][k]) + lo16(w[k]), hi16(r[i][k]) + hi16(w[k]));
      if (EPI == 3) w[k] = pack2(silu_f(lo16(w[k])), silu_f(hi16(w[k])));
    }
    st16(rs, obase + ob + (unsigned)(16 * (i & 3) * ldc) + (i >> 2) * 4096, w);
  }
}

__device__ __forceinline__ float row_sum16(float v) {
  v += __uint_as_float(__builtin_amdgcn_update_dpp(0, (int)__float_as_uint(v), 0x128, 0xf, 0xf, false));
  v += __uint_as_float(__builtin_amdgcn_update_dpp(0, (int)__float_as_uint(v), 0x124, 0xf, 0xf, false));
  v += __uint_as_float(__builtin_amdgcn_update_dpp(0, (int)__float_as_uint(v), 0x122, 0xf, 0xf, false));
  v += __uint_as_float(__builtin_amdgcn_update_dpp(0, (int)__float_as_uint(v), 0x121, 0xf, 0xf, false));
  return v;
}
__device__ __forceinline__ void ln_rows(u32x4 (&a)[8]) {
  f32x2 v[32];
  #pragma unroll
  for (int i = 0; i < 32; ++i) v[i] = (f32x2){lo16(a[i >> 2][i & 3]), hi16(a[i >> 2][i & 3])};
  f32x2 s[4] = {v[0], v[1], v[2], v[3]};
  #pragma unroll
  for (int i = 4; i < 32; ++i) s[i & 3] += v[i];
  s[0] = (s[0] + s[1]) + (s[2] + s[3]);
  const float mean = row_sum16(s[0][0] + s[0][1]) * (1.f / DM);
  #pragma unroll
  for (int i = 0; i < 32; ++i) { const f32x2 d = v[i] - mean; s[i & 3] = i < 4 ? d * d : s[i & 3] + d * d; }
  s[0] = (s[0] + s[1]) + (s[2] + s[3]);
  const float var = row_sum16(s[0][0] + s[0][1]) * (1.f / DM);
  const float mb = rb1(mean), vb = rb1(var);
  const float rden = __builtin_amdgcn_rcpf(rb1(__builtin_amdgcn_sqrtf(rb1(vb + 1e-5f))));
  #pragma unroll
  for (int i = 0; i < 32; ++i) {
    const f32x2 d = v[i] - mb;
    const unsigned w = pack2(d[0], d[1]);
    const f32x2 y = (f32x2){lo16(w), hi16(w)} * rden;
    a[i >> 2][i & 3] = pack2(y[0], y[1]);
  }
}

template <int N>
__device__ __forceinline__ void fin_sum(rsrc_t rs, unsigned po, u32x4* v) {
  u32x4 pv[N][8];
  #pragma unroll
  for (int i = 0; i < 8 * N; ++i) pv[i >> 3][i & 7] = ld16(rs, po + (i >> 3) * 16384 + PT_AT((i >> 1) & 3) + (i & 1) * 1024);
#define PF4(i) __builtin_bit_cast(f32x4, pv[c][i])
  #pragma unroll
  for (int c = 0; c < N; ++c) v[c] = epi_pack((PF4(0) + PF4(2)) + (PF4(4) + PF4(6)), (PF4(1) + PF4(3)) + (PF4(5) + PF4(7)));
}
__device__ __forceinline__ void fin_out(const Ptrs& P, unsigned eo, u32x4 v, u32x4 gv, u32x4 xv) {
  #pragma unroll
  for (int k = 0; k < 4; ++k) {
    v[k] = pack2(lo16(v[k]) * lo16(gv[k]), hi16(v[k]) * hi16(gv[k]));
    v[k] = pack2(lo16(xv[k]) + lo16(v[k]), hi16(xv[k]) + hi16(v[k]));
  }
  *(u32x4*)((char*)P.out + eo) = v;
}

__device__ __forceinline__ void mlp_role(LAS unsigned char* lds, const Ptrs& P, int hf, int slot, int wid) {
  const int wr = wid >> 2, wc = wid & 3;
  const int p2 = hf * 8 + (slot >> 2), pn = slot & 3;
  char* ws = P.ws;
  const rsrc_t rs = WS_RSRC(P);
  const u16* const p1 = P.w1; const u16* const p2w = P.w2; const u16* const p3 = P.w3; const u16* const p4 = P.w4;
#define M_DONE(c) do { VMWAIT(); BARRIER(); if (wid == 0) cnt_add(ws, c, opq(0)); } while (0)
  #pragma unroll 1
  for (int code = 0; code < 7; ++code) {
    int ln = opq(0);
    if (wid == 0) {
      if (code == 0) flag_wait(P, F_LN1(p2 * 4 + (ln & 3)), 0, ln);
      else if (code < 3 || code == 6) {
        const unsigned co = CNT_OFF + (code == 1 ? C_W1(min(max(p2 - 1 + min(ln, 2), 0), 15)) : code == 2 ? C_LN(p2) : C_W3(p2)) * 128;
        for (unsigned spins = 0; __any(ld4(rs, co) < 4u);) {
          __builtin_amdgcn_s_sleep(2);
          if (spin_fail(spins, P, ln)) break;
        }
      }
    }
    if (code < 3 || code == 6) BARRIER();
    TLW(3 * code);
    const bool w3 = code >= 2 && code < 6, wide = code != 5;
    const int lda = code == 6 ? 7168 : 2048, ldb = w3 ? 7168 : 2048, c2 = w3 ? (code - 2) * 8 + (wide ? 2 * pn : pn) : 2 * pn;
    const rsrc_t rb = __builtin_amdgcn_make_buffer_rsrc((void*)(code == 0 ? p1 : code == 1 ? p2w : w3 ? p3 : p4), 0, code < 2 ? DM * DM * 2 : DM * FF * 2, RSRC_W3);
    const unsigned cA = (code == 0 ? WS_XN2 : code == 1 ? WS_H1 : w3 ? WS_X1N : WS_H3) + p2 * 128 * lda, cB = c2 * 256;
    Pf pf;
    pf.on = wc == 0 && (!wr || code < 3 || code == 6); pf.a = wr;
    ln &= 31;
    pf.v = wr ? cA + (pn * 2 + (ln >> 4)) * 16 * lda + (ln & 15) * 128 : cB + ((slot >> 2) * 8 + (ln >> 2)) * ldb + (ln & (wide ? 3 : 1)) * 128;
    pf.step = wr ? 2048 : 64 * ldb;
    f32x4 acq[1][2][4][2];
    gemm_h(lds, rs, rb, cA, cB, lda, ldb, code == 6 ? 56 : 16, pf, wide, code == 4 ? (24 + pn) * 256 : w3 && wide ? cB + 2048 : cB, w3 && code > 2, code == 3 || code == 4 ? (code - 2) * 49152 : 0, acq);
    TLW(3 * code + 1);
    ln = opq(0);
    if (code == 1) {
      epi_rows<2>(acq, rs, WS_X1, 2048, p2, c2, 0, true, wr, wc, ln);
      M_DONE(C_W2(p2));
      if (wid == 0) cnt_wait(P, C_W2(p2), 4, ln);
      BARRIER();
      const unsigned ro = (unsigned)(p2 * 8 + pn * 2 + wr) * 32768u + ((ln >> 2) & 3) * 1024 + blk_in(wc * 4 + (ln >> 4), (ln & 3) * 8);
      u32x4 v[8];
      #pragma unroll
      for (int i = 0; i < 8; ++i) v[i] = ld16(rs, WS_X1 + ro + i * 4096);
      ln_rows(v);
      #pragma unroll
      for (int i = 0; i < 8; ++i) st16(rs, WS_X1N + ro + i * 4096, v[i]);
      M_DONE(C_LN(p2));
    } else if (code == 6) {
      epi_rows<3>(acq, rs, WS_G, 2048, p2, c2, 0, true, wr, wc, ln);
      M_DONE(C_W4(p2));
    } else {
      VMWAIT();
      if (code == 0 && wid < 4) touch(p2w, DM * DM * 2, ((hf * 32 + slot) * 256 + wid * 64 + ln) * 128, lds);
      epi_rows<1>(acq, rs, code ? WS_H3 : WS_H1, ldb, p2, c2, code ? 0 : (pn & 1) ? -1 - (pn >> 1) : 1 + (pn >> 1), wide, wr, wc, ln);
      if (code == 0 && wid == 0) setup_wait(P, ln);
      if (code == 0 || code == 5) M_DONE(code ? C_W3(p2) : C_W1(p2));
    }
    TLW(3 * code + 2);
  }
}

#define FINE 52
__device__ __forceinline__ void fin_role(LAS unsigned char* lds, const Ptrs& P, int wid) {
  const int wr = wid >> 2, wc = wid & 3;
  const rsrc_t rs = WS_RSRC(P);
  volatile LAS int* tk = (volatile LAS int*)(lds + 147456);
  unsigned tks = 0;
#define POP() do { if (wid == 0 && opq(0) == 0) tks = __hip_atomic_fetch_add((gu32*)(P.ws + CNT_OFF + C_HEAD * 128), 1u, RLX_AGENT); } while (0)
  POP();
  for (int round = 0;; ++round) {
    if (wid == 0 && opq(0) == 0) tk[round & 1] = tks;
    LBARRIER();
    const int u = __builtin_amdgcn_readfirstlane(tk[round & 1]);
    if (u >= 2 * FINE + 8 * (XS3 - FINE)) break;
    const bool big = u < 2 * FINE;
    const int sg = big ? u >> 1 : FINE + ((u - 2 * FINE) >> 3), cc0 = big ? (u & 1) * 4 : (u - 2 * FINE) & 7;
    const int ln = opq(0), fq = ln >> 4, cb = 16 * (fq & 1) + 8 * (fq >> 1);
    const unsigned eo = (unsigned)(sg * 32 + wr * 16 + (ln & 15)) * 2048u + (unsigned)(cc0 * 128 + wc * 32 + cb) * 2u;
    const unsigned go = WS_G + (unsigned)(sg * 2 + wr) * 32768u + (cc0 * 4 + wc) * 1024 + blk_in(ln & 15, cb);
    const unsigned po = (unsigned)sg * 262144u + cc0 * 16384 + wc * 4096 + wr * 2048 + ln * 16;
    const int xg = (sg >= XS1) + (sg >= XS2), want = sg + 1 - (xg == 2 ? XS2 : xg * XS1);
    u32x4 v[4], gv[4], xv[4];
    if (big) {
      if (wid == 2) flag_wait(P, F_PRG(5 + xg, ln & 31), want, ln);
      BARRIER();
      fin_sum<2>(rs, po, v);
      asm volatile("" : "+v"(v[0]), "+v"(v[1]) :: "memory");
      #pragma unroll
      for (int c = 0; c < 4; ++c) xv[c] = __builtin_nontemporal_load((const u32x4*)((const char*)P.x + eo + c * 256));
      fin_sum<2>(rs, po + 32768, v + 2);
      if (wid == 1) cnt_wait(P, C_W4(sg >> 2), 4, ln);
      BARRIER();
      #pragma unroll
      for (int c = 0; c < 4; ++c) gv[c] = ld16(rs, go + c * 4096);
      #pragma unroll
      for (int c = 0; c < 4; ++c) fin_out(P, eo + c * 256, v[c], gv[c], xv[c]);
    } else {
      if (wid == 1) cnt_wait(P, C_W4(sg >> 2), 4, ln);
      BARRIER();
      gv[0] = ld16(rs, go);
      xv[0] = __builtin_nontemporal_load((const u32x4*)((const char*)P.x + eo));
      if (wid == 2) flag_wait<3>(P, F_PRG(5 + xg, (ln & 3) * 8 + cc0), want, ln);
      BARRIER();
      fin_sum<1>(rs, po, v);
      fin_out(P, eo, v[0], gv[0], xv[0]);
    }
    POP();
  }
}

__device__ __forceinline__ u32x4 ldsv(LAS unsigned char* p) { return *(volatile LAS u32x4*)p; }
#define DMA_A dman
#define DMA_V dman
#define DMA_O dmaw
#define TOUCH 0
#define TAUX 0
#define POISON4 ((u32x4){~0u, ~0u, ~0u, ~0u})
#define DLYM 0
#define DLYA 8
#define DLYK (DLYA + 8)
#define DLYO 8
#define FLA 0
#define FLB 0
#define CADM 1
#define CADO 1

#define RTC(id)
#define DCA 3
#define DCB 12
#define DCU 8
#define DL1 -7
#define DL2 6
#define DLB 15
#define ADL 0
#define SN0 150
#define MAP 0
#define HELP 256
#define BORD 0
#define QT 0
__device__ __forceinline__ void track(int& est, int d, int gain) { const int e = d - (est >> 4); est += e > 44 ? DCB * 2 : e * (e > 0 ? max(gain, DCU) : gain); }
__device__ __forceinline__ unsigned rfl(unsigned v) { unsigned r; asm volatile("s_nop 0\n\tv_readfirstlane_b32 %0, %1\n\ts_nop 4" : "=s"(r) : "v"(v)); return r; }
__device__ __forceinline__ void plan_wait(rsrc_t rs, unsigned fo, int s, int& lim, unsigned& tm, unsigned& d) {
  while ((d & 255u) != (unsigned)s && (lim -= DEC1) > 0) {
    __builtin_amdgcn_s_sleep(1);
    const u32x2 v = __builtin_amdgcn_raw_buffer_load_b64(rs, 0, fo, AUX_SC1);
    tm = rfl(v.x); d = rfl(v.y);
  }
  const unsigned now = (unsigned)__builtin_amdgcn_s_memrealtime() * 4u;
  tm = now + min((int)(tm - now), 2000);
  d = min(d >> 8, 1000u);
}
#define SPIN_BUDGET (1 << 21)

__device__ __forceinline__ unsigned rec_go(LAS unsigned char* w, const Ptrs& P, u32x2 f, int wid, int lane) {
  if (wid >= 6) {
    if (__any(f.y != P.tag)) f.x = flag_wait(P, wid == 6 ? F_LN0(lane) : F_CAL(lane), 0, lane);
    if (wid == 7) { const unsigned m = near_half(f.x); if (lane == 0) *(volatile LAS unsigned*)w = m; }
  }
  LBARRIER();
  return __builtin_amdgcn_readfirstlane(*(volatile LAS unsigned*)w);
}

#define SLOT(i) ((i) * 16384)
#define LA_MEM SLOT(8)
#define LA_Q SLOT(9)
#define LA_K (SLOT(9) + 2048)
#define LA_P (SLOT(9) + 4096)
#define LA_KS (SLOT(9) + 6144)
#define LA_VM LA_MEM
#define LA_KX (SLOT(9) + 8192)

template <bool QW>
__device__ __forceinline__ void a_wave(LAS unsigned char* lds, Ptrs& P, int h, int wid) {
  char* ws = P.ws;
  const int lane = opq(0), tid = wid * 64 + lane;
  const int j = wid & 3, g = lane >> 4, q4 = (lane & 15) >> 2, p4 = lane & 3;
  const rsrc_t rs = WS_RSRC(P);
  const rsrc_t rq = __builtin_amdgcn_make_buffer_rsrc((void*)P.Wq, 0, HD * DM * 2, RSRC_W3);
  const rsrc_t rk = __builtin_amdgcn_make_buffer_rsrc((void*)P.Wk, 0, HD * DM * 2, RSRC_W3);
  const unsigned lbase = (unsigned)(size_t)lds;
  int lim = SPIN_BUDGET;
  u32x8 wr[12][2];
  const int trow = 16 * (g >> 1) + 4 * (g & 1) + 8 * (q4 >> 1) + (q4 & 1);
  const int tsw = ((q4 & 1) | ((g & 1) << 1) | ((q4 >> 1) << 2)) << 1;
  const int toff = trow * 256 + (((4 * j + (p4 >> 1)) ^ tsw) << 4) + 8 * (p4 & 1);
  {
    unsigned dv[2];
    #pragma unroll
    for (int x = 0; x < 2; ++x) {
      const int row = 8 * wid + 4 * x + (lane >> 4);
      const int sw = ((row & 1) | (((row >> 2) & 1) << 1) | (((row >> 3) & 1) << 2)) << 1;
      dv[x] = (unsigned)(row * (HD * 2) + h * 256 + (((lane & 15) ^ sw) << 4));
    }
#define A_GRP(n) do { _Pragma("unroll") for (int i = 0; i < 8; ++i) \
      DMA_A(((n) & 1) ? rk : rq, dv[i & 1], (4 * (((n) & 1) ? ((n) >> 1) ^ ((n) >> 2) : (((n) >> 1) + 1) & 3) + (i >> 1)) * 64 * HD * 2, lbase + SLOT(4 * ((n) & 1) + (i >> 1)) + wid * 2048 + (i & 1) * 1024); } while (0)
    A_GRP(0); A_GRP(1);
    #pragma unroll
    for (int n = 0; n < 6; ++n) {
      VMW(8);
      BARRIER();
      if ((n & 1) == (QW ? 0 : 1)) {
        #pragma unroll
        for (int b = 0; b < 4; ++b) {
          LAS unsigned char* t = lds + SLOT(QW ? 0 : 4) + ((b ^ j) << 14);
          wr[4 * (n >> 1) + b][0] = rd_frag<8192, 512>(t + toff);
          wr[4 * (n >> 1) + b][1] = rd_frag<8192, 512>(t + (toff ^ 32));
        }
        #pragma unroll
        for (int b = 0; b < 4; ++b) A_PIN(QW, 4 * (n >> 1) + b);
      }
      LBARRIER();
      A_GRP(n + 2);
    }
  }
  const unsigned cb = comm_base(ws);
  u32x2 f0 = {0u, 0u};
  #pragma unroll
  for (int rd = 0; rd < 2; ++rd) {
    if (rd == 0) VMW(8);
    else {
      if (wid >= 6) f0 = ld8(rs, FLG_OFF + (wid == 6 ? F_LN0(lane) : F_CAL(lane)) * 128);
      const unsigned fv = wid == 3 ? FLG_OFF + F_PRG(min((lane >> 1) & 3, 2), h) * 128 + (lane & 1) * 4 : WS_QF + j * (16u << 20) + h * 8192 + lane * 128;
      if (QW) asm volatile("s_mov_b32 m0, %2\n\ts_nop 0\n\tbuffer_load_dword %0, %1, 0 offen sc1 lds" :: "v"(fv), "s"(rs), "s"(lbase + LA_P + (wid == 3 ? 640 : 1024)) : "memory");
      get_tag(P);
      VMWAIT();
    }
    BARRIER();
    u32x8 f[2][2];
    #pragma unroll
    for (int i = 0; i < 2; ++i) {
      f[i][0] = rd_frag<8192, 512>(lds + SLOT((QW ? 0 : 2) + 4 * rd + i) + toff);
      f[i][1] = rd_frag<8192, 512>(lds + SLOT((QW ? 0 : 2) + 4 * rd + i) + (toff ^ 32));
    }
    asm volatile("" : "+v"(f[0][0]), "+v"(f[0][1]), "+v"(f[1][0]), "+v"(f[1][1]));
    LBARRIER();
    #pragma unroll
    for (int i = 0; i < 2; ++i)
      #pragma unroll
      for (int t = 0; t < 2; ++t) {
        LAS unsigned char* p = lds + SLOT((QW ? 0 : 2) + 4 * rd + i) + j * 4096 + t * 2048 + lane * 16;
        *(LAS u32x4*)p = (u32x4){f[i][t][0], f[i][t][1], f[i][t][2], f[i][t][3]};
        *(LAS u32x4*)(p + 1024) = (u32x4){f[i][t][4], f[i][t][5], f[i][t][6], f[i][t][7]};
      }
  }
  const unsigned mask = rec_go(lds + LA_P + 128, P, f0, wid, lane);
  const int idx = (lane & 1) ? 0xEEEE : 0x4444;
  const unsigned ab_a = lbase + LA_MEM + lane * 16, am_a = ab_a + j * 1024;
  const unsigned t0_a = lbase + SLOT((QW ? 0 : 4) + j) + j * 4096 + lane * 16;
  const unsigned iw_a = lbase + (QW ? LA_Q : LA_K) + j * 512 + q4 * 128 + g * 32 + p4 * 4;
  const unsigned sw_a = lbase + LA_KS + (j >> 1) * 1024 + ((j & 1) * 2 + (q4 >> 1)) * 256 + (4 * g + (p4 & 1)) * 16 + ((q4 & 1) * 2 + (p4 >> 1)) * 4;
  const unsigned qr_a = lbase + LA_Q + g * 128 + (lane & 7) * 16;
  const unsigned kr_a = lbase + LA_K + g * 128 + (2 * q4 + (lane & 1)) * 16;
  const unsigned fr_a = lbase + LA_P + p4 * 4;
  if (tid < 12) *(LAS unsigned*)(lds + LA_P + tid * 4) = 0u;
  const unsigned vo_v = VB_AT(h) + j * 512 + lane * 4, md = lbase + LA_VM - VB_AT(h);
  const unsigned oo_v = OB_AT((lane >> 2) & 1, h) + (j * 16 + (g >> 1) * 8 + p4 * 2 + (g & 1)) * 16;
  const unsigned long long onej = 1ull << j;
  const unsigned lkx = lbase + LA_KX + j * 2048;
  u32x8 oxr = {};
  const unsigned xo_v = (lane >> 5) * 2048 + (lane & 31) * 16, vl_a = lbase + LA_VM + (j & 1) * 1024 + lane * 4;
  const int dq = 16 * (j >> 1) + (lane & 15) - 4 * g;
  const unsigned fb = step_base(cb, mask, 0);
  unsigned tgt = (unsigned)__builtin_amdgcn_s_memrealtime() * 4u;
  unsigned long long tp = 0;
  unsigned pt = 0, pd = 0;
  const unsigned long long fmask = __builtin_amdgcn_readfirstlane((16u >> wid) & 1u);
  int known = 0;
  __builtin_amdgcn_s_setprio(QW ? 2 : 1);
  LBARRIER();
  report(P, wid);
  TL(72);
  for (int s = 0; s < NSEG; ++s) {
    const unsigned sb = step_base(cb, mask, s);
    const unsigned sbm = sb - (lbase + LA_MEM);
    const unsigned fo = (unsigned)(s * NH + h) * 8192u + j * 2048 - (lbase + LA_MEM);
    GSDECL;
    RTC(h);
    lim = __builtin_amdgcn_readfirstlane(lim);
    if (QW) {
      if (wid == 3 && s > 0) {
        if (opq(0) == 0) __builtin_amdgcn_raw_buffer_store_b32((unsigned)tp * 4u - tgt, rs, 0, fb + FL_O(h), AUX_SC1);
        plan_wait(rs, fb + FL_T(s), s, lim, pt, pd);
        tgt = pt;
      }
      if (s >= known) {
        const u32x2 v = *(volatile LAS u32x2*)(lds + LA_P + 640 + (opq(0) & 3) * 8);
        if (!__any(v.y != P.tag)) known = rfl(min(min(__builtin_amdgcn_readlane(v.x, 0), __builtin_amdgcn_readlane(v.x, 1)), __builtin_amdgcn_readlane(v.x, 2)));
      }
      kv_wait(P, s, h, known, opq(0));
      const unsigned kxo = WS_KF + fo, vxo = WS_VF + fo;
      if (wid < 3) for (int i = 0; i < ADL; ++i) __builtin_amdgcn_s_sleep(1);
      {
      dmas(rs, ab_a, kxo, lkx);
      dmas(rs, ab_a, kxo + 1024, lkx + 1024);
      dmas(rs, ab_a, WS_QF + fo, lbase + LA_P + 1024);
      dmas(rs, ab_a, WS_QF + fo + 1024, lbase + LA_P + 1024);
      }
      if (wid == 2 && s + 1 >= known) {
        const int ln = opq(0);
        const unsigned fv = FLG_OFF + F_PRG(min((ln >> 1) & 3, 2), h) * 128 + (ln & 1) * 4;
        asm volatile("s_mov_b32 m0, %2\n\ts_nop 0\n\tbuffer_load_dword %0, %1, 0 offen sc1 lds" :: "v"(fv), "s"(rs), "s"(lbase + LA_P + 640) : "memory");
      }
      unsigned rg = 0x3c000000u;
      A_MAIN_Q();
      TL(s);
      GSFLUSH(0, 24 + wid); GSFLUSH(1, 4 + wid); GSFLUSH(2, 12 + j); GSFLUSH(3, 16 + j); GSFLUSH(4, 20 + j);
      if (s + PD < NSEG && !(opq(0) & 8)) __builtin_amdgcn_raw_buffer_store_b128(poison4(), rs, oo_v, step_base(cb, mask, s + PD), AUX_SC1);
    } else {
      const unsigned qxo = WS_QF + fo - (j & 1) * 2048, oxl = h * 8192 + (j & 1) * 4096 + (j >> 1) * 1024;
      const unsigned oxo = (s == 0 || s == NSEG - 1 ? WS_DUM : WS_OX + (unsigned)(s - 1) * 262144u) + oxl;
      A_MAIN_K();
      if (s >= NSEG - 2) {
        __builtin_amdgcn_raw_buffer_store_b128((u32x4){oxr[0], oxr[1], oxr[2], oxr[3]}, rs, xo_v, WS_OX + (unsigned)s * 262144u + oxl, AUX_SC1);
        __builtin_amdgcn_raw_buffer_store_b128((u32x4){oxr[4], oxr[5], oxr[6], oxr[7]}, rs, xo_v + 512, WS_OX + (unsigned)s * 262144u + oxl, AUX_SC1);
      }
      if (wid == 7) GSFLUSH(0, 28);
      GSFLUSH(1, 4 + wid);
    }
  }
  VMWAIT();
  BARRIER();
  if (wid == 4) flag_set(P, F_PRG(4, h), NSEG, opq(0));
}

#define TSL 4
#define LB_MEM 0
#define LB_RED 16384
template <bool VW>
__device__ __forceinline__ void b_wave(LAS unsigned char* lds, Ptrs& P, int r, int c, int wid, int bs, int xg) {
  constexpr int NHD = VW ? 2 : 6;
  char* ws = P.ws;
  const int lane = opq(0), tid = wid * 64 + lane;
  const int j = wid & 3, g = lane >> 4, q4 = (lane & 15) >> 2, p4 = lane & 3;
  const int b = r * 32 + c;
  const rsrc_t rs = WS_RSRC(P);
  const rsrc_t rv = __builtin_amdgcn_make_buffer_rsrc((void*)P.Wv, 0, HD * DM * 2, RSRC_W3);
  const rsrc_t ro = __builtin_amdgcn_make_buffer_rsrc((void*)P.Wo, 0, HD * DM * 2, RSRC_W3);
  const unsigned lbase = (unsigned)(size_t)lds;
  int lim = SPIN_BUDGET;
  const int h0 = ((VW ? 2 * j : 8 + 6 * j) + 5 * c) & 31;
  u32x8 wv[16], wo[6][2][2];
  {
    if ((wid & 3) < 2 + MAP && xg < 2) {
      const unsigned nk = 8 + 2 * xg, M = (bs * (2 + MAP) + (wid & 3)) * 64 + lane, row = M / nk, k = M - row * nk;
      if (row < HELP) touch(VW ? P.Wq : P.Wk, HD * DM * 2, (row < 256 ? row + (VW ? 512 : 256) : row + 512) * (HD * 2) + min(xg * 4096 + (k >> 1) * 1024 + 256, 7936u) + (k & 1) * 128, lds);
    }
    const int nl = MAP ? (xg == 2 ? 8 : 4) : xg == 2 ? 6 : 5;
    #pragma unroll
    for (int i = 0; i < 2; ++i) {
      const unsigned L = bs * 1024 + wid * 128 + i * 64 + lane, row = L / nl;
      touch(P.Wo, HD * DM * 2, row * (DM * 2) + (c - (bs >> 1)) * 64 + (L - row * nl) * 128, lds);
    }
    VMWAIT();
    BARRIER();
    const int pos = lane >> 2;
    const int prow = (pos & 9) | ((pos & 2) << 1) | ((pos & 4) >> 1);
    const unsigned dv0 = (unsigned)(prow * (DM * 2) + c * 64 + ((lane & 3) << 4));
    const unsigned dv1 = (unsigned)(prow * (DM * 2) + c * 64 + (((lane & 3) ^ 2) << 4));
    const int toff = wid * 16384 + (g * 16 + q4) * 64 + (((p4 >> 1) ^ ((g & 1) << 1)) << 4) + 8 * (p4 & 1);
    #pragma unroll
    for (int rd = 0; rd < NHD / 2; ++rd) {
      #pragma unroll
      for (int hh = 0; hh < 2; ++hh) {
        const int h = (h0 + 2 * rd + hh) & 31;
        #pragma unroll
        for (int i = 0; i < 8; ++i)
          DMA_O(ro, (i & 1) ? dv1 : dv0, (unsigned)(h * 128 + i * 16) * (DM * 2), lbase + wid * 16384 + hh * 8192 + i * 1024);
      }
      VMWAIT();
      #pragma unroll
      for (int hh = 0; hh < 2; ++hh)
        #pragma unroll
        for (int ks = 0; ks < 2; ++ks)
          #pragma unroll
          for (int t = 0; t < 2; ++t)
            wo[2 * rd + hh][ks][t] = rd_frag<4096, 256>(lds + (toff ^ (t * 32)) + hh * 8192 + ks * 512);
      #pragma unroll
      for (int hh = 0; hh < 2; ++hh)
        WO_PIN(VW, 2 * rd + hh);
      asm volatile("s_waitcnt lgkmcnt(0)" ::: "memory");
    }
    const int lr = lane >> 3;
    #pragma unroll
    for (int i = 0; i < 16; ++i) {
      const int sw = (((lr >> 2) & 1) | ((i & 1) << 1)) << 1;
      const unsigned dv = (unsigned)(lr * (HD * 2) + b * 128 + (((lane & 7) ^ sw) << 4));
      DMA_V(rv, dv, (unsigned)(wid * 128 + i * 8) * (HD * 2), lbase + wid * 16384 + i * 1024);
    }
    VMWAIT();
    BARRIER();
    if (VW) {
      const int trow = 16 * (g >> 1) + 4 * (g & 1) + 8 * (q4 >> 1) + (q4 & 1);
      const int tsw = ((g & 1) | ((q4 >> 1) << 1)) << 1;
      LAS unsigned char* tv = lds + trow * 128 + (((2 * j + (p4 >> 1)) ^ tsw) << 4) + 8 * (p4 & 1);
      #pragma unroll
      for (int ks = 0; ks < 16; ++ks) wv[ks] = rd_frag<4096, 256>(tv + ks * 8192);
      V_PIN();
    }
    LBARRIER();
  }
  const unsigned cb = comm_base(ws);
  get_tag(P);
  if (wid == 0) touch(P.x, SEQ * DM * 2, (SEQ - 64 + lane) * 2048 + c * 64, lds);
  const unsigned mask = rec_go(lds + LB_RED + 4096 + 64, P, (u32x2){0u, P.tag - 1u}, wid, lane);
  const int idx = (lane & 1) ? 0xEEEE : 0x4444;
  const unsigned am_a = lbase + LB_MEM + lane * 16;
  const unsigned fb = step_base(cb, mask, 0);
  volatile LAS unsigned* tsl = (volatile LAS unsigned*)(lds + LB_RED + 4096);
  if (tid < 8) tsl[tid] = tid ? 0u : (unsigned)__builtin_amdgcn_s_memrealtime() * 4u;
  unsigned dn = 600;
  int da = 560 * 16, gb = 350 * 16;
  if (VW) __builtin_amdgcn_s_setprio(2);
  LBARRIER();
  report(P, wid);
  TL(72);
  for (int s = 0; s < NSEG; ++s) {
    const unsigned sb = step_base(cb, mask, s);
    const int ln = opq(lane), gg = ln >> 4;
    GSDECL;
    RTC(32 + b);
    const int l16 = ln * 16;
    const int xc = 32 * (c & 1) + 2 * (ln & 15);
    const unsigned mo_v = MF_AT(c >> 1) + ((xc >> 4) * 16 + (4 * r + gg) * 2 + ((xc >> 3) & 1)) * 16 + (xc & 7) * 2;
    if (!VW) {
      const unsigned tm = __builtin_amdgcn_readfirstlane(*tsl), tgt = tm + DLB, la = lbase + LB_MEM + j * 2048 + l16, mo = sb + j * 2048;
      B_POLL();
      if (wid == 7) {
        const int late = (int)((unsigned)__builtin_amdgcn_s_memrealtime() * 4u - tgt) - SN0;
        const unsigned to = tm + dn + (late > 80 ? late : 0);
        if (ln == 0) tsl[1] = to;
        if (b == 0) {
          dn = (da >> 4) + DL1;
          if (ln == 0) { *tsl = to + (gb >> 4) + DL2; st8(rs, fb + FL_T(s + 1), (u32x2){to + (gb >> 4) + DL2, (dn << 8) | (s + 1)}); }
          const unsigned va = ld4(rs, fb + FL_O(ln & 31)), vb = ld4(rs, fb + FL_B(ln));
          if (s > 2) {
            track(da, rfl(wmax(va)), s < 8 ? 5 : DCA);
            track(gb, rfl(wmax(vb)), s < 8 ? 5 : DCA);
          }
        }
      }
      if (wid == 7 && s > 0 && s + 2 < NSEG) __builtin_amdgcn_raw_buffer_store_b32(~0u, rs, mo_v, step_base(cb, mask, s + 2), AUX_SC1);
    } else {
      GS(3);
      const unsigned vo = VB_AT(b >> 1) + ((b & 1) * 4 + j) * 256;
      V_PROJ();
      GS(0); GSFLUSH(0, 4 + j);
      if (s + PD < NSEG && ln < 16) st16(rs, step_base(cb, mask, s + PD) + vo + l16, poison4());
    }
    GSFLUSH(3, 24 + wid);
    if (s + 1 == NSEG) break;
    unsigned so[6];
    #pragma unroll
    for (int i = 0; i < NHD; ++i) { const int h = (h0 + i) & 31; so[i] = __builtin_amdgcn_readfirstlane(sb + OB_AT(r, h)); }
    const unsigned sn = __builtin_amdgcn_readfirstlane(step_base(cb, mask, s + 1));
    const unsigned rw_a = lbase + LB_RED + (wid * 128 + (gg & 1) * 64 + (gg >> 1) * 16 + (ln & 15)) * 4;
    const unsigned rr_a = lbase + LB_RED + ln * 8;
    if (wid == 7) {
      if (b != 0) {
        __builtin_amdgcn_s_sleep(4);
        unsigned tm = 0;
        dn = 0;
        plan_wait(rs, fb + FL_T(s + 1), s + 1, lim, tm, dn);
        if (ln == 0) *tsl = tm;
      }
      const unsigned tgt = rfl(tsl[1]);
      T_WAIT();
    }
    lim = __builtin_amdgcn_readfirstlane(lim);
    unsigned long long t1;
    if (VW) WO_V(); else WO_W();
    if (wid == 0) {
      if (ln == 0) st4(rs, fb + FL_B(b), (unsigned)t1 * 4u - tsl[1]);
    }
    if (wid == 0) {
      GSFLUSH(2, 16);
      TL(s);
    }
    GSFLUSH(1, 8 + wid);
    if (wid == 0 || wid == 4) { GSFLUSH(4, 17 + (wid >> 2)); GSFLUSH(0, 19 + (wid >> 2)); }
  }
  __builtin_amdgcn_s_setprio(0);
  const int ln = opq(0);
  TL(64);
  volatile LAS int* rdy = (volatile LAS int*)(lds + 98304 + 3 * 16384);
  if (tid == 0) *rdy = 61;
  LBARRIER();
#define T_SRC(sg, i) (WS_OX + (unsigned)((sg) * NH + ((h0 + (i)) & 31)) * 8192u + r * 1024)
#define T_MMA(i, k, f) do { const u32x8 w0 = wo[i][(k) & 1][0], w1 = wo[i][(k) & 1][1]; const int e = ((k) >> 1) * 4; \
    a0 = MFMA32(((u32x4){w0[e], w0[e + 1], w0[e + 2], w0[e + 3]}), f, a0); a1 = MFMA32(((u32x4){w1[e], w1[e + 1], w1[e + 2], w1[e + 3]}), f, a1); } while (0)
#define T_DMA(sg, i) do { _Pragma("unroll") for (int k = 0; k < 4; ++k) dmas(rs, ln * 16, T_SRC(sg, i) + k * 2048, lbase + j * 24576 + (i) * 4096 + k * 1024); } while (0)
  const int cb2 = 16 * ((ln >> 4) & 1) + 8 * (ln >> 5);
  const unsigned eo = (unsigned)((NSEG - 1 - j) * 32 + r * 16 + (ln & 15)) * 2048u + (unsigned)(c * 32 + cb2) * 2u;
  u32x4 gv, xv;
  if (VW && wid < NSEG - XS3) {
    xv = *(const u32x4*)((const char*)P.x + eo);
    cnt_wait(P, C_W4(15), 4, ln);
    gv = ld16(rs, WS_G + (unsigned)((NSEG - 1 - j) * 2 + r) * 32768u + c * 1024 + blk_in(ln & 15, cb2));
  }
  bool ahead = false;
  f32x4 k0, k1;
  #pragma unroll 1
  for (int sg = XS3; sg < NSEG; ++sg) {
    f32x4 a0 = zero4(), a1 = zero4();
    if (VW) {
      if (wid == 3) {
        for (int w = __builtin_amdgcn_readfirstlane(*rdy); w <= sg;) {
          w = max(w + 1, -wmax(-(int)flag_wait<TSL>(P, F_PRG(4, ln & 31), w + 1, ln)));
          if (ln == 0) *rdy = w;
        }
      } else while (__builtin_amdgcn_readfirstlane(*rdy) <= sg) __builtin_amdgcn_s_sleep(1);
      u32x4 of[8];
      #pragma unroll
      for (int i = 0; i < 8; ++i) of[i] = __builtin_amdgcn_raw_buffer_load_b128(rs, ln * 16 + (i & 3) * 2048, T_SRC(sg, i >> 2), AUX_SC1);
      #pragma unroll
      for (int i = 0; i < 8; ++i) T_MMA(i >> 2, i & 3, of[i]);
    } else {
      if (!ahead) {
        while (__builtin_amdgcn_readfirstlane(*rdy) <= sg) __builtin_amdgcn_s_sleep(1);
        #pragma unroll
        for (int i = 0; i < NHD; ++i) T_DMA(sg, i);
      }
      VMW(20);
      ahead = __builtin_amdgcn_readfirstlane(*rdy) > sg + 1;
      u32x4 of[2][4];
#define T_RD(i) do { _Pragma("unroll") for (int k = 0; k < 4; ++k) of[(i) & 1][k] = *(const LAS u32x4*)(lds + j * 24576 + (i) * 4096 + k * 1024 + ln * 16); } while (0)
      T_RD(0);
      #pragma unroll
      for (int i = 0; i < NHD; ++i) {
        asm volatile("s_waitcnt lgkmcnt(0)" : "+v"(of[i & 1][0]), "+v"(of[i & 1][1]), "+v"(of[i & 1][2]), "+v"(of[i & 1][3]) :: "memory");
        if (i + 1 < NHD) {
          if (ahead) VMW(16); else G_WAIT_V(16 - 4 * (i < 4 ? i : 4));
          T_RD(i + 1);
        }
        #pragma unroll
        for (int k = 0; k < 4; ++k) {
          if (ahead) dmas(rs, ln * 16, T_SRC(sg + 1, i) + k * 2048, lbase + j * 24576 + i * 4096 + k * 1024);
          T_MMA(i, k, of[i & 1][k]);
          __builtin_amdgcn_sched_barrier(0);
        }
      }
    }
    LAS f32x4* pw = (LAS f32x4*)(lds + 98304 + ((sg & 3) * 8 + wid) * 2048) + ln;
    if (wid == 0 && sg == NSEG - 1) { k0 = a0; k1 = a1; } else { pw[0] = a0; pw[64] = a1; }
  }
  if (VW && wid < NSEG - XS3) {
    LBARRIER();
    const LAS f32x4* pr = (const LAS f32x4*)(lds + 98304 + (3 - j) * 16384) + ln;
    f32x4 t[2];
    #pragma unroll
    for (int k = 0; k < 2; ++k)
      t[k] = (((wid ? pr[k * 64] : k ? k1 : k0) + pr[128 + k * 64]) + (pr[256 + k * 64] + pr[384 + k * 64])) + ((pr[512 + k * 64] + pr[640 + k * 64]) + (pr[768 + k * 64] + pr[896 + k * 64]));
    fin_out(P, eo, epi_pack(t[0], t[1]), gv, xv);
  } else LBARRIER();
}

#define DMA_S dman
#define XDL 4
#define XPC 0
#define PW3 11
#define PW4 25
#define SDL 0
#define LS_P 131072
template <int MODE>
__device__ __forceinline__ void s_role(LAS unsigned char* lds, Ptrs& P, const u16* W, int h, unsigned obase, int nseg, int fl, int wid, int s0) {
  constexpr bool XM = MODE >= 2;
  constexpr unsigned RS = XM ? DM * 2 : HD * 2, SS = XM ? 262144u : 65536u;
  const int lane = opq(0);
  const int kh = wid >> 2, tp = wid & 3;
  const rsrc_t rs = WS_RSRC(P);
  const rsrc_t rw = __builtin_amdgcn_make_buffer_rsrc((void*)W, 0, HD * DM * 2, RSRC_W3);
  const unsigned lbase = (unsigned)(size_t)lds;
  u32x8 w[8][2];
  unsigned f0 = 0;
  if (!XM) for (int i = 0; i < SDL; ++i) __builtin_amdgcn_s_sleep(32);
  if (XM && XDL) {
    if (wid == 0) flag_wait(P, F_PRG(4, h), max(s0 - XDL, 0), lane);
    BARRIER();
  }
  {
    const int g = lane >> 4, q4 = (lane & 15) >> 2, p4 = lane & 3;
    const int trow = XM ? 16 * g + 4 * (q4 >> 1) + (q4 & 1) : 8 * g + q4;
#define S_SW(row) ((((row) & 1) | ((((row) >> (XM ? 2 : 1)) & 1) << 1) | ((((row) >> (XM ? 4 : 3)) & 1) << 2)) << 1)
    const int toff = trow * 256 + (((4 * tp + (p4 >> 1)) ^ S_SW(trow)) << 4) + 8 * (p4 & 1);
    unsigned dv[2];
    #pragma unroll
    for (int x = 0; x < 2; ++x) {
      const int row = 8 * wid + 4 * x + (lane >> 4);
      dv[x] = (unsigned)row * RS + (XM ? (h >> 3) * 1024 * RS + (h & 7) * 256 : h * 256) + (((lane & 15) ^ S_SW(row)) << 4);
    }
#define S_IMG(i) do { DMA_S(rw, dv[0], (i) * 64 * RS, lbase + ((i) & 7) * 16384 + wid * 2048); \
                      DMA_S(rw, dv[1], (i) * 64 * RS, lbase + ((i) & 7) * 16384 + wid * 2048 + 1024); if (XM && XPC) __builtin_amdgcn_s_sleep(XPC); } while (0)
    #pragma unroll
    for (int i = 0; i < 8; ++i) S_IMG(i);
    #pragma unroll
    for (int b = 0; b < 4; ++b) {
      if (b == 3 && !XM) { f0 = ld4(rs, FLG_OFF + F_LN0(lane & 7) * 128 + 4); get_tag(P); }
      if (b < 3) VMW(8); else VMWAIT();
      BARRIER();
      if (kh == (b >> 1)) {
        #pragma unroll
        for (int i = 4 * (b & 1); i < 4 * (b & 1) + 4; ++i) {
          w[i][0] = rd_frag<XM ? 2048 : 8192, XM ? 512 : 1024>(lds + toff + i * 16384);
          w[i][1] = rd_frag<XM ? 2048 : 8192, XM ? 512 : 1024>(lds + (toff ^ 32) + i * 16384);
        }
      }
      LBARRIER();
      if (b < 2) {
        #pragma unroll
        for (int i = 4 * b + 8; i < 4 * b + 12; ++i) S_IMG(i);
      }
    }
  }
  TL(XM ? 74 : 72);
  const unsigned l16 = lane * 16;
  volatile LAS int* rd = (volatile LAS int*)(lds + LS_P + 20480);
#define X_READY(want) do { \
    if (wid == 0) { \
      const int n_ = -wmax(-(int)flag_wait(P, F_PRG(4, (h & 24) + (lane & 7)), s0 + (want), lane)) - s0; \
      if (lane == 0) *rd = max(n_, want); \
    } \
    LBARRIER(); } while (0)
  if (MODE == 2) X_READY(2);
  else if (!XM) {
    if (__any(f0 != P.tag)) flag_wait(P, F_LN0(lane & 7), 0, lane);
  }
  unsigned sx = (XM ? WS_OX + s0 * SS + (h >> 3) * 65536 : WS_XN) + kh * 32768 + tp * 4096;
  const unsigned sd = lbase + kh * 65536 + tp * 4096;
  const unsigned xl = kh * 65536 + (XM ? l16 : blk_in(lane & 15, (lane >> 4) * 8));
  const unsigned xp = lbase + xl, pa = lbase + LS_P + tp * 4096 + l16;
  LAS f32x4* const pq = (LAS f32x4*)(lds + LS_P + tp * 4096 + l16);
  if (MODE == 3) {
#define X_GET(t) do { _Pragma("unroll") for (int i = 0; i < 16; ++i) \
      dmas(rs, l16, sx + (t) * SS + ((i >> 2) & 1) * 32768 + (i >> 3) * 16384 + (i & 3) * 1024, sd + ((i >> 2) & 1) * 65536 + ((t) & 1) * 32768 + (i >> 3) * 16384 + (i & 3) * 1024); } while (0)
    bool have = false;
    const unsigned pv = FLG_OFF + F_PRG(kh ? 7 : 4, kh ? h : (h & 24) + (lane & 7)) * 128;
    if (!kh) __builtin_amdgcn_s_setprio(1);
    #pragma unroll 1
    for (int t = 0; t < nseg; ++t) {
      if (!have && !kh) {
        flag_wait<2>(P, F_PRG(4, (h & 24) + (lane & 7)), s0 + t + 1, lane);
        X_GET(t);
        VMW(8);
      }
      BARRIER();
      f32x4 a[4];
      unsigned pg;
      ONE_X(xp + (t & 1) * 32768);
      if (wid == 5) flag_set(P, fl, t, lane);
      have = t + 1 < nseg && !__any(pg < (unsigned)(s0 + t + 2));
      if (!kh && lane == 0) rd[tp] = have;
      pq[kh * 128] = kh ? a[0] : a[2]; pq[kh * 128 + 64] = kh ? a[1] : a[3];
      if (have && !kh) X_GET(t + 1);
      LBARRIER();
      const unsigned oo = obase + (unsigned)(s0 + t) * 0x40000u + tp * 4096 + kh * 2048 + l16;
      st16(rs, oo, __builtin_bit_cast(u32x4, (kh ? a[2] : a[0]) + pq[128 - kh * 128]));
      st16(rs, oo + 1024, __builtin_bit_cast(u32x4, (kh ? a[3] : a[1]) + pq[192 - kh * 128]));
      if (__any(rd[lane & 3] == 0)) {
        VMWAIT();
        BARRIER();
        if (wid == 5) flag_set(P, fl, t + 1, lane);
      } else if (!kh) VMW(10);
      asm volatile("s_waitcnt lgkmcnt(0)" ::: "memory");
      TL(t);
    }
    __builtin_amdgcn_s_setprio(0);
    TL(65);
    return;
  }
  if (XM) {
    #pragma unroll
    for (int i = 0; i < 12; ++i) dmas(rs, l16, sx + (i >> 3) * SS + ((i >> 2) & 1) * 16384 + (i & 3) * 1024, sd + (i >> 2) * 16384 + (i & 3) * 1024);
    VMWAIT();
    BARRIER();
  } else {
    #pragma unroll
    for (int i = 0; i < 16; ++i) dmas(rs, l16, sx + (i >> 3) * SS + ((i >> 2) & 1) * 16384 + (i & 3) * 1024, sd + ((i >> 2) ^ 2) * 16384 + (i & 3) * 1024);
    VMW(12);
    BARRIER();
    f32x4 a[4];
    if (MODE) ONE_V(xp + 32768); else ONE_K(xp + 32768);
    pq[kh * 128] = kh ? a[0] : a[MODE ? 1 : 2]; pq[kh * 128 + 64] = kh ? a[MODE ? 2 : 1] : a[3];
    LBARRIER();
    const f32x4 a0 = (kh ? a[MODE ? 1 : 2] : a[0]) + pq[128 - kh * 128], a1 = (kh ? a[3] : a[MODE ? 2 : 1]) + pq[192 - kh * 128];
    if (MODE) st16(rs, obase + h * 8192 + (2 * tp + kh) * 1024 + l16, (u32x4){pack2(a0[0], a0[1]), pack2(a0[2], a0[3]), pack2(a1[0], a1[1]), pack2(a1[2], a1[3])});
    else st16(rs, obase + h * 8192 + kh * 4096 + tp * 1024 + l16, (u32x4){pack2(a0[0], a1[0]), pack2(a0[1], a1[1]), pack2(a0[2], a1[2]), pack2(a0[3], a1[3])});
    sx += SS;
    #pragma unroll
    for (int i = 0; i < 4; ++i) dmas(rs, l16, sx + SS + i * 1024, sd + 32768 + i * 1024);
    VMW(4);
    BARRIER();
    if (wid == 1) flag_set(P, fl, 1, lane);
    TL(62);
  }
  u32x8 xf[3], ac[2] = {};
  #pragma unroll
  for (int i = 0; i < 3; ++i) {
    const u32x4 a = *(const LAS u32x4*)(lds + xl + i * 2048), b = *(const LAS u32x4*)(lds + xl + i * 2048 + 1024);
    xf[i] = (u32x8){a[0], a[1], a[2], a[3], b[0], b[1], b[2], b[3]};
  }
  unsigned long long fm, pm;
  asm volatile("s_cmp_eq_u32 %2, 1\n\ts_cselect_b64 %0, 1, 0\n\ts_cmp_eq_u32 %3, 1\n\ts_cselect_b64 %1, -1, 0" : "=&s"(fm), "=&s"(pm) : "s"(wid), "s"(XM ? 0 : wid >> 1) : "scc");
  const unsigned fo = FLG_OFF + fl * 128, pd = lbase + LS_P + 16384;
  const unsigned po = 4 * 65536 + h * 2048 + (wid & 1) * 1024 - tp * 4096 - kh * 32768;
  unsigned oo = XM ? obase + tp * 4096 + (s0 - 1) * 0x40000u : obase + (unsigned)h * 8192u + tp * (MODE ? 2048 : 1024), tmp, tmp2;
  unsigned long long em;
  int t = XM ? 0 : 1;
  if (MODE < 2) {
    #pragma unroll 1
    for (int part = 0; part < 4; ++part) {
      if (part == 1) { TL(63); if (wid == 0) setup_wait(P, lane); }
      if (MODE == 1 && part >= 2) {
        #pragma unroll
        for (int i = 0; i < 4; ++i) {
          const unsigned to = ((i * 32 + h) * 512 + wid * 64 + opq(0)) * 128;
          if (part == 2) touch(P.w3, DM * FF * 2, to, lds); else touch(P.w4, DM * FF * 2, to, lds);
        }
      }
      const int te = part == 0 ? 3 : part == 1 ? PW3 : part == 2 ? PW4 : nseg + 1;
      if (MODE) S_LOOP_V(te, 0); else S_LOOP_K(te, 0);
    }
  } else {
    #pragma unroll 1
    while (t < nseg) {
      X_READY(min(t + 4, nseg));
      const int n = __builtin_amdgcn_readfirstlane(*rd), te = n >= nseg ? nseg : (n - 2) & ~1;
      TL(t);
      S_LOOP_X(te, nseg);
    }
  }
  TL(XM ? 65 : 64);
  VMWAIT();
  BARRIER();
  if (wid == 1) flag_set(P, fl, nseg, lane);
}

#define LND 1
#define CALN 6
__device__ __forceinline__ void setup_role(LAS unsigned char* lds, Ptrs& P, int cu, int wid) {
  const int lane = opq(0), tid = wid * 64 + lane;
  const rsrc_t rs = WS_RSRC(P);
  u32x4 v[8];
  const u32x4* px = (const u32x4*)(P.x + (size_t)(cu * 32 + wid * 4 + (lane >> 4)) * DM) + (lane & 15);
  #pragma unroll
  for (int i = 0; i < 8; ++i) v[i] = __builtin_nontemporal_load(px + i * 16);
  const unsigned epo = __hip_atomic_load((gu32*)(P.ws + EPO_OFF), RLX_AGENT);
  const unsigned cb = comm_base(P.ws);
  const u32x4 z = {0u, 0u, 0u, 0u};
  const int cr = 63 - cu;
  if (wid == 0 && cr < 16) st16(rs, CNT_OFF + cr * 1024 + lane * 16, z);
  if (cr < 4) st16(rs, cb + FL_B(0) + cr * 8192 + tid * 16, z);
  if (cr == 4 && tid < 256) {
    const int g4 = tid >> 6, k = (tid >> 5) & 1 & (g4 >> 1);
    st16(rs, WS_H1 + ((g4 & 1) ? 127u * 32768u : 0u) + (g4 * 8 + ((tid >> 2) & 7)) * 1024 + blk_in((g4 & 1) ? 15 - k : k, (tid & 3) * 8), z);
  }
  #pragma unroll
  for (int i = 0; i < 2; ++i) {
    const int f = i * 54 + cr;
    if (cr < PD * NCHUNK) {
      u32x4 m = {~0u, ~0u, ~0u, ~0u};
      if (f < 4) {
        const int ks = (f >> 1) * 8 + wid;
        m = *(const u32x4*)(P.mem0 + ((lane >> 1) & 7) * DM + 64 * ks + 16 * (lane >> 4) + 8 * (lane & 1));
        m.x = canon1(m.x); m.y = canon1(m.y); m.z = canon1(m.z); m.w = canon1(m.w);
      }
      st16(rs, cb + (unsigned)(f / (NCHUNK * 2)) * CB_STEP + (f % (NCHUNK * 2)) * 8192u + tid * 16, m);
    }
  }
  const unsigned bi = blk_in((wid & 3) * 4 + (lane >> 4), (lane & 3) * 8);
  const int c0 = (lane >> 2) & 3;
  ln_rows(v);
  TL(69);
  if (cu >= 8) for (int i = 0; i < LND; ++i) __builtin_amdgcn_s_sleep(32);
  #pragma unroll
  for (int i = 0; i < 8; ++i) st16(rs, WS_XN + xn_blk(cu * 2 + (wid >> 2), 4 * i + c0) + bi, v[i]);
  P.tag = __builtin_amdgcn_readfirstlane(epo) + 1u;
  ln_rows(v);
  if (cu < 8) {
    VMWAIT();
    BARRIER();
    if (wid == 0) flag_set(P, F_LN0(cu), 1, lane);
    TL(66);
  }
  asm volatile("" ::: "memory");
  #pragma unroll
  for (int i = 0; i < 8; ++i) st16(rs, WS_XN2 + (unsigned)(cu * 2 + (wid >> 2)) * 32768u + (4 * i + c0) * 1024 + bi, v[i]);
  touch(P.w1, DM * DM * 2, ((cu & 31) * 512 + tid) * 128, lds);
  VMW(9);
  BARRIER();
  if (wid == 0) flag_set(P, F_LN0(cu), 1, lane);
  TL(70);
  if (wid == 0) VMW(2); else VMW(1);
  BARRIER();
  if (wid == 0) flag_set(P, F_LN1(cu), 1, lane);
  TL(71);
  if (cu < 32 && (wid >> 1) == 1) {
    const int id = cu * 2 + (wid & 1);
    unsigned best[2] = {65535u, 65535u};
    #pragma unroll
    for (int it = 0; it < CALN; ++it) {
      const unsigned long long c0 = __builtin_amdgcn_s_memtime();
      asm volatile("s_waitcnt lgkmcnt(0)" ::: "memory");
      if (lane == 0) st4(rs, cb + (id & 3) * CB_STEP + (30 + ((id >> 2) & 1)) * 16384u + (it & 1) * 8192u + (id >> 3) * 1024u, 0u);
      VMWAIT();
      const unsigned d = (unsigned)(__builtin_amdgcn_s_memtime() - c0);
      if (it >= 2) best[it & 1] = min(best[it & 1], d);
    }
    flag_set(P, F_CAL(id), (best[0] << 16) | best[1], lane);
  }
}

#define TLBP 0
#define CPF 1
__global__ __launch_bounds__(512, 2) void mega_kernel(const u16* x, const u16* mem0, const u16* Wq, const u16* Wk, const u16* Wv, const u16* Wo,
                                                     const u16* w1, const u16* w2, const u16* w3, const u16* w4, u16* out, char* ws) {
  __shared__ __attribute__((aligned(16))) unsigned char smem[163840];
  LAS unsigned char* lds = (LAS unsigned char*)smem;
  Ptrs P;
  P.x = x; P.mem0 = mem0; P.Wq = Wq; P.Wk = Wk; P.Wv = Wv; P.Wo = Wo; P.w1 = w1; P.w2 = w2; P.w3 = w3; P.w4 = w4; P.out = out; P.ws = ws;
  const int xcd = blockIdx.x & 7, slot = blockIdx.x >> 3;
  const int wid = __builtin_amdgcn_readfirstlane(threadIdx.x >> 6);
  TL(79);
  P.tag = 0;
  if (wid == 7) {
    unsigned long long pc;
    unsigned len;
    asm volatile("s_getpc_b64 %0\nMK_BEG:\n\ts_mov_b32 %1, MK_END-MK_BEG" : "=s"(pc), "=s"(len));
    touch((const void*)pc, len, (slot * 64 + opq(0)) * 128, lds);
  }
  const int na = xcd == 2 ? 8 : 12;
  const bool isa = xcd < 3 && slot < na;
  const int ah = xcd == 2 ? slot * 4 + 2 : xcd * 16 + (slot & 3) * 4 + (slot >> 2) + (slot >> 3), bs = slot - na, br = bs & 1, bc = (xcd == 1 ? 22 : xcd * 5) + (bs >> 1);
  if (isa) {
    if (wid < 4) a_wave<true>(lds, P, ah, wid); else a_wave<false>(lds, P, ah, wid);
  } else if (xcd < 3) {
    if (wid < 4) b_wave<true>(lds, P, br, bc, wid, bs, xcd); else b_wave<false>(lds, P, br, bc, wid, bs, xcd);
  } else {
    if ((xcd & 3) == 3) {
      setup_role(lds, P, (xcd >> 2) * 32 + slot, wid);
    } else if (xcd == 6) s_role<1>(lds, P, Wv, slot, WS_VF, NSEG, F_PRG(2, slot), wid, 0);
    else s_role<0>(lds, P, xcd == 4 ? Wk : Wq, slot, xcd == 4 ? WS_KF : WS_QF, NSEG, F_PRG(xcd - 4, slot), wid, 0);
    if ((xcd & 3) == 3) mlp_role(lds, P, xcd >> 2, slot, wid);
    else if (xcd == 6) s_role<3>(lds, P, Wo, slot, PT_AT(slot >> 3) + (slot & 7) * 16384, XS3 - XS2, F_PRG(7, slot), wid, XS2);
    else s_role<2>(lds, P, Wo, slot, PT_AT(slot >> 3) + (slot & 7) * 16384, xcd == 4 ? XS1 : XS2 - XS1, F_PRG(xcd + 1, slot), wid, xcd == 4 ? 0 : XS1);
    report(P, wid);
    fin_role(lds, P, wid);
    if (blockIdx.x == 3 && wid == 0) {
      cnt_wait(P, C_ARR, 256, threadIdx.x);
      if (threadIdx.x == 0) __hip_atomic_store((gu32*)(ws + EPO_OFF), P.tag, RLX_AGENT);
    }
  }
  asm volatile("MK_END:");
  TL(78);
}

extern "C" void kernel_launch(void* const* d_in, const int* in_sizes, int n_in, void* d_out, int out_size,
                              void* d_ws, size_t ws_size, hipStream_t stream) {
  mega_kernel<<<256, 512, 0, stream>>>((const u16*)d_in[0], (const u16*)d_in[1], (const u16*)d_in[2], (const u16*)d_in[3], (const u16*)d_in[4],
                                       (const u16*)d_in[5], (const u16*)d_in[8], (const u16*)d_in[9], (const u16*)d_in[10], (const u16*)d_in[11],
                                       (u16*)d_out, (char*)d_ws);
}
```

```cpp
#include <hip/hip_runtime.h>
#include <hip/hip_bf16.h>
#include <stdint.h>

#define SEQ 2048
#define DM 1024
#define NH 32
#define DH 128
#define HD (NH * DH)
#define MM 8
#define SEG 32
#define NSEG (SEQ / SEG)
#define FF 3584

typedef unsigned short u16;
typedef __attribute__((ext_vector_type(8))) short bf16x8;
typedef __attribute__((ext_vector_type(4))) short bf16x4;
typedef __attribute__((ext_vector_type(4))) float f32x4;
typedef __attribute__((ext_vector_type(4))) unsigned u32x4;
typedef __attribute__((ext_vector_type(2))) unsigned u32x2;
typedef __attribute__((ext_vector_type(2))) float f32x2;
typedef __attribute__((ext_vector_type(2))) __bf16 bf16v2;
typedef __attribute__((ext_vector_type(8))) unsigned u32x8;
#define LAS __attribute__((address_space(3)))
#define GAS __attribute__((address_space(1)))

__device__ __forceinline__ unsigned pack2(float lo, float hi) {
  return __builtin_bit_cast(unsigned, __builtin_convertvector((f32x2){lo, hi}, bf16v2));
}
__device__ __forceinline__ float rb1(float x) { return __uint_as_float(pack2(0.f, x)); }

typedef GAS unsigned gu32;
#define RLX_AGENT __ATOMIC_RELAXED, __HIP_MEMORY_SCOPE_AGENT
#define SPIN_CAP (1u << 20)
#define AUX_SC1 16
#define RSRC_W3 0x00020000
typedef __amdgpu_buffer_rsrc_t rsrc_t;

__device__ __forceinline__ u32x4 ld16(rsrc_t rs, unsigned off) { return __builtin_amdgcn_raw_buffer_load_b128(rs, off, 0, AUX_SC1); }
__device__ __forceinline__ u32x2 ld8(rsrc_t rs, unsigned off) { return __builtin_amdgcn_raw_buffer_load_b64(rs, off, 0, AUX_SC1); }
__device__ __forceinline__ unsigned ld4(rsrc_t rs, unsigned off) { return __builtin_amdgcn_raw_buffer_load_b32(rs, off, 0, AUX_SC1); }
__device__ __forceinline__ void st16(rsrc_t rs, unsigned off, u32x4 v) { __builtin_amdgcn_raw_buffer_store_b128(v, rs, off, 0, AUX_SC1); }
__device__ __forceinline__ void st8(rsrc_t rs, unsigned off, u32x2 v) { __builtin_amdgcn_raw_buffer_store_b64(v, rs, off, 0, AUX_SC1); }
__device__ __forceinline__ void st4(rsrc_t rs, unsigned off, unsigned v) { __builtin_amdgcn_raw_buffer_store_b32(v, rs, off, 0, AUX_SC1); }
__device__ __forceinline__ float lo16(unsigned w) { return __uint_as_float(w << 16); }
__device__ __forceinline__ float hi16(unsigned w) { return __uint_as_float(w & 0xffff0000u); }

__device__ __forceinline__ float xsum16_32(float v) {
  unsigned u = __float_as_uint(v);
  auto a = __builtin_amdgcn_permlane16_swap(u, u, false, false);
  v = __uint_as_float(a[0]) + __uint_as_float(a[1]);
  u = __float_as_uint(v);
  auto b = __builtin_amdgcn_permlane32_swap(u, u, false, false);
  return __uint_as_float(b[0]) + __uint_as_float(b[1]);
}

__device__ __forceinline__ unsigned canon1(unsigned w) { return min(w, 0xfffffffeu); }
__device__ __forceinline__ unsigned max4(u32x4 v) { return max(max(v.x, v.y), max(v.z, v.w)); }
__device__ __forceinline__ int opq(int) { int v; asm volatile("v_mbcnt_lo_u32_b32 %0, -1, 0\n\tv_mbcnt_hi_u32_b32 %0, -1, %0" : "=v"(v)); return v; }
__device__ __forceinline__ u32x4 poison4() { unsigned m = ~0u; asm volatile("" : "+v"(m)); return (u32x4){m, m, m, m}; }
__device__ __forceinline__ f32x4 zero4() { float m = 0.f; asm volatile("" : "+v"(m)); return (f32x4){m, m, m, m}; }
#define MFMA32(a, b, c) __builtin_amdgcn_mfma_f32_16x16x32_bf16(__builtin_bit_cast(bf16x8, a), __builtin_bit_cast(bf16x8, b), c, 0, 0, 0)
#define MFMA16(a, b, c) __builtin_amdgcn_mfma_f32_16x16x16bf16_1k(__builtin_bit_cast(bf16x4, a), __builtin_bit_cast(bf16x4, b), c, 0, 0, 0)
#define LBARRIER() do { asm volatile("s_waitcnt lgkmcnt(0)" ::: "memory"); __builtin_amdgcn_s_barrier(); asm volatile("" ::: "memory"); } while (0)
#define BARRIER() do { asm volatile("" ::: "memory"); __builtin_amdgcn_s_barrier(); asm volatile("" ::: "memory"); } while (0)
#define VMWAIT() asm volatile("s_waitcnt vmcnt(0)" ::: "memory")
#define VMW(n) asm volatile("s_waitcnt vmcnt(" #n ")" ::: "memory")
#define DELAY(n) do { if ((n) > 0) __builtin_amdgcn_s_sleep(n); } while (0)

#define EPO_OFF 0
#define TMO_OFF 128
#define FLG_OFF 256
#define F_LN0(i) (i)
#define F_LN1(i) (64 + (i))
#define F_CAL(i) (128 + (i))
#define F_PRG(t, h) (192 + (t) * 32 + (h))
#define CNT_OFF 65536
#define C_W1(p2) (p2)
#define C_W2(p2) (16 + (p2))
#define C_LN(p2) (32 + (p2))
#define C_W3(p2) (48 + (p2))
#define C_W4(p2) (64 + (p2))
#define C_HEAD 80
#define C_ARR 81
#define NCNT 128
#define WS_XN (1u << 20)
#define WS_XN2 (WS_XN + (4u << 20))
#define WS_QF (WS_XN2 + (4u << 20))
#define WS_KF (WS_QF + (16u << 20))
#define WS_VF (WS_KF + (16u << 20))
#define WS_DUM (WS_VF + (16u << 20))
#define WS_H1 (WS_DUM + (1u << 20))
#define WS_X1 (WS_H1 + (4u << 20) + 65536u)
#define WS_H3 (WS_X1 + (4u << 20))
#define WS_OX WS_QF
#define WS_X1N WS_H1
#define WS_G WS_XN2
#define PT_AT(kq) (WS_KF + ((kq) >> 1) * (16u << 20) + ((kq) & 1) * 131072u)
#define WS_COMM (WS_H3 + (14u << 20))
#define WS_END (WS_COMM + (4u << 20))
#define CB_STEP 524288u
#define PD 3
__device__ __forceinline__ unsigned comm_base(const char* ws) { return WS_COMM + ((0u - ((unsigned)(size_t)ws + WS_COMM)) & 0x1fffffu); }
__device__ __forceinline__ unsigned step_base(unsigned cb, unsigned mask, int s) { return cb + (unsigned)(s & 3) * CB_STEP + mask; }
#define MF_AT(ks) (((ks) >> 3) * 16384u + ((ks) & 7) * 1024u)
#define OB_AT(r, h) ((2u + (r) * 4u + ((h) >> 3)) * 16384u + ((h) & 7) * 1024u)
#define VB_AT(h) ((10u + ((h) >> 2)) * 16384u + ((h) & 3) * 2048u)
#define XS1 24
#define XS2 46
#define XS3 62
#define CSL 8
#define FSL 6
#define SQUICK 3
#define NCHUNK 18
#define FL_O(h) (29u * 16384u + (h) * 128u)
#define FL_B(b) (28u * 16384u + (b) * 128u)
#define FL_T(s) FL_O(32 + ((s) & 3))

struct Ptrs { const u16 *x, *mem0, *Wq, *Wk, *Wv, *Wo, *w1, *w2, *w3, *w4; u16* out; char* ws; unsigned tag; };
#define WS_RSRC(P) __builtin_amdgcn_make_buffer_rsrc((void*)(P).ws, 0, WS_END, RSRC_W3)

__device__ __forceinline__ bool spin_fail(unsigned& spins, const Ptrs& P, int lane) {
  asm volatile("" ::: "memory");
  if (++spins > SPIN_CAP) {
    unsigned tv;
    asm volatile("v_mov_b32 %0, %1" : "=v"(tv) : "s"(P.tag));
    if (lane == 0) __builtin_amdgcn_raw_buffer_store_b32(tv, WS_RSRC(P), 0, TMO_OFF, AUX_SC1);
    return true;
  }
  return (spins & 255u) == 0u && (unsigned)__builtin_amdgcn_readfirstlane(__builtin_amdgcn_raw_buffer_load_b32(WS_RSRC(P), 0, TMO_OFF, AUX_SC1)) == P.tag;
}
__device__ __forceinline__ unsigned cnt_load(char* ws, int idx) {
  return __builtin_amdgcn_readfirstlane(__hip_atomic_load((gu32*)(ws + CNT_OFF + idx * 128), RLX_AGENT));
}
__device__ __forceinline__ void cnt_add(char* ws, int idx, int lane) {
  asm volatile("s_waitcnt vmcnt(0)" ::: "memory");
  if (lane == 0) __hip_atomic_fetch_add((gu32*)(ws + CNT_OFF + idx * 128), 1u, RLX_AGENT);
}
__device__ __forceinline__ void cnt_wait(const Ptrs& P, int idx, unsigned want, int lane) {
  for (unsigned spins = 0;;) {
    if (cnt_load(P.ws, idx) >= want) break;
    __builtin_amdgcn_s_sleep(CSL);
    if (spin_fail(spins, P, lane)) break;
  }
}
template <int SL = FSL>
__device__ __forceinline__ unsigned flag_wait(const Ptrs& P, int f, unsigned want, int lane) {
  const rsrc_t rs = WS_RSRC(P);
  u32x2 v;
  for (unsigned spins = 0;;) {
    v = ld8(rs, FLG_OFF + f * 128);
    if (!__any(v.y != P.tag || v.x < want)) break;
    __builtin_amdgcn_s_sleep(SL);
    if (spin_fail(spins, P, lane)) break;
  }
  asm volatile("" ::: "memory");
  return v.x;
}
__device__ __forceinline__ void flag_set(const Ptrs& P, int f, unsigned val, int lane) {
  if (lane == 0) st8(WS_RSRC(P), FLG_OFF + f * 128, (u32x2){val, P.tag});
}
__device__ __forceinline__ void get_tag(Ptrs& P) { P.tag = __builtin_amdgcn_readfirstlane(__hip_atomic_load((gu32*)(P.ws + EPO_OFF), RLX_AGENT)) + 1u; }
__device__ __forceinline__ void setup_wait(const Ptrs& P, int lane) { flag_wait(P, F_LN0(lane), 0, lane); }
__device__ __forceinline__ void clock_wait(unsigned tgt) {
  while ((int)(tgt - (unsigned)__builtin_amdgcn_s_memrealtime()) > 0) {}
}
__device__ __forceinline__ void report(const Ptrs& P, int wid) {
  if (wid == 0 && opq(0) == 0) __hip_atomic_fetch_add((gu32*)(P.ws + CNT_OFF + C_ARR * 128), 1u, RLX_AGENT);
}
__device__ __forceinline__ void kv_wait(const Ptrs& P, int s, int h, int& known, int lane) {
  if (s < known) return;
  const unsigned v = flag_wait(P, F_PRG(min(lane, 2), h), s + 1, lane);
  known = __builtin_amdgcn_readfirstlane(min(min(__builtin_amdgcn_readlane(v, 0), __builtin_amdgcn_readlane(v, 1)), __builtin_amdgcn_readlane(v, 2)));
}
__device__ __forceinline__ int wmax(int v) {
  v = max(v, __builtin_amdgcn_update_dpp(0, v, 0x128, 0xf, 0xf, false));
  v = max(v, __builtin_amdgcn_update_dpp(0, v, 0x124, 0xf, 0xf, false));
  v = max(v, __builtin_amdgcn_update_dpp(0, v, 0x122, 0xf, 0xf, false));
  v = max(v, __builtin_amdgcn_update_dpp(0, v, 0x121, 0xf, 0xf, false));
  return max(max(__builtin_amdgcn_readlane(v, 0), __builtin_amdgcn_readlane(v, 16)), max(__builtin_amdgcn_readlane(v, 32), __builtin_amdgcn_readlane(v, 48)));
}
__device__ __forceinline__ unsigned near_half(unsigned v) { return wmax(-(int)(v & 65535u)) > wmax(-(int)(v >> 16)) ? 8192u : 0u; }
__device__ __forceinline__ void dmaw(rsrc_t rs, unsigned voff, unsigned soff, unsigned ldsaddr) {
  asm volatile("s_mov_b32 m0, %3\n\ts_nop 0\n\tbuffer_load_dwordx4 %0, %1, %2 offen lds" :: "v"(voff), "s"(rs), "s"(soff), "s"(ldsaddr) : "memory");
}
__device__ __forceinline__ void dman(rsrc_t rs, unsigned voff, unsigned soff, unsigned ldsaddr) {
  asm volatile("s_mov_b32 m0, %3\n\ts_nop 0\n\tbuffer_load_dwordx4 %0, %1, %2 offen nt lds" :: "v"(voff), "s"(rs), "s"(soff), "s"(ldsaddr) : "memory");
}
__device__ __forceinline__ void dmas(rsrc_t rs, unsigned voff, unsigned soff, unsigned ldsaddr) {
  asm volatile("s_mov_b32 m0, %3\n\ts_nop 0\n\tbuffer_load_dwordx4 %0, %1, %2 offen sc1 lds" :: "v"(voff), "s"(rs), "s"(soff), "s"(ldsaddr) : "memory");
}
__device__ __forceinline__ void touch(const void* w, unsigned bytes, unsigned voff, LAS unsigned char* lds) {
  asm volatile("s_mov_b32 m0, %2\n\ts_nop 0\n\tbuffer_load_dword %0, %1, 0 offen lds"
               :: "v"(voff), "s"(__builtin_amdgcn_make_buffer_rsrc((void*)w, 0, bytes, RSRC_W3)), "s"((unsigned)(size_t)lds + 148480) : "memory");
}
__device__ __forceinline__ u32x2 trd(LAS unsigned char* p) {
  return __builtin_bit_cast(u32x2, __builtin_amdgcn_ds_read_tr16_b64_v4i16((LAS bf16x4*)p));
}
template <int BO, int UO>
__device__ __forceinline__ u32x8 rd_frag(LAS unsigned char* p) {
  const u32x2 a = trd(p), b = trd(p + UO), c = trd(p + BO), d = trd(p + BO + UO);
  return (u32x8){a.x, a.y, b.x, b.y, c.x, c.y, d.x, d.y};
}

#define TL(i) do {} while (0)
#define TLW(i) do {} while (0)
#define GSDECL
#define GS(i) do {} while (0)
#define GSFLUSH(k, i) do {} while (0)

#define QPRIO 0
#define VPOS 3
#define VLDI "buffer_load_dword v254, v17, %[rs], %[sb] offen sc1\n\tbuffer_load_dword v255, v17, %[rs], %[sb] offen offset:256 sc1\n\t"
#define VLD(p) VLD##p
#define VLD0
#define VLD1
#define VLD2
#define VLD3 VLDI
#define VLD4
#define DEC1 1
#define DEC2 1
#define DGA
#define DGB
#define DGC
#define DGE
#define DGO
#define DGP
#define DGQ
#define PACE "s_sleep 1\n\t"
#define STR_(x) #x
#define STR(x) STR_(x)
#define GSW
#define GSA(k)
#define GSO(k)
#define A_MAIN_Q() \
  asm volatile( \
    "v_mov_b32 v253, %[tag]\n\t" \
    "buffer_load_dwordx4 v[20:23], v12, %[rs], %[vx] offen sc1\n\t" \
    "buffer_load_dwordx4 v[24:27], v12, %[rs], %[vx] offen offset:1024 sc1\n\t" \
    "ds_read_b128 v[236:239], v9 offset:0\n\t" \
    "ds_read_b128 v[240:243], v9 offset:1024\n\t" \
    "ds_read_b128 v[244:247], v9 offset:2048\n\t" \
    "ds_read_b128 v[248:251], v9 offset:3072\n\t" \
    "v_mov_b32 v0, 0\n\t" \
    "v_mov_b32 v1, 0\n\t" \
    "v_mov_b32 v2, 0\n\t" \
    "v_mov_b32 v3, 0\n\t" \
    "v_mov_b32 v4, 0\n\t" \
    "v_mov_b32 v5, 0\n\t" \
    "v_mov_b32 v6, 0\n\t" \
    "v_mov_b32 v7, 0\n\t" \
    "s_cmp_lg_u32 %[sc], 3\n\t" \
    "s_cbranch_scc1 PB_%=\n\t" \
    "s_getpc_b64 s[92:93]\n\t" \
    "TPA_%=:\n\t" \
    "s_add_u32 s92, s92, TEA_%=-TPA_%=\n\t" \
    "s_addc_u32 s93, s93, 0\n\t" \
    "s_lshr_b32 s91, %[tgt], 2\n\t" \
    "s_sub_u32 s91, s91, 3\n\t" \
    "TWA_%=:\n\t" \
    "s_memrealtime s[88:89]\n\t" \
    DGE \
    "s_waitcnt lgkmcnt(0)\n\t" \
    "s_sub_u32 s89, s91, s88\n\t" \
    "s_lshl_b32 s89, s89, 2\n\t" \
    "s_cmp_gt_i32 s89, 0\n\t" \
    "s_cbranch_scc1 TWA_%=\n\t" \
    "s_lshl_b32 s88, s88, 2\n\t" \
    "s_sub_u32 s89, %[tgt], s88\n\t" \
    "s_max_i32 s89, s89, 0\n\t" \
    "s_mul_i32 s89, s89, 11\n\t" \
    "s_lshr_b32 s89, s89, 1\n\t" \
    DGB \
    "s_and_b32 s89, s89, 0x7c\n\t" \
    "s_sub_u32 s92, s92, s89\n\t" \
    "s_subb_u32 s93, s93, 0\n\t" \
    "s_setpc_b64 s[92:93]\n\t" \
    "s_nop 0\n\t" \
    "s_nop 0\n\t" \
    "s_nop 0\n\t" \
    "s_nop 0\n\t" \
    "s_nop 0\n\t" \
    "s_nop 0\n\t" \
    "s_nop 0\n\t" \
    "s_nop 0\n\t" \
    "s_nop 0\n\t" \
    "s_nop 0\n\t" \
    "s_nop 0\n\t" \
    "s_nop 0\n\t" \
    "s_nop 0\n\t" \
    "s_nop 0\n\t" \
    "s_nop 0\n\t" \
    "s_nop 0\n\t" \
    "s_nop 0\n\t" \
    "s_nop 0\n\t" \
    "s_nop 0\n\t" \
    "s_nop 0\n\t" \
    "s_nop 0\n\t" \
    "s_nop 0\n\t" \
    "TEA_%=:\n\t" \
    "PB_%=:\n\t" \
    "s_waitcnt lgkmcnt(0)\n\t" \
    "s_barrier\n\t" \
    "buffer_load_dwordx4 v[220:223], v8, %[rs], %[m0] offen sc1\n\t" \
    "buffer_load_dwordx4 v[224:227], v8, %[rs], %[m1] offen sc1\n\t" \
    "PM_%=:\n\t" \
    "s_waitcnt vmcnt(1)\n\t" \
    "ds_write_b128 v8, v[220:223] offset:0\n\t" \
    "v_max3_u32 v252, v220, v221, v222\n\t" \
    "s_waitcnt vmcnt(0)\n\t" \
    "ds_write_b128 v8, v[224:227] offset:4096\n\t" \
    "v_max3_u32 v252, v252, v223, v224\n\t" \
    "v_max3_u32 v252, v252, v225, v226\n\t" \
    "v_max_u32 v252, v252, v227\n\t" \
    "v_cmp_eq_u32 vcc, -1, v252\n\t" \
    "s_cbranch_vccz PD_%=\n\t" \
    "s_sub_u32 %[lim], %[lim], 1\n\t" \
    "s_cmp_gt_i32 %[lim], 0\n\t" \
    "s_cbranch_scc0 PD_%=\n\t" \
    "s_sleep " STR(CADM) "\n\t" \
    "buffer_load_dwordx4 v[220:223], v8, %[rs], %[m0] offen sc1\n\t" \
    "buffer_load_dwordx4 v[224:227], v8, %[rs], %[m1] offen sc1\n\t" \
    "s_branch PM_%=\n\t" \
    "PD_%=:\n\t" \
    GSA(0) \
    "v_smfmac_f32_16x16x64_bf16 v[0:3], v[220:223], v[236:243], v10\n\t" \
    "v_xor_b32 v9, 16384, v9\n\t" \
    "ds_read_b128 v[236:239], v9 offset:0\n\t" \
    "ds_read_b128 v[240:243], v9 offset:1024\n\t" \
    "v_smfmac_f32_16x16x64_bf16 v[4:7], v[220:223], v[244:251], v10\n\t" \
    "ds_read_b128 v[244:247], v9 offset:2048\n\t" \
    "ds_read_b128 v[248:251], v9 offset:3072\n\t" \
    "s_waitcnt lgkmcnt(4)\n\t" \
    "s_barrier\n\t" \
    "ds_read_b128 v[228:231], v8 offset:8192\n\t" \
    "ds_read_b128 v[232:235], v8 offset:12288\n\t" \
    "v_xor_b32 v8, 1024, v8\n\t" \
    "ds_read_b128 v[220:223], v8 offset:4096\n\t" \
    "v_smfmac_f32_16x16x64_bf16 v[0:3], v[224:227], v[28:35], v10\n\t" \
    "v_smfmac_f32_16x16x64_bf16 v[4:7], v[224:227], v[36:43], v10\n\t" \
    "ds_read_b128 v[224:227], v8 offset:0\n\t" \
    "s_waitcnt lgkmcnt(3)\n\t" \
    "v_smfmac_f32_16x16x64_bf16 v[0:3], v[228:231], v[92:99], v10\n\t" \
    "v_smfmac_f32_16x16x64_bf16 v[4:7], v[228:231], v[100:107], v10\n\t" \
    "ds_read_b128 v[228:231], v8 offset:8192\n\t" \
    "s_waitcnt lgkmcnt(3)\n\t" \
    "v_smfmac_f32_16x16x64_bf16 v[0:3], v[232:235], v[156:163], v10\n\t" \
    "v_smfmac_f32_16x16x64_bf16 v[4:7], v[232:235], v[164:171], v10\n\t" \
    "ds_read_b128 v[232:235], v8 offset:12288\n\t" \
    "s_waitcnt lgkmcnt(3)\n\t" \
    "v_smfmac_f32_16x16x64_bf16 v[0:3], v[220:223], v[44:51], v10\n\t" \
    "v_smfmac_f32_16x16x64_bf16 v[4:7], v[220:223], v[52:59], v10\n\t" \
    "v_xor_b32 v8, 3072, v8\n\t" \
    "ds_read_b128 v[220:223], v8 offset:4096\n\t" \
    "s_waitcnt lgkmcnt(3)\n\t" \
    "v_smfmac_f32_16x16x64_bf16 v[0:3], v[224:227], v[236:243], v10\n\t" \
    "v_xor_b32 v9, 49152, v9\n\t" \
    "ds_read_b128 v[236:239], v9 offset:0\n\t" \
    "v_smfmac_f32_16x16x64_bf16 v[4:7], v[224:227], v[244:251], v10\n\t" \
    "ds_read_b128 v[240:243], v9 offset:1024\n\t" \
    "s_waitcnt lgkmcnt(4)\n\t" \
    "v_smfmac_f32_16x16x64_bf16 v[0:3], v[228:231], v[108:115], v10\n\t" \
    "ds_read_b128 v[224:227], v8 offset:8192\n\t" \
    "v_smfmac_f32_16x16x64_bf16 v[4:7], v[228:231], v[116:123], v10\n\t" \
    "ds_read_b128 v[244:247], v9 offset:2048\n\t" \
    "s_waitcnt lgkmcnt(5)\n\t" \
    "v_smfmac_f32_16x16x64_bf16 v[0:3], v[232:235], v[172:179], v10\n\t" \
    "ds_read_b128 v[228:231], v8 offset:0\n\t" \
    "v_smfmac_f32_16x16x64_bf16 v[4:7], v[232:235], v[180:187], v10\n\t" \
    "ds_read_b128 v[248:251], v9 offset:3072\n\t" \
    "s_waitcnt lgkmcnt(6)\n\t" \
    "v_smfmac_f32_16x16x64_bf16 v[0:3], v[220:223], v[60:67], v10\n\t" \
    "ds_read_b128 v[232:235], v8 offset:12288\n\t" \
    "v_smfmac_f32_16x16x64_bf16 v[4:7], v[220:223], v[68:75], v10\n\t" \
    "v_xor_b32 v8, 1024, v8\n\t" \
    "ds_read_b128 v[220:223], v8 offset:4096\n\t" \
    "s_waitcnt lgkmcnt(5)\n\t" \
    "v_smfmac_f32_16x16x64_bf16 v[0:3], v[224:227], v[124:131], v10\n\t" \
    "v_smfmac_f32_16x16x64_bf16 v[4:7], v[224:227], v[132:139], v10\n\t" \
    "ds_read_b128 v[224:227], v8 offset:8192\n\t" \
    "s_waitcnt lgkmcnt(3)\n\t" \
    "v_smfmac_f32_16x16x64_bf16 v[0:3], v[228:231], v[236:243], v10\n\t" \
    "v_xor_b32 v9, 16384, v9\n\t" \
    "ds_read_b128 v[236:239], v9 offset:0\n\t" \
    "v_smfmac_f32_16x16x64_bf16 v[4:7], v[228:231], v[244:251], v10\n\t" \
    "ds_read_b128 v[240:243], v9 offset:1024\n\t" \
    "s_waitcnt lgkmcnt(4)\n\t" \
    "v_smfmac_f32_16x16x64_bf16 v[0:3], v[232:235], v[188:195], v10\n\t" \
    "ds_read_b128 v[228:231], v8 offset:12288\n\t" \
    "v_smfmac_f32_16x16x64_bf16 v[4:7], v[232:235], v[196:203], v10\n\t" \
    "ds_read_b128 v[244:247], v9 offset:2048\n\t" \
    "s_waitcnt lgkmcnt(5)\n\t" \
    "v_smfmac_f32_16x16x64_bf16 v[0:3], v[220:223], v[76:83], v10\n\t" \
    "ds_read_b128 v[232:235], v8 offset:0\n\t" \
    "v_xor_b32 v8, 3072, v8\n\t" \
    "v_smfmac_f32_16x16x64_bf16 v[4:7], v[220:223], v[84:91], v10\n\t" \
    "ds_read_b128 v[248:251], v9 offset:3072\n\t" \
    "s_waitcnt lgkmcnt(6)\n\t" \
    "v_smfmac_f32_16x16x64_bf16 v[0:3], v[224:227], v[140:147], v10\n\t" \
    "ds_read_b128 v[220:223], v12 offset:24576\n\t" \
    "v_smfmac_f32_16x16x64_bf16 v[4:7], v[224:227], v[148:155], v10\n\t" \
    "ds_read_b128 v[224:227], v12 offset:28672\n\t" \
    "s_waitcnt lgkmcnt(5)\n\t" \
    "v_smfmac_f32_16x16x64_bf16 v[0:3], v[228:231], v[204:211], v10\n\t" \
    "v_smfmac_f32_16x16x64_bf16 v[4:7], v[228:231], v[212:219], v10\n\t" \
    "ds_read_b128 v[228:231], v12 offset:25600\n\t" \
    "s_waitcnt lgkmcnt(3)\n\t" \
    "v_smfmac_f32_16x16x64_bf16 v[0:3], v[232:235], v[236:243], v10\n\t" \
    "v_xor_b32 v9, 49152, v9\n\t" \
    "v_smfmac_f32_16x16x64_bf16 v[4:7], v[232:235], v[244:251], v10\n\t" \
    VLD(4) \
    "s_nop 5\n\t" \
    "v_add_f32 v0, v0, v1\n\t" \
    "v_add_f32 v2, v2, v3\n\t" \
    "v_add_f32 v4, v4, v5\n\t" \
    "v_add_f32 v6, v6, v7\n\t" \
    "v_mul_f32 v0, %[sl], v0\n\t" \
    "v_mul_f32 v2, %[sl], v2\n\t" \
    "v_mul_f32 v4, %[sl], v4\n\t" \
    "v_mul_f32 v6, %[sl], v6\n\t" \
    "v_cvt_pk_bf16_f32 v0, v0, v4\n\t" \
    "v_cvt_pk_bf16_f32 v2, v2, v6\n\t" \
    "ds_write2_b32 v11, v0, v2 offset0:0 offset1:4\n\t" \
    "s_mov_b64 exec, %[onej]\n\t" \
    "ds_write_b32 v16, v253\n\t" \
    "s_mov_b64 exec, -1\n\t" \
    GSA(1) \
    "s_setprio 0\n\t" \
    "ds_read_b128 v[232:235], v12 offset:29696\n\t" \
    "s_nop 1\n\t" \
    "ds_read_b128 v[0:3], v12 offset:26624\n\t" \
    "s_nop 1\n\t" \
    "ds_read_b128 v[4:7], v12 offset:27648\n\t" \
    "s_nop 1\n\t" \
    "ds_read_b32 v252, v16\n\t" \
    "ds_read_b128 v[236:239], v14 offset:0\n\t" \
    "s_nop 1\n\t" \
    "ds_read_b128 v[240:243], v14 offset:512\n\t" \
    "s_nop 1\n\t" \
    "ds_read_b128 v[244:247], v14 offset:1024\n\t" \
    "s_nop 1\n\t" \
    "s_setprio 2\n\t" \
    VLD(3) \
    "s_setprio 0\n\t" \
    "ds_read_b128 v[248:251], v14 offset:1536\n\t" \
    "s_nop 1\n\t" \
    "s_setprio 2\n\t" \
    "s_waitcnt lgkmcnt(4)\n\t" \
    "v_cmp_ne_u32 vcc, v253, v252\n\t" \
    "s_cbranch_vccnz RP_%=\n\t" \
    "CP_%=:\n\t" \
    "s_setprio 2\n\t" \
    VLD(0) \
    "s_waitcnt lgkmcnt(2)\n\t" \
    "v_mfma_f32_16x16x32_bf16 v[220:223], v[220:223], v[236:239], 0\n\t" \
    "v_mfma_f32_16x16x32_bf16 v[224:227], v[224:227], v[236:239], 0\n\t" \
    "v_mfma_f32_16x16x32_bf16 v[220:223], v[228:231], v[240:243], v[220:223]\n\t" \
    "ds_read_b128 v[228:231], v12 offset:30720\n\t" \
    "v_mfma_f32_16x16x32_bf16 v[224:227], v[232:235], v[240:243], v[224:227]\n\t" \
    "ds_read_b128 v[232:235], v12 offset:31744\n\t" \
    "s_setprio " STR(QPRIO) "\n\t" \
    VLD(1) \
    "s_waitcnt lgkmcnt(2)\n\t" \
    "v_mfma_f32_16x16x32_bf16 v[220:223], v[0:3], v[244:247], v[220:223]\n\t" \
    "v_mfma_f32_16x16x32_bf16 v[220:223], v[4:7], v[248:251], v[220:223]\n\t" \
    "s_waitcnt lgkmcnt(1)\n\t" \
    "v_mfma_f32_16x16x32_bf16 v[224:227], v[228:231], v[244:247], v[224:227]\n\t" \
    "s_waitcnt lgkmcnt(0)\n\t" \
    "v_mfma_f32_16x16x32_bf16 v[224:227], v[232:235], v[248:251], v[224:227]\n\t" \
    GSA(2) \
    "s_setprio 2\n\t" \
    "s_nop 2\n\t" \
    "v_exp_f32 v220, v220\n\t" \
    "v_exp_f32 v221, v221\n\t" \
    "v_exp_f32 v222, v222\n\t" \
    "v_exp_f32 v223, v223\n\t" \
    "v_exp_f32 v224, v224\n\t" \
    "v_exp_f32 v225, v225\n\t" \
    "v_exp_f32 v226, v226\n\t" \
    "v_exp_f32 v227, v227\n\t" \
    "ds_read_b32 v252, v16 offset:16\n\t" \
    "ds_read_b128 v[0:3], v12 offset:22528\n\t" \
    "ds_read_b128 v[4:7], v12 offset:23552\n\t" \
    VLD(2) \
    "v_cvt_pk_bf16_f32 v220, v220, v221\n\t" \
    "v_cvt_pk_bf16_f32 v221, v222, v223\n\t" \
    "v_cvt_pk_bf16_f32 v222, v224, v225\n\t" \
    "v_cvt_pk_bf16_f32 v223, v226, v227\n\t" \
    "v_mov_b32 v224, 0\n\t" \
    "v_mov_b32 v225, 0\n\t" \
    "v_mfma_f32_16x16x32_bf16 v[228:231], v[20:23], v[220:223], 0\n\t" \
    "v_mov_b32 v226, 0\n\t" \
    "v_mov_b32 v227, 0\n\t" \
    "v_mfma_f32_16x16x32_bf16 v[232:235], v[24:27], v[220:223], 0\n\t" \
    "s_waitcnt lgkmcnt(2)\n\t" \
    "v_cmp_ne_u32 vcc, %[tag], v252\n\t" \
    "s_cbranch_vccnz RK_%=\n\t" \
    "CK_%=:\n\t" \
    "s_waitcnt lgkmcnt(1)\n\t" \
    "v_smfmac_f32_16x16x64_bf16 v[224:227], v[0:3], v[236:243], v10\n\t" \
    "s_waitcnt lgkmcnt(0)\n\t" \
    "v_smfmac_f32_16x16x64_bf16 v[224:227], v[4:7], v[244:251], v10\n\t" \
    "v_mov_b32 v236, %[one]\n\t" \
    "v_mov_b32 v237, %[one]\n\t" \
    "v_mov_b32 v238, %[one]\n\t" \
    "v_mov_b32 v239, %[one]\n\t" \
    "v_mov_b32 v1, 0\n\t" \
    "v_mov_b32 v2, 0\n\t" \
    "v_mfma_f32_16x16x32_bf16 v[240:243], v[236:239], v[220:223], 0\n\t" \
    "s_nop 0\n\t" \
    "v_add_f32 v4, v224, v225\n\t" \
    "v_add_f32 v5, v226, v227\n\t" \
    "v_exp_f32 v4, v4\n\t" \
    "v_exp_f32 v5, v5\n\t" \
    "s_nop 0\n\t" \
    "v_cvt_pk_bf16_f32 v0, v4, v5\n\t" \
    "v_cvt_pk_bf16_f32 v3, v4, v5\n\t" \
    GSA(3) \
    "CM_%=:\n\t" \
    "s_nop 0\n\t" \
    "v_mfma_f32_16x16x16_bf16 v[240:243], v[236:237], v[0:1], v[240:243]\n\t" \
    "s_waitcnt vmcnt(0)\n\t" \
    "v_mfma_f32_16x16x16_bf16 v[228:231], v[254:255], v[0:1], v[228:231]\n\t" \
    "v_max_u32 v7, v254, v255\n\t" \
    "v_mfma_f32_16x16x16_bf16 v[232:235], v[254:255], v[2:3], v[232:235]\n\t" \
    "v_cmp_eq_u32 s[88:89], -1, v7\n\t" \
    "s_not_b64 exec, %[hi]\n\t" \
    "s_nop 1\n\t" \
    "v_rcp_f32 v252, v240\n\t" \
    "v_subrev_u32 v253, 0x21800000, v240\n\t" \
    "v_cmp_gt_u32 vcc, v253, %[rg]\n\t" \
    "v_pk_mul_f32 v[228:229], v[228:229], v[252:253] op_sel_hi:[1,0]\n\t" \
    "v_pk_mul_f32 v[230:231], v[230:231], v[252:253] op_sel_hi:[1,0]\n\t" \
    "v_pk_mul_f32 v[232:233], v[232:233], v[252:253] op_sel_hi:[1,0]\n\t" \
    "v_pk_mul_f32 v[234:235], v[234:235], v[252:253] op_sel_hi:[1,0]\n\t" \
    "s_or_b64 vcc, vcc, s[88:89]\n\t" \
    "v_cvt_pk_bf16_f32 v4, v228, v229\n\t" \
    "v_cvt_pk_bf16_f32 v5, v230, v231\n\t" \
    "v_cvt_pk_bf16_f32 v6, v232, v233\n\t" \
    "v_cvt_pk_bf16_f32 v7, v234, v235\n\t" \
    "s_cbranch_scc1 FX_%=\n\t" \
    "buffer_store_dwordx4 v[4:7], v18, %[rs], %[sb] offen sc1\n\t" \
    "s_memrealtime %[tp]\n\t" \
    DGA \
    GSA(4) \
    "v_max3_u32 v252, v4, v5, v6\n\t" \
    "v_max_u32 v252, v252, v7\n\t" \
    "v_cmp_eq_u32 vcc, -1, v252\n\t" \
    "s_cbranch_vccnz RC_%=\n\t" \
    "CC_%=:\n\t" \
    "s_mov_b64 exec, -1\n\t" \
    "v_add_u32 v252, %[vd], v12\n\t" \
    "v_add_u32 v253, %[md], v17\n\t" \
    "ds_write_b128 v252, v[20:23]\n\t" \
    "ds_write_b128 v252, v[24:27] offset:1024\n\t" \
    "ds_write2st64_b32 v253, v254, v255 offset0:0 offset1:1\n\t" \
    "s_waitcnt lgkmcnt(0)\n\t" \
    "s_cmp_lg_u32 %[sc], 3\n\t" \
    "s_cbranch_scc1 NH_%=\n\t" \
    "buffer_load_dwordx2 v[0:1], off, %[rs], %[fl] sc1\n\t" \
    "s_barrier\n\t" \
    "s_waitcnt vmcnt(0)\n\t" \
    "v_readfirstlane_b32 %[pt], v0\n\t" \
    "v_readfirstlane_b32 %[pd], v1\n\t" \
    "s_nop 3\n\t" \
    "s_branch E_%=\n\t" \
    "NH_%=:\n\t" \
    "s_barrier\n\t" \
    "s_branch E_%=\n\t" \
    "RP_%=:\n\t" \
    "s_waitcnt lgkmcnt(0)\n\t" \
    "ds_read_b32 v252, v16\n\t" \
    "ds_read_b128 v[236:239], v14 offset:0\n\t" \
    "ds_read_b128 v[240:243], v14 offset:512\n\t" \
    "ds_read_b128 v[244:247], v14 offset:1024\n\t" \
    "ds_read_b128 v[248:251], v14 offset:1536\n\t" \
    "s_sub_u32 %[lim], %[lim], " STR(DEC1) "\n\t" \
    "s_cmp_gt_i32 %[lim], 0\n\t" \
    "s_waitcnt lgkmcnt(4)\n\t" \
    "s_cbranch_scc0 CP_%=\n\t" \
    "v_cmp_ne_u32 vcc, %[tag], v252\n\t" \
    "s_cbranch_vccnz RP_%=\n\t" \
    "s_branch CP_%=\n\t" \
    "RK_%=:\n\t" \
    "s_waitcnt lgkmcnt(0)\n\t" \
    "ds_read_b32 v252, v16 offset:16\n\t" \
    "ds_read_b128 v[0:3], v12 offset:22528\n\t" \
    "ds_read_b128 v[4:7], v12 offset:23552\n\t" \
    "s_sub_u32 %[lim], %[lim], " STR(DEC1) "\n\t" \
    "s_cmp_gt_i32 %[lim], 0\n\t" \
    "s_waitcnt lgkmcnt(2)\n\t" \
    "s_cbranch_scc0 CK_%=\n\t" \
    "v_cmp_ne_u32 vcc, %[tag], v252\n\t" \
    "s_cbranch_vccnz RK_%=\n\t" \
    "s_branch CK_%=\n\t" \
    "RC_%=:\n\t" \
    "v_min_u32 v4, -2, v4\n\t" \
    "v_min_u32 v5, -2, v5\n\t" \
    "v_min_u32 v6, -2, v6\n\t" \
    "v_min_u32 v7, -2, v7\n\t" \
    "buffer_store_dwordx4 v[4:7], v18, %[rs], %[sb] offen sc1\n\t" \
    "s_branch CC_%=\n\t" \
    "FX_%=:\n\t" \
    "s_mov_b64 exec, -1\n\t" \
    "s_cmp_eq_u64 s[88:89], 0\n\t" \
    "s_cbranch_scc1 RM_%=\n\t" \
    "buffer_load_dword v254, v17, %[rs], %[sb] offen sc1\n\t" \
    "buffer_load_dword v255, v17, %[rs], %[sb] offen offset:256 sc1\n\t" \
    "v_mfma_f32_16x16x32_bf16 v[228:231], v[20:23], v[220:223], 0\n\t" \
    "v_mfma_f32_16x16x32_bf16 v[232:235], v[24:27], v[220:223], 0\n\t" \
    "v_mfma_f32_16x16x32_bf16 v[240:243], v[236:239], v[220:223], 0\n\t" \
    "s_sub_u32 %[lim], %[lim], " STR(DEC2) "\n\t" \
    "s_cmp_gt_i32 %[lim], 0\n\t" \
    "s_cbranch_scc1 CM_%=\n\t" \
    "s_waitcnt vmcnt(0)\n\t" \
    "v_mov_b32 v254, 0\n\t" \
    "v_mov_b32 v255, 0\n\t" \
    "s_branch CM_%=\n\t" \
    "RM_%=:\n\t" \
    "s_mov_b32 %[rg], -1\n\t" \
    "s_nop 7\n\t" \
    "v_add_f32 v4, v224, v225\n\t" \
    "v_add_f32 v5, v226, v227\n\t" \
    "ds_read_b128 v[236:239], v14 offset:0\n\t" \
    "ds_read_b128 v[240:243], v14 offset:512\n\t" \
    "ds_read_b128 v[244:247], v14 offset:1024\n\t" \
    "ds_read_b128 v[248:251], v14 offset:1536\n\t" \
    "ds_read_b128 v[232:235], v12 offset:24576\n\t" \
    "s_waitcnt lgkmcnt(0)\n\t" \
    "v_mfma_f32_16x16x32_bf16 v[220:223], v[232:235], v[236:239], 0\n\t" \
    "ds_read_b128 v[232:235], v12 offset:25600\n\t" \
    "s_waitcnt lgkmcnt(0)\n\t" \
    "v_mfma_f32_16x16x32_bf16 v[220:223], v[232:235], v[240:243], v[220:223]\n\t" \
    "ds_read_b128 v[232:235], v12 offset:26624\n\t" \
    "s_waitcnt lgkmcnt(0)\n\t" \
    "v_mfma_f32_16x16x32_bf16 v[220:223], v[232:235], v[244:247], v[220:223]\n\t" \
    "ds_read_b128 v[232:235], v12 offset:27648\n\t" \
    "s_waitcnt lgkmcnt(0)\n\t" \
    "v_mfma_f32_16x16x32_bf16 v[220:223], v[232:235], v[248:251], v[220:223]\n\t" \
    "ds_read_b128 v[232:235], v12 offset:28672\n\t" \
    "s_waitcnt lgkmcnt(0)\n\t" \
    "v_mfma_f32_16x16x32_bf16 v[224:227], v[232:235], v[236:239], 0\n\t" \
    "ds_read_b128 v[232:235], v12 offset:29696\n\t" \
    "s_waitcnt lgkmcnt(0)\n\t" \
    "v_mfma_f32_16x16x32_bf16 v[224:227], v[232:235], v[240:243], v[224:227]\n\t" \
    "ds_read_b128 v[232:235], v12 offset:30720\n\t" \
    "s_waitcnt lgkmcnt(0)\n\t" \
    "v_mfma_f32_16x16x32_bf16 v[224:227], v[232:235], v[244:247], v[224:227]\n\t" \
    "ds_read_b128 v[232:235], v12 offset:31744\n\t" \
    "s_waitcnt lgkmcnt(0)\n\t" \
    "v_mfma_f32_16x16x32_bf16 v[224:227], v[232:235], v[248:251], v[224:227]\n\t" \
    "v_max3_f32 v252, v220, v221, v222\n\t" \
    "s_nop 6\n\t" \
    "v_max3_f32 v253, v223, v224, v225\n\t" \
    "v_max3_f32 v252, v252, v226, v227\n\t" \
    "v_max3_f32 v253, v253, v4, v5\n\t" \
    "v_max_f32 v252, v252, v253\n\t" \
    "v_mov_b32 v253, v252\n\t" \
    "v_mov_b32 v6, v252\n\t" \
    "s_nop 1\n\t" \
    "v_permlane16_swap_b32 v253, v6\n\t" \
    "v_max_f32 v252, v253, v6\n\t" \
    "v_max_f32 v253, v253, v6\n\t" \
    "s_nop 1\n\t" \
    "v_permlane32_swap_b32 v252, v253\n\t" \
    "v_max_f32 v252, v252, v253\n\t" \
    "v_sub_f32 v220, v220, v252\n\t" \
    "v_sub_f32 v221, v221, v252\n\t" \
    "v_sub_f32 v222, v222, v252\n\t" \
    "v_sub_f32 v223, v223, v252\n\t" \
    "v_sub_f32 v224, v224, v252\n\t" \
    "v_sub_f32 v225, v225, v252\n\t" \
    "v_sub_f32 v226, v226, v252\n\t" \
    "v_sub_f32 v227, v227, v252\n\t" \
    "v_sub_f32 v4, v4, v252\n\t" \
    "v_sub_f32 v5, v5, v252\n\t" \
    "v_exp_f32 v220, v220\n\t" \
    "v_exp_f32 v221, v221\n\t" \
    "v_exp_f32 v222, v222\n\t" \
    "v_exp_f32 v223, v223\n\t" \
    "v_exp_f32 v224, v224\n\t" \
    "v_exp_f32 v225, v225\n\t" \
    "v_exp_f32 v226, v226\n\t" \
    "v_exp_f32 v227, v227\n\t" \
    "v_cvt_pk_bf16_f32 v220, v220, v221\n\t" \
    "v_cvt_pk_bf16_f32 v221, v222, v223\n\t" \
    "v_cvt_pk_bf16_f32 v222, v224, v225\n\t" \
    "v_cvt_pk_bf16_f32 v223, v226, v227\n\t" \
    "v_mov_b32 v236, %[one]\n\t" \
    "v_mov_b32 v237, %[one]\n\t" \
    "v_mov_b32 v238, %[one]\n\t" \
    "v_mov_b32 v239, %[one]\n\t" \
    "v_mfma_f32_16x16x32_bf16 v[228:231], v[20:23], v[220:223], 0\n\t" \
    "v_mfma_f32_16x16x32_bf16 v[232:235], v[24:27], v[220:223], 0\n\t" \
    "v_mfma_f32_16x16x32_bf16 v[240:243], v[236:239], v[220:223], 0\n\t" \
    "v_exp_f32 v4, v4\n\t" \
    "v_exp_f32 v5, v5\n\t" \
    "s_nop 0\n\t" \
    "v_cvt_pk_bf16_f32 v0, v4, v5\n\t" \
    "v_cvt_pk_bf16_f32 v3, v4, v5\n\t" \
    "s_nop 1\n\t" \
    "s_branch CM_%=\n\t" \
    "E_%=:\n\t" \
    GSW \
    "s_nop 1\n\t" \
    : [lim] "+s"(lim), [rg] "+s"(rg), [tp] "=&s"(tp), [pt] "=&s"(pt), [pd] "=&s"(pd) DGO DGP GSO(0) GSO(1) GSO(2) GSO(3) GSO(4) \
    : "{v8}"(am_a), "{v9}"(t0_a), "{v10}"(idx), "{v11}"(iw_a), "{v12}"(ab_a), "{v14}"(qr_a), "{v16}"(fr_a), "{v17}"(vo_v), "{v18}"(oo_v), [rs] "s"(rs), [sb] "s"(sb), [tag] "s"(0xffffff81u + s), [hi] "s"(0xff00ff00ff00ff00ull), [tgt] "s"(tgt), [sc] "s"(wid), [onej] "s"(onej), [sl] "s"(0x3e0293ee), [one] "s"(0x3f803f80), [m0] "s"(sbm), [m1] "s"(sbm + 4096u), [vx] "s"(vxo), [fl] "s"(fb + FL_T(s + 1)), [vd] "s"(8192 + j * 2048), [md] "s"(md), "{v[28:35]}"(wr[0][0]), "{v[36:43]}"(wr[0][1]), "{v[44:51]}"(wr[1][0]), "{v[52:59]}"(wr[1][1]), "{v[60:67]}"(wr[2][0]), "{v[68:75]}"(wr[2][1]), "{v[76:83]}"(wr[3][0]), "{v[84:91]}"(wr[3][1]), "{v[92:99]}"(wr[4][0]), "{v[100:107]}"(wr[4][1]), "{v[108:115]}"(wr[5][0]), "{v[116:123]}"(wr[5][1]), "{v[124:131]}"(wr[6][0]), "{v[132:139]}"(wr[6][1]), "{v[140:147]}"(wr[7][0]), "{v[148:155]}"(wr[7][1]), "{v[156:163]}"(wr[8][0]), "{v[164:171]}"(wr[8][1]), "{v[172:179]}"(wr[9][0]), "{v[180:187]}"(wr[9][1]), "{v[188:195]}"(wr[10][0]), "{v[196:203]}"(wr[10][1]), "{v[204:211]}"(wr[11][0]), "{v[212:219]}"(wr[11][1]) \
    : "memory", "vcc", "scc", "s88", "s89", "s90", "s91", "s92", "s93", "s94", "s95", "v0", "v1", "v2", "v3", "v4", "v5", "v6", "v7", "v20", "v21", "v22", "v23", "v24", "v25", "v26", "v27", "v220", "v221", "v222", "v223", "v224", "v225", "v226", "v227", "v228", "v229", "v230", "v231", "v232", "v233", "v234", "v235", "v236", "v237", "v238", "v239", "v240", "v241", "v242", "v243", "v244", "v245", "v246", "v247", "v248", "v249", "v250", "v251", "v252", "v253", "v254", "v255")

#define A_MAIN_K() \
  asm volatile( \
    "ds_read_b128 v[236:239], v9 offset:0\n\t" \
    "ds_read_b128 v[240:243], v9 offset:1024\n\t" \
    "ds_read_b128 v[244:247], v9 offset:2048\n\t" \
    "ds_read_b128 v[248:251], v9 offset:3072\n\t" \
    "v_mov_b32 v0, 0\n\t" \
    "v_mov_b32 v1, 0\n\t" \
    "v_mov_b32 v2, 0\n\t" \
    "v_mov_b32 v3, 0\n\t" \
    "v_mov_b32 v4, 0\n\t" \
    "v_mov_b32 v5, 0\n\t" \
    "v_mov_b32 v6, 0\n\t" \
    "v_mov_b32 v7, 0\n\t" \
    "s_waitcnt lgkmcnt(0)\n\t" \
    "s_barrier\n\t" \
    "buffer_load_dwordx4 v[220:223], v8, %[rs], %[m0] offen sc1\n\t" \
    "buffer_load_dwordx4 v[224:227], v8, %[rs], %[m1] offen sc1\n\t" \
    GSA(3) \
    "v_mov_b32 v252, v252\n\t" \
    "s_nop 2\n\t" \
    "v_mov_b32 v252, v252\n\t" \
    "s_nop 2\n\t" \
    "v_mov_b32 v252, v252\n\t" \
    "s_nop 2\n\t" \
    "v_mov_b32 v252, v252\n\t" \
    "s_nop 2\n\t" \
    "v_mov_b32 v252, v252\n\t" \
    "s_nop 2\n\t" \
    "v_mov_b32 v252, v252\n\t" \
    "s_nop 2\n\t" \
    "v_mov_b32 v252, v252\n\t" \
    "s_nop 2\n\t" \
    "v_mov_b32 v252, v252\n\t" \
    "s_nop 2\n\t" \
    "v_mov_b32 v252, v252\n\t" \
    "s_nop 2\n\t" \
    "v_mov_b32 v252, v252\n\t" \
    "s_nop 2\n\t" \
    "v_mov_b32 v252, v252\n\t" \
    "s_nop 2\n\t" \
    "v_mov_b32 v252, v252\n\t" \
    "s_nop 2\n\t" \
    "v_mov_b32 v252, v252\n\t" \
    "s_nop 2\n\t" \
    "v_mov_b32 v252, v252\n\t" \
    "s_nop 2\n\t" \
    "v_mov_b32 v252, v252\n\t" \
    "s_nop 2\n\t" \
    "v_mov_b32 v252, v252\n\t" \
    "s_nop 2\n\t" \
    "v_mov_b32 v252, v252\n\t" \
    "s_nop 2\n\t" \
    "v_mov_b32 v252, v252\n\t" \
    "s_nop 2\n\t" \
    "v_mov_b32 v252, v252\n\t" \
    "s_nop 2\n\t" \
    "v_mov_b32 v252, v252\n\t" \
    "s_nop 2\n\t" \
    "v_mov_b32 v252, v252\n\t" \
    "s_nop 2\n\t" \
    "v_mov_b32 v252, v252\n\t" \
    "s_nop 2\n\t" \
    "v_mov_b32 v252, v252\n\t" \
    "s_nop 2\n\t" \
    "v_mov_b32 v252, v252\n\t" \
    "s_nop 2\n\t" \
    "v_mov_b32 v252, v252\n\t" \
    "s_nop 2\n\t" \
    "v_mov_b32 v252, v252\n\t" \
    "s_nop 2\n\t" \
    "v_mov_b32 v252, v252\n\t" \
    "s_nop 2\n\t" \
    "v_mov_b32 v252, v252\n\t" \
    "s_nop 2\n\t" \
    "v_mov_b32 v252, v252\n\t" \
    "s_nop 2\n\t" \
    "v_mov_b32 v252, v252\n\t" \
    "s_nop 2\n\t" \
    "PM_%=:\n\t" \
    "s_waitcnt vmcnt(1)\n\t" \
    "ds_write_b128 v8, v[220:223] offset:8192\n\t" \
    "v_max3_u32 v252, v220, v221, v222\n\t" \
    "s_waitcnt vmcnt(0)\n\t" \
    "ds_write_b128 v8, v[224:227] offset:12288\n\t" \
    "v_max3_u32 v252, v252, v223, v224\n\t" \
    "v_max3_u32 v252, v252, v225, v226\n\t" \
    "v_max_u32 v252, v252, v227\n\t" \
    "v_cmp_eq_u32 vcc, -1, v252\n\t" \
    "s_cbranch_vccz PD_%=\n\t" \
    "s_sub_u32 %[lim], %[lim], 1\n\t" \
    "s_cmp_gt_i32 %[lim], 0\n\t" \
    "s_cbranch_scc0 PD_%=\n\t" \
    "s_sleep " STR(CADM) "\n\t" \
    "buffer_load_dwordx4 v[220:223], v8, %[rs], %[m0] offen sc1\n\t" \
    "buffer_load_dwordx4 v[224:227], v8, %[rs], %[m1] offen sc1\n\t" \
    "s_branch PM_%=\n\t" \
    "PD_%=:\n\t" \
    GSA(0) \
    "v_smfmac_f32_16x16x64_bf16 v[0:3], v[220:223], v[236:243], v10\n\t" \
    "v_xor_b32 v9, 16384, v9\n\t" \
    "v_smfmac_f32_16x16x64_bf16 v[4:7], v[220:223], v[244:251], v10\n\t" \
    "s_waitcnt lgkmcnt(0)\n\t" \
    "s_barrier\n\t" \
    DGA \
    "s_cmp_lg_u32 %[st], 63\n\t" \
    "s_cbranch_scc1 NL_%=\n\t" \
    "s_mov_b64 exec, %[fm]\n\t" \
    "v_mov_b32 v252, %[st]\n\t" \
    "v_mov_b32 v253, %[tag]\n\t" \
    "v_mov_b32 v254, 0\n\t" \
    "buffer_store_dwordx2 v[252:253], v254, %[rs], %[po] offen sc1\n\t" \
    "s_mov_b64 exec, -1\n\t" \
    "NL_%=:\n\t" \
    "ds_read_b128 v[236:239], v9 offset:0\n\t" \
    "ds_read_b128 v[240:243], v9 offset:1024\n\t" \
    "ds_read_b128 v[244:247], v9 offset:2048\n\t" \
    "ds_read_b128 v[248:251], v9 offset:3072\n\t" \
    "ds_read_b128 v[228:231], v8 offset:0\n\t" \
    "ds_read_b128 v[232:235], v8 offset:4096\n\t" \
    "v_smfmac_f32_16x16x64_bf16 v[0:3], v[224:227], v[148:155], v10\n\t" \
    "v_xor_b32 v8, 1024, v8\n\t" \
    "ds_read_b128 v[220:223], v8 offset:8192\n\t" \
    "v_smfmac_f32_16x16x64_bf16 v[4:7], v[224:227], v[156:163], v10\n\t" \
    "ds_read_b128 v[224:227], v8 offset:12288\n\t" \
    "buffer_store_dwordx4 v[212:215], v14, %[rs], %[ox] offen offset:0 sc1\n\t" \
    "buffer_store_dwordx4 v[216:219], v14, %[rs], %[ox] offen offset:512 sc1\n\t" \
    "s_waitcnt lgkmcnt(3)\n\t" \
    "v_smfmac_f32_16x16x64_bf16 v[0:3], v[228:231], v[20:27], v10\n\t" \
    "v_smfmac_f32_16x16x64_bf16 v[4:7], v[228:231], v[28:35], v10\n\t" \
    "ds_read_b128 v[228:231], v8 offset:0\n\t" \
    "s_waitcnt lgkmcnt(3)\n\t" \
    "v_smfmac_f32_16x16x64_bf16 v[0:3], v[232:235], v[84:91], v10\n\t" \
    "v_smfmac_f32_16x16x64_bf16 v[4:7], v[232:235], v[92:99], v10\n\t" \
    "ds_read_b128 v[232:235], v8 offset:4096\n\t" \
    "s_waitcnt lgkmcnt(3)\n\t" \
    "v_smfmac_f32_16x16x64_bf16 v[0:3], v[220:223], v[236:243], v10\n\t" \
    "v_xor_b32 v9, 49152, v9\n\t" \
    "ds_read_b128 v[236:239], v9 offset:0\n\t" \
    "v_smfmac_f32_16x16x64_bf16 v[4:7], v[220:223], v[244:251], v10\n\t" \
    "ds_read_b128 v[240:243], v9 offset:1024\n\t" \
    "s_waitcnt lgkmcnt(4)\n\t" \
    "v_smfmac_f32_16x16x64_bf16 v[0:3], v[224:227], v[164:171], v10\n\t" \
    "v_xor_b32 v8, 3072, v8\n\t" \
    "ds_read_b128 v[220:223], v8 offset:12288\n\t" \
    "v_smfmac_f32_16x16x64_bf16 v[4:7], v[224:227], v[172:179], v10\n\t" \
    "ds_read_b128 v[244:247], v9 offset:2048\n\t" \
    "s_waitcnt lgkmcnt(5)\n\t" \
    "v_smfmac_f32_16x16x64_bf16 v[0:3], v[228:231], v[36:43], v10\n\t" \
    "ds_read_b128 v[224:227], v8 offset:0\n\t" \
    "v_smfmac_f32_16x16x64_bf16 v[4:7], v[228:231], v[44:51], v10\n\t" \
    "ds_read_b128 v[248:251], v9 offset:3072\n\t" \
    "s_waitcnt lgkmcnt(6)\n\t" \
    "v_smfmac_f32_16x16x64_bf16 v[0:3], v[232:235], v[100:107], v10\n\t" \
    "ds_read_b128 v[228:231], v8 offset:8192\n\t" \
    "v_smfmac_f32_16x16x64_bf16 v[4:7], v[232:235], v[108:115], v10\n\t" \
    "ds_read_b128 v[232:235], v8 offset:4096\n\t" \
    "s_waitcnt lgkmcnt(5)\n\t" \
    "v_smfmac_f32_16x16x64_bf16 v[0:3], v[220:223], v[180:187], v10\n\t" \
    "v_smfmac_f32_16x16x64_bf16 v[4:7], v[220:223], v[188:195], v10\n\t" \
    "v_xor_b32 v8, 1024, v8\n\t" \
    "ds_read_b128 v[220:223], v8 offset:12288\n\t" \
    "s_waitcnt lgkmcnt(4)\n\t" \
    "v_smfmac_f32_16x16x64_bf16 v[0:3], v[224:227], v[52:59], v10\n\t" \
    "v_smfmac_f32_16x16x64_bf16 v[4:7], v[224:227], v[60:67], v10\n\t" \
    "ds_read_b128 v[224:227], v8 offset:0\n\t" \
    "s_waitcnt lgkmcnt(3)\n\t" \
    "v_smfmac_f32_16x16x64_bf16 v[0:3], v[228:231], v[236:243], v10\n\t" \
    "v_xor_b32 v9, 16384, v9\n\t" \
    "ds_read_b128 v[236:239], v9 offset:0\n\t" \
    "v_smfmac_f32_16x16x64_bf16 v[4:7], v[228:231], v[244:251], v10\n\t" \
    "ds_read_b128 v[240:243], v9 offset:1024\n\t" \
    "s_waitcnt lgkmcnt(4)\n\t" \
    "v_smfmac_f32_16x16x64_bf16 v[0:3], v[232:235], v[116:123], v10\n\t" \
    "ds_read_b128 v[228:231], v8 offset:4096\n\t" \
    "v_smfmac_f32_16x16x64_bf16 v[4:7], v[232:235], v[124:131], v10\n\t" \
    "ds_read_b128 v[244:247], v9 offset:2048\n\t" \
    "s_waitcnt lgkmcnt(5)\n\t" \
    "v_smfmac_f32_16x16x64_bf16 v[0:3], v[220:223], v[196:203], v10\n\t" \
    "ds_read_b128 v[232:235], v8 offset:8192\n\t" \
    "v_xor_b32 v8, 3072, v8\n\t" \
    "v_smfmac_f32_16x16x64_bf16 v[4:7], v[220:223], v[204:211], v10\n\t" \
    "ds_read_b128 v[248:251], v9 offset:3072\n\t" \
    "s_waitcnt lgkmcnt(6)\n\t" \
    "v_smfmac_f32_16x16x64_bf16 v[0:3], v[224:227], v[68:75], v10\n\t" \
    "v_smfmac_f32_16x16x64_bf16 v[4:7], v[224:227], v[76:83], v10\n\t" \
    "s_waitcnt lgkmcnt(3)\n\t" \
    "v_smfmac_f32_16x16x64_bf16 v[0:3], v[228:231], v[132:139], v10\n\t" \
    "v_smfmac_f32_16x16x64_bf16 v[4:7], v[228:231], v[140:147], v10\n\t" \
    "s_waitcnt lgkmcnt(0)\n\t" \
    "v_smfmac_f32_16x16x64_bf16 v[0:3], v[232:235], v[236:243], v10\n\t" \
    "v_xor_b32 v9, 49152, v9\n\t" \
    "v_smfmac_f32_16x16x64_bf16 v[4:7], v[232:235], v[244:251], v10\n\t" \
    "s_setprio 3\n\t" \
    "v_mov_b32 v253, %[stag]\n\t" \
    "s_nop 3\n\t" \
    "v_add_f32 v0, v0, v1\n\t" \
    "v_add_f32 v2, v2, v3\n\t" \
    "v_add_f32 v4, v4, v5\n\t" \
    "v_add_f32 v6, v6, v7\n\t" \
    "v_cvt_pk_bf16_f32 v0, v0, v4\n\t" \
    "v_cvt_pk_bf16_f32 v2, v2, v6\n\t" \
    "ds_write2_b32 v13, v0, v2 offset0:0 offset1:8\n\t" \
    "s_mov_b64 exec, %[onej]\n\t" \
    "ds_write_b32 v18, v253 offset:16\n\t" \
    "s_mov_b64 exec, -1\n\t" \
    "s_setprio 0\n\t" \
    "ds_write2_b32 v11, v0, v2 offset0:0 offset1:4\n\t" \
    "s_mov_b64 exec, %[onej]\n\t" \
    "ds_write_b32 v18, v253 offset:32\n\t" \
    "s_mov_b64 exec, -1\n\t" \
    GSA(1) \
    "s_setprio 0\n\t" \
    "buffer_load_dwordx4 v[236:239], v12, %[rs], %[qx] offen offset:0 sc1\n\t" \
    "buffer_load_dwordx4 v[240:243], v12, %[rs], %[qx] offen offset:1024 sc1\n\t" \
    "buffer_load_dwordx4 v[244:247], v12, %[rs], %[qx] offen offset:2048 sc1\n\t" \
    "buffer_load_dwordx4 v[248:251], v12, %[rs], %[qx] offen offset:3072 sc1\n\t" \
    "v_cmp_le_i32 s[72:73], 0, v16\n\t" \
    "v_cmp_le_i32 s[74:75], 1, v16\n\t" \
    "v_cmp_le_i32 s[76:77], 2, v16\n\t" \
    "v_cmp_le_i32 s[78:79], 3, v16\n\t" \
    "v_cmp_le_i32 s[80:81], 16, v16\n\t" \
    "v_cmp_le_i32 s[82:83], 17, v16\n\t" \
    "v_cmp_le_i32 s[84:85], 18, v16\n\t" \
    "v_cmp_le_i32 s[86:87], 19, v16\n\t" \
    "v_mov_b32 v254, v254\n\t" \
    "s_nop 2\n\t" \
    "v_mov_b32 v254, v254\n\t" \
    "s_nop 2\n\t" \
    "v_mov_b32 v254, v254\n\t" \
    "s_nop 2\n\t" \
    "v_mov_b32 v254, v254\n\t" \
    "s_nop 2\n\t" \
    "v_mov_b32 v254, v254\n\t" \
    "s_nop 2\n\t" \
    "v_mov_b32 v254, v254\n\t" \
    "s_nop 2\n\t" \
    "v_mov_b32 v254, v254\n\t" \
    "s_nop 2\n\t" \
    "v_mov_b32 v254, v254\n\t" \
    "s_nop 2\n\t" \
    "v_mov_b32 v254, v254\n\t" \
    "s_nop 2\n\t" \
    "v_mov_b32 v254, v254\n\t" \
    "s_nop 2\n\t" \
    "v_mov_b32 v254, v254\n\t" \
    "s_nop 2\n\t" \
    "v_mov_b32 v254, v254\n\t" \
    "s_nop 2\n\t" \
    "v_mov_b32 v254, v254\n\t" \
    "s_nop 2\n\t" \
    "v_mov_b32 v254, v254\n\t" \
    "s_nop 2\n\t" \
    "v_mov_b32 v254, v254\n\t" \
    "s_nop 2\n\t" \
    "v_mov_b32 v254, v254\n\t" \
    "s_nop 2\n\t" \
    "v_mov_b32 v254, v254\n\t" \
    "s_nop 2\n\t" \
    "v_mov_b32 v254, v254\n\t" \
    "s_nop 2\n\t" \
    "v_mov_b32 v254, v254\n\t" \
    "s_nop 2\n\t" \
    "v_mov_b32 v254, v254\n\t" \
    "s_nop 2\n\t" \
    "LW_%=:\n\t" \
    "ds_read_b32 v0, v18 offset:32\n\t" \
    "s_sub_u32 %[lim], %[lim], " STR(DEC1) "\n\t" \
    "s_cmp_gt_i32 %[lim], 0\n\t" \
    "v_mov_b32 v254, v254\n\t" \
    "s_nop 2\n\t" \
    "v_mov_b32 v254, v254\n\t" \
    "s_nop 2\n\t" \
    "v_mov_b32 v254, v254\n\t" \
    "s_nop 2\n\t" \
    "v_mov_b32 v254, v254\n\t" \
    "s_nop 2\n\t" \
    "v_mov_b32 v254, v254\n\t" \
    "s_nop 2\n\t" \
    "s_waitcnt lgkmcnt(0)\n\t" \
    "s_cbranch_scc0 CW_%=\n\t" \
    "v_cmp_ne_u32 vcc, v0, v253\n\t" \
    "s_cbranch_vccnz LW_%=\n\t" \
    "CW_%=:\n\t" \
    "ds_read_b128 v[212:215], v12 offset:24576\n\t" \
    "ds_read_b128 v[216:219], v12 offset:28672\n\t" \
    "ds_read_b128 v[220:223], v15 offset:0\n\t" \
    "ds_read_b128 v[224:227], v12 offset:25600\n\t" \
    "ds_read_b128 v[228:231], v12 offset:29696\n\t" \
    "v_add_u32 v253, %[se], v12\n\t" \
    "v_mov_b32 v254, v254\n\t" \
    "s_nop 2\n\t" \
    "v_mov_b32 v254, v254\n\t" \
    "s_nop 2\n\t" \
    "v_mov_b32 v254, v254\n\t" \
    "s_nop 2\n\t" \
    "v_mov_b32 v254, v254\n\t" \
    "s_nop 2\n\t" \
    "s_waitcnt vmcnt(0)\n\t" \
    "s_waitcnt lgkmcnt(4)\n\t" \
    "v_mfma_f32_16x16x32_bf16 v[0:3], v[212:215], v[236:239], 0\n\t" \
    "ds_read_b128 v[212:215], v15 offset:512\n\t" \
    "s_waitcnt lgkmcnt(4)\n\t" \
    "v_mfma_f32_16x16x32_bf16 v[4:7], v[216:219], v[236:239], 0\n\t" \
    "ds_read_b128 v[216:219], v12 offset:26624\n\t" \
    "s_waitcnt lgkmcnt(4)\n\t" \
    "v_mfma_f32_16x16x32_bf16 v[232:235], v[220:223], v[236:239], 0\n\t" \
    "ds_read_b128 v[220:223], v12 offset:30720\n\t" \
    "s_waitcnt lgkmcnt(4)\n\t" \
    "v_mfma_f32_16x16x32_bf16 v[0:3], v[224:227], v[240:243], v[0:3]\n\t" \
    "ds_read_b128 v[224:227], v15 offset:1024\n\t" \
    "s_waitcnt lgkmcnt(4)\n\t" \
    "v_mfma_f32_16x16x32_bf16 v[4:7], v[228:231], v[240:243], v[4:7]\n\t" \
    "ds_read_b128 v[228:231], v12 offset:27648\n\t" \
    "s_waitcnt lgkmcnt(4)\n\t" \
    "v_mfma_f32_16x16x32_bf16 v[232:235], v[212:215], v[240:243], v[232:235]\n\t" \
    "ds_read_b128 v[212:215], v12 offset:31744\n\t" \
    "s_waitcnt lgkmcnt(4)\n\t" \
    "v_mfma_f32_16x16x32_bf16 v[0:3], v[216:219], v[244:247], v[0:3]\n\t" \
    "ds_read_b128 v[216:219], v15 offset:1536\n\t" \
    "s_waitcnt lgkmcnt(4)\n\t" \
    "v_mfma_f32_16x16x32_bf16 v[4:7], v[220:223], v[244:247], v[4:7]\n\t" \
    "s_waitcnt lgkmcnt(3)\n\t" \
    "v_mfma_f32_16x16x32_bf16 v[232:235], v[224:227], v[244:247], v[232:235]\n\t" \
    "s_waitcnt lgkmcnt(2)\n\t" \
    "v_mfma_f32_16x16x32_bf16 v[0:3], v[228:231], v[248:251], v[0:3]\n\t" \
    "s_waitcnt lgkmcnt(1)\n\t" \
    "v_mfma_f32_16x16x32_bf16 v[4:7], v[212:215], v[248:251], v[4:7]\n\t" \
    "s_waitcnt lgkmcnt(0)\n\t" \
    "v_mfma_f32_16x16x32_bf16 v[232:235], v[216:219], v[248:251], v[232:235]\n\t" \
    "s_barrier\n\t" \
    GSA(4) \
    "ds_read_b128 v[236:239], v253 offset:8192\n\t" \
    "ds_read_b128 v[240:243], v253 offset:9216\n\t" \
    "ds_read_b128 v[244:247], v253 offset:10240\n\t" \
    "ds_read_b128 v[248:251], v253 offset:11264\n\t" \
    "ds_read2st64_b32 v[228:229], v17 offset0:0 offset1:1\n\t" \
    "ds_read2st64_b32 v[230:231], v17 offset0:2 offset1:3\n\t" \
    "s_mov_b64 exec, %[fm]\n\t" \
    "v_mov_b32 v220, %[st]\n\t" \
    "v_mov_b32 v221, %[tag]\n\t" \
    "v_mov_b32 v222, 0\n\t" \
    "buffer_store_dwordx2 v[220:221], v222, %[rs], %[po] offen sc1\n\t" \
    "s_mov_b64 exec, -1\n\t" \
    "v_mov_b32 v254, 0xff800000\n\t" \
    "v_cvt_pk_bf16_f32 v0, 0, v0\n\t" \
    "v_cvt_pk_bf16_f32 v1, 0, v1\n\t" \
    "v_cvt_pk_bf16_f32 v2, 0, v2\n\t" \
    "v_cvt_pk_bf16_f32 v3, 0, v3\n\t" \
    "v_cvt_pk_bf16_f32 v4, 0, v4\n\t" \
    "v_cvt_pk_bf16_f32 v5, 0, v5\n\t" \
    "v_cvt_pk_bf16_f32 v6, 0, v6\n\t" \
    "v_cvt_pk_bf16_f32 v7, 0, v7\n\t" \
    "v_cvt_pk_bf16_f32 v232, 0, v232\n\t" \
    "v_cvt_pk_bf16_f32 v233, 0, v233\n\t" \
    "v_mul_f32 v0, 0x3db504f3, v0\n\t" \
    "v_mul_f32 v1, 0x3db504f3, v1\n\t" \
    "v_mul_f32 v2, 0x3db504f3, v2\n\t" \
    "v_mul_f32 v3, 0x3db504f3, v3\n\t" \
    "v_mul_f32 v4, 0x3db504f3, v4\n\t" \
    "v_mul_f32 v5, 0x3db504f3, v5\n\t" \
    "v_mul_f32 v6, 0x3db504f3, v6\n\t" \
    "v_mul_f32 v7, 0x3db504f3, v7\n\t" \
    "v_mul_f32 v232, 0x3db504f3, v232\n\t" \
    "v_mul_f32 v233, 0x3db504f3, v233\n\t" \
    "v_cvt_pk_bf16_f32 v0, 0, v0\n\t" \
    "v_cvt_pk_bf16_f32 v1, 0, v1\n\t" \
    "v_cvt_pk_bf16_f32 v2, 0, v2\n\t" \
    "v_cvt_pk_bf16_f32 v3, 0, v3\n\t" \
    "v_cvt_pk_bf16_f32 v4, 0, v4\n\t" \
    "v_cvt_pk_bf16_f32 v5, 0, v5\n\t" \
    "v_cvt_pk_bf16_f32 v6, 0, v6\n\t" \
    "v_cvt_pk_bf16_f32 v7, 0, v7\n\t" \
    "v_cvt_pk_bf16_f32 v232, 0, v232\n\t" \
    "v_cvt_pk_bf16_f32 v233, 0, v233\n\t" \
    "v_cndmask_b32 v0, v254, v0, s[72:73]\n\t" \
    "v_cndmask_b32 v1, v254, v1, s[74:75]\n\t" \
    "v_cndmask_b32 v2, v254, v2, s[76:77]\n\t" \
    "v_cndmask_b32 v3, v254, v3, s[78:79]\n\t" \
    "v_cndmask_b32 v4, v254, v4, s[80:81]\n\t" \
    "v_cndmask_b32 v5, v254, v5, s[82:83]\n\t" \
    "v_cndmask_b32 v6, v254, v6, s[84:85]\n\t" \
    "v_cndmask_b32 v7, v254, v7, s[86:87]\n\t" \
    "v_max3_f32 v252, v0, v1, v2\n\t" \
    "v_max3_f32 v255, v3, v4, v5\n\t" \
    "v_max3_f32 v254, v6, v7, v232\n\t" \
    "v_max3_f32 v252, v252, v255, v254\n\t" \
    "v_max_f32 v252, v252, v233\n\t" \
    "v_mov_b32 v255, v252\n\t" \
    "v_mov_b32 v254, v252\n\t" \
    "s_nop 1\n\t" \
    "v_permlane16_swap_b32 v255, v254\n\t" \
    "v_max_f32 v252, v255, v254\n\t" \
    "v_max_f32 v255, v255, v254\n\t" \
    "s_nop 1\n\t" \
    "v_permlane32_swap_b32 v252, v255\n\t" \
    "v_max_f32 v252, v252, v255\n\t" \
    "v_mul_f32 v252, 0xbfb8aa3b, v252\n\t" \
    "v_fma_f32 v0, v0, %[l2e], v252\n\t" \
    "v_fma_f32 v1, v1, %[l2e], v252\n\t" \
    "v_fma_f32 v2, v2, %[l2e], v252\n\t" \
    "v_fma_f32 v3, v3, %[l2e], v252\n\t" \
    "v_fma_f32 v4, v4, %[l2e], v252\n\t" \
    "v_fma_f32 v5, v5, %[l2e], v252\n\t" \
    "v_fma_f32 v6, v6, %[l2e], v252\n\t" \
    "v_fma_f32 v7, v7, %[l2e], v252\n\t" \
    "v_fma_f32 v232, v232, %[l2e], v252\n\t" \
    "v_fma_f32 v233, v233, %[l2e], v252\n\t" \
    "v_exp_f32 v0, v0\n\t" \
    "v_exp_f32 v1, v1\n\t" \
    "v_exp_f32 v2, v2\n\t" \
    "v_exp_f32 v3, v3\n\t" \
    "v_exp_f32 v4, v4\n\t" \
    "v_exp_f32 v5, v5\n\t" \
    "v_exp_f32 v6, v6\n\t" \
    "v_exp_f32 v7, v7\n\t" \
    "v_exp_f32 v232, v232\n\t" \
    "v_exp_f32 v233, v233\n\t" \
    "v_add_f32 v252, v0, v1\n\t" \
    "v_add_f32 v235, v2, v3\n\t" \
    "v_add_f32 v254, v4, v5\n\t" \
    "v_add_f32 v255, v6, v7\n\t" \
    "v_add_f32 v234, v232, v233\n\t" \
    "v_add_f32 v252, v252, v235\n\t" \
    "v_add_f32 v254, v254, v255\n\t" \
    "v_add_f32 v252, v252, v254\n\t" \
    "v_add_f32 v252, v252, v234\n\t" \
    "v_mov_b32 v255, v252\n\t" \
    "v_mov_b32 v254, v252\n\t" \
    "s_nop 1\n\t" \
    "v_permlane16_swap_b32 v255, v254\n\t" \
    "v_add_f32 v252, v255, v254\n\t" \
    "v_add_f32 v255, v255, v254\n\t" \
    "s_nop 1\n\t" \
    "v_permlane32_swap_b32 v252, v255\n\t" \
    "v_add_f32 v252, v252, v255\n\t" \
    "v_rcp_f32 v252, v252\n\t" \
    "s_nop 0\n\t" \
    "v_mul_f32 v0, v0, v252\n\t" \
    "v_mul_f32 v1, v1, v252\n\t" \
    "v_mul_f32 v2, v2, v252\n\t" \
    "v_mul_f32 v3, v3, v252\n\t" \
    "v_mul_f32 v4, v4, v252\n\t" \
    "v_mul_f32 v5, v5, v252\n\t" \
    "v_mul_f32 v6, v6, v252\n\t" \
    "v_mul_f32 v7, v7, v252\n\t" \
    "v_mul_f32 v232, v232, v252\n\t" \
    "v_mul_f32 v233, v233, v252\n\t" \
    "v_cvt_pk_bf16_f32 v0, v0, v1\n\t" \
    "v_cvt_pk_bf16_f32 v1, v2, v3\n\t" \
    "v_cvt_pk_bf16_f32 v2, v4, v5\n\t" \
    "v_cvt_pk_bf16_f32 v3, v6, v7\n\t" \
    "v_cvt_pk_bf16_f32 v4, v232, v233\n\t" \
    "v_mov_b32 v5, 0\n\t" \
    "v_mov_b32 v6, 0\n\t" \
    "v_cvt_pk_bf16_f32 v7, v232, v233\n\t" \
    "s_waitcnt lgkmcnt(0)\n\t" \
    "v_mfma_f32_16x16x32_bf16 v[236:239], v[236:239], v[0:3], 0\n\t" \
    "v_mfma_f32_16x16x32_bf16 v[240:243], v[240:243], v[0:3], 0\n\t" \
    "v_mfma_f32_16x16x32_bf16 v[244:247], v[244:247], v[0:3], 0\n\t" \
    "v_mfma_f32_16x16x32_bf16 v[248:251], v[248:251], v[0:3], 0\n\t" \
    "v_mfma_f32_16x16x16_bf16 v[236:239], v[228:229], v[4:5], v[236:239]\n\t" \
    "v_mfma_f32_16x16x16_bf16 v[240:243], v[228:229], v[6:7], v[240:243]\n\t" \
    "v_mfma_f32_16x16x16_bf16 v[244:247], v[230:231], v[4:5], v[244:247]\n\t" \
    "v_mfma_f32_16x16x16_bf16 v[248:251], v[230:231], v[6:7], v[248:251]\n\t" \
    "s_nop 4\n\t" \
    "v_cvt_pk_bf16_f32 v212, v236, v237\n\t" \
    "v_cvt_pk_bf16_f32 v213, v240, v241\n\t" \
    "v_cvt_pk_bf16_f32 v214, v238, v239\n\t" \
    "v_cvt_pk_bf16_f32 v215, v242, v243\n\t" \
    "v_permlane16_swap_b32 v212, v213\n\t" \
    "s_nop 0\n\t" \
    "v_permlane16_swap_b32 v214, v215\n\t" \
    "v_cvt_pk_bf16_f32 v216, v244, v245\n\t" \
    "v_cvt_pk_bf16_f32 v217, v248, v249\n\t" \
    "v_cvt_pk_bf16_f32 v218, v246, v247\n\t" \
    "v_cvt_pk_bf16_f32 v219, v250, v251\n\t" \
    "v_permlane16_swap_b32 v216, v217\n\t" \
    "s_nop 0\n\t" \
    "v_permlane16_swap_b32 v218, v219\n\t" \
    "s_setprio 1\n\t" \
    GSA(2) \
    GSW \
    "s_nop 1\n\t" \
    : [lim] "+s"(lim), "+{v[212:219]}"(oxr) DGO GSO(0) GSO(1) GSO(2) GSO(3) GSO(4) \
    : "{v8}"(am_a), "{v9}"(t0_a), "{v10}"(idx), "{v11}"(iw_a), "{v12}"(ab_a), "{v13}"(sw_a), "{v14}"(xo_v), "{v15}"(kr_a), "{v16}"(dq), "{v17}"(vl_a), "{v18}"(fr_a), [stag] "s"(0xffffff81u + s), [onej] "s"(onej), [rs] "s"(rs), [m0] "s"(sbm + 16384u), [m1] "s"(sbm + 20480u), [st] "s"(s), [tag] "s"(P.tag), [po] "s"(FLG_OFF + F_PRG(4, h) * 128), [fm] "s"(fmask), [qx] "s"(qxo), [qd] "s"(lbase + LA_VM), [qh] "s"((j & 1) * 2048), [ox] "s"(oxo), [se] "s"((j & 1) * 4096), [l2e] "s"(0x3fb8aa3b), "{v[20:27]}"(wr[0][0]), "{v[28:35]}"(wr[0][1]), "{v[36:43]}"(wr[1][0]), "{v[44:51]}"(wr[1][1]), "{v[52:59]}"(wr[2][0]), "{v[60:67]}"(wr[2][1]), "{v[68:75]}"(wr[3][0]), "{v[76:83]}"(wr[3][1]), "{v[84:91]}"(wr[4][0]), "{v[92:99]}"(wr[4][1]), "{v[100:107]}"(wr[5][0]), "{v[108:115]}"(wr[5][1]), "{v[116:123]}"(wr[6][0]), "{v[124:131]}"(wr[6][1]), "{v[132:139]}"(wr[7][0]), "{v[140:147]}"(wr[7][1]), "{v[148:155]}"(wr[8][0]), "{v[156:163]}"(wr[8][1]), "{v[164:171]}"(wr[9][0]), "{v[172:179]}"(wr[9][1]), "{v[180:187]}"(wr[10][0]), "{v[188:195]}"(wr[10][1]), "{v[196:203]}"(wr[11][0]), "{v[204:211]}"(wr[11][1]) \
    : "memory", "scc", "vcc", "s90", "s91", "s72", "s73", "s74", "s75", "s76", "s77", "s78", "s79", "s80", "s81", "s82", "s83", "s84", "s85", "s86", "s87", "v0", "v1", "v2", "v3", "v4", "v5", "v6", "v7", "v220", "v221", "v222", "v223", "v224", "v225", "v226", "v227", "v228", "v229", "v230", "v231", "v232", "v233", "v234", "v235", "v236", "v237", "v238", "v239", "v240", "v241", "v242", "v243", "v244", "v245", "v246", "v247", "v248", "v249", "v250", "v251", "v252", "v253", "v254", "v255")

#define B_POLL() \
  asm volatile( \
    "s_getpc_b64 s[92:93]\n\t" \
    "TPB_%=:\n\t" \
    "s_add_u32 s92, s92, TEB_%=-TPB_%=\n\t" \
    "s_addc_u32 s93, s93, 0\n\t" \
    "s_lshr_b32 s91, %[tgt], 2\n\t" \
    "s_sub_u32 s91, s91, 3\n\t" \
    "TWB_%=:\n\t" \
    "s_memrealtime s[88:89]\n\t" \
    DGE \
    "s_waitcnt lgkmcnt(0)\n\t" \
    "s_sub_u32 s89, s91, s88\n\t" \
    "s_lshl_b32 s89, s89, 2\n\t" \
    "s_cmp_gt_i32 s89, 0\n\t" \
    "s_cbranch_scc1 TWB_%=\n\t" \
    "s_lshl_b32 s88, s88, 2\n\t" \
    "s_sub_u32 s89, %[tgt], s88\n\t" \
    "s_max_i32 s89, s89, 0\n\t" \
    "s_mul_i32 s89, s89, 11\n\t" \
    "s_lshr_b32 s89, s89, 1\n\t" \
    "s_and_b32 s89, s89, 0x7c\n\t" \
    "s_sub_u32 s92, s92, s89\n\t" \
    "s_subb_u32 s93, s93, 0\n\t" \
    "s_setpc_b64 s[92:93]\n\t" \
    "s_nop 0\n\t" \
    "s_nop 0\n\t" \
    "s_nop 0\n\t" \
    "s_nop 0\n\t" \
    "s_nop 0\n\t" \
    "s_nop 0\n\t" \
    "s_nop 0\n\t" \
    "s_nop 0\n\t" \
    "s_nop 0\n\t" \
    "s_nop 0\n\t" \
    "s_nop 0\n\t" \
    "s_nop 0\n\t" \
    "s_nop 0\n\t" \
    "s_nop 0\n\t" \
    "s_nop 0\n\t" \
    "s_nop 0\n\t" \
    "s_nop 0\n\t" \
    "s_nop 0\n\t" \
    "s_nop 0\n\t" \
    "s_nop 0\n\t" \
    "s_nop 0\n\t" \
    "s_nop 0\n\t" \
    "TEB_%=:\n\t" \
    "buffer_load_dwordx4 v[220:223], %[l16], %[rs], %[m0] offen offset:0 sc1\n\t" \
    "buffer_load_dwordx4 v[224:227], %[l16], %[rs], %[m0] offen offset:1024 sc1\n\t" \
    "buffer_load_dwordx4 v[228:231], %[l16], %[rs], %[m1] offen offset:0 sc1\n\t" \
    "buffer_load_dwordx4 v[232:235], %[l16], %[rs], %[m1] offen offset:1024 sc1\n\t" \
    "v_mov_b32 v252, v252\n\t" \
    "s_nop 2\n\t" \
    "v_mov_b32 v252, v252\n\t" \
    "s_nop 2\n\t" \
    "v_mov_b32 v252, v252\n\t" \
    "s_nop 2\n\t" \
    "v_mov_b32 v252, v252\n\t" \
    "s_nop 2\n\t" \
    "v_mov_b32 v252, v252\n\t" \
    "s_nop 2\n\t" \
    "v_mov_b32 v252, v252\n\t" \
    "s_nop 2\n\t" \
    "v_mov_b32 v252, v252\n\t" \
    "s_nop 2\n\t" \
    "v_mov_b32 v252, v252\n\t" \
    "s_nop 2\n\t" \
    "v_mov_b32 v252, v252\n\t" \
    "s_nop 2\n\t" \
    "v_mov_b32 v252, v252\n\t" \
    "s_nop 2\n\t" \
    "v_mov_b32 v252, v252\n\t" \
    "s_nop 2\n\t" \
    "v_mov_b32 v252, v252\n\t" \
    "s_nop 2\n\t" \
    "v_mov_b32 v252, v252\n\t" \
    "s_nop 2\n\t" \
    "v_mov_b32 v252, v252\n\t" \
    "s_nop 2\n\t" \
    "v_mov_b32 v252, v252\n\t" \
    "s_nop 2\n\t" \
    "v_mov_b32 v252, v252\n\t" \
    "s_nop 2\n\t" \
    "v_mov_b32 v252, v252\n\t" \
    "s_nop 2\n\t" \
    "v_mov_b32 v252, v252\n\t" \
    "s_nop 2\n\t" \
    "v_mov_b32 v252, v252\n\t" \
    "s_nop 2\n\t" \
    "v_mov_b32 v252, v252\n\t" \
    "s_nop 2\n\t" \
    "v_mov_b32 v252, v252\n\t" \
    "s_nop 2\n\t" \
    "v_mov_b32 v252, v252\n\t" \
    "s_nop 2\n\t" \
    "v_mov_b32 v252, v252\n\t" \
    "s_nop 2\n\t" \
    "v_mov_b32 v252, v252\n\t" \
    "s_nop 2\n\t" \
    "PM_%=:\n\t" \
    "s_waitcnt vmcnt(3)\n\t" \
    "ds_write_b128 %[la], v[220:223] offset:0\n\t" \
    "v_max3_u32 v252, v220, v220, v221\n\t" \
    "v_max3_u32 v252, v252, v222, v223\n\t" \
    "s_waitcnt vmcnt(2)\n\t" \
    "ds_write_b128 %[la], v[224:227] offset:1024\n\t" \
    "v_max3_u32 v252, v252, v224, v225\n\t" \
    "v_max3_u32 v252, v252, v226, v227\n\t" \
    "s_waitcnt vmcnt(1)\n\t" \
    "ds_write_b128 %[la], v[228:231] offset:8192\n\t" \
    "v_max3_u32 v252, v252, v228, v229\n\t" \
    "v_max3_u32 v252, v252, v230, v231\n\t" \
    "s_waitcnt vmcnt(0)\n\t" \
    "ds_write_b128 %[la], v[232:235] offset:9216\n\t" \
    "v_max3_u32 v252, v252, v232, v233\n\t" \
    "v_max3_u32 v252, v252, v234, v235\n\t" \
    "v_cmp_eq_u32 vcc, -1, v252\n\t" \
    "s_cbranch_vccz PD_%=\n\t" \
    "s_sub_u32 %[lim], %[lim], 1\n\t" \
    "s_cmp_gt_i32 %[lim], 0\n\t" \
    "s_cbranch_scc0 PD_%=\n\t" \
    "s_sleep " STR(CADM) "\n\t" \
    "buffer_load_dwordx4 v[220:223], %[l16], %[rs], %[m0] offen offset:0 sc1\n\t" \
    "buffer_load_dwordx4 v[224:227], %[l16], %[rs], %[m0] offen offset:1024 sc1\n\t" \
    "buffer_load_dwordx4 v[228:231], %[l16], %[rs], %[m1] offen offset:0 sc1\n\t" \
    "buffer_load_dwordx4 v[232:235], %[l16], %[rs], %[m1] offen offset:1024 sc1\n\t" \
    "s_branch PM_%=\n\t" \
    "PD_%=:\n\t" \
    GSA(3) \
    "s_waitcnt lgkmcnt(0)\n\t" \
    "s_barrier\n\t" \
    : [lim] "+s"(lim) GSO(3) \
    : [l16] "v"(l16), [la] "v"(la), [rs] "s"(rs), [m0] "s"(mo), [m1] "s"(mo + 16384u), [tgt] "s"(tgt) \
    : "memory", "vcc", "scc", "v252", "s88", "s89", "s90", "s91", "s92", "s93", "s94", "s95", "v220", "v221", "v222", "v223", "v224", "v225", "v226", "v227", "v228", "v229", "v230", "v231", "v232", "v233", "v234", "v235")

#define T_WAIT() \
  asm volatile( \
    "s_getpc_b64 s[92:93]\n\t" \
    "TPC_%=:\n\t" \
    "s_add_u32 s92, s92, TEC_%=-TPC_%=\n\t" \
    "s_addc_u32 s93, s93, 0\n\t" \
    "s_lshr_b32 s91, %[tgt], 2\n\t" \
    "s_sub_u32 s91, s91, 3\n\t" \
    "TWC_%=:\n\t" \
    "s_memrealtime s[88:89]\n\t" \
    DGE \
    "s_waitcnt lgkmcnt(0)\n\t" \
    "s_sub_u32 s89, s91, s88\n\t" \
    "s_lshl_b32 s89, s89, 2\n\t" \
    "s_cmp_gt_i32 s89, 0\n\t" \
    "s_cbranch_scc1 TWC_%=\n\t" \
    "s_lshl_b32 s88, s88, 2\n\t" \
    "s_sub_u32 s89, %[tgt], s88\n\t" \
    "s_max_i32 s89, s89, 0\n\t" \
    "s_mul_i32 s89, s89, 11\n\t" \
    "s_lshr_b32 s89, s89, 1\n\t" \
    "s_and_b32 s89, s89, 0x7c\n\t" \
    "s_sub_u32 s92, s92, s89\n\t" \
    "s_subb_u32 s93, s93, 0\n\t" \
    "s_setpc_b64 s[92:93]\n\t" \
    "s_nop 0\n\t" \
    "s_nop 0\n\t" \
    "s_nop 0\n\t" \
    "s_nop 0\n\t" \
    "s_nop 0\n\t" \
    "s_nop 0\n\t" \
    "s_nop 0\n\t" \
    "s_nop 0\n\t" \
    "s_nop 0\n\t" \
    "s_nop 0\n\t" \
    "s_nop 0\n\t" \
    "s_nop 0\n\t" \
    "s_nop 0\n\t" \
    "s_nop 0\n\t" \
    "s_nop 0\n\t" \
    "s_nop 0\n\t" \
    "s_nop 0\n\t" \
    "s_nop 0\n\t" \
    "s_nop 0\n\t" \
    "s_nop 0\n\t" \
    "s_nop 0\n\t" \
    "s_nop 0\n\t" \
    "TEC_%=:\n\t" \
    :  \
    : [tgt] "s"(tgt) \
    : "s88", "s89", "s90", "s91", "s92", "s93", "s94", "s95", "memory", "scc")

#define V_PROJ() \
  asm volatile( \
    "v_mov_b32 v0, 0\n\t" \
    "v_mov_b32 v1, 0\n\t" \
    "v_mov_b32 v2, 0\n\t" \
    "v_mov_b32 v3, 0\n\t" \
    "v_mov_b32 v4, 0\n\t" \
    "v_mov_b32 v5, 0\n\t" \
    "v_mov_b32 v6, 0\n\t" \
    "v_mov_b32 v7, 0\n\t" \
    "s_barrier\n\t" \
    "ds_read_b128 v[220:223], %[am] offset:0\n\t" \
    "ds_read_b128 v[224:227], %[am] offset:1024\n\t" \
    "ds_read_b128 v[228:231], %[am] offset:2048\n\t" \
    "ds_read_b128 v[232:235], %[am] offset:3072\n\t" \
    "ds_read_b128 v[236:239], %[am] offset:4096\n\t" \
    "ds_read_b128 v[240:243], %[am] offset:5120\n\t" \
    "ds_read_b128 v[244:247], %[am] offset:6144\n\t" \
    "ds_read_b128 v[248:251], %[am] offset:7168\n\t" \
    "s_waitcnt lgkmcnt(7)\n\t" \
    "v_smfmac_f32_16x16x64_bf16 v[0:3], v[220:223], v[28:35], %[idx]\n\t" \
    "ds_read_b128 v[220:223], %[am] offset:8192\n\t" \
    "s_waitcnt lgkmcnt(7)\n\t" \
    "v_smfmac_f32_16x16x64_bf16 v[4:7], v[224:227], v[36:43], %[idx]\n\t" \
    "ds_read_b128 v[224:227], %[am] offset:9216\n\t" \
    "s_waitcnt lgkmcnt(7)\n\t" \
    "v_smfmac_f32_16x16x64_bf16 v[0:3], v[228:231], v[44:51], %[idx]\n\t" \
    "ds_read_b128 v[228:231], %[am] offset:10240\n\t" \
    "s_waitcnt lgkmcnt(7)\n\t" \
    "v_smfmac_f32_16x16x64_bf16 v[4:7], v[232:235], v[52:59], %[idx]\n\t" \
    "ds_read_b128 v[232:235], %[am] offset:11264\n\t" \
    "s_waitcnt lgkmcnt(7)\n\t" \
    "v_smfmac_f32_16x16x64_bf16 v[0:3], v[236:239], v[60:67], %[idx]\n\t" \
    "ds_read_b128 v[236:239], %[am] offset:12288\n\t" \
    "s_waitcnt lgkmcnt(7)\n\t" \
    "v_smfmac_f32_16x16x64_bf16 v[4:7], v[240:243], v[68:75], %[idx]\n\t" \
    "ds_read_b128 v[240:243], %[am] offset:13312\n\t" \
    "s_waitcnt lgkmcnt(7)\n\t" \
    "v_smfmac_f32_16x16x64_bf16 v[0:3], v[244:247], v[76:83], %[idx]\n\t" \
    "ds_read_b128 v[244:247], %[am] offset:14336\n\t" \
    "s_waitcnt lgkmcnt(7)\n\t" \
    "v_smfmac_f32_16x16x64_bf16 v[4:7], v[248:251], v[84:91], %[idx]\n\t" \
    "ds_read_b128 v[248:251], %[am] offset:15360\n\t" \
    "s_waitcnt lgkmcnt(7)\n\t" \
    "v_smfmac_f32_16x16x64_bf16 v[0:3], v[220:223], v[92:99], %[idx]\n\t" \
    "s_waitcnt lgkmcnt(6)\n\t" \
    "v_smfmac_f32_16x16x64_bf16 v[4:7], v[224:227], v[100:107], %[idx]\n\t" \
    "s_waitcnt lgkmcnt(5)\n\t" \
    "v_smfmac_f32_16x16x64_bf16 v[0:3], v[228:231], v[108:115], %[idx]\n\t" \
    "s_waitcnt lgkmcnt(4)\n\t" \
    "v_smfmac_f32_16x16x64_bf16 v[4:7], v[232:235], v[116:123], %[idx]\n\t" \
    "s_waitcnt lgkmcnt(3)\n\t" \
    "v_smfmac_f32_16x16x64_bf16 v[0:3], v[236:239], v[124:131], %[idx]\n\t" \
    "s_waitcnt lgkmcnt(2)\n\t" \
    "v_smfmac_f32_16x16x64_bf16 v[4:7], v[240:243], v[132:139], %[idx]\n\t" \
    "s_waitcnt lgkmcnt(1)\n\t" \
    "v_smfmac_f32_16x16x64_bf16 v[0:3], v[244:247], v[140:147], %[idx]\n\t" \
    "s_waitcnt lgkmcnt(0)\n\t" \
    "v_smfmac_f32_16x16x64_bf16 v[4:7], v[248:251], v[148:155], %[idx]\n\t" \
    "s_nop 5\n\t" \
    "v_add_f32 v0, v0, v1\n\t" \
    "v_add_f32 v2, v2, v3\n\t" \
    "v_add_f32 v4, v4, v5\n\t" \
    "v_add_f32 v6, v6, v7\n\t" \
    "v_add_f32 v0, v0, v4\n\t" \
    "v_add_f32 v2, v2, v6\n\t" \
    "v_cvt_pk_bf16_f32 v0, v0, v2\n\t" \
    "v_min_u32 v0, -2, v0\n\t" \
    "s_nop 1\n\t" \
    "v_mov_b32_dpp v1, v0 quad_perm:[1,1,1,1] row_mask:0xf bank_mask:0xf\n\t" \
    "v_mov_b32_dpp v2, v0 quad_perm:[2,2,2,2] row_mask:0xf bank_mask:0xf\n\t" \
    "v_mov_b32_dpp v3, v0 quad_perm:[3,3,3,3] row_mask:0xf bank_mask:0xf\n\t" \
    "s_mov_b64 exec, %[qm]\n\t" \
    "buffer_store_dwordx4 v[0:3], %[l4], %[rs], %[vo] offen sc1\n\t" \
    "s_mov_b64 exec, -1\n\t" \
    "s_nop 1\n\t" \
    :  \
    : [am] "v"(am_a), [idx] "v"(idx), [l4] "v"(ln * 4), [rs] "s"(rs), [vo] "s"(sb + vo), [qm] "s"(0x1111111111111111ull), "{v[28:35]}"(wv[0]), "{v[36:43]}"(wv[1]), "{v[44:51]}"(wv[2]), "{v[52:59]}"(wv[3]), "{v[60:67]}"(wv[4]), "{v[68:75]}"(wv[5]), "{v[76:83]}"(wv[6]), "{v[84:91]}"(wv[7]), "{v[92:99]}"(wv[8]), "{v[100:107]}"(wv[9]), "{v[108:115]}"(wv[10]), "{v[116:123]}"(wv[11]), "{v[124:131]}"(wv[12]), "{v[132:139]}"(wv[13]), "{v[140:147]}"(wv[14]), "{v[148:155]}"(wv[15]) \
    : "memory", "v0", "v1", "v2", "v3", "v4", "v5", "v6", "v7", "v220", "v221", "v222", "v223", "v224", "v225", "v226", "v227", "v228", "v229", "v230", "v231", "v232", "v233", "v234", "v235", "v236", "v237", "v238", "v239", "v240", "v241", "v242", "v243", "v244", "v245", "v246", "v247", "v248", "v249", "v250", "v251")

#define WO_V() \
  asm volatile( \
    "v_mov_b32 v0, 0\n\t" \
    "v_mov_b32 v1, 0\n\t" \
    "v_mov_b32 v2, 0\n\t" \
    "v_mov_b32 v3, 0\n\t" \
    "v_mov_b32 v4, 0\n\t" \
    "v_mov_b32 v5, 0\n\t" \
    "v_mov_b32 v6, 0\n\t" \
    "v_mov_b32 v7, 0\n\t" \
    "v_mov_b32 v8, 0\n\t" \
    "v_mov_b32 v9, 0\n\t" \
    "v_mov_b32 v10, 0\n\t" \
    "v_mov_b32 v11, 0\n\t" \
    "v_mov_b32 v12, 0\n\t" \
    "v_mov_b32 v13, 0\n\t" \
    "v_mov_b32 v14, 0\n\t" \
    "v_mov_b32 v15, 0\n\t" \
    "v_mov_b32 v252, 0\n\t" \
    "s_barrier\n\t" \
    GSA(4) \
    DGC \
    "buffer_load_dwordx4 v[220:223], %[l16], %[rs], %[s0] offen sc1\n\t" \
    "s_nop 7\n\t" \
    "buffer_load_dwordx4 v[224:227], %[l16], %[rs], %[s1] offen sc1\n\t" \
    "A_%=:\n\t" \
    "s_waitcnt vmcnt(1)\n\t" \
    "v_smfmac_f32_16x16x64_bf16 v[0:3], v[220:223], v[156:163], %[idx]\n\t" \
    "v_max3_u32 v252, v252, v220, v221\n\t" \
    "v_smfmac_f32_16x16x64_bf16 v[4:7], v[220:223], v[164:171], %[idx]\n\t" \
    "v_max3_u32 v252, v252, v222, v223\n\t" \
    "v_smfmac_f32_16x16x64_bf16 v[8:11], v[220:223], v[172:179], %[idx]\n\t" \
    "v_smfmac_f32_16x16x64_bf16 v[12:15], v[220:223], v[180:187], %[idx]\n\t" \
    "s_waitcnt vmcnt(0)\n\t" \
    "v_smfmac_f32_16x16x64_bf16 v[0:3], v[224:227], v[188:195], %[idx]\n\t" \
    "v_max3_u32 v252, v252, v224, v225\n\t" \
    "v_smfmac_f32_16x16x64_bf16 v[4:7], v[224:227], v[196:203], %[idx]\n\t" \
    "v_max3_u32 v252, v252, v226, v227\n\t" \
    "v_smfmac_f32_16x16x64_bf16 v[8:11], v[224:227], v[204:211], %[idx]\n\t" \
    "v_smfmac_f32_16x16x64_bf16 v[12:15], v[224:227], v[212:219], %[idx]\n\t" \
    "s_setprio 0\n\t" \
    "v_cmp_eq_u32 vcc, -1, v252\n\t" \
    "s_cbranch_vccnz SL_%=\n\t" \
    "D_%=:\n\t" \
    GSA(1) \
    "s_nop 2\n\t" \
    "v_add_f32 v0, v0, v1\n\t" \
    "v_add_f32 v2, v2, v3\n\t" \
    "v_add_f32 v4, v4, v5\n\t" \
    "v_add_f32 v6, v6, v7\n\t" \
    "v_add_f32 v8, v8, v9\n\t" \
    "v_add_f32 v10, v10, v11\n\t" \
    "v_add_f32 v12, v12, v13\n\t" \
    "v_add_f32 v14, v14, v15\n\t" \
    "v_permlane32_swap_b32 v0, v4\n\t" \
    "v_permlane32_swap_b32 v2, v6\n\t" \
    "v_permlane32_swap_b32 v8, v12\n\t" \
    "v_permlane32_swap_b32 v10, v14\n\t" \
    "v_add_f32 v0, v0, v12\n\t" \
    "v_add_f32 v2, v2, v14\n\t" \
    "ds_write2_b32 %[rw], v0, v2 offset0:0 offset1:32\n\t" \
    "s_setprio 2\n\t" \
    "s_waitcnt lgkmcnt(0)\n\t" \
    "s_barrier\n\t" \
    "s_cmp_eq_u32 %[red], 0\n\t" \
    "s_cbranch_scc0 E_%=\n\t" \
    "ds_read2st64_b64 v[0:3], %[rr] offset0:0 offset1:1\n\t" \
    "ds_read2st64_b64 v[4:7], %[rr] offset0:2 offset1:3\n\t" \
    "ds_read2st64_b64 v[8:11], %[rr] offset0:4 offset1:5\n\t" \
    "ds_read2st64_b64 v[12:15], %[rr] offset0:6 offset1:7\n\t" \
    "s_waitcnt lgkmcnt(3)\n\t" \
    "v_pk_add_f32 v[0:1], v[0:1], v[2:3]\n\t" \
    "s_waitcnt lgkmcnt(2)\n\t" \
    "v_pk_add_f32 v[4:5], v[4:5], v[6:7]\n\t" \
    "s_waitcnt lgkmcnt(1)\n\t" \
    "v_pk_add_f32 v[8:9], v[8:9], v[10:11]\n\t" \
    "v_pk_add_f32 v[0:1], v[0:1], v[4:5]\n\t" \
    "s_waitcnt lgkmcnt(0)\n\t" \
    "v_pk_add_f32 v[12:13], v[12:13], v[14:15]\n\t" \
    "s_nop 0\n\t" \
    "v_pk_add_f32 v[8:9], v[8:9], v[12:13]\n\t" \
    "s_nop 0\n\t" \
    "v_pk_add_f32 v[0:1], v[0:1], v[8:9]\n\t" \
    "s_nop 0\n\t" \
    "v_cvt_pk_bf16_f32 v0, v0, v1\n\t" \
    "v_min_u32 v0, -2, v0\n\t" \
    "buffer_store_dword v0, %[mo], %[rs], %[sn] offen sc1\n\t" \
    "s_memrealtime %[t1]\n\t" \
    DGA \
    "s_waitcnt lgkmcnt(0)\n\t" \
    GSA(2) \
    "s_branch E_%=\n\t" \
    "SL_%=:\n\t" \
    "s_sleep 1\n\t" \
    "buffer_load_dwordx4 v[220:223], %[l16], %[rs], %[s0] offen sc1\n\t" \
    "buffer_load_dwordx4 v[224:227], %[l16], %[rs], %[s1] offen sc1\n\t" \
    "s_sub_u32 %[lim], %[lim], " STR(DEC2) "\n\t" \
    "s_cmp_gt_i32 %[lim], 0\n\t" \
    "s_cbranch_scc0 G_%=\n\t" \
    "s_nop 3\n\t" \
    "v_mov_b32 v0, 0\n\t" \
    "v_mov_b32 v1, 0\n\t" \
    "v_mov_b32 v2, 0\n\t" \
    "v_mov_b32 v3, 0\n\t" \
    "v_mov_b32 v4, 0\n\t" \
    "v_mov_b32 v5, 0\n\t" \
    "v_mov_b32 v6, 0\n\t" \
    "v_mov_b32 v7, 0\n\t" \
    "v_mov_b32 v8, 0\n\t" \
    "v_mov_b32 v9, 0\n\t" \
    "v_mov_b32 v10, 0\n\t" \
    "v_mov_b32 v11, 0\n\t" \
    "v_mov_b32 v12, 0\n\t" \
    "v_mov_b32 v13, 0\n\t" \
    "v_mov_b32 v14, 0\n\t" \
    "v_mov_b32 v15, 0\n\t" \
    "v_mov_b32 v252, 0\n\t" \
    "s_branch A_%=\n\t" \
    "G_%=:\n\t" \
    "s_waitcnt vmcnt(0)\n\t" \
    "s_branch D_%=\n\t" \
    "E_%=:\n\t" \
    GSW \
    : [lim] "+s"(lim) GSO(1) GSO(4), [t1] "=&s"(t1) DGO DGQ GSO(2) \
    : [l16] "v"(l16), [rs] "s"(rs), [idx] "v"(idx), [rw] "v"(rw_a), [s0] "s"(so[0]), [s1] "s"(so[1]), [rr] "v"(rr_a), [mo] "v"(mo_v), [sn] "s"(sn), [red] "s"(wid), "{v[156:163]}"(wo[0][0][0]), "{v[164:171]}"(wo[0][0][1]), "{v[172:179]}"(wo[0][1][0]), "{v[180:187]}"(wo[0][1][1]), "{v[188:195]}"(wo[1][0][0]), "{v[196:203]}"(wo[1][0][1]), "{v[204:211]}"(wo[1][1][0]), "{v[212:219]}"(wo[1][1][1]) \
    : "memory", "vcc", "scc", "v252", "v0", "v1", "v2", "v3", "v4", "v5", "v6", "v7", "v8", "v9", "v10", "v11", "v12", "v13", "v14", "v15", "v220", "v221", "v222", "v223", "v224", "v225", "v226", "v227")

#define WO_W() \
  asm volatile( \
    "v_mov_b32 v0, 0\n\t" \
    "v_mov_b32 v1, 0\n\t" \
    "v_mov_b32 v2, 0\n\t" \
    "v_mov_b32 v3, 0\n\t" \
    "v_mov_b32 v4, 0\n\t" \
    "v_mov_b32 v5, 0\n\t" \
    "v_mov_b32 v6, 0\n\t" \
    "v_mov_b32 v7, 0\n\t" \
    "v_mov_b32 v8, 0\n\t" \
    "v_mov_b32 v9, 0\n\t" \
    "v_mov_b32 v10, 0\n\t" \
    "v_mov_b32 v11, 0\n\t" \
    "v_mov_b32 v12, 0\n\t" \
    "v_mov_b32 v13, 0\n\t" \
    "v_mov_b32 v14, 0\n\t" \
    "v_mov_b32 v15, 0\n\t" \
    "v_mov_b32 v252, 0\n\t" \
    "s_setprio 1\n\t" \
    "s_barrier\n\t" \
    GSA(4) \
    "buffer_load_dwordx4 v[220:223], %[l16], %[rs], %[s0] offen sc1\n\t" \
    "s_nop 15\n\t" \
    "buffer_load_dwordx4 v[224:227], %[l16], %[rs], %[s1] offen sc1\n\t" \
    "s_nop 15\n\t" \
    "buffer_load_dwordx4 v[228:231], %[l16], %[rs], %[s2] offen sc1\n\t" \
    "s_nop 15\n\t" \
    "buffer_load_dwordx4 v[232:235], %[l16], %[rs], %[s3] offen sc1\n\t" \
    "s_nop 15\n\t" \
    "buffer_load_dwordx4 v[236:239], %[l16], %[rs], %[s4] offen sc1\n\t" \
    "s_nop 15\n\t" \
    "buffer_load_dwordx4 v[240:243], %[l16], %[rs], %[s5] offen sc1\n\t" \
    "A_%=:\n\t" \
    "s_waitcnt vmcnt(5)\n\t" \
    "v_smfmac_f32_16x16x64_bf16 v[0:3], v[220:223], v[28:35], %[idx]\n\t" \
    "v_max3_u32 v252, v252, v220, v221\n\t" \
    "v_smfmac_f32_16x16x64_bf16 v[4:7], v[220:223], v[36:43], %[idx]\n\t" \
    "v_max3_u32 v252, v252, v222, v223\n\t" \
    "v_smfmac_f32_16x16x64_bf16 v[8:11], v[220:223], v[44:51], %[idx]\n\t" \
    "v_smfmac_f32_16x16x64_bf16 v[12:15], v[220:223], v[52:59], %[idx]\n\t" \
    "s_waitcnt vmcnt(4)\n\t" \
    "v_smfmac_f32_16x16x64_bf16 v[0:3], v[224:227], v[60:67], %[idx]\n\t" \
    "v_max3_u32 v252, v252, v224, v225\n\t" \
    "v_smfmac_f32_16x16x64_bf16 v[4:7], v[224:227], v[68:75], %[idx]\n\t" \
    "v_max3_u32 v252, v252, v226, v227\n\t" \
    "v_smfmac_f32_16x16x64_bf16 v[8:11], v[224:227], v[76:83], %[idx]\n\t" \
    "v_smfmac_f32_16x16x64_bf16 v[12:15], v[224:227], v[84:91], %[idx]\n\t" \
    "s_waitcnt vmcnt(3)\n\t" \
    "v_smfmac_f32_16x16x64_bf16 v[0:3], v[228:231], v[92:99], %[idx]\n\t" \
    "v_max3_u32 v252, v252, v228, v229\n\t" \
    "v_smfmac_f32_16x16x64_bf16 v[4:7], v[228:231], v[100:107], %[idx]\n\t" \
    "v_max3_u32 v252, v252, v230, v231\n\t" \
    "v_smfmac_f32_16x16x64_bf16 v[8:11], v[228:231], v[108:115], %[idx]\n\t" \
    "v_smfmac_f32_16x16x64_bf16 v[12:15], v[228:231], v[116:123], %[idx]\n\t" \
    "s_waitcnt vmcnt(2)\n\t" \
    "v_smfmac_f32_16x16x64_bf16 v[0:3], v[232:235], v[124:131], %[idx]\n\t" \
    "v_max3_u32 v252, v252, v232, v233\n\t" \
    "v_smfmac_f32_16x16x64_bf16 v[4:7], v[232:235], v[132:139], %[idx]\n\t" \
    "v_max3_u32 v252, v252, v234, v235\n\t" \
    "v_smfmac_f32_16x16x64_bf16 v[8:11], v[232:235], v[140:147], %[idx]\n\t" \
    "v_smfmac_f32_16x16x64_bf16 v[12:15], v[232:235], v[148:155], %[idx]\n\t" \
    "s_waitcnt vmcnt(1)\n\t" \
    "v_smfmac_f32_16x16x64_bf16 v[0:3], v[236:239], v[156:163], %[idx]\n\t" \
    "v_max3_u32 v252, v252, v236, v237\n\t" \
    "v_smfmac_f32_16x16x64_bf16 v[4:7], v[236:239], v[164:171], %[idx]\n\t" \
    "v_max3_u32 v252, v252, v238, v239\n\t" \
    "v_smfmac_f32_16x16x64_bf16 v[8:11], v[236:239], v[172:179], %[idx]\n\t" \
    "v_smfmac_f32_16x16x64_bf16 v[12:15], v[236:239], v[180:187], %[idx]\n\t" \
    "s_waitcnt vmcnt(0)\n\t" \
    "v_smfmac_f32_16x16x64_bf16 v[0:3], v[240:243], v[188:195], %[idx]\n\t" \
    "v_max3_u32 v252, v252, v240, v241\n\t" \
    "v_smfmac_f32_16x16x64_bf16 v[4:7], v[240:243], v[196:203], %[idx]\n\t" \
    "v_max3_u32 v252, v252, v242, v243\n\t" \
    "v_smfmac_f32_16x16x64_bf16 v[8:11], v[240:243], v[204:211], %[idx]\n\t" \
    "v_smfmac_f32_16x16x64_bf16 v[12:15], v[240:243], v[212:219], %[idx]\n\t" \
    "v_cmp_eq_u32 vcc, -1, v252\n\t" \
    "s_cbranch_vccnz SL_%=\n\t" \
    "D_%=:\n\t" \
    GSA(1) \
    "s_nop 2\n\t" \
    "v_add_f32 v0, v0, v1\n\t" \
    "v_add_f32 v2, v2, v3\n\t" \
    "v_add_f32 v4, v4, v5\n\t" \
    "v_add_f32 v6, v6, v7\n\t" \
    "v_add_f32 v8, v8, v9\n\t" \
    "v_add_f32 v10, v10, v11\n\t" \
    "v_add_f32 v12, v12, v13\n\t" \
    "v_add_f32 v14, v14, v15\n\t" \
    "v_permlane32_swap_b32 v0, v4\n\t" \
    "v_permlane32_swap_b32 v2, v6\n\t" \
    "v_permlane32_swap_b32 v8, v12\n\t" \
    "v_permlane32_swap_b32 v10, v14\n\t" \
    "v_add_f32 v0, v0, v12\n\t" \
    "v_add_f32 v2, v2, v14\n\t" \
    "ds_write2_b32 %[rw], v0, v2 offset0:0 offset1:32\n\t" \
    "s_waitcnt lgkmcnt(0)\n\t" \
    "s_barrier\n\t" \
    "s_branch E_%=\n\t" \
    "SL_%=:\n\t" \
    "s_sleep 1\n\t" \
    "buffer_load_dwordx4 v[220:223], %[l16], %[rs], %[s0] offen sc1\n\t" \
    "buffer_load_dwordx4 v[224:227], %[l16], %[rs], %[s1] offen sc1\n\t" \
    "buffer_load_dwordx4 v[228:231], %[l16], %[rs], %[s2] offen sc1\n\t" \
    "buffer_load_dwordx4 v[232:235], %[l16], %[rs], %[s3] offen sc1\n\t" \
    "buffer_load_dwordx4 v[236:239], %[l16], %[rs], %[s4] offen sc1\n\t" \
    "buffer_load_dwordx4 v[240:243], %[l16], %[rs], %[s5] offen sc1\n\t" \
    "s_sub_u32 %[lim], %[lim], " STR(DEC2) "\n\t" \
    "s_cmp_gt_i32 %[lim], 0\n\t" \
    "s_cbranch_scc0 G_%=\n\t" \
    "s_nop 3\n\t" \
    "v_mov_b32 v0, 0\n\t" \
    "v_mov_b32 v1, 0\n\t" \
    "v_mov_b32 v2, 0\n\t" \
    "v_mov_b32 v3, 0\n\t" \
    "v_mov_b32 v4, 0\n\t" \
    "v_mov_b32 v5, 0\n\t" \
    "v_mov_b32 v6, 0\n\t" \
    "v_mov_b32 v7, 0\n\t" \
    "v_mov_b32 v8, 0\n\t" \
    "v_mov_b32 v9, 0\n\t" \
    "v_mov_b32 v10, 0\n\t" \
    "v_mov_b32 v11, 0\n\t" \
    "v_mov_b32 v12, 0\n\t" \
    "v_mov_b32 v13, 0\n\t" \
    "v_mov_b32 v14, 0\n\t" \
    "v_mov_b32 v15, 0\n\t" \
    "v_mov_b32 v252, 0\n\t" \
    "s_branch A_%=\n\t" \
    "G_%=:\n\t" \
    "s_waitcnt vmcnt(0)\n\t" \
    "s_branch D_%=\n\t" \
    "E_%=:\n\t" \
    GSW \
    : [lim] "+s"(lim) GSO(1) GSO(4) \
    : [l16] "v"(l16), [rs] "s"(rs), [idx] "v"(idx), [rw] "v"(rw_a), [s0] "s"(so[0]), [s1] "s"(so[1]), [s2] "s"(so[2]), [s3] "s"(so[3]), [s4] "s"(so[4]), [s5] "s"(so[5]), "{v[28:35]}"(wo[0][0][0]), "{v[36:43]}"(wo[0][0][1]), "{v[44:51]}"(wo[0][1][0]), "{v[52:59]}"(wo[0][1][1]), "{v[60:67]}"(wo[1][0][0]), "{v[68:75]}"(wo[1][0][1]), "{v[76:83]}"(wo[1][1][0]), "{v[84:91]}"(wo[1][1][1]), "{v[92:99]}"(wo[2][0][0]), "{v[100:107]}"(wo[2][0][1]), "{v[108:115]}"(wo[2][1][0]), "{v[116:123]}"(wo[2][1][1]), "{v[124:131]}"(wo[3][0][0]), "{v[132:139]}"(wo[3][0][1]), "{v[140:147]}"(wo[3][1][0]), "{v[148:155]}"(wo[3][1][1]), "{v[156:163]}"(wo[4][0][0]), "{v[164:171]}"(wo[4][0][1]), "{v[172:179]}"(wo[4][1][0]), "{v[180:187]}"(wo[4][1][1]), "{v[188:195]}"(wo[5][0][0]), "{v[196:203]}"(wo[5][0][1]), "{v[204:211]}"(wo[5][1][0]), "{v[212:219]}"(wo[5][1][1]) \
    : "memory", "vcc", "scc", "v252", "v0", "v1", "v2", "v3", "v4", "v5", "v6", "v7", "v8", "v9", "v10", "v11", "v12", "v13", "v14", "v15", "v220", "v221", "v222", "v223", "v224", "v225", "v226", "v227", "v228", "v229", "v230", "v231", "v232", "v233", "v234", "v235", "v236", "v237", "v238", "v239", "v240", "v241", "v242", "v243")

#define S_LOOP_K(TE_, FIN_) \
  asm volatile( \
    "s_cmp_eq_u32 %[kh], 0\n\t" \
    "s_cbranch_scc0 L1_%=\n\t" \
    "L0_%=:\n\t" \
    "s_setprio 0\n\t" \
    "s_nop 3\n\t" \
    "ds_write_b128 %[pa], v[196:199] offset:0\n\t" \
    "ds_write_b128 %[pa], v[200:203] offset:1024\n\t" \
    "ds_write_b128 %[pa], v[204:207] offset:2048\n\t" \
    "ds_write_b128 %[pa], v[208:211] offset:3072\n\t" \
    "s_sub_u32 %[a], %[t], 1\n\t" \
    "v_mov_b32 v53, %[tag]\n\t" \
    "v_mov_b32 v52, %[a]\n\t" \
    "s_sub_u32 %[b], %[t], 2\n\t" \
    "s_cmp_lt_u32 %[b], " STR(SQUICK) "\n\t" \
    "s_cselect_b64 exec, %[fm], 0\n\t" \
    "buffer_store_dwordx2 v[52:53], %[l16], %[rs], %[fo] offen sc1\n\t" \
    "s_mov_b64 exec, -1\n\t" \
    "s_add_u32 m0, %[sd], 49152\n\t" \
    "s_add_u32 %[a], %[sx], 81920\n\t" \
    "s_waitcnt lgkmcnt(0)\n\t" \
    "buffer_load_dwordx4 %[l16], %[rs], %[a] offen offset:0 sc1 lds\n\t" \
    "buffer_load_dwordx4 %[l16], %[rs], %[a] offen offset:1024 sc1 lds\n\t" \
    "buffer_load_dwordx4 %[l16], %[rs], %[a] offen offset:2048 sc1 lds\n\t" \
    "buffer_load_dwordx4 %[l16], %[rs], %[a] offen offset:3072 sc1 lds\n\t" \
    "v_mfma_f32_16x16x32_bf16 v[0:3], v[68:71], v[20:23], 0\n\t" \
    "ds_read_b128 v[44:47], %[xp] offset:6144\n\t" \
    "v_mfma_f32_16x16x32_bf16 v[4:7], v[76:79], v[20:23], 0\n\t" \
    "ds_read_b128 v[48:51], %[xp] offset:7168\n\t" \
    "v_mfma_f32_16x16x32_bf16 v[8:11], v[68:71], v[24:27], 0\n\t" \
    "v_mfma_f32_16x16x32_bf16 v[12:15], v[76:79], v[24:27], 0\n\t" \
    "v_mfma_f32_16x16x32_bf16 v[0:3], v[72:75], v[28:31], v[0:3]\n\t" \
    "ds_read_b128 v[20:23], %[xp] offset:8192\n\t" \
    "v_mfma_f32_16x16x32_bf16 v[4:7], v[80:83], v[28:31], v[4:7]\n\t" \
    "ds_read_b128 v[24:27], %[xp] offset:9216\n\t" \
    "v_mfma_f32_16x16x32_bf16 v[8:11], v[72:75], v[32:35], v[8:11]\n\t" \
    "v_mfma_f32_16x16x32_bf16 v[12:15], v[80:83], v[32:35], v[12:15]\n\t" \
    "v_mfma_f32_16x16x32_bf16 v[0:3], v[84:87], v[36:39], v[0:3]\n\t" \
    "ds_read_b128 v[28:31], %[xp] offset:10240\n\t" \
    "v_mfma_f32_16x16x32_bf16 v[4:7], v[92:95], v[36:39], v[4:7]\n\t" \
    "ds_read_b128 v[32:35], %[xp] offset:11264\n\t" \
    "v_mfma_f32_16x16x32_bf16 v[8:11], v[84:87], v[40:43], v[8:11]\n\t" \
    "s_waitcnt lgkmcnt(4)\n\t" \
    "v_mfma_f32_16x16x32_bf16 v[12:15], v[92:95], v[40:43], v[12:15]\n\t" \
    "v_mfma_f32_16x16x32_bf16 v[0:3], v[88:91], v[44:47], v[0:3]\n\t" \
    "ds_read_b128 v[36:39], %[xp] offset:12288\n\t" \
    "v_mfma_f32_16x16x32_bf16 v[4:7], v[96:99], v[44:47], v[4:7]\n\t" \
    "ds_read_b128 v[40:43], %[xp] offset:13312\n\t" \
    "v_mfma_f32_16x16x32_bf16 v[8:11], v[88:91], v[48:51], v[8:11]\n\t" \
    "s_waitcnt lgkmcnt(4)\n\t" \
    "v_mfma_f32_16x16x32_bf16 v[12:15], v[96:99], v[48:51], v[12:15]\n\t" \
    "v_mfma_f32_16x16x32_bf16 v[0:3], v[100:103], v[20:23], v[0:3]\n\t" \
    "ds_read_b128 v[44:47], %[xp] offset:14336\n\t" \
    "v_mfma_f32_16x16x32_bf16 v[4:7], v[108:111], v[20:23], v[4:7]\n\t" \
    "ds_read_b128 v[48:51], %[xp] offset:15360\n\t" \
    "v_mfma_f32_16x16x32_bf16 v[8:11], v[100:103], v[24:27], v[8:11]\n\t" \
    "s_waitcnt lgkmcnt(4)\n\t" \
    "v_mfma_f32_16x16x32_bf16 v[12:15], v[108:111], v[24:27], v[12:15]\n\t" \
    "v_mfma_f32_16x16x32_bf16 v[0:3], v[104:107], v[28:31], v[0:3]\n\t" \
    "ds_read_b128 v[20:23], %[xp] offset:16384\n\t" \
    "v_mfma_f32_16x16x32_bf16 v[4:7], v[112:115], v[28:31], v[4:7]\n\t" \
    "ds_read_b128 v[24:27], %[xp] offset:17408\n\t" \
    "v_mfma_f32_16x16x32_bf16 v[8:11], v[104:107], v[32:35], v[8:11]\n\t" \
    "s_waitcnt lgkmcnt(4)\n\t" \
    "v_mfma_f32_16x16x32_bf16 v[12:15], v[112:115], v[32:35], v[12:15]\n\t" \
    "v_mfma_f32_16x16x32_bf16 v[0:3], v[116:119], v[36:39], v[0:3]\n\t" \
    "ds_read_b128 v[28:31], %[xp] offset:18432\n\t" \
    "v_mfma_f32_16x16x32_bf16 v[4:7], v[124:127], v[36:39], v[4:7]\n\t" \
    "ds_read_b128 v[32:35], %[xp] offset:19456\n\t" \
    "v_mfma_f32_16x16x32_bf16 v[8:11], v[116:119], v[40:43], v[8:11]\n\t" \
    "s_waitcnt lgkmcnt(4)\n\t" \
    "v_mfma_f32_16x16x32_bf16 v[12:15], v[124:127], v[40:43], v[12:15]\n\t" \
    "v_mfma_f32_16x16x32_bf16 v[0:3], v[120:123], v[44:47], v[0:3]\n\t" \
    "ds_read_b128 v[36:39], %[xp] offset:20480\n\t" \
    "v_mfma_f32_16x16x32_bf16 v[4:7], v[128:131], v[44:47], v[4:7]\n\t" \
    "ds_read_b128 v[40:43], %[xp] offset:21504\n\t" \
    "v_mfma_f32_16x16x32_bf16 v[8:11], v[120:123], v[48:51], v[8:11]\n\t" \
    "s_waitcnt lgkmcnt(4)\n\t" \
    "v_mfma_f32_16x16x32_bf16 v[12:15], v[128:131], v[48:51], v[12:15]\n\t" \
    "s_waitcnt vmcnt(4)\n\t" \
    "s_barrier\n\t" \
    "s_setprio 0\n\t" \
    "s_sub_u32 %[a], %[t], 1\n\t" \
    "v_mov_b32 v53, %[tag]\n\t" \
    "v_mov_b32 v52, %[a]\n\t" \
    "s_cmp_gt_u32 %[t], 1\n\t" \
    "s_cselect_b64 exec, %[fm], 0\n\t" \
    "buffer_store_dwordx2 v[52:53], %[l16], %[rs], %[fo] offen sc1\n\t" \
    "s_mov_b64 exec, %[pm]\n\t" \
    "s_mov_b32 m0, %[pd]\n\t" \
    "s_add_u32 %[a], %[sx], %[po]\n\t" \
    "buffer_load_dwordx4 %[l16], %[rs], %[a] offen sc1 lds\n\t" \
    "s_mov_b64 exec, -1\n\t" \
    "s_add_u32 m0, %[sd], 0\n\t" \
    "s_add_u32 %[a], %[sx], 131072\n\t" \
    "buffer_load_dwordx4 %[l16], %[rs], %[a] offen offset:0 sc1 lds\n\t" \
    "buffer_load_dwordx4 %[l16], %[rs], %[a] offen offset:1024 sc1 lds\n\t" \
    "buffer_load_dwordx4 %[l16], %[rs], %[a] offen offset:2048 sc1 lds\n\t" \
    "buffer_load_dwordx4 %[l16], %[rs], %[a] offen offset:3072 sc1 lds\n\t" \
    "v_mfma_f32_16x16x32_bf16 v[0:3], v[132:135], v[20:23], v[0:3]\n\t" \
    "ds_read_b128 v[44:47], %[xp] offset:22528\n\t" \
    "v_mfma_f32_16x16x32_bf16 v[4:7], v[140:143], v[20:23], v[4:7]\n\t" \
    "ds_read_b128 v[48:51], %[xp] offset:23552\n\t" \
    "v_mfma_f32_16x16x32_bf16 v[8:11], v[132:135], v[24:27], v[8:11]\n\t" \
    "s_waitcnt lgkmcnt(4)\n\t" \
    "v_mfma_f32_16x16x32_bf16 v[12:15], v[140:143], v[24:27], v[12:15]\n\t" \
    "v_mfma_f32_16x16x32_bf16 v[0:3], v[136:139], v[28:31], v[0:3]\n\t" \
    "ds_read_b128 v[20:23], %[xp] offset:24576\n\t" \
    "v_mfma_f32_16x16x32_bf16 v[4:7], v[144:147], v[28:31], v[4:7]\n\t" \
    "ds_read_b128 v[24:27], %[xp] offset:25600\n\t" \
    "v_mfma_f32_16x16x32_bf16 v[8:11], v[136:139], v[32:35], v[8:11]\n\t" \
    "s_waitcnt lgkmcnt(4)\n\t" \
    "v_mfma_f32_16x16x32_bf16 v[12:15], v[144:147], v[32:35], v[12:15]\n\t" \
    "v_mfma_f32_16x16x32_bf16 v[0:3], v[148:151], v[36:39], v[0:3]\n\t" \
    "ds_read_b128 v[28:31], %[xp] offset:26624\n\t" \
    "v_mfma_f32_16x16x32_bf16 v[4:7], v[156:159], v[36:39], v[4:7]\n\t" \
    "ds_read_b128 v[32:35], %[xp] offset:27648\n\t" \
    "v_mfma_f32_16x16x32_bf16 v[8:11], v[148:151], v[40:43], v[8:11]\n\t" \
    "s_waitcnt lgkmcnt(4)\n\t" \
    "v_mfma_f32_16x16x32_bf16 v[12:15], v[156:159], v[40:43], v[12:15]\n\t" \
    "v_mfma_f32_16x16x32_bf16 v[0:3], v[152:155], v[44:47], v[0:3]\n\t" \
    "ds_read_b128 v[36:39], %[xp] offset:28672\n\t" \
    "v_mfma_f32_16x16x32_bf16 v[4:7], v[160:163], v[44:47], v[4:7]\n\t" \
    "ds_read_b128 v[40:43], %[xp] offset:29696\n\t" \
    "v_mfma_f32_16x16x32_bf16 v[8:11], v[152:155], v[48:51], v[8:11]\n\t" \
    "s_waitcnt lgkmcnt(4)\n\t" \
    "v_mfma_f32_16x16x32_bf16 v[12:15], v[160:163], v[48:51], v[12:15]\n\t" \
    "v_mfma_f32_16x16x32_bf16 v[0:3], v[164:167], v[20:23], v[0:3]\n\t" \
    "ds_read_b128 v[44:47], %[xp] offset:30720\n\t" \
    "v_mfma_f32_16x16x32_bf16 v[4:7], v[172:175], v[20:23], v[4:7]\n\t" \
    "ds_read_b128 v[48:51], %[xp] offset:31744\n\t" \
    "v_mfma_f32_16x16x32_bf16 v[8:11], v[164:167], v[24:27], v[8:11]\n\t" \
    "s_waitcnt lgkmcnt(4)\n\t" \
    "v_mfma_f32_16x16x32_bf16 v[12:15], v[172:175], v[24:27], v[12:15]\n\t" \
    "v_mfma_f32_16x16x32_bf16 v[0:3], v[168:171], v[28:31], v[0:3]\n\t" \
    "ds_read_b128 v[20:23], %[xp] offset:32768\n\t" \
    "v_mfma_f32_16x16x32_bf16 v[4:7], v[176:179], v[28:31], v[4:7]\n\t" \
    "ds_read_b128 v[24:27], %[xp] offset:33792\n\t" \
    "v_mfma_f32_16x16x32_bf16 v[8:11], v[168:171], v[32:35], v[8:11]\n\t" \
    "s_waitcnt lgkmcnt(4)\n\t" \
    "v_mfma_f32_16x16x32_bf16 v[12:15], v[176:179], v[32:35], v[12:15]\n\t" \
    "v_mfma_f32_16x16x32_bf16 v[0:3], v[180:183], v[36:39], v[0:3]\n\t" \
    "ds_read_b128 v[28:31], %[xp] offset:34816\n\t" \
    "v_mfma_f32_16x16x32_bf16 v[4:7], v[188:191], v[36:39], v[4:7]\n\t" \
    "ds_read_b128 v[32:35], %[xp] offset:35840\n\t" \
    "v_mfma_f32_16x16x32_bf16 v[8:11], v[180:183], v[40:43], v[8:11]\n\t" \
    "s_waitcnt lgkmcnt(4)\n\t" \
    "v_mfma_f32_16x16x32_bf16 v[12:15], v[188:191], v[40:43], v[12:15]\n\t" \
    "v_mfma_f32_16x16x32_bf16 v[0:3], v[184:187], v[44:47], v[0:3]\n\t" \
    "ds_read_b128 v[36:39], %[xp] offset:36864\n\t" \
    "v_mfma_f32_16x16x32_bf16 v[4:7], v[192:195], v[44:47], v[4:7]\n\t" \
    "ds_read_b128 v[40:43], %[xp] offset:37888\n\t" \
    "v_mfma_f32_16x16x32_bf16 v[8:11], v[184:187], v[48:51], v[8:11]\n\t" \
    "s_waitcnt lgkmcnt(4)\n\t" \
    "v_mfma_f32_16x16x32_bf16 v[12:15], v[192:195], v[48:51], v[12:15]\n\t" \
    "s_add_u32 %[sx], %[sx], 65536\n\t" \
    "s_add_u32 %[t], %[t], 1\n\t" \
    "s_waitcnt vmcnt(4)\n\t" \
    "s_barrier\n\t" \
    "s_setprio 0\n\t" \
    "s_nop 3\n\t" \
    "ds_write_b128 %[pa], v[0:3] offset:0\n\t" \
    "ds_write_b128 %[pa], v[4:7] offset:1024\n\t" \
    "ds_write_b128 %[pa], v[8:11] offset:2048\n\t" \
    "ds_write_b128 %[pa], v[12:15] offset:3072\n\t" \
    "s_sub_u32 %[a], %[t], 1\n\t" \
    "v_mov_b32 v53, %[tag]\n\t" \
    "v_mov_b32 v52, %[a]\n\t" \
    "s_sub_u32 %[b], %[t], 2\n\t" \
    "s_cmp_lt_u32 %[b], " STR(SQUICK) "\n\t" \
    "s_cselect_b64 exec, %[fm], 0\n\t" \
    "buffer_store_dwordx2 v[52:53], %[l16], %[rs], %[fo] offen sc1\n\t" \
    "s_mov_b64 exec, -1\n\t" \
    "s_add_u32 m0, %[sd], 16384\n\t" \
    "s_add_u32 %[a], %[sx], 81920\n\t" \
    "s_waitcnt lgkmcnt(0)\n\t" \
    "buffer_load_dwordx4 %[l16], %[rs], %[a] offen offset:0 sc1 lds\n\t" \
    "buffer_load_dwordx4 %[l16], %[rs], %[a] offen offset:1024 sc1 lds\n\t" \
    "buffer_load_dwordx4 %[l16], %[rs], %[a] offen offset:2048 sc1 lds\n\t" \
    "buffer_load_dwordx4 %[l16], %[rs], %[a] offen offset:3072 sc1 lds\n\t" \
    "v_mfma_f32_16x16x32_bf16 v[196:199], v[68:71], v[20:23], 0\n\t" \
    "ds_read_b128 v[44:47], %[xp] offset:38912\n\t" \
    "v_mfma_f32_16x16x32_bf16 v[200:203], v[76:79], v[20:23], 0\n\t" \
    "ds_read_b128 v[48:51], %[xp] offset:39936\n\t" \
    "v_mfma_f32_16x16x32_bf16 v[204:207], v[68:71], v[24:27], 0\n\t" \
    "v_mfma_f32_16x16x32_bf16 v[208:211], v[76:79], v[24:27], 0\n\t" \
    "v_mfma_f32_16x16x32_bf16 v[196:199], v[72:75], v[28:31], v[196:199]\n\t" \
    "ds_read_b128 v[20:23], %[xp] offset:40960\n\t" \
    "v_mfma_f32_16x16x32_bf16 v[200:203], v[80:83], v[28:31], v[200:203]\n\t" \
    "ds_read_b128 v[24:27], %[xp] offset:41984\n\t" \
    "v_mfma_f32_16x16x32_bf16 v[204:207], v[72:75], v[32:35], v[204:207]\n\t" \
    "v_mfma_f32_16x16x32_bf16 v[208:211], v[80:83], v[32:35], v[208:211]\n\t" \
    "v_mfma_f32_16x16x32_bf16 v[196:199], v[84:87], v[36:39], v[196:199]\n\t" \
    "ds_read_b128 v[28:31], %[xp] offset:43008\n\t" \
    "v_mfma_f32_16x16x32_bf16 v[200:203], v[92:95], v[36:39], v[200:203]\n\t" \
    "ds_read_b128 v[32:35], %[xp] offset:44032\n\t" \
    "v_mfma_f32_16x16x32_bf16 v[204:207], v[84:87], v[40:43], v[204:207]\n\t" \
    "s_waitcnt lgkmcnt(4)\n\t" \
    "v_mfma_f32_16x16x32_bf16 v[208:211], v[92:95], v[40:43], v[208:211]\n\t" \
    "v_mfma_f32_16x16x32_bf16 v[196:199], v[88:91], v[44:47], v[196:199]\n\t" \
    "ds_read_b128 v[36:39], %[xp] offset:45056\n\t" \
    "v_mfma_f32_16x16x32_bf16 v[200:203], v[96:99], v[44:47], v[200:203]\n\t" \
    "ds_read_b128 v[40:43], %[xp] offset:46080\n\t" \
    "v_mfma_f32_16x16x32_bf16 v[204:207], v[88:91], v[48:51], v[204:207]\n\t" \
    "s_waitcnt lgkmcnt(4)\n\t" \
    "v_mfma_f32_16x16x32_bf16 v[208:211], v[96:99], v[48:51], v[208:211]\n\t" \
    "v_mfma_f32_16x16x32_bf16 v[196:199], v[100:103], v[20:23], v[196:199]\n\t" \
    "ds_read_b128 v[44:47], %[xp] offset:47104\n\t" \
    "v_mfma_f32_16x16x32_bf16 v[200:203], v[108:111], v[20:23], v[200:203]\n\t" \
    "ds_read_b128 v[48:51], %[xp] offset:48128\n\t" \
    "v_mfma_f32_16x16x32_bf16 v[204:207], v[100:103], v[24:27], v[204:207]\n\t" \
    "s_waitcnt lgkmcnt(4)\n\t" \
    "v_mfma_f32_16x16x32_bf16 v[208:211], v[108:111], v[24:27], v[208:211]\n\t" \
    "v_mfma_f32_16x16x32_bf16 v[196:199], v[104:107], v[28:31], v[196:199]\n\t" \
    "ds_read_b128 v[20:23], %[xp] offset:49152\n\t" \
    "v_mfma_f32_16x16x32_bf16 v[200:203], v[112:115], v[28:31], v[200:203]\n\t" \
    "ds_read_b128 v[24:27], %[xp] offset:50176\n\t" \
    "v_mfma_f32_16x16x32_bf16 v[204:207], v[104:107], v[32:35], v[204:207]\n\t" \
    "s_waitcnt lgkmcnt(4)\n\t" \
    "v_mfma_f32_16x16x32_bf16 v[208:211], v[112:115], v[32:35], v[208:211]\n\t" \
    "v_mfma_f32_16x16x32_bf16 v[196:199], v[116:119], v[36:39], v[196:199]\n\t" \
    "ds_read_b128 v[28:31], %[xp] offset:51200\n\t" \
    "v_mfma_f32_16x16x32_bf16 v[200:203], v[124:127], v[36:39], v[200:203]\n\t" \
    "ds_read_b128 v[32:35], %[xp] offset:52224\n\t" \
    "v_mfma_f32_16x16x32_bf16 v[204:207], v[116:119], v[40:43], v[204:207]\n\t" \
    "s_waitcnt lgkmcnt(4)\n\t" \
    "v_mfma_f32_16x16x32_bf16 v[208:211], v[124:127], v[40:43], v[208:211]\n\t" \
    "v_mfma_f32_16x16x32_bf16 v[196:199], v[120:123], v[44:47], v[196:199]\n\t" \
    "ds_read_b128 v[36:39], %[xp] offset:53248\n\t" \
    "v_mfma_f32_16x16x32_bf16 v[200:203], v[128:131], v[44:47], v[200:203]\n\t" \
    "ds_read_b128 v[40:43], %[xp] offset:54272\n\t" \
    "v_mfma_f32_16x16x32_bf16 v[204:207], v[120:123], v[48:51], v[204:207]\n\t" \
    "s_waitcnt lgkmcnt(4)\n\t" \
    "v_mfma_f32_16x16x32_bf16 v[208:211], v[128:131], v[48:51], v[208:211]\n\t" \
    "s_waitcnt vmcnt(4)\n\t" \
    "s_barrier\n\t" \
    "s_setprio 0\n\t" \
    "s_sub_u32 %[a], %[t], 1\n\t" \
    "v_mov_b32 v53, %[tag]\n\t" \
    "v_mov_b32 v52, %[a]\n\t" \
    "s_cmp_gt_u32 %[t], 1\n\t" \
    "s_cselect_b64 exec, %[fm], 0\n\t" \
    "buffer_store_dwordx2 v[52:53], %[l16], %[rs], %[fo] offen sc1\n\t" \
    "s_mov_b64 exec, %[pm]\n\t" \
    "s_mov_b32 m0, %[pd]\n\t" \
    "s_add_u32 %[a], %[sx], %[po]\n\t" \
    "buffer_load_dwordx4 %[l16], %[rs], %[a] offen sc1 lds\n\t" \
    "s_mov_b64 exec, -1\n\t" \
    "s_add_u32 m0, %[sd], 32768\n\t" \
    "s_add_u32 %[a], %[sx], 131072\n\t" \
    "buffer_load_dwordx4 %[l16], %[rs], %[a] offen offset:0 sc1 lds\n\t" \
    "buffer_load_dwordx4 %[l16], %[rs], %[a] offen offset:1024 sc1 lds\n\t" \
    "buffer_load_dwordx4 %[l16], %[rs], %[a] offen offset:2048 sc1 lds\n\t" \
    "buffer_load_dwordx4 %[l16], %[rs], %[a] offen offset:3072 sc1 lds\n\t" \
    "v_mfma_f32_16x16x32_bf16 v[196:199], v[132:135], v[20:23], v[196:199]\n\t" \
    "ds_read_b128 v[44:47], %[xp] offset:55296\n\t" \
    "v_mfma_f32_16x16x32_bf16 v[200:203], v[140:143], v[20:23], v[200:203]\n\t" \
    "ds_read_b128 v[48:51], %[xp] offset:56320\n\t" \
    "v_mfma_f32_16x16x32_bf16 v[204:207], v[132:135], v[24:27], v[204:207]\n\t" \
    "s_waitcnt lgkmcnt(4)\n\t" \
    "v_mfma_f32_16x16x32_bf16 v[208:211], v[140:143], v[24:27], v[208:211]\n\t" \
    "v_mfma_f32_16x16x32_bf16 v[196:199], v[136:139], v[28:31], v[196:199]\n\t" \
    "ds_read_b128 v[20:23], %[xp] offset:57344\n\t" \
    "v_mfma_f32_16x16x32_bf16 v[200:203], v[144:147], v[28:31], v[200:203]\n\t" \
    "ds_read_b128 v[24:27], %[xp] offset:58368\n\t" \
    "v_mfma_f32_16x16x32_bf16 v[204:207], v[136:139], v[32:35], v[204:207]\n\t" \
    "s_waitcnt lgkmcnt(4)\n\t" \
    "v_mfma_f32_16x16x32_bf16 v[208:211], v[144:147], v[32:35], v[208:211]\n\t" \
    "v_mfma_f32_16x16x32_bf16 v[196:199], v[148:151], v[36:39], v[196:199]\n\t" \
    "ds_read_b128 v[28:31], %[xp] offset:59392\n\t" \
    "v_mfma_f32_16x16x32_bf16 v[200:203], v[156:159], v[36:39], v[200:203]\n\t" \
    "ds_read_b128 v[32:35], %[xp] offset:60416\n\t" \
    "v_mfma_f32_16x16x32_bf16 v[204:207], v[148:151], v[40:43], v[204:207]\n\t" \
    "s_waitcnt lgkmcnt(4)\n\t" \
    "v_mfma_f32_16x16x32_bf16 v[208:211], v[156:159], v[40:43], v[208:211]\n\t" \
    "v_mfma_f32_16x16x32_bf16 v[196:199], v[152:155], v[44:47], v[196:199]\n\t" \
    "ds_read_b128 v[36:39], %[xp] offset:61440\n\t" \
    "v_mfma_f32_16x16x32_bf16 v[200:203], v[160:163], v[44:47], v[200:203]\n\t" \
    "ds_read_b128 v[40:43], %[xp] offset:62464\n\t" \
    "v_mfma_f32_16x16x32_bf16 v[204:207], v[152:155], v[48:51], v[204:207]\n\t" \
    "s_waitcnt lgkmcnt(4)\n\t" \
    "v_mfma_f32_16x16x32_bf16 v[208:211], v[160:163], v[48:51], v[208:211]\n\t" \
    "v_mfma_f32_16x16x32_bf16 v[196:199], v[164:167], v[20:23], v[196:199]\n\t" \
    "ds_read_b128 v[44:47], %[xp] offset:63488\n\t" \
    "v_mfma_f32_16x16x32_bf16 v[200:203], v[172:175], v[20:23], v[200:203]\n\t" \
    "ds_read_b128 v[48:51], %[xp] offset:64512\n\t" \
    "v_mfma_f32_16x16x32_bf16 v[204:207], v[164:167], v[24:27], v[204:207]\n\t" \
    "s_waitcnt lgkmcnt(4)\n\t" \
    "v_mfma_f32_16x16x32_bf16 v[208:211], v[172:175], v[24:27], v[208:211]\n\t" \
    "v_mfma_f32_16x16x32_bf16 v[196:199], v[168:171], v[28:31], v[196:199]\n\t" \
    "ds_read_b128 v[20:23], %[xp] offset:0\n\t" \
    "v_mfma_f32_16x16x32_bf16 v[200:203], v[176:179], v[28:31], v[200:203]\n\t" \
    "ds_read_b128 v[24:27], %[xp] offset:1024\n\t" \
    "v_mfma_f32_16x16x32_bf16 v[204:207], v[168:171], v[32:35], v[204:207]\n\t" \
    "s_waitcnt lgkmcnt(4)\n\t" \
    "v_mfma_f32_16x16x32_bf16 v[208:211], v[176:179], v[32:35], v[208:211]\n\t" \
    "v_mfma_f32_16x16x32_bf16 v[196:199], v[180:183], v[36:39], v[196:199]\n\t" \
    "ds_read_b128 v[28:31], %[xp] offset:2048\n\t" \
    "v_mfma_f32_16x16x32_bf16 v[200:203], v[188:191], v[36:39], v[200:203]\n\t" \
    "ds_read_b128 v[32:35], %[xp] offset:3072\n\t" \
    "v_mfma_f32_16x16x32_bf16 v[204:207], v[180:183], v[40:43], v[204:207]\n\t" \
    "s_waitcnt lgkmcnt(4)\n\t" \
    "v_mfma_f32_16x16x32_bf16 v[208:211], v[188:191], v[40:43], v[208:211]\n\t" \
    "v_mfma_f32_16x16x32_bf16 v[196:199], v[184:187], v[44:47], v[196:199]\n\t" \
    "ds_read_b128 v[36:39], %[xp] offset:4096\n\t" \
    "v_mfma_f32_16x16x32_bf16 v[200:203], v[192:195], v[44:47], v[200:203]\n\t" \
    "ds_read_b128 v[40:43], %[xp] offset:5120\n\t" \
    "v_mfma_f32_16x16x32_bf16 v[204:207], v[184:187], v[48:51], v[204:207]\n\t" \
    "s_waitcnt lgkmcnt(4)\n\t" \
    "v_mfma_f32_16x16x32_bf16 v[208:211], v[192:195], v[48:51], v[208:211]\n\t" \
    "s_add_u32 %[sx], %[sx], 65536\n\t" \
    "s_add_u32 %[t], %[t], 1\n\t" \
    "s_waitcnt vmcnt(4)\n\t" \
    "s_barrier\n\t" \
    "s_cmp_lt_u32 %[t], %[te]\n\t" \
    "s_cbranch_scc1 L0_%=\n\t" \
    "s_cmp_eq_u32 %[fin], %[te]\n\t" \
    "s_cbranch_scc0 E_%=\n\t" \
    "s_nop 4\n\t" \
    "ds_write_b128 %[pa], v[196:199] offset:0\n\t" \
    "ds_write_b128 %[pa], v[200:203] offset:1024\n\t" \
    "ds_write_b128 %[pa], v[204:207] offset:2048\n\t" \
    "ds_write_b128 %[pa], v[208:211] offset:3072\n\t" \
    "s_waitcnt lgkmcnt(0)\n\t" \
    "s_barrier\n\t" \
    "s_branch E_%=\n\t" \
    "L1_%=:\n\t" \
    "s_setprio 1\n\t" \
    "s_add_u32 m0, %[sd], 49152\n\t" \
    "s_add_u32 %[a], %[sx], 81920\n\t" \
    "v_mfma_f32_16x16x32_bf16 v[0:3], v[68:71], v[20:23], 0\n\t" \
    "ds_read_b128 v[44:47], %[xp] offset:6144\n\t" \
    "v_mfma_f32_16x16x32_bf16 v[4:7], v[76:79], v[20:23], 0\n\t" \
    "ds_read_b128 v[48:51], %[xp] offset:7168\n\t" \
    "v_mfma_f32_16x16x32_bf16 v[8:11], v[68:71], v[24:27], 0\n\t" \
    "s_waitcnt lgkmcnt(4)\n\t" \
    "v_mfma_f32_16x16x32_bf16 v[12:15], v[76:79], v[24:27], 0\n\t" \
    "v_mfma_f32_16x16x32_bf16 v[0:3], v[72:75], v[28:31], v[0:3]\n\t" \
    "ds_read_b128 v[20:23], %[xp] offset:8192\n\t" \
    "v_mfma_f32_16x16x32_bf16 v[4:7], v[80:83], v[28:31], v[4:7]\n\t" \
    "ds_read_b128 v[24:27], %[xp] offset:9216\n\t" \
    "v_mfma_f32_16x16x32_bf16 v[8:11], v[72:75], v[32:35], v[8:11]\n\t" \
    "s_waitcnt lgkmcnt(4)\n\t" \
    "v_mfma_f32_16x16x32_bf16 v[12:15], v[80:83], v[32:35], v[12:15]\n\t" \
    "v_mfma_f32_16x16x32_bf16 v[0:3], v[84:87], v[36:39], v[0:3]\n\t" \
    "ds_read_b128 v[28:31], %[xp] offset:10240\n\t" \
    "v_mfma_f32_16x16x32_bf16 v[4:7], v[92:95], v[36:39], v[4:7]\n\t" \
    "ds_read_b128 v[32:35], %[xp] offset:11264\n\t" \
    "v_mfma_f32_16x16x32_bf16 v[8:11], v[84:87], v[40:43], v[8:11]\n\t" \
    "s_waitcnt lgkmcnt(4)\n\t" \
    "v_mfma_f32_16x16x32_bf16 v[12:15], v[92:95], v[40:43], v[12:15]\n\t" \
    "v_mfma_f32_16x16x32_bf16 v[0:3], v[88:91], v[44:47], v[0:3]\n\t" \
    "ds_read_b128 v[36:39], %[xp] offset:12288\n\t" \
    "v_mfma_f32_16x16x32_bf16 v[4:7], v[96:99], v[44:47], v[4:7]\n\t" \
    "ds_read_b128 v[40:43], %[xp] offset:13312\n\t" \
    "v_mfma_f32_16x16x32_bf16 v[8:11], v[88:91], v[48:51], v[8:11]\n\t" \
    "s_waitcnt lgkmcnt(4)\n\t" \
    "v_mfma_f32_16x16x32_bf16 v[12:15], v[96:99], v[48:51], v[12:15]\n\t" \
    "v_mfma_f32_16x16x32_bf16 v[0:3], v[100:103], v[20:23], v[0:3]\n\t" \
    "ds_read_b128 v[44:47], %[xp] offset:14336\n\t" \
    "v_mfma_f32_16x16x32_bf16 v[4:7], v[108:111], v[20:23], v[4:7]\n\t" \
    "ds_read_b128 v[48:51], %[xp] offset:15360\n\t" \
    "v_mfma_f32_16x16x32_bf16 v[8:11], v[100:103], v[24:27], v[8:11]\n\t" \
    "s_waitcnt lgkmcnt(4)\n\t" \
    "v_mfma_f32_16x16x32_bf16 v[12:15], v[108:111], v[24:27], v[12:15]\n\t" \
    "buffer_load_dwordx4 %[l16], %[rs], %[a] offen offset:0 sc1 lds\n\t" \
    "v_mfma_f32_16x16x32_bf16 v[0:3], v[104:107], v[28:31], v[0:3]\n\t" \
    "ds_read_b128 v[20:23], %[xp] offset:16384\n\t" \
    "v_mfma_f32_16x16x32_bf16 v[4:7], v[112:115], v[28:31], v[4:7]\n\t" \
    "ds_read_b128 v[24:27], %[xp] offset:17408\n\t" \
    "v_mfma_f32_16x16x32_bf16 v[8:11], v[104:107], v[32:35], v[8:11]\n\t" \
    "s_waitcnt lgkmcnt(4)\n\t" \
    "v_mfma_f32_16x16x32_bf16 v[12:15], v[112:115], v[32:35], v[12:15]\n\t" \
    "buffer_load_dwordx4 %[l16], %[rs], %[a] offen offset:1024 sc1 lds\n\t" \
    "v_mfma_f32_16x16x32_bf16 v[0:3], v[116:119], v[36:39], v[0:3]\n\t" \
    "ds_read_b128 v[28:31], %[xp] offset:18432\n\t" \
    "v_mfma_f32_16x16x32_bf16 v[4:7], v[124:127], v[36:39], v[4:7]\n\t" \
    "ds_read_b128 v[32:35], %[xp] offset:19456\n\t" \
    "v_mfma_f32_16x16x32_bf16 v[8:11], v[116:119], v[40:43], v[8:11]\n\t" \
    "s_waitcnt lgkmcnt(4)\n\t" \
    "v_mfma_f32_16x16x32_bf16 v[12:15], v[124:127], v[40:43], v[12:15]\n\t" \
    "buffer_load_dwordx4 %[l16], %[rs], %[a] offen offset:2048 sc1 lds\n\t" \
    "v_mfma_f32_16x16x32_bf16 v[0:3], v[120:123], v[44:47], v[0:3]\n\t" \
    "ds_read_b128 v[36:39], %[xp] offset:20480\n\t" \
    "v_mfma_f32_16x16x32_bf16 v[4:7], v[128:131], v[44:47], v[4:7]\n\t" \
    "ds_read_b128 v[40:43], %[xp] offset:21504\n\t" \
    "v_mfma_f32_16x16x32_bf16 v[8:11], v[120:123], v[48:51], v[8:11]\n\t" \
    "s_waitcnt lgkmcnt(4)\n\t" \
    "v_mfma_f32_16x16x32_bf16 v[12:15], v[128:131], v[48:51], v[12:15]\n\t" \
    "buffer_load_dwordx4 %[l16], %[rs], %[a] offen offset:3072 sc1 lds\n\t" \
    "s_waitcnt vmcnt(4)\n\t" \
    "s_barrier\n\t" \
    "s_setprio 1\n\t" \
    "s_add_u32 m0, %[sd], 0\n\t" \
    "s_add_u32 %[a], %[sx], 131072\n\t" \
    "s_add_u32 %[b], %[oo], 0x1000\n\t" \
    "s_cmp_lg_u32 %[t], 1\n\t" \
    "s_cselect_b64 %[em], -1, 0\n\t" \
    "v_mfma_f32_16x16x32_bf16 v[0:3], v[132:135], v[20:23], v[0:3]\n\t" \
    "ds_read_b128 v[44:47], %[xp] offset:22528\n\t" \
    "v_mfma_f32_16x16x32_bf16 v[4:7], v[140:143], v[20:23], v[4:7]\n\t" \
    "ds_read_b128 v[48:51], %[xp] offset:23552\n\t" \
    "v_mfma_f32_16x16x32_bf16 v[8:11], v[132:135], v[24:27], v[8:11]\n\t" \
    "ds_read_b128 v[52:55], %[pa] offset:0\n\t" \
    "s_waitcnt lgkmcnt(5)\n\t" \
    "v_mfma_f32_16x16x32_bf16 v[12:15], v[140:143], v[24:27], v[12:15]\n\t" \
    "buffer_load_dwordx4 %[l16], %[rs], %[a] offen offset:0 sc1 lds\n\t" \
    "v_mfma_f32_16x16x32_bf16 v[0:3], v[136:139], v[28:31], v[0:3]\n\t" \
    "ds_read_b128 v[20:23], %[xp] offset:24576\n\t" \
    "v_mfma_f32_16x16x32_bf16 v[4:7], v[144:147], v[28:31], v[4:7]\n\t" \
    "ds_read_b128 v[24:27], %[xp] offset:25600\n\t" \
    "v_mfma_f32_16x16x32_bf16 v[8:11], v[136:139], v[32:35], v[8:11]\n\t" \
    "ds_read_b128 v[56:59], %[pa] offset:1024\n\t" \
    "s_waitcnt lgkmcnt(6)\n\t" \
    "v_mfma_f32_16x16x32_bf16 v[12:15], v[144:147], v[32:35], v[12:15]\n\t" \
    "buffer_load_dwordx4 %[l16], %[rs], %[a] offen offset:1024 sc1 lds\n\t" \
    "v_mfma_f32_16x16x32_bf16 v[0:3], v[148:151], v[36:39], v[0:3]\n\t" \
    "ds_read_b128 v[28:31], %[xp] offset:26624\n\t" \
    "v_mfma_f32_16x16x32_bf16 v[4:7], v[156:159], v[36:39], v[4:7]\n\t" \
    "ds_read_b128 v[32:35], %[xp] offset:27648\n\t" \
    "v_mfma_f32_16x16x32_bf16 v[8:11], v[148:151], v[40:43], v[8:11]\n\t" \
    "ds_read_b128 v[60:63], %[pa] offset:2048\n\t" \
    "s_waitcnt lgkmcnt(7)\n\t" \
    "v_mfma_f32_16x16x32_bf16 v[12:15], v[156:159], v[40:43], v[12:15]\n\t" \
    "buffer_load_dwordx4 %[l16], %[rs], %[a] offen offset:2048 sc1 lds\n\t" \
    "v_mfma_f32_16x16x32_bf16 v[0:3], v[152:155], v[44:47], v[0:3]\n\t" \
    "ds_read_b128 v[36:39], %[xp] offset:28672\n\t" \
    "s_waitcnt lgkmcnt(7)\n\t" \
    "v_add_f32 v196, v196, v52\n\t" \
    "v_mfma_f32_16x16x32_bf16 v[4:7], v[160:163], v[44:47], v[4:7]\n\t" \
    "ds_read_b128 v[40:43], %[xp] offset:29696\n\t" \
    "v_add_f32 v197, v197, v53\n\t" \
    "v_mfma_f32_16x16x32_bf16 v[8:11], v[152:155], v[48:51], v[8:11]\n\t" \
    "ds_read_b128 v[64:67], %[pa] offset:3072\n\t" \
    "s_waitcnt lgkmcnt(7)\n\t" \
    "v_mfma_f32_16x16x32_bf16 v[12:15], v[160:163], v[48:51], v[12:15]\n\t" \
    "buffer_load_dwordx4 %[l16], %[rs], %[a] offen offset:3072 sc1 lds\n\t" \
    "v_add_f32 v198, v198, v54\n\t" \
    "v_add_f32 v199, v199, v55\n\t" \
    "v_mfma_f32_16x16x32_bf16 v[0:3], v[164:167], v[20:23], v[0:3]\n\t" \
    "ds_read_b128 v[44:47], %[xp] offset:30720\n\t" \
    "s_waitcnt lgkmcnt(7)\n\t" \
    "v_add_f32 v200, v200, v56\n\t" \
    "v_mfma_f32_16x16x32_bf16 v[4:7], v[172:175], v[20:23], v[4:7]\n\t" \
    "ds_read_b128 v[48:51], %[xp] offset:31744\n\t" \
    "v_add_f32 v201, v201, v57\n\t" \
    "v_mfma_f32_16x16x32_bf16 v[8:11], v[164:167], v[24:27], v[8:11]\n\t" \
    "v_add_f32 v202, v202, v58\n\t" \
    "s_waitcnt lgkmcnt(6)\n\t" \
    "v_mfma_f32_16x16x32_bf16 v[12:15], v[172:175], v[24:27], v[12:15]\n\t" \
    "v_add_f32 v203, v203, v59\n\t" \
    "s_waitcnt lgkmcnt(5)\n\t" \
    "v_add_f32 v204, v204, v60\n\t" \
    "v_add_f32 v205, v205, v61\n\t" \
    "v_mfma_f32_16x16x32_bf16 v[0:3], v[168:171], v[28:31], v[0:3]\n\t" \
    "ds_read_b128 v[20:23], %[xp] offset:32768\n\t" \
    "v_add_f32 v206, v206, v62\n\t" \
    "v_mfma_f32_16x16x32_bf16 v[4:7], v[176:179], v[28:31], v[4:7]\n\t" \
    "ds_read_b128 v[24:27], %[xp] offset:33792\n\t" \
    "v_add_f32 v207, v207, v63\n\t" \
    "v_mfma_f32_16x16x32_bf16 v[8:11], v[168:171], v[32:35], v[8:11]\n\t" \
    "s_waitcnt lgkmcnt(4)\n\t" \
    "v_add_f32 v208, v208, v64\n\t" \
    "v_mfma_f32_16x16x32_bf16 v[12:15], v[176:179], v[32:35], v[12:15]\n\t" \
    "v_add_f32 v209, v209, v65\n\t" \
    "v_add_f32 v210, v210, v66\n\t" \
    "v_add_f32 v211, v211, v67\n\t" \
    "v_mfma_f32_16x16x32_bf16 v[0:3], v[180:183], v[36:39], v[0:3]\n\t" \
    "ds_read_b128 v[28:31], %[xp] offset:34816\n\t" \
    "v_cvt_pk_bf16_f32 v52, v196, v200\n\t" \
    "v_mfma_f32_16x16x32_bf16 v[4:7], v[188:191], v[36:39], v[4:7]\n\t" \
    "ds_read_b128 v[32:35], %[xp] offset:35840\n\t" \
    "v_cvt_pk_bf16_f32 v53, v197, v201\n\t" \
    "v_mfma_f32_16x16x32_bf16 v[8:11], v[180:183], v[40:43], v[8:11]\n\t" \
    "v_cvt_pk_bf16_f32 v54, v198, v202\n\t" \
    "s_waitcnt lgkmcnt(4)\n\t" \
    "v_mfma_f32_16x16x32_bf16 v[12:15], v[188:191], v[40:43], v[12:15]\n\t" \
    "v_cvt_pk_bf16_f32 v55, v199, v203\n\t" \
    "v_cvt_pk_bf16_f32 v56, v204, v208\n\t" \
    "v_cvt_pk_bf16_f32 v57, v205, v209\n\t" \
    "v_mfma_f32_16x16x32_bf16 v[0:3], v[184:187], v[44:47], v[0:3]\n\t" \
    "ds_read_b128 v[36:39], %[xp] offset:36864\n\t" \
    "v_cvt_pk_bf16_f32 v58, v206, v210\n\t" \
    "v_mfma_f32_16x16x32_bf16 v[4:7], v[192:195], v[44:47], v[4:7]\n\t" \
    "ds_read_b128 v[40:43], %[xp] offset:37888\n\t" \
    "v_cvt_pk_bf16_f32 v59, v207, v211\n\t" \
    "v_mfma_f32_16x16x32_bf16 v[8:11], v[184:187], v[48:51], v[8:11]\n\t" \
    "s_waitcnt lgkmcnt(4)\n\t" \
    "v_mfma_f32_16x16x32_bf16 v[12:15], v[192:195], v[48:51], v[12:15]\n\t" \
    "s_mov_b64 exec, %[em]\n\t" \
    "buffer_store_dwordx4 v[52:55], %[l16], %[rs], %[oo] offen sc1\n\t" \
    "buffer_store_dwordx4 v[56:59], %[l16], %[rs], %[b] offen sc1\n\t" \
    "s_mov_b64 exec, -1\n\t" \
    "s_add_u32 %[sx], %[sx], 65536\n\t" \
    "s_add_u32 %[t], %[t], 1\n\t" \
    "s_add_u32 %[oo], %[oo], 262144\n\t" \
    "s_waitcnt vmcnt(6)\n\t" \
    "s_cmp_gt_u32 %[t], " STR(SQUICK) " + 1\n\t" \
    "s_cbranch_scc1 B0_%=\n\t" \
    "s_waitcnt vmcnt(0)\n\t" \
    "B0_%=:\n\t" \
    "s_barrier\n\t" \
    "s_setprio 1\n\t" \
    "s_add_u32 m0, %[sd], 16384\n\t" \
    "s_add_u32 %[a], %[sx], 81920\n\t" \
    "v_mfma_f32_16x16x32_bf16 v[196:199], v[68:71], v[20:23], 0\n\t" \
    "ds_read_b128 v[44:47], %[xp] offset:38912\n\t" \
    "v_mfma_f32_16x16x32_bf16 v[200:203], v[76:79], v[20:23], 0\n\t" \
    "ds_read_b128 v[48:51], %[xp] offset:39936\n\t" \
    "v_mfma_f32_16x16x32_bf16 v[204:207], v[68:71], v[24:27], 0\n\t" \
    "s_waitcnt lgkmcnt(4)\n\t" \
    "v_mfma_f32_16x16x32_bf16 v[208:211], v[76:79], v[24:27], 0\n\t" \
    "v_mfma_f32_16x16x32_bf16 v[196:199], v[72:75], v[28:31], v[196:199]\n\t" \
    "ds_read_b128 v[20:23], %[xp] offset:40960\n\t" \
    "v_mfma_f32_16x16x32_bf16 v[200:203], v[80:83], v[28:31], v[200:203]\n\t" \
    "ds_read_b128 v[24:27], %[xp] offset:41984\n\t" \
    "v_mfma_f32_16x16x32_bf16 v[204:207], v[72:75], v[32:35], v[204:207]\n\t" \
    "s_waitcnt lgkmcnt(4)\n\t" \
    "v_mfma_f32_16x16x32_bf16 v[208:211], v[80:83], v[32:35], v[208:211]\n\t" \
    "v_mfma_f32_16x16x32_bf16 v[196:199], v[84:87], v[36:39], v[196:199]\n\t" \
    "ds_read_b128 v[28:31], %[xp] offset:43008\n\t" \
    "v_mfma_f32_16x16x32_bf16 v[200:203], v[92:95], v[36:39], v[200:203]\n\t" \
    "ds_read_b128 v[32:35], %[xp] offset:44032\n\t" \
    "v_mfma_f32_16x16x32_bf16 v[204:207], v[84:87], v[40:43], v[204:207]\n\t" \
    "s_waitcnt lgkmcnt(4)\n\t" \
    "v_mfma_f32_16x16x32_bf16 v[208:211], v[92:95], v[40:43], v[208:211]\n\t" \
    "v_mfma_f32_16x16x32_bf16 v[196:199], v[88:91], v[44:47], v[196:199]\n\t" \
    "ds_read_b128 v[36:39], %[xp] offset:45056\n\t" \
    "v_mfma_f32_16x16x32_bf16 v[200:203], v[96:99], v[44:47], v[200:203]\n\t" \
    "ds_read_b128 v[40:43], %[xp] offset:46080\n\t" \
    "v_mfma_f32_16x16x32_bf16 v[204:207], v[88:91], v[48:51], v[204:207]\n\t" \
    "s_waitcnt lgkmcnt(4)\n\t" \
    "v_mfma_f32_16x16x32_bf16 v[208:211], v[96:99], v[48:51], v[208:211]\n\t" \
    "v_mfma_f32_16x16x32_bf16 v[196:199], v[100:103], v[20:23], v[196:199]\n\t" \
    "ds_read_b128 v[44:47], %[xp] offset:47104\n\t" \
    "v_mfma_f32_16x16x32_bf16 v[200:203], v[108:111], v[20:23], v[200:203]\n\t" \
    "ds_read_b128 v[48:51], %[xp] offset:48128\n\t" \
    "v_mfma_f32_16x16x32_bf16 v[204:207], v[100:103], v[24:27], v[204:207]\n\t" \
    "s_waitcnt lgkmcnt(4)\n\t" \
    "v_mfma_f32_16x16x32_bf16 v[208:211], v[108:111], v[24:27], v[208:211]\n\t" \
    "buffer_load_dwordx4 %[l16], %[rs], %[a] offen offset:0 sc1 lds\n\t" \
    "v_mfma_f32_16x16x32_bf16 v[196:199], v[104:107], v[28:31], v[196:199]\n\t" \
    "ds_read_b128 v[20:23], %[xp] offset:49152\n\t" \
    "v_mfma_f32_16x16x32_bf16 v[200:203], v[112:115], v[28:31], v[200:203]\n\t" \
    "ds_read_b128 v[24:27], %[xp] offset:50176\n\t" \
    "v_mfma_f32_16x16x32_bf16 v[204:207], v[104:107], v[32:35], v[204:207]\n\t" \
    "s_waitcnt lgkmcnt(4)\n\t" \
    "v_mfma_f32_16x16x32_bf16 v[208:211], v[112:115], v[32:35], v[208:211]\n\t" \
    "buffer_load_dwordx4 %[l16], %[rs], %[a] offen offset:1024 sc1 lds\n\t" \
    "v_mfma_f32_16x16x32_bf16 v[196:199], v[116:119], v[36:39], v[196:199]\n\t" \
    "ds_read_b128 v[28:31], %[xp] offset:51200\n\t" \
    "v_mfma_f32_16x16x32_bf16 v[200:203], v[124:127], v[36:39], v[200:203]\n\t" \
    "ds_read_b128 v[32:35], %[xp] offset:52224\n\t" \
    "v_mfma_f32_16x16x32_bf16 v[204:207], v[116:119], v[40:43], v[204:207]\n\t" \
    "s_waitcnt lgkmcnt(4)\n\t" \
    "v_mfma_f32_16x16x32_bf16 v[208:211], v[124:127], v[40:43], v[208:211]\n\t" \
    "buffer_load_dwordx4 %[l16], %[rs], %[a] offen offset:2048 sc1 lds\n\t" \
    "v_mfma_f32_16x16x32_bf16 v[196:199], v[120:123], v[44:47], v[196:199]\n\t" \
    "ds_read_b128 v[36:39], %[xp] offset:53248\n\t" \
    "v_mfma_f32_16x16x32_bf16 v[200:203], v[128:131], v[44:47], v[200:203]\n\t" \
    "ds_read_b128 v[40:43], %[xp] offset:54272\n\t" \
    "v_mfma_f32_16x16x32_bf16 v[204:207], v[120:123], v[48:51], v[204:207]\n\t" \
    "s_waitcnt lgkmcnt(4)\n\t" \
    "v_mfma_f32_16x16x32_bf16 v[208:211], v[128:131], v[48:51], v[208:211]\n\t" \
    "buffer_load_dwordx4 %[l16], %[rs], %[a] offen offset:3072 sc1 lds\n\t" \
    "s_waitcnt vmcnt(4)\n\t" \
    "s_barrier\n\t" \
    "s_setprio 1\n\t" \
    "s_add_u32 m0, %[sd], 32768\n\t" \
    "s_add_u32 %[a], %[sx], 131072\n\t" \
    "s_add_u32 %[b], %[oo], 0x1000\n\t" \
    "s_cmp_lg_u32 %[t], 1\n\t" \
    "s_cselect_b64 %[em], -1, 0\n\t" \
    "v_mfma_f32_16x16x32_bf16 v[196:199], v[132:135], v[20:23], v[196:199]\n\t" \
    "ds_read_b128 v[44:47], %[xp] offset:55296\n\t" \
    "v_mfma_f32_16x16x32_bf16 v[200:203], v[140:143], v[20:23], v[200:203]\n\t" \
    "ds_read_b128 v[48:51], %[xp] offset:56320\n\t" \
    "v_mfma_f32_16x16x32_bf16 v[204:207], v[132:135], v[24:27], v[204:207]\n\t" \
    "ds_read_b128 v[52:55], %[pa] offset:0\n\t" \
    "s_waitcnt lgkmcnt(5)\n\t" \
    "v_mfma_f32_16x16x32_bf16 v[208:211], v[140:143], v[24:27], v[208:211]\n\t" \
    "buffer_load_dwordx4 %[l16], %[rs], %[a] offen offset:0 sc1 lds\n\t" \
    "v_mfma_f32_16x16x32_bf16 v[196:199], v[136:139], v[28:31], v[196:199]\n\t" \
    "ds_read_b128 v[20:23], %[xp] offset:57344\n\t" \
    "v_mfma_f32_16x16x32_bf16 v[200:203], v[144:147], v[28:31], v[200:203]\n\t" \
    "ds_read_b128 v[24:27], %[xp] offset:58368\n\t" \
    "v_mfma_f32_16x16x32_bf16 v[204:207], v[136:139], v[32:35], v[204:207]\n\t" \
    "ds_read_b128 v[56:59], %[pa] offset:1024\n\t" \
    "s_waitcnt lgkmcnt(6)\n\t" \
    "v_mfma_f32_16x16x32_bf16 v[208:211], v[144:147], v[32:35], v[208:211]\n\t" \
    "buffer_load_dwordx4 %[l16], %[rs], %[a] offen offset:1024 sc1 lds\n\t" \
    "v_mfma_f32_16x16x32_bf16 v[196:199], v[148:151], v[36:39], v[196:199]\n\t" \
    "ds_read_b128 v[28:31], %[xp] offset:59392\n\t" \
    "v_mfma_f32_16x16x32_bf16 v[200:203], v[156:159], v[36:39], v[200:203]\n\t" \
    "ds_read_b128 v[32:35], %[xp] offset:60416\n\t" \
    "v_mfma_f32_16x16x32_bf16 v[204:207], v[148:151], v[40:43], v[204:207]\n\t" \
    "ds_read_b128 v[60:63], %[pa] offset:2048\n\t" \
    "s_waitcnt lgkmcnt(7)\n\t" \
    "v_mfma_f32_16x16x32_bf16 v[208:211], v[156:159], v[40:43], v[208:211]\n\t" \
    "buffer_load_dwordx4 %[l16], %[rs], %[a] offen offset:2048 sc1 lds\n\t" \
    "v_mfma_f32_16x16x32_bf16 v[196:199], v[152:155], v[44:47], v[196:199]\n\t" \
    "ds_read_b128 v[36:39], %[xp] offset:61440\n\t" \
    "s_waitcnt lgkmcnt(7)\n\t" \
    "v_add_f32 v0, v0, v52\n\t" \
    "v_mfma_f32_16x16x32_bf16 v[200:203], v[160:163], v[44:47], v[200:203]\n\t" \
    "ds_read_b128 v[40:43], %[xp] offset:62464\n\t" \
    "v_add_f32 v1, v1, v53\n\t" \
    "v_mfma_f32_16x16x32_bf16 v[204:207], v[152:155], v[48:51], v[204:207]\n\t" \
    "ds_read_b128 v[64:67], %[pa] offset:3072\n\t" \
    "s_waitcnt lgkmcnt(7)\n\t" \
    "v_mfma_f32_16x16x32_bf16 v[208:211], v[160:163], v[48:51], v[208:211]\n\t" \
    "buffer_load_dwordx4 %[l16], %[rs], %[a] offen offset:3072 sc1 lds\n\t" \
    "v_add_f32 v2, v2, v54\n\t" \
    "v_add_f32 v3, v3, v55\n\t" \
    "v_mfma_f32_16x16x32_bf16 v[196:199], v[164:167], v[20:23], v[196:199]\n\t" \
    "ds_read_b128 v[44:47], %[xp] offset:63488\n\t" \
    "s_waitcnt lgkmcnt(7)\n\t" \
    "v_add_f32 v4, v4, v56\n\t" \
    "v_mfma_f32_16x16x32_bf16 v[200:203], v[172:175], v[20:23], v[200:203]\n\t" \
    "ds_read_b128 v[48:51], %[xp] offset:64512\n\t" \
    "v_add_f32 v5, v5, v57\n\t" \
    "v_mfma_f32_16x16x32_bf16 v[204:207], v[164:167], v[24:27], v[204:207]\n\t" \
    "v_add_f32 v6, v6, v58\n\t" \
    "s_waitcnt lgkmcnt(6)\n\t" \
    "v_mfma_f32_16x16x32_bf16 v[208:211], v[172:175], v[24:27], v[208:211]\n\t" \
    "v_add_f32 v7, v7, v59\n\t" \
    "s_waitcnt lgkmcnt(5)\n\t" \
    "v_add_f32 v8, v8, v60\n\t" \
    "v_add_f32 v9, v9, v61\n\t" \
    "v_mfma_f32_16x16x32_bf16 v[196:199], v[168:171], v[28:31], v[196:199]\n\t" \
    "ds_read_b128 v[20:23], %[xp] offset:0\n\t" \
    "v_add_f32 v10, v10, v62\n\t" \
    "v_mfma_f32_16x16x32_bf16 v[200:203], v[176:179], v[28:31], v[200:203]\n\t" \
    "ds_read_b128 v[24:27], %[xp] offset:1024\n\t" \
    "v_add_f32 v11, v11, v63\n\t" \
    "v_mfma_f32_16x16x32_bf16 v[204:207], v[168:171], v[32:35], v[204:207]\n\t" \
    "s_waitcnt lgkmcnt(4)\n\t" \
    "v_add_f32 v12, v12, v64\n\t" \
    "v_mfma_f32_16x16x32_bf16 v[208:211], v[176:179], v[32:35], v[208:211]\n\t" \
    "v_add_f32 v13, v13, v65\n\t" \
    "v_add_f32 v14, v14, v66\n\t" \
    "v_add_f32 v15, v15, v67\n\t" \
    "v_mfma_f32_16x16x32_bf16 v[196:199], v[180:183], v[36:39], v[196:199]\n\t" \
    "ds_read_b128 v[28:31], %[xp] offset:2048\n\t" \
    "v_cvt_pk_bf16_f32 v52, v0, v4\n\t" \
    "v_mfma_f32_16x16x32_bf16 v[200:203], v[188:191], v[36:39], v[200:203]\n\t" \
    "ds_read_b128 v[32:35], %[xp] offset:3072\n\t" \
    "v_cvt_pk_bf16_f32 v53, v1, v5\n\t" \
    "v_mfma_f32_16x16x32_bf16 v[204:207], v[180:183], v[40:43], v[204:207]\n\t" \
    "v_cvt_pk_bf16_f32 v54, v2, v6\n\t" \
    "s_waitcnt lgkmcnt(4)\n\t" \
    "v_mfma_f32_16x16x32_bf16 v[208:211], v[188:191], v[40:43], v[208:211]\n\t" \
    "v_cvt_pk_bf16_f32 v55, v3, v7\n\t" \
    "v_cvt_pk_bf16_f32 v56, v8, v12\n\t" \
    "v_cvt_pk_bf16_f32 v57, v9, v13\n\t" \
    "v_mfma_f32_16x16x32_bf16 v[196:199], v[184:187], v[44:47], v[196:199]\n\t" \
    "ds_read_b128 v[36:39], %[xp] offset:4096\n\t" \
    "v_cvt_pk_bf16_f32 v58, v10, v14\n\t" \
    "v_mfma_f32_16x16x32_bf16 v[200:203], v[192:195], v[44:47], v[200:203]\n\t" \
    "ds_read_b128 v[40:43], %[xp] offset:5120\n\t" \
    "v_cvt_pk_bf16_f32 v59, v11, v15\n\t" \
    "v_mfma_f32_16x16x32_bf16 v[204:207], v[184:187], v[48:51], v[204:207]\n\t" \
    "s_waitcnt lgkmcnt(4)\n\t" \
    "v_mfma_f32_16x16x32_bf16 v[208:211], v[192:195], v[48:51], v[208:211]\n\t" \
    "s_mov_b64 exec, %[em]\n\t" \
    "buffer_store_dwordx4 v[52:55], %[l16], %[rs], %[oo] offen sc1\n\t" \
    "buffer_store_dwordx4 v[56:59], %[l16], %[rs], %[b] offen sc1\n\t" \
    "s_mov_b64 exec, -1\n\t" \
    "s_add_u32 %[sx], %[sx], 65536\n\t" \
    "s_add_u32 %[t], %[t], 1\n\t" \
    "s_add_u32 %[oo], %[oo], 262144\n\t" \
    "s_waitcnt vmcnt(6)\n\t" \
    "s_cmp_gt_u32 %[t], " STR(SQUICK) " + 1\n\t" \
    "s_cbranch_scc1 B1_%=\n\t" \
    "s_waitcnt vmcnt(0)\n\t" \
    "B1_%=:\n\t" \
    "s_barrier\n\t" \
    "s_cmp_lt_u32 %[t], %[te]\n\t" \
    "s_cbranch_scc1 L1_%=\n\t" \
    "s_cmp_eq_u32 %[fin], %[te]\n\t" \
    "s_cbranch_scc0 E_%=\n\t" \
    "s_nop 4\n\t" \
    "s_barrier\n\t" \
    "ds_read_b128 v[52:55], %[pa] offset:0\n\t" \
    "ds_read_b128 v[56:59], %[pa] offset:1024\n\t" \
    "ds_read_b128 v[60:63], %[pa] offset:2048\n\t" \
    "ds_read_b128 v[64:67], %[pa] offset:3072\n\t" \
    "s_add_u32 %[b], %[oo], 0x1000\n\t" \
    "s_waitcnt lgkmcnt(0)\n\t" \
    "v_add_f32 v196, v196, v52\n\t" \
    "v_add_f32 v197, v197, v53\n\t" \
    "v_add_f32 v198, v198, v54\n\t" \
    "v_add_f32 v199, v199, v55\n\t" \
    "v_add_f32 v200, v200, v56\n\t" \
    "v_add_f32 v201, v201, v57\n\t" \
    "v_add_f32 v202, v202, v58\n\t" \
    "v_add_f32 v203, v203, v59\n\t" \
    "v_add_f32 v204, v204, v60\n\t" \
    "v_add_f32 v205, v205, v61\n\t" \
    "v_add_f32 v206, v206, v62\n\t" \
    "v_add_f32 v207, v207, v63\n\t" \
    "v_add_f32 v208, v208, v64\n\t" \
    "v_add_f32 v209, v209, v65\n\t" \
    "v_add_f32 v210, v210, v66\n\t" \
    "v_add_f32 v211, v211, v67\n\t" \
    "v_cvt_pk_bf16_f32 v52, v196, v200\n\t" \
    "v_cvt_pk_bf16_f32 v53, v197, v201\n\t" \
    "v_cvt_pk_bf16_f32 v54, v198, v202\n\t" \
    "v_cvt_pk_bf16_f32 v55, v199, v203\n\t" \
    "v_cvt_pk_bf16_f32 v56, v204, v208\n\t" \
    "v_cvt_pk_bf16_f32 v57, v205, v209\n\t" \
    "v_cvt_pk_bf16_f32 v58, v206, v210\n\t" \
    "v_cvt_pk_bf16_f32 v59, v207, v211\n\t" \
    "buffer_store_dwordx4 v[52:55], %[l16], %[rs], %[oo] offen sc1\n\t" \
    "buffer_store_dwordx4 v[56:59], %[l16], %[rs], %[b] offen sc1\n\t" \
    "E_%=:\n\t" \
    "s_setprio 0\n\t" \
    "s_waitcnt lgkmcnt(0)\n\t" \
    "s_nop 7\n\t" \
    : [t] "+s"(t), [sx] "+s"(sx), [oo] "+s"(oo), [a] "=&s"(tmp), [b] "=&s"(tmp2), [em] "=&s"(em), "+{v[20:27]}"(xf[0]), "+{v[28:35]}"(xf[1]), "+{v[36:43]}"(xf[2]), "+{v[196:203]}"(ac[0]), "+{v[204:211]}"(ac[1]) \
    : [te] "s"(TE_), [fin] "s"(FIN_), [kh] "s"(kh), [sd] "s"(sd), [rs] "s"(rs), [l16] "v"(l16), [xp] "v"(xp), [pa] "v"(pa), [fm] "s"(fm), [pm] "s"(pm), [fo] "s"(fo), [po] "s"(po), [pd] "s"(pd), [tag] "s"(P.tag), "{v[68:75]}"(w[0][0]), "{v[76:83]}"(w[0][1]), "{v[84:91]}"(w[1][0]), "{v[92:99]}"(w[1][1]), "{v[100:107]}"(w[2][0]), "{v[108:115]}"(w[2][1]), "{v[116:123]}"(w[3][0]), "{v[124:131]}"(w[3][1]), "{v[132:139]}"(w[4][0]), "{v[140:147]}"(w[4][1]), "{v[148:155]}"(w[5][0]), "{v[156:163]}"(w[5][1]), "{v[164:171]}"(w[6][0]), "{v[172:179]}"(w[6][1]), "{v[180:187]}"(w[7][0]), "{v[188:195]}"(w[7][1]) \
    : "memory", "scc", "v0", "v1", "v2", "v3", "v4", "v5", "v6", "v7", "v8", "v9", "v10", "v11", "v12", "v13", "v14", "v15", "v44", "v45", "v46", "v47", "v48", "v49", "v50", "v51", "v52", "v53", "v54", "v55", "v56", "v57", "v58", "v59", "v60", "v61", "v62", "v63", "v64", "v65", "v66", "v67")

#define S_LOOP_V(TE_, FIN_) \
  asm volatile( \
    "s_cmp_eq_u32 %[kh], 0\n\t" \
    "s_cbranch_scc0 L1_%=\n\t" \
    "L0_%=:\n\t" \
    "s_setprio 0\n\t" \
    "s_nop 3\n\t" \
    "ds_write_b128 %[pa], v[196:199] offset:0\n\t" \
    "ds_write_b128 %[pa], v[200:203] offset:1024\n\t" \
    "ds_write_b128 %[pa], v[204:207] offset:2048\n\t" \
    "ds_write_b128 %[pa], v[208:211] offset:3072\n\t" \
    "s_sub_u32 %[a], %[t], 1\n\t" \
    "v_mov_b32 v53, %[tag]\n\t" \
    "v_mov_b32 v52, %[a]\n\t" \
    "s_sub_u32 %[b], %[t], 2\n\t" \
    "s_cmp_lt_u32 %[b], " STR(SQUICK) "\n\t" \
    "s_cselect_b64 exec, %[fm], 0\n\t" \
    "buffer_store_dwordx2 v[52:53], %[l16], %[rs], %[fo] offen sc1\n\t" \
    "s_mov_b64 exec, -1\n\t" \
    "s_add_u32 m0, %[sd], 49152\n\t" \
    "s_add_u32 %[a], %[sx], 81920\n\t" \
    "s_waitcnt lgkmcnt(0)\n\t" \
    "buffer_load_dwordx4 %[l16], %[rs], %[a] offen offset:0 sc1 lds\n\t" \
    "buffer_load_dwordx4 %[l16], %[rs], %[a] offen offset:1024 sc1 lds\n\t" \
    "buffer_load_dwordx4 %[l16], %[rs], %[a] offen offset:2048 sc1 lds\n\t" \
    "buffer_load_dwordx4 %[l16], %[rs], %[a] offen offset:3072 sc1 lds\n\t" \
    "v_mfma_f32_16x16x32_bf16 v[0:3], v[20:23], v[68:71], 0\n\t" \
    "ds_read_b128 v[44:47], %[xp] offset:6144\n\t" \
    "v_mfma_f32_16x16x32_bf16 v[4:7], v[20:23], v[76:79], 0\n\t" \
    "ds_read_b128 v[48:51], %[xp] offset:7168\n\t" \
    "v_mfma_f32_16x16x32_bf16 v[8:11], v[24:27], v[68:71], 0\n\t" \
    "v_mfma_f32_16x16x32_bf16 v[12:15], v[24:27], v[76:79], 0\n\t" \
    "v_mfma_f32_16x16x32_bf16 v[0:3], v[28:31], v[72:75], v[0:3]\n\t" \
    "ds_read_b128 v[20:23], %[xp] offset:8192\n\t" \
    "v_mfma_f32_16x16x32_bf16 v[4:7], v[28:31], v[80:83], v[4:7]\n\t" \
    "ds_read_b128 v[24:27], %[xp] offset:9216\n\t" \
    "v_mfma_f32_16x16x32_bf16 v[8:11], v[32:35], v[72:75], v[8:11]\n\t" \
    "v_mfma_f32_16x16x32_bf16 v[12:15], v[32:35], v[80:83], v[12:15]\n\t" \
    "v_mfma_f32_16x16x32_bf16 v[0:3], v[36:39], v[84:87], v[0:3]\n\t" \
    "ds_read_b128 v[28:31], %[xp] offset:10240\n\t" \
    "v_mfma_f32_16x16x32_bf16 v[4:7], v[36:39], v[92:95], v[4:7]\n\t" \
    "ds_read_b128 v[32:35], %[xp] offset:11264\n\t" \
    "v_mfma_f32_16x16x32_bf16 v[8:11], v[40:43], v[84:87], v[8:11]\n\t" \
    "s_waitcnt lgkmcnt(4)\n\t" \
    "v_mfma_f32_16x16x32_bf16 v[12:15], v[40:43], v[92:95], v[12:15]\n\t" \
    "v_mfma_f32_16x16x32_bf16 v[0:3], v[44:47], v[88:91], v[0:3]\n\t" \
    "ds_read_b128 v[36:39], %[xp] offset:12288\n\t" \
    "v_mfma_f32_16x16x32_bf16 v[4:7], v[44:47], v[96:99], v[4:7]\n\t" \
    "ds_read_b128 v[40:43], %[xp] offset:13312\n\t" \
    "v_mfma_f32_16x16x32_bf16 v[8:11], v[48:51], v[88:91], v[8:11]\n\t" \
    "s_waitcnt lgkmcnt(4)\n\t" \
    "v_mfma_f32_16x16x32_bf16 v[12:15], v[48:51], v[96:99], v[12:15]\n\t" \
    "v_mfma_f32_16x16x32_bf16 v[0:3], v[20:23], v[100:103], v[0:3]\n\t" \
    "ds_read_b128 v[44:47], %[xp] offset:14336\n\t" \
    "v_mfma_f32_16x16x32_bf16 v[4:7], v[20:23], v[108:111], v[4:7]\n\t" \
    "ds_read_b128 v[48:51], %[xp] offset:15360\n\t" \
    "v_mfma_f32_16x16x32_bf16 v[8:11], v[24:27], v[100:103], v[8:11]\n\t" \
    "s_waitcnt lgkmcnt(4)\n\t" \
    "v_mfma_f32_16x16x32_bf16 v[12:15], v[24:27], v[108:111], v[12:15]\n\t" \
    "v_mfma_f32_16x16x32_bf16 v[0:3], v[28:31], v[104:107], v[0:3]\n\t" \
    "ds_read_b128 v[20:23], %[xp] offset:16384\n\t" \
    "v_mfma_f32_16x16x32_bf16 v[4:7], v[28:31], v[112:115], v[4:7]\n\t" \
    "ds_read_b128 v[24:27], %[xp] offset:17408\n\t" \
    "v_mfma_f32_16x16x32_bf16 v[8:11], v[32:35], v[104:107], v[8:11]\n\t" \
    "s_waitcnt lgkmcnt(4)\n\t" \
    "v_mfma_f32_16x16x32_bf16 v[12:15], v[32:35], v[112:115], v[12:15]\n\t" \
    "v_mfma_f32_16x16x32_bf16 v[0:3], v[36:39], v[116:119], v[0:3]\n\t" \
    "ds_read_b128 v[28:31], %[xp] offset:18432\n\t" \
    "v_mfma_f32_16x16x32_bf16 v[4:7], v[36:39], v[124:127], v[4:7]\n\t" \
    "ds_read_b128 v[32:35], %[xp] offset:19456\n\t" \
    "v_mfma_f32_16x16x32_bf16 v[8:11], v[40:43], v[116:119], v[8:11]\n\t" \
    "s_waitcnt lgkmcnt(4)\n\t" \
    "v_mfma_f32_16x16x32_bf16 v[12:15], v[40:43], v[124:127], v[12:15]\n\t" \
    "v_mfma_f32_16x16x32_bf16 v[0:3], v[44:47], v[120:123], v[0:3]\n\t" \
    "ds_read_b128 v[36:39], %[xp] offset:20480\n\t" \
    "v_mfma_f32_16x16x32_bf16 v[4:7], v[44:47], v[128:131], v[4:7]\n\t" \
    "ds_read_b128 v[40:43], %[xp] offset:21504\n\t" \
    "v_mfma_f32_16x16x32_bf16 v[8:11], v[48:51], v[120:123], v[8:11]\n\t" \
    "s_waitcnt lgkmcnt(4)\n\t" \
    "v_mfma_f32_16x16x32_bf16 v[12:15], v[48:51], v[128:131], v[12:15]\n\t" \
    "s_waitcnt vmcnt(4)\n\t" \
    "s_barrier\n\t" \
    "s_setprio 0\n\t" \
    "s_sub_u32 %[a], %[t], 1\n\t" \
    "v_mov_b32 v53, %[tag]\n\t" \
    "v_mov_b32 v52, %[a]\n\t" \
    "s_cmp_gt_u32 %[t], 1\n\t" \
    "s_cselect_b64 exec, %[fm], 0\n\t" \
    "buffer_store_dwordx2 v[52:53], %[l16], %[rs], %[fo] offen sc1\n\t" \
    "s_mov_b64 exec, %[pm]\n\t" \
    "s_mov_b32 m0, %[pd]\n\t" \
    "s_add_u32 %[a], %[sx], %[po]\n\t" \
    "buffer_load_dwordx4 %[l16], %[rs], %[a] offen sc1 lds\n\t" \
    "s_mov_b64 exec, -1\n\t" \
    "s_add_u32 m0, %[sd], 0\n\t" \
    "s_add_u32 %[a], %[sx], 131072\n\t" \
    "buffer_load_dwordx4 %[l16], %[rs], %[a] offen offset:0 sc1 lds\n\t" \
    "buffer_load_dwordx4 %[l16], %[rs], %[a] offen offset:1024 sc1 lds\n\t" \
    "buffer_load_dwordx4 %[l16], %[rs], %[a] offen offset:2048 sc1 lds\n\t" \
    "buffer_load_dwordx4 %[l16], %[rs], %[a] offen offset:3072 sc1 lds\n\t" \
    "v_mfma_f32_16x16x32_bf16 v[0:3], v[20:23], v[132:135], v[0:3]\n\t" \
    "ds_read_b128 v[44:47], %[xp] offset:22528\n\t" \
    "v_mfma_f32_16x16x32_bf16 v[4:7], v[20:23], v[140:143], v[4:7]\n\t" \
    "ds_read_b128 v[48:51], %[xp] offset:23552\n\t" \
    "v_mfma_f32_16x16x32_bf16 v[8:11], v[24:27], v[132:135], v[8:11]\n\t" \
    "s_waitcnt lgkmcnt(4)\n\t" \
    "v_mfma_f32_16x16x32_bf16 v[12:15], v[24:27], v[140:143], v[12:15]\n\t" \
    "v_mfma_f32_16x16x32_bf16 v[0:3], v[28:31], v[136:139], v[0:3]\n\t" \
    "ds_read_b128 v[20:23], %[xp] offset:24576\n\t" \
    "v_mfma_f32_16x16x32_bf16 v[4:7], v[28:31], v[144:147], v[4:7]\n\t" \
    "ds_read_b128 v[24:27], %[xp] offset:25600\n\t" \
    "v_mfma_f32_16x16x32_bf16 v[8:11], v[32:35], v[136:139], v[8:11]\n\t" \
    "s_waitcnt lgkmcnt(4)\n\t" \
    "v_mfma_f32_16x16x32_bf16 v[12:15], v[32:35], v[144:147], v[12:15]\n\t" \
    "v_mfma_f32_16x16x32_bf16 v[0:3], v[36:39], v[148:151], v[0:3]\n\t" \
    "ds_read_b128 v[28:31], %[xp] offset:26624\n\t" \
    "v_mfma_f32_16x16x32_bf16 v[4:7], v[36:39], v[156:159], v[4:7]\n\t" \
    "ds_read_b128 v[32:35], %[xp] offset:27648\n\t" \
    "v_mfma_f32_16x16x32_bf16 v[8:11], v[40:43], v[148:151], v[8:11]\n\t" \
    "s_waitcnt lgkmcnt(4)\n\t" \
    "v_mfma_f32_16x16x32_bf16 v[12:15], v[40:43], v[156:159], v[12:15]\n\t" \
    "v_mfma_f32_16x16x32_bf16 v[0:3], v[44:47], v[152:155], v[0:3]\n\t" \
    "ds_read_b128 v[36:39], %[xp] offset:28672\n\t" \
    "v_mfma_f32_16x16x32_bf16 v[4:7], v[44:47], v[160:163], v[4:7]\n\t" \
    "ds_read_b128 v[40:43], %[xp] offset:29696\n\t" \
    "v_mfma_f32_16x16x32_bf16 v[8:11], v[48:51], v[152:155], v[8:11]\n\t" \
    "s_waitcnt lgkmcnt(4)\n\t" \
    "v_mfma_f32_16x16x32_bf16 v[12:15], v[48:51], v[160:163], v[12:15]\n\t" \
    "v_mfma_f32_16x16x32_bf16 v[0:3], v[20:23], v[164:167], v[0:3]\n\t" \
    "ds_read_b128 v[44:47], %[xp] offset:30720\n\t" \
    "v_mfma_f32_16x16x32_bf16 v[4:7], v[20:23], v[172:175], v[4:7]\n\t" \
    "ds_read_b128 v[48:51], %[xp] offset:31744\n\t" \
    "v_mfma_f32_16x16x32_bf16 v[8:11], v[24:27], v[164:167], v[8:11]\n\t" \
    "s_waitcnt lgkmcnt(4)\n\t" \
    "v_mfma_f32_16x16x32_bf16 v[12:15], v[24:27], v[172:175], v[12:15]\n\t" \
    "v_mfma_f32_16x16x32_bf16 v[0:3], v[28:31], v[168:171], v[0:3]\n\t" \
    "ds_read_b128 v[20:23], %[xp] offset:32768\n\t" \
    "v_mfma_f32_16x16x32_bf16 v[4:7], v[28:31], v[176:179], v[4:7]\n\t" \
    "ds_read_b128 v[24:27], %[xp] offset:33792\n\t" \
    "v_mfma_f32_16x16x32_bf16 v[8:11], v[32:35], v[168:171], v[8:11]\n\t" \
    "s_waitcnt lgkmcnt(4)\n\t" \
    "v_mfma_f32_16x16x32_bf16 v[12:15], v[32:35], v[176:179], v[12:15]\n\t" \
    "v_mfma_f32_16x16x32_bf16 v[0:3], v[36:39], v[180:183], v[0:3]\n\t" \
    "ds_read_b128 v[28:31], %[xp] offset:34816\n\t" \
    "v_mfma_f32_16x16x32_bf16 v[4:7], v[36:39], v[188:191], v[4:7]\n\t" \
    "ds_read_b128 v[32:35], %[xp] offset:35840\n\t" \
    "v_mfma_f32_16x16x32_bf16 v[8:11], v[40:43], v[180:183], v[8:11]\n\t" \
    "s_waitcnt lgkmcnt(4)\n\t" \
    "v_mfma_f32_16x16x32_bf16 v[12:15], v[40:43], v[188:191], v[12:15]\n\t" \
    "v_mfma_f32_16x16x32_bf16 v[0:3], v[44:47], v[184:187], v[0:3]\n\t" \
    "ds_read_b128 v[36:39], %[xp] offset:36864\n\t" \
    "v_mfma_f32_16x16x32_bf16 v[4:7], v[44:47], v[192:195], v[4:7]\n\t" \
    "ds_read_b128 v[40:43], %[xp] offset:37888\n\t" \
    "v_mfma_f32_16x16x32_bf16 v[8:11], v[48:51], v[184:187], v[8:11]\n\t" \
    "s_waitcnt lgkmcnt(4)\n\t" \
    "v_mfma_f32_16x16x32_bf16 v[12:15], v[48:51], v[192:195], v[12:15]\n\t" \
    "s_add_u32 %[sx], %[sx], 65536\n\t" \
    "s_add_u32 %[t], %[t], 1\n\t" \
    "s_waitcnt vmcnt(4)\n\t" \
    "s_barrier\n\t" \
    "s_setprio 0\n\t" \
    "s_nop 3\n\t" \
    "ds_write_b128 %[pa], v[0:3] offset:0\n\t" \
    "ds_write_b128 %[pa], v[4:7] offset:1024\n\t" \
    "ds_write_b128 %[pa], v[8:11] offset:2048\n\t" \
    "ds_write_b128 %[pa], v[12:15] offset:3072\n\t" \
    "s_sub_u32 %[a], %[t], 1\n\t" \
    "v_mov_b32 v53, %[tag]\n\t" \
    "v_mov_b32 v52, %[a]\n\t" \
    "s_sub_u32 %[b], %[t], 2\n\t" \
    "s_cmp_lt_u32 %[b], " STR(SQUICK) "\n\t" \
    "s_cselect_b64 exec, %[fm], 0\n\t" \
    "buffer_store_dwordx2 v[52:53], %[l16], %[rs], %[fo] offen sc1\n\t" \
    "s_mov_b64 exec, -1\n\t" \
    "s_add_u32 m0, %[sd], 16384\n\t" \
    "s_add_u32 %[a], %[sx], 81920\n\t" \
    "s_waitcnt lgkmcnt(0)\n\t" \
    "buffer_load_dwordx4 %[l16], %[rs], %[a] offen offset:0 sc1 lds\n\t" \
    "buffer_load_dwordx4 %[l16], %[rs], %[a] offen offset:1024 sc1 lds\n\t" \
    "buffer_load_dwordx4 %[l16], %[rs], %[a] offen offset:2048 sc1 lds\n\t" \
    "buffer_load_dwordx4 %[l16], %[rs], %[a] offen offset:3072 sc1 lds\n\t" \
    "v_mfma_f32_16x16x32_bf16 v[196:199], v[20:23], v[68:71], 0\n\t" \
    "ds_read_b128 v[44:47], %[xp] offset:38912\n\t" \
    "v_mfma_f32_16x16x32_bf16 v[200:203], v[20:23], v[76:79], 0\n\t" \
    "ds_read_b128 v[48:51], %[xp] offset:39936\n\t" \
    "v_mfma_f32_16x16x32_bf16 v[204:207], v[24:27], v[68:71], 0\n\t" \
    "v_mfma_f32_16x16x32_bf16 v[208:211], v[24:27], v[76:79], 0\n\t" \
    "v_mfma_f32_16x16x32_bf16 v[196:199], v[28:31], v[72:75], v[196:199]\n\t" \
    "ds_read_b128 v[20:23], %[xp] offset:40960\n\t" \
    "v_mfma_f32_16x16x32_bf16 v[200:203], v[28:31], v[80:83], v[200:203]\n\t" \
    "ds_read_b128 v[24:27], %[xp] offset:41984\n\t" \
    "v_mfma_f32_16x16x32_bf16 v[204:207], v[32:35], v[72:75], v[204:207]\n\t" \
    "v_mfma_f32_16x16x32_bf16 v[208:211], v[32:35], v[80:83], v[208:211]\n\t" \
    "v_mfma_f32_16x16x32_bf16 v[196:199], v[36:39], v[84:87], v[196:199]\n\t" \
    "ds_read_b128 v[28:31], %[xp] offset:43008\n\t" \
    "v_mfma_f32_16x16x32_bf16 v[200:203], v[36:39], v[92:95], v[200:203]\n\t" \
    "ds_read_b128 v[32:35], %[xp] offset:44032\n\t" \
    "v_mfma_f32_16x16x32_bf16 v[204:207], v[40:43], v[84:87], v[204:207]\n\t" \
    "s_waitcnt lgkmcnt(4)\n\t" \
    "v_mfma_f32_16x16x32_bf16 v[208:211], v[40:43], v[92:95], v[208:211]\n\t" \
    "v_mfma_f32_16x16x32_bf16 v[196:199], v[44:47], v[88:91], v[196:199]\n\t" \
    "ds_read_b128 v[36:39], %[xp] offset:45056\n\t" \
    "v_mfma_f32_16x16x32_bf16 v[200:203], v[44:47], v[96:99], v[200:203]\n\t" \
    "ds_read_b128 v[40:43], %[xp] offset:46080\n\t" \
    "v_mfma_f32_16x16x32_bf16 v[204:207], v[48:51], v[88:91], v[204:207]\n\t" \
    "s_waitcnt lgkmcnt(4)\n\t" \
    "v_mfma_f32_16x16x32_bf16 v[208:211], v[48:51], v[96:99], v[208:211]\n\t" \
    "v_mfma_f32_16x16x32_bf16 v[196:199], v[20:23], v[100:103], v[196:199]\n\t" \
    "ds_read_b128 v[44:47], %[xp] offset:47104\n\t" \
    "v_mfma_f32_16x16x32_bf16 v[200:203], v[20:23], v[108:111], v[200:203]\n\t" \
    "ds_read_b128 v[48:51], %[xp] offset:48128\n\t" \
    "v_mfma_f32_16x16x32_bf16 v[204:207], v[24:27], v[100:103], v[204:207]\n\t" \
    "s_waitcnt lgkmcnt(4)\n\t" \
    "v_mfma_f32_16x16x32_bf16 v[208:211], v[24:27], v[108:111], v[208:211]\n\t" \
    "v_mfma_f32_16x16x32_bf16 v[196:199], v[28:31], v[104:107], v[196:199]\n\t" \
    "ds_read_b128 v[20:23], %[xp] offset:49152\n\t" \
    "v_mfma_f32_16x16x32_bf16 v[200:203], v[28:31], v[112:115], v[200:203]\n\t" \
    "ds_read_b128 v[24:27], %[xp] offset:50176\n\t" \
    "v_mfma_f32_16x16x32_bf16 v[204:207], v[32:35], v[104:107], v[204:207]\n\t" \
    "s_waitcnt lgkmcnt(4)\n\t" \
    "v_mfma_f32_16x16x32_bf16 v[208:211], v[32:35], v[112:115], v[208:211]\n\t" \
    "v_mfma_f32_16x16x32_bf16 v[196:199], v[36:39], v[116:119], v[196:199]\n\t" \
    "ds_read_b128 v[28:31], %[xp] offset:51200\n\t" \
    "v_mfma_f32_16x16x32_bf16 v[200:203], v[36:39], v[124:127], v[200:203]\n\t" \
    "ds_read_b128 v[32:35], %[xp] offset:52224\n\t" \
    "v_mfma_f32_16x16x32_bf16 v[204:207], v[40:43], v[116:119], v[204:207]\n\t" \
    "s_waitcnt lgkmcnt(4)\n\t" \
    "v_mfma_f32_16x16x32_bf16 v[208:211], v[40:43], v[124:127], v[208:211]\n\t" \
    "v_mfma_f32_16x16x32_bf16 v[196:199], v[44:47], v[120:123], v[196:199]\n\t" \
    "ds_read_b128 v[36:39], %[xp] offset:53248\n\t" \
    "v_mfma_f32_16x16x32_bf16 v[200:203], v[44:47], v[128:131], v[200:203]\n\t" \
    "ds_read_b128 v[40:43], %[xp] offset:54272\n\t" \
    "v_mfma_f32_16x16x32_bf16 v[204:207], v[48:51], v[120:123], v[204:207]\n\t" \
    "s_waitcnt lgkmcnt(4)\n\t" \
    "v_mfma_f32_16x16x32_bf16 v[208:211], v[48:51], v[128:131], v[208:211]\n\t" \
    "s_waitcnt vmcnt(4)\n\t" \
    "s_barrier\n\t" \
    "s_setprio 0\n\t" \
    "s_sub_u32 %[a], %[t], 1\n\t" \
    "v_mov_b32 v53, %[tag]\n\t" \
    "v_mov_b32 v52, %[a]\n\t" \
    "s_cmp_gt_u32 %[t], 1\n\t" \
    "s_cselect_b64 exec, %[fm], 0\n\t" \
    "buffer_store_dwordx2 v[52:53], %[l16], %[rs], %[fo] offen sc1\n\t" \
    "s_mov_b64 exec, %[pm]\n\t" \
    "s_mov_b32 m0, %[pd]\n\t" \
    "s_add_u32 %[a], %[sx], %[po]\n\t" \
    "buffer_load_dwordx4 %[l16], %[rs], %[a] offen sc1 lds\n\t" \
    "s_mov_b64 exec, -1\n\t" \
    "s_add_u32 m0, %[sd], 32768\n\t" \
    "s_add_u32 %[a], %[sx], 131072\n\t" \
    "buffer_load_dwordx4 %[l16], %[rs], %[a] offen offset:0 sc1 lds\n\t" \
    "buffer_load_dwordx4 %[l16], %[rs], %[a] offen offset:1024 sc1 lds\n\t" \
    "buffer_load_dwordx4 %[l16], %[rs], %[a] offen offset:2048 sc1 lds\n\t" \
    "buffer_load_dwordx4 %[l16], %[rs], %[a] offen offset:3072 sc1 lds\n\t" \
    "v_mfma_f32_16x16x32_bf16 v[196:199], v[20:23], v[132:135], v[196:199]\n\t" \
    "ds_read_b128 v[44:47], %[xp] offset:55296\n\t" \
    "v_mfma_f32_16x16x32_bf16 v[200:203], v[20:23], v[140:143], v[200:203]\n\t" \
    "ds_read_b128 v[48:51], %[xp] offset:56320\n\t" \
    "v_mfma_f32_16x16x32_bf16 v[204:207], v[24:27], v[132:135], v[204:207]\n\t" \
    "s_waitcnt lgkmcnt(4)\n\t" \
    "v_mfma_f32_16x16x32_bf16 v[208:211], v[24:27], v[140:143], v[208:211]\n\t" \
    "v_mfma_f32_16x16x32_bf16 v[196:199], v[28:31], v[136:139], v[196:199]\n\t" \
    "ds_read_b128 v[20:23], %[xp] offset:57344\n\t" \
    "v_mfma_f32_16x16x32_bf16 v[200:203], v[28:31], v[144:147], v[200:203]\n\t" \
    "ds_read_b128 v[24:27], %[xp] offset:58368\n\t" \
    "v_mfma_f32_16x16x32_bf16 v[204:207], v[32:35], v[136:139], v[204:207]\n\t" \
    "s_waitcnt lgkmcnt(4)\n\t" \
    "v_mfma_f32_16x16x32_bf16 v[208:211], v[32:35], v[144:147], v[208:211]\n\t" \
    "v_mfma_f32_16x16x32_bf16 v[196:199], v[36:39], v[148:151], v[196:199]\n\t" \
    "ds_read_b128 v[28:31], %[xp] offset:59392\n\t" \
    "v_mfma_f32_16x16x32_bf16 v[200:203], v[36:39], v[156:159], v[200:203]\n\t" \
    "ds_read_b128 v[32:35], %[xp] offset:60416\n\t" \
    "v_mfma_f32_16x16x32_bf16 v[204:207], v[40:43], v[148:151], v[204:207]\n\t" \
    "s_waitcnt lgkmcnt(4)\n\t" \
    "v_mfma_f32_16x16x32_bf16 v[208:211], v[40:43], v[156:159], v[208:211]\n\t" \
    "v_mfma_f32_16x16x32_bf16 v[196:199], v[44:47], v[152:155], v[196:199]\n\t" \
    "ds_read_b128 v[36:39], %[xp] offset:61440\n\t" \
    "v_mfma_f32_16x16x32_bf16 v[200:203], v[44:47], v[160:163], v[200:203]\n\t" \
    "ds_read_b128 v[40:43], %[xp] offset:62464\n\t" \
    "v_mfma_f32_16x16x32_bf16 v[204:207], v[48:51], v[152:155], v[204:207]\n\t" \
    "s_waitcnt lgkmcnt(4)\n\t" \
    "v_mfma_f32_16x16x32_bf16 v[208:211], v[48:51], v[160:163], v[208:211]\n\t" \
    "v_mfma_f32_16x16x32_bf16 v[196:199], v[20:23], v[164:167], v[196:199]\n\t" \
    "ds_read_b128 v[44:47], %[xp] offset:63488\n\t" \
    "v_mfma_f32_16x16x32_bf16 v[200:203], v[20:23], v[172:175], v[200:203]\n\t" \
    "ds_read_b128 v[48:51], %[xp] offset:64512\n\t" \
    "v_mfma_f32_16x16x32_bf16 v[204:207], v[24:27], v[164:167], v[204:207]\n\t" \
    "s_waitcnt lgkmcnt(4)\n\t" \
    "v_mfma_f32_16x16x32_bf16 v[208:211], v[24:27], v[172:175], v[208:211]\n\t" \
    "v_mfma_f32_16x16x32_bf16 v[196:199], v[28:31], v[168:171], v[196:199]\n\t" \
    "ds_read_b128 v[20:23], %[xp] offset:0\n\t" \
    "v_mfma_f32_16x16x32_bf16 v[200:203], v[28:31], v[176:179], v[200:203]\n\t" \
    "ds_read_b128 v[24:27], %[xp] offset:1024\n\t" \
    "v_mfma_f32_16x16x32_bf16 v[204:207], v[32:35], v[168:171], v[204:207]\n\t" \
    "s_waitcnt lgkmcnt(4)\n\t" \
    "v_mfma_f32_16x16x32_bf16 v[208:211], v[32:35], v[176:179], v[208:211]\n\t" \
    "v_mfma_f32_16x16x32_bf16 v[196:199], v[36:39], v[180:183], v[196:199]\n\t" \
    "ds_read_b128 v[28:31], %[xp] offset:2048\n\t" \
    "v_mfma_f32_16x16x32_bf16 v[200:203], v[36:39], v[188:191], v[200:203]\n\t" \
    "ds_read_b128 v[32:35], %[xp] offset:3072\n\t" \
    "v_mfma_f32_16x16x32_bf16 v[204:207], v[40:43], v[180:183], v[204:207]\n\t" \
    "s_waitcnt lgkmcnt(4)\n\t" \
    "v_mfma_f32_16x16x32_bf16 v[208:211], v[40:43], v[188:191], v[208:211]\n\t" \
    "v_mfma_f32_16x16x32_bf16 v[196:199], v[44:47], v[184:187], v[196:199]\n\t" \
    "ds_read_b128 v[36:39], %[xp] offset:4096\n\t" \
    "v_mfma_f32_16x16x32_bf16 v[200:203], v[44:47], v[192:195], v[200:203]\n\t" \
    "ds_read_b128 v[40:43], %[xp] offset:5120\n\t" \
    "v_mfma_f32_16x16x32_bf16 v[204:207], v[48:51], v[184:187], v[204:207]\n\t" \
    "s_waitcnt lgkmcnt(4)\n\t" \
    "v_mfma_f32_16x16x32_bf16 v[208:211], v[48:51], v[192:195], v[208:211]\n\t" \
    "s_add_u32 %[sx], %[sx], 65536\n\t" \
    "s_add_u32 %[t], %[t], 1\n\t" \
    "s_waitcnt vmcnt(4)\n\t" \
    "s_barrier\n\t" \
    "s_cmp_lt_u32 %[t], %[te]\n\t" \
    "s_cbranch_scc1 L0_%=\n\t" \
    "s_cmp_eq_u32 %[fin], %[te]\n\t" \
    "s_cbranch_scc0 E_%=\n\t" \
    "s_nop 4\n\t" \
    "ds_write_b128 %[pa], v[196:199] offset:0\n\t" \
    "ds_write_b128 %[pa], v[200:203] offset:1024\n\t" \
    "ds_write_b128 %[pa], v[204:207] offset:2048\n\t" \
    "ds_write_b128 %[pa], v[208:211] offset:3072\n\t" \
    "s_waitcnt lgkmcnt(0)\n\t" \
    "s_barrier\n\t" \
    "s_branch E_%=\n\t" \
    "L1_%=:\n\t" \
    "s_setprio 1\n\t" \
    "s_add_u32 m0, %[sd], 49152\n\t" \
    "s_add_u32 %[a], %[sx], 81920\n\t" \
    "v_mfma_f32_16x16x32_bf16 v[0:3], v[20:23], v[68:71], 0\n\t" \
    "ds_read_b128 v[44:47], %[xp] offset:6144\n\t" \
    "v_mfma_f32_16x16x32_bf16 v[4:7], v[20:23], v[76:79], 0\n\t" \
    "ds_read_b128 v[48:51], %[xp] offset:7168\n\t" \
    "v_mfma_f32_16x16x32_bf16 v[8:11], v[24:27], v[68:71], 0\n\t" \
    "s_waitcnt lgkmcnt(4)\n\t" \
    "v_mfma_f32_16x16x32_bf16 v[12:15], v[24:27], v[76:79], 0\n\t" \
    "v_mfma_f32_16x16x32_bf16 v[0:3], v[28:31], v[72:75], v[0:3]\n\t" \
    "ds_read_b128 v[20:23], %[xp] offset:8192\n\t" \
    "v_mfma_f32_16x16x32_bf16 v[4:7], v[28:31], v[80:83], v[4:7]\n\t" \
    "ds_read_b128 v[24:27], %[xp] offset:9216\n\t" \
    "v_mfma_f32_16x16x32_bf16 v[8:11], v[32:35], v[72:75], v[8:11]\n\t" \
    "s_waitcnt lgkmcnt(4)\n\t" \
    "v_mfma_f32_16x16x32_bf16 v[12:15], v[32:35], v[80:83], v[12:15]\n\t" \
    "v_mfma_f32_16x16x32_bf16 v[0:3], v[36:39], v[84:87], v[0:3]\n\t" \
    "ds_read_b128 v[28:31], %[xp] offset:10240\n\t" \
    "v_mfma_f32_16x16x32_bf16 v[4:7], v[36:39], v[92:95], v[4:7]\n\t" \
    "ds_read_b128 v[32:35], %[xp] offset:11264\n\t" \
    "v_mfma_f32_16x16x32_bf16 v[8:11], v[40:43], v[84:87], v[8:11]\n\t" \
    "s_waitcnt lgkmcnt(4)\n\t" \
    "v_mfma_f32_16x16x32_bf16 v[12:15], v[40:43], v[92:95], v[12:15]\n\t" \
    "v_mfma_f32_16x16x32_bf16 v[0:3], v[44:47], v[88:91], v[0:3]\n\t" \
    "ds_read_b128 v[36:39], %[xp] offset:12288\n\t" \
    "v_mfma_f32_16x16x32_bf16 v[4:7], v[44:47], v[96:99], v[4:7]\n\t" \
    "ds_read_b128 v[40:43], %[xp] offset:13312\n\t" \
    "v_mfma_f32_16x16x32_bf16 v[8:11], v[48:51], v[88:91], v[8:11]\n\t" \
    "s_waitcnt lgkmcnt(4)\n\t" \
    "v_mfma_f32_16x16x32_bf16 v[12:15], v[48:51], v[96:99], v[12:15]\n\t" \
    "v_mfma_f32_16x16x32_bf16 v[0:3], v[20:23], v[100:103], v[0:3]\n\t" \
    "ds_read_b128 v[44:47], %[xp] offset:14336\n\t" \
    "v_mfma_f32_16x16x32_bf16 v[4:7], v[20:23], v[108:111], v[4:7]\n\t" \
    "ds_read_b128 v[48:51], %[xp] offset:15360\n\t" \
    "v_mfma_f32_16x16x32_bf16 v[8:11], v[24:27], v[100:103], v[8:11]\n\t" \
    "s_waitcnt lgkmcnt(4)\n\t" \
    "v_mfma_f32_16x16x32_bf16 v[12:15], v[24:27], v[108:111], v[12:15]\n\t" \
    "buffer_load_dwordx4 %[l16], %[rs], %[a] offen offset:0 sc1 lds\n\t" \
    "v_mfma_f32_16x16x32_bf16 v[0:3], v[28:31], v[104:107], v[0:3]\n\t" \
    "ds_read_b128 v[20:23], %[xp] offset:16384\n\t" \
    "v_mfma_f32_16x16x32_bf16 v[4:7], v[28:31], v[112:115], v[4:7]\n\t" \
    "ds_read_b128 v[24:27], %[xp] offset:17408\n\t" \
    "v_mfma_f32_16x16x32_bf16 v[8:11], v[32:35], v[104:107], v[8:11]\n\t" \
    "s_waitcnt lgkmcnt(4)\n\t" \
    "v_mfma_f32_16x16x32_bf16 v[12:15], v[32:35], v[112:115], v[12:15]\n\t" \
    "buffer_load_dwordx4 %[l16], %[rs], %[a] offen offset:1024 sc1 lds\n\t" \
    "v_mfma_f32_16x16x32_bf16 v[0:3], v[36:39], v[116:119], v[0:3]\n\t" \
    "ds_read_b128 v[28:31], %[xp] offset:18432\n\t" \
    "v_mfma_f32_16x16x32_bf16 v[4:7], v[36:39], v[124:127], v[4:7]\n\t" \
    "ds_read_b128 v[32:35], %[xp] offset:19456\n\t" \
    "v_mfma_f32_16x16x32_bf16 v[8:11], v[40:43], v[116:119], v[8:11]\n\t" \
    "s_waitcnt lgkmcnt(4)\n\t" \
    "v_mfma_f32_16x16x32_bf16 v[12:15], v[40:43], v[124:127], v[12:15]\n\t" \
    "buffer_load_dwordx4 %[l16], %[rs], %[a] offen offset:2048 sc1 lds\n\t" \
    "v_mfma_f32_16x16x32_bf16 v[0:3], v[44:47], v[120:123], v[0:3]\n\t" \
    "ds_read_b128 v[36:39], %[xp] offset:20480\n\t" \
    "v_mfma_f32_16x16x32_bf16 v[4:7], v[44:47], v[128:131], v[4:7]\n\t" \
    "ds_read_b128 v[40:43], %[xp] offset:21504\n\t" \
    "v_mfma_f32_16x16x32_bf16 v[8:11], v[48:51], v[120:123], v[8:11]\n\t" \
    "s_waitcnt lgkmcnt(4)\n\t" \
    "v_mfma_f32_16x16x32_bf16 v[12:15], v[48:51], v[128:131], v[12:15]\n\t" \
    "buffer_load_dwordx4 %[l16], %[rs], %[a] offen offset:3072 sc1 lds\n\t" \
    "s_waitcnt vmcnt(4)\n\t" \
    "s_barrier\n\t" \
    "s_setprio 1\n\t" \
    "s_add_u32 m0, %[sd], 0\n\t" \
    "s_add_u32 %[a], %[sx], 131072\n\t" \
    "s_add_u32 %[b], %[oo], 0x1000\n\t" \
    "s_cmp_lg_u32 %[t], 1\n\t" \
    "s_cselect_b64 %[em], -1, 0\n\t" \
    "v_mfma_f32_16x16x32_bf16 v[0:3], v[20:23], v[132:135], v[0:3]\n\t" \
    "ds_read_b128 v[44:47], %[xp] offset:22528\n\t" \
    "v_mfma_f32_16x16x32_bf16 v[4:7], v[20:23], v[140:143], v[4:7]\n\t" \
    "ds_read_b128 v[48:51], %[xp] offset:23552\n\t" \
    "v_mfma_f32_16x16x32_bf16 v[8:11], v[24:27], v[132:135], v[8:11]\n\t" \
    "ds_read_b128 v[52:55], %[pa] offset:0\n\t" \
    "s_waitcnt lgkmcnt(5)\n\t" \
    "v_mfma_f32_16x16x32_bf16 v[12:15], v[24:27], v[140:143], v[12:15]\n\t" \
    "buffer_load_dwordx4 %[l16], %[rs], %[a] offen offset:0 sc1 lds\n\t" \
    "v_mfma_f32_16x16x32_bf16 v[0:3], v[28:31], v[136:139], v[0:3]\n\t" \
    "ds_read_b128 v[20:23], %[xp] offset:24576\n\t" \
    "v_mfma_f32_16x16x32_bf16 v[4:7], v[28:31], v[144:147], v[4:7]\n\t" \
    "ds_read_b128 v[24:27], %[xp] offset:25600\n\t" \
    "v_mfma_f32_16x16x32_bf16 v[8:11], v[32:35], v[136:139], v[8:11]\n\t" \
    "ds_read_b128 v[56:59], %[pa] offset:1024\n\t" \
    "s_waitcnt lgkmcnt(6)\n\t" \
    "v_mfma_f32_16x16x32_bf16 v[12:15], v[32:35], v[144:147], v[12:15]\n\t" \
    "buffer_load_dwordx4 %[l16], %[rs], %[a] offen offset:1024 sc1 lds\n\t" \
    "v_mfma_f32_16x16x32_bf16 v[0:3], v[36:39], v[148:151], v[0:3]\n\t" \
    "ds_read_b128 v[28:31], %[xp] offset:26624\n\t" \
    "v_mfma_f32_16x16x32_bf16 v[4:7], v[36:39], v[156:159], v[4:7]\n\t" \
    "ds_read_b128 v[32:35], %[xp] offset:27648\n\t" \
    "v_mfma_f32_16x16x32_bf16 v[8:11], v[40:43], v[148:151], v[8:11]\n\t" \
    "ds_read_b128 v[60:63], %[pa] offset:2048\n\t" \
    "s_waitcnt lgkmcnt(7)\n\t" \
    "v_mfma_f32_16x16x32_bf16 v[12:15], v[40:43], v[156:159], v[12:15]\n\t" \
    "buffer_load_dwordx4 %[l16], %[rs], %[a] offen offset:2048 sc1 lds\n\t" \
    "v_mfma_f32_16x16x32_bf16 v[0:3], v[44:47], v[152:155], v[0:3]\n\t" \
    "ds_read_b128 v[36:39], %[xp] offset:28672\n\t" \
    "s_waitcnt lgkmcnt(7)\n\t" \
    "v_add_f32 v196, v196, v52\n\t" \
    "v_mfma_f32_16x16x32_bf16 v[4:7], v[44:47], v[160:163], v[4:7]\n\t" \
    "ds_read_b128 v[40:43], %[xp] offset:29696\n\t" \
    "v_add_f32 v197, v197, v53\n\t" \
    "v_mfma_f32_16x16x32_bf16 v[8:11], v[48:51], v[152:155], v[8:11]\n\t" \
    "ds_read_b128 v[64:67], %[pa] offset:3072\n\t" \
    "s_waitcnt lgkmcnt(7)\n\t" \
    "v_mfma_f32_16x16x32_bf16 v[12:15], v[48:51], v[160:163], v[12:15]\n\t" \
    "buffer_load_dwordx4 %[l16], %[rs], %[a] offen offset:3072 sc1 lds\n\t" \
    "v_add_f32 v198, v198, v54\n\t" \
    "v_add_f32 v199, v199, v55\n\t" \
    "v_mfma_f32_16x16x32_bf16 v[0:3], v[20:23], v[164:167], v[0:3]\n\t" \
    "ds_read_b128 v[44:47], %[xp] offset:30720\n\t" \
    "s_waitcnt lgkmcnt(7)\n\t" \
    "v_add_f32 v200, v200, v56\n\t" \
    "v_mfma_f32_16x16x32_bf16 v[4:7], v[20:23], v[172:175], v[4:7]\n\t" \
    "ds_read_b128 v[48:51], %[xp] offset:31744\n\t" \
    "v_add_f32 v201, v201, v57\n\t" \
    "v_mfma_f32_16x16x32_bf16 v[8:11], v[24:27], v[164:167], v[8:11]\n\t" \
    "v_add_f32 v202, v202, v58\n\t" \
    "s_waitcnt lgkmcnt(6)\n\t" \
    "v_mfma_f32_16x16x32_bf16 v[12:15], v[24:27], v[172:175], v[12:15]\n\t" \
    "v_add_f32 v203, v203, v59\n\t" \
    "s_waitcnt lgkmcnt(5)\n\t" \
    "v_add_f32 v204, v204, v60\n\t" \
    "v_add_f32 v205, v205, v61\n\t" \
    "v_mfma_f32_16x16x32_bf16 v[0:3], v[28:31], v[168:171], v[0:3]\n\t" \
    "ds_read_b128 v[20:23], %[xp] offset:32768\n\t" \
    "v_add_f32 v206, v206, v62\n\t" \
    "v_mfma_f32_16x16x32_bf16 v[4:7], v[28:31], v[176:179], v[4:7]\n\t" \
    "ds_read_b128 v[24:27], %[xp] offset:33792\n\t" \
    "v_add_f32 v207, v207, v63\n\t" \
    "v_mfma_f32_16x16x32_bf16 v[8:11], v[32:35], v[168:171], v[8:11]\n\t" \
    "s_waitcnt lgkmcnt(4)\n\t" \
    "v_add_f32 v208, v208, v64\n\t" \
    "v_mfma_f32_16x16x32_bf16 v[12:15], v[32:35], v[176:179], v[12:15]\n\t" \
    "v_add_f32 v209, v209, v65\n\t" \
    "v_add_f32 v210, v210, v66\n\t" \
    "v_add_f32 v211, v211, v67\n\t" \
    "v_mfma_f32_16x16x32_bf16 v[0:3], v[36:39], v[180:183], v[0:3]\n\t" \
    "ds_read_b128 v[28:31], %[xp] offset:34816\n\t" \
    "v_cvt_pk_bf16_f32 v52, v196, v197\n\t" \
    "v_mfma_f32_16x16x32_bf16 v[4:7], v[36:39], v[188:191], v[4:7]\n\t" \
    "ds_read_b128 v[32:35], %[xp] offset:35840\n\t" \
    "v_cvt_pk_bf16_f32 v53, v198, v199\n\t" \
    "v_mfma_f32_16x16x32_bf16 v[8:11], v[40:43], v[180:183], v[8:11]\n\t" \
    "v_cvt_pk_bf16_f32 v54, v204, v205\n\t" \
    "s_waitcnt lgkmcnt(4)\n\t" \
    "v_mfma_f32_16x16x32_bf16 v[12:15], v[40:43], v[188:191], v[12:15]\n\t" \
    "v_cvt_pk_bf16_f32 v55, v206, v207\n\t" \
    "v_cvt_pk_bf16_f32 v56, v200, v201\n\t" \
    "v_cvt_pk_bf16_f32 v57, v202, v203\n\t" \
    "v_mfma_f32_16x16x32_bf16 v[0:3], v[44:47], v[184:187], v[0:3]\n\t" \
    "ds_read_b128 v[36:39], %[xp] offset:36864\n\t" \
    "v_cvt_pk_bf16_f32 v58, v208, v209\n\t" \
    "v_mfma_f32_16x16x32_bf16 v[4:7], v[44:47], v[192:195], v[4:7]\n\t" \
    "ds_read_b128 v[40:43], %[xp] offset:37888\n\t" \
    "v_cvt_pk_bf16_f32 v59, v210, v211\n\t" \
    "v_mfma_f32_16x16x32_bf16 v[8:11], v[48:51], v[184:187], v[8:11]\n\t" \
    "s_waitcnt lgkmcnt(4)\n\t" \
    "v_mfma_f32_16x16x32_bf16 v[12:15], v[48:51], v[192:195], v[12:15]\n\t" \
    "s_mov_b64 exec, %[em]\n\t" \
    "buffer_store_dwordx4 v[52:55], %[l16], %[rs], %[oo] offen sc1\n\t" \
    "buffer_store_dwordx4 v[56:59], %[l16], %[rs], %[oo] offen offset:1024 sc1\n\t" \
    "s_mov_b64 exec, -1\n\t" \
    "s_add_u32 %[sx], %[sx], 65536\n\t" \
    "s_add_u32 %[t], %[t], 1\n\t" \
    "s_add_u32 %[oo], %[oo], 262144\n\t" \
    "s_waitcnt vmcnt(6)\n\t" \
    "s_cmp_gt_u32 %[t], " STR(SQUICK) " + 1\n\t" \
    "s_cbranch_scc1 B0_%=\n\t" \
    "s_waitcnt vmcnt(0)\n\t" \
    "B0_%=:\n\t" \
    "s_barrier\n\t" \
    "s_setprio 1\n\t" \
    "s_add_u32 m0, %[sd], 16384\n\t" \
    "s_add_u32 %[a], %[sx], 81920\n\t" \
    "v_mfma_f32_16x16x32_bf16 v[196:199], v[20:23], v[68:71], 0\n\t" \
    "ds_read_b128 v[44:47], %[xp] offset:38912\n\t" \
    "v_mfma_f32_16x16x32_bf16 v[200:203], v[20:23], v[76:79], 0\n\t" \
    "ds_read_b128 v[48:51], %[xp] offset:39936\n\t" \
    "v_mfma_f32_16x16x32_bf16 v[204:207], v[24:27], v[68:71], 0\n\t" \
    "s_waitcnt lgkmcnt(4)\n\t" \
    "v_mfma_f32_16x16x32_bf16 v[208:211], v[24:27], v[76:79], 0\n\t" \
    "v_mfma_f32_16x16x32_bf16 v[196:199], v[28:31], v[72:75], v[196:199]\n\t" \
    "ds_read_b128 v[20:23], %[xp] offset:40960\n\t" \
    "v_mfma_f32_16x16x32_bf16 v[200:203], v[28:31], v[80:83], v[200:203]\n\t" \
    "ds_read_b128 v[24:27], %[xp] offset:41984\n\t" \
    "v_mfma_f32_16x16x32_bf16 v[204:207], v[32:35], v[72:75], v[204:207]\n\t" \
    "s_waitcnt lgkmcnt(4)\n\t" \
    "v_mfma_f32_16x16x32_bf16 v[208:211], v[32:35], v[80:83], v[208:211]\n\t" \
    "v_mfma_f32_16x16x32_bf16 v[196:199], v[36:39], v[84:87], v[196:199]\n\t" \
    "ds_read_b128 v[28:31], %[xp] offset:43008\n\t" \
    "v_mfma_f32_16x16x32_bf16 v[200:203], v[36:39], v[92:95], v[200:203]\n\t" \
    "ds_read_b128 v[32:35], %[xp] offset:44032\n\t" \
    "v_mfma_f32_16x16x32_bf16 v[204:207], v[40:43], v[84:87], v[204:207]\n\t" \
    "s_waitcnt lgkmcnt(4)\n\t" \
    "v_mfma_f32_16x16x32_bf16 v[208:211], v[40:43], v[92:95], v[208:211]\n\t" \
    "v_mfma_f32_16x16x32_bf16 v[196:199], v[44:47], v[88:91], v[196:199]\n\t" \
    "ds_read_b128 v[36:39], %[xp] offset:45056\n\t" \
    "v_mfma_f32_16x16x32_bf16 v[200:203], v[44:47], v[96:99], v[200:203]\n\t" \
    "ds_read_b128 v[40:43], %[xp] offset:46080\n\t" \
    "v_mfma_f32_16x16x32_bf16 v[204:207], v[48:51], v[88:91], v[204:207]\n\t" \
    "s_waitcnt lgkmcnt(4)\n\t" \
    "v_mfma_f32_16x16x32_bf16 v[208:211], v[48:51], v[96:99], v[208:211]\n\t" \
    "v_mfma_f32_16x16x32_bf16 v[196:199], v[20:23], v[100:103], v[196:199]\n\t" \
    "ds_read_b128 v[44:47], %[xp] offset:47104\n\t" \
    "v_mfma_f32_16x16x32_bf16 v[200:203], v[20:23], v[108:111], v[200:203]\n\t" \
    "ds_read_b128 v[48:51], %[xp] offset:48128\n\t" \
    "v_mfma_f32_16x16x32_bf16 v[204:207], v[24:27], v[100:103], v[204:207]\n\t" \
    "s_waitcnt lgkmcnt(4)\n\t" \
    "v_mfma_f32_16x16x32_bf16 v[208:211], v[24:27], v[108:111], v[208:211]\n\t" \
    "buffer_load_dwordx4 %[l16], %[rs], %[a] offen offset:0 sc1 lds\n\t" \
    "v_mfma_f32_16x16x32_bf16 v[196:199], v[28:31], v[104:107], v[196:199]\n\t" \
    "ds_read_b128 v[20:23], %[xp] offset:49152\n\t" \
    "v_mfma_f32_16x16x32_bf16 v[200:203], v[28:31], v[112:115], v[200:203]\n\t" \
    "ds_read_b128 v[24:27], %[xp] offset:50176\n\t" \
    "v_mfma_f32_16x16x32_bf16 v[204:207], v[32:35], v[104:107], v[204:207]\n\t" \
    "s_waitcnt lgkmcnt(4)\n\t" \
    "v_mfma_f32_16x16x32_bf16 v[208:211], v[32:35], v[112:115], v[208:211]\n\t" \
    "buffer_load_dwordx4 %[l16], %[rs], %[a] offen offset:1024 sc1 lds\n\t" \
    "v_mfma_f32_16x16x32_bf16 v[196:199], v[36:39], v[116:119], v[196:199]\n\t" \
    "ds_read_b128 v[28:31], %[xp] offset:51200\n\t" \
    "v_mfma_f32_16x16x32_bf16 v[200:203], v[36:39], v[124:127], v[200:203]\n\t" \
    "ds_read_b128 v[32:35], %[xp] offset:52224\n\t" \
    "v_mfma_f32_16x16x32_bf16 v[204:207], v[40:43], v[116:119], v[204:207]\n\t" \
    "s_waitcnt lgkmcnt(4)\n\t" \
    "v_mfma_f32_16x16x32_bf16 v[208:211], v[40:43], v[124:127], v[208:211]\n\t" \
    "buffer_load_dwordx4 %[l16], %[rs], %[a] offen offset:2048 sc1 lds\n\t" \
    "v_mfma_f32_16x16x32_bf16 v[196:199], v[44:47], v[120:123], v[196:199]\n\t" \
    "ds_read_b128 v[36:39], %[xp] offset:53248\n\t" \
    "v_mfma_f32_16x16x32_bf16 v[200:203], v[44:47], v[128:131], v[200:203]\n\t" \
    "ds_read_b128 v[40:43], %[xp] offset:54272\n\t" \
    "v_mfma_f32_16x16x32_bf16 v[204:207], v[48:51], v[120:123], v[204:207]\n\t" \
    "s_waitcnt lgkmcnt(4)\n\t" \
    "v_mfma_f32_16x16x32_bf16 v[208:211], v[48:51], v[128:131], v[208:211]\n\t" \
    "buffer_load_dwordx4 %[l16], %[rs], %[a] offen offset:3072 sc1 lds\n\t" \
    "s_waitcnt vmcnt(4)\n\t" \
    "s_barrier\n\t" \
    "s_setprio 1\n\t" \
    "s_add_u32 m0, %[sd], 32768\n\t" \
    "s_add_u32 %[a], %[sx], 131072\n\t" \
    "s_add_u32 %[b], %[oo], 0x1000\n\t" \
    "s_cmp_lg_u32 %[t], 1\n\t" \
    "s_cselect_b64 %[em], -1, 0\n\t" \
    "v_mfma_f32_16x16x32_bf16 v[196:199], v[20:23], v[132:135], v[196:199]\n\t" \
    "ds_read_b128 v[44:47], %[xp] offset:55296\n\t" \
    "v_mfma_f32_16x16x32_bf16 v[200:203], v[20:23], v[140:143], v[200:203]\n\t" \
    "ds_read_b128 v[48:51], %[xp] offset:56320\n\t" \
    "v_mfma_f32_16x16x32_bf16 v[204:207], v[24:27], v[132:135], v[204:207]\n\t" \
    "ds_read_b128 v[52:55], %[pa] offset:0\n\t" \
    "s_waitcnt lgkmcnt(5)\n\t" \
    "v_mfma_f32_16x16x32_bf16 v[208:211], v[24:27], v[140:143], v[208:211]\n\t" \
    "buffer_load_dwordx4 %[l16], %[rs], %[a] offen offset:0 sc1 lds\n\t" \
    "v_mfma_f32_16x16x32_bf16 v[196:199], v[28:31], v[136:139], v[196:199]\n\t" \
    "ds_read_b128 v[20:23], %[xp] offset:57344\n\t" \
    "v_mfma_f32_16x16x32_bf16 v[200:203], v[28:31], v[144:147], v[200:203]\n\t" \
    "ds_read_b128 v[24:27], %[xp] offset:58368\n\t" \
    "v_mfma_f32_16x16x32_bf16 v[204:207], v[32:35], v[136:139], v[204:207]\n\t" \
    "ds_read_b128 v[56:59], %[pa] offset:1024\n\t" \
    "s_waitcnt lgkmcnt(6)\n\t" \
    "v_mfma_f32_16x16x32_bf16 v[208:211], v[32:35], v[144:147], v[208:211]\n\t" \
    "buffer_load_dwordx4 %[l16], %[rs], %[a] offen offset:1024 sc1 lds\n\t" \
    "v_mfma_f32_16x16x32_bf16 v[196:199], v[36:39], v[148:151], v[196:199]\n\t" \
    "ds_read_b128 v[28:31], %[xp] offset:59392\n\t" \
    "v_mfma_f32_16x16x32_bf16 v[200:203], v[36:39], v[156:159], v[200:203]\n\t" \
    "ds_read_b128 v[32:35], %[xp] offset:60416\n\t" \
    "v_mfma_f32_16x16x32_bf16 v[204:207], v[40:43], v[148:151], v[204:207]\n\t" \
    "ds_read_b128 v[60:63], %[pa] offset:2048\n\t" \
    "s_waitcnt lgkmcnt(7)\n\t" \
    "v_mfma_f32_16x16x32_bf16 v[208:211], v[40:43], v[156:159], v[208:211]\n\t" \
    "buffer_load_dwordx4 %[l16], %[rs], %[a] offen offset:2048 sc1 lds\n\t" \
    "v_mfma_f32_16x16x32_bf16 v[196:199], v[44:47], v[152:155], v[196:199]\n\t" \
    "ds_read_b128 v[36:39], %[xp] offset:61440\n\t" \
    "s_waitcnt lgkmcnt(7)\n\t" \
    "v_add_f32 v0, v0, v52\n\t" \
    "v_mfma_f32_16x16x32_bf16 v[200:203], v[44:47], v[160:163], v[200:203]\n\t" \
    "ds_read_b128 v[40:43], %[xp] offset:62464\n\t" \
    "v_add_f32 v1, v1, v53\n\t" \
    "v_mfma_f32_16x16x32_bf16 v[204:207], v[48:51], v[152:155], v[204:207]\n\t" \
    "ds_read_b128 v[64:67], %[pa] offset:3072\n\t" \
    "s_waitcnt lgkmcnt(7)\n\t" \
    "v_mfma_f32_16x16x32_bf16 v[208:211], v[48:51], v[160:163], v[208:211]\n\t" \
    "buffer_load_dwordx4 %[l16], %[rs], %[a] offen offset:3072 sc1 lds\n\t" \
    "v_add_f32 v2, v2, v54\n\t" \
    "v_add_f32 v3, v3, v55\n\t" \
    "v_mfma_f32_16x16x32_bf16 v[196:199], v[20:23], v[164:167], v[196:199]\n\t" \
    "ds_read_b128 v[44:47], %[xp] offset:63488\n\t" \
    "s_waitcnt lgkmcnt(7)\n\t" \
    "v_add_f32 v4, v4, v56\n\t" \
    "v_mfma_f32_16x16x32_bf16 v[200:203], v[20:23], v[172:175], v[200:203]\n\t" \
    "ds_read_b128 v[48:51], %[xp] offset:64512\n\t" \
    "v_add_f32 v5, v5, v57\n\t" \
    "v_mfma_f32_16x16x32_bf16 v[204:207], v[24:27], v[164:167], v[204:207]\n\t" \
    "v_add_f32 v6, v6, v58\n\t" \
    "s_waitcnt lgkmcnt(6)\n\t" \
    "v_mfma_f32_16x16x32_bf16 v[208:211], v[24:27], v[172:175], v[208:211]\n\t" \
    "v_add_f32 v7, v7, v59\n\t" \
    "s_waitcnt lgkmcnt(5)\n\t" \
    "v_add_f32 v8, v8, v60\n\t" \
    "v_add_f32 v9, v9, v61\n\t" \
    "v_mfma_f32_16x16x32_bf16 v[196:199], v[28:31], v[168:171], v[196:199]\n\t" \
    "ds_read_b128 v[20:23], %[xp] offset:0\n\t" \
    "v_add_f32 v10, v10, v62\n\t" \
    "v_mfma_f32_16x16x32_bf16 v[200:203], v[28:31], v[176:179], v[200:203]\n\t" \
    "ds_read_b128 v[24:27], %[xp] offset:1024\n\t" \
    "v_add_f32 v11, v11, v63\n\t" \
    "v_mfma_f32_16x16x32_bf16 v[204:207], v[32:35], v[168:171], v[204:207]\n\t" \
    "s_waitcnt lgkmcnt(4)\n\t" \
    "v_add_f32 v12, v12, v64\n\t" \
    "v_mfma_f32_16x16x32_bf16 v[208:211], v[32:35], v[176:179], v[208:211]\n\t" \
    "v_add_f32 v13, v13, v65\n\t" \
    "v_add_f32 v14, v14, v66\n\t" \
    "v_add_f32 v15, v15, v67\n\t" \
    "v_mfma_f32_16x16x32_bf16 v[196:199], v[36:39], v[180:183], v[196:199]\n\t" \
    "ds_read_b128 v[28:31], %[xp] offset:2048\n\t" \
    "v_cvt_pk_bf16_f32 v52, v0, v1\n\t" \
    "v_mfma_f32_16x16x32_bf16 v[200:203], v[36:39], v[188:191], v[200:203]\n\t" \
    "ds_read_b128 v[32:35], %[xp] offset:3072\n\t" \
    "v_cvt_pk_bf16_f32 v53, v2, v3\n\t" \
    "v_mfma_f32_16x16x32_bf16 v[204:207], v[40:43], v[180:183], v[204:207]\n\t" \
    "v_cvt_pk_bf16_f32 v54, v8, v9\n\t" \
    "s_waitcnt lgkmcnt(4)\n\t" \
    "v_mfma_f32_16x16x32_bf16 v[208:211], v[40:43], v[188:191], v[208:211]\n\t" \
    "v_cvt_pk_bf16_f32 v55, v10, v11\n\t" \
    "v_cvt_pk_bf16_f32 v56, v4, v5\n\t" \
    "v_cvt_pk_bf16_f32 v57, v6, v7\n\t" \
    "v_mfma_f32_16x16x32_bf16 v[196:199], v[44:47], v[184:187], v[196:199]\n\t" \
    "ds_read_b128 v[36:39], %[xp] offset:4096\n\t" \
    "v_cvt_pk_bf16_f32 v58, v12, v13\n\t" \
    "v_mfma_f32_16x16x32_bf16 v[200:203], v[44:47], v[192:195], v[200:203]\n\t" \
    "ds_read_b128 v[40:43], %[xp] offset:5120\n\t" \
    "v_cvt_pk_bf16_f32 v59, v14, v15\n\t" \
    "v_mfma_f32_16x16x32_bf16 v[204:207], v[48:51], v[184:187], v[204:207]\n\t" \
    "s_waitcnt lgkmcnt(4)\n\t" \
    "v_mfma_f32_16x16x32_bf16 v[208:211], v[48:51], v[192:195], v[208:211]\n\t" \
    "s_mov_b64 exec, %[em]\n\t" \
    "buffer_store_dwordx4 v[52:55], %[l16], %[rs], %[oo] offen sc1\n\t" \
    "buffer_store_dwordx4 v[56:59], %[l16], %[rs], %[oo] offen offset:1024 sc1\n\t" \
    "s_mov_b64 exec, -1\n\t" \
    "s_add_u32 %[sx], %[sx], 65536\n\t" \
    "s_add_u32 %[t], %[t], 1\n\t" \
    "s_add_u32 %[oo], %[oo], 262144\n\t" \
    "s_waitcnt vmcnt(6)\n\t" \
    "s_cmp_gt_u32 %[t], " STR(SQUICK) " + 1\n\t" \
    "s_cbranch_scc1 B1_%=\n\t" \
    "s_waitcnt vmcnt(0)\n\t" \
    "B1_%=:\n\t" \
    "s_barrier\n\t" \
    "s_cmp_lt_u32 %[t], %[te]\n\t" \
    "s_cbranch_scc1 L1_%=\n\t" \
    "s_cmp_eq_u32 %[fin], %[te]\n\t" \
    "s_cbranch_scc0 E_%=\n\t" \
    "s_nop 4\n\t" \
    "s_barrier\n\t" \
    "ds_read_b128 v[52:55], %[pa] offset:0\n\t" \
    "ds_read_b128 v[56:59], %[pa] offset:1024\n\t" \
    "ds_read_b128 v[60:63], %[pa] offset:2048\n\t" \
    "ds_read_b128 v[64:67], %[pa] offset:3072\n\t" \
    "s_add_u32 %[b], %[oo], 0x1000\n\t" \
    "s_waitcnt lgkmcnt(0)\n\t" \
    "v_add_f32 v196, v196, v52\n\t" \
    "v_add_f32 v197, v197, v53\n\t" \
    "v_add_f32 v198, v198, v54\n\t" \
    "v_add_f32 v199, v199, v55\n\t" \
    "v_add_f32 v200, v200, v56\n\t" \
    "v_add_f32 v201, v201, v57\n\t" \
    "v_add_f32 v202, v202, v58\n\t" \
    "v_add_f32 v203, v203, v59\n\t" \
    "v_add_f32 v204, v204, v60\n\t" \
    "v_add_f32 v205, v205, v61\n\t" \
    "v_add_f32 v206, v206, v62\n\t" \
    "v_add_f32 v207, v207, v63\n\t" \
    "v_add_f32 v208, v208, v64\n\t" \
    "v_add_f32 v209, v209, v65\n\t" \
    "v_add_f32 v210, v210, v66\n\t" \
    "v_add_f32 v211, v211, v67\n\t" \
    "v_cvt_pk_bf16_f32 v52, v196, v197\n\t" \
    "v_cvt_pk_bf16_f32 v53, v198, v199\n\t" \
    "v_cvt_pk_bf16_f32 v54, v204, v205\n\t" \
    "v_cvt_pk_bf16_f32 v55, v206, v207\n\t" \
    "v_cvt_pk_bf16_f32 v56, v200, v201\n\t" \
    "v_cvt_pk_bf16_f32 v57, v202, v203\n\t" \
    "v_cvt_pk_bf16_f32 v58, v208, v209\n\t" \
    "v_cvt_pk_bf16_f32 v59, v210, v211\n\t" \
    "buffer_store_dwordx4 v[52:55], %[l16], %[rs], %[oo] offen sc1\n\t" \
    "buffer_store_dwordx4 v[56:59], %[l16], %[rs], %[oo] offen offset:1024 sc1\n\t" \
    "E_%=:\n\t" \
    "s_setprio 0\n\t" \
    "s_waitcnt lgkmcnt(0)\n\t" \
    "s_nop 7\n\t" \
    : [t] "+s"(t), [sx] "+s"(sx), [oo] "+s"(oo), [a] "=&s"(tmp), [b] "=&s"(tmp2), [em] "=&s"(em), "+{v[20:27]}"(xf[0]), "+{v[28:35]}"(xf[1]), "+{v[36:43]}"(xf[2]), "+{v[196:203]}"(ac[0]), "+{v[204:211]}"(ac[1]) \
    : [te] "s"(TE_), [fin] "s"(FIN_), [kh] "s"(kh), [sd] "s"(sd), [rs] "s"(rs), [l16] "v"(l16), [xp] "v"(xp), [pa] "v"(pa), [fm] "s"(fm), [pm] "s"(pm), [fo] "s"(fo), [po] "s"(po), [pd] "s"(pd), [tag] "s"(P.tag), "{v[68:75]}"(w[0][0]), "{v[76:83]}"(w[0][1]), "{v[84:91]}"(w[1][0]), "{v[92:99]}"(w[1][1]), "{v[100:107]}"(w[2][0]), "{v[108:115]}"(w[2][1]), "{v[116:123]}"(w[3][0]), "{v[124:131]}"(w[3][1]), "{v[132:139]}"(w[4][0]), "{v[140:147]}"(w[4][1]), "{v[148:155]}"(w[5][0]), "{v[156:163]}"(w[5][1]), "{v[164:171]}"(w[6][0]), "{v[172:179]}"(w[6][1]), "{v[180:187]}"(w[7][0]), "{v[188:195]}"(w[7][1]) \
    : "memory", "scc", "v0", "v1", "v2", "v3", "v4", "v5", "v6", "v7", "v8", "v9", "v10", "v11", "v12", "v13", "v14", "v15", "v44", "v45", "v46", "v47", "v48", "v49", "v50", "v51", "v52", "v53", "v54", "v55", "v56", "v57", "v58", "v59", "v60", "v61", "v62", "v63", "v64", "v65", "v66", "v67")

#define S_LOOP_X(TE_, FIN_) \
  asm volatile( \
    "s_cmp_eq_u32 %[kh], 0\n\t" \
    "s_cbranch_scc0 L1_%=\n\t" \
    "L0_%=:\n\t" \
    "s_setprio 0\n\t" \
    "s_nop 3\n\t" \
    "ds_write_b128 %[pa], v[196:199] offset:0\n\t" \
    "ds_write_b128 %[pa], v[200:203] offset:1024\n\t" \
    "ds_write_b128 %[pa], v[204:207] offset:2048\n\t" \
    "ds_write_b128 %[pa], v[208:211] offset:3072\n\t" \
    "s_sub_u32 %[a], %[t], 1\n\t" \
    "v_mov_b32 v53, %[tag]\n\t" \
    "v_mov_b32 v52, %[a]\n\t" \
    "s_sub_u32 %[b], %[t], 2\n\t" \
    "s_cmp_lt_u32 %[b], " STR(SQUICK) "\n\t" \
    "s_cselect_b64 exec, %[fm], 0\n\t" \
    "buffer_store_dwordx2 v[52:53], %[l16], %[rs], %[fo] offen sc1\n\t" \
    "s_mov_b64 exec, -1\n\t" \
    "s_add_u32 m0, %[sd], 49152\n\t" \
    "s_add_u32 %[a], %[sx], 278528\n\t" \
    "s_waitcnt lgkmcnt(0)\n\t" \
    "buffer_load_dwordx4 %[l16], %[rs], %[a] offen offset:0 sc1 lds\n\t" \
    "buffer_load_dwordx4 %[l16], %[rs], %[a] offen offset:1024 sc1 lds\n\t" \
    "buffer_load_dwordx4 %[l16], %[rs], %[a] offen offset:2048 sc1 lds\n\t" \
    "buffer_load_dwordx4 %[l16], %[rs], %[a] offen offset:3072 sc1 lds\n\t" \
    "v_mfma_f32_16x16x32_bf16 v[0:3], v[68:71], v[20:23], 0\n\t" \
    "ds_read_b128 v[44:47], %[xp] offset:6144\n\t" \
    "v_mfma_f32_16x16x32_bf16 v[4:7], v[76:79], v[20:23], 0\n\t" \
    "ds_read_b128 v[48:51], %[xp] offset:7168\n\t" \
    "v_mfma_f32_16x16x32_bf16 v[8:11], v[68:71], v[24:27], 0\n\t" \
    "v_mfma_f32_16x16x32_bf16 v[12:15], v[76:79], v[24:27], 0\n\t" \
    "v_mfma_f32_16x16x32_bf16 v[0:3], v[72:75], v[28:31], v[0:3]\n\t" \
    "ds_read_b128 v[20:23], %[xp] offset:8192\n\t" \
    "v_mfma_f32_16x16x32_bf16 v[4:7], v[80:83], v[28:31], v[4:7]\n\t" \
    "ds_read_b128 v[24:27], %[xp] offset:9216\n\t" \
    "v_mfma_f32_16x16x32_bf16 v[8:11], v[72:75], v[32:35], v[8:11]\n\t" \
    "v_mfma_f32_16x16x32_bf16 v[12:15], v[80:83], v[32:35], v[12:15]\n\t" \
    "v_mfma_f32_16x16x32_bf16 v[0:3], v[84:87], v[36:39], v[0:3]\n\t" \
    "ds_read_b128 v[28:31], %[xp] offset:10240\n\t" \
    "v_mfma_f32_16x16x32_bf16 v[4:7], v[92:95], v[36:39], v[4:7]\n\t" \
    "ds_read_b128 v[32:35], %[xp] offset:11264\n\t" \
    "v_mfma_f32_16x16x32_bf16 v[8:11], v[84:87], v[40:43], v[8:11]\n\t" \
    "s_waitcnt lgkmcnt(4)\n\t" \
    "v_mfma_f32_16x16x32_bf16 v[12:15], v[92:95], v[40:43], v[12:15]\n\t" \
    "v_mfma_f32_16x16x32_bf16 v[0:3], v[88:91], v[44:47], v[0:3]\n\t" \
    "ds_read_b128 v[36:39], %[xp] offset:12288\n\t" \
    "v_mfma_f32_16x16x32_bf16 v[4:7], v[96:99], v[44:47], v[4:7]\n\t" \
    "ds_read_b128 v[40:43], %[xp] offset:13312\n\t" \
    "v_mfma_f32_16x16x32_bf16 v[8:11], v[88:91], v[48:51], v[8:11]\n\t" \
    "s_waitcnt lgkmcnt(4)\n\t" \
    "v_mfma_f32_16x16x32_bf16 v[12:15], v[96:99], v[48:51], v[12:15]\n\t" \
    "v_mfma_f32_16x16x32_bf16 v[0:3], v[100:103], v[20:23], v[0:3]\n\t" \
    "ds_read_b128 v[44:47], %[xp] offset:14336\n\t" \
    "v_mfma_f32_16x16x32_bf16 v[4:7], v[108:111], v[20:23], v[4:7]\n\t" \
    "ds_read_b128 v[48:51], %[xp] offset:15360\n\t" \
    "v_mfma_f32_16x16x32_bf16 v[8:11], v[100:103], v[24:27], v[8:11]\n\t" \
    "s_waitcnt lgkmcnt(4)\n\t" \
    "v_mfma_f32_16x16x32_bf16 v[12:15], v[108:111], v[24:27], v[12:15]\n\t" \
    "v_mfma_f32_16x16x32_bf16 v[0:3], v[104:107], v[28:31], v[0:3]\n\t" \
    "ds_read_b128 v[20:23], %[xp] offset:16384\n\t" \
    "v_mfma_f32_16x16x32_bf16 v[4:7], v[112:115], v[28:31], v[4:7]\n\t" \
    "ds_read_b128 v[24:27], %[xp] offset:17408\n\t" \
    "v_mfma_f32_16x16x32_bf16 v[8:11], v[104:107], v[32:35], v[8:11]\n\t" \
    "s_waitcnt lgkmcnt(4)\n\t" \
    "v_mfma_f32_16x16x32_bf16 v[12:15], v[112:115], v[32:35], v[12:15]\n\t" \
    "v_mfma_f32_16x16x32_bf16 v[0:3], v[116:119], v[36:39], v[0:3]\n\t" \
    "ds_read_b128 v[28:31], %[xp] offset:18432\n\t" \
    "v_mfma_f32_16x16x32_bf16 v[4:7], v[124:127], v[36:39], v[4:7]\n\t" \
    "ds_read_b128 v[32:35], %[xp] offset:19456\n\t" \
    "v_mfma_f32_16x16x32_bf16 v[8:11], v[116:119], v[40:43], v[8:11]\n\t" \
    "s_waitcnt lgkmcnt(4)\n\t" \
    "v_mfma_f32_16x16x32_bf16 v[12:15], v[124:127], v[40:43], v[12:15]\n\t" \
    "v_mfma_f32_16x16x32_bf16 v[0:3], v[120:123], v[44:47], v[0:3]\n\t" \
    "ds_read_b128 v[36:39], %[xp] offset:20480\n\t" \
    "v_mfma_f32_16x16x32_bf16 v[4:7], v[128:131], v[44:47], v[4:7]\n\t" \
    "ds_read_b128 v[40:43], %[xp] offset:21504\n\t" \
    "v_mfma_f32_16x16x32_bf16 v[8:11], v[120:123], v[48:51], v[8:11]\n\t" \
    "s_waitcnt lgkmcnt(4)\n\t" \
    "v_mfma_f32_16x16x32_bf16 v[12:15], v[128:131], v[48:51], v[12:15]\n\t" \
    "s_waitcnt vmcnt(4)\n\t" \
    "s_barrier\n\t" \
    "s_setprio 0\n\t" \
    "s_sub_u32 %[a], %[t], 1\n\t" \
    "v_mov_b32 v53, %[tag]\n\t" \
    "v_mov_b32 v52, %[a]\n\t" \
    "s_cmp_gt_u32 %[t], 1\n\t" \
    "s_cselect_b64 exec, %[fm], 0\n\t" \
    "buffer_store_dwordx2 v[52:53], %[l16], %[rs], %[fo] offen sc1\n\t" \
    "s_mov_b64 exec, %[pm]\n\t" \
    "s_mov_b32 m0, %[pd]\n\t" \
    "s_add_u32 %[a], %[sx], %[po]\n\t" \
    "buffer_load_dwordx4 %[l16], %[rs], %[a] offen sc1 lds\n\t" \
    "s_mov_b64 exec, -1\n\t" \
    "s_add_u32 m0, %[sd], 0\n\t" \
    "s_add_u32 %[a], %[sx], 524288\n\t" \
    "buffer_load_dwordx4 %[l16], %[rs], %[a] offen offset:0 sc1 lds\n\t" \
    "buffer_load_dwordx4 %[l16], %[rs], %[a] offen offset:1024 sc1 lds\n\t" \
    "buffer_load_dwordx4 %[l16], %[rs], %[a] offen offset:2048 sc1 lds\n\t" \
    "buffer_load_dwordx4 %[l16], %[rs], %[a] offen offset:3072 sc1 lds\n\t" \
    "v_mfma_f32_16x16x32_bf16 v[0:3], v[132:135], v[20:23], v[0:3]\n\t" \
    "ds_read_b128 v[44:47], %[xp] offset:22528\n\t" \
    "v_mfma_f32_16x16x32_bf16 v[4:7], v[140:143], v[20:23], v[4:7]\n\t" \
    "ds_read_b128 v[48:51], %[xp] offset:23552\n\t" \
    "v_mfma_f32_16x16x32_bf16 v[8:11], v[132:135], v[24:27], v[8:11]\n\t" \
    "s_waitcnt lgkmcnt(4)\n\t" \
    "v_mfma_f32_16x16x32_bf16 v[12:15], v[140:143], v[24:27], v[12:15]\n\t" \
    "v_mfma_f32_16x16x32_bf16 v[0:3], v[136:139], v[28:31], v[0:3]\n\t" \
    "ds_read_b128 v[20:23], %[xp] offset:24576\n\t" \
    "v_mfma_f32_16x16x32_bf16 v[4:7], v[144:147], v[28:31], v[4:7]\n\t" \
    "ds_read_b128 v[24:27], %[xp] offset:25600\n\t" \
    "v_mfma_f32_16x16x32_bf16 v[8:11], v[136:139], v[32:35], v[8:11]\n\t" \
    "s_waitcnt lgkmcnt(4)\n\t" \
    "v_mfma_f32_16x16x32_bf16 v[12:15], v[144:147], v[32:35], v[12:15]\n\t" \
    "v_mfma_f32_16x16x32_bf16 v[0:3], v[148:151], v[36:39], v[0:3]\n\t" \
    "ds_read_b128 v[28:31], %[xp] offset:26624\n\t" \
    "v_mfma_f32_16x16x32_bf16 v[4:7], v[156:159], v[36:39], v[4:7]\n\t" \
    "ds_read_b128 v[32:35], %[xp] offset:27648\n\t" \
    "v_mfma_f32_16x16x32_bf16 v[8:11], v[148:151], v[40:43], v[8:11]\n\t" \
    "s_waitcnt lgkmcnt(4)\n\t" \
    "v_mfma_f32_16x16x32_bf16 v[12:15], v[156:159], v[40:43], v[12:15]\n\t" \
    "v_mfma_f32_16x16x32_bf16 v[0:3], v[152:155], v[44:47], v[0:3]\n\t" \
    "ds_read_b128 v[36:39], %[xp] offset:28672\n\t" \
    "v_mfma_f32_16x16x32_bf16 v[4:7], v[160:163], v[44:47], v[4:7]\n\t" \
    "ds_read_b128 v[40:43], %[xp] offset:29696\n\t" \
    "v_mfma_f32_16x16x32_bf16 v[8:11], v[152:155], v[48:51], v[8:11]\n\t" \
    "s_waitcnt lgkmcnt(4)\n\t" \
    "v_mfma_f32_16x16x32_bf16 v[12:15], v[160:163], v[48:51], v[12:15]\n\t" \
    "v_mfma_f32_16x16x32_bf16 v[0:3], v[164:167], v[20:23], v[0:3]\n\t" \
    "ds_read_b128 v[44:47], %[xp] offset:30720\n\t" \
    "v_mfma_f32_16x16x32_bf16 v[4:7], v[172:175], v[20:23], v[4:7]\n\t" \
    "ds_read_b128 v[48:51], %[xp] offset:31744\n\t" \
    "v_mfma_f32_16x16x32_bf16 v[8:11], v[164:167], v[24:27], v[8:11]\n\t" \
    "s_waitcnt lgkmcnt(4)\n\t" \
    "v_mfma_f32_16x16x32_bf16 v[12:15], v[172:175], v[24:27], v[12:15]\n\t" \
    "v_mfma_f32_16x16x32_bf16 v[0:3], v[168:171], v[28:31], v[0:3]\n\t" \
    "ds_read_b128 v[20:23], %[xp] offset:32768\n\t" \
    "v_mfma_f32_16x16x32_bf16 v[4:7], v[176:179], v[28:31], v[4:7]\n\t" \
    "ds_read_b128 v[24:27], %[xp] offset:33792\n\t" \
    "v_mfma_f32_16x16x32_bf16 v[8:11], v[168:171], v[32:35], v[8:11]\n\t" \
    "s_waitcnt lgkmcnt(4)\n\t" \
    "v_mfma_f32_16x16x32_bf16 v[12:15], v[176:179], v[32:35], v[12:15]\n\t" \
    "v_mfma_f32_16x16x32_bf16 v[0:3], v[180:183], v[36:39], v[0:3]\n\t" \
    "ds_read_b128 v[28:31], %[xp] offset:34816\n\t" \
    "v_mfma_f32_16x16x32_bf16 v[4:7], v[188:191], v[36:39], v[4:7]\n\t" \
    "ds_read_b128 v[32:35], %[xp] offset:35840\n\t" \
    "v_mfma_f32_16x16x32_bf16 v[8:11], v[180:183], v[40:43], v[8:11]\n\t" \
    "s_waitcnt lgkmcnt(4)\n\t" \
    "v_mfma_f32_16x16x32_bf16 v[12:15], v[188:191], v[40:43], v[12:15]\n\t" \
    "v_mfma_f32_16x16x32_bf16 v[0:3], v[184:187], v[44:47], v[0:3]\n\t" \
    "ds_read_b128 v[36:39], %[xp] offset:36864\n\t" \
    "v_mfma_f32_16x16x32_bf16 v[4:7], v[192:195], v[44:47], v[4:7]\n\t" \
    "ds_read_b128 v[40:43], %[xp] offset:37888\n\t" \
    "v_mfma_f32_16x16x32_bf16 v[8:11], v[184:187], v[48:51], v[8:11]\n\t" \
    "s_waitcnt lgkmcnt(4)\n\t" \
    "v_mfma_f32_16x16x32_bf16 v[12:15], v[192:195], v[48:51], v[12:15]\n\t" \
    "s_add_u32 %[sx], %[sx], 262144\n\t" \
    "s_add_u32 %[t], %[t], 1\n\t" \
    "s_waitcnt vmcnt(4)\n\t" \
    "s_barrier\n\t" \
    "s_setprio 0\n\t" \
    "s_nop 3\n\t" \
    "ds_write_b128 %[pa], v[0:3] offset:0\n\t" \
    "ds_write_b128 %[pa], v[4:7] offset:1024\n\t" \
    "ds_write_b128 %[pa], v[8:11] offset:2048\n\t" \
    "ds_write_b128 %[pa], v[12:15] offset:3072\n\t" \
    "s_sub_u32 %[a], %[t], 1\n\t" \
    "v_mov_b32 v53, %[tag]\n\t" \
    "v_mov_b32 v52, %[a]\n\t" \
    "s_sub_u32 %[b], %[t], 2\n\t" \
    "s_cmp_lt_u32 %[b], " STR(SQUICK) "\n\t" \
    "s_cselect_b64 exec, %[fm], 0\n\t" \
    "buffer_store_dwordx2 v[52:53], %[l16], %[rs], %[fo] offen sc1\n\t" \
    "s_mov_b64 exec, -1\n\t" \
    "s_add_u32 m0, %[sd], 16384\n\t" \
    "s_add_u32 %[a], %[sx], 278528\n\t" \
    "s_waitcnt lgkmcnt(0)\n\t" \
    "buffer_load_dwordx4 %[l16], %[rs], %[a] offen offset:0 sc1 lds\n\t" \
    "buffer_load_dwordx4 %[l16], %[rs], %[a] offen offset:1024 sc1 lds\n\t" \
    "buffer_load_dwordx4 %[l16], %[rs], %[a] offen offset:2048 sc1 lds\n\t" \
    "buffer_load_dwordx4 %[l16], %[rs], %[a] offen offset:3072 sc1 lds\n\t" \
    "v_mfma_f32_16x16x32_bf16 v[196:199], v[68:71], v[20:23], 0\n\t" \
    "ds_read_b128 v[44:47], %[xp] offset:38912\n\t" \
    "v_mfma_f32_16x16x32_bf16 v[200:203], v[76:79], v[20:23], 0\n\t" \
    "ds_read_b128 v[48:51], %[xp] offset:39936\n\t" \
    "v_mfma_f32_16x16x32_bf16 v[204:207], v[68:71], v[24:27], 0\n\t" \
    "v_mfma_f32_16x16x32_bf16 v[208:211], v[76:79], v[24:27], 0\n\t" \
    "v_mfma_f32_16x16x32_bf16 v[196:199], v[72:75], v[28:31], v[196:199]\n\t" \
    "ds_read_b128 v[20:23], %[xp] offset:40960\n\t" \
    "v_mfma_f32_16x16x32_bf16 v[200:203], v[80:83], v[28:31], v[200:203]\n\t" \
    "ds_read_b128 v[24:27], %[xp] offset:41984\n\t" \
    "v_mfma_f32_16x16x32_bf16 v[204:207], v[72:75], v[32:35], v[204:207]\n\t" \
    "v_mfma_f32_16x16x32_bf16 v[208:211], v[80:83], v[32:35], v[208:211]\n\t" \
    "v_mfma_f32_16x16x32_bf16 v[196:199], v[84:87], v[36:39], v[196:199]\n\t" \
    "ds_read_b128 v[28:31], %[xp] offset:43008\n\t" \
    "v_mfma_f32_16x16x32_bf16 v[200:203], v[92:95], v[36:39], v[200:203]\n\t" \
    "ds_read_b128 v[32:35], %[xp] offset:44032\n\t" \
    "v_mfma_f32_16x16x32_bf16 v[204:207], v[84:87], v[40:43], v[204:207]\n\t" \
    "s_waitcnt lgkmcnt(4)\n\t" \
    "v_mfma_f32_16x16x32_bf16 v[208:211], v[92:95], v[40:43], v[208:211]\n\t" \
    "v_mfma_f32_16x16x32_bf16 v[196:199], v[88:91], v[44:47], v[196:199]\n\t" \
    "ds_read_b128 v[36:39], %[xp] offset:45056\n\t" \
    "v_mfma_f32_16x16x32_bf16 v[200:203], v[96:99], v[44:47], v[200:203]\n\t" \
    "ds_read_b128 v[40:43], %[xp] offset:46080\n\t" \
    "v_mfma_f32_16x16x32_bf16 v[204:207], v[88:91], v[48:51], v[204:207]\n\t" \
    "s_waitcnt lgkmcnt(4)\n\t" \
    "v_mfma_f32_16x16x32_bf16 v[208:211], v[96:99], v[48:51], v[208:211]\n\t" \
    "v_mfma_f32_16x16x32_bf16 v[196:199], v[100:103], v[20:23], v[196:199]\n\t" \
    "ds_read_b128 v[44:47], %[xp] offset:47104\n\t" \
    "v_mfma_f32_16x16x32_bf16 v[200:203], v[108:111], v[20:23], v[200:203]\n\t" \
    "ds_read_b128 v[48:51], %[xp] offset:48128\n\t" \
    "v_mfma_f32_16x16x32_bf16 v[204:207], v[100:103], v[24:27], v[204:207]\n\t" \
    "s_waitcnt lgkmcnt(4)\n\t" \
    "v_mfma_f32_16x16x32_bf16 v[208:211], v[108:111], v[24:27], v[208:211]\n\t" \
    "v_mfma_f32_16x16x32_bf16 v[196:199], v[104:107], v[28:31], v[196:199]\n\t" \
    "ds_read_b128 v[20:23], %[xp] offset:49152\n\t" \
    "v_mfma_f32_16x16x32_bf16 v[200:203], v[112:115], v[28:31], v[200:203]\n\t" \
    "ds_read_b128 v[24:27], %[xp] offset:50176\n\t" \
    "v_mfma_f32_16x16x32_bf16 v[204:207], v[104:107], v[32:35], v[204:207]\n\t" \
    "s_waitcnt lgkmcnt(4)\n\t" \
    "v_mfma_f32_16x16x32_bf16 v[208:211], v[112:115], v[32:35], v[208:211]\n\t" \
    "v_mfma_f32_16x16x32_bf16 v[196:199], v[116:119], v[36:39], v[196:199]\n\t" \
    "ds_read_b128 v[28:31], %[xp] offset:51200\n\t" \
    "v_mfma_f32_16x16x32_bf16 v[200:203], v[124:127], v[36:39], v[200:203]\n\t" \
    "ds_read_b128 v[32:35], %[xp] offset:52224\n\t" \
    "v_mfma_f32_16x16x32_bf16 v[204:207], v[116:119], v[40:43], v[204:207]\n\t" \
    "s_waitcnt lgkmcnt(4)\n\t" \
    "v_mfma_f32_16x16x32_bf16 v[208:211], v[124:127], v[40:43], v[208:211]\n\t" \
    "v_mfma_f32_16x16x32_bf16 v[196:199], v[120:123], v[44:47], v[196:199]\n\t" \
    "ds_read_b128 v[36:39], %[xp] offset:53248\n\t" \
    "v_mfma_f32_16x16x32_bf16 v[200:203], v[128:131], v[44:47], v[200:203]\n\t" \
    "ds_read_b128 v[40:43], %[xp] offset:54272\n\t" \
    "v_mfma_f32_16x16x32_bf16 v[204:207], v[120:123], v[48:51], v[204:207]\n\t" \
    "s_waitcnt lgkmcnt(4)\n\t" \
    "v_mfma_f32_16x16x32_bf16 v[208:211], v[128:131], v[48:51], v[208:211]\n\t" \
    "s_waitcnt vmcnt(4)\n\t" \
    "s_barrier\n\t" \
    "s_setprio 0\n\t" \
    "s_sub_u32 %[a], %[t], 1\n\t" \
    "v_mov_b32 v53, %[tag]\n\t" \
    "v_mov_b32 v52, %[a]\n\t" \
    "s_cmp_gt_u32 %[t], 1\n\t" \
    "s_cselect_b64 exec, %[fm], 0\n\t" \
    "buffer_store_dwordx2 v[52:53], %[l16], %[rs], %[fo] offen sc1\n\t" \
    "s_mov_b64 exec, %[pm]\n\t" \
    "s_mov_b32 m0, %[pd]\n\t" \
    "s_add_u32 %[a], %[sx], %[po]\n\t" \
    "buffer_load_dwordx4 %[l16], %[rs], %[a] offen sc1 lds\n\t" \
    "s_mov_b64 exec, -1\n\t" \
    "s_add_u32 m0, %[sd], 32768\n\t" \
    "s_add_u32 %[a], %[sx], 524288\n\t" \
    "buffer_load_dwordx4 %[l16], %[rs], %[a] offen offset:0 sc1 lds\n\t" \
    "buffer_load_dwordx4 %[l16], %[rs], %[a] offen offset:1024 sc1 lds\n\t" \
    "buffer_load_dwordx4 %[l16], %[rs], %[a] offen offset:2048 sc1 lds\n\t" \
    "buffer_load_dwordx4 %[l16], %[rs], %[a] offen offset:3072 sc1 lds\n\t" \
    "v_mfma_f32_16x16x32_bf16 v[196:199], v[132:135], v[20:23], v[196:199]\n\t" \
    "ds_read_b128 v[44:47], %[xp] offset:55296\n\t" \
    "v_mfma_f32_16x16x32_bf16 v[200:203], v[140:143], v[20:23], v[200:203]\n\t" \
    "ds_read_b128 v[48:51], %[xp] offset:56320\n\t" \
    "v_mfma_f32_16x16x32_bf16 v[204:207], v[132:135], v[24:27], v[204:207]\n\t" \
    "s_waitcnt lgkmcnt(4)\n\t" \
    "v_mfma_f32_16x16x32_bf16 v[208:211], v[140:143], v[24:27], v[208:211]\n\t" \
    "v_mfma_f32_16x16x32_bf16 v[196:199], v[136:139], v[28:31], v[196:199]\n\t" \
    "ds_read_b128 v[20:23], %[xp] offset:57344\n\t" \
    "v_mfma_f32_16x16x32_bf16 v[200:203], v[144:147], v[28:31], v[200:203]\n\t" \
    "ds_read_b128 v[24:27], %[xp] offset:58368\n\t" \
    "v_mfma_f32_16x16x32_bf16 v[204:207], v[136:139], v[32:35], v[204:207]\n\t" \
    "s_waitcnt lgkmcnt(4)\n\t" \
    "v_mfma_f32_16x16x32_bf16 v[208:211], v[144:147], v[32:35], v[208:211]\n\t" \
    "v_mfma_f32_16x16x32_bf16 v[196:199], v[148:151], v[36:39], v[196:199]\n\t" \
    "ds_read_b128 v[28:31], %[xp] offset:59392\n\t" \
    "v_mfma_f32_16x16x32_bf16 v[200:203], v[156:159], v[36:39], v[200:203]\n\t" \
    "ds_read_b128 v[32:35], %[xp] offset:60416\n\t" \
    "v_mfma_f32_16x16x32_bf16 v[204:207], v[148:151], v[40:43], v[204:207]\n\t" \
    "s_waitcnt lgkmcnt(4)\n\t" \
    "v_mfma_f32_16x16x32_bf16 v[208:211], v[156:159], v[40:43], v[208:211]\n\t" \
    "v_mfma_f32_16x16x32_bf16 v[196:199], v[152:155], v[44:47], v[196:199]\n\t" \
    "ds_read_b128 v[36:39], %[xp] offset:61440\n\t" \
    "v_mfma_f32_16x16x32_bf16 v[200:203], v[160:163], v[44:47], v[200:203]\n\t" \
    "ds_read_b128 v[40:43], %[xp] offset:62464\n\t" \
    "v_mfma_f32_16x16x32_bf16 v[204:207], v[152:155], v[48:51], v[204:207]\n\t" \
    "s_waitcnt lgkmcnt(4)\n\t" \
    "v_mfma_f32_16x16x32_bf16 v[208:211], v[160:163], v[48:51], v[208:211]\n\t" \
    "v_mfma_f32_16x16x32_bf16 v[196:199], v[164:167], v[20:23], v[196:199]\n\t" \
    "ds_read_b128 v[44:47], %[xp] offset:63488\n\t" \
    "v_mfma_f32_16x16x32_bf16 v[200:203], v[172:175], v[20:23], v[200:203]\n\t" \
    "ds_read_b128 v[48:51], %[xp] offset:64512\n\t" \
    "v_mfma_f32_16x16x32_bf16 v[204:207], v[164:167], v[24:27], v[204:207]\n\t" \
    "s_waitcnt lgkmcnt(4)\n\t" \
    "v_mfma_f32_16x16x32_bf16 v[208:211], v[172:175], v[24:27], v[208:211]\n\t" \
    "v_mfma_f32_16x16x32_bf16 v[196:199], v[168:171], v[28:31], v[196:199]\n\t" \
    "ds_read_b128 v[20:23], %[xp] offset:0\n\t" \
    "v_mfma_f32_16x16x32_bf16 v[200:203], v[176:179], v[28:31], v[200:203]\n\t" \
    "ds_read_b128 v[24:27], %[xp] offset:1024\n\t" \
    "v_mfma_f32_16x16x32_bf16 v[204:207], v[168:171], v[32:35], v[204:207]\n\t" \
    "s_waitcnt lgkmcnt(4)\n\t" \
    "v_mfma_f32_16x16x32_bf16 v[208:211], v[176:179], v[32:35], v[208:211]\n\t" \
    "v_mfma_f32_16x16x32_bf16 v[196:199], v[180:183], v[36:39], v[196:199]\n\t" \
    "ds_read_b128 v[28:31], %[xp] offset:2048\n\t" \
    "v_mfma_f32_16x16x32_bf16 v[200:203], v[188:191], v[36:39], v[200:203]\n\t" \
    "ds_read_b128 v[32:35], %[xp] offset:3072\n\t" \
    "v_mfma_f32_16x16x32_bf16 v[204:207], v[180:183], v[40:43], v[204:207]\n\t" \
    "s_waitcnt lgkmcnt(4)\n\t" \
    "v_mfma_f32_16x16x32_bf16 v[208:211], v[188:191], v[40:43], v[208:211]\n\t" \
    "v_mfma_f32_16x16x32_bf16 v[196:199], v[184:187], v[44:47], v[196:199]\n\t" \
    "ds_read_b128 v[36:39], %[xp] offset:4096\n\t" \
    "v_mfma_f32_16x16x32_bf16 v[200:203], v[192:195], v[44:47], v[200:203]\n\t" \
    "ds_read_b128 v[40:43], %[xp] offset:5120\n\t" \
    "v_mfma_f32_16x16x32_bf16 v[204:207], v[184:187], v[48:51], v[204:207]\n\t" \
    "s_waitcnt lgkmcnt(4)\n\t" \
    "v_mfma_f32_16x16x32_bf16 v[208:211], v[192:195], v[48:51], v[208:211]\n\t" \
    "s_add_u32 %[sx], %[sx], 262144\n\t" \
    "s_add_u32 %[t], %[t], 1\n\t" \
    "s_waitcnt vmcnt(4)\n\t" \
    "s_barrier\n\t" \
    "s_cmp_lt_u32 %[t], %[te]\n\t" \
    "s_cbranch_scc1 L0_%=\n\t" \
    "s_cmp_eq_u32 %[fin], %[te]\n\t" \
    "s_cbranch_scc0 E_%=\n\t" \
    "s_nop 4\n\t" \
    "ds_write_b128 %[pa], v[196:199] offset:0\n\t" \
    "ds_write_b128 %[pa], v[200:203] offset:1024\n\t" \
    "ds_write_b128 %[pa], v[204:207] offset:2048\n\t" \
    "ds_write_b128 %[pa], v[208:211] offset:3072\n\t" \
    "s_waitcnt lgkmcnt(0)\n\t" \
    "s_barrier\n\t" \
    "s_branch E_%=\n\t" \
    "L1_%=:\n\t" \
    "s_setprio 1\n\t" \
    "s_add_u32 m0, %[sd], 49152\n\t" \
    "s_add_u32 %[a], %[sx], 278528\n\t" \
    "v_mfma_f32_16x16x32_bf16 v[0:3], v[68:71], v[20:23], 0\n\t" \
    "ds_read_b128 v[44:47], %[xp] offset:6144\n\t" \
    "v_mfma_f32_16x16x32_bf16 v[4:7], v[76:79], v[20:23], 0\n\t" \
    "ds_read_b128 v[48:51], %[xp] offset:7168\n\t" \
    "v_mfma_f32_16x16x32_bf16 v[8:11], v[68:71], v[24:27], 0\n\t" \
    "s_waitcnt lgkmcnt(4)\n\t" \
    "v_mfma_f32_16x16x32_bf16 v[12:15], v[76:79], v[24:27], 0\n\t" \
    "v_mfma_f32_16x16x32_bf16 v[0:3], v[72:75], v[28:31], v[0:3]\n\t" \
    "ds_read_b128 v[20:23], %[xp] offset:8192\n\t" \
    "v_mfma_f32_16x16x32_bf16 v[4:7], v[80:83], v[28:31], v[4:7]\n\t" \
    "ds_read_b128 v[24:27], %[xp] offset:9216\n\t" \
    "v_mfma_f32_16x16x32_bf16 v[8:11], v[72:75], v[32:35], v[8:11]\n\t" \
    "s_waitcnt lgkmcnt(4)\n\t" \
    "v_mfma_f32_16x16x32_bf16 v[12:15], v[80:83], v[32:35], v[12:15]\n\t" \
    "v_mfma_f32_16x16x32_bf16 v[0:3], v[84:87], v[36:39], v[0:3]\n\t" \
    "ds_read_b128 v[28:31], %[xp] offset:10240\n\t" \
    "v_mfma_f32_16x16x32_bf16 v[4:7], v[92:95], v[36:39], v[4:7]\n\t" \
    "ds_read_b128 v[32:35], %[xp] offset:11264\n\t" \
    "v_mfma_f32_16x16x32_bf16 v[8:11], v[84:87], v[40:43], v[8:11]\n\t" \
    "s_waitcnt lgkmcnt(4)\n\t" \
    "v_mfma_f32_16x16x32_bf16 v[12:15], v[92:95], v[40:43], v[12:15]\n\t" \
    "v_mfma_f32_16x16x32_bf16 v[0:3], v[88:91], v[44:47], v[0:3]\n\t" \
    "ds_read_b128 v[36:39], %[xp] offset:12288\n\t" \
    "v_mfma_f32_16x16x32_bf16 v[4:7], v[96:99], v[44:47], v[4:7]\n\t" \
    "ds_read_b128 v[40:43], %[xp] offset:13312\n\t" \
    "v_mfma_f32_16x16x32_bf16 v[8:11], v[88:91], v[48:51], v[8:11]\n\t" \
    "s_waitcnt lgkmcnt(4)\n\t" \
    "v_mfma_f32_16x16x32_bf16 v[12:15], v[96:99], v[48:51], v[12:15]\n\t" \
    "v_mfma_f32_16x16x32_bf16 v[0:3], v[100:103], v[20:23], v[0:3]\n\t" \
    "ds_read_b128 v[44:47], %[xp] offset:14336\n\t" \
    "v_mfma_f32_16x16x32_bf16 v[4:7], v[108:111], v[20:23], v[4:7]\n\t" \
    "ds_read_b128 v[48:51], %[xp] offset:15360\n\t" \
    "v_mfma_f32_16x16x32_bf16 v[8:11], v[100:103], v[24:27], v[8:11]\n\t" \
    "s_waitcnt lgkmcnt(4)\n\t" \
    "v_mfma_f32_16x16x32_bf16 v[12:15], v[108:111], v[24:27], v[12:15]\n\t" \
    "buffer_load_dwordx4 %[l16], %[rs], %[a] offen offset:0 sc1 lds\n\t" \
    "v_mfma_f32_16x16x32_bf16 v[0:3], v[104:107], v[28:31], v[0:3]\n\t" \
    "ds_read_b128 v[20:23], %[xp] offset:16384\n\t" \
    "v_mfma_f32_16x16x32_bf16 v[4:7], v[112:115], v[28:31], v[4:7]\n\t" \
    "ds_read_b128 v[24:27], %[xp] offset:17408\n\t" \
    "v_mfma_f32_16x16x32_bf16 v[8:11], v[104:107], v[32:35], v[8:11]\n\t" \
    "s_waitcnt lgkmcnt(4)\n\t" \
    "v_mfma_f32_16x16x32_bf16 v[12:15], v[112:115], v[32:35], v[12:15]\n\t" \
    "buffer_load_dwordx4 %[l16], %[rs], %[a] offen offset:1024 sc1 lds\n\t" \
    "v_mfma_f32_16x16x32_bf16 v[0:3], v[116:119], v[36:39], v[0:3]\n\t" \
    "ds_read_b128 v[28:31], %[xp] offset:18432\n\t" \
    "v_mfma_f32_16x16x32_bf16 v[4:7], v[124:127], v[36:39], v[4:7]\n\t" \
    "ds_read_b128 v[32:35], %[xp] offset:19456\n\t" \
    "v_mfma_f32_16x16x32_bf16 v[8:11], v[116:119], v[40:43], v[8:11]\n\t" \
    "s_waitcnt lgkmcnt(4)\n\t" \
    "v_mfma_f32_16x16x32_bf16 v[12:15], v[124:127], v[40:43], v[12:15]\n\t" \
    "buffer_load_dwordx4 %[l16], %[rs], %[a] offen offset:2048 sc1 lds\n\t" \
    "v_mfma_f32_16x16x32_bf16 v[0:3], v[120:123], v[44:47], v[0:3]\n\t" \
    "ds_read_b128 v[36:39], %[xp] offset:20480\n\t" \
    "v_mfma_f32_16x16x32_bf16 v[4:7], v[128:131], v[44:47], v[4:7]\n\t" \
    "ds_read_b128 v[40:43], %[xp] offset:21504\n\t" \
    "v_mfma_f32_16x16x32_bf16 v[8:11], v[120:123], v[48:51], v[8:11]\n\t" \
    "s_waitcnt lgkmcnt(4)\n\t" \
    "v_mfma_f32_16x16x32_bf16 v[12:15], v[128:131], v[48:51], v[12:15]\n\t" \
    "buffer_load_dwordx4 %[l16], %[rs], %[a] offen offset:3072 sc1 lds\n\t" \
    "s_waitcnt vmcnt(4)\n\t" \
    "s_barrier\n\t" \
    "s_setprio 1\n\t" \
    "s_add_u32 m0, %[sd], 0\n\t" \
    "s_add_u32 %[a], %[sx], 524288\n\t" \
    "s_add_u32 %[b], %[oo], 0x1000\n\t" \
    "s_cmp_lg_u32 %[t], 0\n\t" \
    "s_cselect_b64 %[em], -1, 0\n\t" \
    "v_mfma_f32_16x16x32_bf16 v[0:3], v[132:135], v[20:23], v[0:3]\n\t" \
    "ds_read_b128 v[44:47], %[xp] offset:22528\n\t" \
    "v_mfma_f32_16x16x32_bf16 v[4:7], v[140:143], v[20:23], v[4:7]\n\t" \
    "ds_read_b128 v[48:51], %[xp] offset:23552\n\t" \
    "v_mfma_f32_16x16x32_bf16 v[8:11], v[132:135], v[24:27], v[8:11]\n\t" \
    "ds_read_b128 v[52:55], %[pa] offset:0\n\t" \
    "s_waitcnt lgkmcnt(5)\n\t" \
    "v_mfma_f32_16x16x32_bf16 v[12:15], v[140:143], v[24:27], v[12:15]\n\t" \
    "buffer_load_dwordx4 %[l16], %[rs], %[a] offen offset:0 sc1 lds\n\t" \
    "v_mfma_f32_16x16x32_bf16 v[0:3], v[136:139], v[28:31], v[0:3]\n\t" \
    "ds_read_b128 v[20:23], %[xp] offset:24576\n\t" \
    "v_mfma_f32_16x16x32_bf16 v[4:7], v[144:147], v[28:31], v[4:7]\n\t" \
    "ds_read_b128 v[24:27], %[xp] offset:25600\n\t" \
    "v_mfma_f32_16x16x32_bf16 v[8:11], v[136:139], v[32:35], v[8:11]\n\t" \
    "ds_read_b128 v[56:59], %[pa] offset:1024\n\t" \
    "s_waitcnt lgkmcnt(6)\n\t" \
    "v_mfma_f32_16x16x32_bf16 v[12:15], v[144:147], v[32:35], v[12:15]\n\t" \
    "buffer_load_dwordx4 %[l16], %[rs], %[a] offen offset:1024 sc1 lds\n\t" \
    "v_mfma_f32_16x16x32_bf16 v[0:3], v[148:151], v[36:39], v[0:3]\n\t" \
    "ds_read_b128 v[28:31], %[xp] offset:26624\n\t" \
    "v_mfma_f32_16x16x32_bf16 v[4:7], v[156:159], v[36:39], v[4:7]\n\t" \
    "ds_read_b128 v[32:35], %[xp] offset:27648\n\t" \
    "v_mfma_f32_16x16x32_bf16 v[8:11], v[148:151], v[40:43], v[8:11]\n\t" \
    "ds_read_b128 v[60:63], %[pa] offset:2048\n\t" \
    "s_waitcnt lgkmcnt(7)\n\t" \
    "v_mfma_f32_16x16x32_bf16 v[12:15], v[156:159], v[40:43], v[12:15]\n\t" \
    "buffer_load_dwordx4 %[l16], %[rs], %[a] offen offset:2048 sc1 lds\n\t" \
    "v_mfma_f32_16x16x32_bf16 v[0:3], v[152:155], v[44:47], v[0:3]\n\t" \
    "ds_read_b128 v[36:39], %[xp] offset:28672\n\t" \
    "s_waitcnt lgkmcnt(7)\n\t" \
    "v_add_f32 v196, v196, v52\n\t" \
    "v_mfma_f32_16x16x32_bf16 v[4:7], v[160:163], v[44:47], v[4:7]\n\t" \
    "ds_read_b128 v[40:43], %[xp] offset:29696\n\t" \
    "v_add_f32 v197, v197, v53\n\t" \
    "v_mfma_f32_16x16x32_bf16 v[8:11], v[152:155], v[48:51], v[8:11]\n\t" \
    "ds_read_b128 v[64:67], %[pa] offset:3072\n\t" \
    "s_waitcnt lgkmcnt(7)\n\t" \
    "v_mfma_f32_16x16x32_bf16 v[12:15], v[160:163], v[48:51], v[12:15]\n\t" \
    "buffer_load_dwordx4 %[l16], %[rs], %[a] offen offset:3072 sc1 lds\n\t" \
    "v_add_f32 v198, v198, v54\n\t" \
    "v_add_f32 v199, v199, v55\n\t" \
    "v_mfma_f32_16x16x32_bf16 v[0:3], v[164:167], v[20:23], v[0:3]\n\t" \
    "ds_read_b128 v[44:47], %[xp] offset:30720\n\t" \
    "s_waitcnt lgkmcnt(7)\n\t" \
    "v_add_f32 v200, v200, v56\n\t" \
    "v_mfma_f32_16x16x32_bf16 v[4:7], v[172:175], v[20:23], v[4:7]\n\t" \
    "ds_read_b128 v[48:51], %[xp] offset:31744\n\t" \
    "v_add_f32 v201, v201, v57\n\t" \
    "v_mfma_f32_16x16x32_bf16 v[8:11], v[164:167], v[24:27], v[8:11]\n\t" \
    "v_add_f32 v202, v202, v58\n\t" \
    "s_waitcnt lgkmcnt(6)\n\t" \
    "v_mfma_f32_16x16x32_bf16 v[12:15], v[172:175], v[24:27], v[12:15]\n\t" \
    "v_add_f32 v203, v203, v59\n\t" \
    "s_waitcnt lgkmcnt(5)\n\t" \
    "v_add_f32 v204, v204, v60\n\t" \
    "v_add_f32 v205, v205, v61\n\t" \
    "v_mfma_f32_16x16x32_bf16 v[0:3], v[168:171], v[28:31], v[0:3]\n\t" \
    "ds_read_b128 v[20:23], %[xp] offset:32768\n\t" \
    "v_add_f32 v206, v206, v62\n\t" \
    "v_mfma_f32_16x16x32_bf16 v[4:7], v[176:179], v[28:31], v[4:7]\n\t" \
    "ds_read_b128 v[24:27], %[xp] offset:33792\n\t" \
    "v_add_f32 v207, v207, v63\n\t" \
    "v_mfma_f32_16x16x32_bf16 v[8:11], v[168:171], v[32:35], v[8:11]\n\t" \
    "s_waitcnt lgkmcnt(4)\n\t" \
    "v_add_f32 v208, v208, v64\n\t" \
    "v_mfma_f32_16x16x32_bf16 v[12:15], v[176:179], v[32:35], v[12:15]\n\t" \
    "v_add_f32 v209, v209, v65\n\t" \
    "v_add_f32 v210, v210, v66\n\t" \
    "v_add_f32 v211, v211, v67\n\t" \
    "v_mfma_f32_16x16x32_bf16 v[0:3], v[180:183], v[36:39], v[0:3]\n\t" \
    "ds_read_b128 v[28:31], %[xp] offset:34816\n\t" \
    "v_mfma_f32_16x16x32_bf16 v[4:7], v[188:191], v[36:39], v[4:7]\n\t" \
    "ds_read_b128 v[32:35], %[xp] offset:35840\n\t" \
    "s_mov_b64 exec, %[em]\n\t" \
    "buffer_store_dwordx4 v[196:199], %[l16], %[rs], %[oo] offen offset:0 sc1\n\t" \
    "s_mov_b64 exec, -1\n\t" \
    "v_mfma_f32_16x16x32_bf16 v[8:11], v[180:183], v[40:43], v[8:11]\n\t" \
    "s_waitcnt lgkmcnt(4)\n\t" \
    "v_mfma_f32_16x16x32_bf16 v[12:15], v[188:191], v[40:43], v[12:15]\n\t" \
    "s_mov_b64 exec, %[em]\n\t" \
    "buffer_store_dwordx4 v[200:203], %[l16], %[rs], %[oo] offen offset:1024 sc1\n\t" \
    "s_mov_b64 exec, -1\n\t" \
    "v_mfma_f32_16x16x32_bf16 v[0:3], v[184:187], v[44:47], v[0:3]\n\t" \
    "ds_read_b128 v[36:39], %[xp] offset:36864\n\t" \
    "v_mfma_f32_16x16x32_bf16 v[4:7], v[192:195], v[44:47], v[4:7]\n\t" \
    "ds_read_b128 v[40:43], %[xp] offset:37888\n\t" \
    "s_mov_b64 exec, %[em]\n\t" \
    "buffer_store_dwordx4 v[204:207], %[l16], %[rs], %[oo] offen offset:2048 sc1\n\t" \
    "s_mov_b64 exec, -1\n\t" \
    "v_mfma_f32_16x16x32_bf16 v[8:11], v[184:187], v[48:51], v[8:11]\n\t" \
    "s_waitcnt lgkmcnt(4)\n\t" \
    "v_mfma_f32_16x16x32_bf16 v[12:15], v[192:195], v[48:51], v[12:15]\n\t" \
    "s_mov_b64 exec, %[em]\n\t" \
    "buffer_store_dwordx4 v[208:211], %[l16], %[rs], %[oo] offen offset:3072 sc1\n\t" \
    "s_mov_b64 exec, -1\n\t" \
    "s_add_u32 %[sx], %[sx], 262144\n\t" \
    "s_add_u32 %[t], %[t], 1\n\t" \
    "s_add_u32 %[oo], %[oo], 262144\n\t" \
    "s_waitcnt vmcnt(8)\n\t" \
    "s_cmp_gt_u32 %[t], " STR(SQUICK) " + 1\n\t" \
    "s_cbranch_scc1 B0_%=\n\t" \
    "s_waitcnt vmcnt(0)\n\t" \
    "B0_%=:\n\t" \
    "s_barrier\n\t" \
    "s_setprio 1\n\t" \
    "s_add_u32 m0, %[sd], 16384\n\t" \
    "s_add_u32 %[a], %[sx], 278528\n\t" \
    "v_mfma_f32_16x16x32_bf16 v[196:199], v[68:71], v[20:23], 0\n\t" \
    "ds_read_b128 v[44:47], %[xp] offset:38912\n\t" \
    "v_mfma_f32_16x16x32_bf16 v[200:203], v[76:79], v[20:23], 0\n\t" \
    "ds_read_b128 v[48:51], %[xp] offset:39936\n\t" \
    "v_mfma_f32_16x16x32_bf16 v[204:207], v[68:71], v[24:27], 0\n\t" \
    "s_waitcnt lgkmcnt(4)\n\t" \
    "v_mfma_f32_16x16x32_bf16 v[208:211], v[76:79], v[24:27], 0\n\t" \
    "v_mfma_f32_16x16x32_bf16 v[196:199], v[72:75], v[28:31], v[196:199]\n\t" \
    "ds_read_b128 v[20:23], %[xp] offset:40960\n\t" \
    "v_mfma_f32_16x16x32_bf16 v[200:203], v[80:83], v[28:31], v[200:203]\n\t" \
    "ds_read_b128 v[24:27], %[xp] offset:41984\n\t" \
    "v_mfma_f32_16x16x32_bf16 v[204:207], v[72:75], v[32:35], v[204:207]\n\t" \
    "s_waitcnt lgkmcnt(4)\n\t" \
    "v_mfma_f32_16x16x32_bf16 v[208:211], v[80:83], v[32:35], v[208:211]\n\t" \
    "v_mfma_f32_16x16x32_bf16 v[196:199], v[84:87], v[36:39], v[196:199]\n\t" \
    "ds_read_b128 v[28:31], %[xp] offset:43008\n\t" \
    "v_mfma_f32_16x16x32_bf16 v[200:203], v[92:95], v[36:39], v[200:203]\n\t" \
    "ds_read_b128 v[32:35], %[xp] offset:44032\n\t" \
    "v_mfma_f32_16x16x32_bf16 v[204:207], v[84:87], v[40:43], v[204:207]\n\t" \
    "s_waitcnt lgkmcnt(4)\n\t" \
    "v_mfma_f32_16x16x32_bf16 v[208:211], v[92:95], v[40:43], v[208:211]\n\t" \
    "v_mfma_f32_16x16x32_bf16 v[196:199], v[88:91], v[44:47], v[196:199]\n\t" \
    "ds_read_b128 v[36:39], %[xp] offset:45056\n\t" \
    "v_mfma_f32_16x16x32_bf16 v[200:203], v[96:99], v[44:47], v[200:203]\n\t" \
    "ds_read_b128 v[40:43], %[xp] offset:46080\n\t" \
    "v_mfma_f32_16x16x32_bf16 v[204:207], v[88:91], v[48:51], v[204:207]\n\t" \
    "s_waitcnt lgkmcnt(4)\n\t" \
    "v_mfma_f32_16x16x32_bf16 v[208:211], v[96:99], v[48:51], v[208:211]\n\t" \
    "v_mfma_f32_16x16x32_bf16 v[196:199], v[100:103], v[20:23], v[196:199]\n\t" \
    "ds_read_b128 v[44:47], %[xp] offset:47104\n\t" \
    "v_mfma_f32_16x16x32_bf16 v[200:203], v[108:111], v[20:23], v[200:203]\n\t" \
    "ds_read_b128 v[48:51], %[xp] offset:48128\n\t" \
    "v_mfma_f32_16x16x32_bf16 v[204:207], v[100:103], v[24:27], v[204:207]\n\t" \
    "s_waitcnt lgkmcnt(4)\n\t" \
    "v_mfma_f32_16x16x32_bf16 v[208:211], v[108:111], v[24:27], v[208:211]\n\t" \
    "buffer_load_dwordx4 %[l16], %[rs], %[a] offen offset:0 sc1 lds\n\t" \
    "v_mfma_f32_16x16x32_bf16 v[196:199], v[104:107], v[28:31], v[196:199]\n\t" \
    "ds_read_b128 v[20:23], %[xp] offset:49152\n\t" \
    "v_mfma_f32_16x16x32_bf16 v[200:203], v[112:115], v[28:31], v[200:203]\n\t" \
    "ds_read_b128 v[24:27], %[xp] offset:50176\n\t" \
    "v_mfma_f32_16x16x32_bf16 v[204:207], v[104:107], v[32:35], v[204:207]\n\t" \
    "s_waitcnt lgkmcnt(4)\n\t" \
    "v_mfma_f32_16x16x32_bf16 v[208:211], v[112:115], v[32:35], v[208:211]\n\t" \
    "buffer_load_dwordx4 %[l16], %[rs], %[a] offen offset:1024 sc1 lds\n\t" \
    "v_mfma_f32_16x16x32_bf16 v[196:199], v[116:119], v[36:39], v[196:199]\n\t" \
    "ds_read_b128 v[28:31], %[xp] offset:51200\n\t" \
    "v_mfma_f32_16x16x32_bf16 v[200:203], v[124:127], v[36:39], v[200:203]\n\t" \
    "ds_read_b128 v[32:35], %[xp] offset:52224\n\t" \
    "v_mfma_f32_16x16x32_bf16 v[204:207], v[116:119], v[40:43], v[204:207]\n\t" \
    "s_waitcnt lgkmcnt(4)\n\t" \
    "v_mfma_f32_16x16x32_bf16 v[208:211], v[124:127], v[40:43], v[208:211]\n\t" \
    "buffer_load_dwordx4 %[l16], %[rs], %[a] offen offset:2048 sc1 lds\n\t" \
    "v_mfma_f32_16x16x32_bf16 v[196:199], v[120:123], v[44:47], v[196:199]\n\t" \
    "ds_read_b128 v[36:39], %[xp] offset:53248\n\t" \
    "v_mfma_f32_16x16x32_bf16 v[200:203], v[128:131], v[44:47], v[200:203]\n\t" \
    "ds_read_b128 v[40:43], %[xp] offset:54272\n\t" \
    "v_mfma_f32_16x16x32_bf16 v[204:207], v[120:123], v[48:51], v[204:207]\n\t" \
    "s_waitcnt lgkmcnt(4)\n\t" \
    "v_mfma_f32_16x16x32_bf16 v[208:211], v[128:131], v[48:51], v[208:211]\n\t" \
    "buffer_load_dwordx4 %[l16], %[rs], %[a] offen offset:3072 sc1 lds\n\t" \
    "s_waitcnt vmcnt(4)\n\t" \
    "s_barrier\n\t" \
    "s_setprio 1\n\t" \
    "s_add_u32 m0, %[sd], 32768\n\t" \
    "s_add_u32 %[a], %[sx], 524288\n\t" \
    "s_add_u32 %[b], %[oo], 0x1000\n\t" \
    "s_cmp_lg_u32 %[t], 0\n\t" \
    "s_cselect_b64 %[em], -1, 0\n\t" \
    "v_mfma_f32_16x16x32_bf16 v[196:199], v[132:135], v[20:23], v[196:199]\n\t" \
    "ds_read_b128 v[44:47], %[xp] offset:55296\n\t" \
    "v_mfma_f32_16x16x32_bf16 v[200:203], v[140:143], v[20:23], v[200:203]\n\t" \
    "ds_read_b128 v[48:51], %[xp] offset:56320\n\t" \
    "v_mfma_f32_16x16x32_bf16 v[204:207], v[132:135], v[24:27], v[204:207]\n\t" \
    "ds_read_b128 v[52:55], %[pa] offset:0\n\t" \
    "s_waitcnt lgkmcnt(5)\n\t" \
    "v_mfma_f32_16x16x32_bf16 v[208:211], v[140:143], v[24:27], v[208:211]\n\t" \
    "buffer_load_dwordx4 %[l16], %[rs], %[a] offen offset:0 sc1 lds\n\t" \
    "v_mfma_f32_16x16x32_bf16 v[196:199], v[136:139], v[28:31], v[196:199]\n\t" \
    "ds_read_b128 v[20:23], %[xp] offset:57344\n\t" \
    "v_mfma_f32_16x16x32_bf16 v[200:203], v[144:147], v[28:31], v[200:203]\n\t" \
    "ds_read_b128 v[24:27], %[xp] offset:58368\n\t" \
    "v_mfma_f32_16x16x32_bf16 v[204:207], v[136:139], v[32:35], v[204:207]\n\t" \
    "ds_read_b128 v[56:59], %[pa] offset:1024\n\t" \
    "s_waitcnt lgkmcnt(6)\n\t" \
    "v_mfma_f32_16x16x32_bf16 v[208:211], v[144:147], v[32:35], v[208:211]\n\t" \
    "buffer_load_dwordx4 %[l16], %[rs], %[a] offen offset:1024 sc1 lds\n\t" \
    "v_mfma_f32_16x16x32_bf16 v[196:199], v[148:151], v[36:39], v[196:199]\n\t" \
    "ds_read_b128 v[28:31], %[xp] offset:59392\n\t" \
    "v_mfma_f32_16x16x32_bf16 v[200:203], v[156:159], v[36:39], v[200:203]\n\t" \
    "ds_read_b128 v[32:35], %[xp] offset:60416\n\t" \
    "v_mfma_f32_16x16x32_bf16 v[204:207], v[148:151], v[40:43], v[204:207]\n\t" \
    "ds_read_b128 v[60:63], %[pa] offset:2048\n\t" \
    "s_waitcnt lgkmcnt(7)\n\t" \
    "v_mfma_f32_16x16x32_bf16 v[208:211], v[156:159], v[40:43], v[208:211]\n\t" \
    "buffer_load_dwordx4 %[l16], %[rs], %[a] offen offset:2048 sc1 lds\n\t" \
    "v_mfma_f32_16x16x32_bf16 v[196:199], v[152:155], v[44:47], v[196:199]\n\t" \
    "ds_read_b128 v[36:39], %[xp] offset:61440\n\t" \
    "s_waitcnt lgkmcnt(7)\n\t" \
    "v_add_f32 v0, v0, v52\n\t" \
    "v_mfma_f32_16x16x32_bf16 v[200:203], v[160:163], v[44:47], v[200:203]\n\t" \
    "ds_read_b128 v[40:43], %[xp] offset:62464\n\t" \
    "v_add_f32 v1, v1, v53\n\t" \
    "v_mfma_f32_16x16x32_bf16 v[204:207], v[152:155], v[48:51], v[204:207]\n\t" \
    "ds_read_b128 v[64:67], %[pa] offset:3072\n\t" \
    "s_waitcnt lgkmcnt(7)\n\t" \
    "v_mfma_f32_16x16x32_bf16 v[208:211], v[160:163], v[48:51], v[208:211]\n\t" \
    "buffer_load_dwordx4 %[l16], %[rs], %[a] offen offset:3072 sc1 lds\n\t" \
    "v_add_f32 v2, v2, v54\n\t" \
    "v_add_f32 v3, v3, v55\n\t" \
    "v_mfma_f32_16x16x32_bf16 v[196:199], v[164:167], v[20:23], v[196:199]\n\t" \
    "ds_read_b128 v[44:47], %[xp] offset:63488\n\t" \
    "s_waitcnt lgkmcnt(7)\n\t" \
    "v_add_f32 v4, v4, v56\n\t" \
    "v_mfma_f32_16x16x32_bf16 v[200:203], v[172:175], v[20:23], v[200:203]\n\t" \
    "ds_read_b128 v[48:51], %[xp] offset:64512\n\t" \
    "v_add_f32 v5, v5, v57\n\t" \
    "v_mfma_f32_16x16x32_bf16 v[204:207], v[164:167], v[24:27], v[204:207]\n\t" \
    "v_add_f32 v6, v6, v58\n\t" \
    "s_waitcnt lgkmcnt(6)\n\t" \
    "v_mfma_f32_16x16x32_bf16 v[208:211], v[172:175], v[24:27], v[208:211]\n\t" \
    "v_add_f32 v7, v7, v59\n\t" \
    "s_waitcnt lgkmcnt(5)\n\t" \
    "v_add_f32 v8, v8, v60\n\t" \
    "v_add_f32 v9, v9, v61\n\t" \
    "v_mfma_f32_16x16x32_bf16 v[196:199], v[168:171], v[28:31], v[196:199]\n\t" \
    "ds_read_b128 v[20:23], %[xp] offset:0\n\t" \
    "v_add_f32 v10, v10, v62\n\t" \
    "v_mfma_f32_16x16x32_bf16 v[200:203], v[176:179], v[28:31], v[200:203]\n\t" \
    "ds_read_b128 v[24:27], %[xp] offset:1024\n\t" \
    "v_add_f32 v11, v11, v63\n\t" \
    "v_mfma_f32_16x16x32_bf16 v[204:207], v[168:171], v[32:35], v[204:207]\n\t" \
    "s_waitcnt lgkmcnt(4)\n\t" \
    "v_add_f32 v12, v12, v64\n\t" \
    "v_mfma_f32_16x16x32_bf16 v[208:211], v[176:179], v[32:35], v[208:211]\n\t" \
    "v_add_f32 v13, v13, v65\n\t" \
    "v_add_f32 v14, v14, v66\n\t" \
    "v_add_f32 v15, v15, v67\n\t" \
    "v_mfma_f32_16x16x32_bf16 v[196:199], v[180:183], v[36:39], v[196:199]\n\t" \
    "ds_read_b128 v[28:31], %[xp] offset:2048\n\t" \
    "v_mfma_f32_16x16x32_bf16 v[200:203], v[188:191], v[36:39], v[200:203]\n\t" \
    "ds_read_b128 v[32:35], %[xp] offset:3072\n\t" \
    "s_mov_b64 exec, %[em]\n\t" \
    "buffer_store_dwordx4 v[0:3], %[l16], %[rs], %[oo] offen offset:0 sc1\n\t" \
    "s_mov_b64 exec, -1\n\t" \
    "v_mfma_f32_16x16x32_bf16 v[204:207], v[180:183], v[40:43], v[204:207]\n\t" \
    "s_waitcnt lgkmcnt(4)\n\t" \
    "v_mfma_f32_16x16x32_bf16 v[208:211], v[188:191], v[40:43], v[208:211]\n\t" \
    "s_mov_b64 exec, %[em]\n\t" \
    "buffer_store_dwordx4 v[4:7], %[l16], %[rs], %[oo] offen offset:1024 sc1\n\t" \
    "s_mov_b64 exec, -1\n\t" \
    "v_mfma_f32_16x16x32_bf16 v[196:199], v[184:187], v[44:47], v[196:199]\n\t" \
    "ds_read_b128 v[36:39], %[xp] offset:4096\n\t" \
    "v_mfma_f32_16x16x32_bf16 v[200:203], v[192:195], v[44:47], v[200:203]\n\t" \
    "ds_read_b128 v[40:43], %[xp] offset:5120\n\t" \
    "s_mov_b64 exec, %[em]\n\t" \
    "buffer_store_dwordx4 v[8:11], %[l16], %[rs], %[oo] offen offset:2048 sc1\n\t" \
    "s_mov_b64 exec, -1\n\t" \
    "v_mfma_f32_16x16x32_bf16 v[204:207], v[184:187], v[48:51], v[204:207]\n\t" \
    "s_waitcnt lgkmcnt(4)\n\t" \
    "v_mfma_f32_16x16x32_bf16 v[208:211], v[192:195], v[48:51], v[208:211]\n\t" \
    "s_mov_b64 exec, %[em]\n\t" \
    "buffer_store_dwordx4 v[12:15], %[l16], %[rs], %[oo] offen offset:3072 sc1\n\t" \
    "s_mov_b64 exec, -1\n\t" \
    "s_add_u32 %[sx], %[sx], 262144\n\t" \
    "s_add_u32 %[t], %[t], 1\n\t" \
    "s_add_u32 %[oo], %[oo], 262144\n\t" \
    "s_waitcnt vmcnt(8)\n\t" \
    "s_cmp_gt_u32 %[t], " STR(SQUICK) " + 1\n\t" \
    "s_cbranch_scc1 B1_%=\n\t" \
    "s_waitcnt vmcnt(0)\n\t" \
    "B1_%=:\n\t" \
    "s_barrier\n\t" \
    "s_cmp_lt_u32 %[t], %[te]\n\t" \
    "s_cbranch_scc1 L1_%=\n\t" \
    "s_cmp_eq_u32 %[fin], %[te]\n\t" \
    "s_cbranch_scc0 E_%=\n\t" \
    "s_nop 4\n\t" \
    "s_barrier\n\t" \
    "ds_read_b128 v[52:55], %[pa] offset:0\n\t" \
    "ds_read_b128 v[56:59], %[pa] offset:1024\n\t" \
    "ds_read_b128 v[60:63], %[pa] offset:2048\n\t" \
    "ds_read_b128 v[64:67], %[pa] offset:3072\n\t" \
    "s_add_u32 %[b], %[oo], 0x1000\n\t" \
    "s_waitcnt lgkmcnt(0)\n\t" \
    "v_add_f32 v196, v196, v52\n\t" \
    "v_add_f32 v197, v197, v53\n\t" \
    "v_add_f32 v198, v198, v54\n\t" \
    "v_add_f32 v199, v199, v55\n\t" \
    "v_add_f32 v200, v200, v56\n\t" \
    "v_add_f32 v201, v201, v57\n\t" \
    "v_add_f32 v202, v202, v58\n\t" \
    "v_add_f32 v203, v203, v59\n\t" \
    "v_add_f32 v204, v204, v60\n\t" \
    "v_add_f32 v205, v205, v61\n\t" \
    "v_add_f32 v206, v206, v62\n\t" \
    "v_add_f32 v207, v207, v63\n\t" \
    "v_add_f32 v208, v208, v64\n\t" \
    "v_add_f32 v209, v209, v65\n\t" \
    "v_add_f32 v210, v210, v66\n\t" \
    "v_add_f32 v211, v211, v67\n\t" \
    "buffer_store_dwordx4 v[196:199], %[l16], %[rs], %[oo] offen offset:0 sc1\n\t" \
    "buffer_store_dwordx4 v[200:203], %[l16], %[rs], %[oo] offen offset:1024 sc1\n\t" \
    "buffer_store_dwordx4 v[204:207], %[l16], %[rs], %[oo] offen offset:2048 sc1\n\t" \
    "buffer_store_dwordx4 v[208:211], %[l16], %[rs], %[oo] offen offset:3072 sc1\n\t" \
    "E_%=:\n\t" \
    "s_setprio 0\n\t" \
    "s_waitcnt lgkmcnt(0)\n\t" \
    "s_nop 7\n\t" \
    : [t] "+s"(t), [sx] "+s"(sx), [oo] "+s"(oo), [a] "=&s"(tmp), [b] "=&s"(tmp2), [em] "=&s"(em), "+{v[20:27]}"(xf[0]), "+{v[28:35]}"(xf[1]), "+{v[36:43]}"(xf[2]), "+{v[196:203]}"(ac[0]), "+{v[204:211]}"(ac[1]) \
    : [te] "s"(TE_), [fin] "s"(FIN_), [kh] "s"(kh), [sd] "s"(sd), [rs] "s"(rs), [l16] "v"(l16), [xp] "v"(xp), [pa] "v"(pa), [fm] "s"(fm), [pm] "s"(pm), [fo] "s"(fo), [po] "s"(po), [pd] "s"(pd), [tag] "s"(P.tag), "{v[68:75]}"(w[0][0]), "{v[76:83]}"(w[0][1]), "{v[84:91]}"(w[1][0]), "{v[92:99]}"(w[1][1]), "{v[100:107]}"(w[2][0]), "{v[108:115]}"(w[2][1]), "{v[116:123]}"(w[3][0]), "{v[124:131]}"(w[3][1]), "{v[132:139]}"(w[4][0]), "{v[140:147]}"(w[4][1]), "{v[148:155]}"(w[5][0]), "{v[156:163]}"(w[5][1]), "{v[164:171]}"(w[6][0]), "{v[172:179]}"(w[6][1]), "{v[180:187]}"(w[7][0]), "{v[188:195]}"(w[7][1]) \
    : "memory", "scc", "v0", "v1", "v2", "v3", "v4", "v5", "v6", "v7", "v8", "v9", "v10", "v11", "v12", "v13", "v14", "v15", "v44", "v45", "v46", "v47", "v48", "v49", "v50", "v51", "v52", "v53", "v54", "v55", "v56", "v57", "v58", "v59", "v60", "v61", "v62", "v63", "v64", "v65", "v66", "v67")

#define ONE_X(XP_) \
  asm volatile( \
    "ds_read_b128 v[20:23], %[xp] offset:0\n\t" \
    "ds_read_b128 v[24:27], %[xp] offset:1024\n\t" \
    "ds_read_b128 v[28:31], %[xp] offset:2048\n\t" \
    "ds_read_b128 v[32:35], %[xp] offset:3072\n\t" \
    "ds_read_b128 v[36:39], %[xp] offset:4096\n\t" \
    "ds_read_b128 v[40:43], %[xp] offset:5120\n\t" \
    "s_waitcnt lgkmcnt(4)\n\t" \
    "v_mfma_f32_16x16x32_bf16 v[0:3], v[68:71], v[20:23], 0\n\t" \
    "ds_read_b128 v[44:47], %[xp] offset:6144\n\t" \
    "v_mfma_f32_16x16x32_bf16 v[4:7], v[76:79], v[20:23], 0\n\t" \
    "ds_read_b128 v[48:51], %[xp] offset:7168\n\t" \
    "v_mfma_f32_16x16x32_bf16 v[8:11], v[68:71], v[24:27], 0\n\t" \
    "s_waitcnt lgkmcnt(4)\n\t" \
    "v_mfma_f32_16x16x32_bf16 v[12:15], v[76:79], v[24:27], 0\n\t" \
    "v_mfma_f32_16x16x32_bf16 v[0:3], v[72:75], v[28:31], v[0:3]\n\t" \
    "ds_read_b128 v[20:23], %[xp] offset:8192\n\t" \
    "v_mfma_f32_16x16x32_bf16 v[4:7], v[80:83], v[28:31], v[4:7]\n\t" \
    "ds_read_b128 v[24:27], %[xp] offset:9216\n\t" \
    "v_mfma_f32_16x16x32_bf16 v[8:11], v[72:75], v[32:35], v[8:11]\n\t" \
    "s_waitcnt lgkmcnt(4)\n\t" \
    "v_mfma_f32_16x16x32_bf16 v[12:15], v[80:83], v[32:35], v[12:15]\n\t" \
    "v_mfma_f32_16x16x32_bf16 v[0:3], v[84:87], v[36:39], v[0:3]\n\t" \
    "ds_read_b128 v[28:31], %[xp] offset:10240\n\t" \
    "v_mfma_f32_16x16x32_bf16 v[4:7], v[92:95], v[36:39], v[4:7]\n\t" \
    "ds_read_b128 v[32:35], %[xp] offset:11264\n\t" \
    "v_mfma_f32_16x16x32_bf16 v[8:11], v[84:87], v[40:43], v[8:11]\n\t" \
    "s_waitcnt lgkmcnt(4)\n\t" \
    "v_mfma_f32_16x16x32_bf16 v[12:15], v[92:95], v[40:43], v[12:15]\n\t" \
    "v_mfma_f32_16x16x32_bf16 v[0:3], v[88:91], v[44:47], v[0:3]\n\t" \
    "ds_read_b128 v[36:39], %[xp] offset:12288\n\t" \
    "v_mfma_f32_16x16x32_bf16 v[4:7], v[96:99], v[44:47], v[4:7]\n\t" \
    "ds_read_b128 v[40:43], %[xp] offset:13312\n\t" \
    "v_mfma_f32_16x16x32_bf16 v[8:11], v[88:91], v[48:51], v[8:11]\n\t" \
    "s_waitcnt lgkmcnt(4)\n\t" \
    "v_mfma_f32_16x16x32_bf16 v[12:15], v[96:99], v[48:51], v[12:15]\n\t" \
    "v_mfma_f32_16x16x32_bf16 v[0:3], v[100:103], v[20:23], v[0:3]\n\t" \
    "ds_read_b128 v[44:47], %[xp] offset:14336\n\t" \
    "v_mfma_f32_16x16x32_bf16 v[4:7], v[108:111], v[20:23], v[4:7]\n\t" \
    "ds_read_b128 v[48:51], %[xp] offset:15360\n\t" \
    "v_mfma_f32_16x16x32_bf16 v[8:11], v[100:103], v[24:27], v[8:11]\n\t" \
    "s_waitcnt lgkmcnt(4)\n\t" \
    "v_mfma_f32_16x16x32_bf16 v[12:15], v[108:111], v[24:27], v[12:15]\n\t" \
    "s_waitcnt vmcnt(0)\n\t" \
    "s_barrier\n\t" \
    "v_mfma_f32_16x16x32_bf16 v[0:3], v[104:107], v[28:31], v[0:3]\n\t" \
    "ds_read_b128 v[20:23], %[xp] offset:16384\n\t" \
    "v_mfma_f32_16x16x32_bf16 v[4:7], v[112:115], v[28:31], v[4:7]\n\t" \
    "ds_read_b128 v[24:27], %[xp] offset:17408\n\t" \
    "v_mfma_f32_16x16x32_bf16 v[8:11], v[104:107], v[32:35], v[8:11]\n\t" \
    "s_waitcnt lgkmcnt(4)\n\t" \
    "v_mfma_f32_16x16x32_bf16 v[12:15], v[112:115], v[32:35], v[12:15]\n\t" \
    "buffer_load_dword v52, %[pv], %[rs], 0 offen sc1\n\t" \
    "v_mfma_f32_16x16x32_bf16 v[0:3], v[116:119], v[36:39], v[0:3]\n\t" \
    "ds_read_b128 v[28:31], %[xp] offset:18432\n\t" \
    "v_mfma_f32_16x16x32_bf16 v[4:7], v[124:127], v[36:39], v[4:7]\n\t" \
    "ds_read_b128 v[32:35], %[xp] offset:19456\n\t" \
    "v_mfma_f32_16x16x32_bf16 v[8:11], v[116:119], v[40:43], v[8:11]\n\t" \
    "s_waitcnt lgkmcnt(4)\n\t" \
    "v_mfma_f32_16x16x32_bf16 v[12:15], v[124:127], v[40:43], v[12:15]\n\t" \
    "v_mfma_f32_16x16x32_bf16 v[0:3], v[120:123], v[44:47], v[0:3]\n\t" \
    "ds_read_b128 v[36:39], %[xp] offset:20480\n\t" \
    "v_mfma_f32_16x16x32_bf16 v[4:7], v[128:131], v[44:47], v[4:7]\n\t" \
    "ds_read_b128 v[40:43], %[xp] offset:21504\n\t" \
    "v_mfma_f32_16x16x32_bf16 v[8:11], v[120:123], v[48:51], v[8:11]\n\t" \
    "s_waitcnt lgkmcnt(4)\n\t" \
    "v_mfma_f32_16x16x32_bf16 v[12:15], v[128:131], v[48:51], v[12:15]\n\t" \
    "v_mfma_f32_16x16x32_bf16 v[0:3], v[132:135], v[20:23], v[0:3]\n\t" \
    "ds_read_b128 v[44:47], %[xp] offset:22528\n\t" \
    "v_mfma_f32_16x16x32_bf16 v[4:7], v[140:143], v[20:23], v[4:7]\n\t" \
    "ds_read_b128 v[48:51], %[xp] offset:23552\n\t" \
    "v_mfma_f32_16x16x32_bf16 v[8:11], v[132:135], v[24:27], v[8:11]\n\t" \
    "s_waitcnt lgkmcnt(4)\n\t" \
    "v_mfma_f32_16x16x32_bf16 v[12:15], v[140:143], v[24:27], v[12:15]\n\t" \
    "v_mfma_f32_16x16x32_bf16 v[0:3], v[136:139], v[28:31], v[0:3]\n\t" \
    "ds_read_b128 v[20:23], %[xp] offset:24576\n\t" \
    "v_mfma_f32_16x16x32_bf16 v[4:7], v[144:147], v[28:31], v[4:7]\n\t" \
    "ds_read_b128 v[24:27], %[xp] offset:25600\n\t" \
    "v_mfma_f32_16x16x32_bf16 v[8:11], v[136:139], v[32:35], v[8:11]\n\t" \
    "s_waitcnt lgkmcnt(4)\n\t" \
    "v_mfma_f32_16x16x32_bf16 v[12:15], v[144:147], v[32:35], v[12:15]\n\t" \
    "v_mfma_f32_16x16x32_bf16 v[0:3], v[148:151], v[36:39], v[0:3]\n\t" \
    "ds_read_b128 v[28:31], %[xp] offset:26624\n\t" \
    "v_mfma_f32_16x16x32_bf16 v[4:7], v[156:159], v[36:39], v[4:7]\n\t" \
    "ds_read_b128 v[32:35], %[xp] offset:27648\n\t" \
    "v_mfma_f32_16x16x32_bf16 v[8:11], v[148:151], v[40:43], v[8:11]\n\t" \
    "s_waitcnt lgkmcnt(4)\n\t" \
    "v_mfma_f32_16x16x32_bf16 v[12:15], v[156:159], v[40:43], v[12:15]\n\t" \
    "v_mfma_f32_16x16x32_bf16 v[0:3], v[152:155], v[44:47], v[0:3]\n\t" \
    "ds_read_b128 v[36:39], %[xp] offset:28672\n\t" \
    "v_mfma_f32_16x16x32_bf16 v[4:7], v[160:163], v[44:47], v[4:7]\n\t" \
    "ds_read_b128 v[40:43], %[xp] offset:29696\n\t" \
    "v_mfma_f32_16x16x32_bf16 v[8:11], v[152:155], v[48:51], v[8:11]\n\t" \
    "s_waitcnt lgkmcnt(4)\n\t" \
    "v_mfma_f32_16x16x32_bf16 v[12:15], v[160:163], v[48:51], v[12:15]\n\t" \
    "v_mfma_f32_16x16x32_bf16 v[0:3], v[164:167], v[20:23], v[0:3]\n\t" \
    "ds_read_b128 v[44:47], %[xp] offset:30720\n\t" \
    "v_mfma_f32_16x16x32_bf16 v[4:7], v[172:175], v[20:23], v[4:7]\n\t" \
    "ds_read_b128 v[48:51], %[xp] offset:31744\n\t" \
    "v_mfma_f32_16x16x32_bf16 v[8:11], v[164:167], v[24:27], v[8:11]\n\t" \
    "s_waitcnt lgkmcnt(4)\n\t" \
    "v_mfma_f32_16x16x32_bf16 v[12:15], v[172:175], v[24:27], v[12:15]\n\t" \
    "v_mfma_f32_16x16x32_bf16 v[0:3], v[168:171], v[28:31], v[0:3]\n\t" \
    "v_mfma_f32_16x16x32_bf16 v[4:7], v[176:179], v[28:31], v[4:7]\n\t" \
    "v_mfma_f32_16x16x32_bf16 v[8:11], v[168:171], v[32:35], v[8:11]\n\t" \
    "s_waitcnt lgkmcnt(2)\n\t" \
    "v_mfma_f32_16x16x32_bf16 v[12:15], v[176:179], v[32:35], v[12:15]\n\t" \
    "v_mfma_f32_16x16x32_bf16 v[0:3], v[180:183], v[36:39], v[0:3]\n\t" \
    "v_mfma_f32_16x16x32_bf16 v[4:7], v[188:191], v[36:39], v[4:7]\n\t" \
    "v_mfma_f32_16x16x32_bf16 v[8:11], v[180:183], v[40:43], v[8:11]\n\t" \
    "s_waitcnt lgkmcnt(0)\n\t" \
    "v_mfma_f32_16x16x32_bf16 v[12:15], v[188:191], v[40:43], v[12:15]\n\t" \
    "v_mfma_f32_16x16x32_bf16 v[0:3], v[184:187], v[44:47], v[0:3]\n\t" \
    "v_mfma_f32_16x16x32_bf16 v[4:7], v[192:195], v[44:47], v[4:7]\n\t" \
    "v_mfma_f32_16x16x32_bf16 v[8:11], v[184:187], v[48:51], v[8:11]\n\t" \
    "v_mfma_f32_16x16x32_bf16 v[12:15], v[192:195], v[48:51], v[12:15]\n\t" \
    "s_waitcnt vmcnt(0)\n\t" \
    "s_nop 7\n\t" \
    : "=&{v52}"(pg), "=&{v[0:3]}"(a[0]), "=&{v[4:7]}"(a[1]), "=&{v[8:11]}"(a[2]), "=&{v[12:15]}"(a[3]) \
    : [xp] "v"(XP_), [pv] "v"(pv), [rs] "s"(rs), "{v[68:75]}"(w[0][0]), "{v[76:83]}"(w[0][1]), "{v[84:91]}"(w[1][0]), "{v[92:99]}"(w[1][1]), "{v[100:107]}"(w[2][0]), "{v[108:115]}"(w[2][1]), "{v[116:123]}"(w[3][0]), "{v[124:131]}"(w[3][1]), "{v[132:139]}"(w[4][0]), "{v[140:147]}"(w[4][1]), "{v[148:155]}"(w[5][0]), "{v[156:163]}"(w[5][1]), "{v[164:171]}"(w[6][0]), "{v[172:179]}"(w[6][1]), "{v[180:187]}"(w[7][0]), "{v[188:195]}"(w[7][1]) \
    : "memory", "v20", "v21", "v22", "v23", "v24", "v25", "v26", "v27", "v28", "v29", "v30", "v31", "v32", "v33", "v34", "v35", "v36", "v37", "v38", "v39", "v40", "v41", "v42", "v43", "v44", "v45", "v46", "v47", "v48", "v49", "v50", "v51")

#define ONE_K(XP_) \
  asm volatile( \
    "ds_read_b128 v[20:23], %[xp] offset:0\n\t" \
    "ds_read_b128 v[24:27], %[xp] offset:1024\n\t" \
    "ds_read_b128 v[28:31], %[xp] offset:2048\n\t" \
    "ds_read_b128 v[32:35], %[xp] offset:3072\n\t" \
    "ds_read_b128 v[36:39], %[xp] offset:4096\n\t" \
    "ds_read_b128 v[40:43], %[xp] offset:5120\n\t" \
    "s_waitcnt lgkmcnt(4)\n\t" \
    "v_mfma_f32_16x16x32_bf16 v[0:3], v[68:71], v[20:23], 0\n\t" \
    "ds_read_b128 v[44:47], %[xp] offset:6144\n\t" \
    "v_mfma_f32_16x16x32_bf16 v[4:7], v[76:79], v[20:23], 0\n\t" \
    "ds_read_b128 v[48:51], %[xp] offset:7168\n\t" \
    "v_mfma_f32_16x16x32_bf16 v[8:11], v[68:71], v[24:27], 0\n\t" \
    "s_waitcnt lgkmcnt(4)\n\t" \
    "v_mfma_f32_16x16x32_bf16 v[12:15], v[76:79], v[24:27], 0\n\t" \
    "v_mfma_f32_16x16x32_bf16 v[0:3], v[72:75], v[28:31], v[0:3]\n\t" \
    "ds_read_b128 v[20:23], %[xp] offset:8192\n\t" \
    "v_mfma_f32_16x16x32_bf16 v[4:7], v[80:83], v[28:31], v[4:7]\n\t" \
    "ds_read_b128 v[24:27], %[xp] offset:9216\n\t" \
    "v_mfma_f32_16x16x32_bf16 v[8:11], v[72:75], v[32:35], v[8:11]\n\t" \
    "s_waitcnt lgkmcnt(4)\n\t" \
    "v_mfma_f32_16x16x32_bf16 v[12:15], v[80:83], v[32:35], v[12:15]\n\t" \
    "v_mfma_f32_16x16x32_bf16 v[0:3], v[84:87], v[36:39], v[0:3]\n\t" \
    "ds_read_b128 v[28:31], %[xp] offset:10240\n\t" \
    "v_mfma_f32_16x16x32_bf16 v[4:7], v[92:95], v[36:39], v[4:7]\n\t" \
    "ds_read_b128 v[32:35], %[xp] offset:11264\n\t" \
    "v_mfma_f32_16x16x32_bf16 v[8:11], v[84:87], v[40:43], v[8:11]\n\t" \
    "s_waitcnt lgkmcnt(4)\n\t" \
    "v_mfma_f32_16x16x32_bf16 v[12:15], v[92:95], v[40:43], v[12:15]\n\t" \
    "v_mfma_f32_16x16x32_bf16 v[0:3], v[88:91], v[44:47], v[0:3]\n\t" \
    "ds_read_b128 v[36:39], %[xp] offset:12288\n\t" \
    "v_mfma_f32_16x16x32_bf16 v[4:7], v[96:99], v[44:47], v[4:7]\n\t" \
    "ds_read_b128 v[40:43], %[xp] offset:13312\n\t" \
    "v_mfma_f32_16x16x32_bf16 v[8:11], v[88:91], v[48:51], v[8:11]\n\t" \
    "s_waitcnt lgkmcnt(4)\n\t" \
    "v_mfma_f32_16x16x32_bf16 v[12:15], v[96:99], v[48:51], v[12:15]\n\t" \
    "v_mfma_f32_16x16x32_bf16 v[0:3], v[100:103], v[20:23], v[0:3]\n\t" \
    "ds_read_b128 v[44:47], %[xp] offset:14336\n\t" \
    "v_mfma_f32_16x16x32_bf16 v[4:7], v[108:111], v[20:23], v[4:7]\n\t" \
    "ds_read_b128 v[48:51], %[xp] offset:15360\n\t" \
    "v_mfma_f32_16x16x32_bf16 v[8:11], v[100:103], v[24:27], v[8:11]\n\t" \
    "s_waitcnt lgkmcnt(4)\n\t" \
    "v_mfma_f32_16x16x32_bf16 v[12:15], v[108:111], v[24:27], v[12:15]\n\t" \
    "s_waitcnt vmcnt(8)\n\t" \
    "s_barrier\n\t" \
    "v_mfma_f32_16x16x32_bf16 v[0:3], v[104:107], v[28:31], v[0:3]\n\t" \
    "ds_read_b128 v[20:23], %[xp] offset:16384\n\t" \
    "v_mfma_f32_16x16x32_bf16 v[4:7], v[112:115], v[28:31], v[4:7]\n\t" \
    "ds_read_b128 v[24:27], %[xp] offset:17408\n\t" \
    "v_mfma_f32_16x16x32_bf16 v[8:11], v[104:107], v[32:35], v[8:11]\n\t" \
    "s_waitcnt lgkmcnt(4)\n\t" \
    "v_mfma_f32_16x16x32_bf16 v[12:15], v[112:115], v[32:35], v[12:15]\n\t" \
    "v_mfma_f32_16x16x32_bf16 v[0:3], v[116:119], v[36:39], v[0:3]\n\t" \
    "ds_read_b128 v[28:31], %[xp] offset:18432\n\t" \
    "v_mfma_f32_16x16x32_bf16 v[4:7], v[124:127], v[36:39], v[4:7]\n\t" \
    "ds_read_b128 v[32:35], %[xp] offset:19456\n\t" \
    "v_mfma_f32_16x16x32_bf16 v[8:11], v[116:119], v[40:43], v[8:11]\n\t" \
    "s_waitcnt lgkmcnt(4)\n\t" \
    "v_mfma_f32_16x16x32_bf16 v[12:15], v[124:127], v[40:43], v[12:15]\n\t" \
    "v_mfma_f32_16x16x32_bf16 v[0:3], v[120:123], v[44:47], v[0:3]\n\t" \
    "ds_read_b128 v[36:39], %[xp] offset:20480\n\t" \
    "v_mfma_f32_16x16x32_bf16 v[4:7], v[128:131], v[44:47], v[4:7]\n\t" \
    "ds_read_b128 v[40:43], %[xp] offset:21504\n\t" \
    "v_mfma_f32_16x16x32_bf16 v[8:11], v[120:123], v[48:51], v[8:11]\n\t" \
    "s_waitcnt lgkmcnt(4)\n\t" \
    "v_mfma_f32_16x16x32_bf16 v[12:15], v[128:131], v[48:51], v[12:15]\n\t" \
    "v_mfma_f32_16x16x32_bf16 v[0:3], v[132:135], v[20:23], v[0:3]\n\t" \
    "ds_read_b128 v[44:47], %[xp] offset:22528\n\t" \
    "v_mfma_f32_16x16x32_bf16 v[4:7], v[140:143], v[20:23], v[4:7]\n\t" \
    "ds_read_b128 v[48:51], %[xp] offset:23552\n\t" \
    "v_mfma_f32_16x16x32_bf16 v[8:11], v[132:135], v[24:27], v[8:11]\n\t" \
    "s_waitcnt lgkmcnt(4)\n\t" \
    "v_mfma_f32_16x16x32_bf16 v[12:15], v[140:143], v[24:27], v[12:15]\n\t" \
    "v_mfma_f32_16x16x32_bf16 v[0:3], v[136:139], v[28:31], v[0:3]\n\t" \
    "ds_read_b128 v[20:23], %[xp] offset:24576\n\t" \
    "v_mfma_f32_16x16x32_bf16 v[4:7], v[144:147], v[28:31], v[4:7]\n\t" \
    "ds_read_b128 v[24:27], %[xp] offset:25600\n\t" \
    "v_mfma_f32_16x16x32_bf16 v[8:11], v[136:139], v[32:35], v[8:11]\n\t" \
    "s_waitcnt lgkmcnt(4)\n\t" \
    "v_mfma_f32_16x16x32_bf16 v[12:15], v[144:147], v[32:35], v[12:15]\n\t" \
    "v_mfma_f32_16x16x32_bf16 v[0:3], v[148:151], v[36:39], v[0:3]\n\t" \
    "ds_read_b128 v[28:31], %[xp] offset:26624\n\t" \
    "v_mfma_f32_16x16x32_bf16 v[4:7], v[156:159], v[36:39], v[4:7]\n\t" \
    "ds_read_b128 v[32:35], %[xp] offset:27648\n\t" \
    "v_mfma_f32_16x16x32_bf16 v[8:11], v[148:151], v[40:43], v[8:11]\n\t" \
    "s_waitcnt lgkmcnt(4)\n\t" \
    "v_mfma_f32_16x16x32_bf16 v[12:15], v[156:159], v[40:43], v[12:15]\n\t" \
    "v_mfma_f32_16x16x32_bf16 v[0:3], v[152:155], v[44:47], v[0:3]\n\t" \
    "ds_read_b128 v[36:39], %[xp] offset:28672\n\t" \
    "v_mfma_f32_16x16x32_bf16 v[4:7], v[160:163], v[44:47], v[4:7]\n\t" \
    "ds_read_b128 v[40:43], %[xp] offset:29696\n\t" \
    "v_mfma_f32_16x16x32_bf16 v[8:11], v[152:155], v[48:51], v[8:11]\n\t" \
    "s_waitcnt lgkmcnt(4)\n\t" \
    "v_mfma_f32_16x16x32_bf16 v[12:15], v[160:163], v[48:51], v[12:15]\n\t" \
    "v_mfma_f32_16x16x32_bf16 v[0:3], v[164:167], v[20:23], v[0:3]\n\t" \
    "ds_read_b128 v[44:47], %[xp] offset:30720\n\t" \
    "v_mfma_f32_16x16x32_bf16 v[4:7], v[172:175], v[20:23], v[4:7]\n\t" \
    "ds_read_b128 v[48:51], %[xp] offset:31744\n\t" \
    "v_mfma_f32_16x16x32_bf16 v[8:11], v[164:167], v[24:27], v[8:11]\n\t" \
    "s_waitcnt lgkmcnt(4)\n\t" \
    "v_mfma_f32_16x16x32_bf16 v[12:15], v[172:175], v[24:27], v[12:15]\n\t" \
    "v_mfma_f32_16x16x32_bf16 v[0:3], v[168:171], v[28:31], v[0:3]\n\t" \
    "v_mfma_f32_16x16x32_bf16 v[4:7], v[176:179], v[28:31], v[4:7]\n\t" \
    "v_mfma_f32_16x16x32_bf16 v[8:11], v[168:171], v[32:35], v[8:11]\n\t" \
    "s_waitcnt lgkmcnt(2)\n\t" \
    "v_mfma_f32_16x16x32_bf16 v[12:15], v[176:179], v[32:35], v[12:15]\n\t" \
    "v_mfma_f32_16x16x32_bf16 v[0:3], v[180:183], v[36:39], v[0:3]\n\t" \
    "v_mfma_f32_16x16x32_bf16 v[4:7], v[188:191], v[36:39], v[4:7]\n\t" \
    "v_mfma_f32_16x16x32_bf16 v[8:11], v[180:183], v[40:43], v[8:11]\n\t" \
    "s_waitcnt lgkmcnt(0)\n\t" \
    "v_mfma_f32_16x16x32_bf16 v[12:15], v[188:191], v[40:43], v[12:15]\n\t" \
    "v_mfma_f32_16x16x32_bf16 v[0:3], v[184:187], v[44:47], v[0:3]\n\t" \
    "v_mfma_f32_16x16x32_bf16 v[4:7], v[192:195], v[44:47], v[4:7]\n\t" \
    "v_mfma_f32_16x16x32_bf16 v[8:11], v[184:187], v[48:51], v[8:11]\n\t" \
    "v_mfma_f32_16x16x32_bf16 v[12:15], v[192:195], v[48:51], v[12:15]\n\t" \
    "s_nop 7\n\t" \
    : "=&{v[0:3]}"(a[0]), "=&{v[4:7]}"(a[1]), "=&{v[8:11]}"(a[2]), "=&{v[12:15]}"(a[3]) \
    : [xp] "v"(XP_), "{v[68:75]}"(w[0][0]), "{v[76:83]}"(w[0][1]), "{v[84:91]}"(w[1][0]), "{v[92:99]}"(w[1][1]), "{v[100:107]}"(w[2][0]), "{v[108:115]}"(w[2][1]), "{v[116:123]}"(w[3][0]), "{v[124:131]}"(w[3][1]), "{v[132:139]}"(w[4][0]), "{v[140:147]}"(w[4][1]), "{v[148:155]}"(w[5][0]), "{v[156:163]}"(w[5][1]), "{v[164:171]}"(w[6][0]), "{v[172:179]}"(w[6][1]), "{v[180:187]}"(w[7][0]), "{v[188:195]}"(w[7][1]) \
    : "memory", "v20", "v21", "v22", "v23", "v24", "v25", "v26", "v27", "v28", "v29", "v30", "v31", "v32", "v33", "v34", "v35", "v36", "v37", "v38", "v39", "v40", "v41", "v42", "v43", "v44", "v45", "v46", "v47", "v48", "v49", "v50", "v51")

#define ONE_V(XP_) \
  asm volatile( \
    "ds_read_b128 v[20:23], %[xp] offset:0\n\t" \
    "ds_read_b128 v[24:27], %[xp] offset:1024\n\t" \
    "ds_read_b128 v[28:31], %[xp] offset:2048\n\t" \
    "ds_read_b128 v[32:35], %[xp] offset:3072\n\t" \
    "ds_read_b128 v[36:39], %[xp] offset:4096\n\t" \
    "ds_read_b128 v[40:43], %[xp] offset:5120\n\t" \
    "s_waitcnt lgkmcnt(4)\n\t" \
    "v_mfma_f32_16x16x32_bf16 v[0:3], v[20:23], v[68:71], 0\n\t" \
    "ds_read_b128 v[44:47], %[xp] offset:6144\n\t" \
    "v_mfma_f32_16x16x32_bf16 v[4:7], v[20:23], v[76:79], 0\n\t" \
    "ds_read_b128 v[48:51], %[xp] offset:7168\n\t" \
    "v_mfma_f32_16x16x32_bf16 v[8:11], v[24:27], v[68:71], 0\n\t" \
    "s_waitcnt lgkmcnt(4)\n\t" \
    "v_mfma_f32_16x16x32_bf16 v[12:15], v[24:27], v[76:79], 0\n\t" \
    "v_mfma_f32_16x16x32_bf16 v[0:3], v[28:31], v[72:75], v[0:3]\n\t" \
    "ds_read_b128 v[20:23], %[xp] offset:8192\n\t" \
    "v_mfma_f32_16x16x32_bf16 v[4:7], v[28:31], v[80:83], v[4:7]\n\t" \
    "ds_read_b128 v[24:27], %[xp] offset:9216\n\t" \
    "v_mfma_f32_16x16x32_bf16 v[8:11], v[32:35], v[72:75], v[8:11]\n\t" \
    "s_waitcnt lgkmcnt(4)\n\t" \
    "v_mfma_f32_16x16x32_bf16 v[12:15], v[32:35], v[80:83], v[12:15]\n\t" \
    "v_mfma_f32_16x16x32_bf16 v[0:3], v[36:39], v[84:87], v[0:3]\n\t" \
    "ds_read_b128 v[28:31], %[xp] offset:10240\n\t" \
    "v_mfma_f32_16x16x32_bf16 v[4:7], v[36:39], v[92:95], v[4:7]\n\t" \
    "ds_read_b128 v[32:35], %[xp] offset:11264\n\t" \
    "v_mfma_f32_16x16x32_bf16 v[8:11], v[40:43], v[84:87], v[8:11]\n\t" \
    "s_waitcnt lgkmcnt(4)\n\t" \
    "v_mfma_f32_16x16x32_bf16 v[12:15], v[40:43], v[92:95], v[12:15]\n\t" \
    "v_mfma_f32_16x16x32_bf16 v[0:3], v[44:47], v[88:91], v[0:3]\n\t" \
    "ds_read_b128 v[36:39], %[xp] offset:12288\n\t" \
    "v_mfma_f32_16x16x32_bf16 v[4:7], v[44:47], v[96:99], v[4:7]\n\t" \
    "ds_read_b128 v[40:43], %[xp] offset:13312\n\t" \
    "v_mfma_f32_16x16x32_bf16 v[8:11], v[48:51], v[88:91], v[8:11]\n\t" \
    "s_waitcnt lgkmcnt(4)\n\t" \
    "v_mfma_f32_16x16x32_bf16 v[12:15], v[48:51], v[96:99], v[12:15]\n\t" \
    "v_mfma_f32_16x16x32_bf16 v[0:3], v[20:23], v[100:103], v[0:3]\n\t" \
    "ds_read_b128 v[44:47], %[xp] offset:14336\n\t" \
    "v_mfma_f32_16x16x32_bf16 v[4:7], v[20:23], v[108:111], v[4:7]\n\t" \
    "ds_read_b128 v[48:51], %[xp] offset:15360\n\t" \
    "v_mfma_f32_16x16x32_bf16 v[8:11], v[24:27], v[100:103], v[8:11]\n\t" \
    "s_waitcnt lgkmcnt(4)\n\t" \
    "v_mfma_f32_16x16x32_bf16 v[12:15], v[24:27], v[108:111], v[12:15]\n\t" \
    "s_waitcnt vmcnt(8)\n\t" \
    "s_barrier\n\t" \
    "v_mfma_f32_16x16x32_bf16 v[0:3], v[28:31], v[104:107], v[0:3]\n\t" \
    "ds_read_b128 v[20:23], %[xp] offset:16384\n\t" \
    "v_mfma_f32_16x16x32_bf16 v[4:7], v[28:31], v[112:115], v[4:7]\n\t" \
    "ds_read_b128 v[24:27], %[xp] offset:17408\n\t" \
    "v_mfma_f32_16x16x32_bf16 v[8:11], v[32:35], v[104:107], v[8:11]\n\t" \
    "s_waitcnt lgkmcnt(4)\n\t" \
    "v_mfma_f32_16x16x32_bf16 v[12:15], v[32:35], v[112:115], v[12:15]\n\t" \
    "v_mfma_f32_16x16x32_bf16 v[0:3], v[36:39], v[116:119], v[0:3]\n\t" \
    "ds_read_b128 v[28:31], %[xp] offset:18432\n\t" \
    "v_mfma_f32_16x16x32_bf16 v[4:7], v[36:39], v[124:127], v[4:7]\n\t" \
    "ds_read_b128 v[32:35], %[xp] offset:19456\n\t" \
    "v_mfma_f32_16x16x32_bf16 v[8:11], v[40:43], v[116:119], v[8:11]\n\t" \
    "s_waitcnt lgkmcnt(4)\n\t" \
    "v_mfma_f32_16x16x32_bf16 v[12:15], v[40:43], v[124:127], v[12:15]\n\t" \
    "v_mfma_f32_16x16x32_bf16 v[0:3], v[44:47], v[120:123], v[0:3]\n\t" \
    "ds_read_b128 v[36:39], %[xp] offset:20480\n\t" \
    "v_mfma_f32_16x16x32_bf16 v[4:7], v[44:47], v[128:131], v[4:7]\n\t" \
    "ds_read_b128 v[40:43], %[xp] offset:21504\n\t" \
    "v_mfma_f32_16x16x32_bf16 v[8:11], v[48:51], v[120:123], v[8:11]\n\t" \
    "s_waitcnt lgkmcnt(4)\n\t" \
    "v_mfma_f32_16x16x32_bf16 v[12:15], v[48:51], v[128:131], v[12:15]\n\t" \
    "v_mfma_f32_16x16x32_bf16 v[0:3], v[20:23], v[132:135], v[0:3]\n\t" \
    "ds_read_b128 v[44:47], %[xp] offset:22528\n\t" \
    "v_mfma_f32_16x16x32_bf16 v[4:7], v[20:23], v[140:143], v[4:7]\n\t" \
    "ds_read_b128 v[48:51], %[xp] offset:23552\n\t" \
    "v_mfma_f32_16x16x32_bf16 v[8:11], v[24:27], v[132:135], v[8:11]\n\t" \
    "s_waitcnt lgkmcnt(4)\n\t" \
    "v_mfma_f32_16x16x32_bf16 v[12:15], v[24:27], v[140:143], v[12:15]\n\t" \
    "v_mfma_f32_16x16x32_bf16 v[0:3], v[28:31], v[136:139], v[0:3]\n\t" \
    "ds_read_b128 v[20:23], %[xp] offset:24576\n\t" \
    "v_mfma_f32_16x16x32_bf16 v[4:7], v[28:31], v[144:147], v[4:7]\n\t" \
    "ds_read_b128 v[24:27], %[xp] offset:25600\n\t" \
    "v_mfma_f32_16x16x32_bf16 v[8:11], v[32:35], v[136:139], v[8:11]\n\t" \
    "s_waitcnt lgkmcnt(4)\n\t" \
    "v_mfma_f32_16x16x32_bf16 v[12:15], v[32:35], v[144:147], v[12:15]\n\t" \
    "v_mfma_f32_16x16x32_bf16 v[0:3], v[36:39], v[148:151], v[0:3]\n\t" \
    "ds_read_b128 v[28:31], %[xp] offset:26624\n\t" \
    "v_mfma_f32_16x16x32_bf16 v[4:7], v[36:39], v[156:159], v[4:7]\n\t" \
    "ds_read_b128 v[32:35], %[xp] offset:27648\n\t" \
    "v_mfma_f32_16x16x32_bf16 v[8:11], v[40:43], v[148:151], v[8:11]\n\t" \
    "s_waitcnt lgkmcnt(4)\n\t" \
    "v_mfma_f32_16x16x32_bf16 v[12:15], v[40:43], v[156:159], v[12:15]\n\t" \
    "v_mfma_f32_16x16x32_bf16 v[0:3], v[44:47], v[152:155], v[0:3]\n\t" \
    "ds_read_b128 v[36:39], %[xp] offset:28672\n\t" \
    "v_mfma_f32_16x16x32_bf16 v[4:7], v[44:47], v[160:163], v[4:7]\n\t" \
    "ds_read_b128 v[40:43], %[xp] offset:29696\n\t" \
    "v_mfma_f32_16x16x32_bf16 v[8:11], v[48:51], v[152:155], v[8:11]\n\t" \
    "s_waitcnt lgkmcnt(4)\n\t" \
    "v_mfma_f32_16x16x32_bf16 v[12:15], v[48:51], v[160:163], v[12:15]\n\t" \
    "v_mfma_f32_16x16x32_bf16 v[0:3], v[20:23], v[164:167], v[0:3]\n\t" \
    "ds_read_b128 v[44:47], %[xp] offset:30720\n\t" \
    "v_mfma_f32_16x16x32_bf16 v[4:7], v[20:23], v[172:175], v[4:7]\n\t" \
    "ds_read_b128 v[48:51], %[xp] offset:31744\n\t" \
    "v_mfma_f32_16x16x32_bf16 v[8:11], v[24:27], v[164:167], v[8:11]\n\t" \
    "s_waitcnt lgkmcnt(4)\n\t" \
    "v_mfma_f32_16x16x32_bf16 v[12:15], v[24:27], v[172:175], v[12:15]\n\t" \
    "v_mfma_f32_16x16x32_bf16 v[0:3], v[28:31], v[168:171], v[0:3]\n\t" \
    "v_mfma_f32_16x16x32_bf16 v[4:7], v[28:31], v[176:179], v[4:7]\n\t" \
    "v_mfma_f32_16x16x32_bf16 v[8:11], v[32:35], v[168:171], v[8:11]\n\t" \
    "s_waitcnt lgkmcnt(2)\n\t" \
    "v_mfma_f32_16x16x32_bf16 v[12:15], v[32:35], v[176:179], v[12:15]\n\t" \
    "v_mfma_f32_16x16x32_bf16 v[0:3], v[36:39], v[180:183], v[0:3]\n\t" \
    "v_mfma_f32_16x16x32_bf16 v[4:7], v[36:39], v[188:191], v[4:7]\n\t" \
    "v_mfma_f32_16x16x32_bf16 v[8:11], v[40:43], v[180:183], v[8:11]\n\t" \
    "s_waitcnt lgkmcnt(0)\n\t" \
    "v_mfma_f32_16x16x32_bf16 v[12:15], v[40:43], v[188:191], v[12:15]\n\t" \
    "v_mfma_f32_16x16x32_bf16 v[0:3], v[44:47], v[184:187], v[0:3]\n\t" \
    "v_mfma_f32_16x16x32_bf16 v[4:7], v[44:47], v[192:195], v[4:7]\n\t" \
    "v_mfma_f32_16x16x32_bf16 v[8:11], v[48:51], v[184:187], v[8:11]\n\t" \
    "v_mfma_f32_16x16x32_bf16 v[12:15], v[48:51], v[192:195], v[12:15]\n\t" \
    "s_nop 7\n\t" \
    : "=&{v[0:3]}"(a[0]), "=&{v[4:7]}"(a[1]), "=&{v[8:11]}"(a[2]), "=&{v[12:15]}"(a[3]) \
    : [xp] "v"(XP_), "{v[68:75]}"(w[0][0]), "{v[76:83]}"(w[0][1]), "{v[84:91]}"(w[1][0]), "{v[92:99]}"(w[1][1]), "{v[100:107]}"(w[2][0]), "{v[108:115]}"(w[2][1]), "{v[116:123]}"(w[3][0]), "{v[124:131]}"(w[3][1]), "{v[132:139]}"(w[4][0]), "{v[140:147]}"(w[4][1]), "{v[148:155]}"(w[5][0]), "{v[156:163]}"(w[5][1]), "{v[164:171]}"(w[6][0]), "{v[172:179]}"(w[6][1]), "{v[180:187]}"(w[7][0]), "{v[188:195]}"(w[7][1]) \
    : "memory", "v20", "v21", "v22", "v23", "v24", "v25", "v26", "v27", "v28", "v29", "v30", "v31", "v32", "v33", "v34", "v35", "v36", "v37", "v38", "v39", "v40", "v41", "v42", "v43", "v44", "v45", "v46", "v47", "v48", "v49", "v50", "v51")

#define H_LOOP1() \
  asm volatile( \
    "v_mov_b32 v20, 0\n\t" \
    "v_mov_b32 v21, 0\n\t" \
    "v_mov_b32 v22, 0\n\t" \
    "v_mov_b32 v23, 0\n\t" \
    "v_mov_b32 v24, 0\n\t" \
    "v_mov_b32 v25, 0\n\t" \
    "v_mov_b32 v26, 0\n\t" \
    "v_mov_b32 v27, 0\n\t" \
    "v_mov_b32 v28, 0\n\t" \
    "v_mov_b32 v29, 0\n\t" \
    "v_mov_b32 v30, 0\n\t" \
    "v_mov_b32 v31, 0\n\t" \
    "v_mov_b32 v32, 0\n\t" \
    "v_mov_b32 v33, 0\n\t" \
    "v_mov_b32 v34, 0\n\t" \
    "v_mov_b32 v35, 0\n\t" \
    "v_mov_b32 v36, 0\n\t" \
    "v_mov_b32 v37, 0\n\t" \
    "v_mov_b32 v38, 0\n\t" \
    "v_mov_b32 v39, 0\n\t" \
    "v_mov_b32 v40, 0\n\t" \
    "v_mov_b32 v41, 0\n\t" \
    "v_mov_b32 v42, 0\n\t" \
    "v_mov_b32 v43, 0\n\t" \
    "v_mov_b32 v44, 0\n\t" \
    "v_mov_b32 v45, 0\n\t" \
    "v_mov_b32 v46, 0\n\t" \
    "v_mov_b32 v47, 0\n\t" \
    "v_mov_b32 v48, 0\n\t" \
    "v_mov_b32 v49, 0\n\t" \
    "v_mov_b32 v50, 0\n\t" \
    "v_mov_b32 v51, 0\n\t" \
    "v_mov_b32 v52, 0\n\t" \
    "v_mov_b32 v53, 0\n\t" \
    "v_mov_b32 v54, 0\n\t" \
    "v_mov_b32 v55, 0\n\t" \
    "v_mov_b32 v56, 0\n\t" \
    "v_mov_b32 v57, 0\n\t" \
    "v_mov_b32 v58, 0\n\t" \
    "v_mov_b32 v59, 0\n\t" \
    "v_mov_b32 v60, 0\n\t" \
    "v_mov_b32 v61, 0\n\t" \
    "v_mov_b32 v62, 0\n\t" \
    "v_mov_b32 v63, 0\n\t" \
    "v_mov_b32 v64, 0\n\t" \
    "v_mov_b32 v65, 0\n\t" \
    "v_mov_b32 v66, 0\n\t" \
    "v_mov_b32 v67, 0\n\t" \
    "v_mov_b32 v68, 0\n\t" \
    "v_mov_b32 v69, 0\n\t" \
    "v_mov_b32 v70, 0\n\t" \
    "v_mov_b32 v71, 0\n\t" \
    "v_mov_b32 v72, 0\n\t" \
    "v_mov_b32 v73, 0\n\t" \
    "v_mov_b32 v74, 0\n\t" \
    "v_mov_b32 v75, 0\n\t" \
    "v_mov_b32 v76, 0\n\t" \
    "v_mov_b32 v77, 0\n\t" \
    "v_mov_b32 v78, 0\n\t" \
    "v_mov_b32 v79, 0\n\t" \
    "v_mov_b32 v80, 0\n\t" \
    "v_mov_b32 v81, 0\n\t" \
    "v_mov_b32 v82, 0\n\t" \
    "v_mov_b32 v83, 0\n\t" \
    "s_waitcnt vmcnt(8)\n\t" \
    "s_barrier\n\t" \
    "ds_read_b64_tr_b16 v[116:117], %[b0] offset:0\n\t" \
    "ds_read_b64_tr_b16 v[118:119], %[b0] offset:1024\n\t" \
    "ds_read_b64_tr_b16 v[124:125], %[b1] offset:0\n\t" \
    "ds_read_b64_tr_b16 v[126:127], %[b1] offset:1024\n\t" \
    "ds_read_b64_tr_b16 v[120:121], %[b0] offset:8192\n\t" \
    "ds_read_b64_tr_b16 v[122:123], %[b0] offset:9216\n\t" \
    "ds_read_b64_tr_b16 v[128:129], %[b1] offset:8192\n\t" \
    "ds_read_b64_tr_b16 v[130:131], %[b1] offset:9216\n\t" \
    "ds_read_b128 v[84:87], %[a] offset:0\n\t" \
    "ds_read_b128 v[88:91], %[a] offset:1024\n\t" \
    "ds_read_b128 v[92:95], %[a] offset:2048\n\t" \
    "ds_read_b128 v[96:99], %[a] offset:3072\n\t" \
    "ds_read_b128 v[100:103], %[a] offset:4096\n\t" \
    "ds_read_b128 v[104:107], %[a] offset:5120\n\t" \
    "ds_read_b128 v[108:111], %[a] offset:6144\n\t" \
    "ds_read_b128 v[112:115], %[a] offset:7168\n\t" \
    "s_add_u32 %[d], %[lw], %[c]\n\t" \
    "s_add_u32 %[y], %[c], 49152\n\t" \
    "s_cmp_eq_u32 %[y], 147456\n\t" \
    "s_cselect_b32 %[y], 0, %[y]\n\t" \
    "s_sub_u32 %[z], %[y], %[c]\n\t" \
    "s_mov_b32 %[c], %[y]\n\t" \
    "s_add_u32 %[x], %[t], 3\n\t" \
    "s_sub_u32 %[y], %[x], %[nt]\n\t" \
    "s_cmp_lt_u32 %[x], %[nt]\n\t" \
    "s_cselect_b32 %[x], %[x], %[y]\n\t" \
    "s_cselect_b32 m0, %[cb], %[cn]\n\t" \
    "s_mul_i32 %[y], %[x], %[kb]\n\t" \
    "s_add_u32 %[y], %[y], m0\n\t" \
    "s_lshl_b32 %[x], %[x], 11\n\t" \
    "s_add_u32 %[x], %[x], %[ca]\n\t" \
    "v_add_u32 %[a], %[z], %[a]\n\t" \
    "v_add_u32 %[b0], %[z], %[b0]\n\t" \
    "v_add_u32 %[b1], %[z], %[b1]\n\t" \
    "L_%=:\n\t" \
    "s_waitcnt vmcnt(4) lgkmcnt(0)\n\t" \
    "s_barrier\n\t" \
    "v_mfma_f32_16x16x32_bf16 v[20:23], v[116:119], v[84:87], v[20:23]\n\t" \
    "ds_read_b64_tr_b16 v[180:181], %[b0] offset:0\n\t" \
    "ds_read_b64_tr_b16 v[182:183], %[b0] offset:1024\n\t" \
    "v_mfma_f32_16x16x32_bf16 v[24:27], v[124:127], v[84:87], v[24:27]\n\t" \
    "ds_read_b64_tr_b16 v[188:189], %[b1] offset:0\n\t" \
    "ds_read_b64_tr_b16 v[190:191], %[b1] offset:1024\n\t" \
    "s_add_u32 m0, %[d], 0\n\t" \
    "v_mfma_f32_16x16x32_bf16 v[20:23], v[120:123], v[88:91], v[20:23]\n\t" \
    "buffer_load_dwordx4 %[vb0], %[rb], %[y] offen lds\n\t" \
    "v_mfma_f32_16x16x32_bf16 v[24:27], v[128:131], v[88:91], v[24:27]\n\t" \
    "ds_read_b64_tr_b16 v[184:185], %[b0] offset:8192\n\t" \
    "ds_read_b64_tr_b16 v[186:187], %[b0] offset:9216\n\t" \
    "s_add_u32 m0, %[d], 8192\n\t" \
    "v_mfma_f32_16x16x32_bf16 v[28:31], v[116:119], v[92:95], v[28:31]\n\t" \
    "buffer_load_dwordx4 %[vb1], %[rb], %[y] offen lds\n\t" \
    "v_mfma_f32_16x16x32_bf16 v[32:35], v[124:127], v[92:95], v[32:35]\n\t" \
    "ds_read_b64_tr_b16 v[192:193], %[b1] offset:8192\n\t" \
    "ds_read_b64_tr_b16 v[194:195], %[b1] offset:9216\n\t" \
    "s_add_u32 m0, %[d], 32768\n\t" \
    "v_mfma_f32_16x16x32_bf16 v[28:31], v[120:123], v[96:99], v[28:31]\n\t" \
    "buffer_load_dwordx4 %[va0], %[ra], %[x] offen sc1 lds\n\t" \
    "v_mfma_f32_16x16x32_bf16 v[32:35], v[128:131], v[96:99], v[32:35]\n\t" \
    "ds_read_b128 v[148:151], %[a] offset:0\n\t" \
    "s_add_u32 m0, %[d], 40960\n\t" \
    "v_mfma_f32_16x16x32_bf16 v[36:39], v[116:119], v[100:103], v[36:39]\n\t" \
    "buffer_load_dwordx4 %[va1], %[ra], %[x] offen sc1 lds\n\t" \
    "v_mfma_f32_16x16x32_bf16 v[40:43], v[124:127], v[100:103], v[40:43]\n\t" \
    "ds_read_b128 v[152:155], %[a] offset:1024\n\t" \
    "ds_read_b128 v[156:159], %[a] offset:2048\n\t" \
    "s_add_u32 %[t], %[t], 1\n\t" \
    "s_add_u32 %[d], %[lw], %[c]\n\t" \
    "s_add_u32 %[y], %[c], 49152\n\t" \
    "v_mfma_f32_16x16x32_bf16 v[36:39], v[120:123], v[104:107], v[36:39]\n\t" \
    "ds_read_b128 v[160:163], %[a] offset:3072\n\t" \
    "ds_read_b128 v[164:167], %[a] offset:4096\n\t" \
    "s_cmp_eq_u32 %[y], 147456\n\t" \
    "s_cselect_b32 %[y], 0, %[y]\n\t" \
    "s_sub_u32 %[z], %[y], %[c]\n\t" \
    "v_mfma_f32_16x16x32_bf16 v[40:43], v[128:131], v[104:107], v[40:43]\n\t" \
    "ds_read_b128 v[168:171], %[a] offset:5120\n\t" \
    "ds_read_b128 v[172:175], %[a] offset:6144\n\t" \
    "s_mov_b32 %[c], %[y]\n\t" \
    "s_add_u32 %[x], %[t], 3\n\t" \
    "s_sub_u32 %[y], %[x], %[nt]\n\t" \
    "v_mfma_f32_16x16x32_bf16 v[44:47], v[116:119], v[108:111], v[44:47]\n\t" \
    "ds_read_b128 v[176:179], %[a] offset:7168\n\t" \
    "s_cmp_lt_u32 %[x], %[nt]\n\t" \
    "s_cselect_b32 %[x], %[x], %[y]\n\t" \
    "s_cselect_b32 m0, %[cb], %[cn]\n\t" \
    "v_mfma_f32_16x16x32_bf16 v[48:51], v[124:127], v[108:111], v[48:51]\n\t" \
    "s_mul_i32 %[y], %[x], %[kb]\n\t" \
    "s_add_u32 %[y], %[y], m0\n\t" \
    "s_lshl_b32 %[x], %[x], 11\n\t" \
    "v_mfma_f32_16x16x32_bf16 v[44:47], v[120:123], v[112:115], v[44:47]\n\t" \
    "s_add_u32 %[x], %[x], %[ca]\n\t" \
    "v_mfma_f32_16x16x32_bf16 v[48:51], v[128:131], v[112:115], v[48:51]\n\t" \
    "v_add_u32 %[a], %[z], %[a]\n\t" \
    "v_add_u32 %[b0], %[z], %[b0]\n\t" \
    "v_add_u32 %[b1], %[z], %[b1]\n\t" \
    "s_waitcnt vmcnt(4) lgkmcnt(0)\n\t" \
    "s_barrier\n\t" \
    "v_mfma_f32_16x16x32_bf16 v[20:23], v[180:183], v[148:151], v[20:23]\n\t" \
    "ds_read_b64_tr_b16 v[116:117], %[b0] offset:0\n\t" \
    "ds_read_b64_tr_b16 v[118:119], %[b0] offset:1024\n\t" \
    "v_mfma_f32_16x16x32_bf16 v[24:27], v[188:191], v[148:151], v[24:27]\n\t" \
    "ds_read_b64_tr_b16 v[124:125], %[b1] offset:0\n\t" \
    "ds_read_b64_tr_b16 v[126:127], %[b1] offset:1024\n\t" \
    "s_add_u32 m0, %[d], 0\n\t" \
    "v_mfma_f32_16x16x32_bf16 v[20:23], v[184:187], v[152:155], v[20:23]\n\t" \
    "buffer_load_dwordx4 %[vb0], %[rb], %[y] offen lds\n\t" \
    "v_mfma_f32_16x16x32_bf16 v[24:27], v[192:195], v[152:155], v[24:27]\n\t" \
    "ds_read_b64_tr_b16 v[120:121], %[b0] offset:8192\n\t" \
    "ds_read_b64_tr_b16 v[122:123], %[b0] offset:9216\n\t" \
    "s_add_u32 m0, %[d], 8192\n\t" \
    "v_mfma_f32_16x16x32_bf16 v[28:31], v[180:183], v[156:159], v[28:31]\n\t" \
    "buffer_load_dwordx4 %[vb1], %[rb], %[y] offen lds\n\t" \
    "v_mfma_f32_16x16x32_bf16 v[32:35], v[188:191], v[156:159], v[32:35]\n\t" \
    "ds_read_b64_tr_b16 v[128:129], %[b1] offset:8192\n\t" \
    "ds_read_b64_tr_b16 v[130:131], %[b1] offset:9216\n\t" \
    "s_add_u32 m0, %[d], 32768\n\t" \
    "v_mfma_f32_16x16x32_bf16 v[28:31], v[184:187], v[160:163], v[28:31]\n\t" \
    "buffer_load_dwordx4 %[va0], %[ra], %[x] offen sc1 lds\n\t" \
    "v_mfma_f32_16x16x32_bf16 v[32:35], v[192:195], v[160:163], v[32:35]\n\t" \
    "ds_read_b128 v[84:87], %[a] offset:0\n\t" \
    "s_add_u32 m0, %[d], 40960\n\t" \
    "v_mfma_f32_16x16x32_bf16 v[36:39], v[180:183], v[164:167], v[36:39]\n\t" \
    "buffer_load_dwordx4 %[va1], %[ra], %[x] offen sc1 lds\n\t" \
    "v_mfma_f32_16x16x32_bf16 v[40:43], v[188:191], v[164:167], v[40:43]\n\t" \
    "ds_read_b128 v[88:91], %[a] offset:1024\n\t" \
    "ds_read_b128 v[92:95], %[a] offset:2048\n\t" \
    "s_add_u32 %[t], %[t], 1\n\t" \
    "s_add_u32 %[d], %[lw], %[c]\n\t" \
    "s_add_u32 %[y], %[c], 49152\n\t" \
    "v_mfma_f32_16x16x32_bf16 v[36:39], v[184:187], v[168:171], v[36:39]\n\t" \
    "ds_read_b128 v[96:99], %[a] offset:3072\n\t" \
    "ds_read_b128 v[100:103], %[a] offset:4096\n\t" \
    "s_cmp_eq_u32 %[y], 147456\n\t" \
    "s_cselect_b32 %[y], 0, %[y]\n\t" \
    "s_sub_u32 %[z], %[y], %[c]\n\t" \
    "v_mfma_f32_16x16x32_bf16 v[40:43], v[192:195], v[168:171], v[40:43]\n\t" \
    "ds_read_b128 v[104:107], %[a] offset:5120\n\t" \
    "ds_read_b128 v[108:111], %[a] offset:6144\n\t" \
    "s_mov_b32 %[c], %[y]\n\t" \
    "s_add_u32 %[x], %[t], 3\n\t" \
    "s_sub_u32 %[y], %[x], %[nt]\n\t" \
    "v_mfma_f32_16x16x32_bf16 v[44:47], v[180:183], v[172:175], v[44:47]\n\t" \
    "ds_read_b128 v[112:115], %[a] offset:7168\n\t" \
    "s_cmp_lt_u32 %[x], %[nt]\n\t" \
    "s_cselect_b32 %[x], %[x], %[y]\n\t" \
    "s_cselect_b32 m0, %[cb], %[cn]\n\t" \
    "v_mfma_f32_16x16x32_bf16 v[48:51], v[188:191], v[172:175], v[48:51]\n\t" \
    "s_mul_i32 %[y], %[x], %[kb]\n\t" \
    "s_add_u32 %[y], %[y], m0\n\t" \
    "s_lshl_b32 %[x], %[x], 11\n\t" \
    "v_mfma_f32_16x16x32_bf16 v[44:47], v[184:187], v[176:179], v[44:47]\n\t" \
    "s_add_u32 %[x], %[x], %[ca]\n\t" \
    "s_cmp_lt_u32 %[t], %[nt]\n\t" \
    "v_mfma_f32_16x16x32_bf16 v[48:51], v[192:195], v[176:179], v[48:51]\n\t" \
    "v_add_u32 %[a], %[z], %[a]\n\t" \
    "v_add_u32 %[b0], %[z], %[b0]\n\t" \
    "v_add_u32 %[b1], %[z], %[b1]\n\t" \
    "s_cbranch_scc1 L_%=\n\t" \
    "s_waitcnt lgkmcnt(0)\n\t" \
    "s_nop 7\n\t" \
    : "=&{v[20:23]}"(acc[0][0][0][0]), "=&{v[24:27]}"(acc[0][0][0][1]), "=&{v[28:31]}"(acc[0][0][1][0]), "=&{v[32:35]}"(acc[0][0][1][1]), "=&{v[36:39]}"(acc[0][0][2][0]), "=&{v[40:43]}"(acc[0][0][2][1]), "=&{v[44:47]}"(acc[0][0][3][0]), "=&{v[48:51]}"(acc[0][0][3][1]), "=&{v[52:55]}"(acc[0][1][0][0]), "=&{v[56:59]}"(acc[0][1][0][1]), "=&{v[60:63]}"(acc[0][1][1][0]), "=&{v[64:67]}"(acc[0][1][1][1]), "=&{v[68:71]}"(acc[0][1][2][0]), "=&{v[72:75]}"(acc[0][1][2][1]), "=&{v[76:79]}"(acc[0][1][3][0]), "=&{v[80:83]}"(acc[0][1][3][1]), [a] "+v"(aoff), [b0] "+v"(boff0), [b1] "+v"(boff1), [c] "+s"(cur), [t] "+s"(t), [d] "=&s"(sd), [x] "=&s"(sx), [y] "=&s"(sy), [z] "=&s"(sz) \
    : [va0] "v"(voffA[0]), [va1] "v"(voffA[1]), [vb0] "v"(voffB[0]), [vb1] "v"(voffB[1]), [ra] "s"(ra), [rb] "s"(rb), [lw] "s"(ldsw), [nt] "s"(nt), [kb] "s"(kB), [ca] "s"(cA), [cb] "s"(cB), [cn] "s"(cN) \
    : "memory", "scc", "v84", "v85", "v86", "v87", "v88", "v89", "v90", "v91", "v92", "v93", "v94", "v95", "v96", "v97", "v98", "v99", "v100", "v101", "v102", "v103", "v104", "v105", "v106", "v107", "v108", "v109", "v110", "v111", "v112", "v113", "v114", "v115", "v116", "v117", "v118", "v119", "v120", "v121", "v122", "v123", "v124", "v125", "v126", "v127", "v128", "v129", "v130", "v131", "v132", "v133", "v134", "v135", "v136", "v137", "v138", "v139", "v140", "v141", "v142", "v143", "v144", "v145", "v146", "v147", "v148", "v149", "v150", "v151", "v152", "v153", "v154", "v155", "v156", "v157", "v158", "v159", "v160", "v161", "v162", "v163", "v164", "v165", "v166", "v167", "v168", "v169", "v170", "v171", "v172", "v173", "v174", "v175", "v176", "v177", "v178", "v179", "v180", "v181", "v182", "v183", "v184", "v185", "v186", "v187", "v188", "v189", "v190", "v191", "v192", "v193", "v194", "v195", "v196", "v197", "v198", "v199", "v200", "v201", "v202", "v203", "v204", "v205", "v206", "v207", "v208", "v209", "v210", "v211")

#define H_LOOP2() \
  asm volatile( \
    "v_mov_b32 v20, 0\n\t" \
    "v_mov_b32 v21, 0\n\t" \
    "v_mov_b32 v22, 0\n\t" \
    "v_mov_b32 v23, 0\n\t" \
    "v_mov_b32 v24, 0\n\t" \
    "v_mov_b32 v25, 0\n\t" \
    "v_mov_b32 v26, 0\n\t" \
    "v_mov_b32 v27, 0\n\t" \
    "v_mov_b32 v28, 0\n\t" \
    "v_mov_b32 v29, 0\n\t" \
    "v_mov_b32 v30, 0\n\t" \
    "v_mov_b32 v31, 0\n\t" \
    "v_mov_b32 v32, 0\n\t" \
    "v_mov_b32 v33, 0\n\t" \
    "v_mov_b32 v34, 0\n\t" \
    "v_mov_b32 v35, 0\n\t" \
    "v_mov_b32 v36, 0\n\t" \
    "v_mov_b32 v37, 0\n\t" \
    "v_mov_b32 v38, 0\n\t" \
    "v_mov_b32 v39, 0\n\t" \
    "v_mov_b32 v40, 0\n\t" \
    "v_mov_b32 v41, 0\n\t" \
    "v_mov_b32 v42, 0\n\t" \
    "v_mov_b32 v43, 0\n\t" \
    "v_mov_b32 v44, 0\n\t" \
    "v_mov_b32 v45, 0\n\t" \
    "v_mov_b32 v46, 0\n\t" \
    "v_mov_b32 v47, 0\n\t" \
    "v_mov_b32 v48, 0\n\t" \
    "v_mov_b32 v49, 0\n\t" \
    "v_mov_b32 v50, 0\n\t" \
    "v_mov_b32 v51, 0\n\t" \
    "v_mov_b32 v52, 0\n\t" \
    "v_mov_b32 v53, 0\n\t" \
    "v_mov_b32 v54, 0\n\t" \
    "v_mov_b32 v55, 0\n\t" \
    "v_mov_b32 v56, 0\n\t" \
    "v_mov_b32 v57, 0\n\t" \
    "v_mov_b32 v58, 0\n\t" \
    "v_mov_b32 v59, 0\n\t" \
    "v_mov_b32 v60, 0\n\t" \
    "v_mov_b32 v61, 0\n\t" \
    "v_mov_b32 v62, 0\n\t" \
    "v_mov_b32 v63, 0\n\t" \
    "v_mov_b32 v64, 0\n\t" \
    "v_mov_b32 v65, 0\n\t" \
    "v_mov_b32 v66, 0\n\t" \
    "v_mov_b32 v67, 0\n\t" \
    "v_mov_b32 v68, 0\n\t" \
    "v_mov_b32 v69, 0\n\t" \
    "v_mov_b32 v70, 0\n\t" \
    "v_mov_b32 v71, 0\n\t" \
    "v_mov_b32 v72, 0\n\t" \
    "v_mov_b32 v73, 0\n\t" \
    "v_mov_b32 v74, 0\n\t" \
    "v_mov_b32 v75, 0\n\t" \
    "v_mov_b32 v76, 0\n\t" \
    "v_mov_b32 v77, 0\n\t" \
    "v_mov_b32 v78, 0\n\t" \
    "v_mov_b32 v79, 0\n\t" \
    "v_mov_b32 v80, 0\n\t" \
    "v_mov_b32 v81, 0\n\t" \
    "v_mov_b32 v82, 0\n\t" \
    "v_mov_b32 v83, 0\n\t" \
    "s_waitcnt vmcnt(12)\n\t" \
    "s_barrier\n\t" \
    "ds_read_b64_tr_b16 v[116:117], %[b0] offset:0\n\t" \
    "ds_read_b64_tr_b16 v[118:119], %[b0] offset:1024\n\t" \
    "ds_read_b64_tr_b16 v[124:125], %[b1] offset:0\n\t" \
    "ds_read_b64_tr_b16 v[126:127], %[b1] offset:1024\n\t" \
    "ds_read_b64_tr_b16 v[120:121], %[b0] offset:8192\n\t" \
    "ds_read_b64_tr_b16 v[122:123], %[b0] offset:9216\n\t" \
    "ds_read_b64_tr_b16 v[128:129], %[b1] offset:8192\n\t" \
    "ds_read_b64_tr_b16 v[130:131], %[b1] offset:9216\n\t" \
    "ds_read_b128 v[84:87], %[a] offset:0\n\t" \
    "ds_read_b128 v[88:91], %[a] offset:1024\n\t" \
    "ds_read_b128 v[92:95], %[a] offset:2048\n\t" \
    "ds_read_b128 v[96:99], %[a] offset:3072\n\t" \
    "ds_read_b128 v[100:103], %[a] offset:4096\n\t" \
    "ds_read_b128 v[104:107], %[a] offset:5120\n\t" \
    "ds_read_b128 v[108:111], %[a] offset:6144\n\t" \
    "ds_read_b128 v[112:115], %[a] offset:7168\n\t" \
    "ds_read_b64_tr_b16 v[132:133], %[b0] offset:16384\n\t" \
    "ds_read_b64_tr_b16 v[134:135], %[b0] offset:17408\n\t" \
    "ds_read_b64_tr_b16 v[140:141], %[b1] offset:16384\n\t" \
    "ds_read_b64_tr_b16 v[142:143], %[b1] offset:17408\n\t" \
    "ds_read_b64_tr_b16 v[136:137], %[b0] offset:24576\n\t" \
    "ds_read_b64_tr_b16 v[138:139], %[b0] offset:25600\n\t" \
    "ds_read_b64_tr_b16 v[144:145], %[b1] offset:24576\n\t" \
    "ds_read_b64_tr_b16 v[146:147], %[b1] offset:25600\n\t" \
    "s_add_u32 %[d], %[lw], %[c]\n\t" \
    "s_add_u32 %[y], %[c], 49152\n\t" \
    "s_cmp_eq_u32 %[y], 147456\n\t" \
    "s_cselect_b32 %[y], 0, %[y]\n\t" \
    "s_sub_u32 %[z], %[y], %[c]\n\t" \
    "s_mov_b32 %[c], %[y]\n\t" \
    "s_add_u32 %[x], %[t], 3\n\t" \
    "s_sub_u32 %[y], %[x], %[nt]\n\t" \
    "s_cmp_lt_u32 %[x], %[nt]\n\t" \
    "s_cselect_b32 %[x], %[x], %[y]\n\t" \
    "s_cselect_b32 m0, %[cb], %[cn]\n\t" \
    "s_mul_i32 %[y], %[x], %[kb]\n\t" \
    "s_add_u32 %[y], %[y], m0\n\t" \
    "s_lshl_b32 %[x], %[x], 11\n\t" \
    "s_add_u32 %[x], %[x], %[ca]\n\t" \
    "v_add_u32 %[a], %[z], %[a]\n\t" \
    "v_add_u32 %[b0], %[z], %[b0]\n\t" \
    "v_add_u32 %[b1], %[z], %[b1]\n\t" \
    "L_%=:\n\t" \
    "s_waitcnt vmcnt(6) lgkmcnt(0)\n\t" \
    "s_barrier\n\t" \
    "v_mfma_f32_16x16x32_bf16 v[20:23], v[116:119], v[84:87], v[20:23]\n\t" \
    "ds_read_b64_tr_b16 v[180:181], %[b0] offset:0\n\t" \
    "ds_read_b64_tr_b16 v[182:183], %[b0] offset:1024\n\t" \
    "v_mfma_f32_16x16x32_bf16 v[24:27], v[124:127], v[84:87], v[24:27]\n\t" \
    "ds_read_b64_tr_b16 v[188:189], %[b1] offset:0\n\t" \
    "ds_read_b64_tr_b16 v[190:191], %[b1] offset:1024\n\t" \
    "v_mfma_f32_16x16x32_bf16 v[20:23], v[120:123], v[88:91], v[20:23]\n\t" \
    "ds_read_b64_tr_b16 v[184:185], %[b0] offset:8192\n\t" \
    "ds_read_b64_tr_b16 v[186:187], %[b0] offset:9216\n\t" \
    "s_add_u32 m0, %[d], 0\n\t" \
    "v_mfma_f32_16x16x32_bf16 v[24:27], v[128:131], v[88:91], v[24:27]\n\t" \
    "buffer_load_dwordx4 %[vb0], %[rb], %[y] offen lds\n\t" \
    "v_mfma_f32_16x16x32_bf16 v[28:31], v[116:119], v[92:95], v[28:31]\n\t" \
    "ds_read_b64_tr_b16 v[192:193], %[b1] offset:8192\n\t" \
    "v_mfma_f32_16x16x32_bf16 v[32:35], v[124:127], v[92:95], v[32:35]\n\t" \
    "ds_read_b64_tr_b16 v[194:195], %[b1] offset:9216\n\t" \
    "v_mfma_f32_16x16x32_bf16 v[28:31], v[120:123], v[96:99], v[28:31]\n\t" \
    "ds_read_b128 v[148:151], %[a] offset:0\n\t" \
    "s_add_u32 m0, %[d], 8192\n\t" \
    "v_mfma_f32_16x16x32_bf16 v[32:35], v[128:131], v[96:99], v[32:35]\n\t" \
    "buffer_load_dwordx4 %[vb1], %[rb], %[y] offen lds\n\t" \
    "v_mfma_f32_16x16x32_bf16 v[36:39], v[116:119], v[100:103], v[36:39]\n\t" \
    "ds_read_b128 v[152:155], %[a] offset:1024\n\t" \
    "v_mfma_f32_16x16x32_bf16 v[40:43], v[124:127], v[100:103], v[40:43]\n\t" \
    "ds_read_b128 v[156:159], %[a] offset:2048\n\t" \
    "v_mfma_f32_16x16x32_bf16 v[36:39], v[120:123], v[104:107], v[36:39]\n\t" \
    "ds_read_b128 v[160:163], %[a] offset:3072\n\t" \
    "s_add_u32 m0, %[d], 32768\n\t" \
    "v_mfma_f32_16x16x32_bf16 v[40:43], v[128:131], v[104:107], v[40:43]\n\t" \
    "buffer_load_dwordx4 %[va0], %[ra], %[x] offen sc1 lds\n\t" \
    "v_mfma_f32_16x16x32_bf16 v[44:47], v[116:119], v[108:111], v[44:47]\n\t" \
    "ds_read_b128 v[164:167], %[a] offset:4096\n\t" \
    "v_mfma_f32_16x16x32_bf16 v[48:51], v[124:127], v[108:111], v[48:51]\n\t" \
    "ds_read_b128 v[168:171], %[a] offset:5120\n\t" \
    "v_mfma_f32_16x16x32_bf16 v[44:47], v[120:123], v[112:115], v[44:47]\n\t" \
    "ds_read_b128 v[172:175], %[a] offset:6144\n\t" \
    "s_add_u32 m0, %[d], 40960\n\t" \
    "v_mfma_f32_16x16x32_bf16 v[48:51], v[128:131], v[112:115], v[48:51]\n\t" \
    "buffer_load_dwordx4 %[va1], %[ra], %[x] offen sc1 lds\n\t" \
    "v_mfma_f32_16x16x32_bf16 v[52:55], v[132:135], v[84:87], v[52:55]\n\t" \
    "ds_read_b128 v[176:179], %[a] offset:7168\n\t" \
    "v_mfma_f32_16x16x32_bf16 v[56:59], v[140:143], v[84:87], v[56:59]\n\t" \
    "ds_read_b64_tr_b16 v[196:197], %[b0] offset:16384\n\t" \
    "v_mfma_f32_16x16x32_bf16 v[52:55], v[136:139], v[88:91], v[52:55]\n\t" \
    "ds_read_b64_tr_b16 v[198:199], %[b0] offset:17408\n\t" \
    "s_add_u32 m0, %[d], 16128\n\t" \
    "v_mfma_f32_16x16x32_bf16 v[56:59], v[144:147], v[88:91], v[56:59]\n\t" \
    "buffer_load_dwordx4 %[vb0], %[rb], %[y] offen offset:256 lds\n\t" \
    "v_mfma_f32_16x16x32_bf16 v[60:63], v[132:135], v[92:95], v[60:63]\n\t" \
    "ds_read_b64_tr_b16 v[204:205], %[b1] offset:16384\n\t" \
    "v_mfma_f32_16x16x32_bf16 v[64:67], v[140:143], v[92:95], v[64:67]\n\t" \
    "ds_read_b64_tr_b16 v[206:207], %[b1] offset:17408\n\t" \
    "v_mfma_f32_16x16x32_bf16 v[60:63], v[136:139], v[96:99], v[60:63]\n\t" \
    "ds_read_b64_tr_b16 v[200:201], %[b0] offset:24576\n\t" \
    "s_add_u32 m0, %[d], 24320\n\t" \
    "v_mfma_f32_16x16x32_bf16 v[64:67], v[144:147], v[96:99], v[64:67]\n\t" \
    "buffer_load_dwordx4 %[vb1], %[rb], %[y] offen offset:256 lds\n\t" \
    "v_mfma_f32_16x16x32_bf16 v[68:71], v[132:135], v[100:103], v[68:71]\n\t" \
    "ds_read_b64_tr_b16 v[202:203], %[b0] offset:25600\n\t" \
    "s_add_u32 %[t], %[t], 1\n\t" \
    "s_add_u32 %[d], %[lw], %[c]\n\t" \
    "v_mfma_f32_16x16x32_bf16 v[72:75], v[140:143], v[100:103], v[72:75]\n\t" \
    "ds_read_b64_tr_b16 v[208:209], %[b1] offset:24576\n\t" \
    "s_add_u32 %[y], %[c], 49152\n\t" \
    "s_cmp_eq_u32 %[y], 147456\n\t" \
    "v_mfma_f32_16x16x32_bf16 v[68:71], v[136:139], v[104:107], v[68:71]\n\t" \
    "ds_read_b64_tr_b16 v[210:211], %[b1] offset:25600\n\t" \
    "s_cselect_b32 %[y], 0, %[y]\n\t" \
    "s_sub_u32 %[z], %[y], %[c]\n\t" \
    "v_mfma_f32_16x16x32_bf16 v[72:75], v[144:147], v[104:107], v[72:75]\n\t" \
    "s_mov_b32 %[c], %[y]\n\t" \
    "s_add_u32 %[x], %[t], 3\n\t" \
    "v_mfma_f32_16x16x32_bf16 v[76:79], v[132:135], v[108:111], v[76:79]\n\t" \
    "s_sub_u32 %[y], %[x], %[nt]\n\t" \
    "s_cmp_lt_u32 %[x], %[nt]\n\t" \
    "v_mfma_f32_16x16x32_bf16 v[80:83], v[140:143], v[108:111], v[80:83]\n\t" \
    "s_cselect_b32 %[x], %[x], %[y]\n\t" \
    "s_cselect_b32 m0, %[cb], %[cn]\n\t" \
    "v_mfma_f32_16x16x32_bf16 v[76:79], v[136:139], v[112:115], v[76:79]\n\t" \
    "s_mul_i32 %[y], %[x], %[kb]\n\t" \
    "s_add_u32 %[y], %[y], m0\n\t" \
    "v_mfma_f32_16x16x32_bf16 v[80:83], v[144:147], v[112:115], v[80:83]\n\t" \
    "s_lshl_b32 %[x], %[x], 11\n\t" \
    "s_add_u32 %[x], %[x], %[ca]\n\t" \
    "v_add_u32 %[a], %[z], %[a]\n\t" \
    "v_add_u32 %[b0], %[z], %[b0]\n\t" \
    "v_add_u32 %[b1], %[z], %[b1]\n\t" \
    "s_waitcnt vmcnt(6) lgkmcnt(0)\n\t" \
    "s_barrier\n\t" \
    "v_mfma_f32_16x16x32_bf16 v[20:23], v[180:183], v[148:151], v[20:23]\n\t" \
    "ds_read_b64_tr_b16 v[116:117], %[b0] offset:0\n\t" \
    "ds_read_b64_tr_b16 v[118:119], %[b0] offset:1024\n\t" \
    "v_mfma_f32_16x16x32_bf16 v[24:27], v[188:191], v[148:151], v[24:27]\n\t" \
    "ds_read_b64_tr_b16 v[124:125], %[b1] offset:0\n\t" \
    "ds_read_b64_tr_b16 v[126:127], %[b1] offset:1024\n\t" \
    "v_mfma_f32_16x16x32_bf16 v[20:23], v[184:187], v[152:155], v[20:23]\n\t" \
    "ds_read_b64_tr_b16 v[120:121], %[b0] offset:8192\n\t" \
    "ds_read_b64_tr_b16 v[122:123], %[b0] offset:9216\n\t" \
    "s_add_u32 m0, %[d], 0\n\t" \
    "v_mfma_f32_16x16x32_bf16 v[24:27], v[192:195], v[152:155], v[24:27]\n\t" \
    "buffer_load_dwordx4 %[vb0], %[rb], %[y] offen lds\n\t" \
    "v_mfma_f32_16x16x32_bf16 v[28:31], v[180:183], v[156:159], v[28:31]\n\t" \
    "ds_read_b64_tr_b16 v[128:129], %[b1] offset:8192\n\t" \
    "v_mfma_f32_16x16x32_bf16 v[32:35], v[188:191], v[156:159], v[32:35]\n\t" \
    "ds_read_b64_tr_b16 v[130:131], %[b1] offset:9216\n\t" \
    "v_mfma_f32_16x16x32_bf16 v[28:31], v[184:187], v[160:163], v[28:31]\n\t" \
    "ds_read_b128 v[84:87], %[a] offset:0\n\t" \
    "s_add_u32 m0, %[d], 8192\n\t" \
    "v_mfma_f32_16x16x32_bf16 v[32:35], v[192:195], v[160:163], v[32:35]\n\t" \
    "buffer_load_dwordx4 %[vb1], %[rb], %[y] offen lds\n\t" \
    "v_mfma_f32_16x16x32_bf16 v[36:39], v[180:183], v[164:167], v[36:39]\n\t" \
    "ds_read_b128 v[88:91], %[a] offset:1024\n\t" \
    "v_mfma_f32_16x16x32_bf16 v[40:43], v[188:191], v[164:167], v[40:43]\n\t" \
    "ds_read_b128 v[92:95], %[a] offset:2048\n\t" \
    "v_mfma_f32_16x16x32_bf16 v[36:39], v[184:187], v[168:171], v[36:39]\n\t" \
    "ds_read_b128 v[96:99], %[a] offset:3072\n\t" \
    "s_add_u32 m0, %[d], 32768\n\t" \
    "v_mfma_f32_16x16x32_bf16 v[40:43], v[192:195], v[168:171], v[40:43]\n\t" \
    "buffer_load_dwordx4 %[va0], %[ra], %[x] offen sc1 lds\n\t" \
    "v_mfma_f32_16x16x32_bf16 v[44:47], v[180:183], v[172:175], v[44:47]\n\t" \
    "ds_read_b128 v[100:103], %[a] offset:4096\n\t" \
    "v_mfma_f32_16x16x32_bf16 v[48:51], v[188:191], v[172:175], v[48:51]\n\t" \
    "ds_read_b128 v[104:107], %[a] offset:5120\n\t" \
    "v_mfma_f32_16x16x32_bf16 v[44:47], v[184:187], v[176:179], v[44:47]\n\t" \
    "ds_read_b128 v[108:111], %[a] offset:6144\n\t" \
    "s_add_u32 m0, %[d], 40960\n\t" \
    "v_mfma_f32_16x16x32_bf16 v[48:51], v[192:195], v[176:179], v[48:51]\n\t" \
    "buffer_load_dwordx4 %[va1], %[ra], %[x] offen sc1 lds\n\t" \
    "v_mfma_f32_16x16x32_bf16 v[52:55], v[196:199], v[148:151], v[52:55]\n\t" \
    "ds_read_b128 v[112:115], %[a] offset:7168\n\t" \
    "v_mfma_f32_16x16x32_bf16 v[56:59], v[204:207], v[148:151], v[56:59]\n\t" \
    "ds_read_b64_tr_b16 v[132:133], %[b0] offset:16384\n\t" \
    "v_mfma_f32_16x16x32_bf16 v[52:55], v[200:203], v[152:155], v[52:55]\n\t" \
    "ds_read_b64_tr_b16 v[134:135], %[b0] offset:17408\n\t" \
    "s_add_u32 m0, %[d], 16128\n\t" \
    "v_mfma_f32_16x16x32_bf16 v[56:59], v[208:211], v[152:155], v[56:59]\n\t" \
    "buffer_load_dwordx4 %[vb0], %[rb], %[y] offen offset:256 lds\n\t" \
    "v_mfma_f32_16x16x32_bf16 v[60:63], v[196:199], v[156:159], v[60:63]\n\t" \
    "ds_read_b64_tr_b16 v[140:141], %[b1] offset:16384\n\t" \
    "v_mfma_f32_16x16x32_bf16 v[64:67], v[204:207], v[156:159], v[64:67]\n\t" \
    "ds_read_b64_tr_b16 v[142:143], %[b1] offset:17408\n\t" \
    "v_mfma_f32_16x16x32_bf16 v[60:63], v[200:203], v[160:163], v[60:63]\n\t" \
    "ds_read_b64_tr_b16 v[136:137], %[b0] offset:24576\n\t" \
    "s_add_u32 m0, %[d], 24320\n\t" \
    "v_mfma_f32_16x16x32_bf16 v[64:67], v[208:211], v[160:163], v[64:67]\n\t" \
    "buffer_load_dwordx4 %[vb1], %[rb], %[y] offen offset:256 lds\n\t" \
    "v_mfma_f32_16x16x32_bf16 v[68:71], v[196:199], v[164:167], v[68:71]\n\t" \
    "ds_read_b64_tr_b16 v[138:139], %[b0] offset:25600\n\t" \
    "s_add_u32 %[t], %[t], 1\n\t" \
    "s_add_u32 %[d], %[lw], %[c]\n\t" \
    "s_add_u32 %[y], %[c], 49152\n\t" \
    "v_mfma_f32_16x16x32_bf16 v[72:75], v[204:207], v[164:167], v[72:75]\n\t" \
    "ds_read_b64_tr_b16 v[144:145], %[b1] offset:24576\n\t" \
    "s_cmp_eq_u32 %[y], 147456\n\t" \
    "s_cselect_b32 %[y], 0, %[y]\n\t" \
    "s_sub_u32 %[z], %[y], %[c]\n\t" \
    "v_mfma_f32_16x16x32_bf16 v[68:71], v[200:203], v[168:171], v[68:71]\n\t" \
    "ds_read_b64_tr_b16 v[146:147], %[b1] offset:25600\n\t" \
    "s_mov_b32 %[c], %[y]\n\t" \
    "s_add_u32 %[x], %[t], 3\n\t" \
    "s_sub_u32 %[y], %[x], %[nt]\n\t" \
    "v_mfma_f32_16x16x32_bf16 v[72:75], v[208:211], v[168:171], v[72:75]\n\t" \
    "s_cmp_lt_u32 %[x], %[nt]\n\t" \
    "s_cselect_b32 %[x], %[x], %[y]\n\t" \
    "s_cselect_b32 m0, %[cb], %[cn]\n\t" \
    "v_mfma_f32_16x16x32_bf16 v[76:79], v[196:199], v[172:175], v[76:79]\n\t" \
    "s_mul_i32 %[y], %[x], %[kb]\n\t" \
    "s_add_u32 %[y], %[y], m0\n\t" \
    "s_lshl_b32 %[x], %[x], 11\n\t" \
    "v_mfma_f32_16x16x32_bf16 v[80:83], v[204:207], v[172:175], v[80:83]\n\t" \
    "s_add_u32 %[x], %[x], %[ca]\n\t" \
    "s_cmp_lt_u32 %[t], %[nt]\n\t" \
    "v_mfma_f32_16x16x32_bf16 v[76:79], v[200:203], v[176:179], v[76:79]\n\t" \
    "v_mfma_f32_16x16x32_bf16 v[80:83], v[208:211], v[176:179], v[80:83]\n\t" \
    "v_add_u32 %[a], %[z], %[a]\n\t" \
    "v_add_u32 %[b0], %[z], %[b0]\n\t" \
    "v_add_u32 %[b1], %[z], %[b1]\n\t" \
    "s_cbranch_scc1 L_%=\n\t" \
    "s_waitcnt lgkmcnt(0)\n\t" \
    "s_nop 7\n\t" \
    : "=&{v[20:23]}"(acc[0][0][0][0]), "=&{v[24:27]}"(acc[0][0][0][1]), "=&{v[28:31]}"(acc[0][0][1][0]), "=&{v[32:35]}"(acc[0][0][1][1]), "=&{v[36:39]}"(acc[0][0][2][0]), "=&{v[40:43]}"(acc[0][0][2][1]), "=&{v[44:47]}"(acc[0][0][3][0]), "=&{v[48:51]}"(acc[0][0][3][1]), "=&{v[52:55]}"(acc[0][1][0][0]), "=&{v[56:59]}"(acc[0][1][0][1]), "=&{v[60:63]}"(acc[0][1][1][0]), "=&{v[64:67]}"(acc[0][1][1][1]), "=&{v[68:71]}"(acc[0][1][2][0]), "=&{v[72:75]}"(acc[0][1][2][1]), "=&{v[76:79]}"(acc[0][1][3][0]), "=&{v[80:83]}"(acc[0][1][3][1]), [a] "+v"(aoff), [b0] "+v"(boff0), [b1] "+v"(boff1), [c] "+s"(cur), [t] "+s"(t), [d] "=&s"(sd), [x] "=&s"(sx), [y] "=&s"(sy), [z] "=&s"(sz) \
    : [va0] "v"(voffA[0]), [va1] "v"(voffA[1]), [vb0] "v"(voffB[0]), [vb1] "v"(voffB[1]), [ra] "s"(ra), [rb] "s"(rb), [lw] "s"(ldsw), [nt] "s"(nt), [kb] "s"(kB), [ca] "s"(cA), [cb] "s"(cB), [cn] "s"(cN) \
    : "memory", "scc", "v84", "v85", "v86", "v87", "v88", "v89", "v90", "v91", "v92", "v93", "v94", "v95", "v96", "v97", "v98", "v99", "v100", "v101", "v102", "v103", "v104", "v105", "v106", "v107", "v108", "v109", "v110", "v111", "v112", "v113", "v114", "v115", "v116", "v117", "v118", "v119", "v120", "v121", "v122", "v123", "v124", "v125", "v126", "v127", "v128", "v129", "v130", "v131", "v132", "v133", "v134", "v135", "v136", "v137", "v138", "v139", "v140", "v141", "v142", "v143", "v144", "v145", "v146", "v147", "v148", "v149", "v150", "v151", "v152", "v153", "v154", "v155", "v156", "v157", "v158", "v159", "v160", "v161", "v162", "v163", "v164", "v165", "v166", "v167", "v168", "v169", "v170", "v171", "v172", "v173", "v174", "v175", "v176", "v177", "v178", "v179", "v180", "v181", "v182", "v183", "v184", "v185", "v186", "v187", "v188", "v189", "v190", "v191", "v192", "v193", "v194", "v195", "v196", "v197", "v198", "v199", "v200", "v201", "v202", "v203", "v204", "v205", "v206", "v207", "v208", "v209", "v210", "v211")
#define AQ_PIN0() asm volatile("" : "+{v[28:35]}"(wr[0][0]), "+{v[36:43]}"(wr[0][1]))
#define AQ_PIN1() asm volatile("" : "+{v[44:51]}"(wr[1][0]), "+{v[52:59]}"(wr[1][1]))
#define AQ_PIN2() asm volatile("" : "+{v[60:67]}"(wr[2][0]), "+{v[68:75]}"(wr[2][1]))
#define AQ_PIN3() asm volatile("" : "+{v[76:83]}"(wr[3][0]), "+{v[84:91]}"(wr[3][1]))
#define AQ_PIN4() asm volatile("" : "+{v[92:99]}"(wr[4][0]), "+{v[100:107]}"(wr[4][1]))
#define AQ_PIN5() asm volatile("" : "+{v[108:115]}"(wr[5][0]), "+{v[116:123]}"(wr[5][1]))
#define AQ_PIN6() asm volatile("" : "+{v[124:131]}"(wr[6][0]), "+{v[132:139]}"(wr[6][1]))
#define AQ_PIN7() asm volatile("" : "+{v[140:147]}"(wr[7][0]), "+{v[148:155]}"(wr[7][1]))
#define AQ_PIN8() asm volatile("" : "+{v[156:163]}"(wr[8][0]), "+{v[164:171]}"(wr[8][1]))
#define AQ_PIN9() asm volatile("" : "+{v[172:179]}"(wr[9][0]), "+{v[180:187]}"(wr[9][1]))
#define AQ_PIN10() asm volatile("" : "+{v[188:195]}"(wr[10][0]), "+{v[196:203]}"(wr[10][1]))
#define AQ_PIN11() asm volatile("" : "+{v[204:211]}"(wr[11][0]), "+{v[212:219]}"(wr[11][1]))
#define AK_PIN0() asm volatile("" : "+{v[20:27]}"(wr[0][0]), "+{v[28:35]}"(wr[0][1]))
#define AK_PIN1() asm volatile("" : "+{v[36:43]}"(wr[1][0]), "+{v[44:51]}"(wr[1][1]))
#define AK_PIN2() asm volatile("" : "+{v[52:59]}"(wr[2][0]), "+{v[60:67]}"(wr[2][1]))
#define AK_PIN3() asm volatile("" : "+{v[68:75]}"(wr[3][0]), "+{v[76:83]}"(wr[3][1]))
#define AK_PIN4() asm volatile("" : "+{v[84:91]}"(wr[4][0]), "+{v[92:99]}"(wr[4][1]))
#define AK_PIN5() asm volatile("" : "+{v[100:107]}"(wr[5][0]), "+{v[108:115]}"(wr[5][1]))
#define AK_PIN6() asm volatile("" : "+{v[116:123]}"(wr[6][0]), "+{v[124:131]}"(wr[6][1]))
#define AK_PIN7() asm volatile("" : "+{v[132:139]}"(wr[7][0]), "+{v[140:147]}"(wr[7][1]))
#define AK_PIN8() asm volatile("" : "+{v[148:155]}"(wr[8][0]), "+{v[156:163]}"(wr[8][1]))
#define AK_PIN9() asm volatile("" : "+{v[164:171]}"(wr[9][0]), "+{v[172:179]}"(wr[9][1]))
#define AK_PIN10() asm volatile("" : "+{v[180:187]}"(wr[10][0]), "+{v[188:195]}"(wr[10][1]))
#define AK_PIN11() asm volatile("" : "+{v[196:203]}"(wr[11][0]), "+{v[204:211]}"(wr[11][1]))
#define V_PIN() asm volatile("" : "+{v[28:35]}"(wv[0]), "+{v[36:43]}"(wv[1]), "+{v[44:51]}"(wv[2]), "+{v[52:59]}"(wv[3]), "+{v[60:67]}"(wv[4]), "+{v[68:75]}"(wv[5]), "+{v[76:83]}"(wv[6]), "+{v[84:91]}"(wv[7]), "+{v[92:99]}"(wv[8]), "+{v[100:107]}"(wv[9]), "+{v[108:115]}"(wv[10]), "+{v[116:123]}"(wv[11]), "+{v[124:131]}"(wv[12]), "+{v[132:139]}"(wv[13]), "+{v[140:147]}"(wv[14]), "+{v[148:155]}"(wv[15]))
#define WOV_PIN0() asm volatile("" : "+{v[156:163]}"(wo[0][0][0]), "+{v[164:171]}"(wo[0][0][1]), "+{v[172:179]}"(wo[0][1][0]), "+{v[180:187]}"(wo[0][1][1]))
#define WOV_PIN1() asm volatile("" : "+{v[188:195]}"(wo[1][0][0]), "+{v[196:203]}"(wo[1][0][1]), "+{v[204:211]}"(wo[1][1][0]), "+{v[212:219]}"(wo[1][1][1]))
#define WOW_PIN0() asm volatile("" : "+{v[28:35]}"(wo[0][0][0]), "+{v[36:43]}"(wo[0][0][1]), "+{v[44:51]}"(wo[0][1][0]), "+{v[52:59]}"(wo[0][1][1]))
#define WOW_PIN1() asm volatile("" : "+{v[60:67]}"(wo[1][0][0]), "+{v[68:75]}"(wo[1][0][1]), "+{v[76:83]}"(wo[1][1][0]), "+{v[84:91]}"(wo[1][1][1]))
#define WOW_PIN2() asm volatile("" : "+{v[92:99]}"(wo[2][0][0]), "+{v[100:107]}"(wo[2][0][1]), "+{v[108:115]}"(wo[2][1][0]), "+{v[116:123]}"(wo[2][1][1]))
#define WOW_PIN3() asm volatile("" : "+{v[124:131]}"(wo[3][0][0]), "+{v[132:139]}"(wo[3][0][1]), "+{v[140:147]}"(wo[3][1][0]), "+{v[148:155]}"(wo[3][1][1]))
#define WOW_PIN4() asm volatile("" : "+{v[156:163]}"(wo[4][0][0]), "+{v[164:171]}"(wo[4][0][1]), "+{v[172:179]}"(wo[4][1][0]), "+{v[180:187]}"(wo[4][1][1]))
#define WOW_PIN5() asm volatile("" : "+{v[188:195]}"(wo[5][0][0]), "+{v[196:203]}"(wo[5][0][1]), "+{v[204:211]}"(wo[5][1][0]), "+{v[212:219]}"(wo[5][1][1]))
#define A_PIN(QW, i) do { switch (i) { case 0: if (QW) AQ_PIN0(); else AK_PIN0(); break; case 1: if (QW) AQ_PIN1(); else AK_PIN1(); break; case 2: if (QW) AQ_PIN2(); else AK_PIN2(); break; case 3: if (QW) AQ_PIN3(); else AK_PIN3(); break; case 4: if (QW) AQ_PIN4(); else AK_PIN4(); break; case 5: if (QW) AQ_PIN5(); else AK_PIN5(); break; case 6: if (QW) AQ_PIN6(); else AK_PIN6(); break; case 7: if (QW) AQ_PIN7(); else AK_PIN7(); break; case 8: if (QW) AQ_PIN8(); else AK_PIN8(); break; case 9: if (QW) AQ_PIN9(); else AK_PIN9(); break; case 10: if (QW) AQ_PIN10(); else AK_PIN10(); break; case 11: if (QW) AQ_PIN11(); else AK_PIN11(); break; } } while (0)
#define WO_PIN(VW, i) do { switch (i) { case 0: if (VW) WOV_PIN0(); else WOW_PIN0(); break; case 1: if (VW) WOV_PIN1(); else WOW_PIN1(); break; case 2: if (!VW) WOW_PIN2(); break; case 3: if (!VW) WOW_PIN3(); break; case 4: if (!VW) WOW_PIN4(); break; case 5: if (!VW) WOW_PIN5(); break; } } while (0)

__device__ __forceinline__ int lds_byte(int r, int c) {
  const int st = (r >> 4) * 2 + (c >> 5), rr = r & 15, cc = c & 31, ob = rr * 64 + cc * 2;
  return st * 1024 + (ob ^ (((ob >> 9) & 1) << 5));
}
#define G_DvoffB dmaw
#define G_ST1(bufoff, r, so, voff, i) G_D##voff(r, (voff)[i], so, ldsw + (bufoff) + (i) * 8192)
#define G_DvoffA dmas
#define G_STAGE(bufoff, so, voff) do { G_ST1(bufoff, G_R##voff, so, voff, 0); G_ST1(bufoff, G_R##voff, so, voff, 1); } while (0)
#define G_RvoffA ra
#define G_RvoffB rb
#define G_WAIT_V(n) asm volatile("s_waitcnt vmcnt(%0)" :: "n"(n) : "memory")
__device__ __forceinline__ void gemm_h(LAS unsigned char* lds, rsrc_t ra, rsrc_t rb, unsigned cA, unsigned cB, int lda, int ldb, int nt,
                                       bool wide, unsigned cN, bool cont, unsigned cur, f32x4 (&acc)[1][2][4][2]) {
  const int wid = __builtin_amdgcn_readfirstlane(threadIdx.x >> 6), lane = opq(0), tid = wid * 64 + lane;
  const int wr = wid >> 2, wc = wid & 3, fr = lane & 15, fq = lane >> 4;
  unsigned voffA[2], voffB[2];
  #pragma unroll
  for (int i = 0; i < 2; ++i) {
    voffA[i] = (unsigned)(((wid >> 1) + 4 * i) * 16 * lda + (wid & 1) * 1024 + lane * 16);
    const int b = tid * 16 + i * 8192, r = b >> 8, pos = (b >> 4) & 15;
    const int c = pos ^ (((r & 3) << 1) | (((r >> 3) & 1) << 3));
    voffB[i] = (unsigned)(r * ldb + c * 16);
  }
  const unsigned kB = 64 * ldb;
  const unsigned ldsw = (unsigned)(size_t)lds + (unsigned)wid * 1024u;
  const int q = (lane & 15) >> 2, p = lane & 3;
  unsigned aoff = (unsigned)(size_t)lds + cur + 32768 + lds_byte(wr * 64 + fr, fq * 8);
  unsigned boff0 = (unsigned)(size_t)lds + cur + (8 * fq + q) * 256 + (((wc * 4 + (p >> 1)) ^ ((q << 1) | ((fq & 1) << 3))) << 4) + 8 * (p & 1), boff1 = boff0 ^ 32;
#define H_STAGE(so, t) do { G_STAGE(so, cB + (t) * kB, voffB); G_STAGE((so) + 32768, cA + (t) * 2048, voffA); if (wide) G_STAGE((so) + 16384, cB + (t) * kB + 256, voffB); } while (0)
  if (!cont) { H_STAGE(0, 0); H_STAGE(49152, 1); H_STAGE(98304, 2); }
  unsigned t = 0, sd, sx, sy, sz;
  if (wide) H_LOOP2(); else H_LOOP1();
}

__device__ __forceinline__ u32x4 epi_pack(const f32x4 a0, const f32x4 a1) {
  const unsigned x0 = pack2(a0[0], a0[1]), x1 = pack2(a0[2], a0[3]);
  const unsigned y0 = pack2(a1[0], a1[1]), y1 = pack2(a1[2], a1[3]);
  auto s0 = __builtin_amdgcn_permlane16_swap(x0, y0, false, false);
  auto s1 = __builtin_amdgcn_permlane16_swap(x1, y1, false, false);
  return (u32x4){s0[0], s1[0], s0[1], s1[1]};
}
__device__ __forceinline__ unsigned gelu2(unsigned w) {
  const f32x2 x = {lo16(w), hi16(w)};
  const float c0 = -2.f * 1.4426950408889634f * 0.7978845608028654f, c1 = c0 * 0.044715f;
  const f32x2 a = x * (x * x * c1 + c0);
  const f32x2 d = (f32x2){__builtin_amdgcn_exp2f(a[0]), __builtin_amdgcn_exp2f(a[1])} + 1.f;
  const f32x2 y = x * (f32x2){__builtin_amdgcn_rcpf(d[0]), __builtin_amdgcn_rcpf(d[1])};
  return pack2(y[0], y[1]);
}
__device__ __forceinline__ float silu_f(float b) { return b * __builtin_amdgcn_rcpf(1.f + __expf(-b)); }

__device__ __forceinline__ unsigned blk_in(int r, int c) { const int ob = r * 64 + c * 2; return ob ^ (((ob >> 9) & 1) << 5); }
__device__ __forceinline__ unsigned xn_blk(int rb, int cb) { return (unsigned)(((rb >> 1) * 32 + cb) * 2 + (rb & 1)) * 1024u; }

template <int EPI>
__device__ __forceinline__ void epi_rows(const f32x4 (&acc)[1][2][4][2], rsrc_t rs, unsigned obase, int ldc, int pm2, int pn2, int sh, bool wide, int wr, int wc, int ln) {
  const int fq = ln >> 4, cb = 16 * (fq & 1) + 8 * (fq >> 1), fr = (ln & 15) + sh;
  const unsigned lo = (unsigned)((fr >> 4) * 16 * ldc) + blk_in(fr & 15, cb);
  const unsigned ob = (unsigned)(pm2 * 128 + 64 * wr) * (unsigned)ldc + (unsigned)(pn2 * 4 + wc) * 1024u + lo;
  u32x4 r[8];
  if (EPI >= 2) {
    #pragma unroll
    for (int i = 0; i < 8; ++i)
      r[i] = ld16(rs, EPI == 2 ? WS_XN + xn_blk(pm2 * 8 + 4 * wr + (i & 3), pn2 * 4 + 4 * (i >> 2) + wc) + lo : WS_X1 + ob + (i & 3) * 32768 + (i >> 2) * 4096);
  }
  #pragma unroll
  for (int i = 0; i < 8; ++i) {
    if (i == 4 && !wide) break;
    u32x4 w = epi_pack(acc[0][i >> 2][i & 3][0], acc[0][i >> 2][i & 3][1]);
    #pragma unroll
    for (int k = 0; k < 4; ++k) {
      w[k] = EPI < 2 ? gelu2(w[k]) : pack2(lo16(r[i][k]) + lo16(w[k]), hi16(r[i][k]) + hi16(w[k]));
      if (EPI == 3) w[k] = pack2(silu_f(lo16(w[k])), silu_f(hi16(w[k])));
    }
    st16(rs, obase + ob + (unsigned)(16 * (i & 3) * ldc) + (i >> 2) * 4096, w);
  }
}

__device__ __forceinline__ float row_sum16(float v) {
  v += __uint_as_float(__builtin_amdgcn_update_dpp(0, (int)__float_as_uint(v), 0x128, 0xf, 0xf, false));
  v += __uint_as_float(__builtin_amdgcn_update_dpp(0, (int)__float_as_uint(v), 0x124, 0xf, 0xf, false));
  v += __uint_as_float(__builtin_amdgcn_update_dpp(0, (int)__float_as_uint(v), 0x122, 0xf, 0xf, false));
  v += __uint_as_float(__builtin_amdgcn_update_dpp(0, (int)__float_as_uint(v), 0x121, 0xf, 0xf, false));
  return v;
}
__device__ __forceinline__ void ln_rows(u32x4 (&a)[8]) {
  f32x2 v[32];
  #pragma unroll
  for (int i = 0; i < 32; ++i) v[i] = (f32x2){lo16(a[i >> 2][i & 3]), hi16(a[i >> 2][i & 3])};
  f32x2 s[4] = {v[0], v[1], v[2], v[3]};
  #pragma unroll
  for (int i = 4; i < 32; ++i) s[i & 3] += v[i];
  s[0] = (s[0] + s[1]) + (s[2] + s[3]);
  const float mean = row_sum16(s[0][0] + s[0][1]) * (1.f / DM);
  #pragma unroll
  for (int i = 0; i < 32; ++i) { const f32x2 d = v[i] - mean; s[i & 3] = i < 4 ? d * d : s[i & 3] + d * d; }
  s[0] = (s[0] + s[1]) + (s[2] + s[3]);
  const float var = row_sum16(s[0][0] + s[0][1]) * (1.f / DM);
  const float mb = rb1(mean), vb = rb1(var);
  const float rden = __builtin_amdgcn_rcpf(rb1(__builtin_amdgcn_sqrtf(rb1(vb + 1e-5f))));
  #pragma unroll
  for (int i = 0; i < 32; ++i) {
    const f32x2 d = v[i] - mb;
    const unsigned w = pack2(d[0], d[1]);
    const f32x2 y = (f32x2){lo16(w), hi16(w)} * rden;
    a[i >> 2][i & 3] = pack2(y[0], y[1]);
  }
}

template <int N>
__device__ __forceinline__ void fin_sum(rsrc_t rs, unsigned po, u32x4* v) {
  u32x4 pv[N][8];
  #pragma unroll
  for (int i = 0; i < 8 * N; ++i) pv[i >> 3][i & 7] = ld16(rs, po + (i >> 3) * 16384 + PT_AT((i >> 1) & 3) + (i & 1) * 1024);
#define PF4(i) __builtin_bit_cast(f32x4, pv[c][i])
  #pragma unroll
  for (int c = 0; c < N; ++c) v[c] = epi_pack((PF4(0) + PF4(2)) + (PF4(4) + PF4(6)), (PF4(1) + PF4(3)) + (PF4(5) + PF4(7)));
}
__device__ __forceinline__ void fin_out(const Ptrs& P, unsigned eo, u32x4 v, u32x4 gv, u32x4 xv) {
  #pragma unroll
  for (int k = 0; k < 4; ++k) {
    v[k] = pack2(lo16(v[k]) * lo16(gv[k]), hi16(v[k]) * hi16(gv[k]));
    v[k] = pack2(lo16(xv[k]) + lo16(v[k]), hi16(xv[k]) + hi16(v[k]));
  }
  *(u32x4*)((char*)P.out + eo) = v;
}

__device__ __forceinline__ void mlp_role(LAS unsigned char* lds, const Ptrs& P, int hf, int slot, int wid) {
  const int wr = wid >> 2, wc = wid & 3;
  const int p2 = hf * 8 + (slot >> 2), pn = slot & 3;
  char* ws = P.ws;
  const rsrc_t rs = WS_RSRC(P);
  const u16* const p1 = P.w1; const u16* const p2w = P.w2; const u16* const p3 = P.w3; const u16* const p4 = P.w4;
#define M_DONE(c) do { VMWAIT(); BARRIER(); if (wid == 0) cnt_add(ws, c, opq(0)); } while (0)
  #pragma unroll 1
  for (int code = 0; code < 7; ++code) {
    int ln = opq(0);
    if (wid == 0) {
      if (code == 0) flag_wait(P, F_LN1(p2 * 4 + (ln & 3)), 0, ln);
      else if (code < 3 || code == 6) {
        const unsigned co = CNT_OFF + (code == 1 ? C_W1(min(max(p2 - 1 + min(ln, 2), 0), 15)) : code == 2 ? C_LN(p2) : C_W3(p2)) * 128;
        for (unsigned spins = 0; __any(ld4(rs, co) < 4u);) {
          __builtin_amdgcn_s_sleep(2);
          if (spin_fail(spins, P, ln)) break;
        }
      }
    }
    if (code < 3 || code == 6) BARRIER();
    TLW(3 * code);
    const bool w3 = code >= 2 && code < 6, wide = code != 5;
    const int lda = code == 6 ? 7168 : 2048, ldb = w3 ? 7168 : 2048, c2 = w3 ? (code - 2) * 8 + (wide ? 2 * pn : pn) : 2 * pn;
    const rsrc_t rb = __builtin_amdgcn_make_buffer_rsrc((void*)(code == 0 ? p1 : code == 1 ? p2w : w3 ? p3 : p4), 0, code < 2 ? DM * DM * 2 : DM * FF * 2, RSRC_W3);
    const unsigned cA = (code == 0 ? WS_XN2 : code == 1 ? WS_H1 : w3 ? WS_X1N : WS_H3) + p2 * 128 * lda, cB = c2 * 256;
    f32x4 acq[1][2][4][2];
    gemm_h(lds, rs, rb, cA, cB, lda, ldb, code == 6 ? 56 : 16, wide, code == 4 ? (24 + pn) * 256 : w3 && wide ? cB + 2048 : cB, w3 && code > 2, code == 3 || code == 4 ? (code - 2) * 49152 : 0, acq);
    TLW(3 * code + 1);
    ln = opq(0);
    if (code == 1) {
      epi_rows<2>(acq, rs, WS_X1, 2048, p2, c2, 0, true, wr, wc, ln);
      M_DONE(C_W2(p2));
      if (wid == 0) cnt_wait(P, C_W2(p2), 4, ln);
      BARRIER();
      const unsigned ro = (unsigned)(p2 * 8 + pn * 2 + wr) * 32768u + ((ln >> 2) & 3) * 1024 + blk_in(wc * 4 + (ln >> 4), (ln & 3) * 8);
      u32x4 v[8];
      #pragma unroll
      for (int i = 0; i < 8; ++i) v[i] = ld16(rs, WS_X1 + ro + i * 4096);
      ln_rows(v);
      #pragma unroll
      for (int i = 0; i < 8; ++i) st16(rs, WS_X1N + ro + i * 4096, v[i]);
      M_DONE(C_LN(p2));
    } else if (code == 6) {
      epi_rows<3>(acq, rs, WS_G, 2048, p2, c2, 0, true, wr, wc, ln);
      M_DONE(C_W4(p2));
    } else {
      VMWAIT();
      if (code == 0 && wid < 4) touch(p2w, DM * DM * 2, ((hf * 32 + slot) * 256 + wid * 64 + ln) * 128, lds);
      epi_rows<1>(acq, rs, code ? WS_H3 : WS_H1, ldb, p2, c2, code ? 0 : (pn & 1) ? -1 - (pn >> 1) : 1 + (pn >> 1), wide, wr, wc, ln);
      if (code == 0 && wid == 0) setup_wait(P, ln);
      if (code == 0 || code == 5) M_DONE(code ? C_W3(p2) : C_W1(p2));
    }
    TLW(3 * code + 2);
  }
}

#define FINE 52
__device__ __forceinline__ void fin_role(LAS unsigned char* lds, const Ptrs& P, int wid) {
  const int wr = wid >> 2, wc = wid & 3;
  const rsrc_t rs = WS_RSRC(P);
  volatile LAS int* tk = (volatile LAS int*)(lds + 147456);
  unsigned tks = 0;
#define POP() do { if (wid == 0 && opq(0) == 0) tks = __hip_atomic_fetch_add((gu32*)(P.ws + CNT_OFF + C_HEAD * 128), 1u, RLX_AGENT); } while (0)
  POP();
  for (int round = 0;; ++round) {
    if (wid == 0 && opq(0) == 0) tk[round & 1] = tks;
    LBARRIER();
    const int u = __builtin_amdgcn_readfirstlane(tk[round & 1]);
    if (u >= 2 * FINE + 8 * (XS3 - FINE)) break;
    const bool big = u < 2 * FINE;
    const int sg = big ? u >> 1 : FINE + ((u - 2 * FINE) >> 3), cc0 = big ? (u & 1) * 4 : (u - 2 * FINE) & 7;
    const int ln = opq(0), fq = ln >> 4, cb = 16 * (fq & 1) + 8 * (fq >> 1);
    const unsigned eo = (unsigned)(sg * 32 + wr * 16 + (ln & 15)) * 2048u + (unsigned)(cc0 * 128 + wc * 32 + cb) * 2u;
    const unsigned go = WS_G + (unsigned)(sg * 2 + wr) * 32768u + (cc0 * 4 + wc) * 1024 + blk_in(ln & 15, cb);
    const unsigned po = (unsigned)sg * 262144u + cc0 * 16384 + wc * 4096 + wr * 2048 + ln * 16;
    const int xg = (sg >= XS1) + (sg >= XS2), want = sg + 1 - (xg == 2 ? XS2 : xg * XS1);
    u32x4 v[4], gv[4], xv[4];
    if (big) {
      if (wid == 2) flag_wait(P, F_PRG(5 + xg, ln & 31), want, ln);
      BARRIER();
      fin_sum<2>(rs, po, v);
      asm volatile("" : "+v"(v[0]), "+v"(v[1]) :: "memory");
      #pragma unroll
      for (int c = 0; c < 4; ++c) xv[c] = __builtin_nontemporal_load((const u32x4*)((const char*)P.x + eo + c * 256));
      fin_sum<2>(rs, po + 32768, v + 2);
      if (wid == 1) cnt_wait(P, C_W4(sg >> 2), 4, ln);
      BARRIER();
      #pragma unroll
      for (int c = 0; c < 4; ++c) gv[c] = ld16(rs, go + c * 4096);
      #pragma unroll
      for (int c = 0; c < 4; ++c) fin_out(P, eo + c * 256, v[c], gv[c], xv[c]);
    } else {
      if (wid == 1) cnt_wait(P, C_W4(sg >> 2), 4, ln);
      BARRIER();
      gv[0] = ld16(rs, go);
      xv[0] = __builtin_nontemporal_load((const u32x4*)((const char*)P.x + eo));
      if (wid == 2) flag_wait<3>(P, F_PRG(5 + xg, (ln & 3) * 8 + cc0), want, ln);
      BARRIER();
      fin_sum<1>(rs, po, v);
      fin_out(P, eo, v[0], gv[0], xv[0]);
    }
    POP();
  }
}

__device__ __forceinline__ u32x4 ldsv(LAS unsigned char* p) { return *(volatile LAS u32x4*)p; }
#define DMA_A dman
#define DMA_V dman
#define DMA_O dmaw
#define TOUCH 0
#define TAUX 0
#define POISON4 ((u32x4){~0u, ~0u, ~0u, ~0u})
#define DLYM 0
#define DLYA 8
#define DLYK (DLYA + 8)
#define DLYO 8
#define FLA 0
#define FLB 0
#define CADM 1
#define CADO 1

#define RTC(id)
#define DCA 3
#define DCB 12
#define DCU 8
#define DL1 -7
#define DL2 6
#define DLB 15
#define ADL 0
#define SN0 150
#define MAP 0
#define HELP 256
#define BORD 0
#define QT 0
__device__ __forceinline__ void track(int& est, int d, int gain) { const int e = d - (est >> 4); est += e > 44 ? DCB * 2 : e * (e > 0 ? max(gain, DCU) : gain); }
__device__ __forceinline__ unsigned rfl(unsigned v) { unsigned r; asm volatile("s_nop 0\n\tv_readfirstlane_b32 %0, %1\n\ts_nop 4" : "=s"(r) : "v"(v)); return r; }
__device__ __forceinline__ void plan_wait(rsrc_t rs, unsigned fo, int s, int& lim, unsigned& tm, unsigned& d) {
  while ((d & 255u) != (unsigned)s && (lim -= DEC1) > 0) {
    __builtin_amdgcn_s_sleep(1);
    const u32x2 v = __builtin_amdgcn_raw_buffer_load_b64(rs, 0, fo, AUX_SC1);
    tm = rfl(v.x); d = rfl(v.y);
  }
  const unsigned now = (unsigned)__builtin_amdgcn_s_memrealtime() * 4u;
  tm = now + min((int)(tm - now), 2000);
  d = min(d >> 8, 1000u);
}
#define SPIN_BUDGET (1 << 21)

__device__ __forceinline__ unsigned rec_go(LAS unsigned char* w, const Ptrs& P, u32x2 f, int wid, int lane) {
  if (wid >= 6) {
    if (__any(f.y != P.tag)) f.x = flag_wait(P, wid == 6 ? F_LN0(lane) : F_CAL(lane), 0, lane);
    if (wid == 7) { const unsigned m = near_half(f.x); if (lane == 0) *(volatile LAS unsigned*)w = m; }
  }
  LBARRIER();
  return __builtin_amdgcn_readfirstlane(*(volatile LAS unsigned*)w);
}

#define SLOT(i) ((i) * 16384)
#define LA_MEM SLOT(8)
#define LA_Q SLOT(9)
#define LA_K (SLOT(9) + 2048)
#define LA_P (SLOT(9) + 4096)
#define LA_KS (SLOT(9) + 6144)
#define LA_VM LA_MEM
#define LA_KX (SLOT(9) + 8192)

template <bool QW>
__device__ __forceinline__ void a_wave(LAS unsigned char* lds, Ptrs& P, int h, int wid) {
  char* ws = P.ws;
  const int lane = opq(0), tid = wid * 64 + lane;
  const int j = wid & 3, g = lane >> 4, q4 = (lane & 15) >> 2, p4 = lane & 3;
  const rsrc_t rs = WS_RSRC(P);
  const rsrc_t rq = __builtin_amdgcn_make_buffer_rsrc((void*)P.Wq, 0, HD * DM * 2, RSRC_W3);
  const rsrc_t rk = __builtin_amdgcn_make_buffer_rsrc((void*)P.Wk, 0, HD * DM * 2, RSRC_W3);
  const unsigned lbase = (unsigned)(size_t)lds;
  int lim = SPIN_BUDGET;
  u32x8 wr[12][2];
  const int trow = 16 * (g >> 1) + 4 * (g & 1) + 8 * (q4 >> 1) + (q4 & 1);
  const int tsw = ((q4 & 1) | ((g & 1) << 1) | ((q4 >> 1) << 2)) << 1;
  const int toff = trow * 256 + (((4 * j + (p4 >> 1)) ^ tsw) << 4) + 8 * (p4 & 1);
  {
    unsigned dv[2];
    #pragma unroll
    for (int x = 0; x < 2; ++x) {
      const int row = 8 * wid + 4 * x + (lane >> 4);
      const int sw = ((row & 1) | (((row >> 2) & 1) << 1) | (((row >> 3) & 1) << 2)) << 1;
      dv[x] = (unsigned)(row * (HD * 2) + h * 256 + (((lane & 15) ^ sw) << 4));
    }
#define A_GRP(n) do { _Pragma("unroll") for (int i = 0; i < 8; ++i) \
      DMA_A(((n) & 1) ? rk : rq, dv[i & 1], (4 * (((n) & 1) ? ((n) >> 1) ^ ((n) >> 2) : (((n) >> 1) + 1) & 3) + (i >> 1)) * 64 * HD * 2, lbase + SLOT(4 * ((n) & 1) + (i >> 1)) + wid * 2048 + (i & 1) * 1024); } while (0)
    A_GRP(0); A_GRP(1);
    #pragma unroll
    for (int n = 0; n < 6; ++n) {
      VMW(8);
      BARRIER();
      if ((n & 1) == (QW ? 0 : 1)) {
        #pragma unroll
        for (int b = 0; b < 4; ++b) {
          LAS unsigned char* t = lds + SLOT(QW ? 0 : 4) + ((b ^ j) << 14);
          wr[4 * (n >> 1) + b][0] = rd_frag<8192, 512>(t + toff);
          wr[4 * (n >> 1) + b][1] = rd_frag<8192, 512>(t + (toff ^ 32));
        }
        #pragma unroll
        for (int b = 0; b < 4; ++b) A_PIN(QW, 4 * (n >> 1) + b);
      }
      LBARRIER();
      A_GRP(n + 2);
    }
  }
  const unsigned cb = comm_base(ws);
  u32x2 f0 = {0u, 0u};
  #pragma unroll
  for (int rd = 0; rd < 2; ++rd) {
    if (rd == 0) VMW(8);
    else {
      if (wid >= 6) f0 = ld8(rs, FLG_OFF + (wid == 6 ? F_LN0(lane) : F_CAL(lane)) * 128);
      const unsigned fv = wid == 3 ? FLG_OFF + F_PRG(min((lane >> 1) & 3, 2), h) * 128 + (lane & 1) * 4 : WS_QF + j * (16u << 20) + h * 8192 + lane * 128;
      if (QW) asm volatile("s_mov_b32 m0, %2\n\ts_nop 0\n\tbuffer_load_dword %0, %1, 0 offen sc1 lds" :: "v"(fv), "s"(rs), "s"(lbase + LA_P + (wid == 3 ? 640 : 1024)) : "memory");
      get_tag(P);
      VMWAIT();
    }
    BARRIER();
    u32x8 f[2][2];
    #pragma unroll
    for (int i = 0; i < 2; ++i) {
      f[i][0] = rd_frag<8192, 512>(lds + SLOT((QW ? 0 : 2) + 4 * rd + i) + toff);
      f[i][1] = rd_frag<8192, 512>(lds + SLOT((QW ? 0 : 2) + 4 * rd + i) + (toff ^ 32));
    }
    asm volatile("" : "+v"(f[0][0]), "+v"(f[0][1]), "+v"(f[1][0]), "+v"(f[1][1]));
    LBARRIER();
    #pragma unroll
    for (int i = 0; i < 2; ++i)
      #pragma unroll
      for (int t = 0; t < 2; ++t) {
        LAS unsigned char* p = lds + SLOT((QW ? 0 : 2) + 4 * rd + i) + j * 4096 + t * 2048 + lane * 16;
        *(LAS u32x4*)p = (u32x4){f[i][t][0], f[i][t][1], f[i][t][2], f[i][t][3]};
        *(LAS u32x4*)(p + 1024) = (u32x4){f[i][t][4], f[i][t][5], f[i][t][6], f[i][t][7]};
      }
  }
  const unsigned mask = rec_go(lds + LA_P + 128, P, f0, wid, lane);
  const int idx = (lane & 1) ? 0xEEEE : 0x4444;
  const unsigned ab_a = lbase + LA_MEM + lane * 16, am_a = ab_a + j * 1024;
  const unsigned t0_a = lbase + SLOT((QW ? 0 : 4) + j) + j * 4096 + lane * 16;
  const unsigned iw_a = lbase + (QW ? LA_Q : LA_K) + j * 512 + q4 * 128 + g * 32 + p4 * 4;
  const unsigned sw_a = lbase + LA_KS + (j >> 1) * 1024 + ((j & 1) * 2 + (q4 >> 1)) * 256 + (4 * g + (p4 & 1)) * 16 + ((q4 & 1) * 2 + (p4 >> 1)) * 4;
  const unsigned qr_a = lbase + LA_Q + g * 128 + (lane & 7) * 16;
  const unsigned kr_a = lbase + LA_K + g * 128 + (2 * q4 + (lane & 1)) * 16;
  const unsigned fr_a = lbase + LA_P + p4 * 4;
  if (tid < 12) *(LAS unsigned*)(lds + LA_P + tid * 4) = 0u;
  const unsigned vo_v = VB_AT(h) + j * 512 + lane * 4, md = lbase + LA_VM - VB_AT(h);
  const unsigned oo_v = OB_AT((lane >> 2) & 1, h) + (j * 16 + (g >> 1) * 8 + p4 * 2 + (g & 1)) * 16;
  const unsigned long long onej = 1ull << j;
  const unsigned lkx = lbase + LA_KX + j * 2048;
  u32x8 oxr = {};
  const unsigned xo_v = (lane >> 5) * 2048 + (lane & 31) * 16, vl_a = lbase + LA_VM + (j & 1) * 1024 + lane * 4;
  const int dq = 16 * (j >> 1) + (lane & 15) - 4 * g;
  const unsigned fb = step_base(cb, mask, 0);
  unsigned tgt = (unsigned)__builtin_amdgcn_s_memrealtime() * 4u;
  unsigned long long tp = 0;
  unsigned pt = 0, pd = 0;
  const unsigned long long fmask = __builtin_amdgcn_readfirstlane((16u >> wid) & 1u);
  int known = 0;
  __builtin_amdgcn_s_setprio(QW ? 2 : 1);
  LBARRIER();
  report(P, wid);
  TL(72);
  for (int s = 0; s < NSEG; ++s) {
    const unsigned sb = step_base(cb, mask, s);
    const unsigned sbm = sb - (lbase + LA_MEM);
    const unsigned fo = (unsigned)(s * NH + h) * 8192u + j * 2048 - (lbase + LA_MEM);
    GSDECL;
    RTC(h);
    lim = __builtin_amdgcn_readfirstlane(lim);
    if (QW) {
      if (wid == 3 && s > 0) {
        if (opq(0) == 0) __builtin_amdgcn_raw_buffer_store_b32((unsigned)tp * 4u - tgt, rs, 0, fb + FL_O(h), AUX_SC1);
        plan_wait(rs, fb + FL_T(s), s, lim, pt, pd);
        tgt = pt;
      }
      if (s >= known) {
        const u32x2 v = *(volatile LAS u32x2*)(lds + LA_P + 640 + (opq(0) & 3) * 8);
        if (!__any(v.y != P.tag)) known = rfl(min(min(__builtin_amdgcn_readlane(v.x, 0), __builtin_amdgcn_readlane(v.x, 1)), __builtin_amdgcn_readlane(v.x, 2)));
      }
      kv_wait(P, s, h, known, opq(0));
      const unsigned kxo = WS_KF + fo, vxo = WS_VF + fo;
      if (wid < 3) for (int i = 0; i < ADL; ++i) __builtin_amdgcn_s_sleep(1);
      {
      dmas(rs, ab_a, kxo, lkx);
      dmas(rs, ab_a, kxo + 1024, lkx + 1024);
      dmas(rs, ab_a, WS_QF + fo, lbase + LA_P + 1024);
      dmas(rs, ab_a, WS_QF + fo + 1024, lbase + LA_P + 1024);
      }
      if (wid == 2 && s + 1 >= known) {
        const int ln = opq(0);
        const unsigned fv = FLG_OFF + F_PRG(min((ln >> 1) & 3, 2), h) * 128 + (ln & 1) * 4;
        asm volatile("s_mov_b32 m0, %2\n\ts_nop 0\n\tbuffer_load_dword %0, %1, 0 offen sc1 lds" :: "v"(fv), "s"(rs), "s"(lbase + LA_P + 640) : "memory");
      }
      unsigned rg = 0x3c000000u;
      A_MAIN_Q();
      TL(s);
      GSFLUSH(0, 24 + wid); GSFLUSH(1, 4 + wid); GSFLUSH(2, 12 + j); GSFLUSH(3, 16 + j); GSFLUSH(4, 20 + j);
      if (s + PD < NSEG && !(opq(0) & 8)) __builtin_amdgcn_raw_buffer_store_b128(poison4(), rs, oo_v, step_base(cb, mask, s + PD), AUX_SC1);
    } else {
      const unsigned qxo = WS_QF + fo - (j & 1) * 2048, oxl = h * 8192 + (j & 1) * 4096 + (j >> 1) * 1024;
      const unsigned oxo = (s == 0 || s == NSEG - 1 ? WS_DUM : WS_OX + (unsigned)(s - 1) * 262144u) + oxl;
      A_MAIN_K();
      if (s >= NSEG - 2) {
        __builtin_amdgcn_raw_buffer_store_b128((u32x4){oxr[0], oxr[1], oxr[2], oxr[3]}, rs, xo_v, WS_OX + (unsigned)s * 262144u + oxl, AUX_SC1);
        __builtin_amdgcn_raw_buffer_store_b128((u32x4){oxr[4], oxr[5], oxr[6], oxr[7]}, rs, xo_v + 512, WS_OX + (unsigned)s * 262144u + oxl, AUX_SC1);
      }
      if (wid == 7) GSFLUSH(0, 28);
      GSFLUSH(1, 4 + wid);
    }
  }
  VMWAIT();
  BARRIER();
  if (wid == 4) flag_set(P, F_PRG(4, h), NSEG, opq(0));
}

#define TSL 4
#define LB_MEM 0
#define LB_RED 16384
template <bool VW>
__device__ __forceinline__ void b_wave(LAS unsigned char* lds, Ptrs& P, int r, int c, int wid, int bs, int xg) {
  constexpr int NHD = VW ? 2 : 6;
  char* ws = P.ws;
  const int lane = opq(0), tid = wid * 64 + lane;
  const int j = wid & 3, g = lane >> 4, q4 = (lane & 15) >> 2, p4 = lane & 3;
  const int b = r * 32 + c;
  const rsrc_t rs = WS_RSRC(P);
  const rsrc_t rv = __builtin_amdgcn_make_buffer_rsrc((void*)P.Wv, 0, HD * DM * 2, RSRC_W3);
  const rsrc_t ro = __builtin_amdgcn_make_buffer_rsrc((void*)P.Wo, 0, HD * DM * 2, RSRC_W3);
  const unsigned lbase = (unsigned)(size_t)lds;
  int lim = SPIN_BUDGET;
  const int h0 = ((VW ? 2 * j : 8 + 6 * j) + 5 * c) & 31;
  u32x8 wv[16], wo[6][2][2];
  {
    if ((wid & 3) < 2 + MAP && xg < 2) {
      const unsigned nk = 8 + 2 * xg, M = (bs * (2 + MAP) + (wid & 3)) * 64 + lane, row = M / nk, k = M - row * nk;
      if (row < HELP) touch(VW ? P.Wq : P.Wk, HD * DM * 2, (row < 256 ? row + (VW ? 512 : 256) : row + 512) * (HD * 2) + min(xg * 4096 + (k >> 1) * 1024 + 256, 7936u) + (k & 1) * 128, lds);
    }
    const int nl = MAP ? (xg == 2 ? 8 : 4) : xg == 2 ? 6 : 5;
    #pragma unroll
    for (int i = 0; i < 2; ++i) {
      const unsigned L = bs * 1024 + wid * 128 + i * 64 + lane, row = L / nl;
      touch(P.Wo, HD * DM * 2, row * (DM * 2) + (c - (bs >> 1)) * 64 + (L - row * nl) * 128, lds);
    }
    VMWAIT();
    BARRIER();
    const int pos = lane >> 2;
    const int prow = (pos & 9) | ((pos & 2) << 1) | ((pos & 4) >> 1);
    const unsigned dv0 = (unsigned)(prow * (DM * 2) + c * 64 + ((lane & 3) << 4));
    const unsigned dv1 = (unsigned)(prow * (DM * 2) + c * 64 + (((lane & 3) ^ 2) << 4));
    const int toff = wid * 16384 + (g * 16 + q4) * 64 + (((p4 >> 1) ^ ((g & 1) << 1)) << 4) + 8 * (p4 & 1);
    #pragma unroll
    for (int rd = 0; rd < NHD / 2; ++rd) {
      #pragma unroll
      for (int hh = 0; hh < 2; ++hh) {
        const int h = (h0 + 2 * rd + hh) & 31;
        #pragma unroll
        for (int i = 0; i < 8; ++i)
          DMA_O(ro, (i & 1) ? dv1 : dv0, (unsigned)(h * 128 + i * 16) * (DM * 2), lbase + wid * 16384 + hh * 8192 + i * 1024);
      }
      VMWAIT();
      #pragma unroll
      for (int hh = 0; hh < 2; ++hh)
        #pragma unroll
        for (int ks = 0; ks < 2; ++ks)
          #pragma unroll
          for (int t = 0; t < 2; ++t)
            wo[2 * rd + hh][ks][t] = rd_frag<4096, 256>(lds + (toff ^ (t * 32)) + hh * 8192 + ks * 512);
      #pragma unroll
      for (int hh = 0; hh < 2; ++hh)
        WO_PIN(VW, 2 * rd + hh);
      asm volatile("s_waitcnt lgkmcnt(0)" ::: "memory");
    }
    const int lr = lane >> 3;
    #pragma unroll
    for (int i = 0; i < 16; ++i) {
      const int sw = (((lr >> 2) & 1) | ((i & 1) << 1)) << 1;
      const unsigned dv = (unsigned)(lr * (HD * 2) + b * 128 + (((lane & 7) ^ sw) << 4));
      DMA_V(rv, dv, (unsigned)(wid * 128 + i * 8) * (HD * 2), lbase + wid * 16384 + i * 1024);
    }
    VMWAIT();
    BARRIER();
    if (VW) {
      const int trow = 16 * (g >> 1) + 4 * (g & 1) + 8 * (q4 >> 1) + (q4 & 1);
      const int tsw = ((g & 1) | ((q4 >> 1) << 1)) << 1;
      LAS unsigned char* tv = lds + trow * 128 + (((2 * j + (p4 >> 1)) ^ tsw) << 4) + 8 * (p4 & 1);
      #pragma unroll
      for (int ks = 0; ks < 16; ++ks) wv[ks] = rd_frag<4096, 256>(tv + ks * 8192);
      V_PIN();
    }
    LBARRIER();
  }
  const unsigned cb = comm_base(ws);
  get_tag(P);
  if (wid == 0) touch(P.x, SEQ * DM * 2, (SEQ - 64 + lane) * 2048 + c * 64, lds);
  const unsigned mask = rec_go(lds + LB_RED + 4096 + 64, P, (u32x2){0u, P.tag - 1u}, wid, lane);
  const int idx = (lane & 1) ? 0xEEEE : 0x4444;
  const unsigned am_a = lbase + LB_MEM + lane * 16;
  const unsigned fb = step_base(cb, mask, 0);
  volatile LAS unsigned* tsl = (volatile LAS unsigned*)(lds + LB_RED + 4096);
  if (tid < 8) tsl[tid] = tid ? 0u : (unsigned)__builtin_amdgcn_s_memrealtime() * 4u;
  unsigned dn = 600;
  int da = 560 * 16, gb = 350 * 16;
  if (VW) __builtin_amdgcn_s_setprio(2);
  LBARRIER();
  report(P, wid);
  TL(72);
  for (int s = 0; s < NSEG; ++s) {
    const unsigned sb = step_base(cb, mask, s);
    const int ln = opq(lane), gg = ln >> 4;
    GSDECL;
    RTC(32 + b);
    const int l16 = ln * 16;
    const int xc = 32 * (c & 1) + 2 * (ln & 15);
    const unsigned mo_v = MF_AT(c >> 1) + ((xc >> 4) * 16 + (4 * r + gg) * 2 + ((xc >> 3) & 1)) * 16 + (xc & 7) * 2;
    if (!VW) {
      const unsigned tm = __builtin_amdgcn_readfirstlane(*tsl), tgt = tm + DLB, la = lbase + LB_MEM + j * 2048 + l16, mo = sb + j * 2048;
      B_POLL();
      if (wid == 7) {
        const int late = (int)((unsigned)__builtin_amdgcn_s_memrealtime() * 4u - tgt) - SN0;
        const unsigned to = tm + dn + (late > 80 ? late : 0);
        if (ln == 0) tsl[1] = to;
        if (b == 0) {
          dn = (da >> 4) + DL1;
          if (ln == 0) { *tsl = to + (gb >> 4) + DL2; st8(rs, fb + FL_T(s + 1), (u32x2){to + (gb >> 4) + DL2, (dn << 8) | (s + 1)}); }
          const unsigned va = ld4(rs, fb + FL_O(ln & 31)), vb = ld4(rs, fb + FL_B(ln));
          if (s > 2) {
            track(da, rfl(wmax(va)), s < 8 ? 5 : DCA);
            track(gb, rfl(wmax(vb)), s < 8 ? 5 : DCA);
          }
        }
      }
      if (wid == 7 && s > 0 && s + 2 < NSEG) __builtin_amdgcn_raw_buffer_store_b32(~0u, rs, mo_v, step_base(cb, mask, s + 2), AUX_SC1);
    } else {
      GS(3);
      const unsigned vo = VB_AT(b >> 1) + ((b & 1) * 4 + j) * 256;
      V_PROJ();
      GS(0); GSFLUSH(0, 4 + j);
      if (s + PD < NSEG && ln < 16) st16(rs, step_base(cb, mask, s + PD) + vo + l16, poison4());
    }
    GSFLUSH(3, 24 + wid);
    if (s + 1 == NSEG) break;
    unsigned so[6];
    #pragma unroll
    for (int i = 0; i < NHD; ++i) { const int h = (h0 + i) & 31; so[i] = __builtin_amdgcn_readfirstlane(sb + OB_AT(r, h)); }
    const unsigned sn = __builtin_amdgcn_readfirstlane(step_base(cb, mask, s + 1));
    const unsigned rw_a = lbase + LB_RED + (wid * 128 + (gg & 1) * 64 + (gg >> 1) * 16 + (ln & 15)) * 4;
    const unsigned rr_a = lbase + LB_RED + ln * 8;
    if (wid == 7) {
      if (b != 0) {
        __builtin_amdgcn_s_sleep(4);
        unsigned tm = 0;
        dn = 0;
        plan_wait(rs, fb + FL_T(s + 1), s + 1, lim, tm, dn);
        if (ln == 0) *tsl = tm;
      }
      const unsigned tgt = rfl(tsl[1]);
      T_WAIT();
    }
    lim = __builtin_amdgcn_readfirstlane(lim);
    unsigned long long t1;
    if (VW) WO_V(); else WO_W();
    if (wid == 0) {
      if (ln == 0) st4(rs, fb + FL_B(b), (unsigned)t1 * 4u - tsl[1]);
    }
    if (wid == 0) {
      GSFLUSH(2, 16);
      TL(s);
    }
    GSFLUSH(1, 8 + wid);
    if (wid == 0 || wid == 4) { GSFLUSH(4, 17 + (wid >> 2)); GSFLUSH(0, 19 + (wid >> 2)); }
  }
  __builtin_amdgcn_s_setprio(0);
  const int ln = opq(0);
  TL(64);
  volatile LAS int* rdy = (volatile LAS int*)(lds + 98304 + 3 * 16384);
  if (tid == 0) *rdy = 61;
  LBARRIER();
#define T_SRC(sg, i) (WS_OX + (unsigned)((sg) * NH + ((h0 + (i)) & 31)) * 8192u + r * 1024)
#define T_MMA(i, k, f) do { const u32x8 w0 = wo[i][(k) & 1][0], w1 = wo[i][(k) & 1][1]; const int e = ((k) >> 1) * 4; \
    a0 = MFMA32(((u32x4){w0[e], w0[e + 1], w0[e + 2], w0[e + 3]}), f, a0); a1 = MFMA32(((u32x4){w1[e], w1[e + 1], w1[e + 2], w1[e + 3]}), f, a1); } while (0)
#define T_DMA(sg, i) do { _Pragma("unroll") for (int k = 0; k < 4; ++k) dmas(rs, ln * 16, T_SRC(sg, i) + k * 2048, lbase + j * 24576 + (i) * 4096 + k * 1024); } while (0)
  const int cb2 = 16 * ((ln >> 4) & 1) + 8 * (ln >> 5);
  const unsigned eo = (unsigned)((NSEG - 1 - j) * 32 + r * 16 + (ln & 15)) * 2048u + (unsigned)(c * 32 + cb2) * 2u;
  u32x4 gv, xv;
  if (VW && wid < NSEG - XS3) {
    xv = *(const u32x4*)((const char*)P.x + eo);
    cnt_wait(P, C_W4(15), 4, ln);
    gv = ld16(rs, WS_G + (unsigned)((NSEG - 1 - j) * 2 + r) * 32768u + c * 1024 + blk_in(ln & 15, cb2));
  }
  bool ahead = false;
  f32x4 k0, k1;
  #pragma unroll 1
  for (int sg = XS3; sg < NSEG; ++sg) {
    f32x4 a0 = zero4(), a1 = zero4();
    if (VW) {
      if (wid == 3) {
        for (int w = __builtin_amdgcn_readfirstlane(*rdy); w <= sg;) {
          w = max(w + 1, -wmax(-(int)flag_wait<TSL>(P, F_PRG(4, ln & 31), w + 1, ln)));
          if (ln == 0) *rdy = w;
        }
      } else while (__builtin_amdgcn_readfirstlane(*rdy) <= sg) __builtin_amdgcn_s_sleep(1);
      u32x4 of[8];
      #pragma unroll
      for (int i = 0; i < 8; ++i) of[i] = __builtin_amdgcn_raw_buffer_load_b128(rs, ln * 16 + (i & 3) * 2048, T_SRC(sg, i >> 2), AUX_SC1);
      #pragma unroll
      for (int i = 0; i < 8; ++i) T_MMA(i >> 2, i & 3, of[i]);
    } else {
      if (!ahead) {
        while (__builtin_amdgcn_readfirstlane(*rdy) <= sg) __builtin_amdgcn_s_sleep(1);
        #pragma unroll
        for (int i = 0; i < NHD; ++i) T_DMA(sg, i);
      }
      VMW(20);
      ahead = __builtin_amdgcn_readfirstlane(*rdy) > sg + 1;
      u32x4 of[2][4];
#define T_RD(i) do { _Pragma("unroll") for (int k = 0; k < 4; ++k) of[(i) & 1][k] = *(const LAS u32x4*)(lds + j * 24576 + (i) * 4096 + k * 1024 + ln * 16); } while (0)
      T_RD(0);
      #pragma unroll
      for (int i = 0; i < NHD; ++i) {
        asm volatile("s_waitcnt lgkmcnt(0)" : "+v"(of[i & 1][0]), "+v"(of[i & 1][1]), "+v"(of[i & 1][2]), "+v"(of[i & 1][3]) :: "memory");
        if (i + 1 < NHD) {
          if (ahead) VMW(16); else G_WAIT_V(16 - 4 * (i < 4 ? i : 4));
          T_RD(i + 1);
        }
        #pragma unroll
        for (int k = 0; k < 4; ++k) {
          if (ahead) dmas(rs, ln * 16, T_SRC(sg + 1, i) + k * 2048, lbase + j * 24576 + i * 4096 + k * 1024);
          T_MMA(i, k, of[i & 1][k]);
          __builtin_amdgcn_sched_barrier(0);
        }
      }
    }
    LAS f32x4* pw = (LAS f32x4*)(lds + 98304 + ((sg & 3) * 8 + wid) * 2048) + ln;
    if (wid == 0 && sg == NSEG - 1) { k0 = a0; k1 = a1; } else { pw[0] = a0; pw[64] = a1; }
  }
  if (VW && wid < NSEG - XS3) {
    LBARRIER();
    const LAS f32x4* pr = (const LAS f32x4*)(lds + 98304 + (3 - j) * 16384) + ln;
    f32x4 t[2];
    #pragma unroll
    for (int k = 0; k < 2; ++k)
      t[k] = (((wid ? pr[k * 64] : k ? k1 : k0) + pr[128 + k * 64]) + (pr[256 + k * 64] + pr[384 + k * 64])) + ((pr[512 + k * 64] + pr[640 + k * 64]) + (pr[768 + k * 64] + pr[896 + k * 64]));
    fin_out(P, eo, epi_pack(t[0], t[1]), gv, xv);
  } else LBARRIER();
}

#define DMA_S dman
#define XDL 4
#define XPC 0
#define PW3 11
#define PW4 25
#define SDL 0
#define LS_P 131072
template <int MODE>
__device__ __forceinline__ void s_role(LAS unsigned char* lds, Ptrs& P, const u16* W, int h, unsigned obase, int nseg, int fl, int wid, int s0) {
  constexpr bool XM = MODE >= 2;
  constexpr unsigned RS = XM ? DM * 2 : HD * 2, SS = XM ? 262144u : 65536u;
  const int lane = opq(0);
  const int kh = wid >> 2, tp = wid & 3;
  const rsrc_t rs = WS_RSRC(P);
  const rsrc_t rw = __builtin_amdgcn_make_buffer_rsrc((void*)W, 0, HD * DM * 2, RSRC_W3);
  const unsigned lbase = (unsigned)(size_t)lds;
  u32x8 w[8][2];
  unsigned f0 = 0;
  if (!XM) for (int i = 0; i < SDL; ++i) __builtin_amdgcn_s_sleep(32);
  if (XM && XDL) {
    if (wid == 0) flag_wait(P, F_PRG(4, h), max(s0 - XDL, 0), lane);
    BARRIER();
  }
  {
    const int g = lane >> 4, q4 = (lane & 15) >> 2, p4 = lane & 3;
    const int trow = XM ? 16 * g + 4 * (q4 >> 1) + (q4 & 1) : 8 * g + q4;
#define S_SW(row) ((((row) & 1) | ((((row) >> (XM ? 2 : 1)) & 1) << 1) | ((((row) >> (XM ? 4 : 3)) & 1) << 2)) << 1)
    const int toff = trow * 256 + (((4 * tp + (p4 >> 1)) ^ S_SW(trow)) << 4) + 8 * (p4 & 1);
    unsigned dv[2];
    #pragma unroll
    for (int x = 0; x < 2; ++x) {
      const int row = 8 * wid + 4 * x + (lane >> 4);
      dv[x] = (unsigned)row * RS + (XM ? (h >> 3) * 1024 * RS + (h & 7) * 256 : h * 256) + (((lane & 15) ^ S_SW(row)) << 4);
    }
#define S_IMG(i) do { DMA_S(rw, dv[0], (i) * 64 * RS, lbase + ((i) & 7) * 16384 + wid * 2048); \
                      DMA_S(rw, dv[1], (i) * 64 * RS, lbase + ((i) & 7) * 16384 + wid * 2048 + 1024); if (XM && XPC) __builtin_amdgcn_s_sleep(XPC); } while (0)
    #pragma unroll
    for (int i = 0; i < 8; ++i) S_IMG(i);
    #pragma unroll
    for (int b = 0; b < 4; ++b) {
      if (b == 3 && !XM) { f0 = ld4(rs, FLG_OFF + F_LN0(lane & 7) * 128 + 4); get_tag(P); }
      if (b < 3) VMW(8); else VMWAIT();
      BARRIER();
      if (kh == (b >> 1)) {
        #pragma unroll
        for (int i = 4 * (b & 1); i < 4 * (b & 1) + 4; ++i) {
          w[i][0] = rd_frag<XM ? 2048 : 8192, XM ? 512 : 1024>(lds + toff + i * 16384);
          w[i][1] = rd_frag<XM ? 2048 : 8192, XM ? 512 : 1024>(lds + (toff ^ 32) + i * 16384);
        }
      }
      LBARRIER();
      if (b < 2) {
        #pragma unroll
        for (int i = 4 * b + 8; i < 4 * b + 12; ++i) S_IMG(i);
      }
    }
  }
  TL(XM ? 74 : 72);
  const unsigned l16 = lane * 16;
  volatile LAS int* rd = (volatile LAS int*)(lds + LS_P + 20480);
#define X_READY(want) do { \
    if (wid == 0) { \
      const int n_ = -wmax(-(int)flag_wait(P, F_PRG(4, (h & 24) + (lane & 7)), s0 + (want), lane)) - s0; \
      if (lane == 0) *rd = max(n_, want); \
    } \
    LBARRIER(); } while (0)
  if (MODE == 2) X_READY(2);
  else if (!XM) {
    if (__any(f0 != P.tag)) flag_wait(P, F_LN0(lane & 7), 0, lane);
  }
  unsigned sx = (XM ? WS_OX + s0 * SS + (h >> 3) * 65536 : WS_XN) + kh * 32768 + tp * 4096;
  const unsigned sd = lbase + kh * 65536 + tp * 4096;
  const unsigned xl = kh * 65536 + (XM ? l16 : blk_in(lane & 15, (lane >> 4) * 8));
  const unsigned xp = lbase + xl, pa = lbase + LS_P + tp * 4096 + l16;
  LAS f32x4* const pq = (LAS f32x4*)(lds + LS_P + tp * 4096 + l16);
  if (MODE == 3) {
#define X_GET(t) do { _Pragma("unroll") for (int i = 0; i < 16; ++i) \
      dmas(rs, l16, sx + (t) * SS + ((i >> 2) & 1) * 32768 + (i >> 3) * 16384 + (i & 3) * 1024, sd + ((i >> 2) & 1) * 65536 + ((t) & 1) * 32768 + (i >> 3) * 16384 + (i & 3) * 1024); } while (0)
    bool have = false;
    const unsigned pv = FLG_OFF + F_PRG(kh ? 7 : 4, kh ? h : (h & 24) + (lane & 7)) * 128;
    if (!kh) __builtin_amdgcn_s_setprio(1);
    #pragma unroll 1
    for (int t = 0; t < nseg; ++t) {
      if (!have && !kh) {
        flag_wait<2>(P, F_PRG(4, (h & 24) + (lane & 7)), s0 + t + 1, lane);
        X_GET(t);
        VMW(8);
      }
      BARRIER();
      f32x4 a[4];
      unsigned pg;
      ONE_X(xp + (t & 1) * 32768);
      if (wid == 5) flag_set(P, fl, t, lane);
      have = t + 1 < nseg && !__any(pg < (unsigned)(s0 + t + 2));
      if (!kh && lane == 0) rd[tp] = have;
      pq[kh * 128] = kh ? a[0] : a[2]; pq[kh * 128 + 64] = kh ? a[1] : a[3];
      if (have && !kh) X_GET(t + 1);
      LBARRIER();
      const unsigned oo = obase + (unsigned)(s0 + t) * 0x40000u + tp * 4096 + kh * 2048 + l16;
      st16(rs, oo, __builtin_bit_cast(u32x4, (kh ? a[2] : a[0]) + pq[128 - kh * 128]));
      st16(rs, oo + 1024, __builtin_bit_cast(u32x4, (kh ? a[3] : a[1]) + pq[192 - kh * 128]));
      if (__any(rd[lane & 3] == 0)) {
        VMWAIT();
        BARRIER();
        if (wid == 5) flag_set(P, fl, t + 1, lane);
      } else if (!kh) VMW(10);
      asm volatile("s_waitcnt lgkmcnt(0)" ::: "memory");
      TL(t);
    }
    __builtin_amdgcn_s_setprio(0);
    TL(65);
    return;
  }
  if (XM) {
    #pragma unroll
    for (int i = 0; i < 12; ++i) dmas(rs, l16, sx + (i >> 3) * SS + ((i >> 2) & 1) * 16384 + (i & 3) * 1024, sd + (i >> 2) * 16384 + (i & 3) * 1024);
    VMWAIT();
    BARRIER();
  } else {
    #pragma unroll
    for (int i = 0; i < 16; ++i) dmas(rs, l16, sx + (i >> 3) * SS + ((i >> 2) & 1) * 16384 + (i & 3) * 1024, sd + ((i >> 2) ^ 2) * 16384 + (i & 3) * 1024);
    VMW(12);
    BARRIER();
    f32x4 a[4];
    if (MODE) ONE_V(xp + 32768); else ONE_K(xp + 32768);
    pq[kh * 128] = kh ? a[0] : a[MODE ? 1 : 2]; pq[kh * 128 + 64] = kh ? a[MODE ? 2 : 1] : a[3];
    LBARRIER();
    const f32x4 a0 = (kh ? a[MODE ? 1 : 2] : a[0]) + pq[128 - kh * 128], a1 = (kh ? a[3] : a[MODE ? 2 : 1]) + pq[192 - kh * 128];
    if (MODE) st16(rs, obase + h * 8192 + (2 * tp + kh) * 1024 + l16, (u32x4){pack2(a0[0], a0[1]), pack2(a0[2], a0[3]), pack2(a1[0], a1[1]), pack2(a1[2], a1[3])});
    else st16(rs, obase + h * 8192 + kh * 4096 + tp * 1024 + l16, (u32x4){pack2(a0[0], a1[0]), pack2(a0[1], a1[1]), pack2(a0[2], a1[2]), pack2(a0[3], a1[3])});
    sx += SS;
    #pragma unroll
    for (int i = 0; i < 4; ++i) dmas(rs, l16, sx + SS + i * 1024, sd + 32768 + i * 1024);
    VMW(4);
    BARRIER();
    if (wid == 1) flag_set(P, fl, 1, lane);
    TL(62);
  }
  u32x8 xf[3], ac[2] = {};
  #pragma unroll
  for (int i = 0; i < 3; ++i) {
    const u32x4 a = *(const LAS u32x4*)(lds + xl + i * 2048), b = *(const LAS u32x4*)(lds + xl + i * 2048 + 1024);
    xf[i] = (u32x8){a[0], a[1], a[2], a[3], b[0], b[1], b[2], b[3]};
  }
  unsigned long long fm, pm;
  asm volatile("s_cmp_eq_u32 %2, 1\n\ts_cselect_b64 %0, 1, 0\n\ts_cmp_eq_u32 %3, 1\n\ts_cselect_b64 %1, -1, 0" : "=&s"(fm), "=&s"(pm) : "s"(wid), "s"(XM ? 0 : wid >> 1) : "scc");
  const unsigned fo = FLG_OFF + fl * 128, pd = lbase + LS_P + 16384;
  const unsigned po = 4 * 65536 + h * 2048 + (wid & 1) * 1024 - tp * 4096 - kh * 32768;
  unsigned oo = XM ? obase + tp * 4096 + (s0 - 1) * 0x40000u : obase + (unsigned)h * 8192u + tp * (MODE ? 2048 : 1024), tmp, tmp2;
  unsigned long long em;
  int t = XM ? 0 : 1;
  if (MODE < 2) {
    #pragma unroll 1
    for (int part = 0; part < 4; ++part) {
      if (part == 1) { TL(63); if (wid == 0) setup_wait(P, lane); }
      if (MODE == 1 && part >= 2) {
        #pragma unroll
        for (int i = 0; i < 4; ++i) {
          const unsigned to = ((i * 32 + h) * 512 + wid * 64 + opq(0)) * 128;
          if (part == 2) touch(P.w3, DM * FF * 2, to, lds); else touch(P.w4, DM * FF * 2, to, lds);
        }
      }
      const int te = part == 0 ? 3 : part == 1 ? PW3 : part == 2 ? PW4 : nseg + 1;
      if (MODE) S_LOOP_V(te, 0); else S_LOOP_K(te, 0);
    }
  } else {
    #pragma unroll 1
    while (t < nseg) {
      X_READY(min(t + 4, nseg));
      const int n = __builtin_amdgcn_readfirstlane(*rd), te = n >= nseg ? nseg : (n - 2) & ~1;
      TL(t);
      S_LOOP_X(te, nseg);
    }
  }
  TL(XM ? 65 : 64);
  VMWAIT();
  BARRIER();
  if (wid == 1) flag_set(P, fl, nseg, lane);
}

#define LND 1
#define CALN 6
__device__ __forceinline__ void setup_role(LAS unsigned char* lds, Ptrs& P, int cu, int wid) {
  const int lane = opq(0), tid = wid * 64 + lane;
  const rsrc_t rs = WS_RSRC(P);
  u32x4 v[8];
  const u32x4* px = (const u32x4*)(P.x + (size_t)(cu * 32 + wid * 4 + (lane >> 4)) * DM) + (lane & 15);
  #pragma unroll
  for (int i = 0; i < 8; ++i) v[i] = __builtin_nontemporal_load(px + i * 16);
  const unsigned epo = __hip_atomic_load((gu32*)(P.ws + EPO_OFF), RLX_AGENT);
  const unsigned cb = comm_base(P.ws);
  const u32x4 z = {0u, 0u, 0u, 0u};
  const int cr = 63 - cu;
  if (wid == 0 && cr < 16) st16(rs, CNT_OFF + cr * 1024 + lane * 16, z);
  if (cr < 4) st16(rs, cb + FL_B(0) + cr * 8192 + tid * 16, z);
  if (cr == 4 && tid < 256) {
    const int g4 = tid >> 6, k = (tid >> 5) & 1 & (g4 >> 1);
    st16(rs, WS_H1 + ((g4 & 1) ? 127u * 32768u : 0u) + (g4 * 8 + ((tid >> 2) & 7)) * 1024 + blk_in((g4 & 1) ? 15 - k : k, (tid & 3) * 8), z);
  }
  #pragma unroll
  for (int i = 0; i < 2; ++i) {
    const int f = i * 54 + cr;
    if (cr < PD * NCHUNK) {
      u32x4 m = {~0u, ~0u, ~0u, ~0u};
      if (f < 4) {
        const int ks = (f >> 1) * 8 + wid;
        m = *(const u32x4*)(P.mem0 + ((lane >> 1) & 7) * DM + 64 * ks + 16 * (lane >> 4) + 8 * (lane & 1));
        m.x = canon1(m.x); m.y = canon1(m.y); m.z = canon1(m.z); m.w = canon1(m.w);
      }
      st16(rs, cb + (unsigned)(f / (NCHUNK * 2)) * CB_STEP + (f % (NCHUNK * 2)) * 8192u + tid * 16, m);
    }
  }
  const unsigned bi = blk_in((wid & 3) * 4 + (lane >> 4), (lane & 3) * 8);
  const int c0 = (lane >> 2) & 3;
  ln_rows(v);
  TL(69);
  if (cu >= 8) for (int i = 0; i < LND; ++i) __builtin_amdgcn_s_sleep(32);
  #pragma unroll
  for (int i = 0; i < 8; ++i) st16(rs, WS_XN + xn_blk(cu * 2 + (wid >> 2), 4 * i + c0) + bi, v[i]);
  P.tag = __builtin_amdgcn_readfirstlane(epo) + 1u;
  ln_rows(v);
  if (cu < 8) {
    VMWAIT();
    BARRIER();
    if (wid == 0) flag_set(P, F_LN0(cu), 1, lane);
    TL(66);
  }
  asm volatile("" ::: "memory");
  #pragma unroll
  for (int i = 0; i < 8; ++i) st16(rs, WS_XN2 + (unsigned)(cu * 2 + (wid >> 2)) * 32768u + (4 * i + c0) * 1024 + bi, v[i]);
  touch(P.w1, DM * DM * 2, ((cu & 31) * 512 + tid) * 128, lds);
  VMW(9);
  BARRIER();
  if (wid == 0) flag_set(P, F_LN0(cu), 1, lane);
  TL(70);
  if (wid == 0) VMW(2); else VMW(1);
  BARRIER();
  if (wid == 0) flag_set(P, F_LN1(cu), 1, lane);
  TL(71);
  if (cu < 32 && (wid >> 1) == 1) {
    const int id = cu * 2 + (wid & 1);
    unsigned best[2] = {65535u, 65535u};
    #pragma unroll
    for (int it = 0; it < CALN; ++it) {
      const unsigned long long c0 = __builtin_amdgcn_s_memtime();
      asm volatile("s_waitcnt lgkmcnt(0)" ::: "memory");
      if (lane == 0) st4(rs, cb + (id & 3) * CB_STEP + (30 + ((id >> 2) & 1)) * 16384u + (it & 1) * 8192u + (id >> 3) * 1024u, 0u);
      VMWAIT();
      const unsigned d = (unsigned)(__builtin_amdgcn_s_memtime() - c0);
      if (it >= 2) best[it & 1] = min(best[it & 1], d);
    }
    flag_set(P, F_CAL(id), (best[0] << 16) | best[1], lane);
  }
}

#define TLBP 0
#define CPF 1
__global__ __launch_bounds__(512, 2) void mega_kernel(const u16* x, const u16* mem0, const u16* Wq, const u16* Wk, const u16* Wv, const u16* Wo,
                                                     const u16* w1, const u16* w2, const u16* w3, const u16* w4, u16* out, char* ws) {
  __shared__ __attribute__((aligned(16))) unsigned char smem[163840];
  LAS unsigned char* lds = (LAS unsigned char*)smem;
  Ptrs P;
  P.x = x; P.mem0 = mem0; P.Wq = Wq; P.Wk = Wk; P.Wv = Wv; P.Wo = Wo; P.w1 = w1; P.w2 = w2; P.w3 = w3; P.w4 = w4; P.out = out; P.ws = ws;
  const int xcd = blockIdx.x & 7, slot = blockIdx.x >> 3;
  const int wid = __builtin_amdgcn_readfirstlane(threadIdx.x >> 6);
  TL(79);
  P.tag = 0;
  if (wid == 7) {
    unsigned long long pc;
    unsigned len;
    asm volatile("s_getpc_b64 %0\nMK_BEG:\n\ts_mov_b32 %1, MK_END-MK_BEG" : "=s"(pc), "=s"(len));
    touch((const void*)pc, len, (slot * 64 + opq(0)) * 128, lds);
  }
  const int na = xcd == 2 ? 8 : 12;
  const bool isa = xcd < 3 && slot < na;
  const int ah = xcd == 2 ? slot * 4 + 2 : xcd * 16 + (slot & 3) * 4 + (slot >> 2) + (slot >> 3), bs = slot - na, br = bs & 1, bc = (xcd == 1 ? 22 : xcd * 5) + (bs >> 1);
  if (isa) {
    if (wid < 4) a_wave<true>(lds, P, ah, wid); else a_wave<false>(lds, P, ah, wid);
  } else if (xcd < 3) {
    if (wid < 4) b_wave<true>(lds, P, br, bc, wid, bs, xcd); else b_wave<false>(lds, P, br, bc, wid, bs, xcd);
  } else {
    if ((xcd & 3) == 3) {
      setup_role(lds, P, (xcd >> 2) * 32 + slot, wid);
    } else if (xcd == 6) s_role<1>(lds, P, Wv, slot, WS_VF, NSEG, F_PRG(2, slot), wid, 0);
    else s_role<0>(lds, P, xcd == 4 ? Wk : Wq, slot, xcd == 4 ? WS_KF : WS_QF, NSEG, F_PRG(xcd - 4, slot), wid, 0);
    if ((xcd & 3) == 3) mlp_role(lds, P, xcd >> 2, slot, wid);
    else if (xcd == 6) s_role<3>(lds, P, Wo, slot, PT_AT(slot >> 3) + (slot & 7) * 16384, XS3 - XS2, F_PRG(7, slot), wid, XS2);
    else s_role<2>(lds, P, Wo, slot, PT_AT(slot >> 3) + (slot & 7) * 16384, xcd == 4 ? XS1 : XS2 - XS1, F_PRG(xcd + 1, slot), wid, xcd == 4 ? 0 : XS1);
    report(P, wid);
    fin_role(lds, P, wid);
    if (blockIdx.x == 3 && wid == 0) {
      cnt_wait(P, C_ARR, 256, threadIdx.x);
      if (threadIdx.x == 0) __hip_atomic_store((gu32*)(ws + EPO_OFF), P.tag, RLX_AGENT);
    }
  }
  asm volatile("MK_END:");
  TL(78);
}

extern "C" void kernel_launch(void* const* d_in, const int* in_sizes, int n_in, void* d_out, int out_size,
                              void* d_ws, size_t ws_size, hipStream_t stream) {
  mega_kernel<<<256, 512, 0, stream>>>((const u16*)d_in[0], (const u16*)d_in[1], (const u16*)d_in[2], (const u16*)d_in[3], (const u16*)d_in[4],
                                       (const u16*)d_in[5], (const u16*)d_in[8], (const u16*)d_in[9], (const u16*)d_in[10], (const u16*)d_in[11],
                                       (u16*)d_out, (char*)d_ws);
}
```

```cpp
#include <hip/hip_runtime.h>
#include <hip/hip_bf16.h>
#include <stdint.h>

#define SEQ 2048
#define DM 1024
#define NH 32
#define DH 128
#define HD (NH * DH)
#define MM 8
#define SEG 32
#define NSEG (SEQ / SEG)
#define FF 3584

typedef unsigned short u16;
typedef __attribute__((ext_vector_type(8))) short bf16x8;
typedef __attribute__((ext_vector_type(4))) short bf16x4;
typedef __attribute__((ext_vector_type(4))) float f32x4;
typedef __attribute__((ext_vector_type(4))) unsigned u32x4;
typedef __attribute__((ext_vector_type(2))) unsigned u32x2;
typedef __attribute__((ext_vector_type(2))) float f32x2;
typedef __attribute__((ext_vector_type(2))) __bf16 bf16v2;
typedef __attribute__((ext_vector_type(8))) unsigned u32x8;
#define LAS __attribute__((address_space(3)))
#define GAS __attribute__((address_space(1)))

__device__ __forceinline__ unsigned pack2(float lo, float hi) {
  return __builtin_bit_cast(unsigned, __builtin_convertvector((f32x2){lo, hi}, bf16v2));
}
__device__ __forceinline__ float rb1(float x) { return __uint_as_float(pack2(0.f, x)); }

typedef GAS unsigned gu32;
#define RLX_AGENT __ATOMIC_RELAXED, __HIP_MEMORY_SCOPE_AGENT
#define SPIN_CAP (1u << 20)
#define AUX_SC1 16
#define RSRC_W3 0x00020000
typedef __amdgpu_buffer_rsrc_t rsrc_t;

__device__ __forceinline__ u32x4 ld16(rsrc_t rs, unsigned off) { return __builtin_amdgcn_raw_buffer_load_b128(rs, off, 0, AUX_SC1); }
__device__ __forceinline__ u32x2 ld8(rsrc_t rs, unsigned off) { return __builtin_amdgcn_raw_buffer_load_b64(rs, off, 0, AUX_SC1); }
__device__ __forceinline__ unsigned ld4(rsrc_t rs, unsigned off) { return __builtin_amdgcn_raw_buffer_load_b32(rs, off, 0, AUX_SC1); }
__device__ __forceinline__ void st16(rsrc_t rs, unsigned off, u32x4 v) { __builtin_amdgcn_raw_buffer_store_b128(v, rs, off, 0, AUX_SC1); }
__device__ __forceinline__ void st8(rsrc_t rs, unsigned off, u32x2 v) { __builtin_amdgcn_raw_buffer_store_b64(v, rs, off, 0, AUX_SC1); }
__device__ __forceinline__ void st4(rsrc_t rs, unsigned off, unsigned v) { __builtin_amdgcn_raw_buffer_store_b32(v, rs, off, 0, AUX_SC1); }
__device__ __forceinline__ float lo16(unsigned w) { return __uint_as_float(w << 16); }
__device__ __forceinline__ float hi16(unsigned w) { return __uint_as_float(w & 0xffff0000u); }

__device__ __forceinline__ float xsum16_32(float v) {
  unsigned u = __float_as_uint(v);
  auto a = __builtin_amdgcn_permlane16_swap(u, u, false, false);
  v = __uint_as_float(a[0]) + __uint_as_float(a[1]);
  u = __float_as_uint(v);
  auto b = __builtin_amdgcn_permlane32_swap(u, u, false, false);
  return __uint_as_float(b[0]) + __uint_as_float(b[1]);
}

__device__ __forceinline__ unsigned canon1(unsigned w) { return min(w, 0xfffffffeu); }
__device__ __forceinline__ unsigned max4(u32x4 v) { return max(max(v.x, v.y), max(v.z, v.w)); }
__device__ __forceinline__ int opq(int) { int v; asm volatile("v_mbcnt_lo_u32_b32 %0, -1, 0\n\tv_mbcnt_hi_u32_b32 %0, -1, %0" : "=v"(v)); return v; }
__device__ __forceinline__ u32x4 poison4() { unsigned m = ~0u; asm volatile("" : "+v"(m)); return (u32x4){m, m, m, m}; }
__device__ __forceinline__ f32x4 zero4() { float m = 0.f; asm volatile("" : "+v"(m)); return (f32x4){m, m, m, m}; }
#define MFMA32(a, b, c) __builtin_amdgcn_mfma_f32_16x16x32_bf16(__builtin_bit_cast(bf16x8, a), __builtin_bit_cast(bf16x8, b), c, 0, 0, 0)
#define MFMA16(a, b, c) __builtin_amdgcn_mfma_f32_16x16x16bf16_1k(__builtin_bit_cast(bf16x4, a), __builtin_bit_cast(bf16x4, b), c, 0, 0, 0)
#define LBARRIER() do { asm volatile("s_waitcnt lgkmcnt(0)" ::: "memory"); __builtin_amdgcn_s_barrier(); asm volatile("" ::: "memory"); } while (0)
#define BARRIER() do { asm volatile("" ::: "memory"); __builtin_amdgcn_s_barrier(); asm volatile("" ::: "memory"); } while (0)
#define VMWAIT() asm volatile("s_waitcnt vmcnt(0)" ::: "memory")
#define VMW(n) asm volatile("s_waitcnt vmcnt(" #n ")" ::: "memory")
#define DELAY(n) do { if ((n) > 0) __builtin_amdgcn_s_sleep(n); } while (0)

#define EPO_OFF 0
#define TMO_OFF 128
#define FLG_OFF 256
#define F_LN0(i) (i)
#define F_LN1(i) (64 + (i))
#define F_CAL(i) (128 + (i))
#define F_PRG(t, h) (192 + (t) * 32 + (h))
#define CNT_OFF 65536
#define C_W1(p2) (p2)
#define C_W2(p2) (16 + (p2))
#define C_LN(p2) (32 + (p2))
#define C_W3(p2) (48 + (p2))
#define C_W4(p2) (64 + (p2))
#define C_HEAD 80
#define C_ARR 81
#define NCNT 128
#define WS_XN (1u << 20)
#define WS_XN2 (WS_XN + (4u << 20))
#define WS_QF (WS_XN2 + (4u << 20))
#define WS_KF (WS_QF + (16u << 20))
#define WS_VF (WS_KF + (16u << 20))
#define WS_DUM (WS_VF + (16u << 20))
#define WS_H1 (WS_DUM + (1u << 20))
#define WS_X1 (WS_H1 + (4u << 20) + 65536u)
#define WS_H3 (WS_X1 + (4u << 20))
#define WS_OX WS_QF
#define WS_X1N WS_H1
#define WS_G WS_XN2
#define WS_PT WS_KF
#define WS_COMM (WS_H3 + (14u << 20))
#define WS_END (WS_COMM + (4u << 20))
#define CB_STEP 524288u
#define PD 3
__device__ __forceinline__ unsigned comm_base(const char* ws) { return WS_COMM + ((0u - ((unsigned)(size_t)ws + WS_COMM)) & 0x1fffffu); }
__device__ __forceinline__ unsigned step_base(unsigned cb, unsigned mask, int s) { return cb + (unsigned)(s & 3) * CB_STEP + mask; }
#define MF_AT(ks) (((ks) >> 3) * 16384u + ((ks) & 7) * 1024u)
#define OB_AT(r, h) ((2u + (r) * 4u + ((h) >> 3)) * 16384u + ((h) & 7) * 1024u)
#define VB_AT(h) ((10u + ((h) >> 2)) * 16384u + ((h) & 3) * 2048u)
#define XS0 4
#define XS1 24
#define XS2 44
#define XS3 62
#define CSL 8
#define FSL 6
#define SQUICK 3
#define NCHUNK 18
#define FL_O(h) (29u * 16384u + (h) * 128u)
#define FL_B(b) (28u * 16384u + (b) * 128u)
#define FL_T(s) FL_O(32 + ((s) & 3))

struct Ptrs { const u16 *x, *mem0, *Wq, *Wk, *Wv, *Wo, *w1, *w2, *w3, *w4; u16* out; char* ws; unsigned tag; };
#define WS_RSRC(P) __builtin_amdgcn_make_buffer_rsrc((void*)(P).ws, 0, WS_END, RSRC_W3)

__device__ __forceinline__ bool spin_fail(unsigned& spins, const Ptrs& P, int lane) {
  asm volatile("" ::: "memory");
  if (++spins > SPIN_CAP) {
    unsigned tv;
    asm volatile("v_mov_b32 %0, %1" : "=v"(tv) : "s"(P.tag));
    if (lane == 0) __builtin_amdgcn_raw_buffer_store_b32(tv, WS_RSRC(P), 0, TMO_OFF, AUX_SC1);
    return true;
  }
  return (spins & 255u) == 0u && (unsigned)__builtin_amdgcn_readfirstlane(__builtin_amdgcn_raw_buffer_load_b32(WS_RSRC(P), 0, TMO_OFF, AUX_SC1)) == P.tag;
}
__device__ __forceinline__ unsigned cnt_load(char* ws, int idx) {
  return __builtin_amdgcn_readfirstlane(__hip_atomic_load((gu32*)(ws + CNT_OFF + idx * 128), RLX_AGENT));
}
__device__ __forceinline__ void cnt_add(char* ws, int idx, int lane) {
  asm volatile("s_waitcnt vmcnt(0)" ::: "memory");
  if (lane == 0) __hip_atomic_fetch_add((gu32*)(ws + CNT_OFF + idx * 128), 1u, RLX_AGENT);
}
__device__ __forceinline__ void cnt_wait(const Ptrs& P, int idx, unsigned want, int lane) {
  for (unsigned spins = 0;;) {
    if (cnt_load(P.ws, idx) >= want) break;
    __builtin_amdgcn_s_sleep(CSL);
    if (spin_fail(spins, P, lane)) break;
  }
}
template <int SL = FSL>
__device__ __forceinline__ unsigned flag_wait(const Ptrs& P, int f, unsigned want, int lane) {
  const rsrc_t rs = WS_RSRC(P);
  u32x2 v;
  for (unsigned spins = 0;;) {
    v = ld8(rs, FLG_OFF + f * 128);
    if (!__any(v.y != P.tag || v.x < want)) break;
    __builtin_amdgcn_s_sleep(SL);
    if (spin_fail(spins, P, lane)) break;
  }
  asm volatile("" ::: "memory");
  return v.x;
}
__device__ __forceinline__ void flag_set(const Ptrs& P, int f, unsigned val, int lane) {
  if (lane == 0) st8(WS_RSRC(P), FLG_OFF + f * 128, (u32x2){val, P.tag});
}
__device__ __forceinline__ void get_tag(Ptrs& P) { P.tag = __builtin_amdgcn_readfirstlane(__hip_atomic_load((gu32*)(P.ws + EPO_OFF), RLX_AGENT)) + 1u; }
__device__ __forceinline__ void setup_wait(const Ptrs& P, int lane) { flag_wait(P, F_LN0(lane), 0, lane); }
__device__ __forceinline__ void clock_wait(unsigned tgt) {
  while ((int)(tgt - (unsigned)__builtin_amdgcn_s_memrealtime()) > 0) {}
}
__device__ __forceinline__ void report(const Ptrs& P, int wid) {
  if (wid == 0 && opq(0) == 0) __hip_atomic_fetch_add((gu32*)(P.ws + CNT_OFF + C_ARR * 128), 1u, RLX_AGENT);
}
__device__ __forceinline__ void kv_wait(const Ptrs& P, int s, int h, int& known, int lane) {
  if (s < known) return;
  const unsigned v = flag_wait(P, F_PRG(min(lane, 2), h), s + 1, lane);
  known = __builtin_amdgcn_readfirstlane(min(min(__builtin_amdgcn_readlane(v, 0), __builtin_amdgcn_readlane(v, 1)), __builtin_amdgcn_readlane(v, 2)));
}
__device__ __forceinline__ int wmax(int v) {
  v = max(v, __builtin_amdgcn_update_dpp(0, v, 0x128, 0xf, 0xf, false));
  v = max(v, __builtin_amdgcn_update_dpp(0, v, 0x124, 0xf, 0xf, false));
  v = max(v, __builtin_amdgcn_update_dpp(0, v, 0x122, 0xf, 0xf, false));
  v = max(v, __builtin_amdgcn_update_dpp(0, v, 0x121, 0xf, 0xf, false));
  return max(max(__builtin_amdgcn_readlane(v, 0), __builtin_amdgcn_readlane(v, 16)), max(__builtin_amdgcn_readlane(v, 32), __builtin_amdgcn_readlane(v, 48)));
}
__device__ __forceinline__ unsigned near_half(unsigned v) { return wmax(-(int)(v & 65535u)) > wmax(-(int)(v >> 16)) ? 8192u : 0u; }
__device__ __forceinline__ void dmaw(rsrc_t rs, unsigned voff, unsigned soff, unsigned ldsaddr) {
  asm volatile("s_mov_b32 m0, %3\n\ts_nop 0\n\tbuffer_load_dwordx4 %0, %1, %2 offen lds" :: "v"(voff), "s"(rs), "s"(soff), "s"(ldsaddr) : "memory");
}
__device__ __forceinline__ void dman(rsrc_t rs, unsigned voff, unsigned soff, unsigned ldsaddr) {
  asm volatile("s_mov_b32 m0, %3\n\ts_nop 0\n\tbuffer_load_dwordx4 %0, %1, %2 offen nt lds" :: "v"(voff), "s"(rs), "s"(soff), "s"(ldsaddr) : "memory");
}
__device__ __forceinline__ void dmas(rsrc_t rs, unsigned voff, unsigned soff, unsigned ldsaddr) {
  asm volatile("s_mov_b32 m0, %3\n\ts_nop 0\n\tbuffer_load_dwordx4 %0, %1, %2 offen sc1 lds" :: "v"(voff), "s"(rs), "s"(soff), "s"(ldsaddr) : "memory");
}
__device__ __forceinline__ void touch(const void* w, unsigned bytes, unsigned voff, LAS unsigned char* lds) {
  asm volatile("s_mov_b32 m0, %2\n\ts_nop 0\n\tbuffer_load_dword %0, %1, 0 offen lds"
               :: "v"(voff), "s"(__builtin_amdgcn_make_buffer_rsrc((void*)w, 0, bytes, RSRC_W3)), "s"((unsigned)(size_t)lds + 148480) : "memory");
}
__device__ __forceinline__ u32x2 trd(LAS unsigned char* p) {
  return __builtin_bit_cast(u32x2, __builtin_amdgcn_ds_read_tr16_b64_v4i16((LAS bf16x4*)p));
}
template <int BO, int UO>
__device__ __forceinline__ u32x8 rd_frag(LAS unsigned char* p) {
  const u32x2 a = trd(p), b = trd(p + UO), c = trd(p + BO), d = trd(p + BO + UO);
  return (u32x8){a.x, a.y, b.x, b.y, c.x, c.y, d.x, d.y};
}

#define TL(i) do {} while (0)
#define TLW(i) do {} while (0)
#define GSDECL
#define GS(i) do {} while (0)
#define GSFLUSH(k, i) do {} while (0)

#define QPRIO 0
#define VPOS 3
#define VLDI "buffer_load_dword v254, v17, %[rs], %[sb] offen sc1\n\tbuffer_load_dword v255, v17, %[rs], %[sb] offen offset:256 sc1\n\t"
#define VLD(p) VLD##p
#define VLD0
#define VLD1
#define VLD2
#define VLD3 VLDI
#define VLD4
#define DEC1 1
#define DEC2 1
#define DGA
#define DGB
#define DGC
#define DGE
#define DGO
#define DGP
#define DGQ
#define PACE "s_sleep 1\n\t"
#define STR_(x) #x
#define STR(x) STR_(x)
#define GSW
#define GSA(k)
#define GSO(k)
#define A_MAIN_Q() \
  asm volatile( \
    "v_mov_b32 v253, %[tag]\n\t" \
    "buffer_load_dwordx4 v[20:23], v12, %[rs], %[vx] offen sc1\n\t" \
    "buffer_load_dwordx4 v[24:27], v12, %[rs], %[vx] offen offset:1024 sc1\n\t" \
    "ds_read_b128 v[236:239], v9 offset:0\n\t" \
    "ds_read_b128 v[240:243], v9 offset:1024\n\t" \
    "ds_read_b128 v[244:247], v9 offset:2048\n\t" \
    "ds_read_b128 v[248:251], v9 offset:3072\n\t" \
    "v_mov_b32 v0, 0\n\t" \
    "v_mov_b32 v1, 0\n\t" \
    "v_mov_b32 v2, 0\n\t" \
    "v_mov_b32 v3, 0\n\t" \
    "v_mov_b32 v4, 0\n\t" \
    "v_mov_b32 v5, 0\n\t" \
    "v_mov_b32 v6, 0\n\t" \
    "v_mov_b32 v7, 0\n\t" \
    "s_cmp_lg_u32 %[sc], 3\n\t" \
    "s_cbranch_scc1 PB_%=\n\t" \
    "s_getpc_b64 s[92:93]\n\t" \
    "TPA_%=:\n\t" \
    "s_add_u32 s92, s92, TEA_%=-TPA_%=\n\t" \
    "s_addc_u32 s93, s93, 0\n\t" \
    "s_lshr_b32 s91, %[tgt], 2\n\t" \
    "s_sub_u32 s91, s91, 3\n\t" \
    "TWA_%=:\n\t" \
    "s_memrealtime s[88:89]\n\t" \
    DGE \
    "s_waitcnt lgkmcnt(0)\n\t" \
    "s_sub_u32 s89, s91, s88\n\t" \
    "s_lshl_b32 s89, s89, 2\n\t" \
    "s_cmp_gt_i32 s89, 0\n\t" \
    "s_cbranch_scc1 TWA_%=\n\t" \
    "s_lshl_b32 s88, s88, 2\n\t" \
    "s_sub_u32 s89, %[tgt], s88\n\t" \
    "s_max_i32 s89, s89, 0\n\t" \
    "s_mul_i32 s89, s89, 11\n\t" \
    "s_lshr_b32 s89, s89, 1\n\t" \
    DGB \
    "s_and_b32 s89, s89, 0x7c\n\t" \
    "s_sub_u32 s92, s92, s89\n\t" \
    "s_subb_u32 s93, s93, 0\n\t" \
    "s_setpc_b64 s[92:93]\n\t" \
    "s_nop 0\n\t" \
    "s_nop 0\n\t" \
    "s_nop 0\n\t" \
    "s_nop 0\n\t" \
    "s_nop 0\n\t" \
    "s_nop 0\n\t" \
    "s_nop 0\n\t" \
    "s_nop 0\n\t" \
    "s_nop 0\n\t" \
    "s_nop 0\n\t" \
    "s_nop 0\n\t" \
    "s_nop 0\n\t" \
    "s_nop 0\n\t" \
    "s_nop 0\n\t" \
    "s_nop 0\n\t" \
    "s_nop 0\n\t" \
    "s_nop 0\n\t" \
    "s_nop 0\n\t" \
    "s_nop 0\n\t" \
    "s_nop 0\n\t" \
    "s_nop 0\n\t" \
    "s_nop 0\n\t" \
    "TEA_%=:\n\t" \
    "PB_%=:\n\t" \
    "s_waitcnt lgkmcnt(0)\n\t" \
    "s_barrier\n\t" \
    "buffer_load_dwordx4 v[220:223], v8, %[rs], %[m0] offen sc1\n\t" \
    "buffer_load_dwordx4 v[224:227], v8, %[rs], %[m1] offen sc1\n\t" \
    "PM_%=:\n\t" \
    "s_waitcnt vmcnt(1)\n\t" \
    "ds_write_b128 v8, v[220:223] offset:0\n\t" \
    "v_max3_u32 v252, v220, v221, v222\n\t" \
    "s_waitcnt vmcnt(0)\n\t" \
    "ds_write_b128 v8, v[224:227] offset:4096\n\t" \
    "v_max3_u32 v252, v252, v223, v224\n\t" \
    "v_max3_u32 v252, v252, v225, v226\n\t" \
    "v_max_u32 v252, v252, v227\n\t" \
    "v_cmp_eq_u32 vcc, -1, v252\n\t" \
    "s_cbranch_vccz PD_%=\n\t" \
    "s_sub_u32 %[lim], %[lim], 1\n\t" \
    "s_cmp_gt_i32 %[lim], 0\n\t" \
    "s_cbranch_scc0 PD_%=\n\t" \
    "s_sleep " STR(CADM) "\n\t" \
    "buffer_load_dwordx4 v[220:223], v8, %[rs], %[m0] offen sc1\n\t" \
    "buffer_load_dwordx4 v[224:227], v8, %[rs], %[m1] offen sc1\n\t" \
    "s_branch PM_%=\n\t" \
    "PD_%=:\n\t" \
    GSA(0) \
    "v_smfmac_f32_16x16x64_bf16 v[0:3], v[220:223], v[236:243], v10\n\t" \
    "v_xor_b32 v9, 16384, v9\n\t" \
    "ds_read_b128 v[236:239], v9 offset:0\n\t" \
    "ds_read_b128 v[240:243], v9 offset:1024\n\t" \
    "v_smfmac_f32_16x16x64_bf16 v[4:7], v[220:223], v[244:251], v10\n\t" \
    "ds_read_b128 v[244:247], v9 offset:2048\n\t" \
    "ds_read_b128 v[248:251], v9 offset:3072\n\t" \
    "s_waitcnt lgkmcnt(4)\n\t" \
    "s_barrier\n\t" \
    "ds_read_b128 v[228:231], v8 offset:8192\n\t" \
    "ds_read_b128 v[232:235], v8 offset:12288\n\t" \
    "v_xor_b32 v8, 1024, v8\n\t" \
    "ds_read_b128 v[220:223], v8 offset:4096\n\t" \
    "v_smfmac_f32_16x16x64_bf16 v[0:3], v[224:227], v[28:35], v10\n\t" \
    "v_smfmac_f32_16x16x64_bf16 v[4:7], v[224:227], v[36:43], v10\n\t" \
    "ds_read_b128 v[224:227], v8 offset:0\n\t" \
    "s_waitcnt lgkmcnt(3)\n\t" \
    "v_smfmac_f32_16x16x64_bf16 v[0:3], v[228:231], v[92:99], v10\n\t" \
    "v_smfmac_f32_16x16x64_bf16 v[4:7], v[228:231], v[100:107], v10\n\t" \
    "ds_read_b128 v[228:231], v8 offset:8192\n\t" \
    "s_waitcnt lgkmcnt(3)\n\t" \
    "v_smfmac_f32_16x16x64_bf16 v[0:3], v[232:235], v[156:163], v10\n\t" \
    "v_smfmac_f32_16x16x64_bf16 v[4:7], v[232:235], v[164:171], v10\n\t" \
    "ds_read_b128 v[232:235], v8 offset:12288\n\t" \
    "s_waitcnt lgkmcnt(3)\n\t" \
    "v_smfmac_f32_16x16x64_bf16 v[0:3], v[220:223], v[44:51], v10\n\t" \
    "v_smfmac_f32_16x16x64_bf16 v[4:7], v[220:223], v[52:59], v10\n\t" \
    "v_xor_b32 v8, 3072, v8\n\t" \
    "ds_read_b128 v[220:223], v8 offset:4096\n\t" \
    "s_waitcnt lgkmcnt(3)\n\t" \
    "v_smfmac_f32_16x16x64_bf16 v[0:3], v[224:227], v[236:243], v10\n\t" \
    "v_xor_b32 v9, 49152, v9\n\t" \
    "ds_read_b128 v[236:239], v9 offset:0\n\t" \
    "v_smfmac_f32_16x16x64_bf16 v[4:7], v[224:227], v[244:251], v10\n\t" \
    "ds_read_b128 v[240:243], v9 offset:1024\n\t" \
    "s_waitcnt lgkmcnt(4)\n\t" \
    "v_smfmac_f32_16x16x64_bf16 v[0:3], v[228:231], v[108:115], v10\n\t" \
    "ds_read_b128 v[224:227], v8 offset:8192\n\t" \
    "v_smfmac_f32_16x16x64_bf16 v[4:7], v[228:231], v[116:123], v10\n\t" \
    "ds_read_b128 v[244:247], v9 offset:2048\n\t" \
    "s_waitcnt lgkmcnt(5)\n\t" \
    "v_smfmac_f32_16x16x64_bf16 v[0:3], v[232:235], v[172:179], v10\n\t" \
    "ds_read_b128 v[228:231], v8 offset:0\n\t" \
    "v_smfmac_f32_16x16x64_bf16 v[4:7], v[232:235], v[180:187], v10\n\t" \
    "ds_read_b128 v[248:251], v9 offset:3072\n\t" \
    "s_waitcnt lgkmcnt(6)\n\t" \
    "v_smfmac_f32_16x16x64_bf16 v[0:3], v[220:223], v[60:67], v10\n\t" \
    "ds_read_b128 v[232:235], v8 offset:12288\n\t" \
    "v_smfmac_f32_16x16x64_bf16 v[4:7], v[220:223], v[68:75], v10\n\t" \
    "v_xor_b32 v8, 1024, v8\n\t" \
    "ds_read_b128 v[220:223], v8 offset:4096\n\t" \
    "s_waitcnt lgkmcnt(5)\n\t" \
    "v_smfmac_f32_16x16x64_bf16 v[0:3], v[224:227], v[124:131], v10\n\t" \
    "v_smfmac_f32_16x16x64_bf16 v[4:7], v[224:227], v[132:139], v10\n\t" \
    "ds_read_b128 v[224:227], v8 offset:8192\n\t" \
    "s_waitcnt lgkmcnt(3)\n\t" \
    "v_smfmac_f32_16x16x64_bf16 v[0:3], v[228:231], v[236:243], v10\n\t" \
    "v_xor_b32 v9, 16384, v9\n\t" \
    "ds_read_b128 v[236:239], v9 offset:0\n\t" \
    "v_smfmac_f32_16x16x64_bf16 v[4:7], v[228:231], v[244:251], v10\n\t" \
    "ds_read_b128 v[240:243], v9 offset:1024\n\t" \
    "s_waitcnt lgkmcnt(4)\n\t" \
    "v_smfmac_f32_16x16x64_bf16 v[0:3], v[232:235], v[188:195], v10\n\t" \
    "ds_read_b128 v[228:231], v8 offset:12288\n\t" \
    "v_smfmac_f32_16x16x64_bf16 v[4:7], v[232:235], v[196:203], v10\n\t" \
    "ds_read_b128 v[244:247], v9 offset:2048\n\t" \
    "s_waitcnt lgkmcnt(5)\n\t" \
    "v_smfmac_f32_16x16x64_bf16 v[0:3], v[220:223], v[76:83], v10\n\t" \
    "ds_read_b128 v[232:235], v8 offset:0\n\t" \
    "v_xor_b32 v8, 3072, v8\n\t" \
    "v_smfmac_f32_16x16x64_bf16 v[4:7], v[220:223], v[84:91], v10\n\t" \
    "ds_read_b128 v[248:251], v9 offset:3072\n\t" \
    "s_waitcnt lgkmcnt(6)\n\t" \
    "v_smfmac_f32_16x16x64_bf16 v[0:3], v[224:227], v[140:147], v10\n\t" \
    "ds_read_b128 v[220:223], v12 offset:24576\n\t" \
    "v_smfmac_f32_16x16x64_bf16 v[4:7], v[224:227], v[148:155], v10\n\t" \
    "ds_read_b128 v[224:227], v12 offset:28672\n\t" \
    "s_waitcnt lgkmcnt(5)\n\t" \
    "v_smfmac_f32_16x16x64_bf16 v[0:3], v[228:231], v[204:211], v10\n\t" \
    "v_smfmac_f32_16x16x64_bf16 v[4:7], v[228:231], v[212:219], v10\n\t" \
    "ds_read_b128 v[228:231], v12 offset:25600\n\t" \
    "s_waitcnt lgkmcnt(3)\n\t" \
    "v_smfmac_f32_16x16x64_bf16 v[0:3], v[232:235], v[236:243], v10\n\t" \
    "v_xor_b32 v9, 49152, v9\n\t" \
    "v_smfmac_f32_16x16x64_bf16 v[4:7], v[232:235], v[244:251], v10\n\t" \
    VLD(4) \
    "s_nop 5\n\t" \
    "v_add_f32 v0, v0, v1\n\t" \
    "v_add_f32 v2, v2, v3\n\t" \
    "v_add_f32 v4, v4, v5\n\t" \
    "v_add_f32 v6, v6, v7\n\t" \
    "v_mul_f32 v0, %[sl], v0\n\t" \
    "v_mul_f32 v2, %[sl], v2\n\t" \
    "v_mul_f32 v4, %[sl], v4\n\t" \
    "v_mul_f32 v6, %[sl], v6\n\t" \
    "v_cvt_pk_bf16_f32 v0, v0, v4\n\t" \
    "v_cvt_pk_bf16_f32 v2, v2, v6\n\t" \
    "ds_write2_b32 v11, v0, v2 offset0:0 offset1:4\n\t" \
    "s_mov_b64 exec, %[onej]\n\t" \
    "ds_write_b32 v16, v253\n\t" \
    "s_mov_b64 exec, -1\n\t" \
    GSA(1) \
    "s_setprio 0\n\t" \
    "ds_read_b128 v[232:235], v12 offset:29696\n\t" \
    "s_nop 1\n\t" \
    "ds_read_b128 v[0:3], v12 offset:26624\n\t" \
    "s_nop 1\n\t" \
    "ds_read_b128 v[4:7], v12 offset:27648\n\t" \
    "s_nop 1\n\t" \
    "ds_read_b32 v252, v16\n\t" \
    "ds_read_b128 v[236:239], v14 offset:0\n\t" \
    "s_nop 1\n\t" \
    "ds_read_b128 v[240:243], v14 offset:512\n\t" \
    "s_nop 1\n\t" \
    "ds_read_b128 v[244:247], v14 offset:1024\n\t" \
    "s_nop 1\n\t" \
    "s_setprio 2\n\t" \
    VLD(3) \
    "s_setprio 0\n\t" \
    "ds_read_b128 v[248:251], v14 offset:1536\n\t" \
    "s_nop 1\n\t" \
    "s_setprio 2\n\t" \
    "s_waitcnt lgkmcnt(4)\n\t" \
    "v_cmp_ne_u32 vcc, v253, v252\n\t" \
    "s_cbranch_vccnz RP_%=\n\t" \
    "CP_%=:\n\t" \
    "s_setprio 2\n\t" \
    VLD(0) \
    "s_waitcnt lgkmcnt(2)\n\t" \
    "v_mfma_f32_16x16x32_bf16 v[220:223], v[220:223], v[236:239], 0\n\t" \
    "v_mfma_f32_16x16x32_bf16 v[224:227], v[224:227], v[236:239], 0\n\t" \
    "v_mfma_f32_16x16x32_bf16 v[220:223], v[228:231], v[240:243], v[220:223]\n\t" \
    "ds_read_b128 v[228:231], v12 offset:30720\n\t" \
    "v_mfma_f32_16x16x32_bf16 v[224:227], v[232:235], v[240:243], v[224:227]\n\t" \
    "ds_read_b128 v[232:235], v12 offset:31744\n\t" \
    "s_setprio " STR(QPRIO) "\n\t" \
    VLD(1) \
    "s_waitcnt lgkmcnt(2)\n\t" \
    "v_mfma_f32_16x16x32_bf16 v[220:223], v[0:3], v[244:247], v[220:223]\n\t" \
    "v_mfma_f32_16x16x32_bf16 v[220:223], v[4:7], v[248:251], v[220:223]\n\t" \
    "s_waitcnt lgkmcnt(1)\n\t" \
    "v_mfma_f32_16x16x32_bf16 v[224:227], v[228:231], v[244:247], v[224:227]\n\t" \
    "s_waitcnt lgkmcnt(0)\n\t" \
    "v_mfma_f32_16x16x32_bf16 v[224:227], v[232:235], v[248:251], v[224:227]\n\t" \
    GSA(2) \
    "s_setprio 2\n\t" \
    "s_nop 2\n\t" \
    "v_exp_f32 v220, v220\n\t" \
    "v_exp_f32 v221, v221\n\t" \
    "v_exp_f32 v222, v222\n\t" \
    "v_exp_f32 v223, v223\n\t" \
    "v_exp_f32 v224, v224\n\t" \
    "v_exp_f32 v225, v225\n\t" \
    "v_exp_f32 v226, v226\n\t" \
    "v_exp_f32 v227, v227\n\t" \
    "ds_read_b32 v252, v16 offset:16\n\t" \
    "ds_read_b128 v[0:3], v12 offset:22528\n\t" \
    "ds_read_b128 v[4:7], v12 offset:23552\n\t" \
    VLD(2) \
    "v_cvt_pk_bf16_f32 v220, v220, v221\n\t" \
    "v_cvt_pk_bf16_f32 v221, v222, v223\n\t" \
    "v_cvt_pk_bf16_f32 v222, v224, v225\n\t" \
    "v_cvt_pk_bf16_f32 v223, v226, v227\n\t" \
    "v_mov_b32 v224, 0\n\t" \
    "v_mov_b32 v225, 0\n\t" \
    "v_mfma_f32_16x16x32_bf16 v[228:231], v[20:23], v[220:223], 0\n\t" \
    "v_mov_b32 v226, 0\n\t" \
    "v_mov_b32 v227, 0\n\t" \
    "v_mfma_f32_16x16x32_bf16 v[232:235], v[24:27], v[220:223], 0\n\t" \
    "s_waitcnt lgkmcnt(2)\n\t" \
    "v_cmp_ne_u32 vcc, %[tag], v252\n\t" \
    "s_cbranch_vccnz RK_%=\n\t" \
    "CK_%=:\n\t" \
    "s_waitcnt lgkmcnt(1)\n\t" \
    "v_smfmac_f32_16x16x64_bf16 v[224:227], v[0:3], v[236:243], v10\n\t" \
    "s_waitcnt lgkmcnt(0)\n\t" \
    "v_smfmac_f32_16x16x64_bf16 v[224:227], v[4:7], v[244:251], v10\n\t" \
    "v_mov_b32 v236, %[one]\n\t" \
    "v_mov_b32 v237, %[one]\n\t" \
    "v_mov_b32 v238, %[one]\n\t" \
    "v_mov_b32 v239, %[one]\n\t" \
    "v_mov_b32 v1, 0\n\t" \
    "v_mov_b32 v2, 0\n\t" \
    "v_mfma_f32_16x16x32_bf16 v[240:243], v[236:239], v[220:223], 0\n\t" \
    "s_nop 0\n\t" \
    "v_add_f32 v4, v224, v225\n\t" \
    "v_add_f32 v5, v226, v227\n\t" \
    "v_exp_f32 v4, v4\n\t" \
    "v_exp_f32 v5, v5\n\t" \
    "s_nop 0\n\t" \
    "v_cvt_pk_bf16_f32 v0, v4, v5\n\t" \
    "v_cvt_pk_bf16_f32 v3, v4, v5\n\t" \
    GSA(3) \
    "CM_%=:\n\t" \
    "s_nop 0\n\t" \
    "v_mfma_f32_16x16x16_bf16 v[240:243], v[236:237], v[0:1], v[240:243]\n\t" \
    "s_waitcnt vmcnt(0)\n\t" \
    "v_mfma_f32_16x16x16_bf16 v[228:231], v[254:255], v[0:1], v[228:231]\n\t" \
    "v_max_u32 v7, v254, v255\n\t" \
    "v_mfma_f32_16x16x16_bf16 v[232:235], v[254:255], v[2:3], v[232:235]\n\t" \
    "v_cmp_eq_u32 s[88:89], -1, v7\n\t" \
    "s_not_b64 exec, %[hi]\n\t" \
    "s_nop 1\n\t" \
    "v_rcp_f32 v252, v240\n\t" \
    "v_subrev_u32 v253, 0x21800000, v240\n\t" \
    "v_cmp_gt_u32 vcc, v253, %[rg]\n\t" \
    "v_pk_mul_f32 v[228:229], v[228:229], v[252:253] op_sel_hi:[1,0]\n\t" \
    "v_pk_mul_f32 v[230:231], v[230:231], v[252:253] op_sel_hi:[1,0]\n\t" \
    "v_pk_mul_f32 v[232:233], v[232:233], v[252:253] op_sel_hi:[1,0]\n\t" \
    "v_pk_mul_f32 v[234:235], v[234:235], v[252:253] op_sel_hi:[1,0]\n\t" \
    "s_or_b64 vcc, vcc, s[88:89]\n\t" \
    "v_cvt_pk_bf16_f32 v4, v228, v229\n\t" \
    "v_cvt_pk_bf16_f32 v5, v230, v231\n\t" \
    "v_cvt_pk_bf16_f32 v6, v232, v233\n\t" \
    "v_cvt_pk_bf16_f32 v7, v234, v235\n\t" \
    "s_cbranch_scc1 FX_%=\n\t" \
    "buffer_store_dwordx4 v[4:7], v18, %[rs], %[sb] offen sc1\n\t" \
    "s_memrealtime %[tp]\n\t" \
    DGA \
    GSA(4) \
    "v_max3_u32 v252, v4, v5, v6\n\t" \
    "v_max_u32 v252, v252, v7\n\t" \
    "v_cmp_eq_u32 vcc, -1, v252\n\t" \
    "s_cbranch_vccnz RC_%=\n\t" \
    "CC_%=:\n\t" \
    "s_mov_b64 exec, -1\n\t" \
    "v_add_u32 v252, %[vd], v12\n\t" \
    "v_add_u32 v253, %[md], v17\n\t" \
    "ds_write_b128 v252, v[20:23]\n\t" \
    "ds_write_b128 v252, v[24:27] offset:1024\n\t" \
    "ds_write2st64_b32 v253, v254, v255 offset0:0 offset1:1\n\t" \
    "s_waitcnt lgkmcnt(0)\n\t" \
    "s_cmp_lg_u32 %[sc], 3\n\t" \
    "s_cbranch_scc1 NH_%=\n\t" \
    "buffer_load_dwordx2 v[0:1], off, %[rs], %[fl] sc1\n\t" \
    "s_barrier\n\t" \
    "s_waitcnt vmcnt(0)\n\t" \
    "v_readfirstlane_b32 %[pt], v0\n\t" \
    "v_readfirstlane_b32 %[pd], v1\n\t" \
    "s_nop 3\n\t" \
    "s_branch E_%=\n\t" \
    "NH_%=:\n\t" \
    "s_barrier\n\t" \
    "s_branch E_%=\n\t" \
    "RP_%=:\n\t" \
    "s_waitcnt lgkmcnt(0)\n\t" \
    "ds_read_b32 v252, v16\n\t" \
    "ds_read_b128 v[236:239], v14 offset:0\n\t" \
    "ds_read_b128 v[240:243], v14 offset:512\n\t" \
    "ds_read_b128 v[244:247], v14 offset:1024\n\t" \
    "ds_read_b128 v[248:251], v14 offset:1536\n\t" \
    "s_sub_u32 %[lim], %[lim], " STR(DEC1) "\n\t" \
    "s_cmp_gt_i32 %[lim], 0\n\t" \
    "s_waitcnt lgkmcnt(4)\n\t" \
    "s_cbranch_scc0 CP_%=\n\t" \
    "v_cmp_ne_u32 vcc, %[tag], v252\n\t" \
    "s_cbranch_vccnz RP_%=\n\t" \
    "s_branch CP_%=\n\t" \
    "RK_%=:\n\t" \
    "s_waitcnt lgkmcnt(0)\n\t" \
    "ds_read_b32 v252, v16 offset:16\n\t" \
    "ds_read_b128 v[0:3], v12 offset:22528\n\t" \
    "ds_read_b128 v[4:7], v12 offset:23552\n\t" \
    "s_sub_u32 %[lim], %[lim], " STR(DEC1) "\n\t" \
    "s_cmp_gt_i32 %[lim], 0\n\t" \
    "s_waitcnt lgkmcnt(2)\n\t" \
    "s_cbranch_scc0 CK_%=\n\t" \
    "v_cmp_ne_u32 vcc, %[tag], v252\n\t" \
    "s_cbranch_vccnz RK_%=\n\t" \
    "s_branch CK_%=\n\t" \
    "RC_%=:\n\t" \
    "v_min_u32 v4, -2, v4\n\t" \
    "v_min_u32 v5, -2, v5\n\t" \
    "v_min_u32 v6, -2, v6\n\t" \
    "v_min_u32 v7, -2, v7\n\t" \
    "buffer_store_dwordx4 v[4:7], v18, %[rs], %[sb] offen sc1\n\t" \
    "s_branch CC_%=\n\t" \
    "FX_%=:\n\t" \
    "s_mov_b64 exec, -1\n\t" \
    "s_cmp_eq_u64 s[88:89], 0\n\t" \
    "s_cbranch_scc1 RM_%=\n\t" \
    "buffer_load_dword v254, v17, %[rs], %[sb] offen sc1\n\t" \
    "buffer_load_dword v255, v17, %[rs], %[sb] offen offset:256 sc1\n\t" \
    "v_mfma_f32_16x16x32_bf16 v[228:231], v[20:23], v[220:223], 0\n\t" \
    "v_mfma_f32_16x16x32_bf16 v[232:235], v[24:27], v[220:223], 0\n\t" \
    "v_mfma_f32_16x16x32_bf16 v[240:243], v[236:239], v[220:223], 0\n\t" \
    "s_sub_u32 %[lim], %[lim], " STR(DEC2) "\n\t" \
    "s_cmp_gt_i32 %[lim], 0\n\t" \
    "s_cbranch_scc1 CM_%=\n\t" \
    "s_waitcnt vmcnt(0)\n\t" \
    "v_mov_b32 v254, 0\n\t" \
    "v_mov_b32 v255, 0\n\t" \
    "s_branch CM_%=\n\t" \
    "RM_%=:\n\t" \
    "s_mov_b32 %[rg], -1\n\t" \
    "s_nop 7\n\t" \
    "v_add_f32 v4, v224, v225\n\t" \
    "v_add_f32 v5, v226, v227\n\t" \
    "ds_read_b128 v[236:239], v14 offset:0\n\t" \
    "ds_read_b128 v[240:243], v14 offset:512\n\t" \
    "ds_read_b128 v[244:247], v14 offset:1024\n\t" \
    "ds_read_b128 v[248:251], v14 offset:1536\n\t" \
    "ds_read_b128 v[232:235], v12 offset:24576\n\t" \
    "s_waitcnt lgkmcnt(0)\n\t" \
    "v_mfma_f32_16x16x32_bf16 v[220:223], v[232:235], v[236:239], 0\n\t" \
    "ds_read_b128 v[232:235], v12 offset:25600\n\t" \
    "s_waitcnt lgkmcnt(0)\n\t" \
    "v_mfma_f32_16x16x32_bf16 v[220:223], v[232:235], v[240:243], v[220:223]\n\t" \
    "ds_read_b128 v[232:235], v12 offset:26624\n\t" \
    "s_waitcnt lgkmcnt(0)\n\t" \
    "v_mfma_f32_16x16x32_bf16 v[220:223], v[232:235], v[244:247], v[220:223]\n\t" \
    "ds_read_b128 v[232:235], v12 offset:27648\n\t" \
    "s_waitcnt lgkmcnt(0)\n\t" \
    "v_mfma_f32_16x16x32_bf16 v[220:223], v[232:235], v[248:251], v[220:223]\n\t" \
    "ds_read_b128 v[232:235], v12 offset:28672\n\t" \
    "s_waitcnt lgkmcnt(0)\n\t" \
    "v_mfma_f32_16x16x32_bf16 v[224:227], v[232:235], v[236:239], 0\n\t" \
    "ds_read_b128 v[232:235], v12 offset:29696\n\t" \
    "s_waitcnt lgkmcnt(0)\n\t" \
    "v_mfma_f32_16x16x32_bf16 v[224:227], v[232:235], v[240:243], v[224:227]\n\t" \
    "ds_read_b128 v[232:235], v12 offset:30720\n\t" \
    "s_waitcnt lgkmcnt(0)\n\t" \
    "v_mfma_f32_16x16x32_bf16 v[224:227], v[232:235], v[244:247], v[224:227]\n\t" \
    "ds_read_b128 v[232:235], v12 offset:31744\n\t" \
    "s_waitcnt lgkmcnt(0)\n\t" \
    "v_mfma_f32_16x16x32_bf16 v[224:227], v[232:235], v[248:251], v[224:227]\n\t" \
    "v_max3_f32 v252, v220, v221, v222\n\t" \
    "s_nop 6\n\t" \
    "v_max3_f32 v253, v223, v224, v225\n\t" \
    "v_max3_f32 v252, v252, v226, v227\n\t" \
    "v_max3_f32 v253, v253, v4, v5\n\t" \
    "v_max_f32 v252, v252, v253\n\t" \
    "v_mov_b32 v253, v252\n\t" \
    "v_mov_b32 v6, v252\n\t" \
    "s_nop 1\n\t" \
    "v_permlane16_swap_b32 v253, v6\n\t" \
    "v_max_f32 v252, v253, v6\n\t" \
    "v_max_f32 v253, v253, v6\n\t" \
    "s_nop 1\n\t" \
    "v_permlane32_swap_b32 v252, v253\n\t" \
    "v_max_f32 v252, v252, v253\n\t" \
    "v_sub_f32 v220, v220, v252\n\t" \
    "v_sub_f32 v221, v221, v252\n\t" \
    "v_sub_f32 v222, v222, v252\n\t" \
    "v_sub_f32 v223, v223, v252\n\t" \
    "v_sub_f32 v224, v224, v252\n\t" \
    "v_sub_f32 v225, v225, v252\n\t" \
    "v_sub_f32 v226, v226, v252\n\t" \
    "v_sub_f32 v227, v227, v252\n\t" \
    "v_sub_f32 v4, v4, v252\n\t" \
    "v_sub_f32 v5, v5, v252\n\t" \
    "v_exp_f32 v220, v220\n\t" \
    "v_exp_f32 v221, v221\n\t" \
    "v_exp_f32 v222, v222\n\t" \
    "v_exp_f32 v223, v223\n\t" \
    "v_exp_f32 v224, v224\n\t" \
    "v_exp_f32 v225, v225\n\t" \
    "v_exp_f32 v226, v226\n\t" \
    "v_exp_f32 v227, v227\n\t" \
    "v_cvt_pk_bf16_f32 v220, v220, v221\n\t" \
    "v_cvt_pk_bf16_f32 v221, v222, v223\n\t" \
    "v_cvt_pk_bf16_f32 v222, v224, v225\n\t" \
    "v_cvt_pk_bf16_f32 v223, v226, v227\n\t" \
    "v_mov_b32 v236, %[one]\n\t" \
    "v_mov_b32 v237, %[one]\n\t" \
    "v_mov_b32 v238, %[one]\n\t" \
    "v_mov_b32 v239, %[one]\n\t" \
    "v_mfma_f32_16x16x32_bf16 v[228:231], v[20:23], v[220:223], 0\n\t" \
    "v_mfma_f32_16x16x32_bf16 v[232:235], v[24:27], v[220:223], 0\n\t" \
    "v_mfma_f32_16x16x32_bf16 v[240:243], v[236:239], v[220:223], 0\n\t" \
    "v_exp_f32 v4, v4\n\t" \
    "v_exp_f32 v5, v5\n\t" \
    "s_nop 0\n\t" \
    "v_cvt_pk_bf16_f32 v0, v4, v5\n\t" \
    "v_cvt_pk_bf16_f32 v3, v4, v5\n\t" \
    "s_nop 1\n\t" \
    "s_branch CM_%=\n\t" \
    "E_%=:\n\t" \
    GSW \
    "s_nop 1\n\t" \
    : [lim] "+s"(lim), [rg] "+s"(rg), [tp] "=&s"(tp), [pt] "=&s"(pt), [pd] "=&s"(pd) DGO DGP GSO(0) GSO(1) GSO(2) GSO(3) GSO(4) \
    : "{v8}"(am_a), "{v9}"(t0_a), "{v10}"(idx), "{v11}"(iw_a), "{v12}"(ab_a), "{v14}"(qr_a), "{v16}"(fr_a), "{v17}"(vo_v), "{v18}"(oo_v), [rs] "s"(rs), [sb] "s"(sb), [tag] "s"(0xffffff81u + s), [hi] "s"(0xff00ff00ff00ff00ull), [tgt] "s"(tgt), [sc] "s"(wid), [onej] "s"(onej), [sl] "s"(0x3e0293ee), [one] "s"(0x3f803f80), [m0] "s"(sbm), [m1] "s"(sbm + 4096u), [vx] "s"(vxo), [fl] "s"(fb + FL_T(s + 1)), [vd] "s"(8192 + j * 2048), [md] "s"(md), "{v[28:35]}"(wr[0][0]), "{v[36:43]}"(wr[0][1]), "{v[44:51]}"(wr[1][0]), "{v[52:59]}"(wr[1][1]), "{v[60:67]}"(wr[2][0]), "{v[68:75]}"(wr[2][1]), "{v[76:83]}"(wr[3][0]), "{v[84:91]}"(wr[3][1]), "{v[92:99]}"(wr[4][0]), "{v[100:107]}"(wr[4][1]), "{v[108:115]}"(wr[5][0]), "{v[116:123]}"(wr[5][1]), "{v[124:131]}"(wr[6][0]), "{v[132:139]}"(wr[6][1]), "{v[140:147]}"(wr[7][0]), "{v[148:155]}"(wr[7][1]), "{v[156:163]}"(wr[8][0]), "{v[164:171]}"(wr[8][1]), "{v[172:179]}"(wr[9][0]), "{v[180:187]}"(wr[9][1]), "{v[188:195]}"(wr[10][0]), "{v[196:203]}"(wr[10][1]), "{v[204:211]}"(wr[11][0]), "{v[212:219]}"(wr[11][1]) \
    : "memory", "vcc", "scc", "s88", "s89", "s90", "s91", "s92", "s93", "s94", "s95", "v0", "v1", "v2", "v3", "v4", "v5", "v6", "v7", "v20", "v21", "v22", "v23", "v24", "v25", "v26", "v27", "v220", "v221", "v222", "v223", "v224", "v225", "v226", "v227", "v228", "v229", "v230", "v231", "v232", "v233", "v234", "v235", "v236", "v237", "v238", "v239", "v240", "v241", "v242", "v243", "v244", "v245", "v246", "v247", "v248", "v249", "v250", "v251", "v252", "v253", "v254", "v255")

#define A_MAIN_K() \
  asm volatile( \
    "ds_read_b128 v[236:239], v9 offset:0\n\t" \
    "ds_read_b128 v[240:243], v9 offset:1024\n\t" \
    "ds_read_b128 v[244:247], v9 offset:2048\n\t" \
    "ds_read_b128 v[248:251], v9 offset:3072\n\t" \
    "v_mov_b32 v0, 0\n\t" \
    "v_mov_b32 v1, 0\n\t" \
    "v_mov_b32 v2, 0\n\t" \
    "v_mov_b32 v3, 0\n\t" \
    "v_mov_b32 v4, 0\n\t" \
    "v_mov_b32 v5, 0\n\t" \
    "v_mov_b32 v6, 0\n\t" \
    "v_mov_b32 v7, 0\n\t" \
    "s_waitcnt lgkmcnt(0)\n\t" \
    "s_barrier\n\t" \
    "buffer_load_dwordx4 v[220:223], v8, %[rs], %[m0] offen sc1\n\t" \
    "buffer_load_dwordx4 v[224:227], v8, %[rs], %[m1] offen sc1\n\t" \
    GSA(3) \
    "v_mov_b32 v252, v252\n\t" \
    "s_nop 2\n\t" \
    "v_mov_b32 v252, v252\n\t" \
    "s_nop 2\n\t" \
    "v_mov_b32 v252, v252\n\t" \
    "s_nop 2\n\t" \
    "v_mov_b32 v252, v252\n\t" \
    "s_nop 2\n\t" \
    "v_mov_b32 v252, v252\n\t" \
    "s_nop 2\n\t" \
    "v_mov_b32 v252, v252\n\t" \
    "s_nop 2\n\t" \
    "v_mov_b32 v252, v252\n\t" \
    "s_nop 2\n\t" \
    "v_mov_b32 v252, v252\n\t" \
    "s_nop 2\n\t" \
    "v_mov_b32 v252, v252\n\t" \
    "s_nop 2\n\t" \
    "v_mov_b32 v252, v252\n\t" \
    "s_nop 2\n\t" \
    "v_mov_b32 v252, v252\n\t" \
    "s_nop 2\n\t" \
    "v_mov_b32 v252, v252\n\t" \
    "s_nop 2\n\t" \
    "v_mov_b32 v252, v252\n\t" \
    "s_nop 2\n\t" \
    "v_mov_b32 v252, v252\n\t" \
    "s_nop 2\n\t" \
    "v_mov_b32 v252, v252\n\t" \
    "s_nop 2\n\t" \
    "v_mov_b32 v252, v252\n\t" \
    "s_nop 2\n\t" \
    "v_mov_b32 v252, v252\n\t" \
    "s_nop 2\n\t" \
    "v_mov_b32 v252, v252\n\t" \
    "s_nop 2\n\t" \
    "v_mov_b32 v252, v252\n\t" \
    "s_nop 2\n\t" \
    "v_mov_b32 v252, v252\n\t" \
    "s_nop 2\n\t" \
    "v_mov_b32 v252, v252\n\t" \
    "s_nop 2\n\t" \
    "v_mov_b32 v252, v252\n\t" \
    "s_nop 2\n\t" \
    "v_mov_b32 v252, v252\n\t" \
    "s_nop 2\n\t" \
    "v_mov_b32 v252, v252\n\t" \
    "s_nop 2\n\t" \
    "v_mov_b32 v252, v252\n\t" \
    "s_nop 2\n\t" \
    "v_mov_b32 v252, v252\n\t" \
    "s_nop 2\n\t" \
    "v_mov_b32 v252, v252\n\t" \
    "s_nop 2\n\t" \
    "v_mov_b32 v252, v252\n\t" \
    "s_nop 2\n\t" \
    "v_mov_b32 v252, v252\n\t" \
    "s_nop 2\n\t" \
    "v_mov_b32 v252, v252\n\t" \
    "s_nop 2\n\t" \
    "PM_%=:\n\t" \
    "s_waitcnt vmcnt(1)\n\t" \
    "ds_write_b128 v8, v[220:223] offset:8192\n\t" \
    "v_max3_u32 v252, v220, v221, v222\n\t" \
    "s_waitcnt vmcnt(0)\n\t" \
    "ds_write_b128 v8, v[224:227] offset:12288\n\t" \
    "v_max3_u32 v252, v252, v223, v224\n\t" \
    "v_max3_u32 v252, v252, v225, v226\n\t" \
    "v_max_u32 v252, v252, v227\n\t" \
    "v_cmp_eq_u32 vcc, -1, v252\n\t" \
    "s_cbranch_vccz PD_%=\n\t" \
    "s_sub_u32 %[lim], %[lim], 1\n\t" \
    "s_cmp_gt_i32 %[lim], 0\n\t" \
    "s_cbranch_scc0 PD_%=\n\t" \
    "s_sleep " STR(CADM) "\n\t" \
    "buffer_load_dwordx4 v[220:223], v8, %[rs], %[m0] offen sc1\n\t" \
    "buffer_load_dwordx4 v[224:227], v8, %[rs], %[m1] offen sc1\n\t" \
    "s_branch PM_%=\n\t" \
    "PD_%=:\n\t" \
    GSA(0) \
    "v_smfmac_f32_16x16x64_bf16 v[0:3], v[220:223], v[236:243], v10\n\t" \
    "v_xor_b32 v9, 16384, v9\n\t" \
    "v_smfmac_f32_16x16x64_bf16 v[4:7], v[220:223], v[244:251], v10\n\t" \
    "s_waitcnt lgkmcnt(0)\n\t" \
    "s_barrier\n\t" \
    DGA \
    "s_cmp_lg_u32 %[st], 63\n\t" \
    "s_cbranch_scc1 NL_%=\n\t" \
    "s_mov_b64 exec, %[fm]\n\t" \
    "v_mov_b32 v252, %[st]\n\t" \
    "v_mov_b32 v253, %[tag]\n\t" \
    "v_mov_b32 v254, 0\n\t" \
    "buffer_store_dwordx2 v[252:253], v254, %[rs], %[po] offen sc1\n\t" \
    "s_mov_b64 exec, -1\n\t" \
    "NL_%=:\n\t" \
    "ds_read_b128 v[236:239], v9 offset:0\n\t" \
    "ds_read_b128 v[240:243], v9 offset:1024\n\t" \
    "ds_read_b128 v[244:247], v9 offset:2048\n\t" \
    "ds_read_b128 v[248:251], v9 offset:3072\n\t" \
    "ds_read_b128 v[228:231], v8 offset:0\n\t" \
    "ds_read_b128 v[232:235], v8 offset:4096\n\t" \
    "v_smfmac_f32_16x16x64_bf16 v[0:3], v[224:227], v[148:155], v10\n\t" \
    "v_xor_b32 v8, 1024, v8\n\t" \
    "ds_read_b128 v[220:223], v8 offset:8192\n\t" \
    "v_smfmac_f32_16x16x64_bf16 v[4:7], v[224:227], v[156:163], v10\n\t" \
    "ds_read_b128 v[224:227], v8 offset:12288\n\t" \
    "buffer_store_dwordx4 v[212:215], v14, %[rs], %[ox] offen offset:0 sc1\n\t" \
    "buffer_store_dwordx4 v[216:219], v14, %[rs], %[ox] offen offset:512 sc1\n\t" \
    "s_waitcnt lgkmcnt(3)\n\t" \
    "v_smfmac_f32_16x16x64_bf16 v[0:3], v[228:231], v[20:27], v10\n\t" \
    "v_smfmac_f32_16x16x64_bf16 v[4:7], v[228:231], v[28:35], v10\n\t" \
    "ds_read_b128 v[228:231], v8 offset:0\n\t" \
    "s_waitcnt lgkmcnt(3)\n\t" \
    "v_smfmac_f32_16x16x64_bf16 v[0:3], v[232:235], v[84:91], v10\n\t" \
    "v_smfmac_f32_16x16x64_bf16 v[4:7], v[232:235], v[92:99], v10\n\t" \
    "ds_read_b128 v[232:235], v8 offset:4096\n\t" \
    "s_waitcnt lgkmcnt(3)\n\t" \
    "v_smfmac_f32_16x16x64_bf16 v[0:3], v[220:223], v[236:243], v10\n\t" \
    "v_xor_b32 v9, 49152, v9\n\t" \
    "ds_read_b128 v[236:239], v9 offset:0\n\t" \
    "v_smfmac_f32_16x16x64_bf16 v[4:7], v[220:223], v[244:251], v10\n\t" \
    "ds_read_b128 v[240:243], v9 offset:1024\n\t" \
    "s_waitcnt lgkmcnt(4)\n\t" \
    "v_smfmac_f32_16x16x64_bf16 v[0:3], v[224:227], v[164:171], v10\n\t" \
    "v_xor_b32 v8, 3072, v8\n\t" \
    "ds_read_b128 v[220:223], v8 offset:12288\n\t" \
    "v_smfmac_f32_16x16x64_bf16 v[4:7], v[224:227], v[172:179], v10\n\t" \
    "ds_read_b128 v[244:247], v9 offset:2048\n\t" \
    "s_waitcnt lgkmcnt(5)\n\t" \
    "v_smfmac_f32_16x16x64_bf16 v[0:3], v[228:231], v[36:43], v10\n\t" \
    "ds_read_b128 v[224:227], v8 offset:0\n\t" \
    "v_smfmac_f32_16x16x64_bf16 v[4:7], v[228:231], v[44:51], v10\n\t" \
    "ds_read_b128 v[248:251], v9 offset:3072\n\t" \
    "s_waitcnt lgkmcnt(6)\n\t" \
    "v_smfmac_f32_16x16x64_bf16 v[0:3], v[232:235], v[100:107], v10\n\t" \
    "ds_read_b128 v[228:231], v8 offset:8192\n\t" \
    "v_smfmac_f32_16x16x64_bf16 v[4:7], v[232:235], v[108:115], v10\n\t" \
    "ds_read_b128 v[232:235], v8 offset:4096\n\t" \
    "s_waitcnt lgkmcnt(5)\n\t" \
    "v_smfmac_f32_16x16x64_bf16 v[0:3], v[220:223], v[180:187], v10\n\t" \
    "v_smfmac_f32_16x16x64_bf16 v[4:7], v[220:223], v[188:195], v10\n\t" \
    "v_xor_b32 v8, 1024, v8\n\t" \
    "ds_read_b128 v[220:223], v8 offset:12288\n\t" \
    "s_waitcnt lgkmcnt(4)\n\t" \
    "v_smfmac_f32_16x16x64_bf16 v[0:3], v[224:227], v[52:59], v10\n\t" \
    "v_smfmac_f32_16x16x64_bf16 v[4:7], v[224:227], v[60:67], v10\n\t" \
    "ds_read_b128 v[224:227], v8 offset:0\n\t" \
    "s_waitcnt lgkmcnt(3)\n\t" \
    "v_smfmac_f32_16x16x64_bf16 v[0:3], v[228:231], v[236:243], v10\n\t" \
    "v_xor_b32 v9, 16384, v9\n\t" \
    "ds_read_b128 v[236:239], v9 offset:0\n\t" \
    "v_smfmac_f32_16x16x64_bf16 v[4:7], v[228:231], v[244:251], v10\n\t" \
    "ds_read_b128 v[240:243], v9 offset:1024\n\t" \
    "s_waitcnt lgkmcnt(4)\n\t" \
    "v_smfmac_f32_16x16x64_bf16 v[0:3], v[232:235], v[116:123], v10\n\t" \
    "ds_read_b128 v[228:231], v8 offset:4096\n\t" \
    "v_smfmac_f32_16x16x64_bf16 v[4:7], v[232:235], v[124:131], v10\n\t" \
    "ds_read_b128 v[244:247], v9 offset:2048\n\t" \
    "s_waitcnt lgkmcnt(5)\n\t" \
    "v_smfmac_f32_16x16x64_bf16 v[0:3], v[220:223], v[196:203], v10\n\t" \
    "ds_read_b128 v[232:235], v8 offset:8192\n\t" \
    "v_xor_b32 v8, 3072, v8\n\t" \
    "v_smfmac_f32_16x16x64_bf16 v[4:7], v[220:223], v[204:211], v10\n\t" \
    "ds_read_b128 v[248:251], v9 offset:3072\n\t" \
    "s_waitcnt lgkmcnt(6)\n\t" \
    "v_smfmac_f32_16x16x64_bf16 v[0:3], v[224:227], v[68:75], v10\n\t" \
    "v_smfmac_f32_16x16x64_bf16 v[4:7], v[224:227], v[76:83], v10\n\t" \
    "s_waitcnt lgkmcnt(3)\n\t" \
    "v_smfmac_f32_16x16x64_bf16 v[0:3], v[228:231], v[132:139], v10\n\t" \
    "v_smfmac_f32_16x16x64_bf16 v[4:7], v[228:231], v[140:147], v10\n\t" \
    "s_waitcnt lgkmcnt(0)\n\t" \
    "v_smfmac_f32_16x16x64_bf16 v[0:3], v[232:235], v[236:243], v10\n\t" \
    "v_xor_b32 v9, 49152, v9\n\t" \
    "v_smfmac_f32_16x16x64_bf16 v[4:7], v[232:235], v[244:251], v10\n\t" \
    "s_setprio 3\n\t" \
    "v_mov_b32 v253, %[stag]\n\t" \
    "s_nop 3\n\t" \
    "v_add_f32 v0, v0, v1\n\t" \
    "v_add_f32 v2, v2, v3\n\t" \
    "v_add_f32 v4, v4, v5\n\t" \
    "v_add_f32 v6, v6, v7\n\t" \
    "v_cvt_pk_bf16_f32 v0, v0, v4\n\t" \
    "v_cvt_pk_bf16_f32 v2, v2, v6\n\t" \
    "ds_write2_b32 v13, v0, v2 offset0:0 offset1:8\n\t" \
    "s_mov_b64 exec, %[onej]\n\t" \
    "ds_write_b32 v18, v253 offset:16\n\t" \
    "s_mov_b64 exec, -1\n\t" \
    "s_setprio 0\n\t" \
    "ds_write2_b32 v11, v0, v2 offset0:0 offset1:4\n\t" \
    "s_mov_b64 exec, %[onej]\n\t" \
    "ds_write_b32 v18, v253 offset:32\n\t" \
    "s_mov_b64 exec, -1\n\t" \
    GSA(1) \
    "s_setprio 0\n\t" \
    "buffer_load_dwordx4 v[236:239], v12, %[rs], %[qx] offen offset:0 sc1\n\t" \
    "buffer_load_dwordx4 v[240:243], v12, %[rs], %[qx] offen offset:1024 sc1\n\t" \
    "buffer_load_dwordx4 v[244:247], v12, %[rs], %[qx] offen offset:2048 sc1\n\t" \
    "buffer_load_dwordx4 v[248:251], v12, %[rs], %[qx] offen offset:3072 sc1\n\t" \
    "v_cmp_le_i32 s[72:73], 0, v16\n\t" \
    "v_cmp_le_i32 s[74:75], 1, v16\n\t" \
    "v_cmp_le_i32 s[76:77], 2, v16\n\t" \
    "v_cmp_le_i32 s[78:79], 3, v16\n\t" \
    "v_cmp_le_i32 s[80:81], 16, v16\n\t" \
    "v_cmp_le_i32 s[82:83], 17, v16\n\t" \
    "v_cmp_le_i32 s[84:85], 18, v16\n\t" \
    "v_cmp_le_i32 s[86:87], 19, v16\n\t" \
    "v_mov_b32 v254, v254\n\t" \
    "s_nop 2\n\t" \
    "v_mov_b32 v254, v254\n\t" \
    "s_nop 2\n\t" \
    "v_mov_b32 v254, v254\n\t" \
    "s_nop 2\n\t" \
    "v_mov_b32 v254, v254\n\t" \
    "s_nop 2\n\t" \
    "v_mov_b32 v254, v254\n\t" \
    "s_nop 2\n\t" \
    "v_mov_b32 v254, v254\n\t" \
    "s_nop 2\n\t" \
    "v_mov_b32 v254, v254\n\t" \
    "s_nop 2\n\t" \
    "v_mov_b32 v254, v254\n\t" \
    "s_nop 2\n\t" \
    "v_mov_b32 v254, v254\n\t" \
    "s_nop 2\n\t" \
    "v_mov_b32 v254, v254\n\t" \
    "s_nop 2\n\t" \
    "v_mov_b32 v254, v254\n\t" \
    "s_nop 2\n\t" \
    "v_mov_b32 v254, v254\n\t" \
    "s_nop 2\n\t" \
    "v_mov_b32 v254, v254\n\t" \
    "s_nop 2\n\t" \
    "v_mov_b32 v254, v254\n\t" \
    "s_nop 2\n\t" \
    "v_mov_b32 v254, v254\n\t" \
    "s_nop 2\n\t" \
    "v_mov_b32 v254, v254\n\t" \
    "s_nop 2\n\t" \
    "v_mov_b32 v254, v254\n\t" \
    "s_nop 2\n\t" \
    "v_mov_b32 v254, v254\n\t" \
    "s_nop 2\n\t" \
    "v_mov_b32 v254, v254\n\t" \
    "s_nop 2\n\t" \
    "v_mov_b32 v254, v254\n\t" \
    "s_nop 2\n\t" \
    "LW_%=:\n\t" \
    "ds_read_b32 v0, v18 offset:32\n\t" \
    "s_sub_u32 %[lim], %[lim], " STR(DEC1) "\n\t" \
    "s_cmp_gt_i32 %[lim], 0\n\t" \
    "v_mov_b32 v254, v254\n\t" \
    "s_nop 2\n\t" \
    "v_mov_b32 v254, v254\n\t" \
    "s_nop 2\n\t" \
    "v_mov_b32 v254, v254\n\t" \
    "s_nop 2\n\t" \
    "v_mov_b32 v254, v254\n\t" \
    "s_nop 2\n\t" \
    "v_mov_b32 v254, v254\n\t" \
    "s_nop 2\n\t" \
    "s_waitcnt lgkmcnt(0)\n\t" \
    "s_cbranch_scc0 CW_%=\n\t" \
    "v_cmp_ne_u32 vcc, v0, v253\n\t" \
    "s_cbranch_vccnz LW_%=\n\t" \
    "CW_%=:\n\t" \
    "ds_read_b128 v[212:215], v12 offset:24576\n\t" \
    "ds_read_b128 v[216:219], v12 offset:28672\n\t" \
    "ds_read_b128 v[220:223], v15 offset:0\n\t" \
    "ds_read_b128 v[224:227], v12 offset:25600\n\t" \
    "ds_read_b128 v[228:231], v12 offset:29696\n\t" \
    "v_add_u32 v253, %[se], v12\n\t" \
    "v_mov_b32 v254, v254\n\t" \
    "s_nop 2\n\t" \
    "v_mov_b32 v254, v254\n\t" \
    "s_nop 2\n\t" \
    "v_mov_b32 v254, v254\n\t" \
    "s_nop 2\n\t" \
    "v_mov_b32 v254, v254\n\t" \
    "s_nop 2\n\t" \
    "s_waitcnt vmcnt(0)\n\t" \
    "s_waitcnt lgkmcnt(4)\n\t" \
    "v_mfma_f32_16x16x32_bf16 v[0:3], v[212:215], v[236:239], 0\n\t" \
    "ds_read_b128 v[212:215], v15 offset:512\n\t" \
    "s_waitcnt lgkmcnt(4)\n\t" \
    "v_mfma_f32_16x16x32_bf16 v[4:7], v[216:219], v[236:239], 0\n\t" \
    "ds_read_b128 v[216:219], v12 offset:26624\n\t" \
    "s_waitcnt lgkmcnt(4)\n\t" \
    "v_mfma_f32_16x16x32_bf16 v[232:235], v[220:223], v[236:239], 0\n\t" \
    "ds_read_b128 v[220:223], v12 offset:30720\n\t" \
    "s_waitcnt lgkmcnt(4)\n\t" \
    "v_mfma_f32_16x16x32_bf16 v[0:3], v[224:227], v[240:243], v[0:3]\n\t" \
    "ds_read_b128 v[224:227], v15 offset:1024\n\t" \
    "s_waitcnt lgkmcnt(4)\n\t" \
    "v_mfma_f32_16x16x32_bf16 v[4:7], v[228:231], v[240:243], v[4:7]\n\t" \
    "ds_read_b128 v[228:231], v12 offset:27648\n\t" \
    "s_waitcnt lgkmcnt(4)\n\t" \
    "v_mfma_f32_16x16x32_bf16 v[232:235], v[212:215], v[240:243], v[232:235]\n\t" \
    "ds_read_b128 v[212:215], v12 offset:31744\n\t" \
    "s_waitcnt lgkmcnt(4)\n\t" \
    "v_mfma_f32_16x16x32_bf16 v[0:3], v[216:219], v[244:247], v[0:3]\n\t" \
    "ds_read_b128 v[216:219], v15 offset:1536\n\t" \
    "s_waitcnt lgkmcnt(4)\n\t" \
    "v_mfma_f32_16x16x32_bf16 v[4:7], v[220:223], v[244:247], v[4:7]\n\t" \
    "s_waitcnt lgkmcnt(3)\n\t" \
    "v_mfma_f32_16x16x32_bf16 v[232:235], v[224:227], v[244:247], v[232:235]\n\t" \
    "s_waitcnt lgkmcnt(2)\n\t" \
    "v_mfma_f32_16x16x32_bf16 v[0:3], v[228:231], v[248:251], v[0:3]\n\t" \
    "s_waitcnt lgkmcnt(1)\n\t" \
    "v_mfma_f32_16x16x32_bf16 v[4:7], v[212:215], v[248:251], v[4:7]\n\t" \
    "s_waitcnt lgkmcnt(0)\n\t" \
    "v_mfma_f32_16x16x32_bf16 v[232:235], v[216:219], v[248:251], v[232:235]\n\t" \
    "s_barrier\n\t" \
    GSA(4) \
    "ds_read_b128 v[236:239], v253 offset:8192\n\t" \
    "ds_read_b128 v[240:243], v253 offset:9216\n\t" \
    "ds_read_b128 v[244:247], v253 offset:10240\n\t" \
    "ds_read_b128 v[248:251], v253 offset:11264\n\t" \
    "ds_read2st64_b32 v[228:229], v17 offset0:0 offset1:1\n\t" \
    "ds_read2st64_b32 v[230:231], v17 offset0:2 offset1:3\n\t" \
    "s_mov_b64 exec, %[fm]\n\t" \
    "v_mov_b32 v220, %[st]\n\t" \
    "v_mov_b32 v221, %[tag]\n\t" \
    "v_mov_b32 v222, 0\n\t" \
    "buffer_store_dwordx2 v[220:221], v222, %[rs], %[po] offen sc1\n\t" \
    "s_mov_b64 exec, -1\n\t" \
    "v_mov_b32 v254, 0xff800000\n\t" \
    "v_cvt_pk_bf16_f32 v0, 0, v0\n\t" \
    "v_cvt_pk_bf16_f32 v1, 0, v1\n\t" \
    "v_cvt_pk_bf16_f32 v2, 0, v2\n\t" \
    "v_cvt_pk_bf16_f32 v3, 0, v3\n\t" \
    "v_cvt_pk_bf16_f32 v4, 0, v4\n\t" \
    "v_cvt_pk_bf16_f32 v5, 0, v5\n\t" \
    "v_cvt_pk_bf16_f32 v6, 0, v6\n\t" \
    "v_cvt_pk_bf16_f32 v7, 0, v7\n\t" \
    "v_cvt_pk_bf16_f32 v232, 0, v232\n\t" \
    "v_cvt_pk_bf16_f32 v233, 0, v233\n\t" \
    "v_mul_f32 v0, 0x3db504f3, v0\n\t" \
    "v_mul_f32 v1, 0x3db504f3, v1\n\t" \
    "v_mul_f32 v2, 0x3db504f3, v2\n\t" \
    "v_mul_f32 v3, 0x3db504f3, v3\n\t" \
    "v_mul_f32 v4, 0x3db504f3, v4\n\t" \
    "v_mul_f32 v5, 0x3db504f3, v5\n\t" \
    "v_mul_f32 v6, 0x3db504f3, v6\n\t" \
    "v_mul_f32 v7, 0x3db504f3, v7\n\t" \
    "v_mul_f32 v232, 0x3db504f3, v232\n\t" \
    "v_mul_f32 v233, 0x3db504f3, v233\n\t" \
    "v_cvt_pk_bf16_f32 v0, 0, v0\n\t" \
    "v_cvt_pk_bf16_f32 v1, 0, v1\n\t" \
    "v_cvt_pk_bf16_f32 v2, 0, v2\n\t" \
    "v_cvt_pk_bf16_f32 v3, 0, v3\n\t" \
    "v_cvt_pk_bf16_f32 v4, 0, v4\n\t" \
    "v_cvt_pk_bf16_f32 v5, 0, v5\n\t" \
    "v_cvt_pk_bf16_f32 v6, 0, v6\n\t" \
    "v_cvt_pk_bf16_f32 v7, 0, v7\n\t" \
    "v_cvt_pk_bf16_f32 v232, 0, v232\n\t" \
    "v_cvt_pk_bf16_f32 v233, 0, v233\n\t" \
    "v_cndmask_b32 v0, v254, v0, s[72:73]\n\t" \
    "v_cndmask_b32 v1, v254, v1, s[74:75]\n\t" \
    "v_cndmask_b32 v2, v254, v2, s[76:77]\n\t" \
    "v_cndmask_b32 v3, v254, v3, s[78:79]\n\t" \
    "v_cndmask_b32 v4, v254, v4, s[80:81]\n\t" \
    "v_cndmask_b32 v5, v254, v5, s[82:83]\n\t" \
    "v_cndmask_b32 v6, v254, v6, s[84:85]\n\t" \
    "v_cndmask_b32 v7, v254, v7, s[86:87]\n\t" \
    "v_max3_f32 v252, v0, v1, v2\n\t" \
    "v_max3_f32 v255, v3, v4, v5\n\t" \
    "v_max3_f32 v254, v6, v7, v232\n\t" \
    "v_max3_f32 v252, v252, v255, v254\n\t" \
    "v_max_f32 v252, v252, v233\n\t" \
    "v_mov_b32 v255, v252\n\t" \
    "v_mov_b32 v254, v252\n\t" \
    "s_nop 1\n\t" \
    "v_permlane16_swap_b32 v255, v254\n\t" \
    "v_max_f32 v252, v255, v254\n\t" \
    "v_max_f32 v255, v255, v254\n\t" \
    "s_nop 1\n\t" \
    "v_permlane32_swap_b32 v252, v255\n\t" \
    "v_max_f32 v252, v252, v255\n\t" \
    "v_mul_f32 v252, 0xbfb8aa3b, v252\n\t" \
    "v_fma_f32 v0, v0, %[l2e], v252\n\t" \
    "v_fma_f32 v1, v1, %[l2e], v252\n\t" \
    "v_fma_f32 v2, v2, %[l2e], v252\n\t" \
    "v_fma_f32 v3, v3, %[l2e], v252\n\t" \
    "v_fma_f32 v4, v4, %[l2e], v252\n\t" \
    "v_fma_f32 v5, v5, %[l2e], v252\n\t" \
    "v_fma_f32 v6, v6, %[l2e], v252\n\t" \
    "v_fma_f32 v7, v7, %[l2e], v252\n\t" \
    "v_fma_f32 v232, v232, %[l2e], v252\n\t" \
    "v_fma_f32 v233, v233, %[l2e], v252\n\t" \
    "v_exp_f32 v0, v0\n\t" \
    "v_exp_f32 v1, v1\n\t" \
    "v_exp_f32 v2, v2\n\t" \
    "v_exp_f32 v3, v3\n\t" \
    "v_exp_f32 v4, v4\n\t" \
    "v_exp_f32 v5, v5\n\t" \
    "v_exp_f32 v6, v6\n\t" \
    "v_exp_f32 v7, v7\n\t" \
    "v_exp_f32 v232, v232\n\t" \
    "v_exp_f32 v233, v233\n\t" \
    "v_add_f32 v252, v0, v1\n\t" \
    "v_add_f32 v235, v2, v3\n\t" \
    "v_add_f32 v254, v4, v5\n\t" \
    "v_add_f32 v255, v6, v7\n\t" \
    "v_add_f32 v234, v232, v233\n\t" \
    "v_add_f32 v252, v252, v235\n\t" \
    "v_add_f32 v254, v254, v255\n\t" \
    "v_add_f32 v252, v252, v254\n\t" \
    "v_add_f32 v252, v252, v234\n\t" \
    "v_mov_b32 v255, v252\n\t" \
    "v_mov_b32 v254, v252\n\t" \
    "s_nop 1\n\t" \
    "v_permlane16_swap_b32 v255, v254\n\t" \
    "v_add_f32 v252, v255, v254\n\t" \
    "v_add_f32 v255, v255, v254\n\t" \
    "s_nop 1\n\t" \
    "v_permlane32_swap_b32 v252, v255\n\t" \
    "v_add_f32 v252, v252, v255\n\t" \
    "v_rcp_f32 v252, v252\n\t" \
    "s_nop 0\n\t" \
    "v_mul_f32 v0, v0, v252\n\t" \
    "v_mul_f32 v1, v1, v252\n\t" \
    "v_mul_f32 v2, v2, v252\n\t" \
    "v_mul_f32 v3, v3, v252\n\t" \
    "v_mul_f32 v4, v4, v252\n\t" \
    "v_mul_f32 v5, v5, v252\n\t" \
    "v_mul_f32 v6, v6, v252\n\t" \
    "v_mul_f32 v7, v7, v252\n\t" \
    "v_mul_f32 v232, v232, v252\n\t" \
    "v_mul_f32 v233, v233, v252\n\t" \
    "v_cvt_pk_bf16_f32 v0, v0, v1\n\t" \
    "v_cvt_pk_bf16_f32 v1, v2, v3\n\t" \
    "v_cvt_pk_bf16_f32 v2, v4, v5\n\t" \
    "v_cvt_pk_bf16_f32 v3, v6, v7\n\t" \
    "v_cvt_pk_bf16_f32 v4, v232, v233\n\t" \
    "v_mov_b32 v5, 0\n\t" \
    "v_mov_b32 v6, 0\n\t" \
    "v_cvt_pk_bf16_f32 v7, v232, v233\n\t" \
    "s_waitcnt lgkmcnt(0)\n\t" \
    "v_mfma_f32_16x16x32_bf16 v[236:239], v[236:239], v[0:3], 0\n\t" \
    "v_mfma_f32_16x16x32_bf16 v[240:243], v[240:243], v[0:3], 0\n\t" \
    "v_mfma_f32_16x16x32_bf16 v[244:247], v[244:247], v[0:3], 0\n\t" \
    "v_mfma_f32_16x16x32_bf16 v[248:251], v[248:251], v[0:3], 0\n\t" \
    "v_mfma_f32_16x16x16_bf16 v[236:239], v[228:229], v[4:5], v[236:239]\n\t" \
    "v_mfma_f32_16x16x16_bf16 v[240:243], v[228:229], v[6:7], v[240:243]\n\t" \
    "v_mfma_f32_16x16x16_bf16 v[244:247], v[230:231], v[4:5], v[244:247]\n\t" \
    "v_mfma_f32_16x16x16_bf16 v[248:251], v[230:231], v[6:7], v[248:251]\n\t" \
    "s_nop 4\n\t" \
    "v_cvt_pk_bf16_f32 v212, v236, v237\n\t" \
    "v_cvt_pk_bf16_f32 v213, v240, v241\n\t" \
    "v_cvt_pk_bf16_f32 v214, v238, v239\n\t" \
    "v_cvt_pk_bf16_f32 v215, v242, v243\n\t" \
    "v_permlane16_swap_b32 v212, v213\n\t" \
    "s_nop 0\n\t" \
    "v_permlane16_swap_b32 v214, v215\n\t" \
    "v_cvt_pk_bf16_f32 v216, v244, v245\n\t" \
    "v_cvt_pk_bf16_f32 v217, v248, v249\n\t" \
    "v_cvt_pk_bf16_f32 v218, v246, v247\n\t" \
    "v_cvt_pk_bf16_f32 v219, v250, v251\n\t" \
    "v_permlane16_swap_b32 v216, v217\n\t" \
    "s_nop 0\n\t" \
    "v_permlane16_swap_b32 v218, v219\n\t" \
    "s_setprio 1\n\t" \
    GSA(2) \
    GSW \
    "s_nop 1\n\t" \
    : [lim] "+s"(lim), "+{v[212:219]}"(oxr) DGO GSO(0) GSO(1) GSO(2) GSO(3) GSO(4) \
    : "{v8}"(am_a), "{v9}"(t0_a), "{v10}"(idx), "{v11}"(iw_a), "{v12}"(ab_a), "{v13}"(sw_a), "{v14}"(xo_v), "{v15}"(kr_a), "{v16}"(dq), "{v17}"(vl_a), "{v18}"(fr_a), [stag] "s"(0xffffff81u + s), [onej] "s"(onej), [rs] "s"(rs), [m0] "s"(sbm + 16384u), [m1] "s"(sbm + 20480u), [st] "s"(s), [tag] "s"(P.tag), [po] "s"(FLG_OFF + F_PRG(4, h) * 128), [fm] "s"(fmask), [qx] "s"(qxo), [qd] "s"(lbase + LA_VM), [qh] "s"((j & 1) * 2048), [ox] "s"(oxo), [se] "s"((j & 1) * 4096), [l2e] "s"(0x3fb8aa3b), "{v[20:27]}"(wr[0][0]), "{v[28:35]}"(wr[0][1]), "{v[36:43]}"(wr[1][0]), "{v[44:51]}"(wr[1][1]), "{v[52:59]}"(wr[2][0]), "{v[60:67]}"(wr[2][1]), "{v[68:75]}"(wr[3][0]), "{v[76:83]}"(wr[3][1]), "{v[84:91]}"(wr[4][0]), "{v[92:99]}"(wr[4][1]), "{v[100:107]}"(wr[5][0]), "{v[108:115]}"(wr[5][1]), "{v[116:123]}"(wr[6][0]), "{v[124:131]}"(wr[6][1]), "{v[132:139]}"(wr[7][0]), "{v[140:147]}"(wr[7][1]), "{v[148:155]}"(wr[8][0]), "{v[156:163]}"(wr[8][1]), "{v[164:171]}"(wr[9][0]), "{v[172:179]}"(wr[9][1]), "{v[180:187]}"(wr[10][0]), "{v[188:195]}"(wr[10][1]), "{v[196:203]}"(wr[11][0]), "{v[204:211]}"(wr[11][1]) \
    : "memory", "scc", "vcc", "s90", "s91", "s72", "s73", "s74", "s75", "s76", "s77", "s78", "s79", "s80", "s81", "s82", "s83", "s84", "s85", "s86", "s87", "v0", "v1", "v2", "v3", "v4", "v5", "v6", "v7", "v220", "v221", "v222", "v223", "v224", "v225", "v226", "v227", "v228", "v229", "v230", "v231", "v232", "v233", "v234", "v235", "v236", "v237", "v238", "v239", "v240", "v241", "v242", "v243", "v244", "v245", "v246", "v247", "v248", "v249", "v250", "v251", "v252", "v253", "v254", "v255")

#define B_POLL() \
  asm volatile( \
    "s_getpc_b64 s[92:93]\n\t" \
    "TPB_%=:\n\t" \
    "s_add_u32 s92, s92, TEB_%=-TPB_%=\n\t" \
    "s_addc_u32 s93, s93, 0\n\t" \
    "s_lshr_b32 s91, %[tgt], 2\n\t" \
    "s_sub_u32 s91, s91, 3\n\t" \
    "TWB_%=:\n\t" \
    "s_memrealtime s[88:89]\n\t" \
    DGE \
    "s_waitcnt lgkmcnt(0)\n\t" \
    "s_sub_u32 s89, s91, s88\n\t" \
    "s_lshl_b32 s89, s89, 2\n\t" \
    "s_cmp_gt_i32 s89, 0\n\t" \
    "s_cbranch_scc1 TWB_%=\n\t" \
    "s_lshl_b32 s88, s88, 2\n\t" \
    "s_sub_u32 s89, %[tgt], s88\n\t" \
    "s_max_i32 s89, s89, 0\n\t" \
    "s_mul_i32 s89, s89, 11\n\t" \
    "s_lshr_b32 s89, s89, 1\n\t" \
    "s_and_b32 s89, s89, 0x7c\n\t" \
    "s_sub_u32 s92, s92, s89\n\t" \
    "s_subb_u32 s93, s93, 0\n\t" \
    "s_setpc_b64 s[92:93]\n\t" \
    "s_nop 0\n\t" \
    "s_nop 0\n\t" \
    "s_nop 0\n\t" \
    "s_nop 0\n\t" \
    "s_nop 0\n\t" \
    "s_nop 0\n\t" \
    "s_nop 0\n\t" \
    "s_nop 0\n\t" \
    "s_nop 0\n\t" \
    "s_nop 0\n\t" \
    "s_nop 0\n\t" \
    "s_nop 0\n\t" \
    "s_nop 0\n\t" \
    "s_nop 0\n\t" \
    "s_nop 0\n\t" \
    "s_nop 0\n\t" \
    "s_nop 0\n\t" \
    "s_nop 0\n\t" \
    "s_nop 0\n\t" \
    "s_nop 0\n\t" \
    "s_nop 0\n\t" \
    "s_nop 0\n\t" \
    "TEB_%=:\n\t" \
    "buffer_load_dwordx4 v[220:223], %[l16], %[rs], %[m0] offen offset:0 sc1\n\t" \
    "buffer_load_dwordx4 v[224:227], %[l16], %[rs], %[m0] offen offset:1024 sc1\n\t" \
    "buffer_load_dwordx4 v[228:231], %[l16], %[rs], %[m1] offen offset:0 sc1\n\t" \
    "buffer_load_dwordx4 v[232:235], %[l16], %[rs], %[m1] offen offset:1024 sc1\n\t" \
    "v_mov_b32 v252, v252\n\t" \
    "s_nop 2\n\t" \
    "v_mov_b32 v252, v252\n\t" \
    "s_nop 2\n\t" \
    "v_mov_b32 v252, v252\n\t" \
    "s_nop 2\n\t" \
    "v_mov_b32 v252, v252\n\t" \
    "s_nop 2\n\t" \
    "v_mov_b32 v252, v252\n\t" \
    "s_nop 2\n\t" \
    "v_mov_b32 v252, v252\n\t" \
    "s_nop 2\n\t" \
    "v_mov_b32 v252, v252\n\t" \
    "s_nop 2\n\t" \
    "v_mov_b32 v252, v252\n\t" \
    "s_nop 2\n\t" \
    "v_mov_b32 v252, v252\n\t" \
    "s_nop 2\n\t" \
    "v_mov_b32 v252, v252\n\t" \
    "s_nop 2\n\t" \
    "v_mov_b32 v252, v252\n\t" \
    "s_nop 2\n\t" \
    "v_mov_b32 v252, v252\n\t" \
    "s_nop 2\n\t" \
    "v_mov_b32 v252, v252\n\t" \
    "s_nop 2\n\t" \
    "v_mov_b32 v252, v252\n\t" \
    "s_nop 2\n\t" \
    "v_mov_b32 v252, v252\n\t" \
    "s_nop 2\n\t" \
    "v_mov_b32 v252, v252\n\t" \
    "s_nop 2\n\t" \
    "v_mov_b32 v252, v252\n\t" \
    "s_nop 2\n\t" \
    "v_mov_b32 v252, v252\n\t" \
    "s_nop 2\n\t" \
    "v_mov_b32 v252, v252\n\t" \
    "s_nop 2\n\t" \
    "v_mov_b32 v252, v252\n\t" \
    "s_nop 2\n\t" \
    "v_mov_b32 v252, v252\n\t" \
    "s_nop 2\n\t" \
    "v_mov_b32 v252, v252\n\t" \
    "s_nop 2\n\t" \
    "v_mov_b32 v252, v252\n\t" \
    "s_nop 2\n\t" \
    "v_mov_b32 v252, v252\n\t" \
    "s_nop 2\n\t" \
    "PM_%=:\n\t" \
    "s_waitcnt vmcnt(3)\n\t" \
    "ds_write_b128 %[la], v[220:223] offset:0\n\t" \
    "v_max3_u32 v252, v220, v220, v221\n\t" \
    "v_max3_u32 v252, v252, v222, v223\n\t" \
    "s_waitcnt vmcnt(2)\n\t" \
    "ds_write_b128 %[la], v[224:227] offset:1024\n\t" \
    "v_max3_u32 v252, v252, v224, v225\n\t" \
    "v_max3_u32 v252, v252, v226, v227\n\t" \
    "s_waitcnt vmcnt(1)\n\t" \
    "ds_write_b128 %[la], v[228:231] offset:8192\n\t" \
    "v_max3_u32 v252, v252, v228, v229\n\t" \
    "v_max3_u32 v252, v252, v230, v231\n\t" \
    "s_waitcnt vmcnt(0)\n\t" \
    "ds_write_b128 %[la], v[232:235] offset:9216\n\t" \
    "v_max3_u32 v252, v252, v232, v233\n\t" \
    "v_max3_u32 v252, v252, v234, v235\n\t" \
    "v_cmp_eq_u32 vcc, -1, v252\n\t" \
    "s_cbranch_vccz PD_%=\n\t" \
    "s_sub_u32 %[lim], %[lim], 1\n\t" \
    "s_cmp_gt_i32 %[lim], 0\n\t" \
    "s_cbranch_scc0 PD_%=\n\t" \
    "s_sleep " STR(CADM) "\n\t" \
    "buffer_load_dwordx4 v[220:223], %[l16], %[rs], %[m0] offen offset:0 sc1\n\t" \
    "buffer_load_dwordx4 v[224:227], %[l16], %[rs], %[m0] offen offset:1024 sc1\n\t" \
    "buffer_load_dwordx4 v[228:231], %[l16], %[rs], %[m1] offen offset:0 sc1\n\t" \
    "buffer_load_dwordx4 v[232:235], %[l16], %[rs], %[m1] offen offset:1024 sc1\n\t" \
    "s_branch PM_%=\n\t" \
    "PD_%=:\n\t" \
    GSA(3) \
    "s_waitcnt lgkmcnt(0)\n\t" \
    "s_barrier\n\t" \
    : [lim] "+s"(lim) GSO(3) \
    : [l16] "v"(l16), [la] "v"(la), [rs] "s"(rs), [m0] "s"(mo), [m1] "s"(mo + 16384u), [tgt] "s"(tgt) \
    : "memory", "vcc", "scc", "v252", "s88", "s89", "s90", "s91", "s92", "s93", "s94", "s95", "v220", "v221", "v222", "v223", "v224", "v225", "v226", "v227", "v228", "v229", "v230", "v231", "v232", "v233", "v234", "v235")

#define T_WAIT() \
  asm volatile( \
    "s_getpc_b64 s[92:93]\n\t" \
    "TPC_%=:\n\t" \
    "s_add_u32 s92, s92, TEC_%=-TPC_%=\n\t" \
    "s_addc_u32 s93, s93, 0\n\t" \
    "s_lshr_b32 s91, %[tgt], 2\n\t" \
    "s_sub_u32 s91, s91, 3\n\t" \
    "TWC_%=:\n\t" \
    "s_memrealtime s[88:89]\n\t" \
    DGE \
    "s_waitcnt lgkmcnt(0)\n\t" \
    "s_sub_u32 s89, s91, s88\n\t" \
    "s_lshl_b32 s89, s89, 2\n\t" \
    "s_cmp_gt_i32 s89, 0\n\t" \
    "s_cbranch_scc1 TWC_%=\n\t" \
    "s_lshl_b32 s88, s88, 2\n\t" \
    "s_sub_u32 s89, %[tgt], s88\n\t" \
    "s_max_i32 s89, s89, 0\n\t" \
    "s_mul_i32 s89, s89, 11\n\t" \
    "s_lshr_b32 s89, s89, 1\n\t" \
    "s_and_b32 s89, s89, 0x7c\n\t" \
    "s_sub_u32 s92, s92, s89\n\t" \
    "s_subb_u32 s93, s93, 0\n\t" \
    "s_setpc_b64 s[92:93]\n\t" \
    "s_nop 0\n\t" \
    "s_nop 0\n\t" \
    "s_nop 0\n\t" \
    "s_nop 0\n\t" \
    "s_nop 0\n\t" \
    "s_nop 0\n\t" \
    "s_nop 0\n\t" \
    "s_nop 0\n\t" \
    "s_nop 0\n\t" \
    "s_nop 0\n\t" \
    "s_nop 0\n\t" \
    "s_nop 0\n\t" \
    "s_nop 0\n\t" \
    "s_nop 0\n\t" \
    "s_nop 0\n\t" \
    "s_nop 0\n\t" \
    "s_nop 0\n\t" \
    "s_nop 0\n\t" \
    "s_nop 0\n\t" \
    "s_nop 0\n\t" \
    "s_nop 0\n\t" \
    "s_nop 0\n\t" \
    "TEC_%=:\n\t" \
    :  \
    : [tgt] "s"(tgt) \
    : "s88", "s89", "s90", "s91", "s92", "s93", "s94", "s95", "memory", "scc")

#define V_PROJ() \
  asm volatile( \
    "v_mov_b32 v0, 0\n\t" \
    "v_mov_b32 v1, 0\n\t" \
    "v_mov_b32 v2, 0\n\t" \
    "v_mov_b32 v3, 0\n\t" \
    "v_mov_b32 v4, 0\n\t" \
    "v_mov_b32 v5, 0\n\t" \
    "v_mov_b32 v6, 0\n\t" \
    "v_mov_b32 v7, 0\n\t" \
    "s_barrier\n\t" \
    "ds_read_b128 v[220:223], %[am] offset:0\n\t" \
    "ds_read_b128 v[224:227], %[am] offset:1024\n\t" \
    "ds_read_b128 v[228:231], %[am] offset:2048\n\t" \
    "ds_read_b128 v[232:235], %[am] offset:3072\n\t" \
    "ds_read_b128 v[236:239], %[am] offset:4096\n\t" \
    "ds_read_b128 v[240:243], %[am] offset:5120\n\t" \
    "ds_read_b128 v[244:247], %[am] offset:6144\n\t" \
    "ds_read_b128 v[248:251], %[am] offset:7168\n\t" \
    "s_waitcnt lgkmcnt(7)\n\t" \
    "v_smfmac_f32_16x16x64_bf16 v[0:3], v[220:223], v[28:35], %[idx]\n\t" \
    "ds_read_b128 v[220:223], %[am] offset:8192\n\t" \
    "s_waitcnt lgkmcnt(7)\n\t" \
    "v_smfmac_f32_16x16x64_bf16 v[4:7], v[224:227], v[36:43], %[idx]\n\t" \
    "ds_read_b128 v[224:227], %[am] offset:9216\n\t" \
    "s_waitcnt lgkmcnt(7)\n\t" \
    "v_smfmac_f32_16x16x64_bf16 v[0:3], v[228:231], v[44:51], %[idx]\n\t" \
    "ds_read_b128 v[228:231], %[am] offset:10240\n\t" \
    "s_waitcnt lgkmcnt(7)\n\t" \
    "v_smfmac_f32_16x16x64_bf16 v[4:7], v[232:235], v[52:59], %[idx]\n\t" \
    "ds_read_b128 v[232:235], %[am] offset:11264\n\t" \
    "s_waitcnt lgkmcnt(7)\n\t" \
    "v_smfmac_f32_16x16x64_bf16 v[0:3], v[236:239], v[60:67], %[idx]\n\t" \
    "ds_read_b128 v[236:239], %[am] offset:12288\n\t" \
    "s_waitcnt lgkmcnt(7)\n\t" \
    "v_smfmac_f32_16x16x64_bf16 v[4:7], v[240:243], v[68:75], %[idx]\n\t" \
    "ds_read_b128 v[240:243], %[am] offset:13312\n\t" \
    "s_waitcnt lgkmcnt(7)\n\t" \
    "v_smfmac_f32_16x16x64_bf16 v[0:3], v[244:247], v[76:83], %[idx]\n\t" \
    "ds_read_b128 v[244:247], %[am] offset:14336\n\t" \
    "s_waitcnt lgkmcnt(7)\n\t" \
    "v_smfmac_f32_16x16x64_bf16 v[4:7], v[248:251], v[84:91], %[idx]\n\t" \
    "ds_read_b128 v[248:251], %[am] offset:15360\n\t" \
    "s_waitcnt lgkmcnt(7)\n\t" \
    "v_smfmac_f32_16x16x64_bf16 v[0:3], v[220:223], v[92:99], %[idx]\n\t" \
    "s_waitcnt lgkmcnt(6)\n\t" \
    "v_smfmac_f32_16x16x64_bf16 v[4:7], v[224:227], v[100:107], %[idx]\n\t" \
    "s_waitcnt lgkmcnt(5)\n\t" \
    "v_smfmac_f32_16x16x64_bf16 v[0:3], v[228:231], v[108:115], %[idx]\n\t" \
    "s_waitcnt lgkmcnt(4)\n\t" \
    "v_smfmac_f32_16x16x64_bf16 v[4:7], v[232:235], v[116:123], %[idx]\n\t" \
    "s_waitcnt lgkmcnt(3)\n\t" \
    "v_smfmac_f32_16x16x64_bf16 v[0:3], v[236:239], v[124:131], %[idx]\n\t" \
    "s_waitcnt lgkmcnt(2)\n\t" \
    "v_smfmac_f32_16x16x64_bf16 v[4:7], v[240:243], v[132:139], %[idx]\n\t" \
    "s_waitcnt lgkmcnt(1)\n\t" \
    "v_smfmac_f32_16x16x64_bf16 v[0:3], v[244:247], v[140:147], %[idx]\n\t" \
    "s_waitcnt lgkmcnt(0)\n\t" \
    "v_smfmac_f32_16x16x64_bf16 v[4:7], v[248:251], v[148:155], %[idx]\n\t" \
    "s_nop 5\n\t" \
    "v_add_f32 v0, v0, v1\n\t" \
    "v_add_f32 v2, v2, v3\n\t" \
    "v_add_f32 v4, v4, v5\n\t" \
    "v_add_f32 v6, v6, v7\n\t" \
    "v_add_f32 v0, v0, v4\n\t" \
    "v_add_f32 v2, v2, v6\n\t" \
    "v_cvt_pk_bf16_f32 v0, v0, v2\n\t" \
    "v_min_u32 v0, -2, v0\n\t" \
    "s_nop 1\n\t" \
    "v_mov_b32_dpp v1, v0 quad_perm:[1,1,1,1] row_mask:0xf bank_mask:0xf\n\t" \
    "v_mov_b32_dpp v2, v0 quad_perm:[2,2,2,2] row_mask:0xf bank_mask:0xf\n\t" \
    "v_mov_b32_dpp v3, v0 quad_perm:[3,3,3,3] row_mask:0xf bank_mask:0xf\n\t" \
    "s_mov_b64 exec, %[qm]\n\t" \
    "buffer_store_dwordx4 v[0:3], %[l4], %[rs], %[vo] offen sc1\n\t" \
    "s_mov_b64 exec, -1\n\t" \
    "s_nop 1\n\t" \
    :  \
    : [am] "v"(am_a), [idx] "v"(idx), [l4] "v"(ln * 4), [rs] "s"(rs), [vo] "s"(sb + vo), [qm] "s"(0x1111111111111111ull), "{v[28:35]}"(wv[0]), "{v[36:43]}"(wv[1]), "{v[44:51]}"(wv[2]), "{v[52:59]}"(wv[3]), "{v[60:67]}"(wv[4]), "{v[68:75]}"(wv[5]), "{v[76:83]}"(wv[6]), "{v[84:91]}"(wv[7]), "{v[92:99]}"(wv[8]), "{v[100:107]}"(wv[9]), "{v[108:115]}"(wv[10]), "{v[116:123]}"(wv[11]), "{v[124:131]}"(wv[12]), "{v[132:139]}"(wv[13]), "{v[140:147]}"(wv[14]), "{v[148:155]}"(wv[15]) \
    : "memory", "v0", "v1", "v2", "v3", "v4", "v5", "v6", "v7", "v220", "v221", "v222", "v223", "v224", "v225", "v226", "v227", "v228", "v229", "v230", "v231", "v232", "v233", "v234", "v235", "v236", "v237", "v238", "v239", "v240", "v241", "v242", "v243", "v244", "v245", "v246", "v247", "v248", "v249", "v250", "v251")

#define WO_V() \
  asm volatile( \
    "v_mov_b32 v0, 0\n\t" \
    "v_mov_b32 v1, 0\n\t" \
    "v_mov_b32 v2, 0\n\t" \
    "v_mov_b32 v3, 0\n\t" \
    "v_mov_b32 v4, 0\n\t" \
    "v_mov_b32 v5, 0\n\t" \
    "v_mov_b32 v6, 0\n\t" \
    "v_mov_b32 v7, 0\n\t" \
    "v_mov_b32 v8, 0\n\t" \
    "v_mov_b32 v9, 0\n\t" \
    "v_mov_b32 v10, 0\n\t" \
    "v_mov_b32 v11, 0\n\t" \
    "v_mov_b32 v12, 0\n\t" \
    "v_mov_b32 v13, 0\n\t" \
    "v_mov_b32 v14, 0\n\t" \
    "v_mov_b32 v15, 0\n\t" \
    "v_mov_b32 v252, 0\n\t" \
    "s_barrier\n\t" \
    GSA(4) \
    DGC \
    "buffer_load_dwordx4 v[220:223], %[l16], %[rs], %[s0] offen sc1\n\t" \
    "s_nop 7\n\t" \
    "buffer_load_dwordx4 v[224:227], %[l16], %[rs], %[s1] offen sc1\n\t" \
    "A_%=:\n\t" \
    "s_waitcnt vmcnt(1)\n\t" \
    "v_smfmac_f32_16x16x64_bf16 v[0:3], v[220:223], v[156:163], %[idx]\n\t" \
    "v_max3_u32 v252, v252, v220, v221\n\t" \
    "v_smfmac_f32_16x16x64_bf16 v[4:7], v[220:223], v[164:171], %[idx]\n\t" \
    "v_max3_u32 v252, v252, v222, v223\n\t" \
    "v_smfmac_f32_16x16x64_bf16 v[8:11], v[220:223], v[172:179], %[idx]\n\t" \
    "v_smfmac_f32_16x16x64_bf16 v[12:15], v[220:223], v[180:187], %[idx]\n\t" \
    "s_waitcnt vmcnt(0)\n\t" \
    "v_smfmac_f32_16x16x64_bf16 v[0:3], v[224:227], v[188:195], %[idx]\n\t" \
    "v_max3_u32 v252, v252, v224, v225\n\t" \
    "v_smfmac_f32_16x16x64_bf16 v[4:7], v[224:227], v[196:203], %[idx]\n\t" \
    "v_max3_u32 v252, v252, v226, v227\n\t" \
    "v_smfmac_f32_16x16x64_bf16 v[8:11], v[224:227], v[204:211], %[idx]\n\t" \
    "v_smfmac_f32_16x16x64_bf16 v[12:15], v[224:227], v[212:219], %[idx]\n\t" \
    "s_setprio 0\n\t" \
    "v_cmp_eq_u32 vcc, -1, v252\n\t" \
    "s_cbranch_vccnz SL_%=\n\t" \
    "D_%=:\n\t" \
    GSA(1) \
    "s_nop 2\n\t" \
    "v_add_f32 v0, v0, v1\n\t" \
    "v_add_f32 v2, v2, v3\n\t" \
    "v_add_f32 v4, v4, v5\n\t" \
    "v_add_f32 v6, v6, v7\n\t" \
    "v_add_f32 v8, v8, v9\n\t" \
    "v_add_f32 v10, v10, v11\n\t" \
    "v_add_f32 v12, v12, v13\n\t" \
    "v_add_f32 v14, v14, v15\n\t" \
    "v_permlane32_swap_b32 v0, v4\n\t" \
    "v_permlane32_swap_b32 v2, v6\n\t" \
    "v_permlane32_swap_b32 v8, v12\n\t" \
    "v_permlane32_swap_b32 v10, v14\n\t" \
    "v_add_f32 v0, v0, v12\n\t" \
    "v_add_f32 v2, v2, v14\n\t" \
    "ds_write2_b32 %[rw], v0, v2 offset0:0 offset1:32\n\t" \
    "s_setprio 2\n\t" \
    "s_waitcnt lgkmcnt(0)\n\t" \
    "s_barrier\n\t" \
    "s_cmp_eq_u32 %[red], 0\n\t" \
    "s_cbranch_scc0 E_%=\n\t" \
    "ds_read2st64_b64 v[0:3], %[rr] offset0:0 offset1:1\n\t" \
    "ds_read2st64_b64 v[4:7], %[rr] offset0:2 offset1:3\n\t" \
    "ds_read2st64_b64 v[8:11], %[rr] offset0:4 offset1:5\n\t" \
    "ds_read2st64_b64 v[12:15], %[rr] offset0:6 offset1:7\n\t" \
    "s_waitcnt lgkmcnt(3)\n\t" \
    "v_pk_add_f32 v[0:1], v[0:1], v[2:3]\n\t" \
    "s_waitcnt lgkmcnt(2)\n\t" \
    "v_pk_add_f32 v[4:5], v[4:5], v[6:7]\n\t" \
    "s_waitcnt lgkmcnt(1)\n\t" \
    "v_pk_add_f32 v[8:9], v[8:9], v[10:11]\n\t" \
    "v_pk_add_f32 v[0:1], v[0:1], v[4:5]\n\t" \
    "s_waitcnt lgkmcnt(0)\n\t" \
    "v_pk_add_f32 v[12:13], v[12:13], v[14:15]\n\t" \
    "s_nop 0\n\t" \
    "v_pk_add_f32 v[8:9], v[8:9], v[12:13]\n\t" \
    "s_nop 0\n\t" \
    "v_pk_add_f32 v[0:1], v[0:1], v[8:9]\n\t" \
    "s_nop 0\n\t" \
    "v_cvt_pk_bf16_f32 v0, v0, v1\n\t" \
    "v_min_u32 v0, -2, v0\n\t" \
    "buffer_store_dword v0, %[mo], %[rs], %[sn] offen sc1\n\t" \
    "s_memrealtime %[t1]\n\t" \
    DGA \
    "s_waitcnt lgkmcnt(0)\n\t" \
    GSA(2) \
    "s_branch E_%=\n\t" \
    "SL_%=:\n\t" \
    "s_sleep 1\n\t" \
    "buffer_load_dwordx4 v[220:223], %[l16], %[rs], %[s0] offen sc1\n\t" \
    "buffer_load_dwordx4 v[224:227], %[l16], %[rs], %[s1] offen sc1\n\t" \
    "s_sub_u32 %[lim], %[lim], " STR(DEC2) "\n\t" \
    "s_cmp_gt_i32 %[lim], 0\n\t" \
    "s_cbranch_scc0 G_%=\n\t" \
    "s_nop 3\n\t" \
    "v_mov_b32 v0, 0\n\t" \
    "v_mov_b32 v1, 0\n\t" \
    "v_mov_b32 v2, 0\n\t" \
    "v_mov_b32 v3, 0\n\t" \
    "v_mov_b32 v4, 0\n\t" \
    "v_mov_b32 v5, 0\n\t" \
    "v_mov_b32 v6, 0\n\t" \
    "v_mov_b32 v7, 0\n\t" \
    "v_mov_b32 v8, 0\n\t" \
    "v_mov_b32 v9, 0\n\t" \
    "v_mov_b32 v10, 0\n\t" \
    "v_mov_b32 v11, 0\n\t" \
    "v_mov_b32 v12, 0\n\t" \
    "v_mov_b32 v13, 0\n\t" \
    "v_mov_b32 v14, 0\n\t" \
    "v_mov_b32 v15, 0\n\t" \
    "v_mov_b32 v252, 0\n\t" \
    "s_branch A_%=\n\t" \
    "G_%=:\n\t" \
    "s_waitcnt vmcnt(0)\n\t" \
    "s_branch D_%=\n\t" \
    "E_%=:\n\t" \
    GSW \
    : [lim] "+s"(lim) GSO(1) GSO(4), [t1] "=&s"(t1) DGO DGQ GSO(2) \
    : [l16] "v"(l16), [rs] "s"(rs), [idx] "v"(idx), [rw] "v"(rw_a), [s0] "s"(so[0]), [s1] "s"(so[1]), [rr] "v"(rr_a), [mo] "v"(mo_v), [sn] "s"(sn), [red] "s"(wid), "{v[156:163]}"(wo[0][0][0]), "{v[164:171]}"(wo[0][0][1]), "{v[172:179]}"(wo[0][1][0]), "{v[180:187]}"(wo[0][1][1]), "{v[188:195]}"(wo[1][0][0]), "{v[196:203]}"(wo[1][0][1]), "{v[204:211]}"(wo[1][1][0]), "{v[212:219]}"(wo[1][1][1]) \
    : "memory", "vcc", "scc", "v252", "v0", "v1", "v2", "v3", "v4", "v5", "v6", "v7", "v8", "v9", "v10", "v11", "v12", "v13", "v14", "v15", "v220", "v221", "v222", "v223", "v224", "v225", "v226", "v227")

#define WO_W() \
  asm volatile( \
    "v_mov_b32 v0, 0\n\t" \
    "v_mov_b32 v1, 0\n\t" \
    "v_mov_b32 v2, 0\n\t" \
    "v_mov_b32 v3, 0\n\t" \
    "v_mov_b32 v4, 0\n\t" \
    "v_mov_b32 v5, 0\n\t" \
    "v_mov_b32 v6, 0\n\t" \
    "v_mov_b32 v7, 0\n\t" \
    "v_mov_b32 v8, 0\n\t" \
    "v_mov_b32 v9, 0\n\t" \
    "v_mov_b32 v10, 0\n\t" \
    "v_mov_b32 v11, 0\n\t" \
    "v_mov_b32 v12, 0\n\t" \
    "v_mov_b32 v13, 0\n\t" \
    "v_mov_b32 v14, 0\n\t" \
    "v_mov_b32 v15, 0\n\t" \
    "v_mov_b32 v252, 0\n\t" \
    "s_setprio 1\n\t" \
    "s_barrier\n\t" \
    GSA(4) \
    "buffer_load_dwordx4 v[220:223], %[l16], %[rs], %[s0] offen sc1\n\t" \
    "s_nop 15\n\t" \
    "buffer_load_dwordx4 v[224:227], %[l16], %[rs], %[s1] offen sc1\n\t" \
    "s_nop 15\n\t" \
    "buffer_load_dwordx4 v[228:231], %[l16], %[rs], %[s2] offen sc1\n\t" \
    "s_nop 15\n\t" \
    "buffer_load_dwordx4 v[232:235], %[l16], %[rs], %[s3] offen sc1\n\t" \
    "s_nop 15\n\t" \
    "buffer_load_dwordx4 v[236:239], %[l16], %[rs], %[s4] offen sc1\n\t" \
    "s_nop 15\n\t" \
    "buffer_load_dwordx4 v[240:243], %[l16], %[rs], %[s5] offen sc1\n\t" \
    "A_%=:\n\t" \
    "s_waitcnt vmcnt(5)\n\t" \
    "v_smfmac_f32_16x16x64_bf16 v[0:3], v[220:223], v[28:35], %[idx]\n\t" \
    "v_max3_u32 v252, v252, v220, v221\n\t" \
    "v_smfmac_f32_16x16x64_bf16 v[4:7], v[220:223], v[36:43], %[idx]\n\t" \
    "v_max3_u32 v252, v252, v222, v223\n\t" \
    "v_smfmac_f32_16x16x64_bf16 v[8:11], v[220:223], v[44:51], %[idx]\n\t" \
    "v_smfmac_f32_16x16x64_bf16 v[12:15], v[220:223], v[52:59], %[idx]\n\t" \
    "s_waitcnt vmcnt(4)\n\t" \
    "v_smfmac_f32_16x16x64_bf16 v[0:3], v[224:227], v[60:67], %[idx]\n\t" \
    "v_max3_u32 v252, v252, v224, v225\n\t" \
    "v_smfmac_f32_16x16x64_bf16 v[4:7], v[224:227], v[68:75], %[idx]\n\t" \
    "v_max3_u32 v252, v252, v226, v227\n\t" \
    "v_smfmac_f32_16x16x64_bf16 v[8:11], v[224:227], v[76:83], %[idx]\n\t" \
    "v_smfmac_f32_16x16x64_bf16 v[12:15], v[224:227], v[84:91], %[idx]\n\t" \
    "s_waitcnt vmcnt(3)\n\t" \
    "v_smfmac_f32_16x16x64_bf16 v[0:3], v[228:231], v[92:99], %[idx]\n\t" \
    "v_max3_u32 v252, v252, v228, v229\n\t" \
    "v_smfmac_f32_16x16x64_bf16 v[4:7], v[228:231], v[100:107], %[idx]\n\t" \
    "v_max3_u32 v252, v252, v230, v231\n\t" \
    "v_smfmac_f32_16x16x64_bf16 v[8:11], v[228:231], v[108:115], %[idx]\n\t" \
    "v_smfmac_f32_16x16x64_bf16 v[12:15], v[228:231], v[116:123], %[idx]\n\t" \
    "s_waitcnt vmcnt(2)\n\t" \
    "v_smfmac_f32_16x16x64_bf16 v[0:3], v[232:235], v[124:131], %[idx]\n\t" \
    "v_max3_u32 v252, v252, v232, v233\n\t" \
    "v_smfmac_f32_16x16x64_bf16 v[4:7], v[232:235], v[132:139], %[idx]\n\t" \
    "v_max3_u32 v252, v252, v234, v235\n\t" \
    "v_smfmac_f32_16x16x64_bf16 v[8:11], v[232:235], v[140:147], %[idx]\n\t" \
    "v_smfmac_f32_16x16x64_bf16 v[12:15], v[232:235], v[148:155], %[idx]\n\t" \
    "s_waitcnt vmcnt(1)\n\t" \
    "v_smfmac_f32_16x16x64_bf16 v[0:3], v[236:239], v[156:163], %[idx]\n\t" \
    "v_max3_u32 v252, v252, v236, v237\n\t" \
    "v_smfmac_f32_16x16x64_bf16 v[4:7], v[236:239], v[164:171], %[idx]\n\t" \
    "v_max3_u32 v252, v252, v238, v239\n\t" \
    "v_smfmac_f32_16x16x64_bf16 v[8:11], v[236:239], v[172:179], %[idx]\n\t" \
    "v_smfmac_f32_16x16x64_bf16 v[12:15], v[236:239], v[180:187], %[idx]\n\t" \
    "s_waitcnt vmcnt(0)\n\t" \
    "v_smfmac_f32_16x16x64_bf16 v[0:3], v[240:243], v[188:195], %[idx]\n\t" \
    "v_max3_u32 v252, v252, v240, v241\n\t" \
    "v_smfmac_f32_16x16x64_bf16 v[4:7], v[240:243], v[196:203], %[idx]\n\t" \
    "v_max3_u32 v252, v252, v242, v243\n\t" \
    "v_smfmac_f32_16x16x64_bf16 v[8:11], v[240:243], v[204:211], %[idx]\n\t" \
    "v_smfmac_f32_16x16x64_bf16 v[12:15], v[240:243], v[212:219], %[idx]\n\t" \
    "v_cmp_eq_u32 vcc, -1, v252\n\t" \
    "s_cbranch_vccnz SL_%=\n\t" \
    "D_%=:\n\t" \
    GSA(1) \
    "s_nop 2\n\t" \
    "v_add_f32 v0, v0, v1\n\t" \
    "v_add_f32 v2, v2, v3\n\t" \
    "v_add_f32 v4, v4, v5\n\t" \
    "v_add_f32 v6, v6, v7\n\t" \
    "v_add_f32 v8, v8, v9\n\t" \
    "v_add_f32 v10, v10, v11\n\t" \
    "v_add_f32 v12, v12, v13\n\t" \
    "v_add_f32 v14, v14, v15\n\t" \
    "v_permlane32_swap_b32 v0, v4\n\t" \
    "v_permlane32_swap_b32 v2, v6\n\t" \
    "v_permlane32_swap_b32 v8, v12\n\t" \
    "v_permlane32_swap_b32 v10, v14\n\t" \
    "v_add_f32 v0, v0, v12\n\t" \
    "v_add_f32 v2, v2, v14\n\t" \
    "ds_write2_b32 %[rw], v0, v2 offset0:0 offset1:32\n\t" \
    "s_waitcnt lgkmcnt(0)\n\t" \
    "s_barrier\n\t" \
    "s_branch E_%=\n\t" \
    "SL_%=:\n\t" \
    "s_sleep 1\n\t" \
    "buffer_load_dwordx4 v[220:223], %[l16], %[rs], %[s0] offen sc1\n\t" \
    "buffer_load_dwordx4 v[224:227], %[l16], %[rs], %[s1] offen sc1\n\t" \
    "buffer_load_dwordx4 v[228:231], %[l16], %[rs], %[s2] offen sc1\n\t" \
    "buffer_load_dwordx4 v[232:235], %[l16], %[rs], %[s3] offen sc1\n\t" \
    "buffer_load_dwordx4 v[236:239], %[l16], %[rs], %[s4] offen sc1\n\t" \
    "buffer_load_dwordx4 v[240:243], %[l16], %[rs], %[s5] offen sc1\n\t" \
    "s_sub_u32 %[lim], %[lim], " STR(DEC2) "\n\t" \
    "s_cmp_gt_i32 %[lim], 0\n\t" \
    "s_cbranch_scc0 G_%=\n\t" \
    "s_nop 3\n\t" \
    "v_mov_b32 v0, 0\n\t" \
    "v_mov_b32 v1, 0\n\t" \
    "v_mov_b32 v2, 0\n\t" \
    "v_mov_b32 v3, 0\n\t" \
    "v_mov_b32 v4, 0\n\t" \
    "v_mov_b32 v5, 0\n\t" \
    "v_mov_b32 v6, 0\n\t" \
    "v_mov_b32 v7, 0\n\t" \
    "v_mov_b32 v8, 0\n\t" \
    "v_mov_b32 v9, 0\n\t" \
    "v_mov_b32 v10, 0\n\t" \
    "v_mov_b32 v11, 0\n\t" \
    "v_mov_b32 v12, 0\n\t" \
    "v_mov_b32 v13, 0\n\t" \
    "v_mov_b32 v14, 0\n\t" \
    "v_mov_b32 v15, 0\n\t" \
    "v_mov_b32 v252, 0\n\t" \
    "s_branch A_%=\n\t" \
    "G_%=:\n\t" \
    "s_waitcnt vmcnt(0)\n\t" \
    "s_branch D_%=\n\t" \
    "E_%=:\n\t" \
    GSW \
    : [lim] "+s"(lim) GSO(1) GSO(4) \
    : [l16] "v"(l16), [rs] "s"(rs), [idx] "v"(idx), [rw] "v"(rw_a), [s0] "s"(so[0]), [s1] "s"(so[1]), [s2] "s"(so[2]), [s3] "s"(so[3]), [s4] "s"(so[4]), [s5] "s"(so[5]), "{v[28:35]}"(wo[0][0][0]), "{v[36:43]}"(wo[0][0][1]), "{v[44:51]}"(wo[0][1][0]), "{v[52:59]}"(wo[0][1][1]), "{v[60:67]}"(wo[1][0][0]), "{v[68:75]}"(wo[1][0][1]), "{v[76:83]}"(wo[1][1][0]), "{v[84:91]}"(wo[1][1][1]), "{v[92:99]}"(wo[2][0][0]), "{v[100:107]}"(wo[2][0][1]), "{v[108:115]}"(wo[2][1][0]), "{v[116:123]}"(wo[2][1][1]), "{v[124:131]}"(wo[3][0][0]), "{v[132:139]}"(wo[3][0][1]), "{v[140:147]}"(wo[3][1][0]), "{v[148:155]}"(wo[3][1][1]), "{v[156:163]}"(wo[4][0][0]), "{v[164:171]}"(wo[4][0][1]), "{v[172:179]}"(wo[4][1][0]), "{v[180:187]}"(wo[4][1][1]), "{v[188:195]}"(wo[5][0][0]), "{v[196:203]}"(wo[5][0][1]), "{v[204:211]}"(wo[5][1][0]), "{v[212:219]}"(wo[5][1][1]) \
    : "memory", "vcc", "scc", "v252", "v0", "v1", "v2", "v3", "v4", "v5", "v6", "v7", "v8", "v9", "v10", "v11", "v12", "v13", "v14", "v15", "v220", "v221", "v222", "v223", "v224", "v225", "v226", "v227", "v228", "v229", "v230", "v231", "v232", "v233", "v234", "v235", "v236", "v237", "v238", "v239", "v240", "v241", "v242", "v243")

#define S_LOOP_K(TE_, FIN_) \
  asm volatile( \
    "s_cmp_eq_u32 %[kh], 0\n\t" \
    "s_cbranch_scc0 L1_%=\n\t" \
    "L0_%=:\n\t" \
    "s_setprio 0\n\t" \
    "s_nop 3\n\t" \
    "ds_write_b128 %[pa], v[196:199] offset:0\n\t" \
    "ds_write_b128 %[pa], v[200:203] offset:1024\n\t" \
    "ds_write_b128 %[pa], v[204:207] offset:2048\n\t" \
    "ds_write_b128 %[pa], v[208:211] offset:3072\n\t" \
    "s_sub_u32 %[a], %[t], 1\n\t" \
    "v_mov_b32 v53, %[tag]\n\t" \
    "v_mov_b32 v52, %[a]\n\t" \
    "s_sub_u32 %[b], %[t], 2\n\t" \
    "s_cmp_lt_u32 %[b], " STR(SQUICK) "\n\t" \
    "s_cselect_b64 exec, %[fm], 0\n\t" \
    "buffer_store_dwordx2 v[52:53], %[l16], %[rs], %[fo] offen sc1\n\t" \
    "s_mov_b64 exec, -1\n\t" \
    "s_add_u32 m0, %[sd], 49152\n\t" \
    "s_add_u32 %[a], %[sx], 81920\n\t" \
    "s_waitcnt lgkmcnt(0)\n\t" \
    "buffer_load_dwordx4 %[l16], %[rs], %[a] offen offset:0 sc1 lds\n\t" \
    "buffer_load_dwordx4 %[l16], %[rs], %[a] offen offset:1024 sc1 lds\n\t" \
    "buffer_load_dwordx4 %[l16], %[rs], %[a] offen offset:2048 sc1 lds\n\t" \
    "buffer_load_dwordx4 %[l16], %[rs], %[a] offen offset:3072 sc1 lds\n\t" \
    "v_mfma_f32_16x16x32_bf16 v[0:3], v[68:71], v[20:23], 0\n\t" \
    "ds_read_b128 v[44:47], %[xp] offset:6144\n\t" \
    "v_mfma_f32_16x16x32_bf16 v[4:7], v[76:79], v[20:23], 0\n\t" \
    "ds_read_b128 v[48:51], %[xp] offset:7168\n\t" \
    "v_mfma_f32_16x16x32_bf16 v[8:11], v[68:71], v[24:27], 0\n\t" \
    "v_mfma_f32_16x16x32_bf16 v[12:15], v[76:79], v[24:27], 0\n\t" \
    "v_mfma_f32_16x16x32_bf16 v[0:3], v[72:75], v[28:31], v[0:3]\n\t" \
    "ds_read_b128 v[20:23], %[xp] offset:8192\n\t" \
    "v_mfma_f32_16x16x32_bf16 v[4:7], v[80:83], v[28:31], v[4:7]\n\t" \
    "ds_read_b128 v[24:27], %[xp] offset:9216\n\t" \
    "v_mfma_f32_16x16x32_bf16 v[8:11], v[72:75], v[32:35], v[8:11]\n\t" \
    "v_mfma_f32_16x16x32_bf16 v[12:15], v[80:83], v[32:35], v[12:15]\n\t" \
    "v_mfma_f32_16x16x32_bf16 v[0:3], v[84:87], v[36:39], v[0:3]\n\t" \
    "ds_read_b128 v[28:31], %[xp] offset:10240\n\t" \
    "v_mfma_f32_16x16x32_bf16 v[4:7], v[92:95], v[36:39], v[4:7]\n\t" \
    "ds_read_b128 v[32:35], %[xp] offset:11264\n\t" \
    "v_mfma_f32_16x16x32_bf16 v[8:11], v[84:87], v[40:43], v[8:11]\n\t" \
    "s_waitcnt lgkmcnt(4)\n\t" \
    "v_mfma_f32_16x16x32_bf16 v[12:15], v[92:95], v[40:43], v[12:15]\n\t" \
    "v_mfma_f32_16x16x32_bf16 v[0:3], v[88:91], v[44:47], v[0:3]\n\t" \
    "ds_read_b128 v[36:39], %[xp] offset:12288\n\t" \
    "v_mfma_f32_16x16x32_bf16 v[4:7], v[96:99], v[44:47], v[4:7]\n\t" \
    "ds_read_b128 v[40:43], %[xp] offset:13312\n\t" \
    "v_mfma_f32_16x16x32_bf16 v[8:11], v[88:91], v[48:51], v[8:11]\n\t" \
    "s_waitcnt lgkmcnt(4)\n\t" \
    "v_mfma_f32_16x16x32_bf16 v[12:15], v[96:99], v[48:51], v[12:15]\n\t" \
    "v_mfma_f32_16x16x32_bf16 v[0:3], v[100:103], v[20:23], v[0:3]\n\t" \
    "ds_read_b128 v[44:47], %[xp] offset:14336\n\t" \
    "v_mfma_f32_16x16x32_bf16 v[4:7], v[108:111], v[20:23], v[4:7]\n\t" \
    "ds_read_b128 v[48:51], %[xp] offset:15360\n\t" \
    "v_mfma_f32_16x16x32_bf16 v[8:11], v[100:103], v[24:27], v[8:11]\n\t" \
    "s_waitcnt lgkmcnt(4)\n\t" \
    "v_mfma_f32_16x16x32_bf16 v[12:15], v[108:111], v[24:27], v[12:15]\n\t" \
    "v_mfma_f32_16x16x32_bf16 v[0:3], v[104:107], v[28:31], v[0:3]\n\t" \
    "ds_read_b128 v[20:23], %[xp] offset:16384\n\t" \
    "v_mfma_f32_16x16x32_bf16 v[4:7], v[112:115], v[28:31], v[4:7]\n\t" \
    "ds_read_b128 v[24:27], %[xp] offset:17408\n\t" \
    "v_mfma_f32_16x16x32_bf16 v[8:11], v[104:107], v[32:35], v[8:11]\n\t" \
    "s_waitcnt lgkmcnt(4)\n\t" \
    "v_mfma_f32_16x16x32_bf16 v[12:15], v[112:115], v[32:35], v[12:15]\n\t" \
    "v_mfma_f32_16x16x32_bf16 v[0:3], v[116:119], v[36:39], v[0:3]\n\t" \
    "ds_read_b128 v[28:31], %[xp] offset:18432\n\t" \
    "v_mfma_f32_16x16x32_bf16 v[4:7], v[124:127], v[36:39], v[4:7]\n\t" \
    "ds_read_b128 v[32:35], %[xp] offset:19456\n\t" \
    "v_mfma_f32_16x16x32_bf16 v[8:11], v[116:119], v[40:43], v[8:11]\n\t" \
    "s_waitcnt lgkmcnt(4)\n\t" \
    "v_mfma_f32_16x16x32_bf16 v[12:15], v[124:127], v[40:43], v[12:15]\n\t" \
    "v_mfma_f32_16x16x32_bf16 v[0:3], v[120:123], v[44:47], v[0:3]\n\t" \
    "ds_read_b128 v[36:39], %[xp] offset:20480\n\t" \
    "v_mfma_f32_16x16x32_bf16 v[4:7], v[128:131], v[44:47], v[4:7]\n\t" \
    "ds_read_b128 v[40:43], %[xp] offset:21504\n\t" \
    "v_mfma_f32_16x16x32_bf16 v[8:11], v[120:123], v[48:51], v[8:11]\n\t" \
    "s_waitcnt lgkmcnt(4)\n\t" \
    "v_mfma_f32_16x16x32_bf16 v[12:15], v[128:131], v[48:51], v[12:15]\n\t" \
    "s_waitcnt vmcnt(4)\n\t" \
    "s_barrier\n\t" \
    "s_setprio 0\n\t" \
    "s_sub_u32 %[a], %[t], 1\n\t" \
    "v_mov_b32 v53, %[tag]\n\t" \
    "v_mov_b32 v52, %[a]\n\t" \
    "s_cmp_gt_u32 %[t], 1\n\t" \
    "s_cselect_b64 exec, %[fm], 0\n\t" \
    "buffer_store_dwordx2 v[52:53], %[l16], %[rs], %[fo] offen sc1\n\t" \
    "s_mov_b64 exec, %[pm]\n\t" \
    "s_mov_b32 m0, %[pd]\n\t" \
    "s_add_u32 %[a], %[sx], %[po]\n\t" \
    "buffer_load_dwordx4 %[l16], %[rs], %[a] offen sc1 lds\n\t" \
    "s_mov_b64 exec, -1\n\t" \
    "s_add_u32 m0, %[sd], 0\n\t" \
    "s_add_u32 %[a], %[sx], 131072\n\t" \
    "buffer_load_dwordx4 %[l16], %[rs], %[a] offen offset:0 sc1 lds\n\t" \
    "buffer_load_dwordx4 %[l16], %[rs], %[a] offen offset:1024 sc1 lds\n\t" \
    "buffer_load_dwordx4 %[l16], %[rs], %[a] offen offset:2048 sc1 lds\n\t" \
    "buffer_load_dwordx4 %[l16], %[rs], %[a] offen offset:3072 sc1 lds\n\t" \
    "v_mfma_f32_16x16x32_bf16 v[0:3], v[132:135], v[20:23], v[0:3]\n\t" \
    "ds_read_b128 v[44:47], %[xp] offset:22528\n\t" \
    "v_mfma_f32_16x16x32_bf16 v[4:7], v[140:143], v[20:23], v[4:7]\n\t" \
    "ds_read_b128 v[48:51], %[xp] offset:23552\n\t" \
    "v_mfma_f32_16x16x32_bf16 v[8:11], v[132:135], v[24:27], v[8:11]\n\t" \
    "s_waitcnt lgkmcnt(4)\n\t" \
    "v_mfma_f32_16x16x32_bf16 v[12:15], v[140:143], v[24:27], v[12:15]\n\t" \
    "v_mfma_f32_16x16x32_bf16 v[0:3], v[136:139], v[28:31], v[0:3]\n\t" \
    "ds_read_b128 v[20:23], %[xp] offset:24576\n\t" \
    "v_mfma_f32_16x16x32_bf16 v[4:7], v[144:147], v[28:31], v[4:7]\n\t" \
    "ds_read_b128 v[24:27], %[xp] offset:25600\n\t" \
    "v_mfma_f32_16x16x32_bf16 v[8:11], v[136:139], v[32:35], v[8:11]\n\t" \
    "s_waitcnt lgkmcnt(4)\n\t" \
    "v_mfma_f32_16x16x32_bf16 v[12:15], v[144:147], v[32:35], v[12:15]\n\t" \
    "v_mfma_f32_16x16x32_bf16 v[0:3], v[148:151], v[36:39], v[0:3]\n\t" \
    "ds_read_b128 v[28:31], %[xp] offset:26624\n\t" \
    "v_mfma_f32_16x16x32_bf16 v[4:7], v[156:159], v[36:39], v[4:7]\n\t" \
    "ds_read_b128 v[32:35], %[xp] offset:27648\n\t" \
    "v_mfma_f32_16x16x32_bf16 v[8:11], v[148:151], v[40:43], v[8:11]\n\t" \
    "s_waitcnt lgkmcnt(4)\n\t" \
    "v_mfma_f32_16x16x32_bf16 v[12:15], v[156:159], v[40:43], v[12:15]\n\t" \
    "v_mfma_f32_16x16x32_bf16 v[0:3], v[152:155], v[44:47], v[0:3]\n\t" \
    "ds_read_b128 v[36:39], %[xp] offset:28672\n\t" \
    "v_mfma_f32_16x16x32_bf16 v[4:7], v[160:163], v[44:47], v[4:7]\n\t" \
    "ds_read_b128 v[40:43], %[xp] offset:29696\n\t" \
    "v_mfma_f32_16x16x32_bf16 v[8:11], v[152:155], v[48:51], v[8:11]\n\t" \
    "s_waitcnt lgkmcnt(4)\n\t" \
    "v_mfma_f32_16x16x32_bf16 v[12:15], v[160:163], v[48:51], v[12:15]\n\t" \
    "v_mfma_f32_16x16x32_bf16 v[0:3], v[164:167], v[20:23], v[0:3]\n\t" \
    "ds_read_b128 v[44:47], %[xp] offset:30720\n\t" \
    "v_mfma_f32_16x16x32_bf16 v[4:7], v[172:175], v[20:23], v[4:7]\n\t" \
    "ds_read_b128 v[48:51], %[xp] offset:31744\n\t" \
    "v_mfma_f32_16x16x32_bf16 v[8:11], v[164:167], v[24:27], v[8:11]\n\t" \
    "s_waitcnt lgkmcnt(4)\n\t" \
    "v_mfma_f32_16x16x32_bf16 v[12:15], v[172:175], v[24:27], v[12:15]\n\t" \
    "v_mfma_f32_16x16x32_bf16 v[0:3], v[168:171], v[28:31], v[0:3]\n\t" \
    "ds_read_b128 v[20:23], %[xp] offset:32768\n\t" \
    "v_mfma_f32_16x16x32_bf16 v[4:7], v[176:179], v[28:31], v[4:7]\n\t" \
    "ds_read_b128 v[24:27], %[xp] offset:33792\n\t" \
    "v_mfma_f32_16x16x32_bf16 v[8:11], v[168:171], v[32:35], v[8:11]\n\t" \
    "s_waitcnt lgkmcnt(4)\n\t" \
    "v_mfma_f32_16x16x32_bf16 v[12:15], v[176:179], v[32:35], v[12:15]\n\t" \
    "v_mfma_f32_16x16x32_bf16 v[0:3], v[180:183], v[36:39], v[0:3]\n\t" \
    "ds_read_b128 v[28:31], %[xp] offset:34816\n\t" \
    "v_mfma_f32_16x16x32_bf16 v[4:7], v[188:191], v[36:39], v[4:7]\n\t" \
    "ds_read_b128 v[32:35], %[xp] offset:35840\n\t" \
    "v_mfma_f32_16x16x32_bf16 v[8:11], v[180:183], v[40:43], v[8:11]\n\t" \
    "s_waitcnt lgkmcnt(4)\n\t" \
    "v_mfma_f32_16x16x32_bf16 v[12:15], v[188:191], v[40:43], v[12:15]\n\t" \
    "v_mfma_f32_16x16x32_bf16 v[0:3], v[184:187], v[44:47], v[0:3]\n\t" \
    "ds_read_b128 v[36:39], %[xp] offset:36864\n\t" \
    "v_mfma_f32_16x16x32_bf16 v[4:7], v[192:195], v[44:47], v[4:7]\n\t" \
    "ds_read_b128 v[40:43], %[xp] offset:37888\n\t" \
    "v_mfma_f32_16x16x32_bf16 v[8:11], v[184:187], v[48:51], v[8:11]\n\t" \
    "s_waitcnt lgkmcnt(4)\n\t" \
    "v_mfma_f32_16x16x32_bf16 v[12:15], v[192:195], v[48:51], v[12:15]\n\t" \
    "s_add_u32 %[sx], %[sx], 65536\n\t" \
    "s_add_u32 %[t], %[t], 1\n\t" \
    "s_waitcnt vmcnt(4)\n\t" \
    "s_barrier\n\t" \
    "s_setprio 0\n\t" \
    "s_nop 3\n\t" \
    "ds_write_b128 %[pa], v[0:3] offset:0\n\t" \
    "ds_write_b128 %[pa], v[4:7] offset:1024\n\t" \
    "ds_write_b128 %[pa], v[8:11] offset:2048\n\t" \
    "ds_write_b128 %[pa], v[12:15] offset:3072\n\t" \
    "s_sub_u32 %[a], %[t], 1\n\t" \
    "v_mov_b32 v53, %[tag]\n\t" \
    "v_mov_b32 v52, %[a]\n\t" \
    "s_sub_u32 %[b], %[t], 2\n\t" \
    "s_cmp_lt_u32 %[b], " STR(SQUICK) "\n\t" \
    "s_cselect_b64 exec, %[fm], 0\n\t" \
    "buffer_store_dwordx2 v[52:53], %[l16], %[rs], %[fo] offen sc1\n\t" \
    "s_mov_b64 exec, -1\n\t" \
    "s_add_u32 m0, %[sd], 16384\n\t" \
    "s_add_u32 %[a], %[sx], 81920\n\t" \
    "s_waitcnt lgkmcnt(0)\n\t" \
    "buffer_load_dwordx4 %[l16], %[rs], %[a] offen offset:0 sc1 lds\n\t" \
    "buffer_load_dwordx4 %[l16], %[rs], %[a] offen offset:1024 sc1 lds\n\t" \
    "buffer_load_dwordx4 %[l16], %[rs], %[a] offen offset:2048 sc1 lds\n\t" \
    "buffer_load_dwordx4 %[l16], %[rs], %[a] offen offset:3072 sc1 lds\n\t" \
    "v_mfma_f32_16x16x32_bf16 v[196:199], v[68:71], v[20:23], 0\n\t" \
    "ds_read_b128 v[44:47], %[xp] offset:38912\n\t" \
    "v_mfma_f32_16x16x32_bf16 v[200:203], v[76:79], v[20:23], 0\n\t" \
    "ds_read_b128 v[48:51], %[xp] offset:39936\n\t" \
    "v_mfma_f32_16x16x32_bf16 v[204:207], v[68:71], v[24:27], 0\n\t" \
    "v_mfma_f32_16x16x32_bf16 v[208:211], v[76:79], v[24:27], 0\n\t" \
    "v_mfma_f32_16x16x32_bf16 v[196:199], v[72:75], v[28:31], v[196:199]\n\t" \
    "ds_read_b128 v[20:23], %[xp] offset:40960\n\t" \
    "v_mfma_f32_16x16x32_bf16 v[200:203], v[80:83], v[28:31], v[200:203]\n\t" \
    "ds_read_b128 v[24:27], %[xp] offset:41984\n\t" \
    "v_mfma_f32_16x16x32_bf16 v[204:207], v[72:75], v[32:35], v[204:207]\n\t" \
    "v_mfma_f32_16x16x32_bf16 v[208:211], v[80:83], v[32:35], v[208:211]\n\t" \
    "v_mfma_f32_16x16x32_bf16 v[196:199], v[84:87], v[36:39], v[196:199]\n\t" \
    "ds_read_b128 v[28:31], %[xp] offset:43008\n\t" \
    "v_mfma_f32_16x16x32_bf16 v[200:203], v[92:95], v[36:39], v[200:203]\n\t" \
    "ds_read_b128 v[32:35], %[xp] offset:44032\n\t" \
    "v_mfma_f32_16x16x32_bf16 v[204:207], v[84:87], v[40:43], v[204:207]\n\t" \
    "s_waitcnt lgkmcnt(4)\n\t" \
    "v_mfma_f32_16x16x32_bf16 v[208:211], v[92:95], v[40:43], v[208:211]\n\t" \
    "v_mfma_f32_16x16x32_bf16 v[196:199], v[88:91], v[44:47], v[196:199]\n\t" \
    "ds_read_b128 v[36:39], %[xp] offset:45056\n\t" \
    "v_mfma_f32_16x16x32_bf16 v[200:203], v[96:99], v[44:47], v[200:203]\n\t" \
    "ds_read_b128 v[40:43], %[xp] offset:46080\n\t" \
    "v_mfma_f32_16x16x32_bf16 v[204:207], v[88:91], v[48:51], v[204:207]\n\t" \
    "s_waitcnt lgkmcnt(4)\n\t" \
    "v_mfma_f32_16x16x32_bf16 v[208:211], v[96:99], v[48:51], v[208:211]\n\t" \
    "v_mfma_f32_16x16x32_bf16 v[196:199], v[100:103], v[20:23], v[196:199]\n\t" \
    "ds_read_b128 v[44:47], %[xp] offset:47104\n\t" \
    "v_mfma_f32_16x16x32_bf16 v[200:203], v[108:111], v[20:23], v[200:203]\n\t" \
    "ds_read_b128 v[48:51], %[xp] offset:48128\n\t" \
    "v_mfma_f32_16x16x32_bf16 v[204:207], v[100:103], v[24:27], v[204:207]\n\t" \
    "s_waitcnt lgkmcnt(4)\n\t" \
    "v_mfma_f32_16x16x32_bf16 v[208:211], v[108:111], v[24:27], v[208:211]\n\t" \
    "v_mfma_f32_16x16x32_bf16 v[196:199], v[104:107], v[28:31], v[196:199]\n\t" \
    "ds_read_b128 v[20:23], %[xp] offset:49152\n\t" \
    "v_mfma_f32_16x16x32_bf16 v[200:203], v[112:115], v[28:31], v[200:203]\n\t" \
    "ds_read_b128 v[24:27], %[xp] offset:50176\n\t" \
    "v_mfma_f32_16x16x32_bf16 v[204:207], v[104:107], v[32:35], v[204:207]\n\t" \
    "s_waitcnt lgkmcnt(4)\n\t" \
    "v_mfma_f32_16x16x32_bf16 v[208:211], v[112:115], v[32:35], v[208:211]\n\t" \
    "v_mfma_f32_16x16x32_bf16 v[196:199], v[116:119], v[36:39], v[196:199]\n\t" \
    "ds_read_b128 v[28:31], %[xp] offset:51200\n\t" \
    "v_mfma_f32_16x16x32_bf16 v[200:203], v[124:127], v[36:39], v[200:203]\n\t" \
    "ds_read_b128 v[32:35], %[xp] offset:52224\n\t" \
    "v_mfma_f32_16x16x32_bf16 v[204:207], v[116:119], v[40:43], v[204:207]\n\t" \
    "s_waitcnt lgkmcnt(4)\n\t" \
    "v_mfma_f32_16x16x32_bf16 v[208:211], v[124:127], v[40:43], v[208:211]\n\t" \
    "v_mfma_f32_16x16x32_bf16 v[196:199], v[120:123], v[44:47], v[196:199]\n\t" \
    "ds_read_b128 v[36:39], %[xp] offset:53248\n\t" \
    "v_mfma_f32_16x16x32_bf16 v[200:203], v[128:131], v[44:47], v[200:203]\n\t" \
    "ds_read_b128 v[40:43], %[xp] offset:54272\n\t" \
    "v_mfma_f32_16x16x32_bf16 v[204:207], v[120:123], v[48:51], v[204:207]\n\t" \
    "s_waitcnt lgkmcnt(4)\n\t" \
    "v_mfma_f32_16x16x32_bf16 v[208:211], v[128:131], v[48:51], v[208:211]\n\t" \
    "s_waitcnt vmcnt(4)\n\t" \
    "s_barrier\n\t" \
    "s_setprio 0\n\t" \
    "s_sub_u32 %[a], %[t], 1\n\t" \
    "v_mov_b32 v53, %[tag]\n\t" \
    "v_mov_b32 v52, %[a]\n\t" \
    "s_cmp_gt_u32 %[t], 1\n\t" \
    "s_cselect_b64 exec, %[fm], 0\n\t" \
    "buffer_store_dwordx2 v[52:53], %[l16], %[rs], %[fo] offen sc1\n\t" \
    "s_mov_b64 exec, %[pm]\n\t" \
    "s_mov_b32 m0, %[pd]\n\t" \
    "s_add_u32 %[a], %[sx], %[po]\n\t" \
    "buffer_load_dwordx4 %[l16], %[rs], %[a] offen sc1 lds\n\t" \
    "s_mov_b64 exec, -1\n\t" \
    "s_add_u32 m0, %[sd], 32768\n\t" \
    "s_add_u32 %[a], %[sx], 131072\n\t" \
    "buffer_load_dwordx4 %[l16], %[rs], %[a] offen offset:0 sc1 lds\n\t" \
    "buffer_load_dwordx4 %[l16], %[rs], %[a] offen offset:1024 sc1 lds\n\t" \
    "buffer_load_dwordx4 %[l16], %[rs], %[a] offen offset:2048 sc1 lds\n\t" \
    "buffer_load_dwordx4 %[l16], %[rs], %[a] offen offset:3072 sc1 lds\n\t" \
    "v_mfma_f32_16x16x32_bf16 v[196:199], v[132:135], v[20:23], v[196:199]\n\t" \
    "ds_read_b128 v[44:47], %[xp] offset:55296\n\t" \
    "v_mfma_f32_16x16x32_bf16 v[200:203], v[140:143], v[20:23], v[200:203]\n\t" \
    "ds_read_b128 v[48:51], %[xp] offset:56320\n\t" \
    "v_mfma_f32_16x16x32_bf16 v[204:207], v[132:135], v[24:27], v[204:207]\n\t" \
    "s_waitcnt lgkmcnt(4)\n\t" \
    "v_mfma_f32_16x16x32_bf16 v[208:211], v[140:143], v[24:27], v[208:211]\n\t" \
    "v_mfma_f32_16x16x32_bf16 v[196:199], v[136:139], v[28:31], v[196:199]\n\t" \
    "ds_read_b128 v[20:23], %[xp] offset:57344\n\t" \
    "v_mfma_f32_16x16x32_bf16 v[200:203], v[144:147], v[28:31], v[200:203]\n\t" \
    "ds_read_b128 v[24:27], %[xp] offset:58368\n\t" \
    "v_mfma_f32_16x16x32_bf16 v[204:207], v[136:139], v[32:35], v[204:207]\n\t" \
    "s_waitcnt lgkmcnt(4)\n\t" \
    "v_mfma_f32_16x16x32_bf16 v[208:211], v[144:147], v[32:35], v[208:211]\n\t" \
    "v_mfma_f32_16x16x32_bf16 v[196:199], v[148:151], v[36:39], v[196:199]\n\t" \
    "ds_read_b128 v[28:31], %[xp] offset:59392\n\t" \
    "v_mfma_f32_16x16x32_bf16 v[200:203], v[156:159], v[36:39], v[200:203]\n\t" \
    "ds_read_b128 v[32:35], %[xp] offset:60416\n\t" \
    "v_mfma_f32_16x16x32_bf16 v[204:207], v[148:151], v[40:43], v[204:207]\n\t" \
    "s_waitcnt lgkmcnt(4)\n\t" \
    "v_mfma_f32_16x16x32_bf16 v[208:211], v[156:159], v[40:43], v[208:211]\n\t" \
    "v_mfma_f32_16x16x32_bf16 v[196:199], v[152:155], v[44:47], v[196:199]\n\t" \
    "ds_read_b128 v[36:39], %[xp] offset:61440\n\t" \
    "v_mfma_f32_16x16x32_bf16 v[200:203], v[160:163], v[44:47], v[200:203]\n\t" \
    "ds_read_b128 v[40:43], %[xp] offset:62464\n\t" \
    "v_mfma_f32_16x16x32_bf16 v[204:207], v[152:155], v[48:51], v[204:207]\n\t" \
    "s_waitcnt lgkmcnt(4)\n\t" \
    "v_mfma_f32_16x16x32_bf16 v[208:211], v[160:163], v[48:51], v[208:211]\n\t" \
    "v_mfma_f32_16x16x32_bf16 v[196:199], v[164:167], v[20:23], v[196:199]\n\t" \
    "ds_read_b128 v[44:47], %[xp] offset:63488\n\t" \
    "v_mfma_f32_16x16x32_bf16 v[200:203], v[172:175], v[20:23], v[200:203]\n\t" \
    "ds_read_b128 v[48:51], %[xp] offset:64512\n\t" \
    "v_mfma_f32_16x16x32_bf16 v[204:207], v[164:167], v[24:27], v[204:207]\n\t" \
    "s_waitcnt lgkmcnt(4)\n\t" \
    "v_mfma_f32_16x16x32_bf16 v[208:211], v[172:175], v[24:27], v[208:211]\n\t" \
    "v_mfma_f32_16x16x32_bf16 v[196:199], v[168:171], v[28:31], v[196:199]\n\t" \
    "ds_read_b128 v[20:23], %[xp] offset:0\n\t" \
    "v_mfma_f32_16x16x32_bf16 v[200:203], v[176:179], v[28:31], v[200:203]\n\t" \
    "ds_read_b128 v[24:27], %[xp] offset:1024\n\t" \
    "v_mfma_f32_16x16x32_bf16 v[204:207], v[168:171], v[32:35], v[204:207]\n\t" \
    "s_waitcnt lgkmcnt(4)\n\t" \
    "v_mfma_f32_16x16x32_bf16 v[208:211], v[176:179], v[32:35], v[208:211]\n\t" \
    "v_mfma_f32_16x16x32_bf16 v[196:199], v[180:183], v[36:39], v[196:199]\n\t" \
    "ds_read_b128 v[28:31], %[xp] offset:2048\n\t" \
    "v_mfma_f32_16x16x32_bf16 v[200:203], v[188:191], v[36:39], v[200:203]\n\t" \
    "ds_read_b128 v[32:35], %[xp] offset:3072\n\t" \
    "v_mfma_f32_16x16x32_bf16 v[204:207], v[180:183], v[40:43], v[204:207]\n\t" \
    "s_waitcnt lgkmcnt(4)\n\t" \
    "v_mfma_f32_16x16x32_bf16 v[208:211], v[188:191], v[40:43], v[208:211]\n\t" \
    "v_mfma_f32_16x16x32_bf16 v[196:199], v[184:187], v[44:47], v[196:199]\n\t" \
    "ds_read_b128 v[36:39], %[xp] offset:4096\n\t" \
    "v_mfma_f32_16x16x32_bf16 v[200:203], v[192:195], v[44:47], v[200:203]\n\t" \
    "ds_read_b128 v[40:43], %[xp] offset:5120\n\t" \
    "v_mfma_f32_16x16x32_bf16 v[204:207], v[184:187], v[48:51], v[204:207]\n\t" \
    "s_waitcnt lgkmcnt(4)\n\t" \
    "v_mfma_f32_16x16x32_bf16 v[208:211], v[192:195], v[48:51], v[208:211]\n\t" \
    "s_add_u32 %[sx], %[sx], 65536\n\t" \
    "s_add_u32 %[t], %[t], 1\n\t" \
    "s_waitcnt vmcnt(4)\n\t" \
    "s_barrier\n\t" \
    "s_cmp_lt_u32 %[t], %[te]\n\t" \
    "s_cbranch_scc1 L0_%=\n\t" \
    "s_cmp_eq_u32 %[fin], %[te]\n\t" \
    "s_cbranch_scc0 E_%=\n\t" \
    "s_nop 4\n\t" \
    "ds_write_b128 %[pa], v[196:199] offset:0\n\t" \
    "ds_write_b128 %[pa], v[200:203] offset:1024\n\t" \
    "ds_write_b128 %[pa], v[204:207] offset:2048\n\t" \
    "ds_write_b128 %[pa], v[208:211] offset:3072\n\t" \
    "s_waitcnt lgkmcnt(0)\n\t" \
    "s_barrier\n\t" \
    "s_branch E_%=\n\t" \
    "L1_%=:\n\t" \
    "s_setprio 1\n\t" \
    "s_add_u32 m0, %[sd], 49152\n\t" \
    "s_add_u32 %[a], %[sx], 81920\n\t" \
    "v_mfma_f32_16x16x32_bf16 v[0:3], v[68:71], v[20:23], 0\n\t" \
    "ds_read_b128 v[44:47], %[xp] offset:6144\n\t" \
    "v_mfma_f32_16x16x32_bf16 v[4:7], v[76:79], v[20:23], 0\n\t" \
    "ds_read_b128 v[48:51], %[xp] offset:7168\n\t" \
    "v_mfma_f32_16x16x32_bf16 v[8:11], v[68:71], v[24:27], 0\n\t" \
    "s_waitcnt lgkmcnt(4)\n\t" \
    "v_mfma_f32_16x16x32_bf16 v[12:15], v[76:79], v[24:27], 0\n\t" \
    "v_mfma_f32_16x16x32_bf16 v[0:3], v[72:75], v[28:31], v[0:3]\n\t" \
    "ds_read_b128 v[20:23], %[xp] offset:8192\n\t" \
    "v_mfma_f32_16x16x32_bf16 v[4:7], v[80:83], v[28:31], v[4:7]\n\t" \
    "ds_read_b128 v[24:27], %[xp] offset:9216\n\t" \
    "v_mfma_f32_16x16x32_bf16 v[8:11], v[72:75], v[32:35], v[8:11]\n\t" \
    "s_waitcnt lgkmcnt(4)\n\t" \
    "v_mfma_f32_16x16x32_bf16 v[12:15], v[80:83], v[32:35], v[12:15]\n\t" \
    "v_mfma_f32_16x16x32_bf16 v[0:3], v[84:87], v[36:39], v[0:3]\n\t" \
    "ds_read_b128 v[28:31], %[xp] offset:10240\n\t" \
    "v_mfma_f32_16x16x32_bf16 v[4:7], v[92:95], v[36:39], v[4:7]\n\t" \
    "ds_read_b128 v[32:35], %[xp] offset:11264\n\t" \
    "v_mfma_f32_16x16x32_bf16 v[8:11], v[84:87], v[40:43], v[8:11]\n\t" \
    "s_waitcnt lgkmcnt(4)\n\t" \
    "v_mfma_f32_16x16x32_bf16 v[12:15], v[92:95], v[40:43], v[12:15]\n\t" \
    "v_mfma_f32_16x16x32_bf16 v[0:3], v[88:91], v[44:47], v[0:3]\n\t" \
    "ds_read_b128 v[36:39], %[xp] offset:12288\n\t" \
    "v_mfma_f32_16x16x32_bf16 v[4:7], v[96:99], v[44:47], v[4:7]\n\t" \
    "ds_read_b128 v[40:43], %[xp] offset:13312\n\t" \
    "v_mfma_f32_16x16x32_bf16 v[8:11], v[88:91], v[48:51], v[8:11]\n\t" \
    "s_waitcnt lgkmcnt(4)\n\t" \
    "v_mfma_f32_16x16x32_bf16 v[12:15], v[96:99], v[48:51], v[12:15]\n\t" \
    "v_mfma_f32_16x16x32_bf16 v[0:3], v[100:103], v[20:23], v[0:3]\n\t" \
    "ds_read_b128 v[44:47], %[xp] offset:14336\n\t" \
    "v_mfma_f32_16x16x32_bf16 v[4:7], v[108:111], v[20:23], v[4:7]\n\t" \
    "ds_read_b128 v[48:51], %[xp] offset:15360\n\t" \
    "v_mfma_f32_16x16x32_bf16 v[8:11], v[100:103], v[24:27], v[8:11]\n\t" \
    "s_waitcnt lgkmcnt(4)\n\t" \
    "v_mfma_f32_16x16x32_bf16 v[12:15], v[108:111], v[24:27], v[12:15]\n\t" \
    "buffer_load_dwordx4 %[l16], %[rs], %[a] offen offset:0 sc1 lds\n\t" \
    "v_mfma_f32_16x16x32_bf16 v[0:3], v[104:107], v[28:31], v[0:3]\n\t" \
    "ds_read_b128 v[20:23], %[xp] offset:16384\n\t" \
    "v_mfma_f32_16x16x32_bf16 v[4:7], v[112:115], v[28:31], v[4:7]\n\t" \
    "ds_read_b128 v[24:27], %[xp] offset:17408\n\t" \
    "v_mfma_f32_16x16x32_bf16 v[8:11], v[104:107], v[32:35], v[8:11]\n\t" \
    "s_waitcnt lgkmcnt(4)\n\t" \
    "v_mfma_f32_16x16x32_bf16 v[12:15], v[112:115], v[32:35], v[12:15]\n\t" \
    "buffer_load_dwordx4 %[l16], %[rs], %[a] offen offset:1024 sc1 lds\n\t" \
    "v_mfma_f32_16x16x32_bf16 v[0:3], v[116:119], v[36:39], v[0:3]\n\t" \
    "ds_read_b128 v[28:31], %[xp] offset:18432\n\t" \
    "v_mfma_f32_16x16x32_bf16 v[4:7], v[124:127], v[36:39], v[4:7]\n\t" \
    "ds_read_b128 v[32:35], %[xp] offset:19456\n\t" \
    "v_mfma_f32_16x16x32_bf16 v[8:11], v[116:119], v[40:43], v[8:11]\n\t" \
    "s_waitcnt lgkmcnt(4)\n\t" \
    "v_mfma_f32_16x16x32_bf16 v[12:15], v[124:127], v[40:43], v[12:15]\n\t" \
    "buffer_load_dwordx4 %[l16], %[rs], %[a] offen offset:2048 sc1 lds\n\t" \
    "v_mfma_f32_16x16x32_bf16 v[0:3], v[120:123], v[44:47], v[0:3]\n\t" \
    "ds_read_b128 v[36:39], %[xp] offset:20480\n\t" \
    "v_mfma_f32_16x16x32_bf16 v[4:7], v[128:131], v[44:47], v[4:7]\n\t" \
    "ds_read_b128 v[40:43], %[xp] offset:21504\n\t" \
    "v_mfma_f32_16x16x32_bf16 v[8:11], v[120:123], v[48:51], v[8:11]\n\t" \
    "s_waitcnt lgkmcnt(4)\n\t" \
    "v_mfma_f32_16x16x32_bf16 v[12:15], v[128:131], v[48:51], v[12:15]\n\t" \
    "buffer_load_dwordx4 %[l16], %[rs], %[a] offen offset:3072 sc1 lds\n\t" \
    "s_waitcnt vmcnt(4)\n\t" \
    "s_barrier\n\t" \
    "s_setprio 1\n\t" \
    "s_add_u32 m0, %[sd], 0\n\t" \
    "s_add_u32 %[a], %[sx], 131072\n\t" \
    "s_add_u32 %[b], %[oo], 0x1000\n\t" \
    "s_cmp_lg_u32 %[t], 1\n\t" \
    "s_cselect_b64 %[em], -1, 0\n\t" \
    "v_mfma_f32_16x16x32_bf16 v[0:3], v[132:135], v[20:23], v[0:3]\n\t" \
    "ds_read_b128 v[44:47], %[xp] offset:22528\n\t" \
    "v_mfma_f32_16x16x32_bf16 v[4:7], v[140:143], v[20:23], v[4:7]\n\t" \
    "ds_read_b128 v[48:51], %[xp] offset:23552\n\t" \
    "v_mfma_f32_16x16x32_bf16 v[8:11], v[132:135], v[24:27], v[8:11]\n\t" \
    "ds_read_b128 v[52:55], %[pa] offset:0\n\t" \
    "s_waitcnt lgkmcnt(5)\n\t" \
    "v_mfma_f32_16x16x32_bf16 v[12:15], v[140:143], v[24:27], v[12:15]\n\t" \
    "buffer_load_dwordx4 %[l16], %[rs], %[a] offen offset:0 sc1 lds\n\t" \
    "v_mfma_f32_16x16x32_bf16 v[0:3], v[136:139], v[28:31], v[0:3]\n\t" \
    "ds_read_b128 v[20:23], %[xp] offset:24576\n\t" \
    "v_mfma_f32_16x16x32_bf16 v[4:7], v[144:147], v[28:31], v[4:7]\n\t" \
    "ds_read_b128 v[24:27], %[xp] offset:25600\n\t" \
    "v_mfma_f32_16x16x32_bf16 v[8:11], v[136:139], v[32:35], v[8:11]\n\t" \
    "ds_read_b128 v[56:59], %[pa] offset:1024\n\t" \
    "s_waitcnt lgkmcnt(6)\n\t" \
    "v_mfma_f32_16x16x32_bf16 v[12:15], v[144:147], v[32:35], v[12:15]\n\t" \
    "buffer_load_dwordx4 %[l16], %[rs], %[a] offen offset:1024 sc1 lds\n\t" \
    "v_mfma_f32_16x16x32_bf16 v[0:3], v[148:151], v[36:39], v[0:3]\n\t" \
    "ds_read_b128 v[28:31], %[xp] offset:26624\n\t" \
    "v_mfma_f32_16x16x32_bf16 v[4:7], v[156:159], v[36:39], v[4:7]\n\t" \
    "ds_read_b128 v[32:35], %[xp] offset:27648\n\t" \
    "v_mfma_f32_16x16x32_bf16 v[8:11], v[148:151], v[40:43], v[8:11]\n\t" \
    "ds_read_b128 v[60:63], %[pa] offset:2048\n\t" \
    "s_waitcnt lgkmcnt(7)\n\t" \
    "v_mfma_f32_16x16x32_bf16 v[12:15], v[156:159], v[40:43], v[12:15]\n\t" \
    "buffer_load_dwordx4 %[l16], %[rs], %[a] offen offset:2048 sc1 lds\n\t" \
    "v_mfma_f32_16x16x32_bf16 v[0:3], v[152:155], v[44:47], v[0:3]\n\t" \
    "ds_read_b128 v[36:39], %[xp] offset:28672\n\t" \
    "s_waitcnt lgkmcnt(7)\n\t" \
    "v_add_f32 v196, v196, v52\n\t" \
    "v_mfma_f32_16x16x32_bf16 v[4:7], v[160:163], v[44:47], v[4:7]\n\t" \
    "ds_read_b128 v[40:43], %[xp] offset:29696\n\t" \
    "v_add_f32 v197, v197, v53\n\t" \
    "v_mfma_f32_16x16x32_bf16 v[8:11], v[152:155], v[48:51], v[8:11]\n\t" \
    "ds_read_b128 v[64:67], %[pa] offset:3072\n\t" \
    "s_waitcnt lgkmcnt(7)\n\t" \
    "v_mfma_f32_16x16x32_bf16 v[12:15], v[160:163], v[48:51], v[12:15]\n\t" \
    "buffer_load_dwordx4 %[l16], %[rs], %[a] offen offset:3072 sc1 lds\n\t" \
    "v_add_f32 v198, v198, v54\n\t" \
    "v_add_f32 v199, v199, v55\n\t" \
    "v_mfma_f32_16x16x32_bf16 v[0:3], v[164:167], v[20:23], v[0:3]\n\t" \
    "ds_read_b128 v[44:47], %[xp] offset:30720\n\t" \
    "s_waitcnt lgkmcnt(7)\n\t" \
    "v_add_f32 v200, v200, v56\n\t" \
    "v_mfma_f32_16x16x32_bf16 v[4:7], v[172:175], v[20:23], v[4:7]\n\t" \
    "ds_read_b128 v[48:51], %[xp] offset:31744\n\t" \
    "v_add_f32 v201, v201, v57\n\t" \
    "v_mfma_f32_16x16x32_bf16 v[8:11], v[164:167], v[24:27], v[8:11]\n\t" \
    "v_add_f32 v202, v202, v58\n\t" \
    "s_waitcnt lgkmcnt(6)\n\t" \
    "v_mfma_f32_16x16x32_bf16 v[12:15], v[172:175], v[24:27], v[12:15]\n\t" \
    "v_add_f32 v203, v203, v59\n\t" \
    "s_waitcnt lgkmcnt(5)\n\t" \
    "v_add_f32 v204, v204, v60\n\t" \
    "v_add_f32 v205, v205, v61\n\t" \
    "v_mfma_f32_16x16x32_bf16 v[0:3], v[168:171], v[28:31], v[0:3]\n\t" \
    "ds_read_b128 v[20:23], %[xp] offset:32768\n\t" \
    "v_add_f32 v206, v206, v62\n\t" \
    "v_mfma_f32_16x16x32_bf16 v[4:7], v[176:179], v[28:31], v[4:7]\n\t" \
    "ds_read_b128 v[24:27], %[xp] offset:33792\n\t" \
    "v_add_f32 v207, v207, v63\n\t" \
    "v_mfma_f32_16x16x32_bf16 v[8:11], v[168:171], v[32:35], v[8:11]\n\t" \
    "s_waitcnt lgkmcnt(4)\n\t" \
    "v_add_f32 v208, v208, v64\n\t" \
    "v_mfma_f32_16x16x32_bf16 v[12:15], v[176:179], v[32:35], v[12:15]\n\t" \
    "v_add_f32 v209, v209, v65\n\t" \
    "v_add_f32 v210, v210, v66\n\t" \
    "v_add_f32 v211, v211, v67\n\t" \
    "v_mfma_f32_16x16x32_bf16 v[0:3], v[180:183], v[36:39], v[0:3]\n\t" \
    "ds_read_b128 v[28:31], %[xp] offset:34816\n\t" \
    "v_cvt_pk_bf16_f32 v52, v196, v200\n\t" \
    "v_mfma_f32_16x16x32_bf16 v[4:7], v[188:191], v[36:39], v[4:7]\n\t" \
    "ds_read_b128 v[32:35], %[xp] offset:35840\n\t" \
    "v_cvt_pk_bf16_f32 v53, v197, v201\n\t" \
    "v_mfma_f32_16x16x32_bf16 v[8:11], v[180:183], v[40:43], v[8:11]\n\t" \
    "v_cvt_pk_bf16_f32 v54, v198, v202\n\t" \
    "s_waitcnt lgkmcnt(4)\n\t" \
    "v_mfma_f32_16x16x32_bf16 v[12:15], v[188:191], v[40:43], v[12:15]\n\t" \
    "v_cvt_pk_bf16_f32 v55, v199, v203\n\t" \
    "v_cvt_pk_bf16_f32 v56, v204, v208\n\t" \
    "v_cvt_pk_bf16_f32 v57, v205, v209\n\t" \
    "v_mfma_f32_16x16x32_bf16 v[0:3], v[184:187], v[44:47], v[0:3]\n\t" \
    "ds_read_b128 v[36:39], %[xp] offset:36864\n\t" \
    "v_cvt_pk_bf16_f32 v58, v206, v210\n\t" \
    "v_mfma_f32_16x16x32_bf16 v[4:7], v[192:195], v[44:47], v[4:7]\n\t" \
    "ds_read_b128 v[40:43], %[xp] offset:37888\n\t" \
    "v_cvt_pk_bf16_f32 v59, v207, v211\n\t" \
    "v_mfma_f32_16x16x32_bf16 v[8:11], v[184:187], v[48:51], v[8:11]\n\t" \
    "s_waitcnt lgkmcnt(4)\n\t" \
    "v_mfma_f32_16x16x32_bf16 v[12:15], v[192:195], v[48:51], v[12:15]\n\t" \
    "s_mov_b64 exec, %[em]\n\t" \
    "buffer_store_dwordx4 v[52:55], %[l16], %[rs], %[oo] offen sc1\n\t" \
    "buffer_store_dwordx4 v[56:59], %[l16], %[rs], %[b] offen sc1\n\t" \
    "s_mov_b64 exec, -1\n\t" \
    "s_add_u32 %[sx], %[sx], 65536\n\t" \
    "s_add_u32 %[t], %[t], 1\n\t" \
    "s_add_u32 %[oo], %[oo], 262144\n\t" \
    "s_waitcnt vmcnt(6)\n\t" \
    "s_cmp_gt_u32 %[t], " STR(SQUICK) " + 1\n\t" \
    "s_cbranch_scc1 B0_%=\n\t" \
    "s_waitcnt vmcnt(0)\n\t" \
    "B0_%=:\n\t" \
    "s_barrier\n\t" \
    "s_setprio 1\n\t" \
    "s_add_u32 m0, %[sd], 16384\n\t" \
    "s_add_u32 %[a], %[sx], 81920\n\t" \
    "v_mfma_f32_16x16x32_bf16 v[196:199], v[68:71], v[20:23], 0\n\t" \
    "ds_read_b128 v[44:47], %[xp] offset:38912\n\t" \
    "v_mfma_f32_16x16x32_bf16 v[200:203], v[76:79], v[20:23], 0\n\t" \
    "ds_read_b128 v[48:51], %[xp] offset:39936\n\t" \
    "v_mfma_f32_16x16x32_bf16 v[204:207], v[68:71], v[24:27], 0\n\t" \
    "s_waitcnt lgkmcnt(4)\n\t" \
    "v_mfma_f32_16x16x32_bf16 v[208:211], v[76:79], v[24:27], 0\n\t" \
    "v_mfma_f32_16x16x32_bf16 v[196:199], v[72:75], v[28:31], v[196:199]\n\t" \
    "ds_read_b128 v[20:23], %[xp] offset:40960\n\t" \
    "v_mfma_f32_16x16x32_bf16 v[200:203], v[80:83], v[28:31], v[200:203]\n\t" \
    "ds_read_b128 v[24:27], %[xp] offset:41984\n\t" \
    "v_mfma_f32_16x16x32_bf16 v[204:207], v[72:75], v[32:35], v[204:207]\n\t" \
    "s_waitcnt lgkmcnt(4)\n\t" \
    "v_mfma_f32_16x16x32_bf16 v[208:211], v[80:83], v[32:35], v[208:211]\n\t" \
    "v_mfma_f32_16x16x32_bf16 v[196:199], v[84:87], v[36:39], v[196:199]\n\t" \
    "ds_read_b128 v[28:31], %[xp] offset:43008\n\t" \
    "v_mfma_f32_16x16x32_bf16 v[200:203], v[92:95], v[36:39], v[200:203]\n\t" \
    "ds_read_b128 v[32:35], %[xp] offset:44032\n\t" \
    "v_mfma_f32_16x16x32_bf16 v[204:207], v[84:87], v[40:43], v[204:207]\n\t" \
    "s_waitcnt lgkmcnt(4)\n\t" \
    "v_mfma_f32_16x16x32_bf16 v[208:211], v[92:95], v[40:43], v[208:211]\n\t" \
    "v_mfma_f32_16x16x32_bf16 v[196:199], v[88:91], v[44:47], v[196:199]\n\t" \
    "ds_read_b128 v[36:39], %[xp] offset:45056\n\t" \
    "v_mfma_f32_16x16x32_bf16 v[200:203], v[96:99], v[44:47], v[200:203]\n\t" \
    "ds_read_b128 v[40:43], %[xp] offset:46080\n\t" \
    "v_mfma_f32_16x16x32_bf16 v[204:207], v[88:91], v[48:51], v[204:207]\n\t" \
    "s_waitcnt lgkmcnt(4)\n\t" \
    "v_mfma_f32_16x16x32_bf16 v[208:211], v[96:99], v[48:51], v[208:211]\n\t" \
    "v_mfma_f32_16x16x32_bf16 v[196:199], v[100:103], v[20:23], v[196:199]\n\t" \
    "ds_read_b128 v[44:47], %[xp] offset:47104\n\t" \
    "v_mfma_f32_16x16x32_bf16 v[200:203], v[108:111], v[20:23], v[200:203]\n\t" \
    "ds_read_b128 v[48:51], %[xp] offset:48128\n\t" \
    "v_mfma_f32_16x16x32_bf16 v[204:207], v[100:103], v[24:27], v[204:207]\n\t" \
    "s_waitcnt lgkmcnt(4)\n\t" \
    "v_mfma_f32_16x16x32_bf16 v[208:211], v[108:111], v[24:27], v[208:211]\n\t" \
    "buffer_load_dwordx4 %[l16], %[rs], %[a] offen offset:0 sc1 lds\n\t" \
    "v_mfma_f32_16x16x32_bf16 v[196:199], v[104:107], v[28:31], v[196:199]\n\t" \
    "ds_read_b128 v[20:23], %[xp] offset:49152\n\t" \
    "v_mfma_f32_16x16x32_bf16 v[200:203], v[112:115], v[28:31], v[200:203]\n\t" \
    "ds_read_b128 v[24:27], %[xp] offset:50176\n\t" \
    "v_mfma_f32_16x16x32_bf16 v[204:207], v[104:107], v[32:35], v[204:207]\n\t" \
    "s_waitcnt lgkmcnt(4)\n\t" \
    "v_mfma_f32_16x16x32_bf16 v[208:211], v[112:115], v[32:35], v[208:211]\n\t" \
    "buffer_load_dwordx4 %[l16], %[rs], %[a] offen offset:1024 sc1 lds\n\t" \
    "v_mfma_f32_16x16x32_bf16 v[196:199], v[116:119], v[36:39], v[196:199]\n\t" \
    "ds_read_b128 v[28:31], %[xp] offset:51200\n\t" \
    "v_mfma_f32_16x16x32_bf16 v[200:203], v[124:127], v[36:39], v[200:203]\n\t" \
    "ds_read_b128 v[32:35], %[xp] offset:52224\n\t" \
    "v_mfma_f32_16x16x32_bf16 v[204:207], v[116:119], v[40:43], v[204:207]\n\t" \
    "s_waitcnt lgkmcnt(4)\n\t" \
    "v_mfma_f32_16x16x32_bf16 v[208:211], v[124:127], v[40:43], v[208:211]\n\t" \
    "buffer_load_dwordx4 %[l16], %[rs], %[a] offen offset:2048 sc1 lds\n\t" \
    "v_mfma_f32_16x16x32_bf16 v[196:199], v[120:123], v[44:47], v[196:199]\n\t" \
    "ds_read_b128 v[36:39], %[xp] offset:53248\n\t" \
    "v_mfma_f32_16x16x32_bf16 v[200:203], v[128:131], v[44:47], v[200:203]\n\t" \
    "ds_read_b128 v[40:43], %[xp] offset:54272\n\t" \
    "v_mfma_f32_16x16x32_bf16 v[204:207], v[120:123], v[48:51], v[204:207]\n\t" \
    "s_waitcnt lgkmcnt(4)\n\t" \
    "v_mfma_f32_16x16x32_bf16 v[208:211], v[128:131], v[48:51], v[208:211]\n\t" \
    "buffer_load_dwordx4 %[l16], %[rs], %[a] offen offset:3072 sc1 lds\n\t" \
    "s_waitcnt vmcnt(4)\n\t" \
    "s_barrier\n\t" \
    "s_setprio 1\n\t" \
    "s_add_u32 m0, %[sd], 32768\n\t" \
    "s_add_u32 %[a], %[sx], 131072\n\t" \
    "s_add_u32 %[b], %[oo], 0x1000\n\t" \
    "s_cmp_lg_u32 %[t], 1\n\t" \
    "s_cselect_b64 %[em], -1, 0\n\t" \
    "v_mfma_f32_16x16x32_bf16 v[196:199], v[132:135], v[20:23], v[196:199]\n\t" \
    "ds_read_b128 v[44:47], %[xp] offset:55296\n\t" \
    "v_mfma_f32_16x16x32_bf16 v[200:203], v[140:143], v[20:23], v[200:203]\n\t" \
    "ds_read_b128 v[48:51], %[xp] offset:56320\n\t" \
    "v_mfma_f32_16x16x32_bf16 v[204:207], v[132:135], v[24:27], v[204:207]\n\t" \
    "ds_read_b128 v[52:55], %[pa] offset:0\n\t" \
    "s_waitcnt lgkmcnt(5)\n\t" \
    "v_mfma_f32_16x16x32_bf16 v[208:211], v[140:143], v[24:27], v[208:211]\n\t" \
    "buffer_load_dwordx4 %[l16], %[rs], %[a] offen offset:0 sc1 lds\n\t" \
    "v_mfma_f32_16x16x32_bf16 v[196:199], v[136:139], v[28:31], v[196:199]\n\t" \
    "ds_read_b128 v[20:23], %[xp] offset:57344\n\t" \
    "v_mfma_f32_16x16x32_bf16 v[200:203], v[144:147], v[28:31], v[200:203]\n\t" \
    "ds_read_b128 v[24:27], %[xp] offset:58368\n\t" \
    "v_mfma_f32_16x16x32_bf16 v[204:207], v[136:139], v[32:35], v[204:207]\n\t" \
    "ds_read_b128 v[56:59], %[pa] offset:1024\n\t" \
    "s_waitcnt lgkmcnt(6)\n\t" \
    "v_mfma_f32_16x16x32_bf16 v[208:211], v[144:147], v[32:35], v[208:211]\n\t" \
    "buffer_load_dwordx4 %[l16], %[rs], %[a] offen offset:1024 sc1 lds\n\t" \
    "v_mfma_f32_16x16x32_bf16 v[196:199], v[148:151], v[36:39], v[196:199]\n\t" \
    "ds_read_b128 v[28:31], %[xp] offset:59392\n\t" \
    "v_mfma_f32_16x16x32_bf16 v[200:203], v[156:159], v[36:39], v[200:203]\n\t" \
    "ds_read_b128 v[32:35], %[xp] offset:60416\n\t" \
    "v_mfma_f32_16x16x32_bf16 v[204:207], v[148:151], v[40:43], v[204:207]\n\t" \
    "ds_read_b128 v[60:63], %[pa] offset:2048\n\t" \
    "s_waitcnt lgkmcnt(7)\n\t" \
    "v_mfma_f32_16x16x32_bf16 v[208:211], v[156:159], v[40:43], v[208:211]\n\t" \
    "buffer_load_dwordx4 %[l16], %[rs], %[a] offen offset:2048 sc1 lds\n\t" \
    "v_mfma_f32_16x16x32_bf16 v[196:199], v[152:155], v[44:47], v[196:199]\n\t" \
    "ds_read_b128 v[36:39], %[xp] offset:61440\n\t" \
    "s_waitcnt lgkmcnt(7)\n\t" \
    "v_add_f32 v0, v0, v52\n\t" \
    "v_mfma_f32_16x16x32_bf16 v[200:203], v[160:163], v[44:47], v[200:203]\n\t" \
    "ds_read_b128 v[40:43], %[xp] offset:62464\n\t" \
    "v_add_f32 v1, v1, v53\n\t" \
    "v_mfma_f32_16x16x32_bf16 v[204:207], v[152:155], v[48:51], v[204:207]\n\t" \
    "ds_read_b128 v[64:67], %[pa] offset:3072\n\t" \
    "s_waitcnt lgkmcnt(7)\n\t" \
    "v_mfma_f32_16x16x32_bf16 v[208:211], v[160:163], v[48:51], v[208:211]\n\t" \
    "buffer_load_dwordx4 %[l16], %[rs], %[a] offen offset:3072 sc1 lds\n\t" \
    "v_add_f32 v2, v2, v54\n\t" \
    "v_add_f32 v3, v3, v55\n\t" \
    "v_mfma_f32_16x16x32_bf16 v[196:199], v[164:167], v[20:23], v[196:199]\n\t" \
    "ds_read_b128 v[44:47], %[xp] offset:63488\n\t" \
    "s_waitcnt lgkmcnt(7)\n\t" \
    "v_add_f32 v4, v4, v56\n\t" \
    "v_mfma_f32_16x16x32_bf16 v[200:203], v[172:175], v[20:23], v[200:203]\n\t" \
    "ds_read_b128 v[48:51], %[xp] offset:64512\n\t" \
    "v_add_f32 v5, v5, v57\n\t" \
    "v_mfma_f32_16x16x32_bf16 v[204:207], v[164:167], v[24:27], v[204:207]\n\t" \
    "v_add_f32 v6, v6, v58\n\t" \
    "s_waitcnt lgkmcnt(6)\n\t" \
    "v_mfma_f32_16x16x32_bf16 v[208:211], v[172:175], v[24:27], v[208:211]\n\t" \
    "v_add_f32 v7, v7, v59\n\t" \
    "s_waitcnt lgkmcnt(5)\n\t" \
    "v_add_f32 v8, v8, v60\n\t" \
    "v_add_f32 v9, v9, v61\n\t" \
    "v_mfma_f32_16x16x32_bf16 v[196:199], v[168:171], v[28:31], v[196:199]\n\t" \
    "ds_read_b128 v[20:23], %[xp] offset:0\n\t" \
    "v_add_f32 v10, v10, v62\n\t" \
    "v_mfma_f32_16x16x32_bf16 v[200:203], v[176:179], v[28:31], v[200:203]\n\t" \
    "ds_read_b128 v[24:27], %[xp] offset:1024\n\t" \
    "v_add_f32 v11, v11, v63\n\t" \
    "v_mfma_f32_16x16x32_bf16 v[204:207], v[168:171], v[32:35], v[204:207]\n\t" \
    "s_waitcnt lgkmcnt(4)\n\t" \
    "v_add_f32 v12, v12, v64\n\t" \
    "v_mfma_f32_16x16x32_bf16 v[208:211], v[176:179], v[32:35], v[208:211]\n\t" \
    "v_add_f32 v13, v13, v65\n\t" \
    "v_add_f32 v14, v14, v66\n\t" \
    "v_add_f32 v15, v15, v67\n\t" \
    "v_mfma_f32_16x16x32_bf16 v[196:199], v[180:183], v[36:39], v[196:199]\n\t" \
    "ds_read_b128 v[28:31], %[xp] offset:2048\n\t" \
    "v_cvt_pk_bf16_f32 v52, v0, v4\n\t" \
    "v_mfma_f32_16x16x32_bf16 v[200:203], v[188:191], v[36:39], v[200:203]\n\t" \
    "ds_read_b128 v[32:35], %[xp] offset:3072\n\t" \
    "v_cvt_pk_bf16_f32 v53, v1, v5\n\t" \
    "v_mfma_f32_16x16x32_bf16 v[204:207], v[180:183], v[40:43], v[204:207]\n\t" \
    "v_cvt_pk_bf16_f32 v54, v2, v6\n\t" \
    "s_waitcnt lgkmcnt(4)\n\t" \
    "v_mfma_f32_16x16x32_bf16 v[208:211], v[188:191], v[40:43], v[208:211]\n\t" \
    "v_cvt_pk_bf16_f32 v55, v3, v7\n\t" \
    "v_cvt_pk_bf16_f32 v56, v8, v12\n\t" \
    "v_cvt_pk_bf16_f32 v57, v9, v13\n\t" \
    "v_mfma_f32_16x16x32_bf16 v[196:199], v[184:187], v[44:47], v[196:199]\n\t" \
    "ds_read_b128 v[36:39], %[xp] offset:4096\n\t" \
    "v_cvt_pk_bf16_f32 v58, v10, v14\n\t" \
    "v_mfma_f32_16x16x32_bf16 v[200:203], v[192:195], v[44:47], v[200:203]\n\t" \
    "ds_read_b128 v[40:43], %[xp] offset:5120\n\t" \
    "v_cvt_pk_bf16_f32 v59, v11, v15\n\t" \
    "v_mfma_f32_16x16x32_bf16 v[204:207], v[184:187], v[48:51], v[204:207]\n\t" \
    "s_waitcnt lgkmcnt(4)\n\t" \
    "v_mfma_f32_16x16x32_bf16 v[208:211], v[192:195], v[48:51], v[208:211]\n\t" \
    "s_mov_b64 exec, %[em]\n\t" \
    "buffer_store_dwordx4 v[52:55], %[l16], %[rs], %[oo] offen sc1\n\t" \
    "buffer_store_dwordx4 v[56:59], %[l16], %[rs], %[b] offen sc1\n\t" \
    "s_mov_b64 exec, -1\n\t" \
    "s_add_u32 %[sx], %[sx], 65536\n\t" \
    "s_add_u32 %[t], %[t], 1\n\t" \
    "s_add_u32 %[oo], %[oo], 262144\n\t" \
    "s_waitcnt vmcnt(6)\n\t" \
    "s_cmp_gt_u32 %[t], " STR(SQUICK) " + 1\n\t" \
    "s_cbranch_scc1 B1_%=\n\t" \
    "s_waitcnt vmcnt(0)\n\t" \
    "B1_%=:\n\t" \
    "s_barrier\n\t" \
    "s_cmp_lt_u32 %[t], %[te]\n\t" \
    "s_cbranch_scc1 L1_%=\n\t" \
    "s_cmp_eq_u32 %[fin], %[te]\n\t" \
    "s_cbranch_scc0 E_%=\n\t" \
    "s_nop 4\n\t" \
    "s_barrier\n\t" \
    "ds_read_b128 v[52:55], %[pa] offset:0\n\t" \
    "ds_read_b128 v[56:59], %[pa] offset:1024\n\t" \
    "ds_read_b128 v[60:63], %[pa] offset:2048\n\t" \
    "ds_read_b128 v[64:67], %[pa] offset:3072\n\t" \
    "s_add_u32 %[b], %[oo], 0x1000\n\t" \
    "s_waitcnt lgkmcnt(0)\n\t" \
    "v_add_f32 v196, v196, v52\n\t" \
    "v_add_f32 v197, v197, v53\n\t" \
    "v_add_f32 v198, v198, v54\n\t" \
    "v_add_f32 v199, v199, v55\n\t" \
    "v_add_f32 v200, v200, v56\n\t" \
    "v_add_f32 v201, v201, v57\n\t" \
    "v_add_f32 v202, v202, v58\n\t" \
    "v_add_f32 v203, v203, v59\n\t" \
    "v_add_f32 v204, v204, v60\n\t" \
    "v_add_f32 v205, v205, v61\n\t" \
    "v_add_f32 v206, v206, v62\n\t" \
    "v_add_f32 v207, v207, v63\n\t" \
    "v_add_f32 v208, v208, v64\n\t" \
    "v_add_f32 v209, v209, v65\n\t" \
    "v_add_f32 v210, v210, v66\n\t" \
    "v_add_f32 v211, v211, v67\n\t" \
    "v_cvt_pk_bf16_f32 v52, v196, v200\n\t" \
    "v_cvt_pk_bf16_f32 v53, v197, v201\n\t" \
    "v_cvt_pk_bf16_f32 v54, v198, v202\n\t" \
    "v_cvt_pk_bf16_f32 v55, v199, v203\n\t" \
    "v_cvt_pk_bf16_f32 v56, v204, v208\n\t" \
    "v_cvt_pk_bf16_f32 v57, v205, v209\n\t" \
    "v_cvt_pk_bf16_f32 v58, v206, v210\n\t" \
    "v_cvt_pk_bf16_f32 v59, v207, v211\n\t" \
    "buffer_store_dwordx4 v[52:55], %[l16], %[rs], %[oo] offen sc1\n\t" \
    "buffer_store_dwordx4 v[56:59], %[l16], %[rs], %[b] offen sc1\n\t" \
    "E_%=:\n\t" \
    "s_setprio 0\n\t" \
    "s_waitcnt lgkmcnt(0)\n\t" \
    "s_nop 7\n\t" \
    : [t] "+s"(t), [sx] "+s"(sx), [oo] "+s"(oo), [a] "=&s"(tmp), [b] "=&s"(tmp2), [em] "=&s"(em), "+{v[20:27]}"(xf[0]), "+{v[28:35]}"(xf[1]), "+{v[36:43]}"(xf[2]), "+{v[196:203]}"(ac[0]), "+{v[204:211]}"(ac[1]) \
    : [te] "s"(TE_), [fin] "s"(FIN_), [kh] "s"(kh), [sd] "s"(sd), [rs] "s"(rs), [l16] "v"(l16), [xp] "v"(xp), [pa] "v"(pa), [fm] "s"(fm), [pm] "s"(pm), [fo] "s"(fo), [po] "s"(po), [pd] "s"(pd), [tag] "s"(P.tag), "{v[68:75]}"(w[0][0]), "{v[76:83]}"(w[0][1]), "{v[84:91]}"(w[1][0]), "{v[92:99]}"(w[1][1]), "{v[100:107]}"(w[2][0]), "{v[108:115]}"(w[2][1]), "{v[116:123]}"(w[3][0]), "{v[124:131]}"(w[3][1]), "{v[132:139]}"(w[4][0]), "{v[140:147]}"(w[4][1]), "{v[148:155]}"(w[5][0]), "{v[156:163]}"(w[5][1]), "{v[164:171]}"(w[6][0]), "{v[172:179]}"(w[6][1]), "{v[180:187]}"(w[7][0]), "{v[188:195]}"(w[7][1]) \
    : "memory", "scc", "v0", "v1", "v2", "v3", "v4", "v5", "v6", "v7", "v8", "v9", "v10", "v11", "v12", "v13", "v14", "v15", "v44", "v45", "v46", "v47", "v48", "v49", "v50", "v51", "v52", "v53", "v54", "v55", "v56", "v57", "v58", "v59", "v60", "v61", "v62", "v63", "v64", "v65", "v66", "v67")

#define S_LOOP_V(TE_, FIN_) \
  asm volatile( \
    "s_cmp_eq_u32 %[kh], 0\n\t" \
    "s_cbranch_scc0 L1_%=\n\t" \
    "L0_%=:\n\t" \
    "s_setprio 0\n\t" \
    "s_nop 3\n\t" \
    "ds_write_b128 %[pa], v[196:199] offset:0\n\t" \
    "ds_write_b128 %[pa], v[200:203] offset:1024\n\t" \
    "ds_write_b128 %[pa], v[204:207] offset:2048\n\t" \
    "ds_write_b128 %[pa], v[208:211] offset:3072\n\t" \
    "s_sub_u32 %[a], %[t], 1\n\t" \
    "v_mov_b32 v53, %[tag]\n\t" \
    "v_mov_b32 v52, %[a]\n\t" \
    "s_sub_u32 %[b], %[t], 2\n\t" \
    "s_cmp_lt_u32 %[b], " STR(SQUICK) "\n\t" \
    "s_cselect_b64 exec, %[fm], 0\n\t" \
    "buffer_store_dwordx2 v[52:53], %[l16], %[rs], %[fo] offen sc1\n\t" \
    "s_mov_b64 exec, -1\n\t" \
    "s_add_u32 m0, %[sd], 49152\n\t" \
    "s_add_u32 %[a], %[sx], 81920\n\t" \
    "s_waitcnt lgkmcnt(0)\n\t" \
    "buffer_load_dwordx4 %[l16], %[rs], %[a] offen offset:0 sc1 lds\n\t" \
    "buffer_load_dwordx4 %[l16], %[rs], %[a] offen offset:1024 sc1 lds\n\t" \
    "buffer_load_dwordx4 %[l16], %[rs], %[a] offen offset:2048 sc1 lds\n\t" \
    "buffer_load_dwordx4 %[l16], %[rs], %[a] offen offset:3072 sc1 lds\n\t" \
    "v_mfma_f32_16x16x32_bf16 v[0:3], v[20:23], v[68:71], 0\n\t" \
    "ds_read_b128 v[44:47], %[xp] offset:6144\n\t" \
    "v_mfma_f32_16x16x32_bf16 v[4:7], v[20:23], v[76:79], 0\n\t" \
    "ds_read_b128 v[48:51], %[xp] offset:7168\n\t" \
    "v_mfma_f32_16x16x32_bf16 v[8:11], v[24:27], v[68:71], 0\n\t" \
    "v_mfma_f32_16x16x32_bf16 v[12:15], v[24:27], v[76:79], 0\n\t" \
    "v_mfma_f32_16x16x32_bf16 v[0:3], v[28:31], v[72:75], v[0:3]\n\t" \
    "ds_read_b128 v[20:23], %[xp] offset:8192\n\t" \
    "v_mfma_f32_16x16x32_bf16 v[4:7], v[28:31], v[80:83], v[4:7]\n\t" \
    "ds_read_b128 v[24:27], %[xp] offset:9216\n\t" \
    "v_mfma_f32_16x16x32_bf16 v[8:11], v[32:35], v[72:75], v[8:11]\n\t" \
    "v_mfma_f32_16x16x32_bf16 v[12:15], v[32:35], v[80:83], v[12:15]\n\t" \
    "v_mfma_f32_16x16x32_bf16 v[0:3], v[36:39], v[84:87], v[0:3]\n\t" \
    "ds_read_b128 v[28:31], %[xp] offset:10240\n\t" \
    "v_mfma_f32_16x16x32_bf16 v[4:7], v[36:39], v[92:95], v[4:7]\n\t" \
    "ds_read_b128 v[32:35], %[xp] offset:11264\n\t" \
    "v_mfma_f32_16x16x32_bf16 v[8:11], v[40:43], v[84:87], v[8:11]\n\t" \
    "s_waitcnt lgkmcnt(4)\n\t" \
    "v_mfma_f32_16x16x32_bf16 v[12:15], v[40:43], v[92:95], v[12:15]\n\t" \
    "v_mfma_f32_16x16x32_bf16 v[0:3], v[44:47], v[88:91], v[0:3]\n\t" \
    "ds_read_b128 v[36:39], %[xp] offset:12288\n\t" \
    "v_mfma_f32_16x16x32_bf16 v[4:7], v[44:47], v[96:99], v[4:7]\n\t" \
    "ds_read_b128 v[40:43], %[xp] offset:13312\n\t" \
    "v_mfma_f32_16x16x32_bf16 v[8:11], v[48:51], v[88:91], v[8:11]\n\t" \
    "s_waitcnt lgkmcnt(4)\n\t" \
    "v_mfma_f32_16x16x32_bf16 v[12:15], v[48:51], v[96:99], v[12:15]\n\t" \
    "v_mfma_f32_16x16x32_bf16 v[0:3], v[20:23], v[100:103], v[0:3]\n\t" \
    "ds_read_b128 v[44:47], %[xp] offset:14336\n\t" \
    "v_mfma_f32_16x16x32_bf16 v[4:7], v[20:23], v[108:111], v[4:7]\n\t" \
    "ds_read_b128 v[48:51], %[xp] offset:15360\n\t" \
    "v_mfma_f32_16x16x32_bf16 v[8:11], v[24:27], v[100:103], v[8:11]\n\t" \
    "s_waitcnt lgkmcnt(4)\n\t" \
    "v_mfma_f32_16x16x32_bf16 v[12:15], v[24:27], v[108:111], v[12:15]\n\t" \
    "v_mfma_f32_16x16x32_bf16 v[0:3], v[28:31], v[104:107], v[0:3]\n\t" \
    "ds_read_b128 v[20:23], %[xp] offset:16384\n\t" \
    "v_mfma_f32_16x16x32_bf16 v[4:7], v[28:31], v[112:115], v[4:7]\n\t" \
    "ds_read_b128 v[24:27], %[xp] offset:17408\n\t" \
    "v_mfma_f32_16x16x32_bf16 v[8:11], v[32:35], v[104:107], v[8:11]\n\t" \
    "s_waitcnt lgkmcnt(4)\n\t" \
    "v_mfma_f32_16x16x32_bf16 v[12:15], v[32:35], v[112:115], v[12:15]\n\t" \
    "v_mfma_f32_16x16x32_bf16 v[0:3], v[36:39], v[116:119], v[0:3]\n\t" \
    "ds_read_b128 v[28:31], %[xp] offset:18432\n\t" \
    "v_mfma_f32_16x16x32_bf16 v[4:7], v[36:39], v[124:127], v[4:7]\n\t" \
    "ds_read_b128 v[32:35], %[xp] offset:19456\n\t" \
    "v_mfma_f32_16x16x32_bf16 v[8:11], v[40:43], v[116:119], v[8:11]\n\t" \
    "s_waitcnt lgkmcnt(4)\n\t" \
    "v_mfma_f32_16x16x32_bf16 v[12:15], v[40:43], v[124:127], v[12:15]\n\t" \
    "v_mfma_f32_16x16x32_bf16 v[0:3], v[44:47], v[120:123], v[0:3]\n\t" \
    "ds_read_b128 v[36:39], %[xp] offset:20480\n\t" \
    "v_mfma_f32_16x16x32_bf16 v[4:7], v[44:47], v[128:131], v[4:7]\n\t" \
    "ds_read_b128 v[40:43], %[xp] offset:21504\n\t" \
    "v_mfma_f32_16x16x32_bf16 v[8:11], v[48:51], v[120:123], v[8:11]\n\t" \
    "s_waitcnt lgkmcnt(4)\n\t" \
    "v_mfma_f32_16x16x32_bf16 v[12:15], v[48:51], v[128:131], v[12:15]\n\t" \
    "s_waitcnt vmcnt(4)\n\t" \
    "s_barrier\n\t" \
    "s_setprio 0\n\t" \
    "s_sub_u32 %[a], %[t], 1\n\t" \
    "v_mov_b32 v53, %[tag]\n\t" \
    "v_mov_b32 v52, %[a]\n\t" \
    "s_cmp_gt_u32 %[t], 1\n\t" \
    "s_cselect_b64 exec, %[fm], 0\n\t" \
    "buffer_store_dwordx2 v[52:53], %[l16], %[rs], %[fo] offen sc1\n\t" \
    "s_mov_b64 exec, %[pm]\n\t" \
    "s_mov_b32 m0, %[pd]\n\t" \
    "s_add_u32 %[a], %[sx], %[po]\n\t" \
    "buffer_load_dwordx4 %[l16], %[rs], %[a] offen sc1 lds\n\t" \
    "s_mov_b64 exec, -1\n\t" \
    "s_add_u32 m0, %[sd], 0\n\t" \
    "s_add_u32 %[a], %[sx], 131072\n\t" \
    "buffer_load_dwordx4 %[l16], %[rs], %[a] offen offset:0 sc1 lds\n\t" \
    "buffer_load_dwordx4 %[l16], %[rs], %[a] offen offset:1024 sc1 lds\n\t" \
    "buffer_load_dwordx4 %[l16], %[rs], %[a] offen offset:2048 sc1 lds\n\t" \
    "buffer_load_dwordx4 %[l16], %[rs], %[a] offen offset:3072 sc1 lds\n\t" \
    "v_mfma_f32_16x16x32_bf16 v[0:3], v[20:23], v[132:135], v[0:3]\n\t" \
    "ds_read_b128 v[44:47], %[xp] offset:22528\n\t" \
    "v_mfma_f32_16x16x32_bf16 v[4:7], v[20:23], v[140:143], v[4:7]\n\t" \
    "ds_read_b128 v[48:51], %[xp] offset:23552\n\t" \
    "v_mfma_f32_16x16x32_bf16 v[8:11], v[24:27], v[132:135], v[8:11]\n\t" \
    "s_waitcnt lgkmcnt(4)\n\t" \
    "v_mfma_f32_16x16x32_bf16 v[12:15], v[24:27], v[140:143], v[12:15]\n\t" \
    "v_mfma_f32_16x16x32_bf16 v[0:3], v[28:31], v[136:139], v[0:3]\n\t" \
    "ds_read_b128 v[20:23], %[xp] offset:24576\n\t" \
    "v_mfma_f32_16x16x32_bf16 v[4:7], v[28:31], v[144:147], v[4:7]\n\t" \
    "ds_read_b128 v[24:27], %[xp] offset:25600\n\t" \
    "v_mfma_f32_16x16x32_bf16 v[8:11], v[32:35], v[136:139], v[8:11]\n\t" \
    "s_waitcnt lgkmcnt(4)\n\t" \
    "v_mfma_f32_16x16x32_bf16 v[12:15], v[32:35], v[144:147], v[12:15]\n\t" \
    "v_mfma_f32_16x16x32_bf16 v[0:3], v[36:39], v[148:151], v[0:3]\n\t" \
    "ds_read_b128 v[28:31], %[xp] offset:26624\n\t" \
    "v_mfma_f32_16x16x32_bf16 v[4:7], v[36:39], v[156:159], v[4:7]\n\t" \
    "ds_read_b128 v[32:35], %[xp] offset:27648\n\t" \
    "v_mfma_f32_16x16x32_bf16 v[8:11], v[40:43], v[148:151], v[8:11]\n\t" \
    "s_waitcnt lgkmcnt(4)\n\t" \
    "v_mfma_f32_16x16x32_bf16 v[12:15], v[40:43], v[156:159], v[12:15]\n\t" \
    "v_mfma_f32_16x16x32_bf16 v[0:3], v[44:47], v[152:155], v[0:3]\n\t" \
    "ds_read_b128 v[36:39], %[xp] offset:28672\n\t" \
    "v_mfma_f32_16x16x32_bf16 v[4:7], v[44:47], v[160:163], v[4:7]\n\t" \
    "ds_read_b128 v[40:43], %[xp] offset:29696\n\t" \
    "v_mfma_f32_16x16x32_bf16 v[8:11], v[48:51], v[152:155], v[8:11]\n\t" \
    "s_waitcnt lgkmcnt(4)\n\t" \
    "v_mfma_f32_16x16x32_bf16 v[12:15], v[48:51], v[160:163], v[12:15]\n\t" \
    "v_mfma_f32_16x16x32_bf16 v[0:3], v[20:23], v[164:167], v[0:3]\n\t" \
    "ds_read_b128 v[44:47], %[xp] offset:30720\n\t" \
    "v_mfma_f32_16x16x32_bf16 v[4:7], v[20:23], v[172:175], v[4:7]\n\t" \
    "ds_read_b128 v[48:51], %[xp] offset:31744\n\t" \
    "v_mfma_f32_16x16x32_bf16 v[8:11], v[24:27], v[164:167], v[8:11]\n\t" \
    "s_waitcnt lgkmcnt(4)\n\t" \
    "v_mfma_f32_16x16x32_bf16 v[12:15], v[24:27], v[172:175], v[12:15]\n\t" \
    "v_mfma_f32_16x16x32_bf16 v[0:3], v[28:31], v[168:171], v[0:3]\n\t" \
    "ds_read_b128 v[20:23], %[xp] offset:32768\n\t" \
    "v_mfma_f32_16x16x32_bf16 v[4:7], v[28:31], v[176:179], v[4:7]\n\t" \
    "ds_read_b128 v[24:27], %[xp] offset:33792\n\t" \
    "v_mfma_f32_16x16x32_bf16 v[8:11], v[32:35], v[168:171], v[8:11]\n\t" \
    "s_waitcnt lgkmcnt(4)\n\t" \
    "v_mfma_f32_16x16x32_bf16 v[12:15], v[32:35], v[176:179], v[12:15]\n\t" \
    "v_mfma_f32_16x16x32_bf16 v[0:3], v[36:39], v[180:183], v[0:3]\n\t" \
    "ds_read_b128 v[28:31], %[xp] offset:34816\n\t" \
    "v_mfma_f32_16x16x32_bf16 v[4:7], v[36:39], v[188:191], v[4:7]\n\t" \
    "ds_read_b128 v[32:35], %[xp] offset:35840\n\t" \
    "v_mfma_f32_16x16x32_bf16 v[8:11], v[40:43], v[180:183], v[8:11]\n\t" \
    "s_waitcnt lgkmcnt(4)\n\t" \
    "v_mfma_f32_16x16x32_bf16 v[12:15], v[40:43], v[188:191], v[12:15]\n\t" \
    "v_mfma_f32_16x16x32_bf16 v[0:3], v[44:47], v[184:187], v[0:3]\n\t" \
    "ds_read_b128 v[36:39], %[xp] offset:36864\n\t" \
    "v_mfma_f32_16x16x32_bf16 v[4:7], v[44:47], v[192:195], v[4:7]\n\t" \
    "ds_read_b128 v[40:43], %[xp] offset:37888\n\t" \
    "v_mfma_f32_16x16x32_bf16 v[8:11], v[48:51], v[184:187], v[8:11]\n\t" \
    "s_waitcnt lgkmcnt(4)\n\t" \
    "v_mfma_f32_16x16x32_bf16 v[12:15], v[48:51], v[192:195], v[12:15]\n\t" \
    "s_add_u32 %[sx], %[sx], 65536\n\t" \
    "s_add_u32 %[t], %[t], 1\n\t" \
    "s_waitcnt vmcnt(4)\n\t" \
    "s_barrier\n\t" \
    "s_setprio 0\n\t" \
    "s_nop 3\n\t" \
    "ds_write_b128 %[pa], v[0:3] offset:0\n\t" \
    "ds_write_b128 %[pa], v[4:7] offset:1024\n\t" \
    "ds_write_b128 %[pa], v[8:11] offset:2048\n\t" \
    "ds_write_b128 %[pa], v[12:15] offset:3072\n\t" \
    "s_sub_u32 %[a], %[t], 1\n\t" \
    "v_mov_b32 v53, %[tag]\n\t" \
    "v_mov_b32 v52, %[a]\n\t" \
    "s_sub_u32 %[b], %[t], 2\n\t" \
    "s_cmp_lt_u32 %[b], " STR(SQUICK) "\n\t" \
    "s_cselect_b64 exec, %[fm], 0\n\t" \
    "buffer_store_dwordx2 v[52:53], %[l16], %[rs], %[fo] offen sc1\n\t" \
    "s_mov_b64 exec, -1\n\t" \
    "s_add_u32 m0, %[sd], 16384\n\t" \
    "s_add_u32 %[a], %[sx], 81920\n\t" \
    "s_waitcnt lgkmcnt(0)\n\t" \
    "buffer_load_dwordx4 %[l16], %[rs], %[a] offen offset:0 sc1 lds\n\t" \
    "buffer_load_dwordx4 %[l16], %[rs], %[a] offen offset:1024 sc1 lds\n\t" \
    "buffer_load_dwordx4 %[l16], %[rs], %[a] offen offset:2048 sc1 lds\n\t" \
    "buffer_load_dwordx4 %[l16], %[rs], %[a] offen offset:3072 sc1 lds\n\t" \
    "v_mfma_f32_16x16x32_bf16 v[196:199], v[20:23], v[68:71], 0\n\t" \
    "ds_read_b128 v[44:47], %[xp] offset:38912\n\t" \
    "v_mfma_f32_16x16x32_bf16 v[200:203], v[20:23], v[76:79], 0\n\t" \
    "ds_read_b128 v[48:51], %[xp] offset:39936\n\t" \
    "v_mfma_f32_16x16x32_bf16 v[204:207], v[24:27], v[68:71], 0\n\t" \
    "v_mfma_f32_16x16x32_bf16 v[208:211], v[24:27], v[76:79], 0\n\t" \
    "v_mfma_f32_16x16x32_bf16 v[196:199], v[28:31], v[72:75], v[196:199]\n\t" \
    "ds_read_b128 v[20:23], %[xp] offset:40960\n\t" \
    "v_mfma_f32_16x16x32_bf16 v[200:203], v[28:31], v[80:83], v[200:203]\n\t" \
    "ds_read_b128 v[24:27], %[xp] offset:41984\n\t" \
    "v_mfma_f32_16x16x32_bf16 v[204:207], v[32:35], v[72:75], v[204:207]\n\t" \
    "v_mfma_f32_16x16x32_bf16 v[208:211], v[32:35], v[80:83], v[208:211]\n\t" \
    "v_mfma_f32_16x16x32_bf16 v[196:199], v[36:39], v[84:87], v[196:199]\n\t" \
    "ds_read_b128 v[28:31], %[xp] offset:43008\n\t" \
    "v_mfma_f32_16x16x32_bf16 v[200:203], v[36:39], v[92:95], v[200:203]\n\t" \
    "ds_read_b128 v[32:35], %[xp] offset:44032\n\t" \
    "v_mfma_f32_16x16x32_bf16 v[204:207], v[40:43], v[84:87], v[204:207]\n\t" \
    "s_waitcnt lgkmcnt(4)\n\t" \
    "v_mfma_f32_16x16x32_bf16 v[208:211], v[40:43], v[92:95], v[208:211]\n\t" \
    "v_mfma_f32_16x16x32_bf16 v[196:199], v[44:47], v[88:91], v[196:199]\n\t" \
    "ds_read_b128 v[36:39], %[xp] offset:45056\n\t" \
    "v_mfma_f32_16x16x32_bf16 v[200:203], v[44:47], v[96:99], v[200:203]\n\t" \
    "ds_read_b128 v[40:43], %[xp] offset:46080\n\t" \
    "v_mfma_f32_16x16x32_bf16 v[204:207], v[48:51], v[88:91], v[204:207]\n\t" \
    "s_waitcnt lgkmcnt(4)\n\t" \
    "v_mfma_f32_16x16x32_bf16 v[208:211], v[48:51], v[96:99], v[208:211]\n\t" \
    "v_mfma_f32_16x16x32_bf16 v[196:199], v[20:23], v[100:103], v[196:199]\n\t" \
    "ds_read_b128 v[44:47], %[xp] offset:47104\n\t" \
    "v_mfma_f32_16x16x32_bf16 v[200:203], v[20:23], v[108:111], v[200:203]\n\t" \
    "ds_read_b128 v[48:51], %[xp] offset:48128\n\t" \
    "v_mfma_f32_16x16x32_bf16 v[204:207], v[24:27], v[100:103], v[204:207]\n\t" \
    "s_waitcnt lgkmcnt(4)\n\t" \
    "v_mfma_f32_16x16x32_bf16 v[208:211], v[24:27], v[108:111], v[208:211]\n\t" \
    "v_mfma_f32_16x16x32_bf16 v[196:199], v[28:31], v[104:107], v[196:199]\n\t" \
    "ds_read_b128 v[20:23], %[xp] offset:49152\n\t" \
    "v_mfma_f32_16x16x32_bf16 v[200:203], v[28:31], v[112:115], v[200:203]\n\t" \
    "ds_read_b128 v[24:27], %[xp] offset:50176\n\t" \
    "v_mfma_f32_16x16x32_bf16 v[204:207], v[32:35], v[104:107], v[204:207]\n\t" \
    "s_waitcnt lgkmcnt(4)\n\t" \
    "v_mfma_f32_16x16x32_bf16 v[208:211], v[32:35], v[112:115], v[208:211]\n\t" \
    "v_mfma_f32_16x16x32_bf16 v[196:199], v[36:39], v[116:119], v[196:199]\n\t" \
    "ds_read_b128 v[28:31], %[xp] offset:51200\n\t" \
    "v_mfma_f32_16x16x32_bf16 v[200:203], v[36:39], v[124:127], v[200:203]\n\t" \
    "ds_read_b128 v[32:35], %[xp] offset:52224\n\t" \
    "v_mfma_f32_16x16x32_bf16 v[204:207], v[40:43], v[116:119], v[204:207]\n\t" \
    "s_waitcnt lgkmcnt(4)\n\t" \
    "v_mfma_f32_16x16x32_bf16 v[208:211], v[40:43], v[124:127], v[208:211]\n\t" \
    "v_mfma_f32_16x16x32_bf16 v[196:199], v[44:47], v[120:123], v[196:199]\n\t" \
    "ds_read_b128 v[36:39], %[xp] offset:53248\n\t" \
    "v_mfma_f32_16x16x32_bf16 v[200:203], v[44:47], v[128:131], v[200:203]\n\t" \
    "ds_read_b128 v[40:43], %[xp] offset:54272\n\t" \
    "v_mfma_f32_16x16x32_bf16 v[204:207], v[48:51], v[120:123], v[204:207]\n\t" \
    "s_waitcnt lgkmcnt(4)\n\t" \
    "v_mfma_f32_16x16x32_bf16 v[208:211], v[48:51], v[128:131], v[208:211]\n\t" \
    "s_waitcnt vmcnt(4)\n\t" \
    "s_barrier\n\t" \
    "s_setprio 0\n\t" \
    "s_sub_u32 %[a], %[t], 1\n\t" \
    "v_mov_b32 v53, %[tag]\n\t" \
    "v_mov_b32 v52, %[a]\n\t" \
    "s_cmp_gt_u32 %[t], 1\n\t" \
    "s_cselect_b64 exec, %[fm], 0\n\t" \
    "buffer_store_dwordx2 v[52:53], %[l16], %[rs], %[fo] offen sc1\n\t" \
    "s_mov_b64 exec, %[pm]\n\t" \
    "s_mov_b32 m0, %[pd]\n\t" \
    "s_add_u32 %[a], %[sx], %[po]\n\t" \
    "buffer_load_dwordx4 %[l16], %[rs], %[a] offen sc1 lds\n\t" \
    "s_mov_b64 exec, -1\n\t" \
    "s_add_u32 m0, %[sd], 32768\n\t" \
    "s_add_u32 %[a], %[sx], 131072\n\t" \
    "buffer_load_dwordx4 %[l16], %[rs], %[a] offen offset:0 sc1 lds\n\t" \
    "buffer_load_dwordx4 %[l16], %[rs], %[a] offen offset:1024 sc1 lds\n\t" \
    "buffer_load_dwordx4 %[l16], %[rs], %[a] offen offset:2048 sc1 lds\n\t" \
    "buffer_load_dwordx4 %[l16], %[rs], %[a] offen offset:3072 sc1 lds\n\t" \
    "v_mfma_f32_16x16x32_bf16 v[196:199], v[20:23], v[132:135], v[196:199]\n\t" \
    "ds_read_b128 v[44:47], %[xp] offset:55296\n\t" \
    "v_mfma_f32_16x16x32_bf16 v[200:203], v[20:23], v[140:143], v[200:203]\n\t" \
    "ds_read_b128 v[48:51], %[xp] offset:56320\n\t" \
    "v_mfma_f32_16x16x32_bf16 v[204:207], v[24:27], v[132:135], v[204:207]\n\t" \
    "s_waitcnt lgkmcnt(4)\n\t" \
    "v_mfma_f32_16x16x32_bf16 v[208:211], v[24:27], v[140:143], v[208:211]\n\t" \
    "v_mfma_f32_16x16x32_bf16 v[196:199], v[28:31], v[136:139], v[196:199]\n\t" \
    "ds_read_b128 v[20:23], %[xp] offset:57344\n\t" \
    "v_mfma_f32_16x16x32_bf16 v[200:203], v[28:31], v[144:147], v[200:203]\n\t" \
    "ds_read_b128 v[24:27], %[xp] offset:58368\n\t" \
    "v_mfma_f32_16x16x32_bf16 v[204:207], v[32:35], v[136:139], v[204:207]\n\t" \
    "s_waitcnt lgkmcnt(4)\n\t" \
    "v_mfma_f32_16x16x32_bf16 v[208:211], v[32:35], v[144:147], v[208:211]\n\t" \
    "v_mfma_f32_16x16x32_bf16 v[196:199], v[36:39], v[148:151], v[196:199]\n\t" \
    "ds_read_b128 v[28:31], %[xp] offset:59392\n\t" \
    "v_mfma_f32_16x16x32_bf16 v[200:203], v[36:39], v[156:159], v[200:203]\n\t" \
    "ds_read_b128 v[32:35], %[xp] offset:60416\n\t" \
    "v_mfma_f32_16x16x32_bf16 v[204:207], v[40:43], v[148:151], v[204:207]\n\t" \
    "s_waitcnt lgkmcnt(4)\n\t" \
    "v_mfma_f32_16x16x32_bf16 v[208:211], v[40:43], v[156:159], v[208:211]\n\t" \
    "v_mfma_f32_16x16x32_bf16 v[196:199], v[44:47], v[152:155], v[196:199]\n\t" \
    "ds_read_b128 v[36:39], %[xp] offset:61440\n\t" \
    "v_mfma_f32_16x16x32_bf16 v[200:203], v[44:47], v[160:163], v[200:203]\n\t" \
    "ds_read_b128 v[40:43], %[xp] offset:62464\n\t" \
    "v_mfma_f32_16x16x32_bf16 v[204:207], v[48:51], v[152:155], v[204:207]\n\t" \
    "s_waitcnt lgkmcnt(4)\n\t" \
    "v_mfma_f32_16x16x32_bf16 v[208:211], v[48:51], v[160:163], v[208:211]\n\t" \
    "v_mfma_f32_16x16x32_bf16 v[196:199], v[20:23], v[164:167], v[196:199]\n\t" \
    "ds_read_b128 v[44:47], %[xp] offset:63488\n\t" \
    "v_mfma_f32_16x16x32_bf16 v[200:203], v[20:23], v[172:175], v[200:203]\n\t" \
    "ds_read_b128 v[48:51], %[xp] offset:64512\n\t" \
    "v_mfma_f32_16x16x32_bf16 v[204:207], v[24:27], v[164:167], v[204:207]\n\t" \
    "s_waitcnt lgkmcnt(4)\n\t" \
    "v_mfma_f32_16x16x32_bf16 v[208:211], v[24:27], v[172:175], v[208:211]\n\t" \
    "v_mfma_f32_16x16x32_bf16 v[196:199], v[28:31], v[168:171], v[196:199]\n\t" \
    "ds_read_b128 v[20:23], %[xp] offset:0\n\t" \
    "v_mfma_f32_16x16x32_bf16 v[200:203], v[28:31], v[176:179], v[200:203]\n\t" \
    "ds_read_b128 v[24:27], %[xp] offset:1024\n\t" \
    "v_mfma_f32_16x16x32_bf16 v[204:207], v[32:35], v[168:171], v[204:207]\n\t" \
    "s_waitcnt lgkmcnt(4)\n\t" \
    "v_mfma_f32_16x16x32_bf16 v[208:211], v[32:35], v[176:179], v[208:211]\n\t" \
    "v_mfma_f32_16x16x32_bf16 v[196:199], v[36:39], v[180:183], v[196:199]\n\t" \
    "ds_read_b128 v[28:31], %[xp] offset:2048\n\t" \
    "v_mfma_f32_16x16x32_bf16 v[200:203], v[36:39], v[188:191], v[200:203]\n\t" \
    "ds_read_b128 v[32:35], %[xp] offset:3072\n\t" \
    "v_mfma_f32_16x16x32_bf16 v[204:207], v[40:43], v[180:183], v[204:207]\n\t" \
    "s_waitcnt lgkmcnt(4)\n\t" \
    "v_mfma_f32_16x16x32_bf16 v[208:211], v[40:43], v[188:191], v[208:211]\n\t" \
    "v_mfma_f32_16x16x32_bf16 v[196:199], v[44:47], v[184:187], v[196:199]\n\t" \
    "ds_read_b128 v[36:39], %[xp] offset:4096\n\t" \
    "v_mfma_f32_16x16x32_bf16 v[200:203], v[44:47], v[192:195], v[200:203]\n\t" \
    "ds_read_b128 v[40:43], %[xp] offset:5120\n\t" \
    "v_mfma_f32_16x16x32_bf16 v[204:207], v[48:51], v[184:187], v[204:207]\n\t" \
    "s_waitcnt lgkmcnt(4)\n\t" \
    "v_mfma_f32_16x16x32_bf16 v[208:211], v[48:51], v[192:195], v[208:211]\n\t" \
    "s_add_u32 %[sx], %[sx], 65536\n\t" \
    "s_add_u32 %[t], %[t], 1\n\t" \
    "s_waitcnt vmcnt(4)\n\t" \
    "s_barrier\n\t" \
    "s_cmp_lt_u32 %[t], %[te]\n\t" \
    "s_cbranch_scc1 L0_%=\n\t" \
    "s_cmp_eq_u32 %[fin], %[te]\n\t" \
    "s_cbranch_scc0 E_%=\n\t" \
    "s_nop 4\n\t" \
    "ds_write_b128 %[pa], v[196:199] offset:0\n\t" \
    "ds_write_b128 %[pa], v[200:203] offset:1024\n\t" \
    "ds_write_b128 %[pa], v[204:207] offset:2048\n\t" \
    "ds_write_b128 %[pa], v[208:211] offset:3072\n\t" \
    "s_waitcnt lgkmcnt(0)\n\t" \
    "s_barrier\n\t" \
    "s_branch E_%=\n\t" \
    "L1_%=:\n\t" \
    "s_setprio 1\n\t" \
    "s_add_u32 m0, %[sd], 49152\n\t" \
    "s_add_u32 %[a], %[sx], 81920\n\t" \
    "v_mfma_f32_16x16x32_bf16 v[0:3], v[20:23], v[68:71], 0\n\t" \
    "ds_read_b128 v[44:47], %[xp] offset:6144\n\t" \
    "v_mfma_f32_16x16x32_bf16 v[4:7], v[20:23], v[76:79], 0\n\t" \
    "ds_read_b128 v[48:51], %[xp] offset:7168\n\t" \
    "v_mfma_f32_16x16x32_bf16 v[8:11], v[24:27], v[68:71], 0\n\t" \
    "s_waitcnt lgkmcnt(4)\n\t" \
    "v_mfma_f32_16x16x32_bf16 v[12:15], v[24:27], v[76:79], 0\n\t" \
    "v_mfma_f32_16x16x32_bf16 v[0:3], v[28:31], v[72:75], v[0:3]\n\t" \
    "ds_read_b128 v[20:23], %[xp] offset:8192\n\t" \
    "v_mfma_f32_16x16x32_bf16 v[4:7], v[28:31], v[80:83], v[4:7]\n\t" \
    "ds_read_b128 v[24:27], %[xp] offset:9216\n\t" \
    "v_mfma_f32_16x16x32_bf16 v[8:11], v[32:35], v[72:75], v[8:11]\n\t" \
    "s_waitcnt lgkmcnt(4)\n\t" \
    "v_mfma_f32_16x16x32_bf16 v[12:15], v[32:35], v[80:83], v[12:15]\n\t" \
    "v_mfma_f32_16x16x32_bf16 v[0:3], v[36:39], v[84:87], v[0:3]\n\t" \
    "ds_read_b128 v[28:31], %[xp] offset:10240\n\t" \
    "v_mfma_f32_16x16x32_bf16 v[4:7], v[36:39], v[92:95], v[4:7]\n\t" \
    "ds_read_b128 v[32:35], %[xp] offset:11264\n\t" \
    "v_mfma_f32_16x16x32_bf16 v[8:11], v[40:43], v[84:87], v[8:11]\n\t" \
    "s_waitcnt lgkmcnt(4)\n\t" \
    "v_mfma_f32_16x16x32_bf16 v[12:15], v[40:43], v[92:95], v[12:15]\n\t" \
    "v_mfma_f32_16x16x32_bf16 v[0:3], v[44:47], v[88:91], v[0:3]\n\t" \
    "ds_read_b128 v[36:39], %[xp] offset:12288\n\t" \
    "v_mfma_f32_16x16x32_bf16 v[4:7], v[44:47], v[96:99], v[4:7]\n\t" \
    "ds_read_b128 v[40:43], %[xp] offset:13312\n\t" \
    "v_mfma_f32_16x16x32_bf16 v[8:11], v[48:51], v[88:91], v[8:11]\n\t" \
    "s_waitcnt lgkmcnt(4)\n\t" \
    "v_mfma_f32_16x16x32_bf16 v[12:15], v[48:51], v[96:99], v[12:15]\n\t" \
    "v_mfma_f32_16x16x32_bf16 v[0:3], v[20:23], v[100:103], v[0:3]\n\t" \
    "ds_read_b128 v[44:47], %[xp] offset:14336\n\t" \
    "v_mfma_f32_16x16x32_bf16 v[4:7], v[20:23], v[108:111], v[4:7]\n\t" \
    "ds_read_b128 v[48:51], %[xp] offset:15360\n\t" \
    "v_mfma_f32_16x16x32_bf16 v[8:11], v[24:27], v[100:103], v[8:11]\n\t" \
    "s_waitcnt lgkmcnt(4)\n\t" \
    "v_mfma_f32_16x16x32_bf16 v[12:15], v[24:27], v[108:111], v[12:15]\n\t" \
    "buffer_load_dwordx4 %[l16], %[rs], %[a] offen offset:0 sc1 lds\n\t" \
    "v_mfma_f32_16x16x32_bf16 v[0:3], v[28:31], v[104:107], v[0:3]\n\t" \
    "ds_read_b128 v[20:23], %[xp] offset:16384\n\t" \
    "v_mfma_f32_16x16x32_bf16 v[4:7], v[28:31], v[112:115], v[4:7]\n\t" \
    "ds_read_b128 v[24:27], %[xp] offset:17408\n\t" \
    "v_mfma_f32_16x16x32_bf16 v[8:11], v[32:35], v[104:107], v[8:11]\n\t" \
    "s_waitcnt lgkmcnt(4)\n\t" \
    "v_mfma_f32_16x16x32_bf16 v[12:15], v[32:35], v[112:115], v[12:15]\n\t" \
    "buffer_load_dwordx4 %[l16], %[rs], %[a] offen offset:1024 sc1 lds\n\t" \
    "v_mfma_f32_16x16x32_bf16 v[0:3], v[36:39], v[116:119], v[0:3]\n\t" \
    "ds_read_b128 v[28:31], %[xp] offset:18432\n\t" \
    "v_mfma_f32_16x16x32_bf16 v[4:7], v[36:39], v[124:127], v[4:7]\n\t" \
    "ds_read_b128 v[32:35], %[xp] offset:19456\n\t" \
    "v_mfma_f32_16x16x32_bf16 v[8:11], v[40:43], v[116:119], v[8:11]\n\t" \
    "s_waitcnt lgkmcnt(4)\n\t" \
    "v_mfma_f32_16x16x32_bf16 v[12:15], v[40:43], v[124:127], v[12:15]\n\t" \
    "buffer_load_dwordx4 %[l16], %[rs], %[a] offen offset:2048 sc1 lds\n\t" \
    "v_mfma_f32_16x16x32_bf16 v[0:3], v[44:47], v[120:123], v[0:3]\n\t" \
    "ds_read_b128 v[36:39], %[xp] offset:20480\n\t" \
    "v_mfma_f32_16x16x32_bf16 v[4:7], v[44:47], v[128:131], v[4:7]\n\t" \
    "ds_read_b128 v[40:43], %[xp] offset:21504\n\t" \
    "v_mfma_f32_16x16x32_bf16 v[8:11], v[48:51], v[120:123], v[8:11]\n\t" \
    "s_waitcnt lgkmcnt(4)\n\t" \
    "v_mfma_f32_16x16x32_bf16 v[12:15], v[48:51], v[128:131], v[12:15]\n\t" \
    "buffer_load_dwordx4 %[l16], %[rs], %[a] offen offset:3072 sc1 lds\n\t" \
    "s_waitcnt vmcnt(4)\n\t" \
    "s_barrier\n\t" \
    "s_setprio 1\n\t" \
    "s_add_u32 m0, %[sd], 0\n\t" \
    "s_add_u32 %[a], %[sx], 131072\n\t" \
    "s_add_u32 %[b], %[oo], 0x1000\n\t" \
    "s_cmp_lg_u32 %[t], 1\n\t" \
    "s_cselect_b64 %[em], -1, 0\n\t" \
    "v_mfma_f32_16x16x32_bf16 v[0:3], v[20:23], v[132:135], v[0:3]\n\t" \
    "ds_read_b128 v[44:47], %[xp] offset:22528\n\t" \
    "v_mfma_f32_16x16x32_bf16 v[4:7], v[20:23], v[140:143], v[4:7]\n\t" \
    "ds_read_b128 v[48:51], %[xp] offset:23552\n\t" \
    "v_mfma_f32_16x16x32_bf16 v[8:11], v[24:27], v[132:135], v[8:11]\n\t" \
    "ds_read_b128 v[52:55], %[pa] offset:0\n\t" \
    "s_waitcnt lgkmcnt(5)\n\t" \
    "v_mfma_f32_16x16x32_bf16 v[12:15], v[24:27], v[140:143], v[12:15]\n\t" \
    "buffer_load_dwordx4 %[l16], %[rs], %[a] offen offset:0 sc1 lds\n\t" \
    "v_mfma_f32_16x16x32_bf16 v[0:3], v[28:31], v[136:139], v[0:3]\n\t" \
    "ds_read_b128 v[20:23], %[xp] offset:24576\n\t" \
    "v_mfma_f32_16x16x32_bf16 v[4:7], v[28:31], v[144:147], v[4:7]\n\t" \
    "ds_read_b128 v[24:27], %[xp] offset:25600\n\t" \
    "v_mfma_f32_16x16x32_bf16 v[8:11], v[32:35], v[136:139], v[8:11]\n\t" \
    "ds_read_b128 v[56:59], %[pa] offset:1024\n\t" \
    "s_waitcnt lgkmcnt(6)\n\t" \
    "v_mfma_f32_16x16x32_bf16 v[12:15], v[32:35], v[144:147], v[12:15]\n\t" \
    "buffer_load_dwordx4 %[l16], %[rs], %[a] offen offset:1024 sc1 lds\n\t" \
    "v_mfma_f32_16x16x32_bf16 v[0:3], v[36:39], v[148:151], v[0:3]\n\t" \
    "ds_read_b128 v[28:31], %[xp] offset:26624\n\t" \
    "v_mfma_f32_16x16x32_bf16 v[4:7], v[36:39], v[156:159], v[4:7]\n\t" \
    "ds_read_b128 v[32:35], %[xp] offset:27648\n\t" \
    "v_mfma_f32_16x16x32_bf16 v[8:11], v[40:43], v[148:151], v[8:11]\n\t" \
    "ds_read_b128 v[60:63], %[pa] offset:2048\n\t" \
    "s_waitcnt lgkmcnt(7)\n\t" \
    "v_mfma_f32_16x16x32_bf16 v[12:15], v[40:43], v[156:159], v[12:15]\n\t" \
    "buffer_load_dwordx4 %[l16], %[rs], %[a] offen offset:2048 sc1 lds\n\t" \
    "v_mfma_f32_16x16x32_bf16 v[0:3], v[44:47], v[152:155], v[0:3]\n\t" \
    "ds_read_b128 v[36:39], %[xp] offset:28672\n\t" \
    "s_waitcnt lgkmcnt(7)\n\t" \
    "v_add_f32 v196, v196, v52\n\t" \
    "v_mfma_f32_16x16x32_bf16 v[4:7], v[44:47], v[160:163], v[4:7]\n\t" \
    "ds_read_b128 v[40:43], %[xp] offset:29696\n\t" \
    "v_add_f32 v197, v197, v53\n\t" \
    "v_mfma_f32_16x16x32_bf16 v[8:11], v[48:51], v[152:155], v[8:11]\n\t" \
    "ds_read_b128 v[64:67], %[pa] offset:3072\n\t" \
    "s_waitcnt lgkmcnt(7)\n\t" \
    "v_mfma_f32_16x16x32_bf16 v[12:15], v[48:51], v[160:163], v[12:15]\n\t" \
    "buffer_load_dwordx4 %[l16], %[rs], %[a] offen offset:3072 sc1 lds\n\t" \
    "v_add_f32 v198, v198, v54\n\t" \
    "v_add_f32 v199, v199, v55\n\t" \
    "v_mfma_f32_16x16x32_bf16 v[0:3], v[20:23], v[164:167], v[0:3]\n\t" \
    "ds_read_b128 v[44:47], %[xp] offset:30720\n\t" \
    "s_waitcnt lgkmcnt(7)\n\t" \
    "v_add_f32 v200, v200, v56\n\t" \
    "v_mfma_f32_16x16x32_bf16 v[4:7], v[20:23], v[172:175], v[4:7]\n\t" \
    "ds_read_b128 v[48:51], %[xp] offset:31744\n\t" \
    "v_add_f32 v201, v201, v57\n\t" \
    "v_mfma_f32_16x16x32_bf16 v[8:11], v[24:27], v[164:167], v[8:11]\n\t" \
    "v_add_f32 v202, v202, v58\n\t" \
    "s_waitcnt lgkmcnt(6)\n\t" \
    "v_mfma_f32_16x16x32_bf16 v[12:15], v[24:27], v[172:175], v[12:15]\n\t" \
    "v_add_f32 v203, v203, v59\n\t" \
    "s_waitcnt lgkmcnt(5)\n\t" \
    "v_add_f32 v204, v204, v60\n\t" \
    "v_add_f32 v205, v205, v61\n\t" \
    "v_mfma_f32_16x16x32_bf16 v[0:3], v[28:31], v[168:171], v[0:3]\n\t" \
    "ds_read_b128 v[20:23], %[xp] offset:32768\n\t" \
    "v_add_f32 v206, v206, v62\n\t" \
    "v_mfma_f32_16x16x32_bf16 v[4:7], v[28:31], v[176:179], v[4:7]\n\t" \
    "ds_read_b128 v[24:27], %[xp] offset:33792\n\t" \
    "v_add_f32 v207, v207, v63\n\t" \
    "v_mfma_f32_16x16x32_bf16 v[8:11], v[32:35], v[168:171], v[8:11]\n\t" \
    "s_waitcnt lgkmcnt(4)\n\t" \
    "v_add_f32 v208, v208, v64\n\t" \
    "v_mfma_f32_16x16x32_bf16 v[12:15], v[32:35], v[176:179], v[12:15]\n\t" \
    "v_add_f32 v209, v209, v65\n\t" \
    "v_add_f32 v210, v210, v66\n\t" \
    "v_add_f32 v211, v211, v67\n\t" \
    "v_mfma_f32_16x16x32_bf16 v[0:3], v[36:39], v[180:183], v[0:3]\n\t" \
    "ds_read_b128 v[28:31], %[xp] offset:34816\n\t" \
    "v_cvt_pk_bf16_f32 v52, v196, v197\n\t" \
    "v_mfma_f32_16x16x32_bf16 v[4:7], v[36:39], v[188:191], v[4:7]\n\t" \
    "ds_read_b128 v[32:35], %[xp] offset:35840\n\t" \
    "v_cvt_pk_bf16_f32 v53, v198, v199\n\t" \
    "v_mfma_f32_16x16x32_bf16 v[8:11], v[40:43], v[180:183], v[8:11]\n\t" \
    "v_cvt_pk_bf16_f32 v54, v204, v205\n\t" \
    "s_waitcnt lgkmcnt(4)\n\t" \
    "v_mfma_f32_16x16x32_bf16 v[12:15], v[40:43], v[188:191], v[12:15]\n\t" \
    "v_cvt_pk_bf16_f32 v55, v206, v207\n\t" \
    "v_cvt_pk_bf16_f32 v56, v200, v201\n\t" \
    "v_cvt_pk_bf16_f32 v57, v202, v203\n\t" \
    "v_mfma_f32_16x16x32_bf16 v[0:3], v[44:47], v[184:187], v[0:3]\n\t" \
    "ds_read_b128 v[36:39], %[xp] offset:36864\n\t" \
    "v_cvt_pk_bf16_f32 v58, v208, v209\n\t" \
    "v_mfma_f32_16x16x32_bf16 v[4:7], v[44:47], v[192:195], v[4:7]\n\t" \
    "ds_read_b128 v[40:43], %[xp] offset:37888\n\t" \
    "v_cvt_pk_bf16_f32 v59, v210, v211\n\t" \
    "v_mfma_f32_16x16x32_bf16 v[8:11], v[48:51], v[184:187], v[8:11]\n\t" \
    "s_waitcnt lgkmcnt(4)\n\t" \
    "v_mfma_f32_16x16x32_bf16 v[12:15], v[48:51], v[192:195], v[12:15]\n\t" \
    "s_mov_b64 exec, %[em]\n\t" \
    "buffer_store_dwordx4 v[52:55], %[l16], %[rs], %[oo] offen sc1\n\t" \
    "buffer_store_dwordx4 v[56:59], %[l16], %[rs], %[oo] offen offset:1024 sc1\n\t" \
    "s_mov_b64 exec, -1\n\t" \
    "s_add_u32 %[sx], %[sx], 65536\n\t" \
    "s_add_u32 %[t], %[t], 1\n\t" \
    "s_add_u32 %[oo], %[oo], 262144\n\t" \
    "s_waitcnt vmcnt(6)\n\t" \
    "s_cmp_gt_u32 %[t], " STR(SQUICK) " + 1\n\t" \
    "s_cbranch_scc1 B0_%=\n\t" \
    "s_waitcnt vmcnt(0)\n\t" \
    "B0_%=:\n\t" \
    "s_barrier\n\t" \
    "s_setprio 1\n\t" \
    "s_add_u32 m0, %[sd], 16384\n\t" \
    "s_add_u32 %[a], %[sx], 81920\n\t" \
    "v_mfma_f32_16x16x32_bf16 v[196:199], v[20:23], v[68:71], 0\n\t" \
    "ds_read_b128 v[44:47], %[xp] offset:38912\n\t" \
    "v_mfma_f32_16x16x32_bf16 v[200:203], v[20:23], v[76:79], 0\n\t" \
    "ds_read_b128 v[48:51], %[xp] offset:39936\n\t" \
    "v_mfma_f32_16x16x32_bf16 v[204:207], v[24:27], v[68:71], 0\n\t" \
    "s_waitcnt lgkmcnt(4)\n\t" \
    "v_mfma_f32_16x16x32_bf16 v[208:211], v[24:27], v[76:79], 0\n\t" \
    "v_mfma_f32_16x16x32_bf16 v[196:199], v[28:31], v[72:75], v[196:199]\n\t" \
    "ds_read_b128 v[20:23], %[xp] offset:40960\n\t" \
    "v_mfma_f32_16x16x32_bf16 v[200:203], v[28:31], v[80:83], v[200:203]\n\t" \
    "ds_read_b128 v[24:27], %[xp] offset:41984\n\t" \
    "v_mfma_f32_16x16x32_bf16 v[204:207], v[32:35], v[72:75], v[204:207]\n\t" \
    "s_waitcnt lgkmcnt(4)\n\t" \
    "v_mfma_f32_16x16x32_bf16 v[208:211], v[32:35], v[80:83], v[208:211]\n\t" \
    "v_mfma_f32_16x16x32_bf16 v[196:199], v[36:39], v[84:87], v[196:199]\n\t" \
    "ds_read_b128 v[28:31], %[xp] offset:43008\n\t" \
    "v_mfma_f32_16x16x32_bf16 v[200:203], v[36:39], v[92:95], v[200:203]\n\t" \
    "ds_read_b128 v[32:35], %[xp] offset:44032\n\t" \
    "v_mfma_f32_16x16x32_bf16 v[204:207], v[40:43], v[84:87], v[204:207]\n\t" \
    "s_waitcnt lgkmcnt(4)\n\t" \
    "v_mfma_f32_16x16x32_bf16 v[208:211], v[40:43], v[92:95], v[208:211]\n\t" \
    "v_mfma_f32_16x16x32_bf16 v[196:199], v[44:47], v[88:91], v[196:199]\n\t" \
    "ds_read_b128 v[36:39], %[xp] offset:45056\n\t" \
    "v_mfma_f32_16x16x32_bf16 v[200:203], v[44:47], v[96:99], v[200:203]\n\t" \
    "ds_read_b128 v[40:43], %[xp] offset:46080\n\t" \
    "v_mfma_f32_16x16x32_bf16 v[204:207], v[48:51], v[88:91], v[204:207]\n\t" \
    "s_waitcnt lgkmcnt(4)\n\t" \
    "v_mfma_f32_16x16x32_bf16 v[208:211], v[48:51], v[96:99], v[208:211]\n\t" \
    "v_mfma_f32_16x16x32_bf16 v[196:199], v[20:23], v[100:103], v[196:199]\n\t" \
    "ds_read_b128 v[44:47], %[xp] offset:47104\n\t" \
    "v_mfma_f32_16x16x32_bf16 v[200:203], v[20:23], v[108:111], v[200:203]\n\t" \
    "ds_read_b128 v[48:51], %[xp] offset:48128\n\t" \
    "v_mfma_f32_16x16x32_bf16 v[204:207], v[24:27], v[100:103], v[204:207]\n\t" \
    "s_waitcnt lgkmcnt(4)\n\t" \
    "v_mfma_f32_16x16x32_bf16 v[208:211], v[24:27], v[108:111], v[208:211]\n\t" \
    "buffer_load_dwordx4 %[l16], %[rs], %[a] offen offset:0 sc1 lds\n\t" \
    "v_mfma_f32_16x16x32_bf16 v[196:199], v[28:31], v[104:107], v[196:199]\n\t" \
    "ds_read_b128 v[20:23], %[xp] offset:49152\n\t" \
    "v_mfma_f32_16x16x32_bf16 v[200:203], v[28:31], v[112:115], v[200:203]\n\t" \
    "ds_read_b128 v[24:27], %[xp] offset:50176\n\t" \
    "v_mfma_f32_16x16x32_bf16 v[204:207], v[32:35], v[104:107], v[204:207]\n\t" \
    "s_waitcnt lgkmcnt(4)\n\t" \
    "v_mfma_f32_16x16x32_bf16 v[208:211], v[32:35], v[112:115], v[208:211]\n\t" \
    "buffer_load_dwordx4 %[l16], %[rs], %[a] offen offset:1024 sc1 lds\n\t" \
    "v_mfma_f32_16x16x32_bf16 v[196:199], v[36:39], v[116:119], v[196:199]\n\t" \
    "ds_read_b128 v[28:31], %[xp] offset:51200\n\t" \
    "v_mfma_f32_16x16x32_bf16 v[200:203], v[36:39], v[124:127], v[200:203]\n\t" \
    "ds_read_b128 v[32:35], %[xp] offset:52224\n\t" \
    "v_mfma_f32_16x16x32_bf16 v[204:207], v[40:43], v[116:119], v[204:207]\n\t" \
    "s_waitcnt lgkmcnt(4)\n\t" \
    "v_mfma_f32_16x16x32_bf16 v[208:211], v[40:43], v[124:127], v[208:211]\n\t" \
    "buffer_load_dwordx4 %[l16], %[rs], %[a] offen offset:2048 sc1 lds\n\t" \
    "v_mfma_f32_16x16x32_bf16 v[196:199], v[44:47], v[120:123], v[196:199]\n\t" \
    "ds_read_b128 v[36:39], %[xp] offset:53248\n\t" \
    "v_mfma_f32_16x16x32_bf16 v[200:203], v[44:47], v[128:131], v[200:203]\n\t" \
    "ds_read_b128 v[40:43], %[xp] offset:54272\n\t" \
    "v_mfma_f32_16x16x32_bf16 v[204:207], v[48:51], v[120:123], v[204:207]\n\t" \
    "s_waitcnt lgkmcnt(4)\n\t" \
    "v_mfma_f32_16x16x32_bf16 v[208:211], v[48:51], v[128:131], v[208:211]\n\t" \
    "buffer_load_dwordx4 %[l16], %[rs], %[a] offen offset:3072 sc1 lds\n\t" \
    "s_waitcnt vmcnt(4)\n\t" \
    "s_barrier\n\t" \
    "s_setprio 1\n\t" \
    "s_add_u32 m0, %[sd], 32768\n\t" \
    "s_add_u32 %[a], %[sx], 131072\n\t" \
    "s_add_u32 %[b], %[oo], 0x1000\n\t" \
    "s_cmp_lg_u32 %[t], 1\n\t" \
    "s_cselect_b64 %[em], -1, 0\n\t" \
    "v_mfma_f32_16x16x32_bf16 v[196:199], v[20:23], v[132:135], v[196:199]\n\t" \
    "ds_read_b128 v[44:47], %[xp] offset:55296\n\t" \
    "v_mfma_f32_16x16x32_bf16 v[200:203], v[20:23], v[140:143], v[200:203]\n\t" \
    "ds_read_b128 v[48:51], %[xp] offset:56320\n\t" \
    "v_mfma_f32_16x16x32_bf16 v[204:207], v[24:27], v[132:135], v[204:207]\n\t" \
    "ds_read_b128 v[52:55], %[pa] offset:0\n\t" \
    "s_waitcnt lgkmcnt(5)\n\t" \
    "v_mfma_f32_16x16x32_bf16 v[208:211], v[24:27], v[140:143], v[208:211]\n\t" \
    "buffer_load_dwordx4 %[l16], %[rs], %[a] offen offset:0 sc1 lds\n\t" \
    "v_mfma_f32_16x16x32_bf16 v[196:199], v[28:31], v[136:139], v[196:199]\n\t" \
    "ds_read_b128 v[20:23], %[xp] offset:57344\n\t" \
    "v_mfma_f32_16x16x32_bf16 v[200:203], v[28:31], v[144:147], v[200:203]\n\t" \
    "ds_read_b128 v[24:27], %[xp] offset:58368\n\t" \
    "v_mfma_f32_16x16x32_bf16 v[204:207], v[32:35], v[136:139], v[204:207]\n\t" \
    "ds_read_b128 v[56:59], %[pa] offset:1024\n\t" \
    "s_waitcnt lgkmcnt(6)\n\t" \
    "v_mfma_f32_16x16x32_bf16 v[208:211], v[32:35], v[144:147], v[208:211]\n\t" \
    "buffer_load_dwordx4 %[l16], %[rs], %[a] offen offset:1024 sc1 lds\n\t" \
    "v_mfma_f32_16x16x32_bf16 v[196:199], v[36:39], v[148:151], v[196:199]\n\t" \
    "ds_read_b128 v[28:31], %[xp] offset:59392\n\t" \
    "v_mfma_f32_16x16x32_bf16 v[200:203], v[36:39], v[156:159], v[200:203]\n\t" \
    "ds_read_b128 v[32:35], %[xp] offset:60416\n\t" \
    "v_mfma_f32_16x16x32_bf16 v[204:207], v[40:43], v[148:151], v[204:207]\n\t" \
    "ds_read_b128 v[60:63], %[pa] offset:2048\n\t" \
    "s_waitcnt lgkmcnt(7)\n\t" \
    "v_mfma_f32_16x16x32_bf16 v[208:211], v[40:43], v[156:159], v[208:211]\n\t" \
    "buffer_load_dwordx4 %[l16], %[rs], %[a] offen offset:2048 sc1 lds\n\t" \
    "v_mfma_f32_16x16x32_bf16 v[196:199], v[44:47], v[152:155], v[196:199]\n\t" \
    "ds_read_b128 v[36:39], %[xp] offset:61440\n\t" \
    "s_waitcnt lgkmcnt(7)\n\t" \
    "v_add_f32 v0, v0, v52\n\t" \
    "v_mfma_f32_16x16x32_bf16 v[200:203], v[44:47], v[160:163], v[200:203]\n\t" \
    "ds_read_b128 v[40:43], %[xp] offset:62464\n\t" \
    "v_add_f32 v1, v1, v53\n\t" \
    "v_mfma_f32_16x16x32_bf16 v[204:207], v[48:51], v[152:155], v[204:207]\n\t" \
    "ds_read_b128 v[64:67], %[pa] offset:3072\n\t" \
    "s_waitcnt lgkmcnt(7)\n\t" \
    "v_mfma_f32_16x16x32_bf16 v[208:211], v[48:51], v[160:163], v[208:211]\n\t" \
    "buffer_load_dwordx4 %[l16], %[rs], %[a] offen offset:3072 sc1 lds\n\t" \
    "v_add_f32 v2, v2, v54\n\t" \
    "v_add_f32 v3, v3, v55\n\t" \
    "v_mfma_f32_16x16x32_bf16 v[196:199], v[20:23], v[164:167], v[196:199]\n\t" \
    "ds_read_b128 v[44:47], %[xp] offset:63488\n\t" \
    "s_waitcnt lgkmcnt(7)\n\t" \
    "v_add_f32 v4, v4, v56\n\t" \
    "v_mfma_f32_16x16x32_bf16 v[200:203], v[20:23], v[172:175], v[200:203]\n\t" \
    "ds_read_b128 v[48:51], %[xp] offset:64512\n\t" \
    "v_add_f32 v5, v5, v57\n\t" \
    "v_mfma_f32_16x16x32_bf16 v[204:207], v[24:27], v[164:167], v[204:207]\n\t" \
    "v_add_f32 v6, v6, v58\n\t" \
    "s_waitcnt lgkmcnt(6)\n\t" \
    "v_mfma_f32_16x16x32_bf16 v[208:211], v[24:27], v[172:175], v[208:211]\n\t" \
    "v_add_f32 v7, v7, v59\n\t" \
    "s_waitcnt lgkmcnt(5)\n\t" \
    "v_add_f32 v8, v8, v60\n\t" \
    "v_add_f32 v9, v9, v61\n\t" \
    "v_mfma_f32_16x16x32_bf16 v[196:199], v[28:31], v[168:171], v[196:199]\n\t" \
    "ds_read_b128 v[20:23], %[xp] offset:0\n\t" \
    "v_add_f32 v10, v10, v62\n\t" \
    "v_mfma_f32_16x16x32_bf16 v[200:203], v[28:31], v[176:179], v[200:203]\n\t" \
    "ds_read_b128 v[24:27], %[xp] offset:1024\n\t" \
    "v_add_f32 v11, v11, v63\n\t" \
    "v_mfma_f32_16x16x32_bf16 v[204:207], v[32:35], v[168:171], v[204:207]\n\t" \
    "s_waitcnt lgkmcnt(4)\n\t" \
    "v_add_f32 v12, v12, v64\n\t" \
    "v_mfma_f32_16x16x32_bf16 v[208:211], v[32:35], v[176:179], v[208:211]\n\t" \
    "v_add_f32 v13, v13, v65\n\t" \
    "v_add_f32 v14, v14, v66\n\t" \
    "v_add_f32 v15, v15, v67\n\t" \
    "v_mfma_f32_16x16x32_bf16 v[196:199], v[36:39], v[180:183], v[196:199]\n\t" \
    "ds_read_b128 v[28:31], %[xp] offset:2048\n\t" \
    "v_cvt_pk_bf16_f32 v52, v0, v1\n\t" \
    "v_mfma_f32_16x16x32_bf16 v[200:203], v[36:39], v[188:191], v[200:203]\n\t" \
    "ds_read_b128 v[32:35], %[xp] offset:3072\n\t" \
    "v_cvt_pk_bf16_f32 v53, v2, v3\n\t" \
    "v_mfma_f32_16x16x32_bf16 v[204:207], v[40:43], v[180:183], v[204:207]\n\t" \
    "v_cvt_pk_bf16_f32 v54, v8, v9\n\t" \
    "s_waitcnt lgkmcnt(4)\n\t" \
    "v_mfma_f32_16x16x32_bf16 v[208:211], v[40:43], v[188:191], v[208:211]\n\t" \
    "v_cvt_pk_bf16_f32 v55, v10, v11\n\t" \
    "v_cvt_pk_bf16_f32 v56, v4, v5\n\t" \
    "v_cvt_pk_bf16_f32 v57, v6, v7\n\t" \
    "v_mfma_f32_16x16x32_bf16 v[196:199], v[44:47], v[184:187], v[196:199]\n\t" \
    "ds_read_b128 v[36:39], %[xp] offset:4096\n\t" \
    "v_cvt_pk_bf16_f32 v58, v12, v13\n\t" \
    "v_mfma_f32_16x16x32_bf16 v[200:203], v[44:47], v[192:195], v[200:203]\n\t" \
    "ds_read_b128 v[40:43], %[xp] offset:5120\n\t" \
    "v_cvt_pk_bf16_f32 v59, v14, v15\n\t" \
    "v_mfma_f32_16x16x32_bf16 v[204:207], v[48:51], v[184:187], v[204:207]\n\t" \
    "s_waitcnt lgkmcnt(4)\n\t" \
    "v_mfma_f32_16x16x32_bf16 v[208:211], v[48:51], v[192:195], v[208:211]\n\t" \
    "s_mov_b64 exec, %[em]\n\t" \
    "buffer_store_dwordx4 v[52:55], %[l16], %[rs], %[oo] offen sc1\n\t" \
    "buffer_store_dwordx4 v[56:59], %[l16], %[rs], %[oo] offen offset:1024 sc1\n\t" \
    "s_mov_b64 exec, -1\n\t" \
    "s_add_u32 %[sx], %[sx], 65536\n\t" \
    "s_add_u32 %[t], %[t], 1\n\t" \
    "s_add_u32 %[oo], %[oo], 262144\n\t" \
    "s_waitcnt vmcnt(6)\n\t" \
    "s_cmp_gt_u32 %[t], " STR(SQUICK) " + 1\n\t" \
    "s_cbranch_scc1 B1_%=\n\t" \
    "s_waitcnt vmcnt(0)\n\t" \
    "B1_%=:\n\t" \
    "s_barrier\n\t" \
    "s_cmp_lt_u32 %[t], %[te]\n\t" \
    "s_cbranch_scc1 L1_%=\n\t" \
    "s_cmp_eq_u32 %[fin], %[te]\n\t" \
    "s_cbranch_scc0 E_%=\n\t" \
    "s_nop 4\n\t" \
    "s_barrier\n\t" \
    "ds_read_b128 v[52:55], %[pa] offset:0\n\t" \
    "ds_read_b128 v[56:59], %[pa] offset:1024\n\t" \
    "ds_read_b128 v[60:63], %[pa] offset:2048\n\t" \
    "ds_read_b128 v[64:67], %[pa] offset:3072\n\t" \
    "s_add_u32 %[b], %[oo], 0x1000\n\t" \
    "s_waitcnt lgkmcnt(0)\n\t" \
    "v_add_f32 v196, v196, v52\n\t" \
    "v_add_f32 v197, v197, v53\n\t" \
    "v_add_f32 v198, v198, v54\n\t" \
    "v_add_f32 v199, v199, v55\n\t" \
    "v_add_f32 v200, v200, v56\n\t" \
    "v_add_f32 v201, v201, v57\n\t" \
    "v_add_f32 v202, v202, v58\n\t" \
    "v_add_f32 v203, v203, v59\n\t" \
    "v_add_f32 v204, v204, v60\n\t" \
    "v_add_f32 v205, v205, v61\n\t" \
    "v_add_f32 v206, v206, v62\n\t" \
    "v_add_f32 v207, v207, v63\n\t" \
    "v_add_f32 v208, v208, v64\n\t" \
    "v_add_f32 v209, v209, v65\n\t" \
    "v_add_f32 v210, v210, v66\n\t" \
    "v_add_f32 v211, v211, v67\n\t" \
    "v_cvt_pk_bf16_f32 v52, v196, v197\n\t" \
    "v_cvt_pk_bf16_f32 v53, v198, v199\n\t" \
    "v_cvt_pk_bf16_f32 v54, v204, v205\n\t" \
    "v_cvt_pk_bf16_f32 v55, v206, v207\n\t" \
    "v_cvt_pk_bf16_f32 v56, v200, v201\n\t" \
    "v_cvt_pk_bf16_f32 v57, v202, v203\n\t" \
    "v_cvt_pk_bf16_f32 v58, v208, v209\n\t" \
    "v_cvt_pk_bf16_f32 v59, v210, v211\n\t" \
    "buffer_store_dwordx4 v[52:55], %[l16], %[rs], %[oo] offen sc1\n\t" \
    "buffer_store_dwordx4 v[56:59], %[l16], %[rs], %[oo] offen offset:1024 sc1\n\t" \
    "E_%=:\n\t" \
    "s_setprio 0\n\t" \
    "s_waitcnt lgkmcnt(0)\n\t" \
    "s_nop 7\n\t" \
    : [t] "+s"(t), [sx] "+s"(sx), [oo] "+s"(oo), [a] "=&s"(tmp), [b] "=&s"(tmp2), [em] "=&s"(em), "+{v[20:27]}"(xf[0]), "+{v[28:35]}"(xf[1]), "+{v[36:43]}"(xf[2]), "+{v[196:203]}"(ac[0]), "+{v[204:211]}"(ac[1]) \
    : [te] "s"(TE_), [fin] "s"(FIN_), [kh] "s"(kh), [sd] "s"(sd), [rs] "s"(rs), [l16] "v"(l16), [xp] "v"(xp), [pa] "v"(pa), [fm] "s"(fm), [pm] "s"(pm), [fo] "s"(fo), [po] "s"(po), [pd] "s"(pd), [tag] "s"(P.tag), "{v[68:75]}"(w[0][0]), "{v[76:83]}"(w[0][1]), "{v[84:91]}"(w[1][0]), "{v[92:99]}"(w[1][1]), "{v[100:107]}"(w[2][0]), "{v[108:115]}"(w[2][1]), "{v[116:123]}"(w[3][0]), "{v[124:131]}"(w[3][1]), "{v[132:139]}"(w[4][0]), "{v[140:147]}"(w[4][1]), "{v[148:155]}"(w[5][0]), "{v[156:163]}"(w[5][1]), "{v[164:171]}"(w[6][0]), "{v[172:179]}"(w[6][1]), "{v[180:187]}"(w[7][0]), "{v[188:195]}"(w[7][1]) \
    : "memory", "scc", "v0", "v1", "v2", "v3", "v4", "v5", "v6", "v7", "v8", "v9", "v10", "v11", "v12", "v13", "v14", "v15", "v44", "v45", "v46", "v47", "v48", "v49", "v50", "v51", "v52", "v53", "v54", "v55", "v56", "v57", "v58", "v59", "v60", "v61", "v62", "v63", "v64", "v65", "v66", "v67")

#define S_LOOP_X(TE_, FIN_) \
  asm volatile( \
    "s_cmp_eq_u32 %[kh], 0\n\t" \
    "s_cbranch_scc0 L1_%=\n\t" \
    "L0_%=:\n\t" \
    "s_setprio 0\n\t" \
    "s_nop 3\n\t" \
    "ds_write_b128 %[pa], v[196:199] offset:0\n\t" \
    "ds_write_b128 %[pa], v[200:203] offset:1024\n\t" \
    "ds_write_b128 %[pa], v[204:207] offset:2048\n\t" \
    "ds_write_b128 %[pa], v[208:211] offset:3072\n\t" \
    "s_sub_u32 %[a], %[t], 1\n\t" \
    "v_mov_b32 v53, %[tag]\n\t" \
    "v_mov_b32 v52, %[a]\n\t" \
    "s_sub_u32 %[b], %[t], 2\n\t" \
    "s_cmp_lt_u32 %[b], " STR(SQUICK) "\n\t" \
    "s_cselect_b64 exec, %[fm], 0\n\t" \
    "buffer_store_dwordx2 v[52:53], %[l16], %[rs], %[fo] offen sc1\n\t" \
    "s_mov_b64 exec, -1\n\t" \
    "s_add_u32 m0, %[sd], 49152\n\t" \
    "s_add_u32 %[a], %[sx], 278528\n\t" \
    "s_waitcnt lgkmcnt(0)\n\t" \
    "buffer_load_dwordx4 %[l16], %[rs], %[a] offen offset:0 sc1 lds\n\t" \
    "buffer_load_dwordx4 %[l16], %[rs], %[a] offen offset:1024 sc1 lds\n\t" \
    "buffer_load_dwordx4 %[l16], %[rs], %[a] offen offset:2048 sc1 lds\n\t" \
    "buffer_load_dwordx4 %[l16], %[rs], %[a] offen offset:3072 sc1 lds\n\t" \
    "v_mfma_f32_16x16x32_bf16 v[0:3], v[68:71], v[20:23], 0\n\t" \
    "ds_read_b128 v[44:47], %[xp] offset:6144\n\t" \
    "v_mfma_f32_16x16x32_bf16 v[4:7], v[76:79], v[20:23], 0\n\t" \
    "ds_read_b128 v[48:51], %[xp] offset:7168\n\t" \
    "v_mfma_f32_16x16x32_bf16 v[8:11], v[68:71], v[24:27], 0\n\t" \
    "v_mfma_f32_16x16x32_bf16 v[12:15], v[76:79], v[24:27], 0\n\t" \
    "v_mfma_f32_16x16x32_bf16 v[0:3], v[72:75], v[28:31], v[0:3]\n\t" \
    "ds_read_b128 v[20:23], %[xp] offset:8192\n\t" \
    "v_mfma_f32_16x16x32_bf16 v[4:7], v[80:83], v[28:31], v[4:7]\n\t" \
    "ds_read_b128 v[24:27], %[xp] offset:9216\n\t" \
    "v_mfma_f32_16x16x32_bf16 v[8:11], v[72:75], v[32:35], v[8:11]\n\t" \
    "v_mfma_f32_16x16x32_bf16 v[12:15], v[80:83], v[32:35], v[12:15]\n\t" \
    "v_mfma_f32_16x16x32_bf16 v[0:3], v[84:87], v[36:39], v[0:3]\n\t" \
    "ds_read_b128 v[28:31], %[xp] offset:10240\n\t" \
    "v_mfma_f32_16x16x32_bf16 v[4:7], v[92:95], v[36:39], v[4:7]\n\t" \
    "ds_read_b128 v[32:35], %[xp] offset:11264\n\t" \
    "v_mfma_f32_16x16x32_bf16 v[8:11], v[84:87], v[40:43], v[8:11]\n\t" \
    "s_waitcnt lgkmcnt(4)\n\t" \
    "v_mfma_f32_16x16x32_bf16 v[12:15], v[92:95], v[40:43], v[12:15]\n\t" \
    "v_mfma_f32_16x16x32_bf16 v[0:3], v[88:91], v[44:47], v[0:3]\n\t" \
    "ds_read_b128 v[36:39], %[xp] offset:12288\n\t" \
    "v_mfma_f32_16x16x32_bf16 v[4:7], v[96:99], v[44:47], v[4:7]\n\t" \
    "ds_read_b128 v[40:43], %[xp] offset:13312\n\t" \
    "v_mfma_f32_16x16x32_bf16 v[8:11], v[88:91], v[48:51], v[8:11]\n\t" \
    "s_waitcnt lgkmcnt(4)\n\t" \
    "v_mfma_f32_16x16x32_bf16 v[12:15], v[96:99], v[48:51], v[12:15]\n\t" \
    "v_mfma_f32_16x16x32_bf16 v[0:3], v[100:103], v[20:23], v[0:3]\n\t" \
    "ds_read_b128 v[44:47], %[xp] offset:14336\n\t" \
    "v_mfma_f32_16x16x32_bf16 v[4:7], v[108:111], v[20:23], v[4:7]\n\t" \
    "ds_read_b128 v[48:51], %[xp] offset:15360\n\t" \
    "v_mfma_f32_16x16x32_bf16 v[8:11], v[100:103], v[24:27], v[8:11]\n\t" \
    "s_waitcnt lgkmcnt(4)\n\t" \
    "v_mfma_f32_16x16x32_bf16 v[12:15], v[108:111], v[24:27], v[12:15]\n\t" \
    "v_mfma_f32_16x16x32_bf16 v[0:3], v[104:107], v[28:31], v[0:3]\n\t" \
    "ds_read_b128 v[20:23], %[xp] offset:16384\n\t" \
    "v_mfma_f32_16x16x32_bf16 v[4:7], v[112:115], v[28:31], v[4:7]\n\t" \
    "ds_read_b128 v[24:27], %[xp] offset:17408\n\t" \
    "v_mfma_f32_16x16x32_bf16 v[8:11], v[104:107], v[32:35], v[8:11]\n\t" \
    "s_waitcnt lgkmcnt(4)\n\t" \
    "v_mfma_f32_16x16x32_bf16 v[12:15], v[112:115], v[32:35], v[12:15]\n\t" \
    "v_mfma_f32_16x16x32_bf16 v[0:3], v[116:119], v[36:39], v[0:3]\n\t" \
    "ds_read_b128 v[28:31], %[xp] offset:18432\n\t" \
    "v_mfma_f32_16x16x32_bf16 v[4:7], v[124:127], v[36:39], v[4:7]\n\t" \
    "ds_read_b128 v[32:35], %[xp] offset:19456\n\t" \
    "v_mfma_f32_16x16x32_bf16 v[8:11], v[116:119], v[40:43], v[8:11]\n\t" \
    "s_waitcnt lgkmcnt(4)\n\t" \
    "v_mfma_f32_16x16x32_bf16 v[12:15], v[124:127], v[40:43], v[12:15]\n\t" \
    "v_mfma_f32_16x16x32_bf16 v[0:3], v[120:123], v[44:47], v[0:3]\n\t" \
    "ds_read_b128 v[36:39], %[xp] offset:20480\n\t" \
    "v_mfma_f32_16x16x32_bf16 v[4:7], v[128:131], v[44:47], v[4:7]\n\t" \
    "ds_read_b128 v[40:43], %[xp] offset:21504\n\t" \
    "v_mfma_f32_16x16x32_bf16 v[8:11], v[120:123], v[48:51], v[8:11]\n\t" \
    "s_waitcnt lgkmcnt(4)\n\t" \
    "v_mfma_f32_16x16x32_bf16 v[12:15], v[128:131], v[48:51], v[12:15]\n\t" \
    "s_waitcnt vmcnt(4)\n\t" \
    "s_barrier\n\t" \
    "s_setprio 0\n\t" \
    "s_sub_u32 %[a], %[t], 1\n\t" \
    "v_mov_b32 v53, %[tag]\n\t" \
    "v_mov_b32 v52, %[a]\n\t" \
    "s_cmp_gt_u32 %[t], 1\n\t" \
    "s_cselect_b64 exec, %[fm], 0\n\t" \
    "buffer_store_dwordx2 v[52:53], %[l16], %[rs], %[fo] offen sc1\n\t" \
    "s_mov_b64 exec, %[pm]\n\t" \
    "s_mov_b32 m0, %[pd]\n\t" \
    "s_add_u32 %[a], %[sx], %[po]\n\t" \
    "buffer_load_dwordx4 %[l16], %[rs], %[a] offen sc1 lds\n\t" \
    "s_mov_b64 exec, -1\n\t" \
    "s_add_u32 m0, %[sd], 0\n\t" \
    "s_add_u32 %[a], %[sx], 524288\n\t" \
    "buffer_load_dwordx4 %[l16], %[rs], %[a] offen offset:0 sc1 lds\n\t" \
    "buffer_load_dwordx4 %[l16], %[rs], %[a] offen offset:1024 sc1 lds\n\t" \
    "buffer_load_dwordx4 %[l16], %[rs], %[a] offen offset:2048 sc1 lds\n\t" \
    "buffer_load_dwordx4 %[l16], %[rs], %[a] offen offset:3072 sc1 lds\n\t" \
    "v_mfma_f32_16x16x32_bf16 v[0:3], v[132:135], v[20:23], v[0:3]\n\t" \
    "ds_read_b128 v[44:47], %[xp] offset:22528\n\t" \
    "v_mfma_f32_16x16x32_bf16 v[4:7], v[140:143], v[20:23], v[4:7]\n\t" \
    "ds_read_b128 v[48:51], %[xp] offset:23552\n\t" \
    "v_mfma_f32_16x16x32_bf16 v[8:11], v[132:135], v[24:27], v[8:11]\n\t" \
    "s_waitcnt lgkmcnt(4)\n\t" \
    "v_mfma_f32_16x16x32_bf16 v[12:15], v[140:143], v[24:27], v[12:15]\n\t" \
    "v_mfma_f32_16x16x32_bf16 v[0:3], v[136:139], v[28:31], v[0:3]\n\t" \
    "ds_read_b128 v[20:23], %[xp] offset:24576\n\t" \
    "v_mfma_f32_16x16x32_bf16 v[4:7], v[144:147], v[28:31], v[4:7]\n\t" \
    "ds_read_b128 v[24:27], %[xp] offset:25600\n\t" \
    "v_mfma_f32_16x16x32_bf16 v[8:11], v[136:139], v[32:35], v[8:11]\n\t" \
    "s_waitcnt lgkmcnt(4)\n\t" \
    "v_mfma_f32_16x16x32_bf16 v[12:15], v[144:147], v[32:35], v[12:15]\n\t" \
    "v_mfma_f32_16x16x32_bf16 v[0:3], v[148:151], v[36:39], v[0:3]\n\t" \
    "ds_read_b128 v[28:31], %[xp] offset:26624\n\t" \
    "v_mfma_f32_16x16x32_bf16 v[4:7], v[156:159], v[36:39], v[4:7]\n\t" \
    "ds_read_b128 v[32:35], %[xp] offset:27648\n\t" \
    "v_mfma_f32_16x16x32_bf16 v[8:11], v[148:151], v[40:43], v[8:11]\n\t" \
    "s_waitcnt lgkmcnt(4)\n\t" \
    "v_mfma_f32_16x16x32_bf16 v[12:15], v[156:159], v[40:43], v[12:15]\n\t" \
    "v_mfma_f32_16x16x32_bf16 v[0:3], v[152:155], v[44:47], v[0:3]\n\t" \
    "ds_read_b128 v[36:39], %[xp] offset:28672\n\t" \
    "v_mfma_f32_16x16x32_bf16 v[4:7], v[160:163], v[44:47], v[4:7]\n\t" \
    "ds_read_b128 v[40:43], %[xp] offset:29696\n\t" \
    "v_mfma_f32_16x16x32_bf16 v[8:11], v[152:155], v[48:51], v[8:11]\n\t" \
    "s_waitcnt lgkmcnt(4)\n\t" \
    "v_mfma_f32_16x16x32_bf16 v[12:15], v[160:163], v[48:51], v[12:15]\n\t" \
    "v_mfma_f32_16x16x32_bf16 v[0:3], v[164:167], v[20:23], v[0:3]\n\t" \
    "ds_read_b128 v[44:47], %[xp] offset:30720\n\t" \
    "v_mfma_f32_16x16x32_bf16 v[4:7], v[172:175], v[20:23], v[4:7]\n\t" \
    "ds_read_b128 v[48:51], %[xp] offset:31744\n\t" \
    "v_mfma_f32_16x16x32_bf16 v[8:11], v[164:167], v[24:27], v[8:11]\n\t" \
    "s_waitcnt lgkmcnt(4)\n\t" \
    "v_mfma_f32_16x16x32_bf16 v[12:15], v[172:175], v[24:27], v[12:15]\n\t" \
    "v_mfma_f32_16x16x32_bf16 v[0:3], v[168:171], v[28:31], v[0:3]\n\t" \
    "ds_read_b128 v[20:23], %[xp] offset:32768\n\t" \
    "v_mfma_f32_16x16x32_bf16 v[4:7], v[176:179], v[28:31], v[4:7]\n\t" \
    "ds_read_b128 v[24:27], %[xp] offset:33792\n\t" \
    "v_mfma_f32_16x16x32_bf16 v[8:11], v[168:171], v[32:35], v[8:11]\n\t" \
    "s_waitcnt lgkmcnt(4)\n\t" \
    "v_mfma_f32_16x16x32_bf16 v[12:15], v[176:179], v[32:35], v[12:15]\n\t" \
    "v_mfma_f32_16x16x32_bf16 v[0:3], v[180:183], v[36:39], v[0:3]\n\t" \
    "ds_read_b128 v[28:31], %[xp] offset:34816\n\t" \
    "v_mfma_f32_16x16x32_bf16 v[4:7], v[188:191], v[36:39], v[4:7]\n\t" \
    "ds_read_b128 v[32:35], %[xp] offset:35840\n\t" \
    "v_mfma_f32_16x16x32_bf16 v[8:11], v[180:183], v[40:43], v[8:11]\n\t" \
    "s_waitcnt lgkmcnt(4)\n\t" \
    "v_mfma_f32_16x16x32_bf16 v[12:15], v[188:191], v[40:43], v[12:15]\n\t" \
    "v_mfma_f32_16x16x32_bf16 v[0:3], v[184:187], v[44:47], v[0:3]\n\t" \
    "ds_read_b128 v[36:39], %[xp] offset:36864\n\t" \
    "v_mfma_f32_16x16x32_bf16 v[4:7], v[192:195], v[44:47], v[4:7]\n\t" \
    "ds_read_b128 v[40:43], %[xp] offset:37888\n\t" \
    "v_mfma_f32_16x16x32_bf16 v[8:11], v[184:187], v[48:51], v[8:11]\n\t" \
    "s_waitcnt lgkmcnt(4)\n\t" \
    "v_mfma_f32_16x16x32_bf16 v[12:15], v[192:195], v[48:51], v[12:15]\n\t" \
    "s_add_u32 %[sx], %[sx], 262144\n\t" \
    "s_add_u32 %[t], %[t], 1\n\t" \
    "s_waitcnt vmcnt(4)\n\t" \
    "s_barrier\n\t" \
    "s_setprio 0\n\t" \
    "s_nop 3\n\t" \
    "ds_write_b128 %[pa], v[0:3] offset:0\n\t" \
    "ds_write_b128 %[pa], v[4:7] offset:1024\n\t" \
    "ds_write_b128 %[pa], v[8:11] offset:2048\n\t" \
    "ds_write_b128 %[pa], v[12:15] offset:3072\n\t" \
    "s_sub_u32 %[a], %[t], 1\n\t" \
    "v_mov_b32 v53, %[tag]\n\t" \
    "v_mov_b32 v52, %[a]\n\t" \
    "s_sub_u32 %[b], %[t], 2\n\t" \
    "s_cmp_lt_u32 %[b], " STR(SQUICK) "\n\t" \
    "s_cselect_b64 exec, %[fm], 0\n\t" \
    "buffer_store_dwordx2 v[52:53], %[l16], %[rs], %[fo] offen sc1\n\t" \
    "s_mov_b64 exec, -1\n\t" \
    "s_add_u32 m0, %[sd], 16384\n\t" \
    "s_add_u32 %[a], %[sx], 278528\n\t" \
    "s_waitcnt lgkmcnt(0)\n\t" \
    "buffer_load_dwordx4 %[l16], %[rs], %[a] offen offset:0 sc1 lds\n\t" \
    "buffer_load_dwordx4 %[l16], %[rs], %[a] offen offset:1024 sc1 lds\n\t" \
    "buffer_load_dwordx4 %[l16], %[rs], %[a] offen offset:2048 sc1 lds\n\t" \
    "buffer_load_dwordx4 %[l16], %[rs], %[a] offen offset:3072 sc1 lds\n\t" \
    "v_mfma_f32_16x16x32_bf16 v[196:199], v[68:71], v[20:23], 0\n\t" \
    "ds_read_b128 v[44:47], %[xp] offset:38912\n\t" \
    "v_mfma_f32_16x16x32_bf16 v[200:203], v[76:79], v[20:23], 0\n\t" \
    "ds_read_b128 v[48:51], %[xp] offset:39936\n\t" \
    "v_mfma_f32_16x16x32_bf16 v[204:207], v[68:71], v[24:27], 0\n\t" \
    "v_mfma_f32_16x16x32_bf16 v[208:211], v[76:79], v[24:27], 0\n\t" \
    "v_mfma_f32_16x16x32_bf16 v[196:199], v[72:75], v[28:31], v[196:199]\n\t" \
    "ds_read_b128 v[20:23], %[xp] offset:40960\n\t" \
    "v_mfma_f32_16x16x32_bf16 v[200:203], v[80:83], v[28:31], v[200:203]\n\t" \
    "ds_read_b128 v[24:27], %[xp] offset:41984\n\t" \
    "v_mfma_f32_16x16x32_bf16 v[204:207], v[72:75], v[32:35], v[204:207]\n\t" \
    "v_mfma_f32_16x16x32_bf16 v[208:211], v[80:83], v[32:35], v[208:211]\n\t" \
    "v_mfma_f32_16x16x32_bf16 v[196:199], v[84:87], v[36:39], v[196:199]\n\t" \
    "ds_read_b128 v[28:31], %[xp] offset:43008\n\t" \
    "v_mfma_f32_16x16x32_bf16 v[200:203], v[92:95], v[36:39], v[200:203]\n\t" \
    "ds_read_b128 v[32:35], %[xp] offset:44032\n\t" \
    "v_mfma_f32_16x16x32_bf16 v[204:207], v[84:87], v[40:43], v[204:207]\n\t" \
    "s_waitcnt lgkmcnt(4)\n\t" \
    "v_mfma_f32_16x16x32_bf16 v[208:211], v[92:95], v[40:43], v[208:211]\n\t" \
    "v_mfma_f32_16x16x32_bf16 v[196:199], v[88:91], v[44:47], v[196:199]\n\t" \
    "ds_read_b128 v[36:39], %[xp] offset:45056\n\t" \
    "v_mfma_f32_16x16x32_bf16 v[200:203], v[96:99], v[44:47], v[200:203]\n\t" \
    "ds_read_b128 v[40:43], %[xp] offset:46080\n\t" \
    "v_mfma_f32_16x16x32_bf16 v[204:207], v[88:91], v[48:51], v[204:207]\n\t" \
    "s_waitcnt lgkmcnt(4)\n\t" \
    "v_mfma_f32_16x16x32_bf16 v[208:211], v[96:99], v[48:51], v[208:211]\n\t" \
    "v_mfma_f32_16x16x32_bf16 v[196:199], v[100:103], v[20:23], v[196:199]\n\t" \
    "ds_read_b128 v[44:47], %[xp] offset:47104\n\t" \
    "v_mfma_f32_16x16x32_bf16 v[200:203], v[108:111], v[20:23], v[200:203]\n\t" \
    "ds_read_b128 v[48:51], %[xp] offset:48128\n\t" \
    "v_mfma_f32_16x16x32_bf16 v[204:207], v[100:103], v[24:27], v[204:207]\n\t" \
    "s_waitcnt lgkmcnt(4)\n\t" \
    "v_mfma_f32_16x16x32_bf16 v[208:211], v[108:111], v[24:27], v[208:211]\n\t" \
    "v_mfma_f32_16x16x32_bf16 v[196:199], v[104:107], v[28:31], v[196:199]\n\t" \
    "ds_read_b128 v[20:23], %[xp] offset:49152\n\t" \
    "v_mfma_f32_16x16x32_bf16 v[200:203], v[112:115], v[28:31], v[200:203]\n\t" \
    "ds_read_b128 v[24:27], %[xp] offset:50176\n\t" \
    "v_mfma_f32_16x16x32_bf16 v[204:207], v[104:107], v[32:35], v[204:207]\n\t" \
    "s_waitcnt lgkmcnt(4)\n\t" \
    "v_mfma_f32_16x16x32_bf16 v[208:211], v[112:115], v[32:35], v[208:211]\n\t" \
    "v_mfma_f32_16x16x32_bf16 v[196:199], v[116:119], v[36:39], v[196:199]\n\t" \
    "ds_read_b128 v[28:31], %[xp] offset:51200\n\t" \
    "v_mfma_f32_16x16x32_bf16 v[200:203], v[124:127], v[36:39], v[200:203]\n\t" \
    "ds_read_b128 v[32:35], %[xp] offset:52224\n\t" \
    "v_mfma_f32_16x16x32_bf16 v[204:207], v[116:119], v[40:43], v[204:207]\n\t" \
    "s_waitcnt lgkmcnt(4)\n\t" \
    "v_mfma_f32_16x16x32_bf16 v[208:211], v[124:127], v[40:43], v[208:211]\n\t" \
    "v_mfma_f32_16x16x32_bf16 v[196:199], v[120:123], v[44:47], v[196:199]\n\t" \
    "ds_read_b128 v[36:39], %[xp] offset:53248\n\t" \
    "v_mfma_f32_16x16x32_bf16 v[200:203], v[128:131], v[44:47], v[200:203]\n\t" \
    "ds_read_b128 v[40:43], %[xp] offset:54272\n\t" \
    "v_mfma_f32_16x16x32_bf16 v[204:207], v[120:123], v[48:51], v[204:207]\n\t" \
    "s_waitcnt lgkmcnt(4)\n\t" \
    "v_mfma_f32_16x16x32_bf16 v[208:211], v[128:131], v[48:51], v[208:211]\n\t" \
    "s_waitcnt vmcnt(4)\n\t" \
    "s_barrier\n\t" \
    "s_setprio 0\n\t" \
    "s_sub_u32 %[a], %[t], 1\n\t" \
    "v_mov_b32 v53, %[tag]\n\t" \
    "v_mov_b32 v52, %[a]\n\t" \
    "s_cmp_gt_u32 %[t], 1\n\t" \
    "s_cselect_b64 exec, %[fm], 0\n\t" \
    "buffer_store_dwordx2 v[52:53], %[l16], %[rs], %[fo] offen sc1\n\t" \
    "s_mov_b64 exec, %[pm]\n\t" \
    "s_mov_b32 m0, %[pd]\n\t" \
    "s_add_u32 %[a], %[sx], %[po]\n\t" \
    "buffer_load_dwordx4 %[l16], %[rs], %[a] offen sc1 lds\n\t" \
    "s_mov_b64 exec, -1\n\t" \
    "s_add_u32 m0, %[sd], 32768\n\t" \
    "s_add_u32 %[a], %[sx], 524288\n\t" \
    "buffer_load_dwordx4 %[l16], %[rs], %[a] offen offset:0 sc1 lds\n\t" \
    "buffer_load_dwordx4 %[l16], %[rs], %[a] offen offset:1024 sc1 lds\n\t" \
    "buffer_load_dwordx4 %[l16], %[rs], %[a] offen offset:2048 sc1 lds\n\t" \
    "buffer_load_dwordx4 %[l16], %[rs], %[a] offen offset:3072 sc1 lds\n\t" \
    "v_mfma_f32_16x16x32_bf16 v[196:199], v[132:135], v[20:23], v[196:199]\n\t" \
    "ds_read_b128 v[44:47], %[xp] offset:55296\n\t" \
    "v_mfma_f32_16x16x32_bf16 v[200:203], v[140:143], v[20:23], v[200:203]\n\t" \
    "ds_read_b128 v[48:51], %[xp] offset:56320\n\t" \
    "v_mfma_f32_16x16x32_bf16 v[204:207], v[132:135], v[24:27], v[204:207]\n\t" \
    "s_waitcnt lgkmcnt(4)\n\t" \
    "v_mfma_f32_16x16x32_bf16 v[208:211], v[140:143], v[24:27], v[208:211]\n\t" \
    "v_mfma_f32_16x16x32_bf16 v[196:199], v[136:139], v[28:31], v[196:199]\n\t" \
    "ds_read_b128 v[20:23], %[xp] offset:57344\n\t" \
    "v_mfma_f32_16x16x32_bf16 v[200:203], v[144:147], v[28:31], v[200:203]\n\t" \
    "ds_read_b128 v[24:27], %[xp] offset:58368\n\t" \
    "v_mfma_f32_16x16x32_bf16 v[204:207], v[136:139], v[32:35], v[204:207]\n\t" \
    "s_waitcnt lgkmcnt(4)\n\t" \
    "v_mfma_f32_16x16x32_bf16 v[208:211], v[144:147], v[32:35], v[208:211]\n\t" \
    "v_mfma_f32_16x16x32_bf16 v[196:199], v[148:151], v[36:39], v[196:199]\n\t" \
    "ds_read_b128 v[28:31], %[xp] offset:59392\n\t" \
    "v_mfma_f32_16x16x32_bf16 v[200:203], v[156:159], v[36:39], v[200:203]\n\t" \
    "ds_read_b128 v[32:35], %[xp] offset:60416\n\t" \
    "v_mfma_f32_16x16x32_bf16 v[204:207], v[148:151], v[40:43], v[204:207]\n\t" \
    "s_waitcnt lgkmcnt(4)\n\t" \
    "v_mfma_f32_16x16x32_bf16 v[208:211], v[156:159], v[40:43], v[208:211]\n\t" \
    "v_mfma_f32_16x16x32_bf16 v[196:199], v[152:155], v[44:47], v[196:199]\n\t" \
    "ds_read_b128 v[36:39], %[xp] offset:61440\n\t" \
    "v_mfma_f32_16x16x32_bf16 v[200:203], v[160:163], v[44:47], v[200:203]\n\t" \
    "ds_read_b128 v[40:43], %[xp] offset:62464\n\t" \
    "v_mfma_f32_16x16x32_bf16 v[204:207], v[152:155], v[48:51], v[204:207]\n\t" \
    "s_waitcnt lgkmcnt(4)\n\t" \
    "v_mfma_f32_16x16x32_bf16 v[208:211], v[160:163], v[48:51], v[208:211]\n\t" \
    "v_mfma_f32_16x16x32_bf16 v[196:199], v[164:167], v[20:23], v[196:199]\n\t" \
    "ds_read_b128 v[44:47], %[xp] offset:63488\n\t" \
    "v_mfma_f32_16x16x32_bf16 v[200:203], v[172:175], v[20:23], v[200:203]\n\t" \
    "ds_read_b128 v[48:51], %[xp] offset:64512\n\t" \
    "v_mfma_f32_16x16x32_bf16 v[204:207], v[164:167], v[24:27], v[204:207]\n\t" \
    "s_waitcnt lgkmcnt(4)\n\t" \
    "v_mfma_f32_16x16x32_bf16 v[208:211], v[172:175], v[24:27], v[208:211]\n\t" \
    "v_mfma_f32_16x16x32_bf16 v[196:199], v[168:171], v[28:31], v[196:199]\n\t" \
    "ds_read_b128 v[20:23], %[xp] offset:0\n\t" \
    "v_mfma_f32_16x16x32_bf16 v[200:203], v[176:179], v[28:31], v[200:203]\n\t" \
    "ds_read_b128 v[24:27], %[xp] offset:1024\n\t" \
    "v_mfma_f32_16x16x32_bf16 v[204:207], v[168:171], v[32:35], v[204:207]\n\t" \
    "s_waitcnt lgkmcnt(4)\n\t" \
    "v_mfma_f32_16x16x32_bf16 v[208:211], v[176:179], v[32:35], v[208:211]\n\t" \
    "v_mfma_f32_16x16x32_bf16 v[196:199], v[180:183], v[36:39], v[196:199]\n\t" \
    "ds_read_b128 v[28:31], %[xp] offset:2048\n\t" \
    "v_mfma_f32_16x16x32_bf16 v[200:203], v[188:191], v[36:39], v[200:203]\n\t" \
    "ds_read_b128 v[32:35], %[xp] offset:3072\n\t" \
    "v_mfma_f32_16x16x32_bf16 v[204:207], v[180:183], v[40:43], v[204:207]\n\t" \
    "s_waitcnt lgkmcnt(4)\n\t" \
    "v_mfma_f32_16x16x32_bf16 v[208:211], v[188:191], v[40:43], v[208:211]\n\t" \
    "v_mfma_f32_16x16x32_bf16 v[196:199], v[184:187], v[44:47], v[196:199]\n\t" \
    "ds_read_b128 v[36:39], %[xp] offset:4096\n\t" \
    "v_mfma_f32_16x16x32_bf16 v[200:203], v[192:195], v[44:47], v[200:203]\n\t" \
    "ds_read_b128 v[40:43], %[xp] offset:5120\n\t" \
    "v_mfma_f32_16x16x32_bf16 v[204:207], v[184:187], v[48:51], v[204:207]\n\t" \
    "s_waitcnt lgkmcnt(4)\n\t" \
    "v_mfma_f32_16x16x32_bf16 v[208:211], v[192:195], v[48:51], v[208:211]\n\t" \
    "s_add_u32 %[sx], %[sx], 262144\n\t" \
    "s_add_u32 %[t], %[t], 1\n\t" \
    "s_waitcnt vmcnt(4)\n\t" \
    "s_barrier\n\t" \
    "s_cmp_lt_u32 %[t], %[te]\n\t" \
    "s_cbranch_scc1 L0_%=\n\t" \
    "s_cmp_eq_u32 %[fin], %[te]\n\t" \
    "s_cbranch_scc0 E_%=\n\t" \
    "s_nop 4\n\t" \
    "ds_write_b128 %[pa], v[196:199] offset:0\n\t" \
    "ds_write_b128 %[pa], v[200:203] offset:1024\n\t" \
    "ds_write_b128 %[pa], v[204:207] offset:2048\n\t" \
    "ds_write_b128 %[pa], v[208:211] offset:3072\n\t" \
    "s_waitcnt lgkmcnt(0)\n\t" \
    "s_barrier\n\t" \
    "s_branch E_%=\n\t" \
    "L1_%=:\n\t" \
    "s_setprio 1\n\t" \
    "s_add_u32 m0, %[sd], 49152\n\t" \
    "s_add_u32 %[a], %[sx], 278528\n\t" \
    "v_mfma_f32_16x16x32_bf16 v[0:3], v[68:71], v[20:23], 0\n\t" \
    "ds_read_b128 v[44:47], %[xp] offset:6144\n\t" \
    "v_mfma_f32_16x16x32_bf16 v[4:7], v[76:79], v[20:23], 0\n\t" \
    "ds_read_b128 v[48:51], %[xp] offset:7168\n\t" \
    "v_mfma_f32_16x16x32_bf16 v[8:11], v[68:71], v[24:27], 0\n\t" \
    "s_waitcnt lgkmcnt(4)\n\t" \
    "v_mfma_f32_16x16x32_bf16 v[12:15], v[76:79], v[24:27], 0\n\t" \
    "v_mfma_f32_16x16x32_bf16 v[0:3], v[72:75], v[28:31], v[0:3]\n\t" \
    "ds_read_b128 v[20:23], %[xp] offset:8192\n\t" \
    "v_mfma_f32_16x16x32_bf16 v[4:7], v[80:83], v[28:31], v[4:7]\n\t" \
    "ds_read_b128 v[24:27], %[xp] offset:9216\n\t" \
    "v_mfma_f32_16x16x32_bf16 v[8:11], v[72:75], v[32:35], v[8:11]\n\t" \
    "s_waitcnt lgkmcnt(4)\n\t" \
    "v_mfma_f32_16x16x32_bf16 v[12:15], v[80:83], v[32:35], v[12:15]\n\t" \
    "v_mfma_f32_16x16x32_bf16 v[0:3], v[84:87], v[36:39], v[0:3]\n\t" \
    "ds_read_b128 v[28:31], %[xp] offset:10240\n\t" \
    "v_mfma_f32_16x16x32_bf16 v[4:7], v[92:95], v[36:39], v[4:7]\n\t" \
    "ds_read_b128 v[32:35], %[xp] offset:11264\n\t" \
    "v_mfma_f32_16x16x32_bf16 v[8:11], v[84:87], v[40:43], v[8:11]\n\t" \
    "s_waitcnt lgkmcnt(4)\n\t" \
    "v_mfma_f32_16x16x32_bf16 v[12:15], v[92:95], v[40:43], v[12:15]\n\t" \
    "v_mfma_f32_16x16x32_bf16 v[0:3], v[88:91], v[44:47], v[0:3]\n\t" \
    "ds_read_b128 v[36:39], %[xp] offset:12288\n\t" \
    "v_mfma_f32_16x16x32_bf16 v[4:7], v[96:99], v[44:47], v[4:7]\n\t" \
    "ds_read_b128 v[40:43], %[xp] offset:13312\n\t" \
    "v_mfma_f32_16x16x32_bf16 v[8:11], v[88:91], v[48:51], v[8:11]\n\t" \
    "s_waitcnt lgkmcnt(4)\n\t" \
    "v_mfma_f32_16x16x32_bf16 v[12:15], v[96:99], v[48:51], v[12:15]\n\t" \
    "v_mfma_f32_16x16x32_bf16 v[0:3], v[100:103], v[20:23], v[0:3]\n\t" \
    "ds_read_b128 v[44:47], %[xp] offset:14336\n\t" \
    "v_mfma_f32_16x16x32_bf16 v[4:7], v[108:111], v[20:23], v[4:7]\n\t" \
    "ds_read_b128 v[48:51], %[xp] offset:15360\n\t" \
    "v_mfma_f32_16x16x32_bf16 v[8:11], v[100:103], v[24:27], v[8:11]\n\t" \
    "s_waitcnt lgkmcnt(4)\n\t" \
    "v_mfma_f32_16x16x32_bf16 v[12:15], v[108:111], v[24:27], v[12:15]\n\t" \
    "buffer_load_dwordx4 %[l16], %[rs], %[a] offen offset:0 sc1 lds\n\t" \
    "v_mfma_f32_16x16x32_bf16 v[0:3], v[104:107], v[28:31], v[0:3]\n\t" \
    "ds_read_b128 v[20:23], %[xp] offset:16384\n\t" \
    "v_mfma_f32_16x16x32_bf16 v[4:7], v[112:115], v[28:31], v[4:7]\n\t" \
    "ds_read_b128 v[24:27], %[xp] offset:17408\n\t" \
    "v_mfma_f32_16x16x32_bf16 v[8:11], v[104:107], v[32:35], v[8:11]\n\t" \
    "s_waitcnt lgkmcnt(4)\n\t" \
    "v_mfma_f32_16x16x32_bf16 v[12:15], v[112:115], v[32:35], v[12:15]\n\t" \
    "buffer_load_dwordx4 %[l16], %[rs], %[a] offen offset:1024 sc1 lds\n\t" \
    "v_mfma_f32_16x16x32_bf16 v[0:3], v[116:119], v[36:39], v[0:3]\n\t" \
    "ds_read_b128 v[28:31], %[xp] offset:18432\n\t" \
    "v_mfma_f32_16x16x32_bf16 v[4:7], v[124:127], v[36:39], v[4:7]\n\t" \
    "ds_read_b128 v[32:35], %[xp] offset:19456\n\t" \
    "v_mfma_f32_16x16x32_bf16 v[8:11], v[116:119], v[40:43], v[8:11]\n\t" \
    "s_waitcnt lgkmcnt(4)\n\t" \
    "v_mfma_f32_16x16x32_bf16 v[12:15], v[124:127], v[40:43], v[12:15]\n\t" \
    "buffer_load_dwordx4 %[l16], %[rs], %[a] offen offset:2048 sc1 lds\n\t" \
    "v_mfma_f32_16x16x32_bf16 v[0:3], v[120:123], v[44:47], v[0:3]\n\t" \
    "ds_read_b128 v[36:39], %[xp] offset:20480\n\t" \
    "v_mfma_f32_16x16x32_bf16 v[4:7], v[128:131], v[44:47], v[4:7]\n\t" \
    "ds_read_b128 v[40:43], %[xp] offset:21504\n\t" \
    "v_mfma_f32_16x16x32_bf16 v[8:11], v[120:123], v[48:51], v[8:11]\n\t" \
    "s_waitcnt lgkmcnt(4)\n\t" \
    "v_mfma_f32_16x16x32_bf16 v[12:15], v[128:131], v[48:51], v[12:15]\n\t" \
    "buffer_load_dwordx4 %[l16], %[rs], %[a] offen offset:3072 sc1 lds\n\t" \
    "s_waitcnt vmcnt(4)\n\t" \
    "s_barrier\n\t" \
    "s_setprio 1\n\t" \
    "s_add_u32 m0, %[sd], 0\n\t" \
    "s_add_u32 %[a], %[sx], 524288\n\t" \
    "s_add_u32 %[b], %[oo], 0x1000\n\t" \
    "s_cmp_lg_u32 %[t], 0\n\t" \
    "s_cselect_b64 %[em], -1, 0\n\t" \
    "v_mfma_f32_16x16x32_bf16 v[0:3], v[132:135], v[20:23], v[0:3]\n\t" \
    "ds_read_b128 v[44:47], %[xp] offset:22528\n\t" \
    "v_mfma_f32_16x16x32_bf16 v[4:7], v[140:143], v[20:23], v[4:7]\n\t" \
    "ds_read_b128 v[48:51], %[xp] offset:23552\n\t" \
    "v_mfma_f32_16x16x32_bf16 v[8:11], v[132:135], v[24:27], v[8:11]\n\t" \
    "ds_read_b128 v[52:55], %[pa] offset:0\n\t" \
    "s_waitcnt lgkmcnt(5)\n\t" \
    "v_mfma_f32_16x16x32_bf16 v[12:15], v[140:143], v[24:27], v[12:15]\n\t" \
    "buffer_load_dwordx4 %[l16], %[rs], %[a] offen offset:0 sc1 lds\n\t" \
    "v_mfma_f32_16x16x32_bf16 v[0:3], v[136:139], v[28:31], v[0:3]\n\t" \
    "ds_read_b128 v[20:23], %[xp] offset:24576\n\t" \
    "v_mfma_f32_16x16x32_bf16 v[4:7], v[144:147], v[28:31], v[4:7]\n\t" \
    "ds_read_b128 v[24:27], %[xp] offset:25600\n\t" \
    "v_mfma_f32_16x16x32_bf16 v[8:11], v[136:139], v[32:35], v[8:11]\n\t" \
    "ds_read_b128 v[56:59], %[pa] offset:1024\n\t" \
    "s_waitcnt lgkmcnt(6)\n\t" \
    "v_mfma_f32_16x16x32_bf16 v[12:15], v[144:147], v[32:35], v[12:15]\n\t" \
    "buffer_load_dwordx4 %[l16], %[rs], %[a] offen offset:1024 sc1 lds\n\t" \
    "v_mfma_f32_16x16x32_bf16 v[0:3], v[148:151], v[36:39], v[0:3]\n\t" \
    "ds_read_b128 v[28:31], %[xp] offset:26624\n\t" \
    "v_mfma_f32_16x16x32_bf16 v[4:7], v[156:159], v[36:39], v[4:7]\n\t" \
    "ds_read_b128 v[32:35], %[xp] offset:27648\n\t" \
    "v_mfma_f32_16x16x32_bf16 v[8:11], v[148:151], v[40:43], v[8:11]\n\t" \
    "ds_read_b128 v[60:63], %[pa] offset:2048\n\t" \
    "s_waitcnt lgkmcnt(7)\n\t" \
    "v_mfma_f32_16x16x32_bf16 v[12:15], v[156:159], v[40:43], v[12:15]\n\t" \
    "buffer_load_dwordx4 %[l16], %[rs], %[a] offen offset:2048 sc1 lds\n\t" \
    "v_mfma_f32_16x16x32_bf16 v[0:3], v[152:155], v[44:47], v[0:3]\n\t" \
    "ds_read_b128 v[36:39], %[xp] offset:28672\n\t" \
    "s_waitcnt lgkmcnt(7)\n\t" \
    "v_add_f32 v196, v196, v52\n\t" \
    "v_mfma_f32_16x16x32_bf16 v[4:7], v[160:163], v[44:47], v[4:7]\n\t" \
    "ds_read_b128 v[40:43], %[xp] offset:29696\n\t" \
    "v_add_f32 v197, v197, v53\n\t" \
    "v_mfma_f32_16x16x32_bf16 v[8:11], v[152:155], v[48:51], v[8:11]\n\t" \
    "ds_read_b128 v[64:67], %[pa] offset:3072\n\t" \
    "s_waitcnt lgkmcnt(7)\n\t" \
    "v_mfma_f32_16x16x32_bf16 v[12:15], v[160:163], v[48:51], v[12:15]\n\t" \
    "buffer_load_dwordx4 %[l16], %[rs], %[a] offen offset:3072 sc1 lds\n\t" \
    "v_add_f32 v198, v198, v54\n\t" \
    "v_add_f32 v199, v199, v55\n\t" \
    "v_mfma_f32_16x16x32_bf16 v[0:3], v[164:167], v[20:23], v[0:3]\n\t" \
    "ds_read_b128 v[44:47], %[xp] offset:30720\n\t" \
    "s_waitcnt lgkmcnt(7)\n\t" \
    "v_add_f32 v200, v200, v56\n\t" \
    "v_mfma_f32_16x16x32_bf16 v[4:7], v[172:175], v[20:23], v[4:7]\n\t" \
    "ds_read_b128 v[48:51], %[xp] offset:31744\n\t" \
    "v_add_f32 v201, v201, v57\n\t" \
    "v_mfma_f32_16x16x32_bf16 v[8:11], v[164:167], v[24:27], v[8:11]\n\t" \
    "v_add_f32 v202, v202, v58\n\t" \
    "s_waitcnt lgkmcnt(6)\n\t" \
    "v_mfma_f32_16x16x32_bf16 v[12:15], v[172:175], v[24:27], v[12:15]\n\t" \
    "v_add_f32 v203, v203, v59\n\t" \
    "s_waitcnt lgkmcnt(5)\n\t" \
    "v_add_f32 v204, v204, v60\n\t" \
    "v_add_f32 v205, v205, v61\n\t" \
    "v_mfma_f32_16x16x32_bf16 v[0:3], v[168:171], v[28:31], v[0:3]\n\t" \
    "ds_read_b128 v[20:23], %[xp] offset:32768\n\t" \
    "v_add_f32 v206, v206, v62\n\t" \
    "v_mfma_f32_16x16x32_bf16 v[4:7], v[176:179], v[28:31], v[4:7]\n\t" \
    "ds_read_b128 v[24:27], %[xp] offset:33792\n\t" \
    "v_add_f32 v207, v207, v63\n\t" \
    "v_mfma_f32_16x16x32_bf16 v[8:11], v[168:171], v[32:35], v[8:11]\n\t" \
    "s_waitcnt lgkmcnt(4)\n\t" \
    "v_add_f32 v208, v208, v64\n\t" \
    "v_mfma_f32_16x16x32_bf16 v[12:15], v[176:179], v[32:35], v[12:15]\n\t" \
    "v_add_f32 v209, v209, v65\n\t" \
    "v_add_f32 v210, v210, v66\n\t" \
    "v_add_f32 v211, v211, v67\n\t" \
    "v_mfma_f32_16x16x32_bf16 v[0:3], v[180:183], v[36:39], v[0:3]\n\t" \
    "ds_read_b128 v[28:31], %[xp] offset:34816\n\t" \
    "v_cvt_pk_bf16_f32 v52, v196, v200\n\t" \
    "v_mfma_f32_16x16x32_bf16 v[4:7], v[188:191], v[36:39], v[4:7]\n\t" \
    "ds_read_b128 v[32:35], %[xp] offset:35840\n\t" \
    "v_cvt_pk_bf16_f32 v53, v197, v201\n\t" \
    "v_mfma_f32_16x16x32_bf16 v[8:11], v[180:183], v[40:43], v[8:11]\n\t" \
    "v_cvt_pk_bf16_f32 v54, v198, v202\n\t" \
    "s_waitcnt lgkmcnt(4)\n\t" \
    "v_mfma_f32_16x16x32_bf16 v[12:15], v[188:191], v[40:43], v[12:15]\n\t" \
    "v_cvt_pk_bf16_f32 v55, v199, v203\n\t" \
    "v_cvt_pk_bf16_f32 v56, v204, v208\n\t" \
    "v_cvt_pk_bf16_f32 v57, v205, v209\n\t" \
    "v_mfma_f32_16x16x32_bf16 v[0:3], v[184:187], v[44:47], v[0:3]\n\t" \
    "ds_read_b128 v[36:39], %[xp] offset:36864\n\t" \
    "v_cvt_pk_bf16_f32 v58, v206, v210\n\t" \
    "v_mfma_f32_16x16x32_bf16 v[4:7], v[192:195], v[44:47], v[4:7]\n\t" \
    "ds_read_b128 v[40:43], %[xp] offset:37888\n\t" \
    "v_cvt_pk_bf16_f32 v59, v207, v211\n\t" \
    "v_mfma_f32_16x16x32_bf16 v[8:11], v[184:187], v[48:51], v[8:11]\n\t" \
    "s_waitcnt lgkmcnt(4)\n\t" \
    "v_mfma_f32_16x16x32_bf16 v[12:15], v[192:195], v[48:51], v[12:15]\n\t" \
    "s_mov_b64 exec, %[em]\n\t" \
    "buffer_store_dwordx4 v[52:55], %[l16], %[rs], %[oo] offen sc1\n\t" \
    "buffer_store_dwordx4 v[56:59], %[l16], %[rs], %[b] offen sc1\n\t" \
    "s_mov_b64 exec, -1\n\t" \
    "s_add_u32 %[sx], %[sx], 262144\n\t" \
    "s_add_u32 %[t], %[t], 1\n\t" \
    "s_add_u32 %[oo], %[oo], 262144\n\t" \
    "s_waitcnt vmcnt(6)\n\t" \
    "s_cmp_gt_u32 %[t], " STR(SQUICK) " + 1\n\t" \
    "s_cbranch_scc1 B0_%=\n\t" \
    "s_waitcnt vmcnt(0)\n\t" \
    "B0_%=:\n\t" \
    "s_barrier\n\t" \
    "s_setprio 1\n\t" \
    "s_add_u32 m0, %[sd], 16384\n\t" \
    "s_add_u32 %[a], %[sx], 278528\n\t" \
    "v_mfma_f32_16x16x32_bf16 v[196:199], v[68:71], v[20:23], 0\n\t" \
    "ds_read_b128 v[44:47], %[xp] offset:38912\n\t" \
    "v_mfma_f32_16x16x32_bf16 v[200:203], v[76:79], v[20:23], 0\n\t" \
    "ds_read_b128 v[48:51], %[xp] offset:39936\n\t" \
    "v_mfma_f32_16x16x32_bf16 v[204:207], v[68:71], v[24:27], 0\n\t" \
    "s_waitcnt lgkmcnt(4)\n\t" \
    "v_mfma_f32_16x16x32_bf16 v[208:211], v[76:79], v[24:27], 0\n\t" \
    "v_mfma_f32_16x16x32_bf16 v[196:199], v[72:75], v[28:31], v[196:199]\n\t" \
    "ds_read_b128 v[20:23], %[xp] offset:40960\n\t" \
    "v_mfma_f32_16x16x32_bf16 v[200:203], v[80:83], v[28:31], v[200:203]\n\t" \
    "ds_read_b128 v[24:27], %[xp] offset:41984\n\t" \
    "v_mfma_f32_16x16x32_bf16 v[204:207], v[72:75], v[32:35], v[204:207]\n\t" \
    "s_waitcnt lgkmcnt(4)\n\t" \
    "v_mfma_f32_16x16x32_bf16 v[208:211], v[80:83], v[32:35], v[208:211]\n\t" \
    "v_mfma_f32_16x16x32_bf16 v[196:199], v[84:87], v[36:39], v[196:199]\n\t" \
    "ds_read_b128 v[28:31], %[xp] offset:43008\n\t" \
    "v_mfma_f32_16x16x32_bf16 v[200:203], v[92:95], v[36:39], v[200:203]\n\t" \
    "ds_read_b128 v[32:35], %[xp] offset:44032\n\t" \
    "v_mfma_f32_16x16x32_bf16 v[204:207], v[84:87], v[40:43], v[204:207]\n\t" \
    "s_waitcnt lgkmcnt(4)\n\t" \
    "v_mfma_f32_16x16x32_bf16 v[208:211], v[92:95], v[40:43], v[208:211]\n\t" \
    "v_mfma_f32_16x16x32_bf16 v[196:199], v[88:91], v[44:47], v[196:199]\n\t" \
    "ds_read_b128 v[36:39], %[xp] offset:45056\n\t" \
    "v_mfma_f32_16x16x32_bf16 v[200:203], v[96:99], v[44:47], v[200:203]\n\t" \
    "ds_read_b128 v[40:43], %[xp] offset:46080\n\t" \
    "v_mfma_f32_16x16x32_bf16 v[204:207], v[88:91], v[48:51], v[204:207]\n\t" \
    "s_waitcnt lgkmcnt(4)\n\t" \
    "v_mfma_f32_16x16x32_bf16 v[208:211], v[96:99], v[48:51], v[208:211]\n\t" \
    "v_mfma_f32_16x16x32_bf16 v[196:199], v[100:103], v[20:23], v[196:199]\n\t" \
    "ds_read_b128 v[44:47], %[xp] offset:47104\n\t" \
    "v_mfma_f32_16x16x32_bf16 v[200:203], v[108:111], v[20:23], v[200:203]\n\t" \
    "ds_read_b128 v[48:51], %[xp] offset:48128\n\t" \
    "v_mfma_f32_16x16x32_bf16 v[204:207], v[100:103], v[24:27], v[204:207]\n\t" \
    "s_waitcnt lgkmcnt(4)\n\t" \
    "v_mfma_f32_16x16x32_bf16 v[208:211], v[108:111], v[24:27], v[208:211]\n\t" \
    "buffer_load_dwordx4 %[l16], %[rs], %[a] offen offset:0 sc1 lds\n\t" \
    "v_mfma_f32_16x16x32_bf16 v[196:199], v[104:107], v[28:31], v[196:199]\n\t" \
    "ds_read_b128 v[20:23], %[xp] offset:49152\n\t" \
    "v_mfma_f32_16x16x32_bf16 v[200:203], v[112:115], v[28:31], v[200:203]\n\t" \
    "ds_read_b128 v[24:27], %[xp] offset:50176\n\t" \
    "v_mfma_f32_16x16x32_bf16 v[204:207], v[104:107], v[32:35], v[204:207]\n\t" \
    "s_waitcnt lgkmcnt(4)\n\t" \
    "v_mfma_f32_16x16x32_bf16 v[208:211], v[112:115], v[32:35], v[208:211]\n\t" \
    "buffer_load_dwordx4 %[l16], %[rs], %[a] offen offset:1024 sc1 lds\n\t" \
    "v_mfma_f32_16x16x32_bf16 v[196:199], v[116:119], v[36:39], v[196:199]\n\t" \
    "ds_read_b128 v[28:31], %[xp] offset:51200\n\t" \
    "v_mfma_f32_16x16x32_bf16 v[200:203], v[124:127], v[36:39], v[200:203]\n\t" \
    "ds_read_b128 v[32:35], %[xp] offset:52224\n\t" \
    "v_mfma_f32_16x16x32_bf16 v[204:207], v[116:119], v[40:43], v[204:207]\n\t" \
    "s_waitcnt lgkmcnt(4)\n\t" \
    "v_mfma_f32_16x16x32_bf16 v[208:211], v[124:127], v[40:43], v[208:211]\n\t" \
    "buffer_load_dwordx4 %[l16], %[rs], %[a] offen offset:2048 sc1 lds\n\t" \
    "v_mfma_f32_16x16x32_bf16 v[196:199], v[120:123], v[44:47], v[196:199]\n\t" \
    "ds_read_b128 v[36:39], %[xp] offset:53248\n\t" \
    "v_mfma_f32_16x16x32_bf16 v[200:203], v[128:131], v[44:47], v[200:203]\n\t" \
    "ds_read_b128 v[40:43], %[xp] offset:54272\n\t" \
    "v_mfma_f32_16x16x32_bf16 v[204:207], v[120:123], v[48:51], v[204:207]\n\t" \
    "s_waitcnt lgkmcnt(4)\n\t" \
    "v_mfma_f32_16x16x32_bf16 v[208:211], v[128:131], v[48:51], v[208:211]\n\t" \
    "buffer_load_dwordx4 %[l16], %[rs], %[a] offen offset:3072 sc1 lds\n\t" \
    "s_waitcnt vmcnt(4)\n\t" \
    "s_barrier\n\t" \
    "s_setprio 1\n\t" \
    "s_add_u32 m0, %[sd], 32768\n\t" \
    "s_add_u32 %[a], %[sx], 524288\n\t" \
    "s_add_u32 %[b], %[oo], 0x1000\n\t" \
    "s_cmp_lg_u32 %[t], 0\n\t" \
    "s_cselect_b64 %[em], -1, 0\n\t" \
    "v_mfma_f32_16x16x32_bf16 v[196:199], v[132:135], v[20:23], v[196:199]\n\t" \
    "ds_read_b128 v[44:47], %[xp] offset:55296\n\t" \
    "v_mfma_f32_16x16x32_bf16 v[200:203], v[140:143], v[20:23], v[200:203]\n\t" \
    "ds_read_b128 v[48:51], %[xp] offset:56320\n\t" \
    "v_mfma_f32_16x16x32_bf16 v[204:207], v[132:135], v[24:27], v[204:207]\n\t" \
    "ds_read_b128 v[52:55], %[pa] offset:0\n\t" \
    "s_waitcnt lgkmcnt(5)\n\t" \
    "v_mfma_f32_16x16x32_bf16 v[208:211], v[140:143], v[24:27], v[208:211]\n\t" \
    "buffer_load_dwordx4 %[l16], %[rs], %[a] offen offset:0 sc1 lds\n\t" \
    "v_mfma_f32_16x16x32_bf16 v[196:199], v[136:139], v[28:31], v[196:199]\n\t" \
    "ds_read_b128 v[20:23], %[xp] offset:57344\n\t" \
    "v_mfma_f32_16x16x32_bf16 v[200:203], v[144:147], v[28:31], v[200:203]\n\t" \
    "ds_read_b128 v[24:27], %[xp] offset:58368\n\t" \
    "v_mfma_f32_16x16x32_bf16 v[204:207], v[136:139], v[32:35], v[204:207]\n\t" \
    "ds_read_b128 v[56:59], %[pa] offset:1024\n\t" \
    "s_waitcnt lgkmcnt(6)\n\t" \
    "v_mfma_f32_16x16x32_bf16 v[208:211], v[144:147], v[32:35], v[208:211]\n\t" \
    "buffer_load_dwordx4 %[l16], %[rs], %[a] offen offset:1024 sc1 lds\n\t" \
    "v_mfma_f32_16x16x32_bf16 v[196:199], v[148:151], v[36:39], v[196:199]\n\t" \
    "ds_read_b128 v[28:31], %[xp] offset:59392\n\t" \
    "v_mfma_f32_16x16x32_bf16 v[200:203], v[156:159], v[36:39], v[200:203]\n\t" \
    "ds_read_b128 v[32:35], %[xp] offset:60416\n\t" \
    "v_mfma_f32_16x16x32_bf16 v[204:207], v[148:151], v[40:43], v[204:207]\n\t" \
    "ds_read_b128 v[60:63], %[pa] offset:2048\n\t" \
    "s_waitcnt lgkmcnt(7)\n\t" \
    "v_mfma_f32_16x16x32_bf16 v[208:211], v[156:159], v[40:43], v[208:211]\n\t" \
    "buffer_load_dwordx4 %[l16], %[rs], %[a] offen offset:2048 sc1 lds\n\t" \
    "v_mfma_f32_16x16x32_bf16 v[196:199], v[152:155], v[44:47], v[196:199]\n\t" \
    "ds_read_b128 v[36:39], %[xp] offset:61440\n\t" \
    "s_waitcnt lgkmcnt(7)\n\t" \
    "v_add_f32 v0, v0, v52\n\t" \
    "v_mfma_f32_16x16x32_bf16 v[200:203], v[160:163], v[44:47], v[200:203]\n\t" \
    "ds_read_b128 v[40:43], %[xp] offset:62464\n\t" \
    "v_add_f32 v1, v1, v53\n\t" \
    "v_mfma_f32_16x16x32_bf16 v[204:207], v[152:155], v[48:51], v[204:207]\n\t" \
    "ds_read_b128 v[64:67], %[pa] offset:3072\n\t" \
    "s_waitcnt lgkmcnt(7)\n\t" \
    "v_mfma_f32_16x16x32_bf16 v[208:211], v[160:163], v[48:51], v[208:211]\n\t" \
    "buffer_load_dwordx4 %[l16], %[rs], %[a] offen offset:3072 sc1 lds\n\t" \
    "v_add_f32 v2, v2, v54\n\t" \
    "v_add_f32 v3, v3, v55\n\t" \
    "v_mfma_f32_16x16x32_bf16 v[196:199], v[164:167], v[20:23], v[196:199]\n\t" \
    "ds_read_b128 v[44:47], %[xp] offset:63488\n\t" \
    "s_waitcnt lgkmcnt(7)\n\t" \
    "v_add_f32 v4, v4, v56\n\t" \
    "v_mfma_f32_16x16x32_bf16 v[200:203], v[172:175], v[20:23], v[200:203]\n\t" \
    "ds_read_b128 v[48:51], %[xp] offset:64512\n\t" \
    "v_add_f32 v5, v5, v57\n\t" \
    "v_mfma_f32_16x16x32_bf16 v[204:207], v[164:167], v[24:27], v[204:207]\n\t" \
    "v_add_f32 v6, v6, v58\n\t" \
    "s_waitcnt lgkmcnt(6)\n\t" \
    "v_mfma_f32_16x16x32_bf16 v[208:211], v[172:175], v[24:27], v[208:211]\n\t" \
    "v_add_f32 v7, v7, v59\n\t" \
    "s_waitcnt lgkmcnt(5)\n\t" \
    "v_add_f32 v8, v8, v60\n\t" \
    "v_add_f32 v9, v9, v61\n\t" \
    "v_mfma_f32_16x16x32_bf16 v[196:199], v[168:171], v[28:31], v[196:199]\n\t" \
    "ds_read_b128 v[20:23], %[xp] offset:0\n\t" \
    "v_add_f32 v10, v10, v62\n\t" \
    "v_mfma_f32_16x16x32_bf16 v[200:203], v[176:179], v[28:31], v[200:203]\n\t" \
    "ds_read_b128 v[24:27], %[xp] offset:1024\n\t" \
    "v_add_f32 v11, v11, v63\n\t" \
    "v_mfma_f32_16x16x32_bf16 v[204:207], v[168:171], v[32:35], v[204:207]\n\t" \
    "s_waitcnt lgkmcnt(4)\n\t" \
    "v_add_f32 v12, v12, v64\n\t" \
    "v_mfma_f32_16x16x32_bf16 v[208:211], v[176:179], v[32:35], v[208:211]\n\t" \
    "v_add_f32 v13, v13, v65\n\t" \
    "v_add_f32 v14, v14, v66\n\t" \
    "v_add_f32 v15, v15, v67\n\t" \
    "v_mfma_f32_16x16x32_bf16 v[196:199], v[180:183], v[36:39], v[196:199]\n\t" \
    "ds_read_b128 v[28:31], %[xp] offset:2048\n\t" \
    "v_cvt_pk_bf16_f32 v52, v0, v4\n\t" \
    "v_mfma_f32_16x16x32_bf16 v[200:203], v[188:191], v[36:39], v[200:203]\n\t" \
    "ds_read_b128 v[32:35], %[xp] offset:3072\n\t" \
    "v_cvt_pk_bf16_f32 v53, v1, v5\n\t" \
    "v_mfma_f32_16x16x32_bf16 v[204:207], v[180:183], v[40:43], v[204:207]\n\t" \
    "v_cvt_pk_bf16_f32 v54, v2, v6\n\t" \
    "s_waitcnt lgkmcnt(4)\n\t" \
    "v_mfma_f32_16x16x32_bf16 v[208:211], v[188:191], v[40:43], v[208:211]\n\t" \
    "v_cvt_pk_bf16_f32 v55, v3, v7\n\t" \
    "v_cvt_pk_bf16_f32 v56, v8, v12\n\t" \
    "v_cvt_pk_bf16_f32 v57, v9, v13\n\t" \
    "v_mfma_f32_16x16x32_bf16 v[196:199], v[184:187], v[44:47], v[196:199]\n\t" \
    "ds_read_b128 v[36:39], %[xp] offset:4096\n\t" \
    "v_cvt_pk_bf16_f32 v58, v10, v14\n\t" \
    "v_mfma_f32_16x16x32_bf16 v[200:203], v[192:195], v[44:47], v[200:203]\n\t" \
    "ds_read_b128 v[40:43], %[xp] offset:5120\n\t" \
    "v_cvt_pk_bf16_f32 v59, v11, v15\n\t" \
    "v_mfma_f32_16x16x32_bf16 v[204:207], v[184:187], v[48:51], v[204:207]\n\t" \
    "s_waitcnt lgkmcnt(4)\n\t" \
    "v_mfma_f32_16x16x32_bf16 v[208:211], v[192:195], v[48:51], v[208:211]\n\t" \
    "s_mov_b64 exec, %[em]\n\t" \
    "buffer_store_dwordx4 v[52:55], %[l16], %[rs], %[oo] offen sc1\n\t" \
    "buffer_store_dwordx4 v[56:59], %[l16], %[rs], %[b] offen sc1\n\t" \
    "s_mov_b64 exec, -1\n\t" \
    "s_add_u32 %[sx], %[sx], 262144\n\t" \
    "s_add_u32 %[t], %[t], 1\n\t" \
    "s_add_u32 %[oo], %[oo], 262144\n\t" \
    "s_waitcnt vmcnt(6)\n\t" \
    "s_cmp_gt_u32 %[t], " STR(SQUICK) " + 1\n\t" \
    "s_cbranch_scc1 B1_%=\n\t" \
    "s_waitcnt vmcnt(0)\n\t" \
    "B1_%=:\n\t" \
    "s_barrier\n\t" \
    "s_cmp_lt_u32 %[t], %[te]\n\t" \
    "s_cbranch_scc1 L1_%=\n\t" \
    "s_cmp_eq_u32 %[fin], %[te]\n\t" \
    "s_cbranch_scc0 E_%=\n\t" \
    "s_nop 4\n\t" \
    "s_barrier\n\t" \
    "ds_read_b128 v[52:55], %[pa] offset:0\n\t" \
    "ds_read_b128 v[56:59], %[pa] offset:1024\n\t" \
    "ds_read_b128 v[60:63], %[pa] offset:2048\n\t" \
    "ds_read_b128 v[64:67], %[pa] offset:3072\n\t" \
    "s_add_u32 %[b], %[oo], 0x1000\n\t" \
    "s_waitcnt lgkmcnt(0)\n\t" \
    "v_add_f32 v196, v196, v52\n\t" \
    "v_add_f32 v197, v197, v53\n\t" \
    "v_add_f32 v198, v198, v54\n\t" \
    "v_add_f32 v199, v199, v55\n\t" \
    "v_add_f32 v200, v200, v56\n\t" \
    "v_add_f32 v201, v201, v57\n\t" \
    "v_add_f32 v202, v202, v58\n\t" \
    "v_add_f32 v203, v203, v59\n\t" \
    "v_add_f32 v204, v204, v60\n\t" \
    "v_add_f32 v205, v205, v61\n\t" \
    "v_add_f32 v206, v206, v62\n\t" \
    "v_add_f32 v207, v207, v63\n\t" \
    "v_add_f32 v208, v208, v64\n\t" \
    "v_add_f32 v209, v209, v65\n\t" \
    "v_add_f32 v210, v210, v66\n\t" \
    "v_add_f32 v211, v211, v67\n\t" \
    "v_cvt_pk_bf16_f32 v52, v196, v200\n\t" \
    "v_cvt_pk_bf16_f32 v53, v197, v201\n\t" \
    "v_cvt_pk_bf16_f32 v54, v198, v202\n\t" \
    "v_cvt_pk_bf16_f32 v55, v199, v203\n\t" \
    "v_cvt_pk_bf16_f32 v56, v204, v208\n\t" \
    "v_cvt_pk_bf16_f32 v57, v205, v209\n\t" \
    "v_cvt_pk_bf16_f32 v58, v206, v210\n\t" \
    "v_cvt_pk_bf16_f32 v59, v207, v211\n\t" \
    "buffer_store_dwordx4 v[52:55], %[l16], %[rs], %[oo] offen sc1\n\t" \
    "buffer_store_dwordx4 v[56:59], %[l16], %[rs], %[b] offen sc1\n\t" \
    "E_%=:\n\t" \
    "s_setprio 0\n\t" \
    "s_waitcnt lgkmcnt(0)\n\t" \
    "s_nop 7\n\t" \
    : [t] "+s"(t), [sx] "+s"(sx), [oo] "+s"(oo), [a] "=&s"(tmp), [b] "=&s"(tmp2), [em] "=&s"(em), "+{v[20:27]}"(xf[0]), "+{v[28:35]}"(xf[1]), "+{v[36:43]}"(xf[2]), "+{v[196:203]}"(ac[0]), "+{v[204:211]}"(ac[1]) \
    : [te] "s"(TE_), [fin] "s"(FIN_), [kh] "s"(kh), [sd] "s"(sd), [rs] "s"(rs), [l16] "v"(l16), [xp] "v"(xp), [pa] "v"(pa), [fm] "s"(fm), [pm] "s"(pm), [fo] "s"(fo), [po] "s"(po), [pd] "s"(pd), [tag] "s"(P.tag), "{v[68:75]}"(w[0][0]), "{v[76:83]}"(w[0][1]), "{v[84:91]}"(w[1][0]), "{v[92:99]}"(w[1][1]), "{v[100:107]}"(w[2][0]), "{v[108:115]}"(w[2][1]), "{v[116:123]}"(w[3][0]), "{v[124:131]}"(w[3][1]), "{v[132:139]}"(w[4][0]), "{v[140:147]}"(w[4][1]), "{v[148:155]}"(w[5][0]), "{v[156:163]}"(w[5][1]), "{v[164:171]}"(w[6][0]), "{v[172:179]}"(w[6][1]), "{v[180:187]}"(w[7][0]), "{v[188:195]}"(w[7][1]) \
    : "memory", "scc", "v0", "v1", "v2", "v3", "v4", "v5", "v6", "v7", "v8", "v9", "v10", "v11", "v12", "v13", "v14", "v15", "v44", "v45", "v46", "v47", "v48", "v49", "v50", "v51", "v52", "v53", "v54", "v55", "v56", "v57", "v58", "v59", "v60", "v61", "v62", "v63", "v64", "v65", "v66", "v67")

#define ONE_X(XP_) \
  asm volatile( \
    "ds_read_b128 v[20:23], %[xp] offset:0\n\t" \
    "ds_read_b128 v[24:27], %[xp] offset:1024\n\t" \
    "ds_read_b128 v[28:31], %[xp] offset:2048\n\t" \
    "ds_read_b128 v[32:35], %[xp] offset:3072\n\t" \
    "ds_read_b128 v[36:39], %[xp] offset:4096\n\t" \
    "ds_read_b128 v[40:43], %[xp] offset:5120\n\t" \
    "s_waitcnt lgkmcnt(4)\n\t" \
    "v_mfma_f32_16x16x32_bf16 v[0:3], v[68:71], v[20:23], 0\n\t" \
    "ds_read_b128 v[44:47], %[xp] offset:6144\n\t" \
    "v_mfma_f32_16x16x32_bf16 v[4:7], v[76:79], v[20:23], 0\n\t" \
    "ds_read_b128 v[48:51], %[xp] offset:7168\n\t" \
    "v_mfma_f32_16x16x32_bf16 v[8:11], v[68:71], v[24:27], 0\n\t" \
    "s_waitcnt lgkmcnt(4)\n\t" \
    "v_mfma_f32_16x16x32_bf16 v[12:15], v[76:79], v[24:27], 0\n\t" \
    "v_mfma_f32_16x16x32_bf16 v[0:3], v[72:75], v[28:31], v[0:3]\n\t" \
    "ds_read_b128 v[20:23], %[xp] offset:8192\n\t" \
    "v_mfma_f32_16x16x32_bf16 v[4:7], v[80:83], v[28:31], v[4:7]\n\t" \
    "ds_read_b128 v[24:27], %[xp] offset:9216\n\t" \
    "v_mfma_f32_16x16x32_bf16 v[8:11], v[72:75], v[32:35], v[8:11]\n\t" \
    "s_waitcnt lgkmcnt(4)\n\t" \
    "v_mfma_f32_16x16x32_bf16 v[12:15], v[80:83], v[32:35], v[12:15]\n\t" \
    "v_mfma_f32_16x16x32_bf16 v[0:3], v[84:87], v[36:39], v[0:3]\n\t" \
    "ds_read_b128 v[28:31], %[xp] offset:10240\n\t" \
    "v_mfma_f32_16x16x32_bf16 v[4:7], v[92:95], v[36:39], v[4:7]\n\t" \
    "ds_read_b128 v[32:35], %[xp] offset:11264\n\t" \
    "v_mfma_f32_16x16x32_bf16 v[8:11], v[84:87], v[40:43], v[8:11]\n\t" \
    "s_waitcnt lgkmcnt(4)\n\t" \
    "v_mfma_f32_16x16x32_bf16 v[12:15], v[92:95], v[40:43], v[12:15]\n\t" \
    "v_mfma_f32_16x16x32_bf16 v[0:3], v[88:91], v[44:47], v[0:3]\n\t" \
    "ds_read_b128 v[36:39], %[xp] offset:12288\n\t" \
    "v_mfma_f32_16x16x32_bf16 v[4:7], v[96:99], v[44:47], v[4:7]\n\t" \
    "ds_read_b128 v[40:43], %[xp] offset:13312\n\t" \
    "v_mfma_f32_16x16x32_bf16 v[8:11], v[88:91], v[48:51], v[8:11]\n\t" \
    "s_waitcnt lgkmcnt(4)\n\t" \
    "v_mfma_f32_16x16x32_bf16 v[12:15], v[96:99], v[48:51], v[12:15]\n\t" \
    "v_mfma_f32_16x16x32_bf16 v[0:3], v[100:103], v[20:23], v[0:3]\n\t" \
    "ds_read_b128 v[44:47], %[xp] offset:14336\n\t" \
    "v_mfma_f32_16x16x32_bf16 v[4:7], v[108:111], v[20:23], v[4:7]\n\t" \
    "ds_read_b128 v[48:51], %[xp] offset:15360\n\t" \
    "v_mfma_f32_16x16x32_bf16 v[8:11], v[100:103], v[24:27], v[8:11]\n\t" \
    "s_waitcnt lgkmcnt(4)\n\t" \
    "v_mfma_f32_16x16x32_bf16 v[12:15], v[108:111], v[24:27], v[12:15]\n\t" \
    "s_waitcnt vmcnt(0)\n\t" \
    "s_barrier\n\t" \
    "v_mfma_f32_16x16x32_bf16 v[0:3], v[104:107], v[28:31], v[0:3]\n\t" \
    "ds_read_b128 v[20:23], %[xp] offset:16384\n\t" \
    "v_mfma_f32_16x16x32_bf16 v[4:7], v[112:115], v[28:31], v[4:7]\n\t" \
    "ds_read_b128 v[24:27], %[xp] offset:17408\n\t" \
    "v_mfma_f32_16x16x32_bf16 v[8:11], v[104:107], v[32:35], v[8:11]\n\t" \
    "s_waitcnt lgkmcnt(4)\n\t" \
    "v_mfma_f32_16x16x32_bf16 v[12:15], v[112:115], v[32:35], v[12:15]\n\t" \
    "buffer_load_dword v52, %[pv], %[rs], 0 offen sc1\n\t" \
    "v_mfma_f32_16x16x32_bf16 v[0:3], v[116:119], v[36:39], v[0:3]\n\t" \
    "ds_read_b128 v[28:31], %[xp] offset:18432\n\t" \
    "v_mfma_f32_16x16x32_bf16 v[4:7], v[124:127], v[36:39], v[4:7]\n\t" \
    "ds_read_b128 v[32:35], %[xp] offset:19456\n\t" \
    "v_mfma_f32_16x16x32_bf16 v[8:11], v[116:119], v[40:43], v[8:11]\n\t" \
    "s_waitcnt lgkmcnt(4)\n\t" \
    "v_mfma_f32_16x16x32_bf16 v[12:15], v[124:127], v[40:43], v[12:15]\n\t" \
    "v_mfma_f32_16x16x32_bf16 v[0:3], v[120:123], v[44:47], v[0:3]\n\t" \
    "ds_read_b128 v[36:39], %[xp] offset:20480\n\t" \
    "v_mfma_f32_16x16x32_bf16 v[4:7], v[128:131], v[44:47], v[4:7]\n\t" \
    "ds_read_b128 v[40:43], %[xp] offset:21504\n\t" \
    "v_mfma_f32_16x16x32_bf16 v[8:11], v[120:123], v[48:51], v[8:11]\n\t" \
    "s_waitcnt lgkmcnt(4)\n\t" \
    "v_mfma_f32_16x16x32_bf16 v[12:15], v[128:131], v[48:51], v[12:15]\n\t" \
    "v_mfma_f32_16x16x32_bf16 v[0:3], v[132:135], v[20:23], v[0:3]\n\t" \
    "ds_read_b128 v[44:47], %[xp] offset:22528\n\t" \
    "v_mfma_f32_16x16x32_bf16 v[4:7], v[140:143], v[20:23], v[4:7]\n\t" \
    "ds_read_b128 v[48:51], %[xp] offset:23552\n\t" \
    "v_mfma_f32_16x16x32_bf16 v[8:11], v[132:135], v[24:27], v[8:11]\n\t" \
    "s_waitcnt lgkmcnt(4)\n\t" \
    "v_mfma_f32_16x16x32_bf16 v[12:15], v[140:143], v[24:27], v[12:15]\n\t" \
    "v_mfma_f32_16x16x32_bf16 v[0:3], v[136:139], v[28:31], v[0:3]\n\t" \
    "ds_read_b128 v[20:23], %[xp] offset:24576\n\t" \
    "v_mfma_f32_16x16x32_bf16 v[4:7], v[144:147], v[28:31], v[4:7]\n\t" \
    "ds_read_b128 v[24:27], %[xp] offset:25600\n\t" \
    "v_mfma_f32_16x16x32_bf16 v[8:11], v[136:139], v[32:35], v[8:11]\n\t" \
    "s_waitcnt lgkmcnt(4)\n\t" \
    "v_mfma_f32_16x16x32_bf16 v[12:15], v[144:147], v[32:35], v[12:15]\n\t" \
    "v_mfma_f32_16x16x32_bf16 v[0:3], v[148:151], v[36:39], v[0:3]\n\t" \
    "ds_read_b128 v[28:31], %[xp] offset:26624\n\t" \
    "v_mfma_f32_16x16x32_bf16 v[4:7], v[156:159], v[36:39], v[4:7]\n\t" \
    "ds_read_b128 v[32:35], %[xp] offset:27648\n\t" \
    "v_mfma_f32_16x16x32_bf16 v[8:11], v[148:151], v[40:43], v[8:11]\n\t" \
    "s_waitcnt lgkmcnt(4)\n\t" \
    "v_mfma_f32_16x16x32_bf16 v[12:15], v[156:159], v[40:43], v[12:15]\n\t" \
    "v_mfma_f32_16x16x32_bf16 v[0:3], v[152:155], v[44:47], v[0:3]\n\t" \
    "ds_read_b128 v[36:39], %[xp] offset:28672\n\t" \
    "v_mfma_f32_16x16x32_bf16 v[4:7], v[160:163], v[44:47], v[4:7]\n\t" \
    "ds_read_b128 v[40:43], %[xp] offset:29696\n\t" \
    "v_mfma_f32_16x16x32_bf16 v[8:11], v[152:155], v[48:51], v[8:11]\n\t" \
    "s_waitcnt lgkmcnt(4)\n\t" \
    "v_mfma_f32_16x16x32_bf16 v[12:15], v[160:163], v[48:51], v[12:15]\n\t" \
    "v_mfma_f32_16x16x32_bf16 v[0:3], v[164:167], v[20:23], v[0:3]\n\t" \
    "ds_read_b128 v[44:47], %[xp] offset:30720\n\t" \
    "v_mfma_f32_16x16x32_bf16 v[4:7], v[172:175], v[20:23], v[4:7]\n\t" \
    "ds_read_b128 v[48:51], %[xp] offset:31744\n\t" \
    "v_mfma_f32_16x16x32_bf16 v[8:11], v[164:167], v[24:27], v[8:11]\n\t" \
    "s_waitcnt lgkmcnt(4)\n\t" \
    "v_mfma_f32_16x16x32_bf16 v[12:15], v[172:175], v[24:27], v[12:15]\n\t" \
    "v_mfma_f32_16x16x32_bf16 v[0:3], v[168:171], v[28:31], v[0:3]\n\t" \
    "v_mfma_f32_16x16x32_bf16 v[4:7], v[176:179], v[28:31], v[4:7]\n\t" \
    "v_mfma_f32_16x16x32_bf16 v[8:11], v[168:171], v[32:35], v[8:11]\n\t" \
    "s_waitcnt lgkmcnt(2)\n\t" \
    "v_mfma_f32_16x16x32_bf16 v[12:15], v[176:179], v[32:35], v[12:15]\n\t" \
    "v_mfma_f32_16x16x32_bf16 v[0:3], v[180:183], v[36:39], v[0:3]\n\t" \
    "v_mfma_f32_16x16x32_bf16 v[4:7], v[188:191], v[36:39], v[4:7]\n\t" \
    "v_mfma_f32_16x16x32_bf16 v[8:11], v[180:183], v[40:43], v[8:11]\n\t" \
    "s_waitcnt lgkmcnt(0)\n\t" \
    "v_mfma_f32_16x16x32_bf16 v[12:15], v[188:191], v[40:43], v[12:15]\n\t" \
    "v_mfma_f32_16x16x32_bf16 v[0:3], v[184:187], v[44:47], v[0:3]\n\t" \
    "v_mfma_f32_16x16x32_bf16 v[4:7], v[192:195], v[44:47], v[4:7]\n\t" \
    "v_mfma_f32_16x16x32_bf16 v[8:11], v[184:187], v[48:51], v[8:11]\n\t" \
    "v_mfma_f32_16x16x32_bf16 v[12:15], v[192:195], v[48:51], v[12:15]\n\t" \
    "s_waitcnt vmcnt(0)\n\t" \
    "s_nop 7\n\t" \
    : "=&{v52}"(pg), "=&{v[0:3]}"(a[0]), "=&{v[4:7]}"(a[1]), "=&{v[8:11]}"(a[2]), "=&{v[12:15]}"(a[3]) \
    : [xp] "v"(XP_), [pv] "v"(pv), [rs] "s"(rs), "{v[68:75]}"(w[0][0]), "{v[76:83]}"(w[0][1]), "{v[84:91]}"(w[1][0]), "{v[92:99]}"(w[1][1]), "{v[100:107]}"(w[2][0]), "{v[108:115]}"(w[2][1]), "{v[116:123]}"(w[3][0]), "{v[124:131]}"(w[3][1]), "{v[132:139]}"(w[4][0]), "{v[140:147]}"(w[4][1]), "{v[148:155]}"(w[5][0]), "{v[156:163]}"(w[5][1]), "{v[164:171]}"(w[6][0]), "{v[172:179]}"(w[6][1]), "{v[180:187]}"(w[7][0]), "{v[188:195]}"(w[7][1]) \
    : "memory", "v20", "v21", "v22", "v23", "v24", "v25", "v26", "v27", "v28", "v29", "v30", "v31", "v32", "v33", "v34", "v35", "v36", "v37", "v38", "v39", "v40", "v41", "v42", "v43", "v44", "v45", "v46", "v47", "v48", "v49", "v50", "v51")

#define ONE_K(XP_) \
  asm volatile( \
    "ds_read_b128 v[20:23], %[xp] offset:0\n\t" \
    "ds_read_b128 v[24:27], %[xp] offset:1024\n\t" \
    "ds_read_b128 v[28:31], %[xp] offset:2048\n\t" \
    "ds_read_b128 v[32:35], %[xp] offset:3072\n\t" \
    "ds_read_b128 v[36:39], %[xp] offset:4096\n\t" \
    "ds_read_b128 v[40:43], %[xp] offset:5120\n\t" \
    "s_waitcnt lgkmcnt(4)\n\t" \
    "v_mfma_f32_16x16x32_bf16 v[0:3], v[68:71], v[20:23], 0\n\t" \
    "ds_read_b128 v[44:47], %[xp] offset:6144\n\t" \
    "v_mfma_f32_16x16x32_bf16 v[4:7], v[76:79], v[20:23], 0\n\t" \
    "ds_read_b128 v[48:51], %[xp] offset:7168\n\t" \
    "v_mfma_f32_16x16x32_bf16 v[8:11], v[68:71], v[24:27], 0\n\t" \
    "s_waitcnt lgkmcnt(4)\n\t" \
    "v_mfma_f32_16x16x32_bf16 v[12:15], v[76:79], v[24:27], 0\n\t" \
    "v_mfma_f32_16x16x32_bf16 v[0:3], v[72:75], v[28:31], v[0:3]\n\t" \
    "ds_read_b128 v[20:23], %[xp] offset:8192\n\t" \
    "v_mfma_f32_16x16x32_bf16 v[4:7], v[80:83], v[28:31], v[4:7]\n\t" \
    "ds_read_b128 v[24:27], %[xp] offset:9216\n\t" \
    "v_mfma_f32_16x16x32_bf16 v[8:11], v[72:75], v[32:35], v[8:11]\n\t" \
    "s_waitcnt lgkmcnt(4)\n\t" \
    "v_mfma_f32_16x16x32_bf16 v[12:15], v[80:83], v[32:35], v[12:15]\n\t" \
    "v_mfma_f32_16x16x32_bf16 v[0:3], v[84:87], v[36:39], v[0:3]\n\t" \
    "ds_read_b128 v[28:31], %[xp] offset:10240\n\t" \
    "v_mfma_f32_16x16x32_bf16 v[4:7], v[92:95], v[36:39], v[4:7]\n\t" \
    "ds_read_b128 v[32:35], %[xp] offset:11264\n\t" \
    "v_mfma_f32_16x16x32_bf16 v[8:11], v[84:87], v[40:43], v[8:11]\n\t" \
    "s_waitcnt lgkmcnt(4)\n\t" \
    "v_mfma_f32_16x16x32_bf16 v[12:15], v[92:95], v[40:43], v[12:15]\n\t" \
    "v_mfma_f32_16x16x32_bf16 v[0:3], v[88:91], v[44:47], v[0:3]\n\t" \
    "ds_read_b128 v[36:39], %[xp] offset:12288\n\t" \
    "v_mfma_f32_16x16x32_bf16 v[4:7], v[96:99], v[44:47], v[4:7]\n\t" \
    "ds_read_b128 v[40:43], %[xp] offset:13312\n\t" \
    "v_mfma_f32_16x16x32_bf16 v[8:11], v[88:91], v[48:51], v[8:11]\n\t" \
    "s_waitcnt lgkmcnt(4)\n\t" \
    "v_mfma_f32_16x16x32_bf16 v[12:15], v[96:99], v[48:51], v[12:15]\n\t" \
    "v_mfma_f32_16x16x32_bf16 v[0:3], v[100:103], v[20:23], v[0:3]\n\t" \
    "ds_read_b128 v[44:47], %[xp] offset:14336\n\t" \
    "v_mfma_f32_16x16x32_bf16 v[4:7], v[108:111], v[20:23], v[4:7]\n\t" \
    "ds_read_b128 v[48:51], %[xp] offset:15360\n\t" \
    "v_mfma_f32_16x16x32_bf16 v[8:11], v[100:103], v[24:27], v[8:11]\n\t" \
    "s_waitcnt lgkmcnt(4)\n\t" \
    "v_mfma_f32_16x16x32_bf16 v[12:15], v[108:111], v[24:27], v[12:15]\n\t" \
    "s_waitcnt vmcnt(8)\n\t" \
    "s_barrier\n\t" \
    "v_mfma_f32_16x16x32_bf16 v[0:3], v[104:107], v[28:31], v[0:3]\n\t" \
    "ds_read_b128 v[20:23], %[xp] offset:16384\n\t" \
    "v_mfma_f32_16x16x32_bf16 v[4:7], v[112:115], v[28:31], v[4:7]\n\t" \
    "ds_read_b128 v[24:27], %[xp] offset:17408\n\t" \
    "v_mfma_f32_16x16x32_bf16 v[8:11], v[104:107], v[32:35], v[8:11]\n\t" \
    "s_waitcnt lgkmcnt(4)\n\t" \
    "v_mfma_f32_16x16x32_bf16 v[12:15], v[112:115], v[32:35], v[12:15]\n\t" \
    "v_mfma_f32_16x16x32_bf16 v[0:3], v[116:119], v[36:39], v[0:3]\n\t" \
    "ds_read_b128 v[28:31], %[xp] offset:18432\n\t" \
    "v_mfma_f32_16x16x32_bf16 v[4:7], v[124:127], v[36:39], v[4:7]\n\t" \
    "ds_read_b128 v[32:35], %[xp] offset:19456\n\t" \
    "v_mfma_f32_16x16x32_bf16 v[8:11], v[116:119], v[40:43], v[8:11]\n\t" \
    "s_waitcnt lgkmcnt(4)\n\t" \
    "v_mfma_f32_16x16x32_bf16 v[12:15], v[124:127], v[40:43], v[12:15]\n\t" \
    "v_mfma_f32_16x16x32_bf16 v[0:3], v[120:123], v[44:47], v[0:3]\n\t" \
    "ds_read_b128 v[36:39], %[xp] offset:20480\n\t" \
    "v_mfma_f32_16x16x32_bf16 v[4:7], v[128:131], v[44:47], v[4:7]\n\t" \
    "ds_read_b128 v[40:43], %[xp] offset:21504\n\t" \
    "v_mfma_f32_16x16x32_bf16 v[8:11], v[120:123], v[48:51], v[8:11]\n\t" \
    "s_waitcnt lgkmcnt(4)\n\t" \
    "v_mfma_f32_16x16x32_bf16 v[12:15], v[128:131], v[48:51], v[12:15]\n\t" \
    "v_mfma_f32_16x16x32_bf16 v[0:3], v[132:135], v[20:23], v[0:3]\n\t" \
    "ds_read_b128 v[44:47], %[xp] offset:22528\n\t" \
    "v_mfma_f32_16x16x32_bf16 v[4:7], v[140:143], v[20:23], v[4:7]\n\t" \
    "ds_read_b128 v[48:51], %[xp] offset:23552\n\t" \
    "v_mfma_f32_16x16x32_bf16 v[8:11], v[132:135], v[24:27], v[8:11]\n\t" \
    "s_waitcnt lgkmcnt(4)\n\t" \
    "v_mfma_f32_16x16x32_bf16 v[12:15], v[140:143], v[24:27], v[12:15]\n\t" \
    "v_mfma_f32_16x16x32_bf16 v[0:3], v[136:139], v[28:31], v[0:3]\n\t" \
    "ds_read_b128 v[20:23], %[xp] offset:24576\n\t" \
    "v_mfma_f32_16x16x32_bf16 v[4:7], v[144:147], v[28:31], v[4:7]\n\t" \
    "ds_read_b128 v[24:27], %[xp] offset:25600\n\t" \
    "v_mfma_f32_16x16x32_bf16 v[8:11], v[136:139], v[32:35], v[8:11]\n\t" \
    "s_waitcnt lgkmcnt(4)\n\t" \
    "v_mfma_f32_16x16x32_bf16 v[12:15], v[144:147], v[32:35], v[12:15]\n\t" \
    "v_mfma_f32_16x16x32_bf16 v[0:3], v[148:151], v[36:39], v[0:3]\n\t" \
    "ds_read_b128 v[28:31], %[xp] offset:26624\n\t" \
    "v_mfma_f32_16x16x32_bf16 v[4:7], v[156:159], v[36:39], v[4:7]\n\t" \
    "ds_read_b128 v[32:35], %[xp] offset:27648\n\t" \
    "v_mfma_f32_16x16x32_bf16 v[8:11], v[148:151], v[40:43], v[8:11]\n\t" \
    "s_waitcnt lgkmcnt(4)\n\t" \
    "v_mfma_f32_16x16x32_bf16 v[12:15], v[156:159], v[40:43], v[12:15]\n\t" \
    "v_mfma_f32_16x16x32_bf16 v[0:3], v[152:155], v[44:47], v[0:3]\n\t" \
    "ds_read_b128 v[36:39], %[xp] offset:28672\n\t" \
    "v_mfma_f32_16x16x32_bf16 v[4:7], v[160:163], v[44:47], v[4:7]\n\t" \
    "ds_read_b128 v[40:43], %[xp] offset:29696\n\t" \
    "v_mfma_f32_16x16x32_bf16 v[8:11], v[152:155], v[48:51], v[8:11]\n\t" \
    "s_waitcnt lgkmcnt(4)\n\t" \
    "v_mfma_f32_16x16x32_bf16 v[12:15], v[160:163], v[48:51], v[12:15]\n\t" \
    "v_mfma_f32_16x16x32_bf16 v[0:3], v[164:167], v[20:23], v[0:3]\n\t" \
    "ds_read_b128 v[44:47], %[xp] offset:30720\n\t" \
    "v_mfma_f32_16x16x32_bf16 v[4:7], v[172:175], v[20:23], v[4:7]\n\t" \
    "ds_read_b128 v[48:51], %[xp] offset:31744\n\t" \
    "v_mfma_f32_16x16x32_bf16 v[8:11], v[164:167], v[24:27], v[8:11]\n\t" \
    "s_waitcnt lgkmcnt(4)\n\t" \
    "v_mfma_f32_16x16x32_bf16 v[12:15], v[172:175], v[24:27], v[12:15]\n\t" \
    "v_mfma_f32_16x16x32_bf16 v[0:3], v[168:171], v[28:31], v[0:3]\n\t" \
    "v_mfma_f32_16x16x32_bf16 v[4:7], v[176:179], v[28:31], v[4:7]\n\t" \
    "v_mfma_f32_16x16x32_bf16 v[8:11], v[168:171], v[32:35], v[8:11]\n\t" \
    "s_waitcnt lgkmcnt(2)\n\t" \
    "v_mfma_f32_16x16x32_bf16 v[12:15], v[176:179], v[32:35], v[12:15]\n\t" \
    "v_mfma_f32_16x16x32_bf16 v[0:3], v[180:183], v[36:39], v[0:3]\n\t" \
    "v_mfma_f32_16x16x32_bf16 v[4:7], v[188:191], v[36:39], v[4:7]\n\t" \
    "v_mfma_f32_16x16x32_bf16 v[8:11], v[180:183], v[40:43], v[8:11]\n\t" \
    "s_waitcnt lgkmcnt(0)\n\t" \
    "v_mfma_f32_16x16x32_bf16 v[12:15], v[188:191], v[40:43], v[12:15]\n\t" \
    "v_mfma_f32_16x16x32_bf16 v[0:3], v[184:187], v[44:47], v[0:3]\n\t" \
    "v_mfma_f32_16x16x32_bf16 v[4:7], v[192:195], v[44:47], v[4:7]\n\t" \
    "v_mfma_f32_16x16x32_bf16 v[8:11], v[184:187], v[48:51], v[8:11]\n\t" \
    "v_mfma_f32_16x16x32_bf16 v[12:15], v[192:195], v[48:51], v[12:15]\n\t" \
    "s_nop 7\n\t" \
    : "=&{v[0:3]}"(a[0]), "=&{v[4:7]}"(a[1]), "=&{v[8:11]}"(a[2]), "=&{v[12:15]}"(a[3]) \
    : [xp] "v"(XP_), "{v[68:75]}"(w[0][0]), "{v[76:83]}"(w[0][1]), "{v[84:91]}"(w[1][0]), "{v[92:99]}"(w[1][1]), "{v[100:107]}"(w[2][0]), "{v[108:115]}"(w[2][1]), "{v[116:123]}"(w[3][0]), "{v[124:131]}"(w[3][1]), "{v[132:139]}"(w[4][0]), "{v[140:147]}"(w[4][1]), "{v[148:155]}"(w[5][0]), "{v[156:163]}"(w[5][1]), "{v[164:171]}"(w[6][0]), "{v[172:179]}"(w[6][1]), "{v[180:187]}"(w[7][0]), "{v[188:195]}"(w[7][1]) \
    : "memory", "v20", "v21", "v22", "v23", "v24", "v25", "v26", "v27", "v28", "v29", "v30", "v31", "v32", "v33", "v34", "v35", "v36", "v37", "v38", "v39", "v40", "v41", "v42", "v43", "v44", "v45", "v46", "v47", "v48", "v49", "v50", "v51")

#define ONE_V(XP_) \
  asm volatile( \
    "ds_read_b128 v[20:23], %[xp] offset:0\n\t" \
    "ds_read_b128 v[24:27], %[xp] offset:1024\n\t" \
    "ds_read_b128 v[28:31], %[xp] offset:2048\n\t" \
    "ds_read_b128 v[32:35], %[xp] offset:3072\n\t" \
    "ds_read_b128 v[36:39], %[xp] offset:4096\n\t" \
    "ds_read_b128 v[40:43], %[xp] offset:5120\n\t" \
    "s_waitcnt lgkmcnt(4)\n\t" \
    "v_mfma_f32_16x16x32_bf16 v[0:3], v[20:23], v[68:71], 0\n\t" \
    "ds_read_b128 v[44:47], %[xp] offset:6144\n\t" \
    "v_mfma_f32_16x16x32_bf16 v[4:7], v[20:23], v[76:79], 0\n\t" \
    "ds_read_b128 v[48:51], %[xp] offset:7168\n\t" \
    "v_mfma_f32_16x16x32_bf16 v[8:11], v[24:27], v[68:71], 0\n\t" \
    "s_waitcnt lgkmcnt(4)\n\t" \
    "v_mfma_f32_16x16x32_bf16 v[12:15], v[24:27], v[76:79], 0\n\t" \
    "v_mfma_f32_16x16x32_bf16 v[0:3], v[28:31], v[72:75], v[0:3]\n\t" \
    "ds_read_b128 v[20:23], %[xp] offset:8192\n\t" \
    "v_mfma_f32_16x16x32_bf16 v[4:7], v[28:31], v[80:83], v[4:7]\n\t" \
    "ds_read_b128 v[24:27], %[xp] offset:9216\n\t" \
    "v_mfma_f32_16x16x32_bf16 v[8:11], v[32:35], v[72:75], v[8:11]\n\t" \
    "s_waitcnt lgkmcnt(4)\n\t" \
    "v_mfma_f32_16x16x32_bf16 v[12:15], v[32:35], v[80:83], v[12:15]\n\t" \
    "v_mfma_f32_16x16x32_bf16 v[0:3], v[36:39], v[84:87], v[0:3]\n\t" \
    "ds_read_b128 v[28:31], %[xp] offset:10240\n\t" \
    "v_mfma_f32_16x16x32_bf16 v[4:7], v[36:39], v[92:95], v[4:7]\n\t" \
    "ds_read_b128 v[32:35], %[xp] offset:11264\n\t" \
    "v_mfma_f32_16x16x32_bf16 v[8:11], v[40:43], v[84:87], v[8:11]\n\t" \
    "s_waitcnt lgkmcnt(4)\n\t" \
    "v_mfma_f32_16x16x32_bf16 v[12:15], v[40:43], v[92:95], v[12:15]\n\t" \
    "v_mfma_f32_16x16x32_bf16 v[0:3], v[44:47], v[88:91], v[0:3]\n\t" \
    "ds_read_b128 v[36:39], %[xp] offset:12288\n\t" \
    "v_mfma_f32_16x16x32_bf16 v[4:7], v[44:47], v[96:99], v[4:7]\n\t" \
    "ds_read_b128 v[40:43], %[xp] offset:13312\n\t" \
    "v_mfma_f32_16x16x32_bf16 v[8:11], v[48:51], v[88:91], v[8:11]\n\t" \
    "s_waitcnt lgkmcnt(4)\n\t" \
    "v_mfma_f32_16x16x32_bf16 v[12:15], v[48:51], v[96:99], v[12:15]\n\t" \
    "v_mfma_f32_16x16x32_bf16 v[0:3], v[20:23], v[100:103], v[0:3]\n\t" \
    "ds_read_b128 v[44:47], %[xp] offset:14336\n\t" \
    "v_mfma_f32_16x16x32_bf16 v[4:7], v[20:23], v[108:111], v[4:7]\n\t" \
    "ds_read_b128 v[48:51], %[xp] offset:15360\n\t" \
    "v_mfma_f32_16x16x32_bf16 v[8:11], v[24:27], v[100:103], v[8:11]\n\t" \
    "s_waitcnt lgkmcnt(4)\n\t" \
    "v_mfma_f32_16x16x32_bf16 v[12:15], v[24:27], v[108:111], v[12:15]\n\t" \
    "s_waitcnt vmcnt(8)\n\t" \
    "s_barrier\n\t" \
    "v_mfma_f32_16x16x32_bf16 v[0:3], v[28:31], v[104:107], v[0:3]\n\t" \
    "ds_read_b128 v[20:23], %[xp] offset:16384\n\t" \
    "v_mfma_f32_16x16x32_bf16 v[4:7], v[28:31], v[112:115], v[4:7]\n\t" \
    "ds_read_b128 v[24:27], %[xp] offset:17408\n\t" \
    "v_mfma_f32_16x16x32_bf16 v[8:11], v[32:35], v[104:107], v[8:11]\n\t" \
    "s_waitcnt lgkmcnt(4)\n\t" \
    "v_mfma_f32_16x16x32_bf16 v[12:15], v[32:35], v[112:115], v[12:15]\n\t" \
    "v_mfma_f32_16x16x32_bf16 v[0:3], v[36:39], v[116:119], v[0:3]\n\t" \
    "ds_read_b128 v[28:31], %[xp] offset:18432\n\t" \
    "v_mfma_f32_16x16x32_bf16 v[4:7], v[36:39], v[124:127], v[4:7]\n\t" \
    "ds_read_b128 v[32:35], %[xp] offset:19456\n\t" \
    "v_mfma_f32_16x16x32_bf16 v[8:11], v[40:43], v[116:119], v[8:11]\n\t" \
    "s_waitcnt lgkmcnt(4)\n\t" \
    "v_mfma_f32_16x16x32_bf16 v[12:15], v[40:43], v[124:127], v[12:15]\n\t" \
    "v_mfma_f32_16x16x32_bf16 v[0:3], v[44:47], v[120:123], v[0:3]\n\t" \
    "ds_read_b128 v[36:39], %[xp] offset:20480\n\t" \
    "v_mfma_f32_16x16x32_bf16 v[4:7], v[44:47], v[128:131], v[4:7]\n\t" \
    "ds_read_b128 v[40:43], %[xp] offset:21504\n\t" \
    "v_mfma_f32_16x16x32_bf16 v[8:11], v[48:51], v[120:123], v[8:11]\n\t" \
    "s_waitcnt lgkmcnt(4)\n\t" \
    "v_mfma_f32_16x16x32_bf16 v[12:15], v[48:51], v[128:131], v[12:15]\n\t" \
    "v_mfma_f32_16x16x32_bf16 v[0:3], v[20:23], v[132:135], v[0:3]\n\t" \
    "ds_read_b128 v[44:47], %[xp] offset:22528\n\t" \
    "v_mfma_f32_16x16x32_bf16 v[4:7], v[20:23], v[140:143], v[4:7]\n\t" \
    "ds_read_b128 v[48:51], %[xp] offset:23552\n\t" \
    "v_mfma_f32_16x16x32_bf16 v[8:11], v[24:27], v[132:135], v[8:11]\n\t" \
    "s_waitcnt lgkmcnt(4)\n\t" \
    "v_mfma_f32_16x16x32_bf16 v[12:15], v[24:27], v[140:143], v[12:15]\n\t" \
    "v_mfma_f32_16x16x32_bf16 v[0:3], v[28:31], v[136:139], v[0:3]\n\t" \
    "ds_read_b128 v[20:23], %[xp] offset:24576\n\t" \
    "v_mfma_f32_16x16x32_bf16 v[4:7], v[28:31], v[144:147], v[4:7]\n\t" \
    "ds_read_b128 v[24:27], %[xp] offset:25600\n\t" \
    "v_mfma_f32_16x16x32_bf16 v[8:11], v[32:35], v[136:139], v[8:11]\n\t" \
    "s_waitcnt lgkmcnt(4)\n\t" \
    "v_mfma_f32_16x16x32_bf16 v[12:15], v[32:35], v[144:147], v[12:15]\n\t" \
    "v_mfma_f32_16x16x32_bf16 v[0:3], v[36:39], v[148:151], v[0:3]\n\t" \
    "ds_read_b128 v[28:31], %[xp] offset:26624\n\t" \
    "v_mfma_f32_16x16x32_bf16 v[4:7], v[36:39], v[156:159], v[4:7]\n\t" \
    "ds_read_b128 v[32:35], %[xp] offset:27648\n\t" \
    "v_mfma_f32_16x16x32_bf16 v[8:11], v[40:43], v[148:151], v[8:11]\n\t" \
    "s_waitcnt lgkmcnt(4)\n\t" \
    "v_mfma_f32_16x16x32_bf16 v[12:15], v[40:43], v[156:159], v[12:15]\n\t" \
    "v_mfma_f32_16x16x32_bf16 v[0:3], v[44:47], v[152:155], v[0:3]\n\t" \
    "ds_read_b128 v[36:39], %[xp] offset:28672\n\t" \
    "v_mfma_f32_16x16x32_bf16 v[4:7], v[44:47], v[160:163], v[4:7]\n\t" \
    "ds_read_b128 v[40:43], %[xp] offset:29696\n\t" \
    "v_mfma_f32_16x16x32_bf16 v[8:11], v[48:51], v[152:155], v[8:11]\n\t" \
    "s_waitcnt lgkmcnt(4)\n\t" \
    "v_mfma_f32_16x16x32_bf16 v[12:15], v[48:51], v[160:163], v[12:15]\n\t" \
    "v_mfma_f32_16x16x32_bf16 v[0:3], v[20:23], v[164:167], v[0:3]\n\t" \
    "ds_read_b128 v[44:47], %[xp] offset:30720\n\t" \
    "v_mfma_f32_16x16x32_bf16 v[4:7], v[20:23], v[172:175], v[4:7]\n\t" \
    "ds_read_b128 v[48:51], %[xp] offset:31744\n\t" \
    "v_mfma_f32_16x16x32_bf16 v[8:11], v[24:27], v[164:167], v[8:11]\n\t" \
    "s_waitcnt lgkmcnt(4)\n\t" \
    "v_mfma_f32_16x16x32_bf16 v[12:15], v[24:27], v[172:175], v[12:15]\n\t" \
    "v_mfma_f32_16x16x32_bf16 v[0:3], v[28:31], v[168:171], v[0:3]\n\t" \
    "v_mfma_f32_16x16x32_bf16 v[4:7], v[28:31], v[176:179], v[4:7]\n\t" \
    "v_mfma_f32_16x16x32_bf16 v[8:11], v[32:35], v[168:171], v[8:11]\n\t" \
    "s_waitcnt lgkmcnt(2)\n\t" \
    "v_mfma_f32_16x16x32_bf16 v[12:15], v[32:35], v[176:179], v[12:15]\n\t" \
    "v_mfma_f32_16x16x32_bf16 v[0:3], v[36:39], v[180:183], v[0:3]\n\t" \
    "v_mfma_f32_16x16x32_bf16 v[4:7], v[36:39], v[188:191], v[4:7]\n\t" \
    "v_mfma_f32_16x16x32_bf16 v[8:11], v[40:43], v[180:183], v[8:11]\n\t" \
    "s_waitcnt lgkmcnt(0)\n\t" \
    "v_mfma_f32_16x16x32_bf16 v[12:15], v[40:43], v[188:191], v[12:15]\n\t" \
    "v_mfma_f32_16x16x32_bf16 v[0:3], v[44:47], v[184:187], v[0:3]\n\t" \
    "v_mfma_f32_16x16x32_bf16 v[4:7], v[44:47], v[192:195], v[4:7]\n\t" \
    "v_mfma_f32_16x16x32_bf16 v[8:11], v[48:51], v[184:187], v[8:11]\n\t" \
    "v_mfma_f32_16x16x32_bf16 v[12:15], v[48:51], v[192:195], v[12:15]\n\t" \
    "s_nop 7\n\t" \
    : "=&{v[0:3]}"(a[0]), "=&{v[4:7]}"(a[1]), "=&{v[8:11]}"(a[2]), "=&{v[12:15]}"(a[3]) \
    : [xp] "v"(XP_), "{v[68:75]}"(w[0][0]), "{v[76:83]}"(w[0][1]), "{v[84:91]}"(w[1][0]), "{v[92:99]}"(w[1][1]), "{v[100:107]}"(w[2][0]), "{v[108:115]}"(w[2][1]), "{v[116:123]}"(w[3][0]), "{v[124:131]}"(w[3][1]), "{v[132:139]}"(w[4][0]), "{v[140:147]}"(w[4][1]), "{v[148:155]}"(w[5][0]), "{v[156:163]}"(w[5][1]), "{v[164:171]}"(w[6][0]), "{v[172:179]}"(w[6][1]), "{v[180:187]}"(w[7][0]), "{v[188:195]}"(w[7][1]) \
    : "memory", "v20", "v21", "v22", "v23", "v24", "v25", "v26", "v27", "v28", "v29", "v30", "v31", "v32", "v33", "v34", "v35", "v36", "v37", "v38", "v39", "v40", "v41", "v42", "v43", "v44", "v45", "v46", "v47", "v48", "v49", "v50", "v51")

#define H_LOOP1() \
  asm volatile( \
    "v_mov_b32 v20, 0\n\t" \
    "v_mov_b32 v21, 0\n\t" \
    "v_mov_b32 v22, 0\n\t" \
    "v_mov_b32 v23, 0\n\t" \
    "v_mov_b32 v24, 0\n\t" \
    "v_mov_b32 v25, 0\n\t" \
    "v_mov_b32 v26, 0\n\t" \
    "v_mov_b32 v27, 0\n\t" \
    "v_mov_b32 v28, 0\n\t" \
    "v_mov_b32 v29, 0\n\t" \
    "v_mov_b32 v30, 0\n\t" \
    "v_mov_b32 v31, 0\n\t" \
    "v_mov_b32 v32, 0\n\t" \
    "v_mov_b32 v33, 0\n\t" \
    "v_mov_b32 v34, 0\n\t" \
    "v_mov_b32 v35, 0\n\t" \
    "v_mov_b32 v36, 0\n\t" \
    "v_mov_b32 v37, 0\n\t" \
    "v_mov_b32 v38, 0\n\t" \
    "v_mov_b32 v39, 0\n\t" \
    "v_mov_b32 v40, 0\n\t" \
    "v_mov_b32 v41, 0\n\t" \
    "v_mov_b32 v42, 0\n\t" \
    "v_mov_b32 v43, 0\n\t" \
    "v_mov_b32 v44, 0\n\t" \
    "v_mov_b32 v45, 0\n\t" \
    "v_mov_b32 v46, 0\n\t" \
    "v_mov_b32 v47, 0\n\t" \
    "v_mov_b32 v48, 0\n\t" \
    "v_mov_b32 v49, 0\n\t" \
    "v_mov_b32 v50, 0\n\t" \
    "v_mov_b32 v51, 0\n\t" \
    "v_mov_b32 v52, 0\n\t" \
    "v_mov_b32 v53, 0\n\t" \
    "v_mov_b32 v54, 0\n\t" \
    "v_mov_b32 v55, 0\n\t" \
    "v_mov_b32 v56, 0\n\t" \
    "v_mov_b32 v57, 0\n\t" \
    "v_mov_b32 v58, 0\n\t" \
    "v_mov_b32 v59, 0\n\t" \
    "v_mov_b32 v60, 0\n\t" \
    "v_mov_b32 v61, 0\n\t" \
    "v_mov_b32 v62, 0\n\t" \
    "v_mov_b32 v63, 0\n\t" \
    "v_mov_b32 v64, 0\n\t" \
    "v_mov_b32 v65, 0\n\t" \
    "v_mov_b32 v66, 0\n\t" \
    "v_mov_b32 v67, 0\n\t" \
    "v_mov_b32 v68, 0\n\t" \
    "v_mov_b32 v69, 0\n\t" \
    "v_mov_b32 v70, 0\n\t" \
    "v_mov_b32 v71, 0\n\t" \
    "v_mov_b32 v72, 0\n\t" \
    "v_mov_b32 v73, 0\n\t" \
    "v_mov_b32 v74, 0\n\t" \
    "v_mov_b32 v75, 0\n\t" \
    "v_mov_b32 v76, 0\n\t" \
    "v_mov_b32 v77, 0\n\t" \
    "v_mov_b32 v78, 0\n\t" \
    "v_mov_b32 v79, 0\n\t" \
    "v_mov_b32 v80, 0\n\t" \
    "v_mov_b32 v81, 0\n\t" \
    "v_mov_b32 v82, 0\n\t" \
    "v_mov_b32 v83, 0\n\t" \
    "s_waitcnt vmcnt(8)\n\t" \
    "s_barrier\n\t" \
    "ds_read_b64_tr_b16 v[116:117], %[b0] offset:0\n\t" \
    "ds_read_b64_tr_b16 v[118:119], %[b0] offset:1024\n\t" \
    "ds_read_b64_tr_b16 v[124:125], %[b1] offset:0\n\t" \
    "ds_read_b64_tr_b16 v[126:127], %[b1] offset:1024\n\t" \
    "ds_read_b64_tr_b16 v[120:121], %[b0] offset:8192\n\t" \
    "ds_read_b64_tr_b16 v[122:123], %[b0] offset:9216\n\t" \
    "ds_read_b64_tr_b16 v[128:129], %[b1] offset:8192\n\t" \
    "ds_read_b64_tr_b16 v[130:131], %[b1] offset:9216\n\t" \
    "ds_read_b128 v[84:87], %[a] offset:0\n\t" \
    "ds_read_b128 v[88:91], %[a] offset:1024\n\t" \
    "ds_read_b128 v[92:95], %[a] offset:2048\n\t" \
    "ds_read_b128 v[96:99], %[a] offset:3072\n\t" \
    "ds_read_b128 v[100:103], %[a] offset:4096\n\t" \
    "ds_read_b128 v[104:107], %[a] offset:5120\n\t" \
    "ds_read_b128 v[108:111], %[a] offset:6144\n\t" \
    "ds_read_b128 v[112:115], %[a] offset:7168\n\t" \
    "s_add_u32 %[d], %[lw], %[c]\n\t" \
    "s_add_u32 %[y], %[c], 49152\n\t" \
    "s_cmp_eq_u32 %[y], 147456\n\t" \
    "s_cselect_b32 %[y], 0, %[y]\n\t" \
    "s_sub_u32 %[z], %[y], %[c]\n\t" \
    "s_mov_b32 %[c], %[y]\n\t" \
    "s_add_u32 %[x], %[t], 3\n\t" \
    "s_sub_u32 %[y], %[x], %[nt]\n\t" \
    "s_cmp_lt_u32 %[x], %[nt]\n\t" \
    "s_cselect_b32 %[x], %[x], %[y]\n\t" \
    "s_cselect_b32 m0, %[cb], %[cn]\n\t" \
    "s_mul_i32 %[y], %[x], %[kb]\n\t" \
    "s_add_u32 %[y], %[y], m0\n\t" \
    "s_lshl_b32 %[x], %[x], 11\n\t" \
    "s_add_u32 %[x], %[x], %[ca]\n\t" \
    "v_add_u32 %[a], %[z], %[a]\n\t" \
    "v_add_u32 %[b0], %[z], %[b0]\n\t" \
    "v_add_u32 %[b1], %[z], %[b1]\n\t" \
    "L_%=:\n\t" \
    "s_waitcnt vmcnt(4) lgkmcnt(0)\n\t" \
    "s_barrier\n\t" \
    "v_mfma_f32_16x16x32_bf16 v[20:23], v[116:119], v[84:87], v[20:23]\n\t" \
    "ds_read_b64_tr_b16 v[180:181], %[b0] offset:0\n\t" \
    "ds_read_b64_tr_b16 v[182:183], %[b0] offset:1024\n\t" \
    "v_mfma_f32_16x16x32_bf16 v[24:27], v[124:127], v[84:87], v[24:27]\n\t" \
    "ds_read_b64_tr_b16 v[188:189], %[b1] offset:0\n\t" \
    "ds_read_b64_tr_b16 v[190:191], %[b1] offset:1024\n\t" \
    "s_add_u32 m0, %[d], 0\n\t" \
    "v_mfma_f32_16x16x32_bf16 v[20:23], v[120:123], v[88:91], v[20:23]\n\t" \
    "buffer_load_dwordx4 %[vb0], %[rb], %[y] offen lds\n\t" \
    "v_mfma_f32_16x16x32_bf16 v[24:27], v[128:131], v[88:91], v[24:27]\n\t" \
    "ds_read_b64_tr_b16 v[184:185], %[b0] offset:8192\n\t" \
    "ds_read_b64_tr_b16 v[186:187], %[b0] offset:9216\n\t" \
    "s_add_u32 m0, %[d], 8192\n\t" \
    "v_mfma_f32_16x16x32_bf16 v[28:31], v[116:119], v[92:95], v[28:31]\n\t" \
    "buffer_load_dwordx4 %[vb1], %[rb], %[y] offen lds\n\t" \
    "v_mfma_f32_16x16x32_bf16 v[32:35], v[124:127], v[92:95], v[32:35]\n\t" \
    "ds_read_b64_tr_b16 v[192:193], %[b1] offset:8192\n\t" \
    "ds_read_b64_tr_b16 v[194:195], %[b1] offset:9216\n\t" \
    "s_add_u32 m0, %[d], 32768\n\t" \
    "v_mfma_f32_16x16x32_bf16 v[28:31], v[120:123], v[96:99], v[28:31]\n\t" \
    "buffer_load_dwordx4 %[va0], %[ra], %[x] offen sc1 lds\n\t" \
    "v_mfma_f32_16x16x32_bf16 v[32:35], v[128:131], v[96:99], v[32:35]\n\t" \
    "ds_read_b128 v[148:151], %[a] offset:0\n\t" \
    "s_add_u32 m0, %[d], 40960\n\t" \
    "v_mfma_f32_16x16x32_bf16 v[36:39], v[116:119], v[100:103], v[36:39]\n\t" \
    "buffer_load_dwordx4 %[va1], %[ra], %[x] offen sc1 lds\n\t" \
    "v_mfma_f32_16x16x32_bf16 v[40:43], v[124:127], v[100:103], v[40:43]\n\t" \
    "ds_read_b128 v[152:155], %[a] offset:1024\n\t" \
    "ds_read_b128 v[156:159], %[a] offset:2048\n\t" \
    "s_add_u32 %[t], %[t], 1\n\t" \
    "s_add_u32 %[d], %[lw], %[c]\n\t" \
    "s_add_u32 %[y], %[c], 49152\n\t" \
    "v_mfma_f32_16x16x32_bf16 v[36:39], v[120:123], v[104:107], v[36:39]\n\t" \
    "ds_read_b128 v[160:163], %[a] offset:3072\n\t" \
    "ds_read_b128 v[164:167], %[a] offset:4096\n\t" \
    "s_cmp_eq_u32 %[y], 147456\n\t" \
    "s_cselect_b32 %[y], 0, %[y]\n\t" \
    "s_sub_u32 %[z], %[y], %[c]\n\t" \
    "v_mfma_f32_16x16x32_bf16 v[40:43], v[128:131], v[104:107], v[40:43]\n\t" \
    "ds_read_b128 v[168:171], %[a] offset:5120\n\t" \
    "ds_read_b128 v[172:175], %[a] offset:6144\n\t" \
    "s_mov_b32 %[c], %[y]\n\t" \
    "s_add_u32 %[x], %[t], 3\n\t" \
    "s_sub_u32 %[y], %[x], %[nt]\n\t" \
    "v_mfma_f32_16x16x32_bf16 v[44:47], v[116:119], v[108:111], v[44:47]\n\t" \
    "ds_read_b128 v[176:179], %[a] offset:7168\n\t" \
    "s_cmp_lt_u32 %[x], %[nt]\n\t" \
    "s_cselect_b32 %[x], %[x], %[y]\n\t" \
    "s_cselect_b32 m0, %[cb], %[cn]\n\t" \
    "v_mfma_f32_16x16x32_bf16 v[48:51], v[124:127], v[108:111], v[48:51]\n\t" \
    "s_mul_i32 %[y], %[x], %[kb]\n\t" \
    "s_add_u32 %[y], %[y], m0\n\t" \
    "s_lshl_b32 %[x], %[x], 11\n\t" \
    "v_mfma_f32_16x16x32_bf16 v[44:47], v[120:123], v[112:115], v[44:47]\n\t" \
    "s_add_u32 %[x], %[x], %[ca]\n\t" \
    "v_mfma_f32_16x16x32_bf16 v[48:51], v[128:131], v[112:115], v[48:51]\n\t" \
    "v_add_u32 %[a], %[z], %[a]\n\t" \
    "v_add_u32 %[b0], %[z], %[b0]\n\t" \
    "v_add_u32 %[b1], %[z], %[b1]\n\t" \
    "s_waitcnt vmcnt(4) lgkmcnt(0)\n\t" \
    "s_barrier\n\t" \
    "v_mfma_f32_16x16x32_bf16 v[20:23], v[180:183], v[148:151], v[20:23]\n\t" \
    "ds_read_b64_tr_b16 v[116:117], %[b0] offset:0\n\t" \
    "ds_read_b64_tr_b16 v[118:119], %[b0] offset:1024\n\t" \
    "v_mfma_f32_16x16x32_bf16 v[24:27], v[188:191], v[148:151], v[24:27]\n\t" \
    "ds_read_b64_tr_b16 v[124:125], %[b1] offset:0\n\t" \
    "ds_read_b64_tr_b16 v[126:127], %[b1] offset:1024\n\t" \
    "s_add_u32 m0, %[d], 0\n\t" \
    "v_mfma_f32_16x16x32_bf16 v[20:23], v[184:187], v[152:155], v[20:23]\n\t" \
    "buffer_load_dwordx4 %[vb0], %[rb], %[y] offen lds\n\t" \
    "v_mfma_f32_16x16x32_bf16 v[24:27], v[192:195], v[152:155], v[24:27]\n\t" \
    "ds_read_b64_tr_b16 v[120:121], %[b0] offset:8192\n\t" \
    "ds_read_b64_tr_b16 v[122:123], %[b0] offset:9216\n\t" \
    "s_add_u32 m0, %[d], 8192\n\t" \
    "v_mfma_f32_16x16x32_bf16 v[28:31], v[180:183], v[156:159], v[28:31]\n\t" \
    "buffer_load_dwordx4 %[vb1], %[rb], %[y] offen lds\n\t" \
    "v_mfma_f32_16x16x32_bf16 v[32:35], v[188:191], v[156:159], v[32:35]\n\t" \
    "ds_read_b64_tr_b16 v[128:129], %[b1] offset:8192\n\t" \
    "ds_read_b64_tr_b16 v[130:131], %[b1] offset:9216\n\t" \
    "s_add_u32 m0, %[d], 32768\n\t" \
    "v_mfma_f32_16x16x32_bf16 v[28:31], v[184:187], v[160:163], v[28:31]\n\t" \
    "buffer_load_dwordx4 %[va0], %[ra], %[x] offen sc1 lds\n\t" \
    "v_mfma_f32_16x16x32_bf16 v[32:35], v[192:195], v[160:163], v[32:35]\n\t" \
    "ds_read_b128 v[84:87], %[a] offset:0\n\t" \
    "s_add_u32 m0, %[d], 40960\n\t" \
    "v_mfma_f32_16x16x32_bf16 v[36:39], v[180:183], v[164:167], v[36:39]\n\t" \
    "buffer_load_dwordx4 %[va1], %[ra], %[x] offen sc1 lds\n\t" \
    "v_mfma_f32_16x16x32_bf16 v[40:43], v[188:191], v[164:167], v[40:43]\n\t" \
    "ds_read_b128 v[88:91], %[a] offset:1024\n\t" \
    "ds_read_b128 v[92:95], %[a] offset:2048\n\t" \
    "s_add_u32 %[t], %[t], 1\n\t" \
    "s_add_u32 %[d], %[lw], %[c]\n\t" \
    "s_add_u32 %[y], %[c], 49152\n\t" \
    "v_mfma_f32_16x16x32_bf16 v[36:39], v[184:187], v[168:171], v[36:39]\n\t" \
    "ds_read_b128 v[96:99], %[a] offset:3072\n\t" \
    "ds_read_b128 v[100:103], %[a] offset:4096\n\t" \
    "s_cmp_eq_u32 %[y], 147456\n\t" \
    "s_cselect_b32 %[y], 0, %[y]\n\t" \
    "s_sub_u32 %[z], %[y], %[c]\n\t" \
    "v_mfma_f32_16x16x32_bf16 v[40:43], v[192:195], v[168:171], v[40:43]\n\t" \
    "ds_read_b128 v[104:107], %[a] offset:5120\n\t" \
    "ds_read_b128 v[108:111], %[a] offset:6144\n\t" \
    "s_mov_b32 %[c], %[y]\n\t" \
    "s_add_u32 %[x], %[t], 3\n\t" \
    "s_sub_u32 %[y], %[x], %[nt]\n\t" \
    "v_mfma_f32_16x16x32_bf16 v[44:47], v[180:183], v[172:175], v[44:47]\n\t" \
    "ds_read_b128 v[112:115], %[a] offset:7168\n\t" \
    "s_cmp_lt_u32 %[x], %[nt]\n\t" \
    "s_cselect_b32 %[x], %[x], %[y]\n\t" \
    "s_cselect_b32 m0, %[cb], %[cn]\n\t" \
    "v_mfma_f32_16x16x32_bf16 v[48:51], v[188:191], v[172:175], v[48:51]\n\t" \
    "s_mul_i32 %[y], %[x], %[kb]\n\t" \
    "s_add_u32 %[y], %[y], m0\n\t" \
    "s_lshl_b32 %[x], %[x], 11\n\t" \
    "v_mfma_f32_16x16x32_bf16 v[44:47], v[184:187], v[176:179], v[44:47]\n\t" \
    "s_add_u32 %[x], %[x], %[ca]\n\t" \
    "s_cmp_lt_u32 %[t], %[nt]\n\t" \
    "v_mfma_f32_16x16x32_bf16 v[48:51], v[192:195], v[176:179], v[48:51]\n\t" \
    "v_add_u32 %[a], %[z], %[a]\n\t" \
    "v_add_u32 %[b0], %[z], %[b0]\n\t" \
    "v_add_u32 %[b1], %[z], %[b1]\n\t" \
    "s_cbranch_scc1 L_%=\n\t" \
    "s_waitcnt lgkmcnt(0)\n\t" \
    "s_nop 7\n\t" \
    : "=&{v[20:23]}"(acc[0][0][0][0]), "=&{v[24:27]}"(acc[0][0][0][1]), "=&{v[28:31]}"(acc[0][0][1][0]), "=&{v[32:35]}"(acc[0][0][1][1]), "=&{v[36:39]}"(acc[0][0][2][0]), "=&{v[40:43]}"(acc[0][0][2][1]), "=&{v[44:47]}"(acc[0][0][3][0]), "=&{v[48:51]}"(acc[0][0][3][1]), "=&{v[52:55]}"(acc[0][1][0][0]), "=&{v[56:59]}"(acc[0][1][0][1]), "=&{v[60:63]}"(acc[0][1][1][0]), "=&{v[64:67]}"(acc[0][1][1][1]), "=&{v[68:71]}"(acc[0][1][2][0]), "=&{v[72:75]}"(acc[0][1][2][1]), "=&{v[76:79]}"(acc[0][1][3][0]), "=&{v[80:83]}"(acc[0][1][3][1]), [a] "+v"(aoff), [b0] "+v"(boff0), [b1] "+v"(boff1), [c] "+s"(cur), [t] "+s"(t), [d] "=&s"(sd), [x] "=&s"(sx), [y] "=&s"(sy), [z] "=&s"(sz) \
    : [va0] "v"(voffA[0]), [va1] "v"(voffA[1]), [vb0] "v"(voffB[0]), [vb1] "v"(voffB[1]), [ra] "s"(ra), [rb] "s"(rb), [lw] "s"(ldsw), [nt] "s"(nt), [kb] "s"(kB), [ca] "s"(cA), [cb] "s"(cB), [cn] "s"(cN) \
    : "memory", "scc", "v84", "v85", "v86", "v87", "v88", "v89", "v90", "v91", "v92", "v93", "v94", "v95", "v96", "v97", "v98", "v99", "v100", "v101", "v102", "v103", "v104", "v105", "v106", "v107", "v108", "v109", "v110", "v111", "v112", "v113", "v114", "v115", "v116", "v117", "v118", "v119", "v120", "v121", "v122", "v123", "v124", "v125", "v126", "v127", "v128", "v129", "v130", "v131", "v132", "v133", "v134", "v135", "v136", "v137", "v138", "v139", "v140", "v141", "v142", "v143", "v144", "v145", "v146", "v147", "v148", "v149", "v150", "v151", "v152", "v153", "v154", "v155", "v156", "v157", "v158", "v159", "v160", "v161", "v162", "v163", "v164", "v165", "v166", "v167", "v168", "v169", "v170", "v171", "v172", "v173", "v174", "v175", "v176", "v177", "v178", "v179", "v180", "v181", "v182", "v183", "v184", "v185", "v186", "v187", "v188", "v189", "v190", "v191", "v192", "v193", "v194", "v195", "v196", "v197", "v198", "v199", "v200", "v201", "v202", "v203", "v204", "v205", "v206", "v207", "v208", "v209", "v210", "v211")

#define H_LOOP2() \
  asm volatile( \
    "v_mov_b32 v20, 0\n\t" \
    "v_mov_b32 v21, 0\n\t" \
    "v_mov_b32 v22, 0\n\t" \
    "v_mov_b32 v23, 0\n\t" \
    "v_mov_b32 v24, 0\n\t" \
    "v_mov_b32 v25, 0\n\t" \
    "v_mov_b32 v26, 0\n\t" \
    "v_mov_b32 v27, 0\n\t" \
    "v_mov_b32 v28, 0\n\t" \
    "v_mov_b32 v29, 0\n\t" \
    "v_mov_b32 v30, 0\n\t" \
    "v_mov_b32 v31, 0\n\t" \
    "v_mov_b32 v32, 0\n\t" \
    "v_mov_b32 v33, 0\n\t" \
    "v_mov_b32 v34, 0\n\t" \
    "v_mov_b32 v35, 0\n\t" \
    "v_mov_b32 v36, 0\n\t" \
    "v_mov_b32 v37, 0\n\t" \
    "v_mov_b32 v38, 0\n\t" \
    "v_mov_b32 v39, 0\n\t" \
    "v_mov_b32 v40, 0\n\t" \
    "v_mov_b32 v41, 0\n\t" \
    "v_mov_b32 v42, 0\n\t" \
    "v_mov_b32 v43, 0\n\t" \
    "v_mov_b32 v44, 0\n\t" \
    "v_mov_b32 v45, 0\n\t" \
    "v_mov_b32 v46, 0\n\t" \
    "v_mov_b32 v47, 0\n\t" \
    "v_mov_b32 v48, 0\n\t" \
    "v_mov_b32 v49, 0\n\t" \
    "v_mov_b32 v50, 0\n\t" \
    "v_mov_b32 v51, 0\n\t" \
    "v_mov_b32 v52, 0\n\t" \
    "v_mov_b32 v53, 0\n\t" \
    "v_mov_b32 v54, 0\n\t" \
    "v_mov_b32 v55, 0\n\t" \
    "v_mov_b32 v56, 0\n\t" \
    "v_mov_b32 v57, 0\n\t" \
    "v_mov_b32 v58, 0\n\t" \
    "v_mov_b32 v59, 0\n\t" \
    "v_mov_b32 v60, 0\n\t" \
    "v_mov_b32 v61, 0\n\t" \
    "v_mov_b32 v62, 0\n\t" \
    "v_mov_b32 v63, 0\n\t" \
    "v_mov_b32 v64, 0\n\t" \
    "v_mov_b32 v65, 0\n\t" \
    "v_mov_b32 v66, 0\n\t" \
    "v_mov_b32 v67, 0\n\t" \
    "v_mov_b32 v68, 0\n\t" \
    "v_mov_b32 v69, 0\n\t" \
    "v_mov_b32 v70, 0\n\t" \
    "v_mov_b32 v71, 0\n\t" \
    "v_mov_b32 v72, 0\n\t" \
    "v_mov_b32 v73, 0\n\t" \
    "v_mov_b32 v74, 0\n\t" \
    "v_mov_b32 v75, 0\n\t" \
    "v_mov_b32 v76, 0\n\t" \
    "v_mov_b32 v77, 0\n\t" \
    "v_mov_b32 v78, 0\n\t" \
    "v_mov_b32 v79, 0\n\t" \
    "v_mov_b32 v80, 0\n\t" \
    "v_mov_b32 v81, 0\n\t" \
    "v_mov_b32 v82, 0\n\t" \
    "v_mov_b32 v83, 0\n\t" \
    "s_waitcnt vmcnt(12)\n\t" \
    "s_barrier\n\t" \
    "ds_read_b64_tr_b16 v[116:117], %[b0] offset:0\n\t" \
    "ds_read_b64_tr_b16 v[118:119], %[b0] offset:1024\n\t" \
    "ds_read_b64_tr_b16 v[124:125], %[b1] offset:0\n\t" \
    "ds_read_b64_tr_b16 v[126:127], %[b1] offset:1024\n\t" \
    "ds_read_b64_tr_b16 v[120:121], %[b0] offset:8192\n\t" \
    "ds_read_b64_tr_b16 v[122:123], %[b0] offset:9216\n\t" \
    "ds_read_b64_tr_b16 v[128:129], %[b1] offset:8192\n\t" \
    "ds_read_b64_tr_b16 v[130:131], %[b1] offset:9216\n\t" \
    "ds_read_b128 v[84:87], %[a] offset:0\n\t" \
    "ds_read_b128 v[88:91], %[a] offset:1024\n\t" \
    "ds_read_b128 v[92:95], %[a] offset:2048\n\t" \
    "ds_read_b128 v[96:99], %[a] offset:3072\n\t" \
    "ds_read_b128 v[100:103], %[a] offset:4096\n\t" \
    "ds_read_b128 v[104:107], %[a] offset:5120\n\t" \
    "ds_read_b128 v[108:111], %[a] offset:6144\n\t" \
    "ds_read_b128 v[112:115], %[a] offset:7168\n\t" \
    "ds_read_b64_tr_b16 v[132:133], %[b0] offset:16384\n\t" \
    "ds_read_b64_tr_b16 v[134:135], %[b0] offset:17408\n\t" \
    "ds_read_b64_tr_b16 v[140:141], %[b1] offset:16384\n\t" \
    "ds_read_b64_tr_b16 v[142:143], %[b1] offset:17408\n\t" \
    "ds_read_b64_tr_b16 v[136:137], %[b0] offset:24576\n\t" \
    "ds_read_b64_tr_b16 v[138:139], %[b0] offset:25600\n\t" \
    "ds_read_b64_tr_b16 v[144:145], %[b1] offset:24576\n\t" \
    "ds_read_b64_tr_b16 v[146:147], %[b1] offset:25600\n\t" \
    "s_add_u32 %[d], %[lw], %[c]\n\t" \
    "s_add_u32 %[y], %[c], 49152\n\t" \
    "s_cmp_eq_u32 %[y], 147456\n\t" \
    "s_cselect_b32 %[y], 0, %[y]\n\t" \
    "s_sub_u32 %[z], %[y], %[c]\n\t" \
    "s_mov_b32 %[c], %[y]\n\t" \
    "s_add_u32 %[x], %[t], 3\n\t" \
    "s_sub_u32 %[y], %[x], %[nt]\n\t" \
    "s_cmp_lt_u32 %[x], %[nt]\n\t" \
    "s_cselect_b32 %[x], %[x], %[y]\n\t" \
    "s_cselect_b32 m0, %[cb], %[cn]\n\t" \
    "s_mul_i32 %[y], %[x], %[kb]\n\t" \
    "s_add_u32 %[y], %[y], m0\n\t" \
    "s_lshl_b32 %[x], %[x], 11\n\t" \
    "s_add_u32 %[x], %[x], %[ca]\n\t" \
    "v_add_u32 %[a], %[z], %[a]\n\t" \
    "v_add_u32 %[b0], %[z], %[b0]\n\t" \
    "v_add_u32 %[b1], %[z], %[b1]\n\t" \
    "L_%=:\n\t" \
    "s_waitcnt vmcnt(6) lgkmcnt(0)\n\t" \
    "s_barrier\n\t" \
    "v_mfma_f32_16x16x32_bf16 v[20:23], v[116:119], v[84:87], v[20:23]\n\t" \
    "ds_read_b64_tr_b16 v[180:181], %[b0] offset:0\n\t" \
    "ds_read_b64_tr_b16 v[182:183], %[b0] offset:1024\n\t" \
    "v_mfma_f32_16x16x32_bf16 v[24:27], v[124:127], v[84:87], v[24:27]\n\t" \
    "ds_read_b64_tr_b16 v[188:189], %[b1] offset:0\n\t" \
    "ds_read_b64_tr_b16 v[190:191], %[b1] offset:1024\n\t" \
    "v_mfma_f32_16x16x32_bf16 v[20:23], v[120:123], v[88:91], v[20:23]\n\t" \
    "ds_read_b64_tr_b16 v[184:185], %[b0] offset:8192\n\t" \
    "ds_read_b64_tr_b16 v[186:187], %[b0] offset:9216\n\t" \
    "s_add_u32 m0, %[d], 0\n\t" \
    "v_mfma_f32_16x16x32_bf16 v[24:27], v[128:131], v[88:91], v[24:27]\n\t" \
    "buffer_load_dwordx4 %[vb0], %[rb], %[y] offen lds\n\t" \
    "v_mfma_f32_16x16x32_bf16 v[28:31], v[116:119], v[92:95], v[28:31]\n\t" \
    "ds_read_b64_tr_b16 v[192:193], %[b1] offset:8192\n\t" \
    "v_mfma_f32_16x16x32_bf16 v[32:35], v[124:127], v[92:95], v[32:35]\n\t" \
    "ds_read_b64_tr_b16 v[194:195], %[b1] offset:9216\n\t" \
    "v_mfma_f32_16x16x32_bf16 v[28:31], v[120:123], v[96:99], v[28:31]\n\t" \
    "ds_read_b128 v[148:151], %[a] offset:0\n\t" \
    "s_add_u32 m0, %[d], 8192\n\t" \
    "v_mfma_f32_16x16x32_bf16 v[32:35], v[128:131], v[96:99], v[32:35]\n\t" \
    "buffer_load_dwordx4 %[vb1], %[rb], %[y] offen lds\n\t" \
    "v_mfma_f32_16x16x32_bf16 v[36:39], v[116:119], v[100:103], v[36:39]\n\t" \
    "ds_read_b128 v[152:155], %[a] offset:1024\n\t" \
    "v_mfma_f32_16x16x32_bf16 v[40:43], v[124:127], v[100:103], v[40:43]\n\t" \
    "ds_read_b128 v[156:159], %[a] offset:2048\n\t" \
    "v_mfma_f32_16x16x32_bf16 v[36:39], v[120:123], v[104:107], v[36:39]\n\t" \
    "ds_read_b128 v[160:163], %[a] offset:3072\n\t" \
    "s_add_u32 m0, %[d], 32768\n\t" \
    "v_mfma_f32_16x16x32_bf16 v[40:43], v[128:131], v[104:107], v[40:43]\n\t" \
    "buffer_load_dwordx4 %[va0], %[ra], %[x] offen sc1 lds\n\t" \
    "v_mfma_f32_16x16x32_bf16 v[44:47], v[116:119], v[108:111], v[44:47]\n\t" \
    "ds_read_b128 v[164:167], %[a] offset:4096\n\t" \
    "v_mfma_f32_16x16x32_bf16 v[48:51], v[124:127], v[108:111], v[48:51]\n\t" \
    "ds_read_b128 v[168:171], %[a] offset:5120\n\t" \
    "v_mfma_f32_16x16x32_bf16 v[44:47], v[120:123], v[112:115], v[44:47]\n\t" \
    "ds_read_b128 v[172:175], %[a] offset:6144\n\t" \
    "s_add_u32 m0, %[d], 40960\n\t" \
    "v_mfma_f32_16x16x32_bf16 v[48:51], v[128:131], v[112:115], v[48:51]\n\t" \
    "buffer_load_dwordx4 %[va1], %[ra], %[x] offen sc1 lds\n\t" \
    "v_mfma_f32_16x16x32_bf16 v[52:55], v[132:135], v[84:87], v[52:55]\n\t" \
    "ds_read_b128 v[176:179], %[a] offset:7168\n\t" \
    "v_mfma_f32_16x16x32_bf16 v[56:59], v[140:143], v[84:87], v[56:59]\n\t" \
    "ds_read_b64_tr_b16 v[196:197], %[b0] offset:16384\n\t" \
    "v_mfma_f32_16x16x32_bf16 v[52:55], v[136:139], v[88:91], v[52:55]\n\t" \
    "ds_read_b64_tr_b16 v[198:199], %[b0] offset:17408\n\t" \
    "s_add_u32 m0, %[d], 16128\n\t" \
    "v_mfma_f32_16x16x32_bf16 v[56:59], v[144:147], v[88:91], v[56:59]\n\t" \
    "buffer_load_dwordx4 %[vb0], %[rb], %[y] offen offset:256 lds\n\t" \
    "v_mfma_f32_16x16x32_bf16 v[60:63], v[132:135], v[92:95], v[60:63]\n\t" \
    "ds_read_b64_tr_b16 v[204:205], %[b1] offset:16384\n\t" \
    "v_mfma_f32_16x16x32_bf16 v[64:67], v[140:143], v[92:95], v[64:67]\n\t" \
    "ds_read_b64_tr_b16 v[206:207], %[b1] offset:17408\n\t" \
    "v_mfma_f32_16x16x32_bf16 v[60:63], v[136:139], v[96:99], v[60:63]\n\t" \
    "ds_read_b64_tr_b16 v[200:201], %[b0] offset:24576\n\t" \
    "s_add_u32 m0, %[d], 24320\n\t" \
    "v_mfma_f32_16x16x32_bf16 v[64:67], v[144:147], v[96:99], v[64:67]\n\t" \
    "buffer_load_dwordx4 %[vb1], %[rb], %[y] offen offset:256 lds\n\t" \
    "v_mfma_f32_16x16x32_bf16 v[68:71], v[132:135], v[100:103], v[68:71]\n\t" \
    "ds_read_b64_tr_b16 v[202:203], %[b0] offset:25600\n\t" \
    "s_add_u32 %[t], %[t], 1\n\t" \
    "s_add_u32 %[d], %[lw], %[c]\n\t" \
    "v_mfma_f32_16x16x32_bf16 v[72:75], v[140:143], v[100:103], v[72:75]\n\t" \
    "ds_read_b64_tr_b16 v[208:209], %[b1] offset:24576\n\t" \
    "s_add_u32 %[y], %[c], 49152\n\t" \
    "s_cmp_eq_u32 %[y], 147456\n\t" \
    "v_mfma_f32_16x16x32_bf16 v[68:71], v[136:139], v[104:107], v[68:71]\n\t" \
    "ds_read_b64_tr_b16 v[210:211], %[b1] offset:25600\n\t" \
    "s_cselect_b32 %[y], 0, %[y]\n\t" \
    "s_sub_u32 %[z], %[y], %[c]\n\t" \
    "v_mfma_f32_16x16x32_bf16 v[72:75], v[144:147], v[104:107], v[72:75]\n\t" \
    "s_mov_b32 %[c], %[y]\n\t" \
    "s_add_u32 %[x], %[t], 3\n\t" \
    "v_mfma_f32_16x16x32_bf16 v[76:79], v[132:135], v[108:111], v[76:79]\n\t" \
    "s_sub_u32 %[y], %[x], %[nt]\n\t" \
    "s_cmp_lt_u32 %[x], %[nt]\n\t" \
    "v_mfma_f32_16x16x32_bf16 v[80:83], v[140:143], v[108:111], v[80:83]\n\t" \
    "s_cselect_b32 %[x], %[x], %[y]\n\t" \
    "s_cselect_b32 m0, %[cb], %[cn]\n\t" \
    "v_mfma_f32_16x16x32_bf16 v[76:79], v[136:139], v[112:115], v[76:79]\n\t" \
    "s_mul_i32 %[y], %[x], %[kb]\n\t" \
    "s_add_u32 %[y], %[y], m0\n\t" \
    "v_mfma_f32_16x16x32_bf16 v[80:83], v[144:147], v[112:115], v[80:83]\n\t" \
    "s_lshl_b32 %[x], %[x], 11\n\t" \
    "s_add_u32 %[x], %[x], %[ca]\n\t" \
    "v_add_u32 %[a], %[z], %[a]\n\t" \
    "v_add_u32 %[b0], %[z], %[b0]\n\t" \
    "v_add_u32 %[b1], %[z], %[b1]\n\t" \
    "s_waitcnt vmcnt(6) lgkmcnt(0)\n\t" \
    "s_barrier\n\t" \
    "v_mfma_f32_16x16x32_bf16 v[20:23], v[180:183], v[148:151], v[20:23]\n\t" \
    "ds_read_b64_tr_b16 v[116:117], %[b0] offset:0\n\t" \
    "ds_read_b64_tr_b16 v[118:119], %[b0] offset:1024\n\t" \
    "v_mfma_f32_16x16x32_bf16 v[24:27], v[188:191], v[148:151], v[24:27]\n\t" \
    "ds_read_b64_tr_b16 v[124:125], %[b1] offset:0\n\t" \
    "ds_read_b64_tr_b16 v[126:127], %[b1] offset:1024\n\t" \
    "v_mfma_f32_16x16x32_bf16 v[20:23], v[184:187], v[152:155], v[20:23]\n\t" \
    "ds_read_b64_tr_b16 v[120:121], %[b0] offset:8192\n\t" \
    "ds_read_b64_tr_b16 v[122:123], %[b0] offset:9216\n\t" \
    "s_add_u32 m0, %[d], 0\n\t" \
    "v_mfma_f32_16x16x32_bf16 v[24:27], v[192:195], v[152:155], v[24:27]\n\t" \
    "buffer_load_dwordx4 %[vb0], %[rb], %[y] offen lds\n\t" \
    "v_mfma_f32_16x16x32_bf16 v[28:31], v[180:183], v[156:159], v[28:31]\n\t" \
    "ds_read_b64_tr_b16 v[128:129], %[b1] offset:8192\n\t" \
    "v_mfma_f32_16x16x32_bf16 v[32:35], v[188:191], v[156:159], v[32:35]\n\t" \
    "ds_read_b64_tr_b16 v[130:131], %[b1] offset:9216\n\t" \
    "v_mfma_f32_16x16x32_bf16 v[28:31], v[184:187], v[160:163], v[28:31]\n\t" \
    "ds_read_b128 v[84:87], %[a] offset:0\n\t" \
    "s_add_u32 m0, %[d], 8192\n\t" \
    "v_mfma_f32_16x16x32_bf16 v[32:35], v[192:195], v[160:163], v[32:35]\n\t" \
    "buffer_load_dwordx4 %[vb1], %[rb], %[y] offen lds\n\t" \
    "v_mfma_f32_16x16x32_bf16 v[36:39], v[180:183], v[164:167], v[36:39]\n\t" \
    "ds_read_b128 v[88:91], %[a] offset:1024\n\t" \
    "v_mfma_f32_16x16x32_bf16 v[40:43], v[188:191], v[164:167], v[40:43]\n\t" \
    "ds_read_b128 v[92:95], %[a] offset:2048\n\t" \
    "v_mfma_f32_16x16x32_bf16 v[36:39], v[184:187], v[168:171], v[36:39]\n\t" \
    "ds_read_b128 v[96:99], %[a] offset:3072\n\t" \
    "s_add_u32 m0, %[d], 32768\n\t" \
    "v_mfma_f32_16x16x32_bf16 v[40:43], v[192:195], v[168:171], v[40:43]\n\t" \
    "buffer_load_dwordx4 %[va0], %[ra], %[x] offen sc1 lds\n\t" \
    "v_mfma_f32_16x16x32_bf16 v[44:47], v[180:183], v[172:175], v[44:47]\n\t" \
    "ds_read_b128 v[100:103], %[a] offset:4096\n\t" \
    "v_mfma_f32_16x16x32_bf16 v[48:51], v[188:191], v[172:175], v[48:51]\n\t" \
    "ds_read_b128 v[104:107], %[a] offset:5120\n\t" \
    "v_mfma_f32_16x16x32_bf16 v[44:47], v[184:187], v[176:179], v[44:47]\n\t" \
    "ds_read_b128 v[108:111], %[a] offset:6144\n\t" \
    "s_add_u32 m0, %[d], 40960\n\t" \
    "v_mfma_f32_16x16x32_bf16 v[48:51], v[192:195], v[176:179], v[48:51]\n\t" \
    "buffer_load_dwordx4 %[va1], %[ra], %[x] offen sc1 lds\n\t" \
    "v_mfma_f32_16x16x32_bf16 v[52:55], v[196:199], v[148:151], v[52:55]\n\t" \
    "ds_read_b128 v[112:115], %[a] offset:7168\n\t" \
    "v_mfma_f32_16x16x32_bf16 v[56:59], v[204:207], v[148:151], v[56:59]\n\t" \
    "ds_read_b64_tr_b16 v[132:133], %[b0] offset:16384\n\t" \
    "v_mfma_f32_16x16x32_bf16 v[52:55], v[200:203], v[152:155], v[52:55]\n\t" \
    "ds_read_b64_tr_b16 v[134:135], %[b0] offset:17408\n\t" \
    "s_add_u32 m0, %[d], 16128\n\t" \
    "v_mfma_f32_16x16x32_bf16 v[56:59], v[208:211], v[152:155], v[56:59]\n\t" \
    "buffer_load_dwordx4 %[vb0], %[rb], %[y] offen offset:256 lds\n\t" \
    "v_mfma_f32_16x16x32_bf16 v[60:63], v[196:199], v[156:159], v[60:63]\n\t" \
    "ds_read_b64_tr_b16 v[140:141], %[b1] offset:16384\n\t" \
    "v_mfma_f32_16x16x32_bf16 v[64:67], v[204:207], v[156:159], v[64:67]\n\t" \
    "ds_read_b64_tr_b16 v[142:143], %[b1] offset:17408\n\t" \
    "v_mfma_f32_16x16x32_bf16 v[60:63], v[200:203], v[160:163], v[60:63]\n\t" \
    "ds_read_b64_tr_b16 v[136:137], %[b0] offset:24576\n\t" \
    "s_add_u32 m0, %[d], 24320\n\t" \
    "v_mfma_f32_16x16x32_bf16 v[64:67], v[208:211], v[160:163], v[64:67]\n\t" \
    "buffer_load_dwordx4 %[vb1], %[rb], %[y] offen offset:256 lds\n\t" \
    "v_mfma_f32_16x16x32_bf16 v[68:71], v[196:199], v[164:167], v[68:71]\n\t" \
    "ds_read_b64_tr_b16 v[138:139], %[b0] offset:25600\n\t" \
    "s_add_u32 %[t], %[t], 1\n\t" \
    "s_add_u32 %[d], %[lw], %[c]\n\t" \
    "s_add_u32 %[y], %[c], 49152\n\t" \
    "v_mfma_f32_16x16x32_bf16 v[72:75], v[204:207], v[164:167], v[72:75]\n\t" \
    "ds_read_b64_tr_b16 v[144:145], %[b1] offset:24576\n\t" \
    "s_cmp_eq_u32 %[y], 147456\n\t" \
    "s_cselect_b32 %[y], 0, %[y]\n\t" \
    "s_sub_u32 %[z], %[y], %[c]\n\t" \
    "v_mfma_f32_16x16x32_bf16 v[68:71], v[200:203], v[168:171], v[68:71]\n\t" \
    "ds_read_b64_tr_b16 v[146:147], %[b1] offset:25600\n\t" \
    "s_mov_b32 %[c], %[y]\n\t" \
    "s_add_u32 %[x], %[t], 3\n\t" \
    "s_sub_u32 %[y], %[x], %[nt]\n\t" \
    "v_mfma_f32_16x16x32_bf16 v[72:75], v[208:211], v[168:171], v[72:75]\n\t" \
    "s_cmp_lt_u32 %[x], %[nt]\n\t" \
    "s_cselect_b32 %[x], %[x], %[y]\n\t" \
    "s_cselect_b32 m0, %[cb], %[cn]\n\t" \
    "v_mfma_f32_16x16x32_bf16 v[76:79], v[196:199], v[172:175], v[76:79]\n\t" \
    "s_mul_i32 %[y], %[x], %[kb]\n\t" \
    "s_add_u32 %[y], %[y], m0\n\t" \
    "s_lshl_b32 %[x], %[x], 11\n\t" \
    "v_mfma_f32_16x16x32_bf16 v[80:83], v[204:207], v[172:175], v[80:83]\n\t" \
    "s_add_u32 %[x], %[x], %[ca]\n\t" \
    "s_cmp_lt_u32 %[t], %[nt]\n\t" \
    "v_mfma_f32_16x16x32_bf16 v[76:79], v[200:203], v[176:179], v[76:79]\n\t" \
    "v_mfma_f32_16x16x32_bf16 v[80:83], v[208:211], v[176:179], v[80:83]\n\t" \
    "v_add_u32 %[a], %[z], %[a]\n\t" \
    "v_add_u32 %[b0], %[z], %[b0]\n\t" \
    "v_add_u32 %[b1], %[z], %[b1]\n\t" \
    "s_cbranch_scc1 L_%=\n\t" \
    "s_waitcnt lgkmcnt(0)\n\t" \
    "s_nop 7\n\t" \
    : "=&{v[20:23]}"(acc[0][0][0][0]), "=&{v[24:27]}"(acc[0][0][0][1]), "=&{v[28:31]}"(acc[0][0][1][0]), "=&{v[32:35]}"(acc[0][0][1][1]), "=&{v[36:39]}"(acc[0][0][2][0]), "=&{v[40:43]}"(acc[0][0][2][1]), "=&{v[44:47]}"(acc[0][0][3][0]), "=&{v[48:51]}"(acc[0][0][3][1]), "=&{v[52:55]}"(acc[0][1][0][0]), "=&{v[56:59]}"(acc[0][1][0][1]), "=&{v[60:63]}"(acc[0][1][1][0]), "=&{v[64:67]}"(acc[0][1][1][1]), "=&{v[68:71]}"(acc[0][1][2][0]), "=&{v[72:75]}"(acc[0][1][2][1]), "=&{v[76:79]}"(acc[0][1][3][0]), "=&{v[80:83]}"(acc[0][1][3][1]), [a] "+v"(aoff), [b0] "+v"(boff0), [b1] "+v"(boff1), [c] "+s"(cur), [t] "+s"(t), [d] "=&s"(sd), [x] "=&s"(sx), [y] "=&s"(sy), [z] "=&s"(sz) \
    : [va0] "v"(voffA[0]), [va1] "v"(voffA[1]), [vb0] "v"(voffB[0]), [vb1] "v"(voffB[1]), [ra] "s"(ra), [rb] "s"(rb), [lw] "s"(ldsw), [nt] "s"(nt), [kb] "s"(kB), [ca] "s"(cA), [cb] "s"(cB), [cn] "s"(cN) \
    : "memory", "scc", "v84", "v85", "v86", "v87", "v88", "v89", "v90", "v91", "v92", "v93", "v94", "v95", "v96", "v97", "v98", "v99", "v100", "v101", "v102", "v103", "v104", "v105", "v106", "v107", "v108", "v109", "v110", "v111", "v112", "v113", "v114", "v115", "v116", "v117", "v118", "v119", "v120", "v121", "v122", "v123", "v124", "v125", "v126", "v127", "v128", "v129", "v130", "v131", "v132", "v133", "v134", "v135", "v136", "v137", "v138", "v139", "v140", "v141", "v142", "v143", "v144", "v145", "v146", "v147", "v148", "v149", "v150", "v151", "v152", "v153", "v154", "v155", "v156", "v157", "v158", "v159", "v160", "v161", "v162", "v163", "v164", "v165", "v166", "v167", "v168", "v169", "v170", "v171", "v172", "v173", "v174", "v175", "v176", "v177", "v178", "v179", "v180", "v181", "v182", "v183", "v184", "v185", "v186", "v187", "v188", "v189", "v190", "v191", "v192", "v193", "v194", "v195", "v196", "v197", "v198", "v199", "v200", "v201", "v202", "v203", "v204", "v205", "v206", "v207", "v208", "v209", "v210", "v211")

#define H_LOOP2S() \
  asm volatile( \
    "v_mov_b32 v20, 0\n\t" \
    "v_mov_b32 v21, 0\n\t" \
    "v_mov_b32 v22, 0\n\t" \
    "v_mov_b32 v23, 0\n\t" \
    "v_mov_b32 v24, 0\n\t" \
    "v_mov_b32 v25, 0\n\t" \
    "v_mov_b32 v26, 0\n\t" \
    "v_mov_b32 v27, 0\n\t" \
    "v_mov_b32 v28, 0\n\t" \
    "v_mov_b32 v29, 0\n\t" \
    "v_mov_b32 v30, 0\n\t" \
    "v_mov_b32 v31, 0\n\t" \
    "v_mov_b32 v32, 0\n\t" \
    "v_mov_b32 v33, 0\n\t" \
    "v_mov_b32 v34, 0\n\t" \
    "v_mov_b32 v35, 0\n\t" \
    "v_mov_b32 v36, 0\n\t" \
    "v_mov_b32 v37, 0\n\t" \
    "v_mov_b32 v38, 0\n\t" \
    "v_mov_b32 v39, 0\n\t" \
    "v_mov_b32 v40, 0\n\t" \
    "v_mov_b32 v41, 0\n\t" \
    "v_mov_b32 v42, 0\n\t" \
    "v_mov_b32 v43, 0\n\t" \
    "v_mov_b32 v44, 0\n\t" \
    "v_mov_b32 v45, 0\n\t" \
    "v_mov_b32 v46, 0\n\t" \
    "v_mov_b32 v47, 0\n\t" \
    "v_mov_b32 v48, 0\n\t" \
    "v_mov_b32 v49, 0\n\t" \
    "v_mov_b32 v50, 0\n\t" \
    "v_mov_b32 v51, 0\n\t" \
    "v_mov_b32 v52, 0\n\t" \
    "v_mov_b32 v53, 0\n\t" \
    "v_mov_b32 v54, 0\n\t" \
    "v_mov_b32 v55, 0\n\t" \
    "v_mov_b32 v56, 0\n\t" \
    "v_mov_b32 v57, 0\n\t" \
    "v_mov_b32 v58, 0\n\t" \
    "v_mov_b32 v59, 0\n\t" \
    "v_mov_b32 v60, 0\n\t" \
    "v_mov_b32 v61, 0\n\t" \
    "v_mov_b32 v62, 0\n\t" \
    "v_mov_b32 v63, 0\n\t" \
    "v_mov_b32 v64, 0\n\t" \
    "v_mov_b32 v65, 0\n\t" \
    "v_mov_b32 v66, 0\n\t" \
    "v_mov_b32 v67, 0\n\t" \
    "v_mov_b32 v68, 0\n\t" \
    "v_mov_b32 v69, 0\n\t" \
    "v_mov_b32 v70, 0\n\t" \
    "v_mov_b32 v71, 0\n\t" \
    "v_mov_b32 v72, 0\n\t" \
    "v_mov_b32 v73, 0\n\t" \
    "v_mov_b32 v74, 0\n\t" \
    "v_mov_b32 v75, 0\n\t" \
    "v_mov_b32 v76, 0\n\t" \
    "v_mov_b32 v77, 0\n\t" \
    "v_mov_b32 v78, 0\n\t" \
    "v_mov_b32 v79, 0\n\t" \
    "v_mov_b32 v80, 0\n\t" \
    "v_mov_b32 v81, 0\n\t" \
    "v_mov_b32 v82, 0\n\t" \
    "v_mov_b32 v83, 0\n\t" \
    "s_waitcnt vmcnt(12)\n\t" \
    "s_barrier\n\t" \
    "ds_read_b64_tr_b16 v[116:117], %[b0] offset:0\n\t" \
    "ds_read_b64_tr_b16 v[118:119], %[b0] offset:1024\n\t" \
    "ds_read_b64_tr_b16 v[124:125], %[b1] offset:0\n\t" \
    "ds_read_b64_tr_b16 v[126:127], %[b1] offset:1024\n\t" \
    "ds_read_b64_tr_b16 v[120:121], %[b0] offset:8192\n\t" \
    "ds_read_b64_tr_b16 v[122:123], %[b0] offset:9216\n\t" \
    "ds_read_b64_tr_b16 v[128:129], %[b1] offset:8192\n\t" \
    "ds_read_b64_tr_b16 v[130:131], %[b1] offset:9216\n\t" \
    "ds_read_b128 v[84:87], %[a] offset:0\n\t" \
    "ds_read_b128 v[88:91], %[a] offset:1024\n\t" \
    "ds_read_b128 v[92:95], %[a] offset:2048\n\t" \
    "ds_read_b128 v[96:99], %[a] offset:3072\n\t" \
    "ds_read_b128 v[100:103], %[a] offset:4096\n\t" \
    "ds_read_b128 v[104:107], %[a] offset:5120\n\t" \
    "ds_read_b128 v[108:111], %[a] offset:6144\n\t" \
    "ds_read_b128 v[112:115], %[a] offset:7168\n\t" \
    "ds_read_b64_tr_b16 v[132:133], %[b0] offset:16384\n\t" \
    "ds_read_b64_tr_b16 v[134:135], %[b0] offset:17408\n\t" \
    "ds_read_b64_tr_b16 v[140:141], %[b1] offset:16384\n\t" \
    "ds_read_b64_tr_b16 v[142:143], %[b1] offset:17408\n\t" \
    "ds_read_b64_tr_b16 v[136:137], %[b0] offset:24576\n\t" \
    "ds_read_b64_tr_b16 v[138:139], %[b0] offset:25600\n\t" \
    "ds_read_b64_tr_b16 v[144:145], %[b1] offset:24576\n\t" \
    "ds_read_b64_tr_b16 v[146:147], %[b1] offset:25600\n\t" \
    "s_add_u32 %[d], %[lw], %[c]\n\t" \
    "s_add_u32 %[y], %[c], 49152\n\t" \
    "s_cmp_eq_u32 %[y], 147456\n\t" \
    "s_cselect_b32 %[y], 0, %[y]\n\t" \
    "s_sub_u32 %[z], %[y], %[c]\n\t" \
    "s_mov_b32 %[c], %[y]\n\t" \
    "s_add_u32 %[x], %[t], 3\n\t" \
    "s_sub_u32 %[y], %[x], %[nt]\n\t" \
    "s_cmp_lt_u32 %[x], %[nt]\n\t" \
    "s_cselect_b32 %[x], %[x], %[y]\n\t" \
    "s_cselect_b32 m0, %[cb], %[cn]\n\t" \
    "s_mul_i32 %[y], %[x], %[kb]\n\t" \
    "s_add_u32 %[y], %[y], m0\n\t" \
    "s_lshl_b32 %[x], %[x], 11\n\t" \
    "s_add_u32 %[x], %[x], %[ca]\n\t" \
    "v_add_u32 %[a], %[z], %[a]\n\t" \
    "v_add_u32 %[b0], %[z], %[b0]\n\t" \
    "v_add_u32 %[b1], %[z], %[b1]\n\t" \
    "L_%=:\n\t" \
    "s_waitcnt vmcnt(6) lgkmcnt(0)\n\t" \
    "s_barrier\n\t" \
    "s_sleep 2\n\t" \
    "s_nop 5\n\t" \
    "v_mfma_f32_16x16x32_bf16 v[20:23], v[116:119], v[84:87], v[20:23]\n\t" \
    "ds_read_b64_tr_b16 v[180:181], %[b0] offset:0\n\t" \
    "ds_read_b64_tr_b16 v[182:183], %[b0] offset:1024\n\t" \
    "v_mfma_f32_16x16x32_bf16 v[24:27], v[124:127], v[84:87], v[24:27]\n\t" \
    "ds_read_b64_tr_b16 v[188:189], %[b1] offset:0\n\t" \
    "ds_read_b64_tr_b16 v[190:191], %[b1] offset:1024\n\t" \
    "v_mfma_f32_16x16x32_bf16 v[20:23], v[120:123], v[88:91], v[20:23]\n\t" \
    "ds_read_b64_tr_b16 v[184:185], %[b0] offset:8192\n\t" \
    "ds_read_b64_tr_b16 v[186:187], %[b0] offset:9216\n\t" \
    "s_add_u32 m0, %[d], 0\n\t" \
    "v_mfma_f32_16x16x32_bf16 v[24:27], v[128:131], v[88:91], v[24:27]\n\t" \
    "buffer_load_dwordx4 %[vb0], %[rb], %[y] offen lds\n\t" \
    "v_mfma_f32_16x16x32_bf16 v[28:31], v[116:119], v[92:95], v[28:31]\n\t" \
    "ds_read_b64_tr_b16 v[192:193], %[b1] offset:8192\n\t" \
    "v_mfma_f32_16x16x32_bf16 v[32:35], v[124:127], v[92:95], v[32:35]\n\t" \
    "ds_read_b64_tr_b16 v[194:195], %[b1] offset:9216\n\t" \
    "v_mfma_f32_16x16x32_bf16 v[28:31], v[120:123], v[96:99], v[28:31]\n\t" \
    "ds_read_b128 v[148:151], %[a] offset:0\n\t" \
    "s_add_u32 m0, %[d], 8192\n\t" \
    "v_mfma_f32_16x16x32_bf16 v[32:35], v[128:131], v[96:99], v[32:35]\n\t" \
    "buffer_load_dwordx4 %[vb1], %[rb], %[y] offen lds\n\t" \
    "v_mfma_f32_16x16x32_bf16 v[36:39], v[116:119], v[100:103], v[36:39]\n\t" \
    "ds_read_b128 v[152:155], %[a] offset:1024\n\t" \
    "v_mfma_f32_16x16x32_bf16 v[40:43], v[124:127], v[100:103], v[40:43]\n\t" \
    "ds_read_b128 v[156:159], %[a] offset:2048\n\t" \
    "v_mfma_f32_16x16x32_bf16 v[36:39], v[120:123], v[104:107], v[36:39]\n\t" \
    "ds_read_b128 v[160:163], %[a] offset:3072\n\t" \
    "s_add_u32 m0, %[d], 32768\n\t" \
    "v_mfma_f32_16x16x32_bf16 v[40:43], v[128:131], v[104:107], v[40:43]\n\t" \
    "buffer_load_dwordx4 %[va0], %[ra], %[x] offen sc1 lds\n\t" \
    "v_mfma_f32_16x16x32_bf16 v[44:47], v[116:119], v[108:111], v[44:47]\n\t" \
    "ds_read_b128 v[164:167], %[a] offset:4096\n\t" \
    "v_mfma_f32_16x16x32_bf16 v[48:51], v[124:127], v[108:111], v[48:51]\n\t" \
    "ds_read_b128 v[168:171], %[a] offset:5120\n\t" \
    "v_mfma_f32_16x16x32_bf16 v[44:47], v[120:123], v[112:115], v[44:47]\n\t" \
    "ds_read_b128 v[172:175], %[a] offset:6144\n\t" \
    "s_add_u32 m0, %[d], 40960\n\t" \
    "v_mfma_f32_16x16x32_bf16 v[48:51], v[128:131], v[112:115], v[48:51]\n\t" \
    "buffer_load_dwordx4 %[va1], %[ra], %[x] offen sc1 lds\n\t" \
    "v_mfma_f32_16x16x32_bf16 v[52:55], v[132:135], v[84:87], v[52:55]\n\t" \
    "ds_read_b128 v[176:179], %[a] offset:7168\n\t" \
    "v_mfma_f32_16x16x32_bf16 v[56:59], v[140:143], v[84:87], v[56:59]\n\t" \
    "ds_read_b64_tr_b16 v[196:197], %[b0] offset:16384\n\t" \
    "v_mfma_f32_16x16x32_bf16 v[52:55], v[136:139], v[88:91], v[52:55]\n\t" \
    "ds_read_b64_tr_b16 v[198:199], %[b0] offset:17408\n\t" \
    "s_add_u32 m0, %[d], 16128\n\t" \
    "v_mfma_f32_16x16x32_bf16 v[56:59], v[144:147], v[88:91], v[56:59]\n\t" \
    "buffer_load_dwordx4 %[vb0], %[rb], %[y] offen offset:256 lds\n\t" \
    "v_mfma_f32_16x16x32_bf16 v[60:63], v[132:135], v[92:95], v[60:63]\n\t" \
    "ds_read_b64_tr_b16 v[204:205], %[b1] offset:16384\n\t" \
    "v_mfma_f32_16x16x32_bf16 v[64:67], v[140:143], v[92:95], v[64:67]\n\t" \
    "ds_read_b64_tr_b16 v[206:207], %[b1] offset:17408\n\t" \
    "v_mfma_f32_16x16x32_bf16 v[60:63], v[136:139], v[96:99], v[60:63]\n\t" \
    "ds_read_b64_tr_b16 v[200:201], %[b0] offset:24576\n\t" \
    "s_add_u32 m0, %[d], 24320\n\t" \
    "v_mfma_f32_16x16x32_bf16 v[64:67], v[144:147], v[96:99], v[64:67]\n\t" \
    "buffer_load_dwordx4 %[vb1], %[rb], %[y] offen offset:256 lds\n\t" \
    "v_mfma_f32_16x16x32_bf16 v[68:71], v[132:135], v[100:103], v[68:71]\n\t" \
    "ds_read_b64_tr_b16 v[202:203], %[b0] offset:25600\n\t" \
    "s_add_u32 %[t], %[t], 1\n\t" \
    "s_add_u32 %[d], %[lw], %[c]\n\t" \
    "v_mfma_f32_16x16x32_bf16 v[72:75], v[140:143], v[100:103], v[72:75]\n\t" \
    "ds_read_b64_tr_b16 v[208:209], %[b1] offset:24576\n\t" \
    "s_add_u32 %[y], %[c], 49152\n\t" \
    "s_cmp_eq_u32 %[y], 147456\n\t" \
    "v_mfma_f32_16x16x32_bf16 v[68:71], v[136:139], v[104:107], v[68:71]\n\t" \
    "ds_read_b64_tr_b16 v[210:211], %[b1] offset:25600\n\t" \
    "s_cselect_b32 %[y], 0, %[y]\n\t" \
    "s_sub_u32 %[z], %[y], %[c]\n\t" \
    "v_mfma_f32_16x16x32_bf16 v[72:75], v[144:147], v[104:107], v[72:75]\n\t" \
    "s_mov_b32 %[c], %[y]\n\t" \
    "s_add_u32 %[x], %[t], 3\n\t" \
    "v_mfma_f32_16x16x32_bf16 v[76:79], v[132:135], v[108:111], v[76:79]\n\t" \
    "s_sub_u32 %[y], %[x], %[nt]\n\t" \
    "s_cmp_lt_u32 %[x], %[nt]\n\t" \
    "v_mfma_f32_16x16x32_bf16 v[80:83], v[140:143], v[108:111], v[80:83]\n\t" \
    "s_cselect_b32 %[x], %[x], %[y]\n\t" \
    "s_cselect_b32 m0, %[cb], %[cn]\n\t" \
    "v_mfma_f32_16x16x32_bf16 v[76:79], v[136:139], v[112:115], v[76:79]\n\t" \
    "s_mul_i32 %[y], %[x], %[kb]\n\t" \
    "s_add_u32 %[y], %[y], m0\n\t" \
    "v_mfma_f32_16x16x32_bf16 v[80:83], v[144:147], v[112:115], v[80:83]\n\t" \
    "s_lshl_b32 %[x], %[x], 11\n\t" \
    "s_add_u32 %[x], %[x], %[ca]\n\t" \
    "v_add_u32 %[a], %[z], %[a]\n\t" \
    "v_add_u32 %[b0], %[z], %[b0]\n\t" \
    "v_add_u32 %[b1], %[z], %[b1]\n\t" \
    "s_waitcnt vmcnt(6) lgkmcnt(0)\n\t" \
    "s_barrier\n\t" \
    "s_sleep 2\n\t" \
    "s_nop 5\n\t" \
    "v_mfma_f32_16x16x32_bf16 v[20:23], v[180:183], v[148:151], v[20:23]\n\t" \
    "ds_read_b64_tr_b16 v[116:117], %[b0] offset:0\n\t" \
    "ds_read_b64_tr_b16 v[118:119], %[b0] offset:1024\n\t" \
    "v_mfma_f32_16x16x32_bf16 v[24:27], v[188:191], v[148:151], v[24:27]\n\t" \
    "ds_read_b64_tr_b16 v[124:125], %[b1] offset:0\n\t" \
    "ds_read_b64_tr_b16 v[126:127], %[b1] offset:1024\n\t" \
    "v_mfma_f32_16x16x32_bf16 v[20:23], v[184:187], v[152:155], v[20:23]\n\t" \
    "ds_read_b64_tr_b16 v[120:121], %[b0] offset:8192\n\t" \
    "ds_read_b64_tr_b16 v[122:123], %[b0] offset:9216\n\t" \
    "s_add_u32 m0, %[d], 0\n\t" \
    "v_mfma_f32_16x16x32_bf16 v[24:27], v[192:195], v[152:155], v[24:27]\n\t" \
    "buffer_load_dwordx4 %[vb0], %[rb], %[y] offen lds\n\t" \
    "v_mfma_f32_16x16x32_bf16 v[28:31], v[180:183], v[156:159], v[28:31]\n\t" \
    "ds_read_b64_tr_b16 v[128:129], %[b1] offset:8192\n\t" \
    "v_mfma_f32_16x16x32_bf16 v[32:35], v[188:191], v[156:159], v[32:35]\n\t" \
    "ds_read_b64_tr_b16 v[130:131], %[b1] offset:9216\n\t" \
    "v_mfma_f32_16x16x32_bf16 v[28:31], v[184:187], v[160:163], v[28:31]\n\t" \
    "ds_read_b128 v[84:87], %[a] offset:0\n\t" \
    "s_add_u32 m0, %[d], 8192\n\t" \
    "v_mfma_f32_16x16x32_bf16 v[32:35], v[192:195], v[160:163], v[32:35]\n\t" \
    "buffer_load_dwordx4 %[vb1], %[rb], %[y] offen lds\n\t" \
    "v_mfma_f32_16x16x32_bf16 v[36:39], v[180:183], v[164:167], v[36:39]\n\t" \
    "ds_read_b128 v[88:91], %[a] offset:1024\n\t" \
    "v_mfma_f32_16x16x32_bf16 v[40:43], v[188:191], v[164:167], v[40:43]\n\t" \
    "ds_read_b128 v[92:95], %[a] offset:2048\n\t" \
    "v_mfma_f32_16x16x32_bf16 v[36:39], v[184:187], v[168:171], v[36:39]\n\t" \
    "ds_read_b128 v[96:99], %[a] offset:3072\n\t" \
    "s_add_u32 m0, %[d], 32768\n\t" \
    "v_mfma_f32_16x16x32_bf16 v[40:43], v[192:195], v[168:171], v[40:43]\n\t" \
    "buffer_load_dwordx4 %[va0], %[ra], %[x] offen sc1 lds\n\t" \
    "v_mfma_f32_16x16x32_bf16 v[44:47], v[180:183], v[172:175], v[44:47]\n\t" \
    "ds_read_b128 v[100:103], %[a] offset:4096\n\t" \
    "v_mfma_f32_16x16x32_bf16 v[48:51], v[188:191], v[172:175], v[48:51]\n\t" \
    "ds_read_b128 v[104:107], %[a] offset:5120\n\t" \
    "v_mfma_f32_16x16x32_bf16 v[44:47], v[184:187], v[176:179], v[44:47]\n\t" \
    "ds_read_b128 v[108:111], %[a] offset:6144\n\t" \
    "s_add_u32 m0, %[d], 40960\n\t" \
    "v_mfma_f32_16x16x32_bf16 v[48:51], v[192:195], v[176:179], v[48:51]\n\t" \
    "buffer_load_dwordx4 %[va1], %[ra], %[x] offen sc1 lds\n\t" \
    "v_mfma_f32_16x16x32_bf16 v[52:55], v[196:199], v[148:151], v[52:55]\n\t" \
    "ds_read_b128 v[112:115], %[a] offset:7168\n\t" \
    "v_mfma_f32_16x16x32_bf16 v[56:59], v[204:207], v[148:151], v[56:59]\n\t" \
    "ds_read_b64_tr_b16 v[132:133], %[b0] offset:16384\n\t" \
    "v_mfma_f32_16x16x32_bf16 v[52:55], v[200:203], v[152:155], v[52:55]\n\t" \
    "ds_read_b64_tr_b16 v[134:135], %[b0] offset:17408\n\t" \
    "s_add_u32 m0, %[d], 16128\n\t" \
    "v_mfma_f32_16x16x32_bf16 v[56:59], v[208:211], v[152:155], v[56:59]\n\t" \
    "buffer_load_dwordx4 %[vb0], %[rb], %[y] offen offset:256 lds\n\t" \
    "v_mfma_f32_16x16x32_bf16 v[60:63], v[196:199], v[156:159], v[60:63]\n\t" \
    "ds_read_b64_tr_b16 v[140:141], %[b1] offset:16384\n\t" \
    "v_mfma_f32_16x16x32_bf16 v[64:67], v[204:207], v[156:159], v[64:67]\n\t" \
    "ds_read_b64_tr_b16 v[142:143], %[b1] offset:17408\n\t" \
    "v_mfma_f32_16x16x32_bf16 v[60:63], v[200:203], v[160:163], v[60:63]\n\t" \
    "ds_read_b64_tr_b16 v[136:137], %[b0] offset:24576\n\t" \
    "s_add_u32 m0, %[d], 24320\n\t" \
    "v_mfma_f32_16x16x32_bf16 v[64:67], v[208:211], v[160:163], v[64:67]\n\t" \
    "buffer_load_dwordx4 %[vb1], %[rb], %[y] offen offset:256 lds\n\t" \
    "v_mfma_f32_16x16x32_bf16 v[68:71], v[196:199], v[164:167], v[68:71]\n\t" \
    "ds_read_b64_tr_b16 v[138:139], %[b0] offset:25600\n\t" \
    "s_add_u32 %[t], %[t], 1\n\t" \
    "s_add_u32 %[d], %[lw], %[c]\n\t" \
    "s_add_u32 %[y], %[c], 49152\n\t" \
    "v_mfma_f32_16x16x32_bf16 v[72:75], v[204:207], v[164:167], v[72:75]\n\t" \
    "ds_read_b64_tr_b16 v[144:145], %[b1] offset:24576\n\t" \
    "s_cmp_eq_u32 %[y], 147456\n\t" \
    "s_cselect_b32 %[y], 0, %[y]\n\t" \
    "s_sub_u32 %[z], %[y], %[c]\n\t" \
    "v_mfma_f32_16x16x32_bf16 v[68:71], v[200:203], v[168:171], v[68:71]\n\t" \
    "ds_read_b64_tr_b16 v[146:147], %[b1] offset:25600\n\t" \
    "s_mov_b32 %[c], %[y]\n\t" \
    "s_add_u32 %[x], %[t], 3\n\t" \
    "s_sub_u32 %[y], %[x], %[nt]\n\t" \
    "v_mfma_f32_16x16x32_bf16 v[72:75], v[208:211], v[168:171], v[72:75]\n\t" \
    "s_cmp_lt_u32 %[x], %[nt]\n\t" \
    "s_cselect_b32 %[x], %[x], %[y]\n\t" \
    "s_cselect_b32 m0, %[cb], %[cn]\n\t" \
    "v_mfma_f32_16x16x32_bf16 v[76:79], v[196:199], v[172:175], v[76:79]\n\t" \
    "s_mul_i32 %[y], %[x], %[kb]\n\t" \
    "s_add_u32 %[y], %[y], m0\n\t" \
    "s_lshl_b32 %[x], %[x], 11\n\t" \
    "v_mfma_f32_16x16x32_bf16 v[80:83], v[204:207], v[172:175], v[80:83]\n\t" \
    "s_add_u32 %[x], %[x], %[ca]\n\t" \
    "s_cmp_lt_u32 %[t], %[nt]\n\t" \
    "v_mfma_f32_16x16x32_bf16 v[76:79], v[200:203], v[176:179], v[76:79]\n\t" \
    "v_mfma_f32_16x16x32_bf16 v[80:83], v[208:211], v[176:179], v[80:83]\n\t" \
    "v_add_u32 %[a], %[z], %[a]\n\t" \
    "v_add_u32 %[b0], %[z], %[b0]\n\t" \
    "v_add_u32 %[b1], %[z], %[b1]\n\t" \
    "s_cbranch_scc1 L_%=\n\t" \
    "s_waitcnt lgkmcnt(0)\n\t" \
    "s_nop 7\n\t" \
    : "=&{v[20:23]}"(acc[0][0][0][0]), "=&{v[24:27]}"(acc[0][0][0][1]), "=&{v[28:31]}"(acc[0][0][1][0]), "=&{v[32:35]}"(acc[0][0][1][1]), "=&{v[36:39]}"(acc[0][0][2][0]), "=&{v[40:43]}"(acc[0][0][2][1]), "=&{v[44:47]}"(acc[0][0][3][0]), "=&{v[48:51]}"(acc[0][0][3][1]), "=&{v[52:55]}"(acc[0][1][0][0]), "=&{v[56:59]}"(acc[0][1][0][1]), "=&{v[60:63]}"(acc[0][1][1][0]), "=&{v[64:67]}"(acc[0][1][1][1]), "=&{v[68:71]}"(acc[0][1][2][0]), "=&{v[72:75]}"(acc[0][1][2][1]), "=&{v[76:79]}"(acc[0][1][3][0]), "=&{v[80:83]}"(acc[0][1][3][1]), [a] "+v"(aoff), [b0] "+v"(boff0), [b1] "+v"(boff1), [c] "+s"(cur), [t] "+s"(t), [d] "=&s"(sd), [x] "=&s"(sx), [y] "=&s"(sy), [z] "=&s"(sz) \
    : [va0] "v"(voffA[0]), [va1] "v"(voffA[1]), [vb0] "v"(voffB[0]), [vb1] "v"(voffB[1]), [ra] "s"(ra), [rb] "s"(rb), [lw] "s"(ldsw), [nt] "s"(nt), [kb] "s"(kB), [ca] "s"(cA), [cb] "s"(cB), [cn] "s"(cN) \
    : "memory", "scc", "v84", "v85", "v86", "v87", "v88", "v89", "v90", "v91", "v92", "v93", "v94", "v95", "v96", "v97", "v98", "v99", "v100", "v101", "v102", "v103", "v104", "v105", "v106", "v107", "v108", "v109", "v110", "v111", "v112", "v113", "v114", "v115", "v116", "v117", "v118", "v119", "v120", "v121", "v122", "v123", "v124", "v125", "v126", "v127", "v128", "v129", "v130", "v131", "v132", "v133", "v134", "v135", "v136", "v137", "v138", "v139", "v140", "v141", "v142", "v143", "v144", "v145", "v146", "v147", "v148", "v149", "v150", "v151", "v152", "v153", "v154", "v155", "v156", "v157", "v158", "v159", "v160", "v161", "v162", "v163", "v164", "v165", "v166", "v167", "v168", "v169", "v170", "v171", "v172", "v173", "v174", "v175", "v176", "v177", "v178", "v179", "v180", "v181", "v182", "v183", "v184", "v185", "v186", "v187", "v188", "v189", "v190", "v191", "v192", "v193", "v194", "v195", "v196", "v197", "v198", "v199", "v200", "v201", "v202", "v203", "v204", "v205", "v206", "v207", "v208", "v209", "v210", "v211")
#define AQ_PIN0() asm volatile("" : "+{v[28:35]}"(wr[0][0]), "+{v[36:43]}"(wr[0][1]))
#define AQ_PIN1() asm volatile("" : "+{v[44:51]}"(wr[1][0]), "+{v[52:59]}"(wr[1][1]))
#define AQ_PIN2() asm volatile("" : "+{v[60:67]}"(wr[2][0]), "+{v[68:75]}"(wr[2][1]))
#define AQ_PIN3() asm volatile("" : "+{v[76:83]}"(wr[3][0]), "+{v[84:91]}"(wr[3][1]))
#define AQ_PIN4() asm volatile("" : "+{v[92:99]}"(wr[4][0]), "+{v[100:107]}"(wr[4][1]))
#define AQ_PIN5() asm volatile("" : "+{v[108:115]}"(wr[5][0]), "+{v[116:123]}"(wr[5][1]))
#define AQ_PIN6() asm volatile("" : "+{v[124:131]}"(wr[6][0]), "+{v[132:139]}"(wr[6][1]))
#define AQ_PIN7() asm volatile("" : "+{v[140:147]}"(wr[7][0]), "+{v[148:155]}"(wr[7][1]))
#define AQ_PIN8() asm volatile("" : "+{v[156:163]}"(wr[8][0]), "+{v[164:171]}"(wr[8][1]))
#define AQ_PIN9() asm volatile("" : "+{v[172:179]}"(wr[9][0]), "+{v[180:187]}"(wr[9][1]))
#define AQ_PIN10() asm volatile("" : "+{v[188:195]}"(wr[10][0]), "+{v[196:203]}"(wr[10][1]))
#define AQ_PIN11() asm volatile("" : "+{v[204:211]}"(wr[11][0]), "+{v[212:219]}"(wr[11][1]))
#define AK_PIN0() asm volatile("" : "+{v[20:27]}"(wr[0][0]), "+{v[28:35]}"(wr[0][1]))
#define AK_PIN1() asm volatile("" : "+{v[36:43]}"(wr[1][0]), "+{v[44:51]}"(wr[1][1]))
#define AK_PIN2() asm volatile("" : "+{v[52:59]}"(wr[2][0]), "+{v[60:67]}"(wr[2][1]))
#define AK_PIN3() asm volatile("" : "+{v[68:75]}"(wr[3][0]), "+{v[76:83]}"(wr[3][1]))
#define AK_PIN4() asm volatile("" : "+{v[84:91]}"(wr[4][0]), "+{v[92:99]}"(wr[4][1]))
#define AK_PIN5() asm volatile("" : "+{v[100:107]}"(wr[5][0]), "+{v[108:115]}"(wr[5][1]))
#define AK_PIN6() asm volatile("" : "+{v[116:123]}"(wr[6][0]), "+{v[124:131]}"(wr[6][1]))
#define AK_PIN7() asm volatile("" : "+{v[132:139]}"(wr[7][0]), "+{v[140:147]}"(wr[7][1]))
#define AK_PIN8() asm volatile("" : "+{v[148:155]}"(wr[8][0]), "+{v[156:163]}"(wr[8][1]))
#define AK_PIN9() asm volatile("" : "+{v[164:171]}"(wr[9][0]), "+{v[172:179]}"(wr[9][1]))
#define AK_PIN10() asm volatile("" : "+{v[180:187]}"(wr[10][0]), "+{v[188:195]}"(wr[10][1]))
#define AK_PIN11() asm volatile("" : "+{v[196:203]}"(wr[11][0]), "+{v[204:211]}"(wr[11][1]))
#define V_PIN() asm volatile("" : "+{v[28:35]}"(wv[0]), "+{v[36:43]}"(wv[1]), "+{v[44:51]}"(wv[2]), "+{v[52:59]}"(wv[3]), "+{v[60:67]}"(wv[4]), "+{v[68:75]}"(wv[5]), "+{v[76:83]}"(wv[6]), "+{v[84:91]}"(wv[7]), "+{v[92:99]}"(wv[8]), "+{v[100:107]}"(wv[9]), "+{v[108:115]}"(wv[10]), "+{v[116:123]}"(wv[11]), "+{v[124:131]}"(wv[12]), "+{v[132:139]}"(wv[13]), "+{v[140:147]}"(wv[14]), "+{v[148:155]}"(wv[15]))
#define WOV_PIN0() asm volatile("" : "+{v[156:163]}"(wo[0][0][0]), "+{v[164:171]}"(wo[0][0][1]), "+{v[172:179]}"(wo[0][1][0]), "+{v[180:187]}"(wo[0][1][1]))
#define WOV_PIN1() asm volatile("" : "+{v[188:195]}"(wo[1][0][0]), "+{v[196:203]}"(wo[1][0][1]), "+{v[204:211]}"(wo[1][1][0]), "+{v[212:219]}"(wo[1][1][1]))
#define WOW_PIN0() asm volatile("" : "+{v[28:35]}"(wo[0][0][0]), "+{v[36:43]}"(wo[0][0][1]), "+{v[44:51]}"(wo[0][1][0]), "+{v[52:59]}"(wo[0][1][1]))
#define WOW_PIN1() asm volatile("" : "+{v[60:67]}"(wo[1][0][0]), "+{v[68:75]}"(wo[1][0][1]), "+{v[76:83]}"(wo[1][1][0]), "+{v[84:91]}"(wo[1][1][1]))
#define WOW_PIN2() asm volatile("" : "+{v[92:99]}"(wo[2][0][0]), "+{v[100:107]}"(wo[2][0][1]), "+{v[108:115]}"(wo[2][1][0]), "+{v[116:123]}"(wo[2][1][1]))
#define WOW_PIN3() asm volatile("" : "+{v[124:131]}"(wo[3][0][0]), "+{v[132:139]}"(wo[3][0][1]), "+{v[140:147]}"(wo[3][1][0]), "+{v[148:155]}"(wo[3][1][1]))
#define WOW_PIN4() asm volatile("" : "+{v[156:163]}"(wo[4][0][0]), "+{v[164:171]}"(wo[4][0][1]), "+{v[172:179]}"(wo[4][1][0]), "+{v[180:187]}"(wo[4][1][1]))
#define WOW_PIN5() asm volatile("" : "+{v[188:195]}"(wo[5][0][0]), "+{v[196:203]}"(wo[5][0][1]), "+{v[204:211]}"(wo[5][1][0]), "+{v[212:219]}"(wo[5][1][1]))
#define A_PIN(QW, i) do { switch (i) { case 0: if (QW) AQ_PIN0(); else AK_PIN0(); break; case 1: if (QW) AQ_PIN1(); else AK_PIN1(); break; case 2: if (QW) AQ_PIN2(); else AK_PIN2(); break; case 3: if (QW) AQ_PIN3(); else AK_PIN3(); break; case 4: if (QW) AQ_PIN4(); else AK_PIN4(); break; case 5: if (QW) AQ_PIN5(); else AK_PIN5(); break; case 6: if (QW) AQ_PIN6(); else AK_PIN6(); break; case 7: if (QW) AQ_PIN7(); else AK_PIN7(); break; case 8: if (QW) AQ_PIN8(); else AK_PIN8(); break; case 9: if (QW) AQ_PIN9(); else AK_PIN9(); break; case 10: if (QW) AQ_PIN10(); else AK_PIN10(); break; case 11: if (QW) AQ_PIN11(); else AK_PIN11(); break; } } while (0)
#define WO_PIN(VW, i) do { switch (i) { case 0: if (VW) WOV_PIN0(); else WOW_PIN0(); break; case 1: if (VW) WOV_PIN1(); else WOW_PIN1(); break; case 2: if (!VW) WOW_PIN2(); break; case 3: if (!VW) WOW_PIN3(); break; case 4: if (!VW) WOW_PIN4(); break; case 5: if (!VW) WOW_PIN5(); break; } } while (0)

__device__ __forceinline__ int lds_byte(int r, int c) {
  const int st = (r >> 4) * 2 + (c >> 5), rr = r & 15, cc = c & 31, ob = rr * 64 + cc * 2;
  return st * 1024 + (ob ^ (((ob >> 9) & 1) << 5));
}
#define G_DvoffB dmaw
#define G_ST1(bufoff, r, so, voff, i) G_D##voff(r, (voff)[i], so, ldsw + (bufoff) + (i) * 8192)
#define G_DvoffA dmas
#define G_STAGE(bufoff, so, voff) do { G_ST1(bufoff, G_R##voff, so, voff, 0); G_ST1(bufoff, G_R##voff, so, voff, 1); } while (0)
#define G_RvoffA ra
#define G_RvoffB rb
#define G_WAIT_V(n) asm volatile("s_waitcnt vmcnt(%0)" :: "n"(n) : "memory")
__device__ __forceinline__ void gemm_h(LAS unsigned char* lds, rsrc_t ra, rsrc_t rb, unsigned cA, unsigned cB, int lda, int ldb, int nt,
                                       bool wide, bool slow, unsigned cN, bool cont, unsigned cur, f32x4 (&acc)[1][2][4][2]) {
  const int wid = __builtin_amdgcn_readfirstlane(threadIdx.x >> 6), lane = opq(0), tid = wid * 64 + lane;
  const int wr = wid >> 2, wc = wid & 3, fr = lane & 15, fq = lane >> 4;
  unsigned voffA[2], voffB[2];
  #pragma unroll
  for (int i = 0; i < 2; ++i) {
    voffA[i] = (unsigned)(((wid >> 1) + 4 * i) * 16 * lda + (wid & 1) * 1024 + lane * 16);
    const int b = tid * 16 + i * 8192, r = b >> 8, pos = (b >> 4) & 15;
    const int c = pos ^ (((r & 3) << 1) | (((r >> 3) & 1) << 3));
    voffB[i] = (unsigned)(r * ldb + c * 16);
  }
  const unsigned kB = 64 * ldb;
  const unsigned ldsw = (unsigned)(size_t)lds + (unsigned)wid * 1024u;
  const int q = (lane & 15) >> 2, p = lane & 3;
  unsigned aoff = (unsigned)(size_t)lds + cur + 32768 + lds_byte(wr * 64 + fr, fq * 8);
  unsigned boff0 = (unsigned)(size_t)lds + cur + (8 * fq + q) * 256 + (((wc * 4 + (p >> 1)) ^ ((q << 1) | ((fq & 1) << 3))) << 4) + 8 * (p & 1), boff1 = boff0 ^ 32;
#define H_STAGE(so, t) do { G_STAGE(so, cB + (t) * kB, voffB); G_STAGE((so) + 32768, cA + (t) * 2048, voffA); if (wide) G_STAGE((so) + 16384, cB + (t) * kB + 256, voffB); } while (0)
  if (!cont) { H_STAGE(0, 0); H_STAGE(49152, 1); H_STAGE(98304, 2); }
  unsigned t = 0, sd, sx, sy, sz;
  if (slow) H_LOOP2S(); else if (wide) H_LOOP2(); else H_LOOP1();
}

__device__ __forceinline__ u32x4 epi_pack(const f32x4 a0, const f32x4 a1) {
  const unsigned x0 = pack2(a0[0], a0[1]), x1 = pack2(a0[2], a0[3]);
  const unsigned y0 = pack2(a1[0], a1[1]), y1 = pack2(a1[2], a1[3]);
  auto s0 = __builtin_amdgcn_permlane16_swap(x0, y0, false, false);
  auto s1 = __builtin_amdgcn_permlane16_swap(x1, y1, false, false);
  return (u32x4){s0[0], s1[0], s0[1], s1[1]};
}
__device__ __forceinline__ unsigned gelu2(unsigned w) {
  const f32x2 x = {lo16(w), hi16(w)};
  const float c0 = -2.f * 1.4426950408889634f * 0.7978845608028654f, c1 = c0 * 0.044715f;
  const f32x2 a = x * (x * x * c1 + c0);
  const f32x2 d = (f32x2){__builtin_amdgcn_exp2f(a[0]), __builtin_amdgcn_exp2f(a[1])} + 1.f;
  const f32x2 y = x * (f32x2){__builtin_amdgcn_rcpf(d[0]), __builtin_amdgcn_rcpf(d[1])};
  return pack2(y[0], y[1]);
}
__device__ __forceinline__ float silu_f(float b) { return b * __builtin_amdgcn_rcpf(1.f + __expf(-b)); }

__device__ __forceinline__ unsigned blk_in(int r, int c) { const int ob = r * 64 + c * 2; return ob ^ (((ob >> 9) & 1) << 5); }
__device__ __forceinline__ unsigned xn_blk(int rb, int cb) { return (unsigned)(((rb >> 1) * 32 + cb) * 2 + (rb & 1)) * 1024u; }

template <int EPI>
__device__ __forceinline__ void epi_rows(const f32x4 (&acc)[1][2][4][2], rsrc_t rs, unsigned obase, int ldc, int pm2, int pn2, int sh, bool wide, int wr, int wc, int ln) {
  const int fq = ln >> 4, cb = 16 * (fq & 1) + 8 * (fq >> 1), fr = (ln & 15) + sh;
  const unsigned lo = (unsigned)((fr >> 4) * 16 * ldc) + blk_in(fr & 15, cb);
  const unsigned ob = (unsigned)(pm2 * 128 + 64 * wr) * (unsigned)ldc + (unsigned)(pn2 * 4 + wc) * 1024u + lo;
  u32x4 r[8];
  if (EPI >= 2) {
    #pragma unroll
    for (int i = 0; i < 8; ++i)
      r[i] = ld16(rs, EPI == 2 ? WS_XN + xn_blk(pm2 * 8 + 4 * wr + (i & 3), pn2 * 4 + 4 * (i >> 2) + wc) + lo : WS_X1 + ob + (i & 3) * 32768 + (i >> 2) * 4096);
  }
  #pragma unroll
  for (int i = 0; i < 8; ++i) {
    if (i == 4 && !wide) break;
    u32x4 w = epi_pack(acc[0][i >> 2][i & 3][0], acc[0][i >> 2][i & 3][1]);
    #pragma unroll
    for (int k = 0; k < 4; ++k) {
      w[k] = EPI < 2 ? gelu2(w[k]) : pack2(lo16(r[i][k]) + lo16(w[k]), hi16(r[i][k]) + hi16(w[k]));
      if (EPI == 3) w[k] = pack2(silu_f(lo16(w[k])), silu_f(hi16(w[k])));
    }
    st16(rs, obase + ob + (unsigned)(16 * (i & 3) * ldc) + (i >> 2) * 4096, w);
  }
}

__device__ __forceinline__ float row_sum16(float v) {
  v += __uint_as_float(__builtin_amdgcn_update_dpp(0, (int)__float_as_uint(v), 0x128, 0xf, 0xf, false));
  v += __uint_as_float(__builtin_amdgcn_update_dpp(0, (int)__float_as_uint(v), 0x124, 0xf, 0xf, false));
  v += __uint_as_float(__builtin_amdgcn_update_dpp(0, (int)__float_as_uint(v), 0x122, 0xf, 0xf, false));
  v += __uint_as_float(__builtin_amdgcn_update_dpp(0, (int)__float_as_uint(v), 0x121, 0xf, 0xf, false));
  return v;
}
__device__ __forceinline__ void ln_rows(u32x4 (&a)[8]) {
  f32x2 v[32];
  #pragma unroll
  for (int i = 0; i < 32; ++i) v[i] = (f32x2){lo16(a[i >> 2][i & 3]), hi16(a[i >> 2][i & 3])};
  f32x2 s[4] = {v[0], v[1], v[2], v[3]};
  #pragma unroll
  for (int i = 4; i < 32; ++i) s[i & 3] += v[i];
  s[0] = (s[0] + s[1]) + (s[2] + s[3]);
  const float mean = row_sum16(s[0][0] + s[0][1]) * (1.f / DM);
  #pragma unroll
  for (int i = 0; i < 32; ++i) { const f32x2 d = v[i] - mean; s[i & 3] = i < 4 ? d * d : s[i & 3] + d * d; }
  s[0] = (s[0] + s[1]) + (s[2] + s[3]);
  const float var = row_sum16(s[0][0] + s[0][1]) * (1.f / DM);
  const float mb = rb1(mean), vb = rb1(var);
  const float rden = __builtin_amdgcn_rcpf(rb1(__builtin_amdgcn_sqrtf(rb1(vb + 1e-5f))));
  #pragma unroll
  for (int i = 0; i < 32; ++i) {
    const f32x2 d = v[i] - mb;
    const unsigned w = pack2(d[0], d[1]);
    const f32x2 y = (f32x2){lo16(w), hi16(w)} * rden;
    a[i >> 2][i & 3] = pack2(y[0], y[1]);
  }
}

template <int N>
__device__ __forceinline__ void fin_sum(rsrc_t rs, unsigned po, u32x4* v) {
  u32x4 pv[N][4];
  #pragma unroll
  for (int i = 0; i < 4 * N; ++i) pv[i >> 2][i & 3] = ld16(rs, po + (i >> 2) * 8192 + (i & 3) * 65536);
  #pragma unroll
  for (int c = 0; c < N; ++c) {
    f32x4 a0, a1;
    #pragma unroll
    for (int k = 0; k < 4; ++k) {
      a0[k] = (lo16(pv[c][0][k]) + lo16(pv[c][1][k])) + (lo16(pv[c][2][k]) + lo16(pv[c][3][k]));
      a1[k] = (hi16(pv[c][0][k]) + hi16(pv[c][1][k])) + (hi16(pv[c][2][k]) + hi16(pv[c][3][k]));
    }
    v[c] = epi_pack(a0, a1);
  }
}
__device__ __forceinline__ void fin_out(const Ptrs& P, unsigned eo, u32x4 v, u32x4 gv, u32x4 xv) {
  #pragma unroll
  for (int k = 0; k < 4; ++k) {
    v[k] = pack2(lo16(v[k]) * lo16(gv[k]), hi16(v[k]) * hi16(gv[k]));
    v[k] = pack2(lo16(xv[k]) + lo16(v[k]), hi16(xv[k]) + hi16(v[k]));
  }
  *(u32x4*)((char*)P.out + eo) = v;
}

#define STG 56
#define SLOWC 6
__device__ __forceinline__ void mlp_role(LAS unsigned char* lds, const Ptrs& P, int hf, int slot, int wid) {
  const int wr = wid >> 2, wc = wid & 3;
  const int p2 = hf * 8 + (slot >> 2), pn = slot & 3;
  char* ws = P.ws;
  const rsrc_t rs = WS_RSRC(P);
  const u16* const p1 = P.w1; const u16* const p2w = P.w2; const u16* const p3 = P.w3; const u16* const p4 = P.w4;
#define M_DONE(c) do { VMWAIT(); BARRIER(); if (wid == 0) cnt_add(ws, c, opq(0)); } while (0)
  BARRIER();
  #pragma unroll
  for (int i = 0; i < 4; ++i) touch(i < 2 ? p3 : p4, DM * FF * 2, (((i & 1) * 64 + hf * 32 + slot) * 512 + wid * 64 + opq(0)) * 128, lds);
  if (hf == 0) touch(p2w, DM * DM * 2, (slot * 512 + wid * 64 + opq(0)) * 128, lds);
  if (STG && hf == 0) { VMWAIT(); for (int i = 0; i < 7 - (slot >> 2); ++i) __builtin_amdgcn_s_sleep(STG); }
  #pragma unroll 1
  for (int code = 0; code < 7; ++code) {
    int ln = opq(0);
    if (wid == 0) {
      if (code == 0) flag_wait(P, F_LN1(p2 * 4 + (ln & 3)), 0, ln);
      else if (code < 3 || code == 6) {
        const unsigned co = CNT_OFF + (code == 1 ? C_W1(min(max(p2 - 1 + min(ln, 2), 0), 15)) : code == 2 ? C_LN(p2) : C_W3(p2)) * 128;
        for (unsigned spins = 0; __any(ld4(rs, co) < 4u);) {
          __builtin_amdgcn_s_sleep(2);
          if (spin_fail(spins, P, ln)) break;
        }
      }
    }
    if (code < 3 || code == 6) BARRIER();
    TLW(3 * code);
    const bool w3 = code >= 2 && code < 6, wide = code != 5;
    const int lda = code == 6 ? 7168 : 2048, ldb = w3 ? 7168 : 2048, c2 = w3 ? (code - 2) * 8 + (wide ? 2 * pn : pn) : 2 * pn;
    const rsrc_t rb = __builtin_amdgcn_make_buffer_rsrc((void*)(code == 0 ? p1 : code == 1 ? p2w : w3 ? p3 : p4), 0, code < 2 ? DM * DM * 2 : DM * FF * 2, RSRC_W3);
    const unsigned cA = (code == 0 ? WS_XN2 : code == 1 ? WS_H1 : w3 ? WS_X1N : WS_H3) + p2 * 128 * lda, cB = c2 * 256;
    f32x4 acq[1][2][4][2];
    gemm_h(lds, rs, rb, cA, cB, lda, ldb, code == 6 ? 56 : 16, wide, hf == 0 && wide && code >= SLOWC, code == 4 ? (24 + pn) * 256 : w3 && wide ? cB + 2048 : cB, w3 && code > 2, code == 3 || code == 4 ? (code - 2) * 49152 : 0, acq);
    TLW(3 * code + 1);
    ln = opq(0);
    if (code == 1) {
      epi_rows<2>(acq, rs, WS_X1, 2048, p2, c2, 0, true, wr, wc, ln);
      M_DONE(C_W2(p2));
      if (wid == 0) cnt_wait(P, C_W2(p2), 4, ln);
      BARRIER();
      const unsigned ro = (unsigned)(p2 * 8 + pn * 2 + wr) * 32768u + ((ln >> 2) & 3) * 1024 + blk_in(wc * 4 + (ln >> 4), (ln & 3) * 8);
      u32x4 v[8];
      #pragma unroll
      for (int i = 0; i < 8; ++i) v[i] = ld16(rs, WS_X1 + ro + i * 4096);
      ln_rows(v);
      #pragma unroll
      for (int i = 0; i < 8; ++i) st16(rs, WS_X1N + ro + i * 4096, v[i]);
      M_DONE(C_LN(p2));
    } else if (code == 6) {
      epi_rows<3>(acq, rs, WS_G, 2048, p2, c2, 0, true, wr, wc, ln);
      M_DONE(C_W4(p2));
    } else {
      VMWAIT();
      epi_rows<1>(acq, rs, code ? WS_H3 : WS_H1, ldb, p2, c2, code ? 0 : (pn & 1) ? -1 - (pn >> 1) : 1 + (pn >> 1), wide, wr, wc, ln);
      if (code == 0 && wid == 0) setup_wait(P, ln);
      if (code == 0 || code == 5) M_DONE(code ? C_W3(p2) : C_W1(p2));
    }
    TLW(3 * code + 2);
  }
}

#define FPC 40
#define FINE 52
__device__ __forceinline__ void fin_role(LAS unsigned char* lds, const Ptrs& P, int wid) {
  const int wr = wid >> 2, wc = wid & 3;
  const rsrc_t rs = WS_RSRC(P);
  volatile LAS int* tk = (volatile LAS int*)(lds + 147456);
  unsigned tks = 0;
#define POP() do { if (wid == 0 && opq(0) == 0) tks = __hip_atomic_fetch_add((gu32*)(P.ws + CNT_OFF + C_HEAD * 128), 1u, RLX_AGENT); } while (0)
  POP();
  for (int round = 0;; ++round) {
    if (wid == 0 && opq(0) == 0) tk[round & 1] = tks;
    LBARRIER();
    const int u = __builtin_amdgcn_readfirstlane(tk[round & 1]);
    if (u >= 2 * FINE + 8 * (XS3 - FINE)) break;
    const bool big = u < 2 * FINE;
    const int sg = big ? u >> 1 : FINE + ((u - 2 * FINE) >> 3), cc0 = big ? (u & 1) * 4 : (u - 2 * FINE) & 7;
    const int ln = opq(0), fq = ln >> 4, cb = 16 * (fq & 1) + 8 * (fq >> 1);
    const unsigned eo = (unsigned)(sg * 32 + wr * 16 + (ln & 15)) * 2048u + (unsigned)(cc0 * 128 + wc * 32 + cb) * 2u;
    const unsigned go = WS_G + (unsigned)(sg * 2 + wr) * 32768u + (cc0 * 4 + wc) * 1024 + blk_in(ln & 15, cb);
    const unsigned po = WS_PT + (unsigned)sg * 262144u + cc0 * 8192 + wr * 4096 + wc * 1024 + ln * 16;
    const int xg = sg < XS0 || sg >= XS2 ? 2 : sg >= XS1, want = sg + 1 - (sg < XS0 ? 0 : xg == 2 ? XS2 - XS0 : xg ? XS1 : XS0);
    u32x4 v[4], gv[4], xv[4];
    if (big) {
      if (wid == 2) flag_wait(P, F_PRG(5 + xg, ln & 31), want, ln);
      BARRIER();
      fin_sum<2>(rs, po, v);
      asm volatile("" : "+v"(v[0]), "+v"(v[1]) :: "memory");
      DELAY(FPC);
      #pragma unroll
      for (int c = 0; c < 4; ++c) xv[c] = __builtin_nontemporal_load((const u32x4*)((const char*)P.x + eo + c * 256));
      fin_sum<2>(rs, po + 16384, v + 2);
      if (wid == 1) cnt_wait(P, C_W4(sg >> 2), 4, ln);
      BARRIER();
      #pragma unroll
      for (int c = 0; c < 4; ++c) gv[c] = ld16(rs, go + c * 4096);
      #pragma unroll
      for (int c = 0; c < 4; ++c) fin_out(P, eo + c * 256, v[c], gv[c], xv[c]);
    } else {
      if (wid == 1) cnt_wait(P, C_W4(sg >> 2), 4, ln);
      BARRIER();
      gv[0] = ld16(rs, go);
      xv[0] = __builtin_nontemporal_load((const u32x4*)((const char*)P.x + eo));
      if (wid == 2) flag_wait<3>(P, F_PRG(5 + xg, (ln & 3) * 8 + cc0), want, ln);
      BARRIER();
      fin_sum<1>(rs, po, v);
      fin_out(P, eo, v[0], gv[0], xv[0]);
    }
    POP();
  }
}

__device__ __forceinline__ u32x4 ldsv(LAS unsigned char* p) { return *(volatile LAS u32x4*)p; }
#define DMA_A dman
#define DMA_V dman
#define DMA_O dmaw
#define TOUCH 0
#define TAUX 0
#define POISON4 ((u32x4){~0u, ~0u, ~0u, ~0u})
#define DLYM 0
#define DLYA 8
#define DLYK (DLYA + 8)
#define DLYO 8
#define FLA 0
#define FLB 0
#define CADM 1
#define CADO 1

#define RTC(id)
#define DCA 3
#define DCB 12
#define DCU 8
#define DL1 -7
#define DL2 6
#define DLB 15
#define ADL 0
#define SN0 150
#define MAP 0
#define HELP 256
#define BORD 0
#define QT 0
__device__ __forceinline__ void track(int& est, int d, int gain) { const int e = d - (est >> 4); est += e > 44 ? DCB * 2 : e * (e > 0 ? max(gain, DCU) : gain); }
__device__ __forceinline__ unsigned rfl(unsigned v) { unsigned r; asm volatile("s_nop 0\n\tv_readfirstlane_b32 %0, %1\n\ts_nop 4" : "=s"(r) : "v"(v)); return r; }
__device__ __forceinline__ void plan_wait(rsrc_t rs, unsigned fo, int s, int& lim, unsigned& tm, unsigned& d) {
  while ((d & 255u) != (unsigned)s && (lim -= DEC1) > 0) {
    __builtin_amdgcn_s_sleep(1);
    const u32x2 v = __builtin_amdgcn_raw_buffer_load_b64(rs, 0, fo, AUX_SC1);
    tm = rfl(v.x); d = rfl(v.y);
  }
  const unsigned now = (unsigned)__builtin_amdgcn_s_memrealtime() * 4u;
  tm = now + min((int)(tm - now), 2000);
  d = min(d >> 8, 1000u);
}
#define SPIN_BUDGET (1 << 21)

__device__ __forceinline__ unsigned rec_go(LAS unsigned char* w, const Ptrs& P, u32x2 f, int wid, int lane) {
  if (wid >= 6) {
    if (__any(f.y != P.tag)) f.x = flag_wait(P, wid == 6 ? F_LN0(lane) : F_CAL(lane), 0, lane);
    if (wid == 7) { const unsigned m = near_half(f.x); if (lane == 0) *(volatile LAS unsigned*)w = m; }
  }
  LBARRIER();
  return __builtin_amdgcn_readfirstlane(*(volatile LAS unsigned*)w);
}

#define SLOT(i) ((i) * 16384)
#define LA_MEM SLOT(8)
#define LA_Q SLOT(9)
#define LA_K (SLOT(9) + 2048)
#define LA_P (SLOT(9) + 4096)
#define LA_KS (SLOT(9) + 6144)
#define LA_VM LA_MEM
#define LA_KX (SLOT(9) + 8192)

template <bool QW>
__device__ __forceinline__ void a_wave(LAS unsigned char* lds, Ptrs& P, int h, int wid) {
  char* ws = P.ws;
  const int lane = opq(0), tid = wid * 64 + lane;
  const int j = wid & 3, g = lane >> 4, q4 = (lane & 15) >> 2, p4 = lane & 3;
  const rsrc_t rs = WS_RSRC(P);
  const rsrc_t rq = __builtin_amdgcn_make_buffer_rsrc((void*)P.Wq, 0, HD * DM * 2, RSRC_W3);
  const rsrc_t rk = __builtin_amdgcn_make_buffer_rsrc((void*)P.Wk, 0, HD * DM * 2, RSRC_W3);
  const unsigned lbase = (unsigned)(size_t)lds;
  int lim = SPIN_BUDGET;
  u32x8 wr[12][2];
  const int trow = 16 * (g >> 1) + 4 * (g & 1) + 8 * (q4 >> 1) + (q4 & 1);
  const int tsw = ((q4 & 1) | ((g & 1) << 1) | ((q4 >> 1) << 2)) << 1;
  const int toff = trow * 256 + (((4 * j + (p4 >> 1)) ^ tsw) << 4) + 8 * (p4 & 1);
  {
    unsigned dv[2];
    #pragma unroll
    for (int x = 0; x < 2; ++x) {
      const int row = 8 * wid + 4 * x + (lane >> 4);
      const int sw = ((row & 1) | (((row >> 2) & 1) << 1) | (((row >> 3) & 1) << 2)) << 1;
      dv[x] = (unsigned)(row * (HD * 2) + h * 256 + (((lane & 15) ^ sw) << 4));
    }
#define A_GRP(n) do { _Pragma("unroll") for (int i = 0; i < 8; ++i) \
      DMA_A(((n) & 1) ? rk : rq, dv[i & 1], (4 * (((n) & 1) ? ((n) >> 1) ^ ((n) >> 2) : (((n) >> 1) + 1) & 3) + (i >> 1)) * 64 * HD * 2, lbase + SLOT(4 * ((n) & 1) + (i >> 1)) + wid * 2048 + (i & 1) * 1024); } while (0)
    A_GRP(0); A_GRP(1);
    #pragma unroll
    for (int n = 0; n < 6; ++n) {
      VMW(8);
      BARRIER();
      if ((n & 1) == (QW ? 0 : 1)) {
        #pragma unroll
        for (int b = 0; b < 4; ++b) {
          LAS unsigned char* t = lds + SLOT(QW ? 0 : 4) + ((b ^ j) << 14);
          wr[4 * (n >> 1) + b][0] = rd_frag<8192, 512>(t + toff);
          wr[4 * (n >> 1) + b][1] = rd_frag<8192, 512>(t + (toff ^ 32));
        }
        #pragma unroll
        for (int b = 0; b < 4; ++b) A_PIN(QW, 4 * (n >> 1) + b);
      }
      LBARRIER();
      A_GRP(n + 2);
    }
  }
  const unsigned cb = comm_base(ws);
  u32x2 f0 = {0u, 0u};
  #pragma unroll
  for (int rd = 0; rd < 2; ++rd) {
    if (rd == 0) VMW(8);
    else {
      if (wid >= 6) f0 = ld8(rs, FLG_OFF + (wid == 6 ? F_LN0(lane) : F_CAL(lane)) * 128);
      const unsigned fv = wid == 3 ? FLG_OFF + F_PRG(min((lane >> 1) & 3, 2), h) * 128 + (lane & 1) * 4 : WS_QF + j * (16u << 20) + h * 8192 + lane * 128;
      if (QW) asm volatile("s_mov_b32 m0, %2\n\ts_nop 0\n\tbuffer_load_dword %0, %1, 0 offen sc1 lds" :: "v"(fv), "s"(rs), "s"(lbase + LA_P + (wid == 3 ? 640 : 1024)) : "memory");
      get_tag(P);
      VMWAIT();
    }
    BARRIER();
    u32x8 f[2][2];
    #pragma unroll
    for (int i = 0; i < 2; ++i) {
      f[i][0] = rd_frag<8192, 512>(lds + SLOT((QW ? 0 : 2) + 4 * rd + i) + toff);
      f[i][1] = rd_frag<8192, 512>(lds + SLOT((QW ? 0 : 2) + 4 * rd + i) + (toff ^ 32));
    }
    asm volatile("" : "+v"(f[0][0]), "+v"(f[0][1]), "+v"(f[1][0]), "+v"(f[1][1]));
    LBARRIER();
    #pragma unroll
    for (int i = 0; i < 2; ++i)
      #pragma unroll
      for (int t = 0; t < 2; ++t) {
        LAS unsigned char* p = lds + SLOT((QW ? 0 : 2) + 4 * rd + i) + j * 4096 + t * 2048 + lane * 16;
        *(LAS u32x4*)p = (u32x4){f[i][t][0], f[i][t][1], f[i][t][2], f[i][t][3]};
        *(LAS u32x4*)(p + 1024) = (u32x4){f[i][t][4], f[i][t][5], f[i][t][6], f[i][t][7]};
      }
  }
  const unsigned mask = rec_go(lds + LA_P + 128, P, f0, wid, lane);
  const int idx = (lane & 1) ? 0xEEEE : 0x4444;
  const unsigned ab_a = lbase + LA_MEM + lane * 16, am_a = ab_a + j * 1024;
  const unsigned t0_a = lbase + SLOT((QW ? 0 : 4) + j) + j * 4096 + lane * 16;
  const unsigned iw_a = lbase + (QW ? LA_Q : LA_K) + j * 512 + q4 * 128 + g * 32 + p4 * 4;
  const unsigned sw_a = lbase + LA_KS + (j >> 1) * 1024 + ((j & 1) * 2 + (q4 >> 1)) * 256 + (4 * g + (p4 & 1)) * 16 + ((q4 & 1) * 2 + (p4 >> 1)) * 4;
  const unsigned qr_a = lbase + LA_Q + g * 128 + (lane & 7) * 16;
  const unsigned kr_a = lbase + LA_K + g * 128 + (2 * q4 + (lane & 1)) * 16;
  const unsigned fr_a = lbase + LA_P + p4 * 4;
  if (tid < 12) *(LAS unsigned*)(lds + LA_P + tid * 4) = 0u;
  const unsigned vo_v = VB_AT(h) + j * 512 + lane * 4, md = lbase + LA_VM - VB_AT(h);
  const unsigned oo_v = OB_AT((lane >> 2) & 1, h) + (j * 16 + (g >> 1) * 8 + p4 * 2 + (g & 1)) * 16;
  const unsigned long long onej = 1ull << j;
  const unsigned lkx = lbase + LA_KX + j * 2048;
  u32x8 oxr = {};
  const unsigned xo_v = (lane >> 5) * 2048 + (lane & 31) * 16, vl_a = lbase + LA_VM + (j & 1) * 1024 + lane * 4;
  const int dq = 16 * (j >> 1) + (lane & 15) - 4 * g;
  const unsigned fb = step_base(cb, mask, 0);
  unsigned tgt = (unsigned)__builtin_amdgcn_s_memrealtime() * 4u;
  unsigned long long tp = 0;
  unsigned pt = 0, pd = 0;
  const unsigned long long fmask = __builtin_amdgcn_readfirstlane((16u >> wid) & 1u);
  int known = 0;
  __builtin_amdgcn_s_setprio(QW ? 2 : 1);
  LBARRIER();
  report(P, wid);
  TL(72);
  for (int s = 0; s < NSEG; ++s) {
    const unsigned sb = step_base(cb, mask, s);
    const unsigned sbm = sb - (lbase + LA_MEM);
    const unsigned fo = (unsigned)(s * NH + h) * 8192u + j * 2048 - (lbase + LA_MEM);
    GSDECL;
    RTC(h);
    lim = __builtin_amdgcn_readfirstlane(lim);
    if (QW) {
      if (wid == 3 && s > 0) {
        if (opq(0) == 0) __builtin_amdgcn_raw_buffer_store_b32((unsigned)tp * 4u - tgt, rs, 0, fb + FL_O(h), AUX_SC1);
        plan_wait(rs, fb + FL_T(s), s, lim, pt, pd);
        tgt = pt;
      }
      if (s >= known) {
        const u32x2 v = *(volatile LAS u32x2*)(lds + LA_P + 640 + (opq(0) & 3) * 8);
        if (!__any(v.y != P.tag)) known = rfl(min(min(__builtin_amdgcn_readlane(v.x, 0), __builtin_amdgcn_readlane(v.x, 1)), __builtin_amdgcn_readlane(v.x, 2)));
      }
      kv_wait(P, s, h, known, opq(0));
      const unsigned kxo = WS_KF + fo, vxo = WS_VF + fo;
      if (wid < 3) for (int i = 0; i < ADL; ++i) __builtin_amdgcn_s_sleep(1);
      {
      dmas(rs, ab_a, kxo, lkx);
      dmas(rs, ab_a, kxo + 1024, lkx + 1024);
      dmas(rs, ab_a, WS_QF + fo, lbase + LA_P + 1024);
      dmas(rs, ab_a, WS_QF + fo + 1024, lbase + LA_P + 1024);
      }
      if (wid == 2 && s + 1 >= known) {
        const int ln = opq(0);
        const unsigned fv = FLG_OFF + F_PRG(min((ln >> 1) & 3, 2), h) * 128 + (ln & 1) * 4;
        asm volatile("s_mov_b32 m0, %2\n\ts_nop 0\n\tbuffer_load_dword %0, %1, 0 offen sc1 lds" :: "v"(fv), "s"(rs), "s"(lbase + LA_P + 640) : "memory");
      }
      unsigned rg = 0x3c000000u;
      A_MAIN_Q();
      TL(s);
      GSFLUSH(0, 24 + wid); GSFLUSH(1, 4 + wid); GSFLUSH(2, 12 + j); GSFLUSH(3, 16 + j); GSFLUSH(4, 20 + j);
      if (s + PD < NSEG && !(opq(0) & 8)) __builtin_amdgcn_raw_buffer_store_b128(poison4(), rs, oo_v, step_base(cb, mask, s + PD), AUX_SC1);
    } else {
      const unsigned qxo = WS_QF + fo - (j & 1) * 2048, oxl = h * 8192 + (j & 1) * 4096 + (j >> 1) * 1024;
      const unsigned oxo = (s == 0 || s == NSEG - 1 ? WS_DUM : WS_OX + (unsigned)(s - 1) * 262144u) + oxl;
      A_MAIN_K();
      if (s >= NSEG - 2) {
        __builtin_amdgcn_raw_buffer_store_b128((u32x4){oxr[0], oxr[1], oxr[2], oxr[3]}, rs, xo_v, WS_OX + (unsigned)s * 262144u + oxl, AUX_SC1);
        __builtin_amdgcn_raw_buffer_store_b128((u32x4){oxr[4], oxr[5], oxr[6], oxr[7]}, rs, xo_v + 512, WS_OX + (unsigned)s * 262144u + oxl, AUX_SC1);
      }
      if (wid == 7) GSFLUSH(0, 28);
      GSFLUSH(1, 4 + wid);
    }
  }
  VMWAIT();
  BARRIER();
  if (wid == 4) flag_set(P, F_PRG(4, h), NSEG, opq(0));
}

#define TSL 4
#define LB_MEM 0
#define LB_RED 16384
template <bool VW>
__device__ __forceinline__ void b_wave(LAS unsigned char* lds, Ptrs& P, int r, int c, int wid, int bs, int xg) {
  constexpr int NHD = VW ? 2 : 6;
  char* ws = P.ws;
  const int lane = opq(0), tid = wid * 64 + lane;
  const int j = wid & 3, g = lane >> 4, q4 = (lane & 15) >> 2, p4 = lane & 3;
  const int b = r * 32 + c;
  const rsrc_t rs = WS_RSRC(P);
  const rsrc_t rv = __builtin_amdgcn_make_buffer_rsrc((void*)P.Wv, 0, HD * DM * 2, RSRC_W3);
  const rsrc_t ro = __builtin_amdgcn_make_buffer_rsrc((void*)P.Wo, 0, HD * DM * 2, RSRC_W3);
  const unsigned lbase = (unsigned)(size_t)lds;
  int lim = SPIN_BUDGET;
  const int h0 = ((VW ? 2 * j : 8 + 6 * j) + 5 * c) & 31;
  u32x8 wv[16], wo[6][2][2];
  {
    if ((wid & 3) < 2 + MAP && xg < 2) {
      const unsigned nk = 8 + 2 * xg, M = (bs * (2 + MAP) + (wid & 3)) * 64 + lane, row = M / nk, k = M - row * nk;
      if (row < HELP) touch(VW ? P.Wq : P.Wk, HD * DM * 2, (row < 256 ? row + (VW ? 512 : 256) : row + 512) * (HD * 2) + min(xg * 4096 + (k >> 1) * 1024 + 256, 7936u) + (k & 1) * 128, lds);
    }
    const int nl = MAP ? (xg == 2 ? 8 : 4) : xg == 2 ? 6 : 5;
    #pragma unroll
    for (int i = 0; i < 2; ++i) {
      const unsigned L = bs * 1024 + wid * 128 + i * 64 + lane, row = L / nl;
      touch(P.Wo, HD * DM * 2, row * (DM * 2) + (c - (bs >> 1)) * 64 + (L - row * nl) * 128, lds);
    }
    VMWAIT();
    BARRIER();
    const int pos = lane >> 2;
    const int prow = (pos & 9) | ((pos & 2) << 1) | ((pos & 4) >> 1);
    const unsigned dv0 = (unsigned)(prow * (DM * 2) + c * 64 + ((lane & 3) << 4));
    const unsigned dv1 = (unsigned)(prow * (DM * 2) + c * 64 + (((lane & 3) ^ 2) << 4));
    const int toff = wid * 16384 + (g * 16 + q4) * 64 + (((p4 >> 1) ^ ((g & 1) << 1)) << 4) + 8 * (p4 & 1);
    #pragma unroll
    for (int rd = 0; rd < NHD / 2; ++rd) {
      #pragma unroll
      for (int hh = 0; hh < 2; ++hh) {
        const int h = (h0 + 2 * rd + hh) & 31;
        #pragma unroll
        for (int i = 0; i < 8; ++i)
          DMA_O(ro, (i & 1) ? dv1 : dv0, (unsigned)(h * 128 + i * 16) * (DM * 2), lbase + wid * 16384 + hh * 8192 + i * 1024);
      }
      VMWAIT();
      #pragma unroll
      for (int hh = 0; hh < 2; ++hh)
        #pragma unroll
        for (int ks = 0; ks < 2; ++ks)
          #pragma unroll
          for (int t = 0; t < 2; ++t)
            wo[2 * rd + hh][ks][t] = rd_frag<4096, 256>(lds + (toff ^ (t * 32)) + hh * 8192 + ks * 512);
      #pragma unroll
      for (int hh = 0; hh < 2; ++hh)
        WO_PIN(VW, 2 * rd + hh);
      asm volatile("s_waitcnt lgkmcnt(0)" ::: "memory");
    }
    const int lr = lane >> 3;
    #pragma unroll
    for (int i = 0; i < 16; ++i) {
      const int sw = (((lr >> 2) & 1) | ((i & 1) << 1)) << 1;
      const unsigned dv = (unsigned)(lr * (HD * 2) + b * 128 + (((lane & 7) ^ sw) << 4));
      DMA_V(rv, dv, (unsigned)(wid * 128 + i * 8) * (HD * 2), lbase + wid * 16384 + i * 1024);
    }
    VMWAIT();
    BARRIER();
    if (VW) {
      const int trow = 16 * (g >> 1) + 4 * (g & 1) + 8 * (q4 >> 1) + (q4 & 1);
      const int tsw = ((g & 1) | ((q4 >> 1) << 1)) << 1;
      LAS unsigned char* tv = lds + trow * 128 + (((2 * j + (p4 >> 1)) ^ tsw) << 4) + 8 * (p4 & 1);
      #pragma unroll
      for (int ks = 0; ks < 16; ++ks) wv[ks] = rd_frag<4096, 256>(tv + ks * 8192);
      V_PIN();
    }
    LBARRIER();
  }
  const unsigned cb = comm_base(ws);
  get_tag(P);
  if (wid == 0) touch(P.x, SEQ * DM * 2, (SEQ - 64 + lane) * 2048 + c * 64, lds);
  const unsigned mask = rec_go(lds + LB_RED + 4096 + 64, P, (u32x2){0u, P.tag - 1u}, wid, lane);
  const int idx = (lane & 1) ? 0xEEEE : 0x4444;
  const unsigned am_a = lbase + LB_MEM + lane * 16;
  const unsigned fb = step_base(cb, mask, 0);
  volatile LAS unsigned* tsl = (volatile LAS unsigned*)(lds + LB_RED + 4096);
  if (tid < 8) tsl[tid] = tid ? 0u : (unsigned)__builtin_amdgcn_s_memrealtime() * 4u;
  unsigned dn = 600;
  int da = 560 * 16, gb = 350 * 16;
  if (VW) __builtin_amdgcn_s_setprio(2);
  LBARRIER();
  report(P, wid);
  TL(72);
  for (int s = 0; s < NSEG; ++s) {
    const unsigned sb = step_base(cb, mask, s);
    const int ln = opq(lane), gg = ln >> 4;
    GSDECL;
    RTC(32 + b);
    const int l16 = ln * 16;
    const int xc = 32 * (c & 1) + 2 * (ln & 15);
    const unsigned mo_v = MF_AT(c >> 1) + ((xc >> 4) * 16 + (4 * r + gg) * 2 + ((xc >> 3) & 1)) * 16 + (xc & 7) * 2;
    if (!VW) {
      const unsigned tm = __builtin_amdgcn_readfirstlane(*tsl), tgt = tm + DLB, la = lbase + LB_MEM + j * 2048 + l16, mo = sb + j * 2048;
      B_POLL();
      if (wid == 7) {
        const int late = (int)((unsigned)__builtin_amdgcn_s_memrealtime() * 4u - tgt) - SN0;
        const unsigned to = tm + dn + (late > 80 ? late : 0);
        if (ln == 0) tsl[1] = to;
        if (b == 0) {
          dn = (da >> 4) + DL1;
          if (ln == 0) { *tsl = to + (gb >> 4) + DL2; st8(rs, fb + FL_T(s + 1), (u32x2){to + (gb >> 4) + DL2, (dn << 8) | (s + 1)}); }
          const unsigned va = ld4(rs, fb + FL_O(ln & 31)), vb = ld4(rs, fb + FL_B(ln));
          if (s > 2) {
            track(da, rfl(wmax(va)), s < 8 ? 5 : DCA);
            track(gb, rfl(wmax(vb)), s < 8 ? 5 : DCA);
          }
        }
      }
      if (wid == 7 && s > 0 && s + 2 < NSEG) __builtin_amdgcn_raw_buffer_store_b32(~0u, rs, mo_v, step_base(cb, mask, s + 2), AUX_SC1);
    } else {
      GS(3);
      const unsigned vo = VB_AT(b >> 1) + ((b & 1) * 4 + j) * 256;
      V_PROJ();
      GS(0); GSFLUSH(0, 4 + j);
      if (s + PD < NSEG && ln < 16) st16(rs, step_base(cb, mask, s + PD) + vo + l16, poison4());
    }
    GSFLUSH(3, 24 + wid);
    if (s + 1 == NSEG) break;
    unsigned so[6];
    #pragma unroll
    for (int i = 0; i < NHD; ++i) { const int h = (h0 + i) & 31; so[i] = __builtin_amdgcn_readfirstlane(sb + OB_AT(r, h)); }
    const unsigned sn = __builtin_amdgcn_readfirstlane(step_base(cb, mask, s + 1));
    const unsigned rw_a = lbase + LB_RED + (wid * 128 + (gg & 1) * 64 + (gg >> 1) * 16 + (ln & 15)) * 4;
    const unsigned rr_a = lbase + LB_RED + ln * 8;
    if (wid == 7) {
      if (b != 0) {
        __builtin_amdgcn_s_sleep(4);
        unsigned tm = 0;
        dn = 0;
        plan_wait(rs, fb + FL_T(s + 1), s + 1, lim, tm, dn);
        if (ln == 0) *tsl = tm;
      }
      const unsigned tgt = rfl(tsl[1]);
      T_WAIT();
    }
    lim = __builtin_amdgcn_readfirstlane(lim);
    unsigned long long t1;
    if (VW) WO_V(); else WO_W();
    if (wid == 0) {
      if (ln == 0) st4(rs, fb + FL_B(b), (unsigned)t1 * 4u - tsl[1]);
    }
    if (wid == 0) {
      GSFLUSH(2, 16);
      TL(s);
    }
    GSFLUSH(1, 8 + wid);
    if (wid == 0 || wid == 4) { GSFLUSH(4, 17 + (wid >> 2)); GSFLUSH(0, 19 + (wid >> 2)); }
  }
  __builtin_amdgcn_s_setprio(0);
  const int ln = opq(0);
  TL(64);
  volatile LAS int* rdy = (volatile LAS int*)(lds + 98304 + 3 * 16384);
  if (tid == 0) *rdy = 61;
  LBARRIER();
#define T_SRC(sg, i) (WS_OX + (unsigned)((sg) * NH + ((h0 + (i)) & 31)) * 8192u + r * 1024)
#define T_MMA(i, k, f) do { const u32x8 w0 = wo[i][(k) & 1][0], w1 = wo[i][(k) & 1][1]; const int e = ((k) >> 1) * 4; \
    a0 = MFMA32(((u32x4){w0[e], w0[e + 1], w0[e + 2], w0[e + 3]}), f, a0); a1 = MFMA32(((u32x4){w1[e], w1[e + 1], w1[e + 2], w1[e + 3]}), f, a1); } while (0)
#define T_DMA(sg, i) do { _Pragma("unroll") for (int k = 0; k < 4; ++k) dmas(rs, ln * 16, T_SRC(sg, i) + k * 2048, lbase + j * 24576 + (i) * 4096 + k * 1024); } while (0)
  const int cb2 = 16 * ((ln >> 4) & 1) + 8 * (ln >> 5);
  const unsigned eo = (unsigned)((NSEG - 1 - j) * 32 + r * 16 + (ln & 15)) * 2048u + (unsigned)(c * 32 + cb2) * 2u;
  u32x4 gv, xv;
  if (VW && wid < NSEG - XS3) {
    xv = *(const u32x4*)((const char*)P.x + eo);
    cnt_wait(P, C_W4(15), 4, ln);
    gv = ld16(rs, WS_G + (unsigned)((NSEG - 1 - j) * 2 + r) * 32768u + c * 1024 + blk_in(ln & 15, cb2));
  }
  bool ahead = false;
  f32x4 k0, k1;
  #pragma unroll 1
  for (int sg = XS3; sg < NSEG; ++sg) {
    f32x4 a0 = zero4(), a1 = zero4();
    if (VW) {
      if (wid == 3) {
        for (int w = __builtin_amdgcn_readfirstlane(*rdy); w <= sg;) {
          w = max(w + 1, -wmax(-(int)flag_wait<TSL>(P, F_PRG(4, ln & 31), w + 1, ln)));
          if (ln == 0) *rdy = w;
        }
      } else while (__builtin_amdgcn_readfirstlane(*rdy) <= sg) __builtin_amdgcn_s_sleep(1);
      u32x4 of[8];
      #pragma unroll
      for (int i = 0; i < 8; ++i) of[i] = __builtin_amdgcn_raw_buffer_load_b128(rs, ln * 16 + (i & 3) * 2048, T_SRC(sg, i >> 2), AUX_SC1);
      #pragma unroll
      for (int i = 0; i < 8; ++i) T_MMA(i >> 2, i & 3, of[i]);
    } else {
      if (!ahead) {
        while (__builtin_amdgcn_readfirstlane(*rdy) <= sg) __builtin_amdgcn_s_sleep(1);
        #pragma unroll
        for (int i = 0; i < NHD; ++i) T_DMA(sg, i);
      }
      VMW(20);
      ahead = __builtin_amdgcn_readfirstlane(*rdy) > sg + 1;
      u32x4 of[2][4];
#define T_RD(i) do { _Pragma("unroll") for (int k = 0; k < 4; ++k) of[(i) & 1][k] = *(const LAS u32x4*)(lds + j * 24576 + (i) * 4096 + k * 1024 + ln * 16); } while (0)
      T_RD(0);
      #pragma unroll
      for (int i = 0; i < NHD; ++i) {
        asm volatile("s_waitcnt lgkmcnt(0)" : "+v"(of[i & 1][0]), "+v"(of[i & 1][1]), "+v"(of[i & 1][2]), "+v"(of[i & 1][3]) :: "memory");
        if (i + 1 < NHD) {
          if (ahead) VMW(16); else G_WAIT_V(16 - 4 * (i < 4 ? i : 4));
          T_RD(i + 1);
        }
        #pragma unroll
        for (int k = 0; k < 4; ++k) {
          if (ahead) dmas(rs, ln * 16, T_SRC(sg + 1, i) + k * 2048, lbase + j * 24576 + i * 4096 + k * 1024);
          T_MMA(i, k, of[i & 1][k]);
          __builtin_amdgcn_sched_barrier(0);
        }
      }
    }
    LAS f32x4* pw = (LAS f32x4*)(lds + 98304 + ((sg & 3) * 8 + wid) * 2048) + ln;
    if (wid == 0 && sg == NSEG - 1) { k0 = a0; k1 = a1; } else { pw[0] = a0; pw[64] = a1; }
  }
  if (VW && wid < NSEG - XS3) {
    LBARRIER();
    const LAS f32x4* pr = (const LAS f32x4*)(lds + 98304 + (3 - j) * 16384) + ln;
    f32x4 t[2];
    #pragma unroll
    for (int k = 0; k < 2; ++k)
      t[k] = (((wid ? pr[k * 64] : k ? k1 : k0) + pr[128 + k * 64]) + (pr[256 + k * 64] + pr[384 + k * 64])) + ((pr[512 + k * 64] + pr[640 + k * 64]) + (pr[768 + k * 64] + pr[896 + k * 64]));
    fin_out(P, eo, epi_pack(t[0], t[1]), gv, xv);
  } else LBARRIER();
}

#define DMA_S dman
#define XPC 20
#define LS_P 131072
template <int MODE>
__device__ __forceinline__ void s_role(LAS unsigned char* lds, Ptrs& P, const u16* W, int h, unsigned obase, int nseg, int fl, int wid, int s0) {
  constexpr bool XM = MODE == 2;
  constexpr unsigned RS = XM ? DM * 2 : HD * 2, SS = XM ? 262144u : 65536u;
  const int lane = opq(0);
  const int kh = wid >> 2, tp = wid & 3;
  const rsrc_t rs = WS_RSRC(P);
  const rsrc_t rw = __builtin_amdgcn_make_buffer_rsrc((void*)W, 0, HD * DM * 2, RSRC_W3);
  const unsigned lbase = (unsigned)(size_t)lds;
  u32x8 w[8][2];
  unsigned f0 = 0;
  {
    const int g = lane >> 4, q4 = (lane & 15) >> 2, p4 = lane & 3;
    const int trow = XM ? 16 * g + 4 * (q4 >> 1) + (q4 & 1) : 8 * g + q4;
#define S_SW(row) ((((row) & 1) | ((((row) >> (XM ? 2 : 1)) & 1) << 1) | ((((row) >> (XM ? 4 : 3)) & 1) << 2)) << 1)
    const int toff = trow * 256 + (((4 * tp + (p4 >> 1)) ^ S_SW(trow)) << 4) + 8 * (p4 & 1);
    unsigned dv[2];
    #pragma unroll
    for (int x = 0; x < 2; ++x) {
      const int row = 8 * wid + 4 * x + (lane >> 4);
      dv[x] = (unsigned)row * RS + (XM ? (h >> 3) * 1024 * RS + (h & 7) * 256 : h * 256) + (((lane & 15) ^ S_SW(row)) << 4);
    }
#define S_IMG(i) do { DMA_S(rw, dv[0], (i) * 64 * RS, lbase + ((i) & 7) * 16384 + wid * 2048); \
                      DMA_S(rw, dv[1], (i) * 64 * RS, lbase + ((i) & 7) * 16384 + wid * 2048 + 1024); if (XM && XPC) __builtin_amdgcn_s_sleep(XPC); } while (0)
    #pragma unroll
    for (int i = 0; i < 8; ++i) S_IMG(i);
    #pragma unroll
    for (int b = 0; b < 4; ++b) {
      if (b == 3 && !XM) { f0 = ld4(rs, FLG_OFF + F_LN0(lane & 7) * 128 + 4); get_tag(P); }
      if (b < 3) VMW(8); else VMWAIT();
      BARRIER();
      if (kh == (b >> 1)) {
        #pragma unroll
        for (int i = 4 * (b & 1); i < 4 * (b & 1) + 4; ++i) {
          w[i][0] = rd_frag<XM ? 2048 : 8192, XM ? 512 : 1024>(lds + toff + i * 16384);
          w[i][1] = rd_frag<XM ? 2048 : 8192, XM ? 512 : 1024>(lds + (toff ^ 32) + i * 16384);
        }
      }
      LBARRIER();
      if (b < 2) {
        #pragma unroll
        for (int i = 4 * b + 8; i < 4 * b + 12; ++i) S_IMG(i);
      }
    }
  }
  TL(XM ? 74 : 72);
  const unsigned l16 = lane * 16;
  volatile LAS int* rd = (volatile LAS int*)(lds + LS_P + 20480);
#define X_READY(want) do { \
    if (wid == 0) { \
      const int n_ = -wmax(-(int)flag_wait(P, F_PRG(4, (h & 24) + (lane & 7)), s0 + (want), lane)) - s0; \
      if (lane == 0) *rd = max(n_, want); \
    } \
    LBARRIER(); } while (0)
  if (MODE == 2) X_READY(2);
  else if (!XM) {
    if (__any(f0 != P.tag)) flag_wait(P, F_LN0(lane & 7), 0, lane);
  }
  unsigned sx = (XM ? WS_OX + s0 * SS + (h >> 3) * 65536 : WS_XN) + kh * 32768 + tp * 4096;
  const unsigned sd = lbase + kh * 65536 + tp * 4096;
  const unsigned xl = kh * 65536 + (XM ? l16 : blk_in(lane & 15, (lane >> 4) * 8));
  const unsigned xp = lbase + xl, pa = lbase + LS_P + tp * 4096 + l16;
  LAS f32x4* const pq = (LAS f32x4*)(lds + LS_P + tp * 4096 + l16);
  if (XM) {
    #pragma unroll
    for (int i = 0; i < 12; ++i) dmas(rs, l16, sx + (i >> 3) * SS + ((i >> 2) & 1) * 16384 + (i & 3) * 1024, sd + (i >> 2) * 16384 + (i & 3) * 1024);
    VMWAIT();
    BARRIER();
  } else {
    #pragma unroll
    for (int i = 0; i < 16; ++i) dmas(rs, l16, sx + (i >> 3) * SS + ((i >> 2) & 1) * 16384 + (i & 3) * 1024, sd + ((i >> 2) ^ 2) * 16384 + (i & 3) * 1024);
    VMW(12);
    BARRIER();
    f32x4 a[4];
    if (MODE) ONE_V(xp + 32768); else ONE_K(xp + 32768);
    pq[kh * 128] = kh ? a[0] : a[MODE ? 1 : 2]; pq[kh * 128 + 64] = kh ? a[MODE ? 2 : 1] : a[3];
    LBARRIER();
    const f32x4 a0 = (kh ? a[MODE ? 1 : 2] : a[0]) + pq[128 - kh * 128], a1 = (kh ? a[3] : a[MODE ? 2 : 1]) + pq[192 - kh * 128];
    if (MODE) st16(rs, obase + h * 8192 + (2 * tp + kh) * 1024 + l16, (u32x4){pack2(a0[0], a0[1]), pack2(a0[2], a0[3]), pack2(a1[0], a1[1]), pack2(a1[2], a1[3])});
    else st16(rs, obase + h * 8192 + kh * 4096 + tp * 1024 + l16, (u32x4){pack2(a0[0], a1[0]), pack2(a0[1], a1[1]), pack2(a0[2], a1[2]), pack2(a0[3], a1[3])});
    sx += SS;
    #pragma unroll
    for (int i = 0; i < 4; ++i) dmas(rs, l16, sx + SS + i * 1024, sd + 32768 + i * 1024);
    VMW(4);
    BARRIER();
    if (wid == 1) flag_set(P, fl, 1, lane);
    TL(62);
  }
  u32x8 xf[3], ac[2] = {};
  #pragma unroll
  for (int i = 0; i < 3; ++i) {
    const u32x4 a = *(const LAS u32x4*)(lds + xl + i * 2048), b = *(const LAS u32x4*)(lds + xl + i * 2048 + 1024);
    xf[i] = (u32x8){a[0], a[1], a[2], a[3], b[0], b[1], b[2], b[3]};
  }
  unsigned long long fm, pm;
  asm volatile("s_cmp_eq_u32 %2, 1\n\ts_cselect_b64 %0, 1, 0\n\ts_cmp_eq_u32 %3, 1\n\ts_cselect_b64 %1, -1, 0" : "=&s"(fm), "=&s"(pm) : "s"(wid), "s"(XM ? 0 : wid >> 1) : "scc");
  const unsigned fo = FLG_OFF + fl * 128, pd = lbase + LS_P + 16384;
  const unsigned po = 4 * 65536 + h * 2048 + (wid & 1) * 1024 - tp * 4096 - kh * 32768;
  unsigned oo = obase + (unsigned)h * 8192u + tp * (MODE == 1 ? 2048 : 1024) + (XM ? (s0 - 1) * 0x40000u : 0u), tmp, tmp2;
  unsigned long long em;
  int t = XM ? 0 : 1;
  if (MODE < 2) {
    #pragma unroll 1
    for (int part = 0; part < 2; ++part) {
      if (part == 1) { TL(63); if (wid == 0) setup_wait(P, lane); }
      if (MODE) S_LOOP_V(part ? nseg + 1 : 3, 0); else S_LOOP_K(part ? nseg + 1 : 3, 0);
    }
  } else {
    #pragma unroll 1
    while (t < nseg) {
      X_READY(min(t + 4, nseg));
      const int n = __builtin_amdgcn_readfirstlane(*rd), te = n >= nseg ? nseg : (n - 2) & ~1;
      TL(t);
      S_LOOP_X(te, nseg);
    }
  }
  TL(XM ? 65 : 64);
  VMWAIT();
  BARRIER();
  if (wid == 5) flag_set(P, fl, nseg, lane);
  if (XM && s0 == 0) {
    sx = WS_OX + (h >> 3) * 65536 + tp * 4096;
#define X_GET(t) do { _Pragma("unroll") for (int i = 0; i < 16; ++i) \
      dmas(rs, l16, sx + (t) * SS + ((i >> 2) & 1) * 32768 + (i >> 3) * 16384 + (i & 3) * 1024, sd + ((i >> 2) & 1) * 65536 + ((t) & 1) * 32768 + (i >> 3) * 16384 + (i & 3) * 1024); } while (0)
    bool have = false;
    const unsigned pv = FLG_OFF + F_PRG(kh ? 7 : 4, kh ? h : (h & 24) + (lane & 7)) * 128;
    if (!kh) __builtin_amdgcn_s_setprio(1);
    #pragma unroll 1
    for (int t = XS2; t < XS3; ++t) {
      if (!have && !kh) {
        flag_wait<2>(P, F_PRG(4, (h & 24) + (lane & 7)), t + 1, lane);
        X_GET(t);
        VMW(8);
      }
      BARRIER();
      f32x4 a[4];
      unsigned pg;
      ONE_X(xp + (t & 1) * 32768);
      if (wid == 5) flag_set(P, fl, t - XS2 + XS0, lane);
      have = t + 1 < XS3 && !__any(pg < (unsigned)(t + 2));
      if (!kh && lane == 0) rd[tp] = have;
      pq[kh * 128] = kh ? a[0] : a[2]; pq[kh * 128 + 64] = kh ? a[1] : a[3];
      if (have && !kh) X_GET(t + 1);
      LBARRIER();
      const f32x4 a0 = (kh ? a[2] : a[0]) + pq[128 - kh * 128], a1 = (kh ? a[3] : a[1]) + pq[192 - kh * 128];
      st16(rs, obase + (unsigned)(t * NH + h) * 8192u + kh * 4096 + tp * 1024 + l16, (u32x4){pack2(a0[0], a1[0]), pack2(a0[1], a1[1]), pack2(a0[2], a1[2]), pack2(a0[3], a1[3])});
      if (__any(rd[lane & 3] == 0)) {
        VMWAIT();
        BARRIER();
        if (wid == 5) flag_set(P, fl, t + 1 - XS2 + XS0, lane);
      } else if (!kh) VMW(9);
      asm volatile("s_waitcnt lgkmcnt(0)" ::: "memory");
      TL(t - XS2 + XS0);
    }
    __builtin_amdgcn_s_setprio(0);
    TL(65);
  }
}

#define LND 1
#define CALN 6
__device__ __forceinline__ void setup_role(LAS unsigned char* lds, Ptrs& P, int cu, int wid) {
  const int lane = opq(0), tid = wid * 64 + lane;
  const rsrc_t rs = WS_RSRC(P);
  u32x4 v[8];
  const u32x4* px = (const u32x4*)(P.x + (size_t)(cu * 32 + wid * 4 + (lane >> 4)) * DM) + (lane & 15);
  #pragma unroll
  for (int i = 0; i < 8; ++i) v[i] = __builtin_nontemporal_load(px + i * 16);
  const unsigned epo = __hip_atomic_load((gu32*)(P.ws + EPO_OFF), RLX_AGENT);
  const unsigned cb = comm_base(P.ws);
  const u32x4 z = {0u, 0u, 0u, 0u};
  const int cr = 63 - cu;
  if (wid == 0 && cr < 16) st16(rs, CNT_OFF + cr * 1024 + lane * 16, z);
  if (cr < 4) st16(rs, cb + FL_B(0) + cr * 8192 + tid * 16, z);
  if (cr == 4 && tid < 256) {
    const int g4 = tid >> 6, k = (tid >> 5) & 1 & (g4 >> 1);
    st16(rs, WS_H1 + ((g4 & 1) ? 127u * 32768u : 0u) + (g4 * 8 + ((tid >> 2) & 7)) * 1024 + blk_in((g4 & 1) ? 15 - k : k, (tid & 3) * 8), z);
  }
  #pragma unroll
  for (int i = 0; i < 2; ++i) {
    const int f = i * 54 + cr;
    if (cr < PD * NCHUNK) {
      u32x4 m = {~0u, ~0u, ~0u, ~0u};
      if (f < 4) {
        const int ks = (f >> 1) * 8 + wid;
        m = *(const u32x4*)(P.mem0 + ((lane >> 1) & 7) * DM + 64 * ks + 16 * (lane >> 4) + 8 * (lane & 1));
        m.x = canon1(m.x); m.y = canon1(m.y); m.z = canon1(m.z); m.w = canon1(m.w);
      }
      st16(rs, cb + (unsigned)(f / (NCHUNK * 2)) * CB_STEP + (f % (NCHUNK * 2)) * 8192u + tid * 16, m);
    }
  }
  const unsigned bi = blk_in((wid & 3) * 4 + (lane >> 4), (lane & 3) * 8);
  const int c0 = (lane >> 2) & 3;
  ln_rows(v);
  TL(69);
  if (cu >= 8) for (int i = 0; i < LND; ++i) __builtin_amdgcn_s_sleep(32);
  #pragma unroll
  for (int i = 0; i < 8; ++i) st16(rs, WS_XN + xn_blk(cu * 2 + (wid >> 2), 4 * i + c0) + bi, v[i]);
  P.tag = __builtin_amdgcn_readfirstlane(epo) + 1u;
  ln_rows(v);
  if (cu < 8) {
    VMWAIT();
    BARRIER();
    if (wid == 0) flag_set(P, F_LN0(cu), 1, lane);
    TL(66);
  }
  asm volatile("" ::: "memory");
  #pragma unroll
  for (int i = 0; i < 8; ++i) st16(rs, WS_XN2 + (unsigned)(cu * 2 + (wid >> 2)) * 32768u + (4 * i + c0) * 1024 + bi, v[i]);
  touch(P.w1, DM * DM * 2, ((cu & 31) * 512 + tid) * 128, lds);
  VMW(9);
  BARRIER();
  if (wid == 0) flag_set(P, F_LN0(cu), 1, lane);
  TL(70);
  if (wid == 0) VMW(2); else VMW(1);
  BARRIER();
  if (wid == 0) flag_set(P, F_LN1(cu), 1, lane);
  TL(71);
  if (cu < 32 && (wid >> 1) == 1) {
    const int id = cu * 2 + (wid & 1);
    unsigned best[2] = {65535u, 65535u};
    #pragma unroll
    for (int it = 0; it < CALN; ++it) {
      const unsigned long long c0 = __builtin_amdgcn_s_memtime();
      asm volatile("s_waitcnt lgkmcnt(0)" ::: "memory");
      if (lane == 0) st4(rs, cb + (id & 3) * CB_STEP + (30 + ((id >> 2) & 1)) * 16384u + (it & 1) * 8192u + (id >> 3) * 1024u, 0u);
      VMWAIT();
      const unsigned d = (unsigned)(__builtin_amdgcn_s_memtime() - c0);
      if (it >= 2) best[it & 1] = min(best[it & 1], d);
    }
    flag_set(P, F_CAL(id), (best[0] << 16) | best[1], lane);
  }
}

#define TLBP 0
#define CPF 1
__global__ __launch_bounds__(512, 2) void mega_kernel(const u16* x, const u16* mem0, const u16* Wq, const u16* Wk, const u16* Wv, const u16* Wo,
                                                     const u16* w1, const u16* w2, const u16* w3, const u16* w4, u16* out, char* ws) {
  __shared__ __attribute__((aligned(16))) unsigned char smem[163840];
  LAS unsigned char* lds = (LAS unsigned char*)smem;
  Ptrs P;
  P.x = x; P.mem0 = mem0; P.Wq = Wq; P.Wk = Wk; P.Wv = Wv; P.Wo = Wo; P.w1 = w1; P.w2 = w2; P.w3 = w3; P.w4 = w4; P.out = out; P.ws = ws;
  const int xcd = blockIdx.x & 7, slot = blockIdx.x >> 3;
  const int wid = __builtin_amdgcn_readfirstlane(threadIdx.x >> 6);
  TL(79);
  P.tag = 0;
  if (wid == 7) {
    unsigned long long pc;
    unsigned len;
    asm volatile("s_getpc_b64 %0\nMK_BEG:\n\ts_mov_b32 %1, MK_END-MK_BEG" : "=s"(pc), "=s"(len));
    touch((const void*)pc, len, (slot * 64 + opq(0)) * 128, lds);
  }
  const int na = xcd == 2 ? 8 : 12;
  const bool isa = xcd < 3 && slot < na;
  const int ah = xcd == 2 ? slot * 4 + 2 : xcd * 16 + (slot & 3) * 4 + (slot >> 2) + (slot >> 3), bs = slot - na, br = bs & 1, bc = (xcd == 1 ? 22 : xcd * 5) + (bs >> 1);
  if (isa) {
    if (wid < 4) a_wave<true>(lds, P, ah, wid); else a_wave<false>(lds, P, ah, wid);
  } else if (xcd < 3) {
    if (wid < 4) b_wave<true>(lds, P, br, bc, wid, bs, xcd); else b_wave<false>(lds, P, br, bc, wid, bs, xcd);
  } else {
    if ((xcd & 3) == 3) {
      setup_role(lds, P, (xcd >> 2) * 32 + slot, wid);
    } else if (xcd == 6) s_role<1>(lds, P, Wv, slot, WS_VF, NSEG, F_PRG(2, slot), wid, 0);
    else s_role<0>(lds, P, xcd == 4 ? Wk : Wq, slot, xcd == 4 ? WS_KF : WS_QF, NSEG, F_PRG(xcd - 4, slot), wid, 0);
    if ((xcd & 3) == 3) mlp_role(lds, P, xcd >> 2, slot, wid);
    else s_role<2>(lds, P, Wo, slot, WS_PT, xcd == 4 ? XS1 - XS0 : xcd == 5 ? XS2 - XS1 : XS0, F_PRG(xcd + 1, slot), wid, xcd == 4 ? XS0 : xcd == 5 ? XS1 : 0);
    report(P, wid);
    fin_role(lds, P, wid);
    if (blockIdx.x == 3 && wid == 0) {
      cnt_wait(P, C_ARR, 256, threadIdx.x);
      if (threadIdx.x == 0) __hip_atomic_store((gu32*)(ws + EPO_OFF), P.tag, RLX_AGENT);
    }
  }
  asm volatile("MK_END:");
  TL(78);
}

extern "C" void kernel_launch(void* const* d_in, const int* in_sizes, int n_in, void* d_out, int out_size,
                              void* d_ws, size_t ws_size, hipStream_t stream) {
  mega_kernel<<<256, 512, 0, stream>>>((const u16*)d_in[0], (const u16*)d_in[1], (const u16*)d_in[2], (const u16*)d_in[3], (const u16*)d_in[4],
                                       (const u16*)d_in[5], (const u16*)d_in[8], (const u16*)d_in[9], (const u16*)d_in[10], (const u16*)d_in[11],
                                       (u16*)d_out, (char*)d_ws);
}
```

```cpp
#include <hip/hip_runtime.h>
#include <hip/hip_bf16.h>
#include <stdint.h>

#define SEQ 2048
#define DM 1024
#define NH 32
#define DH 128
#define HD (NH * DH)
#define MM 8
#define SEG 32
#define NSEG (SEQ / SEG)
#define FF 3584

typedef unsigned short u16;
typedef __attribute__((ext_vector_type(8))) short bf16x8;
typedef __attribute__((ext_vector_type(4))) short bf16x4;
typedef __attribute__((ext_vector_type(4))) float f32x4;
typedef __attribute__((ext_vector_type(4))) unsigned u32x4;
typedef __attribute__((ext_vector_type(2))) unsigned u32x2;
typedef __attribute__((ext_vector_type(2))) float f32x2;
typedef __attribute__((ext_vector_type(2))) __bf16 bf16v2;
typedef __attribute__((ext_vector_type(8))) unsigned u32x8;
#define LAS __attribute__((address_space(3)))
#define GAS __attribute__((address_space(1)))

__device__ __forceinline__ unsigned pack2(float lo, float hi) {
  return __builtin_bit_cast(unsigned, __builtin_convertvector((f32x2){lo, hi}, bf16v2));
}
__device__ __forceinline__ float rb1(float x) { return __uint_as_float(pack2(0.f, x)); }

typedef GAS unsigned gu32;
#define RLX_AGENT __ATOMIC_RELAXED, __HIP_MEMORY_SCOPE_AGENT
#define SPIN_CAP (1u << 20)
#define AUX_SC1 16
#define RSRC_W3 0x00020000
typedef __amdgpu_buffer_rsrc_t rsrc_t;

__device__ __forceinline__ u32x4 ld16(rsrc_t rs, unsigned off) { return __builtin_amdgcn_raw_buffer_load_b128(rs, off, 0, AUX_SC1); }
__device__ __forceinline__ u32x2 ld8(rsrc_t rs, unsigned off) { return __builtin_amdgcn_raw_buffer_load_b64(rs, off, 0, AUX_SC1); }
__device__ __forceinline__ unsigned ld4(rsrc_t rs, unsigned off) { return __builtin_amdgcn_raw_buffer_load_b32(rs, off, 0, AUX_SC1); }
__device__ __forceinline__ void st16(rsrc_t rs, unsigned off, u32x4 v) { __builtin_amdgcn_raw_buffer_store_b128(v, rs, off, 0, AUX_SC1); }
__device__ __forceinline__ void st8(rsrc_t rs, unsigned off, u32x2 v) { __builtin_amdgcn_raw_buffer_store_b64(v, rs, off, 0, AUX_SC1); }
__device__ __forceinline__ void st4(rsrc_t rs, unsigned off, unsigned v) { __builtin_amdgcn_raw_buffer_store_b32(v, rs, off, 0, AUX_SC1); }
__device__ __forceinline__ float lo16(unsigned w) { return __uint_as_float(w << 16); }
__device__ __forceinline__ float hi16(unsigned w) { return __uint_as_float(w & 0xffff0000u); }

__device__ __forceinline__ unsigned canon1(unsigned w) { return min(w, 0xfffffffeu); }
__device__ __forceinline__ unsigned max4(u32x4 v) { return max(max(v.x, v.y), max(v.z, v.w)); }
__device__ __forceinline__ int opq(int) { int v; asm volatile("v_mbcnt_lo_u32_b32 %0, -1, 0\n\tv_mbcnt_hi_u32_b32 %0, -1, %0" : "=v"(v)); return v; }
__device__ __forceinline__ u32x4 poison4() { unsigned m = ~0u; asm volatile("" : "+v"(m)); return (u32x4){m, m, m, m}; }
__device__ __forceinline__ f32x4 zero4() { float m = 0.f; asm volatile("" : "+v"(m)); return (f32x4){m, m, m, m}; }
#define MFMA32(a, b, c) __builtin_amdgcn_mfma_f32_16x16x32_bf16(__builtin_bit_cast(bf16x8, a), __builtin_bit_cast(bf16x8, b), c, 0, 0, 0)
#define MFMA16(a, b, c) __builtin_amdgcn_mfma_f32_16x16x16bf16_1k(__builtin_bit_cast(bf16x4, a), __builtin_bit_cast(bf16x4, b), c, 0, 0, 0)
#define LBARRIER() do { asm volatile("s_waitcnt lgkmcnt(0)" ::: "memory"); __builtin_amdgcn_s_barrier(); asm volatile("" ::: "memory"); } while (0)
#define BARRIER() do { asm volatile("" ::: "memory"); __builtin_amdgcn_s_barrier(); asm volatile("" ::: "memory"); } while (0)
#define VMWAIT() asm volatile("s_waitcnt vmcnt(0)" ::: "memory")
#define VMW(n) asm volatile("s_waitcnt vmcnt(" #n ")" ::: "memory")
#define DELAY(n) do { if ((n) > 0) __builtin_amdgcn_s_sleep(n); } while (0)

#define EPO_OFF 0
#define TMO_OFF 128
#define FLG_OFF 256
#define F_LN0(i) (i)
#define F_LN1(i) (64 + (i))
#define F_CAL(i) (128 + (i))
#define F_PRG(t, h) (192 + (t) * 32 + (h))
#define CNT_OFF 65536
#define C_W1(p2) (p2)
#define C_W2(p2) (16 + (p2))
#define C_LN(p2) (32 + (p2))
#define C_W3(p2) (48 + (p2))
#define C_W4(p2) (64 + (p2))
#define C_HEAD 80
#define C_ARR 81
#define NCNT 128
#define WS_XN (1u << 20)
#define WS_XN2 (WS_XN + (4u << 20))
#define WS_QF (WS_XN2 + (4u << 20))
#define WS_KF (WS_QF + (16u << 20))
#define WS_VF (WS_KF + (16u << 20))
#define WS_DUM (WS_VF + (16u << 20))
#define WS_H1 (WS_DUM + (1u << 20))
#define WS_X1 (WS_H1 + (4u << 20) + 65536u)
#define WS_H3 (WS_X1 + (4u << 20))
#define WS_OX WS_QF
#define WS_X1N WS_H1
#define WS_G WS_XN2
#define WS_PT WS_KF
#define WS_COMM (WS_H3 + (14u << 20))
#define WS_END (WS_COMM + (4u << 20))
#define CB_STEP 524288u
#define PD 3
__device__ __forceinline__ unsigned comm_base(const char* ws) { return WS_COMM + ((0u - ((unsigned)(size_t)ws + WS_COMM)) & 0x1fffffu); }
__device__ __forceinline__ unsigned step_base(unsigned cb, unsigned mask, int s) { return cb + (unsigned)(s & 3) * CB_STEP + mask; }
#define MF_AT(ks) (((ks) >> 3) * 16384u + ((ks) & 7) * 1024u)
#define OB_AT(r, h) ((2u + (r) * 4u + ((h) >> 3)) * 16384u + ((h) & 7) * 1024u)
#define VB_AT(h) ((10u + ((h) >> 2)) * 16384u + ((h) & 3) * 2048u)
#define XS0 4
#define XS1 24
#define XS2 44
#define XS3 62
#define CSL 8
#define FSL 6
#define SQUICK 3
#define NCHUNK 18
#define FL_O(h) (29u * 16384u + (h) * 128u)
#define FL_B(b) (28u * 16384u + (b) * 128u)
#define FL_T(s) FL_O(32 + ((s) & 3))

struct Ptrs { const u16 *x, *mem0, *Wq, *Wk, *Wv, *Wo, *w1, *w2, *w3, *w4; u16* out; char* ws; unsigned tag; };
#define WS_RSRC(P) __builtin_amdgcn_make_buffer_rsrc((void*)(P).ws, 0, WS_END, RSRC_W3)

__device__ __forceinline__ bool spin_fail(unsigned& spins, const Ptrs& P, int lane) {
  asm volatile("" ::: "memory");
  if (++spins > SPIN_CAP) {
    unsigned tv;
    asm volatile("v_mov_b32 %0, %1" : "=v"(tv) : "s"(P.tag));
    if (lane == 0) __builtin_amdgcn_raw_buffer_store_b32(tv, WS_RSRC(P), 0, TMO_OFF, AUX_SC1);
    return true;
  }
  return (spins & 255u) == 0u && (unsigned)__builtin_amdgcn_readfirstlane(__builtin_amdgcn_raw_buffer_load_b32(WS_RSRC(P), 0, TMO_OFF, AUX_SC1)) == P.tag;
}
__device__ __forceinline__ unsigned cnt_load(char* ws, int idx) {
  return __builtin_amdgcn_readfirstlane(__hip_atomic_load((gu32*)(ws + CNT_OFF + idx * 128), RLX_AGENT));
}
__device__ __forceinline__ void cnt_add(char* ws, int idx, int lane) {
  asm volatile("s_waitcnt vmcnt(0)" ::: "memory");
  if (lane == 0) __hip_atomic_fetch_add((gu32*)(ws + CNT_OFF + idx * 128), 1u, RLX_AGENT);
}
__device__ __forceinline__ void cnt_wait(const Ptrs& P, int idx, unsigned want, int lane) {
  for (unsigned spins = 0;;) {
    if (cnt_load(P.ws, idx) >= want) break;
    __builtin_amdgcn_s_sleep(CSL);
    if (spin_fail(spins, P, lane)) break;
  }
}
template <int SL = FSL>
__device__ __forceinline__ unsigned flag_wait(const Ptrs& P, int f, unsigned want, int lane) {
  const rsrc_t rs = WS_RSRC(P);
  u32x2 v;
  for (unsigned spins = 0;;) {
    v = ld8(rs, FLG_OFF + f * 128);
    if (!__any(v.y != P.tag || v.x < want)) break;
    __builtin_amdgcn_s_sleep(SL);
    if (spin_fail(spins, P, lane)) break;
  }
  asm volatile("" ::: "memory");
  return v.x;
}
__device__ __forceinline__ void flag_set(const Ptrs& P, int f, unsigned val, int lane) {
  if (lane == 0) st8(WS_RSRC(P), FLG_OFF + f * 128, (u32x2){val, P.tag});
}
__device__ __forceinline__ void get_tag(Ptrs& P) { P.tag = __builtin_amdgcn_readfirstlane(__hip_atomic_load((gu32*)(P.ws + EPO_OFF), RLX_AGENT)) + 1u; }
__device__ __forceinline__ void setup_wait(const Ptrs& P, int lane) { flag_wait(P, F_LN0(lane), 0, lane); }
__device__ __forceinline__ void report(const Ptrs& P, int wid) {
  if (wid == 0 && opq(0) == 0) __hip_atomic_fetch_add((gu32*)(P.ws + CNT_OFF + C_ARR * 128), 1u, RLX_AGENT);
}
__device__ __forceinline__ void kv_wait(const Ptrs& P, int s, int h, int& known, int lane) {
  if (s < known) return;
  const unsigned v = flag_wait(P, F_PRG(min(lane, 2), h), s + 1, lane);
  known = __builtin_amdgcn_readfirstlane(min(min(__builtin_amdgcn_readlane(v, 0), __builtin_amdgcn_readlane(v, 1)), __builtin_amdgcn_readlane(v, 2)));
}
__device__ __forceinline__ int wmax(int v) {
  v = max(v, __builtin_amdgcn_update_dpp(0, v, 0x128, 0xf, 0xf, false));
  v = max(v, __builtin_amdgcn_update_dpp(0, v, 0x124, 0xf, 0xf, false));
  v = max(v, __builtin_amdgcn_update_dpp(0, v, 0x122, 0xf, 0xf, false));
  v = max(v, __builtin_amdgcn_update_dpp(0, v, 0x121, 0xf, 0xf, false));
  return max(max(__builtin_amdgcn_readlane(v, 0), __builtin_amdgcn_readlane(v, 16)), max(__builtin_amdgcn_readlane(v, 32), __builtin_amdgcn_readlane(v, 48)));
}
__device__ __forceinline__ unsigned near_half(unsigned v) { return wmax(-(int)(v & 65535u)) > wmax(-(int)(v >> 16)) ? 8192u : 0u; }
__device__ __forceinline__ void dmaw(rsrc_t rs, unsigned voff, unsigned soff, unsigned ldsaddr) {
  asm volatile("s_mov_b32 m0, %3\n\ts_nop 0\n\tbuffer_load_dwordx4 %0, %1, %2 offen lds" :: "v"(voff), "s"(rs), "s"(soff), "s"(ldsaddr) : "memory");
}
__device__ __forceinline__ void dman(rsrc_t rs, unsigned voff, unsigned soff, unsigned ldsaddr) {
  asm volatile("s_mov_b32 m0, %3\n\ts_nop 0\n\tbuffer_load_dwordx4 %0, %1, %2 offen nt lds" :: "v"(voff), "s"(rs), "s"(soff), "s"(ldsaddr) : "memory");
}
__device__ __forceinline__ void dmas(rsrc_t rs, unsigned voff, unsigned soff, unsigned ldsaddr) {
  asm volatile("s_mov_b32 m0, %3\n\ts_nop 0\n\tbuffer_load_dwordx4 %0, %1, %2 offen sc1 lds" :: "v"(voff), "s"(rs), "s"(soff), "s"(ldsaddr) : "memory");
}
__device__ __forceinline__ void touch(const void* w, unsigned bytes, unsigned voff, LAS unsigned char* lds) {
  asm volatile("s_mov_b32 m0, %2\n\ts_nop 0\n\tbuffer_load_dword %0, %1, 0 offen lds"
               :: "v"(voff), "s"(__builtin_amdgcn_make_buffer_rsrc((void*)w, 0, bytes, RSRC_W3)), "s"((unsigned)(size_t)lds + 148480) : "memory");
}
__device__ __forceinline__ u32x2 trd(LAS unsigned char* p) {
  return __builtin_bit_cast(u32x2, __builtin_amdgcn_ds_read_tr16_b64_v4i16((LAS bf16x4*)p));
}
template <int BO, int UO>
__device__ __forceinline__ u32x8 rd_frag(LAS unsigned char* p) {
  const u32x2 a = trd(p), b = trd(p + UO), c = trd(p + BO), d = trd(p + BO + UO);
  return (u32x8){a.x, a.y, b.x, b.y, c.x, c.y, d.x, d.y};
}

#define TL(i) do {} while (0)
#define TLW(i) do {} while (0)
#define GSDECL
#define GS(i) do {} while (0)
#define GSFLUSH(k, i) do {} while (0)

#define QPRIO 0
#define VPOS 3
#define VLDI "buffer_load_dword v254, v17, %[rs], %[sb] offen sc1\n\tbuffer_load_dword v255, v17, %[rs], %[sb] offen offset:256 sc1\n\t"
#define VLD(p) VLD##p
#define VLD0
#define VLD1
#define VLD2
#define VLD3 VLDI
#define VLD4
#define DEC1 1
#define DEC2 1
#define DGA
#define DGB
#define DGC
#define DGE
#define DGO
#define DGP
#define DGQ
#define PACE "s_sleep 1\n\t"
#define STR_(x) #x
#define STR(x) STR_(x)
#define GSW
#define GSA(k)
#define GSO(k)
#define A_LAST_Q() \
  asm volatile( \
    "v_mov_b32 v253, %[tag]\n\t" \
    "buffer_load_dwordx4 v[20:23], v12, %[rs], %[vx] offen sc1\n\t" \
    "buffer_load_dwordx4 v[24:27], v12, %[rs], %[vx] offen offset:1024 sc1\n\t" \
    "v_mov_b32 v0, 0\n\t" \
    "v_mov_b32 v1, 0\n\t" \
    "v_mov_b32 v2, 0\n\t" \
    "v_mov_b32 v3, 0\n\t" \
    "v_mov_b32 v4, 0\n\t" \
    "v_mov_b32 v5, 0\n\t" \
    "v_mov_b32 v6, 0\n\t" \
    "v_mov_b32 v7, 0\n\t" \
    "s_cmp_lg_u32 %[sc], 3\n\t" \
    "s_cbranch_scc1 PB_%=\n\t" \
    "s_getpc_b64 s[92:93]\n\t" \
    "TPA_%=:\n\t" \
    "s_add_u32 s92, s92, TEA_%=-TPA_%=\n\t" \
    "s_addc_u32 s93, s93, 0\n\t" \
    "s_lshr_b32 s91, %[tgt], 2\n\t" \
    "s_sub_u32 s91, s91, 3\n\t" \
    "TWA_%=:\n\t" \
    "s_memrealtime s[88:89]\n\t" \
    "s_waitcnt lgkmcnt(0)\n\t" \
    "s_sub_u32 s89, s91, s88\n\t" \
    "s_lshl_b32 s89, s89, 2\n\t" \
    "s_cmp_gt_i32 s89, 0\n\t" \
    "s_cbranch_scc1 TWA_%=\n\t" \
    "s_lshl_b32 s88, s88, 2\n\t" \
    "s_sub_u32 s89, %[tgt], s88\n\t" \
    "s_max_i32 s89, s89, 0\n\t" \
    "s_mul_i32 s89, s89, 11\n\t" \
    "s_lshr_b32 s89, s89, 1\n\t" \
    "s_and_b32 s89, s89, 0x7c\n\t" \
    "s_sub_u32 s92, s92, s89\n\t" \
    "s_subb_u32 s93, s93, 0\n\t" \
    "s_setpc_b64 s[92:93]\n\t" \
    "s_nop 0\n\t" \
    "s_nop 0\n\t" \
    "s_nop 0\n\t" \
    "s_nop 0\n\t" \
    "s_nop 0\n\t" \
    "s_nop 0\n\t" \
    "s_nop 0\n\t" \
    "s_nop 0\n\t" \
    "s_nop 0\n\t" \
    "s_nop 0\n\t" \
    "s_nop 0\n\t" \
    "s_nop 0\n\t" \
    "s_nop 0\n\t" \
    "s_nop 0\n\t" \
    "s_nop 0\n\t" \
    "s_nop 0\n\t" \
    "s_nop 0\n\t" \
    "s_nop 0\n\t" \
    "s_nop 0\n\t" \
    "s_nop 0\n\t" \
    "s_nop 0\n\t" \
    "s_nop 0\n\t" \
    "TEA_%=:\n\t" \
    "PB_%=:\n\t" \
    "s_waitcnt lgkmcnt(0)\n\t" \
    "s_barrier\n\t" \
    "buffer_load_dwordx4 v[220:223], v8, %[rs], %[m0] offen sc1\n\t" \
    "buffer_load_dwordx4 v[224:227], v8, %[rs], %[m1] offen sc1\n\t" \
    "PM_%=:\n\t" \
    "s_waitcnt vmcnt(1)\n\t" \
    "ds_write_b128 v8, v[220:223] offset:0\n\t" \
    "v_max3_u32 v252, v220, v221, v222\n\t" \
    "s_waitcnt vmcnt(0)\n\t" \
    "ds_write_b128 v8, v[224:227] offset:4096\n\t" \
    "v_max3_u32 v252, v252, v223, v224\n\t" \
    "v_max3_u32 v252, v252, v225, v226\n\t" \
    "v_max_u32 v252, v252, v227\n\t" \
    "v_cmp_eq_u32 vcc, -1, v252\n\t" \
    "s_cbranch_vccz PD_%=\n\t" \
    "s_sub_u32 %[lim], %[lim], 1\n\t" \
    "s_cmp_gt_i32 %[lim], 0\n\t" \
    "s_cbranch_scc0 PD_%=\n\t" \
    "s_sleep " STR(CADM) "\n\t" \
    "buffer_load_dwordx4 v[220:223], v8, %[rs], %[m0] offen sc1\n\t" \
    "buffer_load_dwordx4 v[224:227], v8, %[rs], %[m1] offen sc1\n\t" \
    "s_branch PM_%=\n\t" \
    "PD_%=:\n\t" \
    "s_waitcnt lgkmcnt(0)\n\t" \
    "s_barrier\n\t" \
    "s_sleep 6\n\t" \
    "LV_%=:\n\t" \
    "buffer_load_dword v254, v17, %[rs], %[sb] offen sc1\n\t" \
    "buffer_load_dword v255, v17, %[rs], %[sb] offen offset:256 sc1\n\t" \
    "s_sub_u32 %[lim], %[lim], 1\n\t" \
    "s_cmp_gt_i32 %[lim], 0\n\t" \
    "s_waitcnt vmcnt(0)\n\t" \
    "ds_read_b32 v252, v16 offset:16\n\t" \
    "s_cbranch_scc0 LD_%=\n\t" \
    "v_max_u32 v7, v254, v255\n\t" \
    "v_cmp_eq_u32 s[88:89], -1, v7\n\t" \
    "s_waitcnt lgkmcnt(0)\n\t" \
    "v_cmp_ne_u32 vcc, v253, v252\n\t" \
    "s_nop 3\n\t" \
    "s_or_b64 vcc, vcc, s[88:89]\n\t" \
    "s_cbranch_scc1 LV_%=\n\t" \
    "LD_%=:\n\t" \
    "s_waitcnt lgkmcnt(0)\n\t" \
    "v_add_u32 v252, %[vd], v12\n\t" \
    "v_add_u32 v253, %[md], v17\n\t" \
    "ds_write_b128 v252, v[20:23]\n\t" \
    "ds_write_b128 v252, v[24:27] offset:1024\n\t" \
    "ds_write2st64_b32 v253, v254, v255 offset0:0 offset1:1\n\t" \
    "s_waitcnt lgkmcnt(0)\n\t" \
    "s_barrier\n\t" \
    : [lim] "+s"(lim) \
    : "{v8}"(am_a), "{v12}"(ab_a), "{v16}"(fr_a), "{v17}"(vo_v), [rs] "s"(rs), [sb] "s"(sb), [tag] "s"(0xffffff81u + s), [tgt] "s"(tgt), [sc] "s"(wid), [m0] "s"(sbm), [m1] "s"(sbm + 4096u), [vx] "s"(vxo), [vd] "s"(8192 + j * 2048), [md] "s"(md) \
    : "memory", "vcc", "scc", "s88", "s89", "s90", "s91", "s92", "s93", "s94", "s95", "v0", "v1", "v2", "v3", "v4", "v5", "v6", "v7", "v20", "v21", "v22", "v23", "v24", "v25", "v26", "v27", "v220", "v221", "v222", "v223", "v224", "v225", "v226", "v227", "v228", "v229", "v230", "v231", "v232", "v233", "v234", "v235", "v236", "v237", "v238", "v239", "v240", "v241", "v242", "v243", "v244", "v245", "v246", "v247", "v248", "v249", "v250", "v251", "v252", "v253", "v254", "v255")
#define A_MAIN_Q() \
  asm volatile( \
    "v_mov_b32 v253, %[tag]\n\t" \
    "buffer_load_dwordx4 v[20:23], v12, %[rs], %[vx] offen sc1\n\t" \
    "buffer_load_dwordx4 v[24:27], v12, %[rs], %[vx] offen offset:1024 sc1\n\t" \
    "ds_read_b128 v[236:239], v9 offset:0\n\t" \
    "ds_read_b128 v[240:243], v9 offset:1024\n\t" \
    "ds_read_b128 v[244:247], v9 offset:2048\n\t" \
    "ds_read_b128 v[248:251], v9 offset:3072\n\t" \
    "v_mov_b32 v0, 0\n\t" \
    "v_mov_b32 v1, 0\n\t" \
    "v_mov_b32 v2, 0\n\t" \
    "v_mov_b32 v3, 0\n\t" \
    "v_mov_b32 v4, 0\n\t" \
    "v_mov_b32 v5, 0\n\t" \
    "v_mov_b32 v6, 0\n\t" \
    "v_mov_b32 v7, 0\n\t" \
    "s_cmp_lg_u32 %[sc], 3\n\t" \
    "s_cbranch_scc1 PB_%=\n\t" \
    "s_getpc_b64 s[92:93]\n\t" \
    "TPA_%=:\n\t" \
    "s_add_u32 s92, s92, TEA_%=-TPA_%=\n\t" \
    "s_addc_u32 s93, s93, 0\n\t" \
    "s_lshr_b32 s91, %[tgt], 2\n\t" \
    "s_sub_u32 s91, s91, 3\n\t" \
    "TWA_%=:\n\t" \
    "s_memrealtime s[88:89]\n\t" \
    DGE \
    "s_waitcnt lgkmcnt(0)\n\t" \
    "s_sub_u32 s89, s91, s88\n\t" \
    "s_lshl_b32 s89, s89, 2\n\t" \
    "s_cmp_gt_i32 s89, 0\n\t" \
    "s_cbranch_scc1 TWA_%=\n\t" \
    "s_lshl_b32 s88, s88, 2\n\t" \
    "s_sub_u32 s89, %[tgt], s88\n\t" \
    "s_max_i32 s89, s89, 0\n\t" \
    "s_mul_i32 s89, s89, 11\n\t" \
    "s_lshr_b32 s89, s89, 1\n\t" \
    DGB \
    "s_and_b32 s89, s89, 0x7c\n\t" \
    "s_sub_u32 s92, s92, s89\n\t" \
    "s_subb_u32 s93, s93, 0\n\t" \
    "s_setpc_b64 s[92:93]\n\t" \
    "s_nop 0\n\t" \
    "s_nop 0\n\t" \
    "s_nop 0\n\t" \
    "s_nop 0\n\t" \
    "s_nop 0\n\t" \
    "s_nop 0\n\t" \
    "s_nop 0\n\t" \
    "s_nop 0\n\t" \
    "s_nop 0\n\t" \
    "s_nop 0\n\t" \
    "s_nop 0\n\t" \
    "s_nop 0\n\t" \
    "s_nop 0\n\t" \
    "s_nop 0\n\t" \
    "s_nop 0\n\t" \
    "s_nop 0\n\t" \
    "s_nop 0\n\t" \
    "s_nop 0\n\t" \
    "s_nop 0\n\t" \
    "s_nop 0\n\t" \
    "s_nop 0\n\t" \
    "s_nop 0\n\t" \
    "TEA_%=:\n\t" \
    "PB_%=:\n\t" \
    "s_waitcnt lgkmcnt(0)\n\t" \
    "s_barrier\n\t" \
    "buffer_load_dwordx4 v[220:223], v8, %[rs], %[m0] offen sc1\n\t" \
    "buffer_load_dwordx4 v[224:227], v8, %[rs], %[m1] offen sc1\n\t" \
    "PM_%=:\n\t" \
    "s_waitcnt vmcnt(1)\n\t" \
    "ds_write_b128 v8, v[220:223] offset:0\n\t" \
    "v_max3_u32 v252, v220, v221, v222\n\t" \
    "s_waitcnt vmcnt(0)\n\t" \
    "ds_write_b128 v8, v[224:227] offset:4096\n\t" \
    "v_max3_u32 v252, v252, v223, v224\n\t" \
    "v_max3_u32 v252, v252, v225, v226\n\t" \
    "v_max_u32 v252, v252, v227\n\t" \
    "v_cmp_eq_u32 vcc, -1, v252\n\t" \
    "s_cbranch_vccz PD_%=\n\t" \
    "s_sub_u32 %[lim], %[lim], 1\n\t" \
    "s_cmp_gt_i32 %[lim], 0\n\t" \
    "s_cbranch_scc0 PD_%=\n\t" \
    "s_sleep " STR(CADM) "\n\t" \
    "buffer_load_dwordx4 v[220:223], v8, %[rs], %[m0] offen sc1\n\t" \
    "buffer_load_dwordx4 v[224:227], v8, %[rs], %[m1] offen sc1\n\t" \
    "s_branch PM_%=\n\t" \
    "PD_%=:\n\t" \
    GSA(0) \
    "v_smfmac_f32_16x16x64_bf16 v[0:3], v[220:223], v[236:243], v10\n\t" \
    "v_xor_b32 v9, 16384, v9\n\t" \
    "ds_read_b128 v[236:239], v9 offset:0\n\t" \
    "ds_read_b128 v[240:243], v9 offset:1024\n\t" \
    "v_smfmac_f32_16x16x64_bf16 v[4:7], v[220:223], v[244:251], v10\n\t" \
    "ds_read_b128 v[244:247], v9 offset:2048\n\t" \
    "ds_read_b128 v[248:251], v9 offset:3072\n\t" \
    "s_waitcnt lgkmcnt(4)\n\t" \
    "s_barrier\n\t" \
    "ds_read_b128 v[228:231], v8 offset:8192\n\t" \
    "ds_read_b128 v[232:235], v8 offset:12288\n\t" \
    "v_xor_b32 v8, 1024, v8\n\t" \
    "ds_read_b128 v[220:223], v8 offset:4096\n\t" \
    "v_smfmac_f32_16x16x64_bf16 v[0:3], v[224:227], v[28:35], v10\n\t" \
    "v_smfmac_f32_16x16x64_bf16 v[4:7], v[224:227], v[36:43], v10\n\t" \
    "ds_read_b128 v[224:227], v8 offset:0\n\t" \
    "s_waitcnt lgkmcnt(3)\n\t" \
    "v_smfmac_f32_16x16x64_bf16 v[0:3], v[228:231], v[92:99], v10\n\t" \
    "v_smfmac_f32_16x16x64_bf16 v[4:7], v[228:231], v[100:107], v10\n\t" \
    "ds_read_b128 v[228:231], v8 offset:8192\n\t" \
    "s_waitcnt lgkmcnt(3)\n\t" \
    "v_smfmac_f32_16x16x64_bf16 v[0:3], v[232:235], v[156:163], v10\n\t" \
    "v_smfmac_f32_16x16x64_bf16 v[4:7], v[232:235], v[164:171], v10\n\t" \
    "ds_read_b128 v[232:235], v8 offset:12288\n\t" \
    "s_waitcnt lgkmcnt(3)\n\t" \
    "v_smfmac_f32_16x16x64_bf16 v[0:3], v[220:223], v[44:51], v10\n\t" \
    "v_smfmac_f32_16x16x64_bf16 v[4:7], v[220:223], v[52:59], v10\n\t" \
    "v_xor_b32 v8, 3072, v8\n\t" \
    "ds_read_b128 v[220:223], v8 offset:4096\n\t" \
    "s_waitcnt lgkmcnt(3)\n\t" \
    "v_smfmac_f32_16x16x64_bf16 v[0:3], v[224:227], v[236:243], v10\n\t" \
    "v_xor_b32 v9, 49152, v9\n\t" \
    "ds_read_b128 v[236:239], v9 offset:0\n\t" \
    "v_smfmac_f32_16x16x64_bf16 v[4:7], v[224:227], v[244:251], v10\n\t" \
    "ds_read_b128 v[240:243], v9 offset:1024\n\t" \
    "s_waitcnt lgkmcnt(4)\n\t" \
    "v_smfmac_f32_16x16x64_bf16 v[0:3], v[228:231], v[108:115], v10\n\t" \
    "ds_read_b128 v[224:227], v8 offset:8192\n\t" \
    "v_smfmac_f32_16x16x64_bf16 v[4:7], v[228:231], v[116:123], v10\n\t" \
    "ds_read_b128 v[244:247], v9 offset:2048\n\t" \
    "s_waitcnt lgkmcnt(5)\n\t" \
    "v_smfmac_f32_16x16x64_bf16 v[0:3], v[232:235], v[172:179], v10\n\t" \
    "ds_read_b128 v[228:231], v8 offset:0\n\t" \
    "v_smfmac_f32_16x16x64_bf16 v[4:7], v[232:235], v[180:187], v10\n\t" \
    "ds_read_b128 v[248:251], v9 offset:3072\n\t" \
    "s_waitcnt lgkmcnt(6)\n\t" \
    "v_smfmac_f32_16x16x64_bf16 v[0:3], v[220:223], v[60:67], v10\n\t" \
    "ds_read_b128 v[232:235], v8 offset:12288\n\t" \
    "v_smfmac_f32_16x16x64_bf16 v[4:7], v[220:223], v[68:75], v10\n\t" \
    "v_xor_b32 v8, 1024, v8\n\t" \
    "ds_read_b128 v[220:223], v8 offset:4096\n\t" \
    "s_waitcnt lgkmcnt(5)\n\t" \
    "v_smfmac_f32_16x16x64_bf16 v[0:3], v[224:227], v[124:131], v10\n\t" \
    "v_smfmac_f32_16x16x64_bf16 v[4:7], v[224:227], v[132:139], v10\n\t" \
    "ds_read_b128 v[224:227], v8 offset:8192\n\t" \
    "s_waitcnt lgkmcnt(3)\n\t" \
    "v_smfmac_f32_16x16x64_bf16 v[0:3], v[228:231], v[236:243], v10\n\t" \
    "v_xor_b32 v9, 16384, v9\n\t" \
    "ds_read_b128 v[236:239], v9 offset:0\n\t" \
    "v_smfmac_f32_16x16x64_bf16 v[4:7], v[228:231], v[244:251], v10\n\t" \
    "ds_read_b128 v[240:243], v9 offset:1024\n\t" \
    "s_waitcnt lgkmcnt(4)\n\t" \
    "v_smfmac_f32_16x16x64_bf16 v[0:3], v[232:235], v[188:195], v10\n\t" \
    "ds_read_b128 v[228:231], v8 offset:12288\n\t" \
    "v_smfmac_f32_16x16x64_bf16 v[4:7], v[232:235], v[196:203], v10\n\t" \
    "ds_read_b128 v[244:247], v9 offset:2048\n\t" \
    "s_waitcnt lgkmcnt(5)\n\t" \
    "v_smfmac_f32_16x16x64_bf16 v[0:3], v[220:223], v[76:83], v10\n\t" \
    "ds_read_b128 v[232:235], v8 offset:0\n\t" \
    "v_xor_b32 v8, 3072, v8\n\t" \
    "v_smfmac_f32_16x16x64_bf16 v[4:7], v[220:223], v[84:91], v10\n\t" \
    "ds_read_b128 v[248:251], v9 offset:3072\n\t" \
    "s_waitcnt lgkmcnt(6)\n\t" \
    "v_smfmac_f32_16x16x64_bf16 v[0:3], v[224:227], v[140:147], v10\n\t" \
    "ds_read_b128 v[220:223], v12 offset:24576\n\t" \
    "v_smfmac_f32_16x16x64_bf16 v[4:7], v[224:227], v[148:155], v10\n\t" \
    "ds_read_b128 v[224:227], v12 offset:28672\n\t" \
    "s_waitcnt lgkmcnt(5)\n\t" \
    "v_smfmac_f32_16x16x64_bf16 v[0:3], v[228:231], v[204:211], v10\n\t" \
    "v_smfmac_f32_16x16x64_bf16 v[4:7], v[228:231], v[212:219], v10\n\t" \
    "ds_read_b128 v[228:231], v12 offset:25600\n\t" \
    "s_waitcnt lgkmcnt(3)\n\t" \
    "v_smfmac_f32_16x16x64_bf16 v[0:3], v[232:235], v[236:243], v10\n\t" \
    "v_xor_b32 v9, 49152, v9\n\t" \
    "v_smfmac_f32_16x16x64_bf16 v[4:7], v[232:235], v[244:251], v10\n\t" \
    VLD(4) \
    "s_nop 5\n\t" \
    "v_add_f32 v0, v0, v1\n\t" \
    "v_add_f32 v2, v2, v3\n\t" \
    "v_add_f32 v4, v4, v5\n\t" \
    "v_add_f32 v6, v6, v7\n\t" \
    "v_mul_f32 v0, %[sl], v0\n\t" \
    "v_mul_f32 v2, %[sl], v2\n\t" \
    "v_mul_f32 v4, %[sl], v4\n\t" \
    "v_mul_f32 v6, %[sl], v6\n\t" \
    "v_cvt_pk_bf16_f32 v0, v0, v4\n\t" \
    "v_cvt_pk_bf16_f32 v2, v2, v6\n\t" \
    "ds_write2_b32 v11, v0, v2 offset0:0 offset1:4\n\t" \
    "s_mov_b64 exec, %[onej]\n\t" \
    "ds_write_b32 v16, v253\n\t" \
    "s_mov_b64 exec, -1\n\t" \
    GSA(1) \
    "s_setprio 0\n\t" \
    "ds_read_b128 v[232:235], v12 offset:29696\n\t" \
    "s_nop 1\n\t" \
    "ds_read_b128 v[0:3], v12 offset:26624\n\t" \
    "s_nop 1\n\t" \
    "ds_read_b128 v[4:7], v12 offset:27648\n\t" \
    "s_nop 1\n\t" \
    "ds_read_b32 v252, v16\n\t" \
    "ds_read_b128 v[236:239], v14 offset:0\n\t" \
    "s_nop 1\n\t" \
    "ds_read_b128 v[240:243], v14 offset:512\n\t" \
    "s_nop 1\n\t" \
    "ds_read_b128 v[244:247], v14 offset:1024\n\t" \
    "s_nop 1\n\t" \
    "s_setprio 2\n\t" \
    VLD(3) \
    "s_setprio 0\n\t" \
    "ds_read_b128 v[248:251], v14 offset:1536\n\t" \
    "s_nop 1\n\t" \
    "s_setprio 2\n\t" \
    "s_waitcnt lgkmcnt(4)\n\t" \
    "v_cmp_ne_u32 vcc, v253, v252\n\t" \
    "s_cbranch_vccnz RP_%=\n\t" \
    "CP_%=:\n\t" \
    "s_setprio 2\n\t" \
    VLD(0) \
    "s_waitcnt lgkmcnt(2)\n\t" \
    "v_mfma_f32_16x16x32_bf16 v[220:223], v[220:223], v[236:239], 0\n\t" \
    "v_mfma_f32_16x16x32_bf16 v[224:227], v[224:227], v[236:239], 0\n\t" \
    "v_mfma_f32_16x16x32_bf16 v[220:223], v[228:231], v[240:243], v[220:223]\n\t" \
    "ds_read_b128 v[228:231], v12 offset:30720\n\t" \
    "v_mfma_f32_16x16x32_bf16 v[224:227], v[232:235], v[240:243], v[224:227]\n\t" \
    "ds_read_b128 v[232:235], v12 offset:31744\n\t" \
    "s_setprio " STR(QPRIO) "\n\t" \
    VLD(1) \
    "s_waitcnt lgkmcnt(2)\n\t" \
    "v_mfma_f32_16x16x32_bf16 v[220:223], v[0:3], v[244:247], v[220:223]\n\t" \
    "v_mfma_f32_16x16x32_bf16 v[220:223], v[4:7], v[248:251], v[220:223]\n\t" \
    "s_waitcnt lgkmcnt(1)\n\t" \
    "v_mfma_f32_16x16x32_bf16 v[224:227], v[228:231], v[244:247], v[224:227]\n\t" \
    "s_waitcnt lgkmcnt(0)\n\t" \
    "v_mfma_f32_16x16x32_bf16 v[224:227], v[232:235], v[248:251], v[224:227]\n\t" \
    GSA(2) \
    "s_setprio 2\n\t" \
    "s_nop 2\n\t" \
    "v_exp_f32 v220, v220\n\t" \
    "v_exp_f32 v221, v221\n\t" \
    "v_exp_f32 v222, v222\n\t" \
    "v_exp_f32 v223, v223\n\t" \
    "v_exp_f32 v224, v224\n\t" \
    "v_exp_f32 v225, v225\n\t" \
    "v_exp_f32 v226, v226\n\t" \
    "v_exp_f32 v227, v227\n\t" \
    "v_cvt_pk_bf16_f32 v220, v220, v221\n\t" \
    "v_cvt_pk_bf16_f32 v221, v222, v223\n\t" \
    "ds_read_b32 v252, v16 offset:16\n\t" \
    "ds_read_b128 v[0:3], v12 offset:22528\n\t" \
    "ds_read_b128 v[4:7], v12 offset:23552\n\t" \
    VLD(2) \
    "v_cvt_pk_bf16_f32 v222, v224, v225\n\t" \
    "v_cvt_pk_bf16_f32 v223, v226, v227\n\t" \
    "v_mov_b32 v224, 0\n\t" \
    "v_mov_b32 v225, 0\n\t" \
    "v_mfma_f32_16x16x32_bf16 v[228:231], v[20:23], v[220:223], 0\n\t" \
    "v_mov_b32 v226, 0\n\t" \
    "v_mov_b32 v227, 0\n\t" \
    "v_mfma_f32_16x16x32_bf16 v[232:235], v[24:27], v[220:223], 0\n\t" \
    "s_waitcnt lgkmcnt(2)\n\t" \
    "v_cmp_ne_u32 vcc, %[tag], v252\n\t" \
    "s_cbranch_vccnz RK_%=\n\t" \
    "CK_%=:\n\t" \
    "s_waitcnt lgkmcnt(1)\n\t" \
    "v_smfmac_f32_16x16x64_bf16 v[224:227], v[0:3], v[236:243], v10\n\t" \
    "s_waitcnt lgkmcnt(0)\n\t" \
    "v_smfmac_f32_16x16x64_bf16 v[224:227], v[4:7], v[244:251], v10\n\t" \
    "v_mov_b32 v236, %[one]\n\t" \
    "v_mov_b32 v237, %[one]\n\t" \
    "v_mov_b32 v238, %[one]\n\t" \
    "v_mov_b32 v239, %[one]\n\t" \
    "v_mov_b32 v1, 0\n\t" \
    "v_mov_b32 v2, 0\n\t" \
    "v_mfma_f32_16x16x32_bf16 v[240:243], v[236:239], v[220:223], 0\n\t" \
    "s_nop 0\n\t" \
    "v_add_f32 v4, v224, v225\n\t" \
    "v_add_f32 v5, v226, v227\n\t" \
    "v_exp_f32 v4, v4\n\t" \
    "v_exp_f32 v5, v5\n\t" \
    "s_nop 0\n\t" \
    "v_cvt_pk_bf16_f32 v0, v4, v5\n\t" \
    "v_cvt_pk_bf16_f32 v3, v4, v5\n\t" \
    GSA(3) \
    "CM_%=:\n\t" \
    "s_nop 0\n\t" \
    "v_mfma_f32_16x16x16_bf16 v[240:243], v[236:237], v[0:1], v[240:243]\n\t" \
    "s_waitcnt vmcnt(0)\n\t" \
    "v_mfma_f32_16x16x16_bf16 v[228:231], v[254:255], v[0:1], v[228:231]\n\t" \
    "v_max_u32 v7, v254, v255\n\t" \
    "v_mfma_f32_16x16x16_bf16 v[232:235], v[254:255], v[2:3], v[232:235]\n\t" \
    "v_cmp_eq_u32 s[88:89], -1, v7\n\t" \
    "s_not_b64 exec, %[hi]\n\t" \
    "s_nop 1\n\t" \
    "v_rcp_f32 v252, v240\n\t" \
    "v_subrev_u32 v253, 0x21800000, v240\n\t" \
    "v_cmp_gt_u32 vcc, v253, %[rg]\n\t" \
    "v_pk_mul_f32 v[228:229], v[228:229], v[252:253] op_sel_hi:[1,0]\n\t" \
    "v_pk_mul_f32 v[230:231], v[230:231], v[252:253] op_sel_hi:[1,0]\n\t" \
    "v_pk_mul_f32 v[232:233], v[232:233], v[252:253] op_sel_hi:[1,0]\n\t" \
    "v_pk_mul_f32 v[234:235], v[234:235], v[252:253] op_sel_hi:[1,0]\n\t" \
    "s_or_b64 vcc, vcc, s[88:89]\n\t" \
    "v_cvt_pk_bf16_f32 v4, v228, v229\n\t" \
    "v_cvt_pk_bf16_f32 v5, v230, v231\n\t" \
    "v_cvt_pk_bf16_f32 v6, v232, v233\n\t" \
    "v_cvt_pk_bf16_f32 v7, v234, v235\n\t" \
    "s_cbranch_scc1 FX_%=\n\t" \
    "buffer_store_dwordx4 v[4:7], v18, %[rs], %[sb] offen sc1\n\t" \
    "s_memrealtime %[tp]\n\t" \
    DGA \
    GSA(4) \
    "v_max3_u32 v252, v4, v5, v6\n\t" \
    "v_max_u32 v252, v252, v7\n\t" \
    "v_cmp_eq_u32 vcc, -1, v252\n\t" \
    "s_cbranch_vccnz RC_%=\n\t" \
    "CC_%=:\n\t" \
    "s_mov_b64 exec, -1\n\t" \
    "v_add_u32 v252, %[vd], v12\n\t" \
    "v_add_u32 v253, %[md], v17\n\t" \
    "ds_write_b128 v252, v[20:23]\n\t" \
    "ds_write_b128 v252, v[24:27] offset:1024\n\t" \
    "ds_write2st64_b32 v253, v254, v255 offset0:0 offset1:1\n\t" \
    "s_waitcnt lgkmcnt(0)\n\t" \
    "s_cmp_lg_u32 %[sc], 3\n\t" \
    "s_cbranch_scc1 NH_%=\n\t" \
    "buffer_load_dwordx2 v[0:1], off, %[rs], %[fl] sc1\n\t" \
    "s_barrier\n\t" \
    "s_waitcnt vmcnt(0)\n\t" \
    "v_readfirstlane_b32 %[pt], v0\n\t" \
    "v_readfirstlane_b32 %[pd], v1\n\t" \
    "s_nop 3\n\t" \
    "s_branch E_%=\n\t" \
    "NH_%=:\n\t" \
    "s_barrier\n\t" \
    "s_branch E_%=\n\t" \
    "RP_%=:\n\t" \
    "s_waitcnt lgkmcnt(0)\n\t" \
    "ds_read_b32 v252, v16\n\t" \
    "ds_read_b128 v[236:239], v14 offset:0\n\t" \
    "ds_read_b128 v[240:243], v14 offset:512\n\t" \
    "ds_read_b128 v[244:247], v14 offset:1024\n\t" \
    "ds_read_b128 v[248:251], v14 offset:1536\n\t" \
    "s_sub_u32 %[lim], %[lim], " STR(DEC1) "\n\t" \
    "s_cmp_gt_i32 %[lim], 0\n\t" \
    "s_waitcnt lgkmcnt(4)\n\t" \
    "s_cbranch_scc0 CP_%=\n\t" \
    "v_cmp_ne_u32 vcc, %[tag], v252\n\t" \
    "s_cbranch_vccnz RP_%=\n\t" \
    "s_branch CP_%=\n\t" \
    "RK_%=:\n\t" \
    "s_waitcnt lgkmcnt(0)\n\t" \
    "ds_read_b32 v252, v16 offset:16\n\t" \
    "ds_read_b128 v[0:3], v12 offset:22528\n\t" \
    "ds_read_b128 v[4:7], v12 offset:23552\n\t" \
    "s_sub_u32 %[lim], %[lim], " STR(DEC1) "\n\t" \
    "s_cmp_gt_i32 %[lim], 0\n\t" \
    "s_waitcnt lgkmcnt(2)\n\t" \
    "s_cbranch_scc0 CK_%=\n\t" \
    "v_cmp_ne_u32 vcc, %[tag], v252\n\t" \
    "s_cbranch_vccnz RK_%=\n\t" \
    "s_branch CK_%=\n\t" \
    "RC_%=:\n\t" \
    "v_min_u32 v4, -2, v4\n\t" \
    "v_min_u32 v5, -2, v5\n\t" \
    "v_min_u32 v6, -2, v6\n\t" \
    "v_min_u32 v7, -2, v7\n\t" \
    "buffer_store_dwordx4 v[4:7], v18, %[rs], %[sb] offen sc1\n\t" \
    "s_branch CC_%=\n\t" \
    "FX_%=:\n\t" \
    "s_mov_b64 exec, -1\n\t" \
    "s_cmp_eq_u64 s[88:89], 0\n\t" \
    "s_cbranch_scc1 RM_%=\n\t" \
    "buffer_load_dword v254, v17, %[rs], %[sb] offen sc1\n\t" \
    "buffer_load_dword v255, v17, %[rs], %[sb] offen offset:256 sc1\n\t" \
    "v_mfma_f32_16x16x32_bf16 v[228:231], v[20:23], v[220:223], 0\n\t" \
    "v_mfma_f32_16x16x32_bf16 v[232:235], v[24:27], v[220:223], 0\n\t" \
    "v_mfma_f32_16x16x32_bf16 v[240:243], v[236:239], v[220:223], 0\n\t" \
    "s_sub_u32 %[lim], %[lim], " STR(DEC2) "\n\t" \
    "s_cmp_gt_i32 %[lim], 0\n\t" \
    "s_cbranch_scc1 CM_%=\n\t" \
    "s_waitcnt vmcnt(0)\n\t" \
    "v_mov_b32 v254, 0\n\t" \
    "v_mov_b32 v255, 0\n\t" \
    "s_branch CM_%=\n\t" \
    "RM_%=:\n\t" \
    "s_mov_b32 %[rg], -1\n\t" \
    "s_nop 7\n\t" \
    "v_add_f32 v4, v224, v225\n\t" \
    "v_add_f32 v5, v226, v227\n\t" \
    "ds_read_b128 v[236:239], v14 offset:0\n\t" \
    "ds_read_b128 v[240:243], v14 offset:512\n\t" \
    "ds_read_b128 v[244:247], v14 offset:1024\n\t" \
    "ds_read_b128 v[248:251], v14 offset:1536\n\t" \
    "ds_read_b128 v[232:235], v12 offset:24576\n\t" \
    "s_waitcnt lgkmcnt(0)\n\t" \
    "v_mfma_f32_16x16x32_bf16 v[220:223], v[232:235], v[236:239], 0\n\t" \
    "ds_read_b128 v[232:235], v12 offset:25600\n\t" \
    "s_waitcnt lgkmcnt(0)\n\t" \
    "v_mfma_f32_16x16x32_bf16 v[220:223], v[232:235], v[240:243], v[220:223]\n\t" \
    "ds_read_b128 v[232:235], v12 offset:26624\n\t" \
    "s_waitcnt lgkmcnt(0)\n\t" \
    "v_mfma_f32_16x16x32_bf16 v[220:223], v[232:235], v[244:247], v[220:223]\n\t" \
    "ds_read_b128 v[232:235], v12 offset:27648\n\t" \
    "s_waitcnt lgkmcnt(0)\n\t" \
    "v_mfma_f32_16x16x32_bf16 v[220:223], v[232:235], v[248:251], v[220:223]\n\t" \
    "ds_read_b128 v[232:235], v12 offset:28672\n\t" \
    "s_waitcnt lgkmcnt(0)\n\t" \
    "v_mfma_f32_16x16x32_bf16 v[224:227], v[232:235], v[236:239], 0\n\t" \
    "ds_read_b128 v[232:235], v12 offset:29696\n\t" \
    "s_waitcnt lgkmcnt(0)\n\t" \
    "v_mfma_f32_16x16x32_bf16 v[224:227], v[232:235], v[240:243], v[224:227]\n\t" \
    "ds_read_b128 v[232:235], v12 offset:30720\n\t" \
    "s_waitcnt lgkmcnt(0)\n\t" \
    "v_mfma_f32_16x16x32_bf16 v[224:227], v[232:235], v[244:247], v[224:227]\n\t" \
    "ds_read_b128 v[232:235], v12 offset:31744\n\t" \
    "s_waitcnt lgkmcnt(0)\n\t" \
    "v_mfma_f32_16x16x32_bf16 v[224:227], v[232:235], v[248:251], v[224:227]\n\t" \
    "v_max3_f32 v252, v220, v221, v222\n\t" \
    "s_nop 6\n\t" \
    "v_max3_f32 v253, v223, v224, v225\n\t" \
    "v_max3_f32 v252, v252, v226, v227\n\t" \
    "v_max3_f32 v253, v253, v4, v5\n\t" \
    "v_max_f32 v252, v252, v253\n\t" \
    "v_mov_b32 v253, v252\n\t" \
    "v_mov_b32 v6, v252\n\t" \
    "s_nop 1\n\t" \
    "v_permlane16_swap_b32 v253, v6\n\t" \
    "v_max_f32 v252, v253, v6\n\t" \
    "v_max_f32 v253, v253, v6\n\t" \
    "s_nop 1\n\t" \
    "v_permlane32_swap_b32 v252, v253\n\t" \
    "v_max_f32 v252, v252, v253\n\t" \
    "v_sub_f32 v220, v220, v252\n\t" \
    "v_sub_f32 v221, v221, v252\n\t" \
    "v_sub_f32 v222, v222, v252\n\t" \
    "v_sub_f32 v223, v223, v252\n\t" \
    "v_sub_f32 v224, v224, v252\n\t" \
    "v_sub_f32 v225, v225, v252\n\t" \
    "v_sub_f32 v226, v226, v252\n\t" \
    "v_sub_f32 v227, v227, v252\n\t" \
    "v_sub_f32 v4, v4, v252\n\t" \
    "v_sub_f32 v5, v5, v252\n\t" \
    "v_exp_f32 v220, v220\n\t" \
    "v_exp_f32 v221, v221\n\t" \
    "v_exp_f32 v222, v222\n\t" \
    "v_exp_f32 v223, v223\n\t" \
    "v_exp_f32 v224, v224\n\t" \
    "v_exp_f32 v225, v225\n\t" \
    "v_exp_f32 v226, v226\n\t" \
    "v_exp_f32 v227, v227\n\t" \
    "v_cvt_pk_bf16_f32 v220, v220, v221\n\t" \
    "v_cvt_pk_bf16_f32 v221, v222, v223\n\t" \
    "v_cvt_pk_bf16_f32 v222, v224, v225\n\t" \
    "v_cvt_pk_bf16_f32 v223, v226, v227\n\t" \
    "v_mov_b32 v236, %[one]\n\t" \
    "v_mov_b32 v237, %[one]\n\t" \
    "v_mov_b32 v238, %[one]\n\t" \
    "v_mov_b32 v239, %[one]\n\t" \
    "v_mfma_f32_16x16x32_bf16 v[228:231], v[20:23], v[220:223], 0\n\t" \
    "v_mfma_f32_16x16x32_bf16 v[232:235], v[24:27], v[220:223], 0\n\t" \
    "v_mfma_f32_16x16x32_bf16 v[240:243], v[236:239], v[220:223], 0\n\t" \
    "v_exp_f32 v4, v4\n\t" \
    "v_exp_f32 v5, v5\n\t" \
    "s_nop 0\n\t" \
    "v_cvt_pk_bf16_f32 v0, v4, v5\n\t" \
    "v_cvt_pk_bf16_f32 v3, v4, v5\n\t" \
    "s_nop 1\n\t" \
    "s_branch CM_%=\n\t" \
    "E_%=:\n\t" \
    GSW \
    "s_nop 1\n\t" \
    : [lim] "+s"(lim), [rg] "+s"(rg), [tp] "=&s"(tp), [pt] "=&s"(pt), [pd] "=&s"(pd) DGO DGP GSO(0) GSO(1) GSO(2) GSO(3) GSO(4) \
    : "{v8}"(am_a), "{v9}"(t0_a), "{v10}"(idx), "{v11}"(iw_a), "{v12}"(ab_a), "{v14}"(qr_a), "{v16}"(fr_a), "{v17}"(vo_v), "{v18}"(oo_v), [rs] "s"(rs), [sb] "s"(sb), [tag] "s"(0xffffff81u + s), [hi] "s"(0xff00ff00ff00ff00ull), [tgt] "s"(tgt), [sc] "s"(wid), [onej] "s"(onej), [sl] "s"(0x3e0293ee), [one] "s"(0x3f803f80), [m0] "s"(sbm), [m1] "s"(sbm + 4096u), [vx] "s"(vxo), [fl] "s"(fb + FL_T(s + 1)), [vd] "s"(8192 + j * 2048), [md] "s"(md), "{v[28:35]}"(wr[0][0]), "{v[36:43]}"(wr[0][1]), "{v[44:51]}"(wr[1][0]), "{v[52:59]}"(wr[1][1]), "{v[60:67]}"(wr[2][0]), "{v[68:75]}"(wr[2][1]), "{v[76:83]}"(wr[3][0]), "{v[84:91]}"(wr[3][1]), "{v[92:99]}"(wr[4][0]), "{v[100:107]}"(wr[4][1]), "{v[108:115]}"(wr[5][0]), "{v[116:123]}"(wr[5][1]), "{v[124:131]}"(wr[6][0]), "{v[132:139]}"(wr[6][1]), "{v[140:147]}"(wr[7][0]), "{v[148:155]}"(wr[7][1]), "{v[156:163]}"(wr[8][0]), "{v[164:171]}"(wr[8][1]), "{v[172:179]}"(wr[9][0]), "{v[180:187]}"(wr[9][1]), "{v[188:195]}"(wr[10][0]), "{v[196:203]}"(wr[10][1]), "{v[204:211]}"(wr[11][0]), "{v[212:219]}"(wr[11][1]) \
    : "memory", "vcc", "scc", "s88", "s89", "s90", "s91", "s92", "s93", "s94", "s95", "v0", "v1", "v2", "v3", "v4", "v5", "v6", "v7", "v20", "v21", "v22", "v23", "v24", "v25", "v26", "v27", "v220", "v221", "v222", "v223", "v224", "v225", "v226", "v227", "v228", "v229", "v230", "v231", "v232", "v233", "v234", "v235", "v236", "v237", "v238", "v239", "v240", "v241", "v242", "v243", "v244", "v245", "v246", "v247", "v248", "v249", "v250", "v251", "v252", "v253", "v254", "v255")

#define A_MAIN_K() \
  asm volatile( \
    "ds_read_b128 v[236:239], v9 offset:0\n\t" \
    "ds_read_b128 v[240:243], v9 offset:1024\n\t" \
    "ds_read_b128 v[244:247], v9 offset:2048\n\t" \
    "ds_read_b128 v[248:251], v9 offset:3072\n\t" \
    "v_mov_b32 v0, 0\n\t" \
    "v_mov_b32 v1, 0\n\t" \
    "v_mov_b32 v2, 0\n\t" \
    "v_mov_b32 v3, 0\n\t" \
    "v_mov_b32 v4, 0\n\t" \
    "v_mov_b32 v5, 0\n\t" \
    "v_mov_b32 v6, 0\n\t" \
    "v_mov_b32 v7, 0\n\t" \
    "s_waitcnt lgkmcnt(0)\n\t" \
    "s_barrier\n\t" \
    "buffer_load_dwordx4 v[212:215], v8, %[rs], %[m0] offen sc1\n\t" \
    "buffer_load_dwordx4 v[216:219], v8, %[rs], %[m1] offen sc1\n\t" \
    "v_mov_b32 v252, v252\n\t" \
    "s_nop 2\n\t" \
    "v_mov_b32 v252, v252\n\t" \
    "s_nop 2\n\t" \
    "v_mov_b32 v252, v252\n\t" \
    "s_nop 2\n\t" \
    "v_mov_b32 v252, v252\n\t" \
    "s_nop 2\n\t" \
    "buffer_store_dwordx4 v[220:223], v14, %[rs], %[ox] offen offset:0 sc1\n\t" \
    "buffer_store_dwordx4 v[224:227], v14, %[rs], %[ox] offen offset:512 sc1\n\t" \
    GSA(3) \
    "v_mov_b32 v252, v252\n\t" \
    "s_nop 2\n\t" \
    "v_mov_b32 v252, v252\n\t" \
    "s_nop 2\n\t" \
    "v_mov_b32 v252, v252\n\t" \
    "s_nop 2\n\t" \
    "v_mov_b32 v252, v252\n\t" \
    "s_nop 2\n\t" \
    "v_mov_b32 v252, v252\n\t" \
    "s_nop 2\n\t" \
    "v_mov_b32 v252, v252\n\t" \
    "s_nop 2\n\t" \
    "v_mov_b32 v252, v252\n\t" \
    "s_nop 2\n\t" \
    "v_mov_b32 v252, v252\n\t" \
    "s_nop 2\n\t" \
    "v_mov_b32 v252, v252\n\t" \
    "s_nop 2\n\t" \
    "v_mov_b32 v252, v252\n\t" \
    "s_nop 2\n\t" \
    "v_mov_b32 v252, v252\n\t" \
    "s_nop 2\n\t" \
    "v_mov_b32 v252, v252\n\t" \
    "s_nop 2\n\t" \
    "v_mov_b32 v252, v252\n\t" \
    "s_nop 2\n\t" \
    "v_mov_b32 v252, v252\n\t" \
    "s_nop 2\n\t" \
    "v_mov_b32 v252, v252\n\t" \
    "s_nop 2\n\t" \
    "v_mov_b32 v252, v252\n\t" \
    "s_nop 2\n\t" \
    "v_mov_b32 v252, v252\n\t" \
    "s_nop 2\n\t" \
    "v_mov_b32 v252, v252\n\t" \
    "s_nop 2\n\t" \
    "PM_%=:\n\t" \
    "s_waitcnt vmcnt(3)\n\t" \
    "ds_write_b128 v8, v[212:215] offset:8192\n\t" \
    "v_max3_u32 v252, v212, v213, v214\n\t" \
    "s_waitcnt vmcnt(2)\n\t" \
    "ds_write_b128 v8, v[216:219] offset:12288\n\t" \
    "v_max3_u32 v252, v252, v215, v216\n\t" \
    "v_max3_u32 v252, v252, v217, v218\n\t" \
    "v_max_u32 v252, v252, v219\n\t" \
    "v_cmp_eq_u32 vcc, -1, v252\n\t" \
    "s_cbranch_vccz PD_%=\n\t" \
    "s_sub_u32 %[lim], %[lim], 1\n\t" \
    "s_cmp_gt_i32 %[lim], 0\n\t" \
    "s_cbranch_scc0 PD_%=\n\t" \
    "s_sleep " STR(CADM) "\n\t" \
    "buffer_load_dwordx4 v[212:215], v8, %[rs], %[m0] offen sc1\n\t" \
    "buffer_load_dwordx4 v[216:219], v8, %[rs], %[m1] offen sc1\n\t" \
    "s_waitcnt vmcnt(0)\n\t" \
    "s_branch PM_%=\n\t" \
    "PD_%=:\n\t" \
    GSA(0) \
    "v_smfmac_f32_16x16x64_bf16 v[0:3], v[212:215], v[236:243], v10\n\t" \
    "v_xor_b32 v9, 16384, v9\n\t" \
    "v_smfmac_f32_16x16x64_bf16 v[4:7], v[212:215], v[244:251], v10\n\t" \
    "s_waitcnt lgkmcnt(0)\n\t" \
    "s_barrier\n\t" \
    DGA \
    "s_cmp_lg_u32 %[st], 63\n\t" \
    "s_cbranch_scc1 NL_%=\n\t" \
    "s_mov_b64 exec, %[fm]\n\t" \
    "v_mov_b32 v252, %[st]\n\t" \
    "v_mov_b32 v253, %[tag]\n\t" \
    "buffer_store_dwordx2 v[252:253], off, %[rs], %[po] sc1\n\t" \
    "s_mov_b64 exec, -1\n\t" \
    "NL_%=:\n\t" \
    "ds_read_b128 v[236:239], v9 offset:0\n\t" \
    "ds_read_b128 v[240:243], v9 offset:1024\n\t" \
    "ds_read_b128 v[244:247], v9 offset:2048\n\t" \
    "ds_read_b128 v[248:251], v9 offset:3072\n\t" \
    "ds_read_b128 v[220:223], v8 offset:0\n\t" \
    "ds_read_b128 v[224:227], v8 offset:4096\n\t" \
    "v_xor_b32 v8, 1024, v8\n\t" \
    "ds_read_b128 v[228:231], v8 offset:8192\n\t" \
    "v_smfmac_f32_16x16x64_bf16 v[0:3], v[216:219], v[148:155], v10\n\t" \
    "ds_read_b128 v[232:235], v8 offset:12288\n\t" \
    "v_smfmac_f32_16x16x64_bf16 v[4:7], v[216:219], v[156:163], v10\n\t" \
    "ds_read_b128 v[212:215], v8 offset:0\n\t" \
    "s_waitcnt lgkmcnt(4)\n\t" \
    "v_smfmac_f32_16x16x64_bf16 v[0:3], v[220:223], v[20:27], v10\n\t" \
    "ds_read_b128 v[216:219], v8 offset:4096\n\t" \
    "v_smfmac_f32_16x16x64_bf16 v[4:7], v[220:223], v[28:35], v10\n\t" \
    "v_xor_b32 v8, 3072, v8\n\t" \
    "ds_read_b128 v[220:223], v8 offset:12288\n\t" \
    "s_waitcnt lgkmcnt(5)\n\t" \
    "v_smfmac_f32_16x16x64_bf16 v[0:3], v[224:227], v[84:91], v10\n\t" \
    "v_smfmac_f32_16x16x64_bf16 v[4:7], v[224:227], v[92:99], v10\n\t" \
    "ds_read_b128 v[224:227], v8 offset:0\n\t" \
    "s_waitcnt lgkmcnt(5)\n\t" \
    "v_smfmac_f32_16x16x64_bf16 v[0:3], v[228:231], v[236:243], v10\n\t" \
    "v_xor_b32 v9, 49152, v9\n\t" \
    "ds_read_b128 v[236:239], v9 offset:0\n\t" \
    "v_smfmac_f32_16x16x64_bf16 v[4:7], v[228:231], v[244:251], v10\n\t" \
    "ds_read_b128 v[240:243], v9 offset:1024\n\t" \
    "s_waitcnt lgkmcnt(6)\n\t" \
    "v_smfmac_f32_16x16x64_bf16 v[0:3], v[232:235], v[164:171], v10\n\t" \
    "ds_read_b128 v[244:247], v9 offset:2048\n\t" \
    "v_smfmac_f32_16x16x64_bf16 v[4:7], v[232:235], v[172:179], v10\n\t" \
    "ds_read_b128 v[248:251], v9 offset:3072\n\t" \
    "s_waitcnt lgkmcnt(7)\n\t" \
    "v_smfmac_f32_16x16x64_bf16 v[0:3], v[212:215], v[36:43], v10\n\t" \
    "ds_read_b128 v[228:231], v8 offset:8192\n\t" \
    "v_smfmac_f32_16x16x64_bf16 v[4:7], v[212:215], v[44:51], v10\n\t" \
    "ds_read_b128 v[232:235], v8 offset:4096\n\t" \
    "s_waitcnt lgkmcnt(8)\n\t" \
    "v_smfmac_f32_16x16x64_bf16 v[0:3], v[216:219], v[100:107], v10\n\t" \
    "v_xor_b32 v8, 1024, v8\n\t" \
    "ds_read_b128 v[212:215], v8 offset:12288\n\t" \
    "v_smfmac_f32_16x16x64_bf16 v[4:7], v[216:219], v[108:115], v10\n\t" \
    "ds_read_b128 v[216:219], v8 offset:0\n\t" \
    "s_waitcnt lgkmcnt(9)\n\t" \
    "v_smfmac_f32_16x16x64_bf16 v[0:3], v[220:223], v[180:187], v10\n\t" \
    "v_smfmac_f32_16x16x64_bf16 v[4:7], v[220:223], v[188:195], v10\n\t" \
    "ds_read_b128 v[220:223], v8 offset:4096\n\t" \
    "s_waitcnt lgkmcnt(9)\n\t" \
    "v_smfmac_f32_16x16x64_bf16 v[0:3], v[224:227], v[52:59], v10\n\t" \
    "v_smfmac_f32_16x16x64_bf16 v[4:7], v[224:227], v[60:67], v10\n\t" \
    "ds_read_b128 v[224:227], v8 offset:8192\n\t" \
    "v_xor_b32 v8, 3072, v8\n\t" \
    "s_waitcnt lgkmcnt(5)\n\t" \
    "v_smfmac_f32_16x16x64_bf16 v[0:3], v[228:231], v[236:243], v10\n\t" \
    "v_xor_b32 v9, 16384, v9\n\t" \
    "ds_read_b128 v[236:239], v9 offset:0\n\t" \
    "v_smfmac_f32_16x16x64_bf16 v[4:7], v[228:231], v[244:251], v10\n\t" \
    "ds_read_b128 v[240:243], v9 offset:1024\n\t" \
    "s_waitcnt lgkmcnt(6)\n\t" \
    "v_smfmac_f32_16x16x64_bf16 v[0:3], v[232:235], v[116:123], v10\n\t" \
    "ds_read_b128 v[244:247], v9 offset:2048\n\t" \
    "v_smfmac_f32_16x16x64_bf16 v[4:7], v[232:235], v[124:131], v10\n\t" \
    "ds_read_b128 v[248:251], v9 offset:3072\n\t" \
    "s_waitcnt lgkmcnt(7)\n\t" \
    "v_smfmac_f32_16x16x64_bf16 v[0:3], v[212:215], v[196:203], v10\n\t" \
    "v_smfmac_f32_16x16x64_bf16 v[4:7], v[212:215], v[204:211], v10\n\t" \
    "s_waitcnt lgkmcnt(6)\n\t" \
    "v_smfmac_f32_16x16x64_bf16 v[0:3], v[216:219], v[68:75], v10\n\t" \
    "v_smfmac_f32_16x16x64_bf16 v[4:7], v[216:219], v[76:83], v10\n\t" \
    "s_waitcnt lgkmcnt(5)\n\t" \
    "v_smfmac_f32_16x16x64_bf16 v[0:3], v[220:223], v[132:139], v10\n\t" \
    "v_smfmac_f32_16x16x64_bf16 v[4:7], v[220:223], v[140:147], v10\n\t" \
    "s_waitcnt lgkmcnt(0)\n\t" \
    "v_smfmac_f32_16x16x64_bf16 v[0:3], v[224:227], v[236:243], v10\n\t" \
    "v_xor_b32 v9, 49152, v9\n\t" \
    "v_smfmac_f32_16x16x64_bf16 v[4:7], v[224:227], v[244:251], v10\n\t" \
    "s_setprio 3\n\t" \
    "v_mov_b32 v253, %[stag]\n\t" \
    "s_nop 3\n\t" \
    "v_add_f32 v0, v0, v1\n\t" \
    "v_add_f32 v2, v2, v3\n\t" \
    "v_add_f32 v4, v4, v5\n\t" \
    "v_add_f32 v6, v6, v7\n\t" \
    "v_cvt_pk_bf16_f32 v0, v0, v4\n\t" \
    "v_cvt_pk_bf16_f32 v2, v2, v6\n\t" \
    "ds_write2_b32 v13, v0, v2 offset0:0 offset1:8\n\t" \
    "s_mov_b64 exec, %[onej]\n\t" \
    "ds_write_b32 v18, v253 offset:16\n\t" \
    "s_mov_b64 exec, -1\n\t" \
    "s_setprio 0\n\t" \
    "ds_write2_b32 v11, v0, v2 offset0:0 offset1:4\n\t" \
    "s_mov_b64 exec, %[onej]\n\t" \
    "ds_write_b32 v18, v253 offset:32\n\t" \
    "s_mov_b64 exec, -1\n\t" \
    GSA(1) \
    "s_setprio 0\n\t" \
    "buffer_load_dwordx4 v[236:239], v12, %[rs], %[qx] offen offset:0 sc1\n\t" \
    "buffer_load_dwordx4 v[240:243], v12, %[rs], %[qx] offen offset:1024 sc1\n\t" \
    "buffer_load_dwordx4 v[244:247], v12, %[rs], %[qx] offen offset:2048 sc1\n\t" \
    "buffer_load_dwordx4 v[248:251], v12, %[rs], %[qx] offen offset:3072 sc1\n\t" \
    "v_cmp_le_i32 s[72:73], 0, v16\n\t" \
    "v_cmp_le_i32 s[74:75], 1, v16\n\t" \
    "v_cmp_le_i32 s[76:77], 2, v16\n\t" \
    "v_cmp_le_i32 s[78:79], 3, v16\n\t" \
    "v_cmp_le_i32 s[80:81], 16, v16\n\t" \
    "v_cmp_le_i32 s[82:83], 17, v16\n\t" \
    "v_cmp_le_i32 s[84:85], 18, v16\n\t" \
    "v_cmp_le_i32 s[86:87], 19, v16\n\t" \
    "s_cmp_eq_u32 %[st], 63\n\t" \
    "s_cbranch_scc1 LW_%=\n\t" \
    "v_mov_b32 v254, v254\n\t" \
    "s_nop 2\n\t" \
    "v_mov_b32 v254, v254\n\t" \
    "s_nop 2\n\t" \
    "v_mov_b32 v254, v254\n\t" \
    "s_nop 2\n\t" \
    "v_mov_b32 v254, v254\n\t" \
    "s_nop 2\n\t" \
    "v_mov_b32 v254, v254\n\t" \
    "s_nop 2\n\t" \
    "v_mov_b32 v254, v254\n\t" \
    "s_nop 2\n\t" \
    "v_mov_b32 v254, v254\n\t" \
    "s_nop 2\n\t" \
    "v_mov_b32 v254, v254\n\t" \
    "s_nop 2\n\t" \
    "v_mov_b32 v254, v254\n\t" \
    "s_nop 2\n\t" \
    "v_mov_b32 v254, v254\n\t" \
    "s_nop 2\n\t" \
    "v_mov_b32 v254, v254\n\t" \
    "s_nop 2\n\t" \
    "v_mov_b32 v254, v254\n\t" \
    "s_nop 2\n\t" \
    "v_mov_b32 v254, v254\n\t" \
    "s_nop 2\n\t" \
    "v_mov_b32 v254, v254\n\t" \
    "s_nop 2\n\t" \
    "v_mov_b32 v254, v254\n\t" \
    "s_nop 2\n\t" \
    "v_mov_b32 v254, v254\n\t" \
    "s_nop 2\n\t" \
    "v_mov_b32 v254, v254\n\t" \
    "s_nop 2\n\t" \
    "v_mov_b32 v254, v254\n\t" \
    "s_nop 2\n\t" \
    "v_mov_b32 v254, v254\n\t" \
    "s_nop 2\n\t" \
    "LW_%=:\n\t" \
    "ds_read_b32 v0, v18 offset:32\n\t" \
    "s_sub_u32 %[lim], %[lim], " STR(DEC1) "\n\t" \
    "s_cmp_gt_i32 %[lim], 0\n\t" \
    "v_mov_b32 v254, v254\n\t" \
    "s_nop 2\n\t" \
    "v_mov_b32 v254, v254\n\t" \
    "s_nop 2\n\t" \
    "v_mov_b32 v254, v254\n\t" \
    "s_nop 2\n\t" \
    "v_mov_b32 v254, v254\n\t" \
    "s_nop 2\n\t" \
    "v_mov_b32 v254, v254\n\t" \
    "s_nop 2\n\t" \
    "s_waitcnt lgkmcnt(0)\n\t" \
    "s_cbranch_scc0 CW_%=\n\t" \
    "v_cmp_ne_u32 vcc, v0, v253\n\t" \
    "s_cbranch_vccnz LW_%=\n\t" \
    "CW_%=:\n\t" \
    "ds_read_b128 v[212:215], v12 offset:24576\n\t" \
    "ds_read_b128 v[216:219], v12 offset:28672\n\t" \
    "ds_read_b128 v[220:223], v15 offset:0\n\t" \
    "ds_read_b128 v[224:227], v12 offset:25600\n\t" \
    "ds_read_b128 v[228:231], v12 offset:29696\n\t" \
    "v_add_u32 v253, %[se], v12\n\t" \
    "v_mov_b32 v254, v254\n\t" \
    "s_nop 2\n\t" \
    "v_mov_b32 v254, v254\n\t" \
    "s_nop 2\n\t" \
    "v_mov_b32 v254, v254\n\t" \
    "s_nop 2\n\t" \
    "v_mov_b32 v254, v254\n\t" \
    "s_nop 2\n\t" \
    "s_waitcnt vmcnt(0)\n\t" \
    "s_waitcnt lgkmcnt(4)\n\t" \
    "v_mfma_f32_16x16x32_bf16 v[0:3], v[212:215], v[236:239], 0\n\t" \
    "ds_read_b128 v[212:215], v15 offset:512\n\t" \
    "s_waitcnt lgkmcnt(4)\n\t" \
    "v_mfma_f32_16x16x32_bf16 v[4:7], v[216:219], v[236:239], 0\n\t" \
    "ds_read_b128 v[216:219], v12 offset:26624\n\t" \
    "s_waitcnt lgkmcnt(4)\n\t" \
    "v_mfma_f32_16x16x32_bf16 v[232:235], v[220:223], v[236:239], 0\n\t" \
    "ds_read_b128 v[220:223], v12 offset:30720\n\t" \
    "s_waitcnt lgkmcnt(4)\n\t" \
    "v_mfma_f32_16x16x32_bf16 v[0:3], v[224:227], v[240:243], v[0:3]\n\t" \
    "ds_read_b128 v[224:227], v15 offset:1024\n\t" \
    "s_waitcnt lgkmcnt(4)\n\t" \
    "v_mfma_f32_16x16x32_bf16 v[4:7], v[228:231], v[240:243], v[4:7]\n\t" \
    "ds_read_b128 v[228:231], v12 offset:27648\n\t" \
    "s_waitcnt lgkmcnt(4)\n\t" \
    "v_mfma_f32_16x16x32_bf16 v[232:235], v[212:215], v[240:243], v[232:235]\n\t" \
    "ds_read_b128 v[212:215], v12 offset:31744\n\t" \
    "s_waitcnt lgkmcnt(4)\n\t" \
    "v_mfma_f32_16x16x32_bf16 v[0:3], v[216:219], v[244:247], v[0:3]\n\t" \
    "ds_read_b128 v[216:219], v15 offset:1536\n\t" \
    "s_waitcnt lgkmcnt(4)\n\t" \
    "v_mfma_f32_16x16x32_bf16 v[4:7], v[220:223], v[244:247], v[4:7]\n\t" \
    "s_waitcnt lgkmcnt(3)\n\t" \
    "v_mfma_f32_16x16x32_bf16 v[232:235], v[224:227], v[244:247], v[232:235]\n\t" \
    "s_waitcnt lgkmcnt(2)\n\t" \
    "v_mfma_f32_16x16x32_bf16 v[0:3], v[228:231], v[248:251], v[0:3]\n\t" \
    "s_waitcnt lgkmcnt(1)\n\t" \
    "v_mfma_f32_16x16x32_bf16 v[4:7], v[212:215], v[248:251], v[4:7]\n\t" \
    "s_waitcnt lgkmcnt(0)\n\t" \
    "v_mfma_f32_16x16x32_bf16 v[232:235], v[216:219], v[248:251], v[232:235]\n\t" \
    "s_barrier\n\t" \
    GSA(4) \
    "ds_read_b128 v[236:239], v253 offset:8192\n\t" \
    "ds_read_b128 v[240:243], v253 offset:9216\n\t" \
    "ds_read_b128 v[244:247], v253 offset:10240\n\t" \
    "ds_read_b128 v[248:251], v253 offset:11264\n\t" \
    "ds_read2st64_b32 v[228:229], v17 offset0:0 offset1:1\n\t" \
    "ds_read2st64_b32 v[230:231], v17 offset0:2 offset1:3\n\t" \
    "s_mov_b64 exec, %[fm]\n\t" \
    "v_mov_b32 v220, %[st]\n\t" \
    "v_mov_b32 v221, %[tag]\n\t" \
    "buffer_store_dwordx2 v[220:221], off, %[rs], %[po] sc1\n\t" \
    "s_mov_b64 exec, -1\n\t" \
    "v_mov_b32 v254, 0xff800000\n\t" \
    "v_cvt_pk_bf16_f32 v0, 0, v0\n\t" \
    "v_cvt_pk_bf16_f32 v1, 0, v1\n\t" \
    "v_cvt_pk_bf16_f32 v2, 0, v2\n\t" \
    "v_cvt_pk_bf16_f32 v3, 0, v3\n\t" \
    "v_cvt_pk_bf16_f32 v4, 0, v4\n\t" \
    "v_cvt_pk_bf16_f32 v5, 0, v5\n\t" \
    "v_cvt_pk_bf16_f32 v6, 0, v6\n\t" \
    "v_cvt_pk_bf16_f32 v7, 0, v7\n\t" \
    "v_cvt_pk_bf16_f32 v232, 0, v232\n\t" \
    "v_cvt_pk_bf16_f32 v233, 0, v233\n\t" \
    "v_mul_f32 v0, 0x3db504f3, v0\n\t" \
    "v_mul_f32 v1, 0x3db504f3, v1\n\t" \
    "v_mul_f32 v2, 0x3db504f3, v2\n\t" \
    "v_mul_f32 v3, 0x3db504f3, v3\n\t" \
    "v_mul_f32 v4, 0x3db504f3, v4\n\t" \
    "v_mul_f32 v5, 0x3db504f3, v5\n\t" \
    "v_mul_f32 v6, 0x3db504f3, v6\n\t" \
    "v_mul_f32 v7, 0x3db504f3, v7\n\t" \
    "v_mul_f32 v232, 0x3db504f3, v232\n\t" \
    "v_mul_f32 v233, 0x3db504f3, v233\n\t" \
    "v_cvt_pk_bf16_f32 v0, 0, v0\n\t" \
    "v_cvt_pk_bf16_f32 v1, 0, v1\n\t" \
    "v_cvt_pk_bf16_f32 v2, 0, v2\n\t" \
    "v_cvt_pk_bf16_f32 v3, 0, v3\n\t" \
    "v_cvt_pk_bf16_f32 v4, 0, v4\n\t" \
    "v_cvt_pk_bf16_f32 v5, 0, v5\n\t" \
    "v_cvt_pk_bf16_f32 v6, 0, v6\n\t" \
    "v_cvt_pk_bf16_f32 v7, 0, v7\n\t" \
    "v_cvt_pk_bf16_f32 v232, 0, v232\n\t" \
    "v_cvt_pk_bf16_f32 v233, 0, v233\n\t" \
    "v_cndmask_b32 v0, v254, v0, s[72:73]\n\t" \
    "v_cndmask_b32 v1, v254, v1, s[74:75]\n\t" \
    "v_cndmask_b32 v2, v254, v2, s[76:77]\n\t" \
    "v_cndmask_b32 v3, v254, v3, s[78:79]\n\t" \
    "v_cndmask_b32 v4, v254, v4, s[80:81]\n\t" \
    "v_cndmask_b32 v5, v254, v5, s[82:83]\n\t" \
    "v_cndmask_b32 v6, v254, v6, s[84:85]\n\t" \
    "v_cndmask_b32 v7, v254, v7, s[86:87]\n\t" \
    "v_max3_f32 v252, v0, v1, v2\n\t" \
    "v_max3_f32 v255, v3, v4, v5\n\t" \
    "v_max3_f32 v254, v6, v7, v232\n\t" \
    "v_max3_f32 v252, v252, v255, v254\n\t" \
    "v_max_f32 v252, v252, v233\n\t" \
    "v_mov_b32 v255, v252\n\t" \
    "v_mov_b32 v254, v252\n\t" \
    "s_nop 1\n\t" \
    "v_permlane16_swap_b32 v255, v254\n\t" \
    "v_max_f32 v252, v255, v254\n\t" \
    "v_max_f32 v255, v255, v254\n\t" \
    "s_nop 1\n\t" \
    "v_permlane32_swap_b32 v252, v255\n\t" \
    "v_max_f32 v252, v252, v255\n\t" \
    "v_mul_f32 v252, 0xbfb8aa3b, v252\n\t" \
    "v_fma_f32 v0, v0, %[l2e], v252\n\t" \
    "v_fma_f32 v1, v1, %[l2e], v252\n\t" \
    "v_fma_f32 v2, v2, %[l2e], v252\n\t" \
    "v_fma_f32 v3, v3, %[l2e], v252\n\t" \
    "v_fma_f32 v4, v4, %[l2e], v252\n\t" \
    "v_fma_f32 v5, v5, %[l2e], v252\n\t" \
    "v_fma_f32 v6, v6, %[l2e], v252\n\t" \
    "v_fma_f32 v7, v7, %[l2e], v252\n\t" \
    "v_fma_f32 v232, v232, %[l2e], v252\n\t" \
    "v_fma_f32 v233, v233, %[l2e], v252\n\t" \
    "v_exp_f32 v0, v0\n\t" \
    "v_exp_f32 v1, v1\n\t" \
    "v_exp_f32 v2, v2\n\t" \
    "v_exp_f32 v3, v3\n\t" \
    "v_exp_f32 v4, v4\n\t" \
    "v_exp_f32 v5, v5\n\t" \
    "v_exp_f32 v6, v6\n\t" \
    "v_exp_f32 v7, v7\n\t" \
    "v_exp_f32 v232, v232\n\t" \
    "v_exp_f32 v233, v233\n\t" \
    "v_add_f32 v252, v0, v1\n\t" \
    "v_add_f32 v235, v2, v3\n\t" \
    "v_add_f32 v254, v4, v5\n\t" \
    "v_add_f32 v255, v6, v7\n\t" \
    "v_add_f32 v234, v232, v233\n\t" \
    "v_add_f32 v252, v252, v235\n\t" \
    "v_add_f32 v254, v254, v255\n\t" \
    "v_add_f32 v252, v252, v254\n\t" \
    "v_add_f32 v252, v252, v234\n\t" \
    "v_mov_b32 v255, v252\n\t" \
    "v_mov_b32 v254, v252\n\t" \
    "s_nop 1\n\t" \
    "v_permlane16_swap_b32 v255, v254\n\t" \
    "v_add_f32 v252, v255, v254\n\t" \
    "v_add_f32 v255, v255, v254\n\t" \
    "s_nop 1\n\t" \
    "v_permlane32_swap_b32 v252, v255\n\t" \
    "v_add_f32 v252, v252, v255\n\t" \
    "v_rcp_f32 v252, v252\n\t" \
    "s_nop 0\n\t" \
    "v_mul_f32 v0, v0, v252\n\t" \
    "v_mul_f32 v1, v1, v252\n\t" \
    "v_mul_f32 v2, v2, v252\n\t" \
    "v_mul_f32 v3, v3, v252\n\t" \
    "v_mul_f32 v4, v4, v252\n\t" \
    "v_mul_f32 v5, v5, v252\n\t" \
    "v_mul_f32 v6, v6, v252\n\t" \
    "v_mul_f32 v7, v7, v252\n\t" \
    "v_mul_f32 v232, v232, v252\n\t" \
    "v_mul_f32 v233, v233, v252\n\t" \
    "v_cvt_pk_bf16_f32 v0, v0, v1\n\t" \
    "v_cvt_pk_bf16_f32 v1, v2, v3\n\t" \
    "v_cvt_pk_bf16_f32 v2, v4, v5\n\t" \
    "v_cvt_pk_bf16_f32 v3, v6, v7\n\t" \
    "v_cvt_pk_bf16_f32 v4, v232, v233\n\t" \
    "v_mov_b32 v5, 0\n\t" \
    "v_mov_b32 v6, 0\n\t" \
    "v_cvt_pk_bf16_f32 v7, v232, v233\n\t" \
    "s_waitcnt lgkmcnt(0)\n\t" \
    "v_mfma_f32_16x16x32_bf16 v[236:239], v[236:239], v[0:3], 0\n\t" \
    "v_mfma_f32_16x16x32_bf16 v[240:243], v[240:243], v[0:3], 0\n\t" \
    "v_mfma_f32_16x16x32_bf16 v[244:247], v[244:247], v[0:3], 0\n\t" \
    "v_mfma_f32_16x16x32_bf16 v[248:251], v[248:251], v[0:3], 0\n\t" \
    "v_mfma_f32_16x16x16_bf16 v[236:239], v[228:229], v[4:5], v[236:239]\n\t" \
    "v_mfma_f32_16x16x16_bf16 v[240:243], v[228:229], v[6:7], v[240:243]\n\t" \
    "v_mfma_f32_16x16x16_bf16 v[244:247], v[230:231], v[4:5], v[244:247]\n\t" \
    "v_mfma_f32_16x16x16_bf16 v[248:251], v[230:231], v[6:7], v[248:251]\n\t" \
    "s_nop 4\n\t" \
    "v_cvt_pk_bf16_f32 v220, v236, v237\n\t" \
    "v_cvt_pk_bf16_f32 v221, v240, v241\n\t" \
    "v_cvt_pk_bf16_f32 v222, v238, v239\n\t" \
    "v_cvt_pk_bf16_f32 v223, v242, v243\n\t" \
    "v_permlane16_swap_b32 v220, v221\n\t" \
    "s_nop 0\n\t" \
    "v_permlane16_swap_b32 v222, v223\n\t" \
    "v_cvt_pk_bf16_f32 v224, v244, v245\n\t" \
    "v_cvt_pk_bf16_f32 v225, v248, v249\n\t" \
    "v_cvt_pk_bf16_f32 v226, v246, v247\n\t" \
    "v_cvt_pk_bf16_f32 v227, v250, v251\n\t" \
    "v_permlane16_swap_b32 v224, v225\n\t" \
    "s_nop 0\n\t" \
    "v_permlane16_swap_b32 v226, v227\n\t" \
    "s_setprio 1\n\t" \
    GSA(2) \
    GSW \
    "s_nop 1\n\t" \
    : [lim] "+s"(lim), "+{v[220:227]}"(oxr) DGO GSO(0) GSO(1) GSO(2) GSO(3) GSO(4) \
    : "{v8}"(am_a), "{v9}"(t0_a), "{v10}"(idx), "{v11}"(iw_a), "{v12}"(ab_a), "{v13}"(sw_a), "{v14}"(xo_v), "{v15}"(kr_a), "{v16}"(dq), "{v17}"(vl_a), "{v18}"(fr_a), [stag] "s"(0xffffff81u + s), [onej] "s"(onej), [rs] "s"(rs), [m0] "s"(sbm + 16384u), [m1] "s"(sbm + 20480u), [st] "s"(s), [tag] "s"(P.tag), [po] "s"(FLG_OFF + F_PRG(4, h) * 128), [fm] "s"(fmask), [qx] "s"(qxo), [ox] "s"(oxo), [se] "s"((j & 1) * 4096), [l2e] "s"(0x3fb8aa3b), "{v[20:27]}"(wr[0][0]), "{v[28:35]}"(wr[0][1]), "{v[36:43]}"(wr[1][0]), "{v[44:51]}"(wr[1][1]), "{v[52:59]}"(wr[2][0]), "{v[60:67]}"(wr[2][1]), "{v[68:75]}"(wr[3][0]), "{v[76:83]}"(wr[3][1]), "{v[84:91]}"(wr[4][0]), "{v[92:99]}"(wr[4][1]), "{v[100:107]}"(wr[5][0]), "{v[108:115]}"(wr[5][1]), "{v[116:123]}"(wr[6][0]), "{v[124:131]}"(wr[6][1]), "{v[132:139]}"(wr[7][0]), "{v[140:147]}"(wr[7][1]), "{v[148:155]}"(wr[8][0]), "{v[156:163]}"(wr[8][1]), "{v[164:171]}"(wr[9][0]), "{v[172:179]}"(wr[9][1]), "{v[180:187]}"(wr[10][0]), "{v[188:195]}"(wr[10][1]), "{v[196:203]}"(wr[11][0]), "{v[204:211]}"(wr[11][1]) \
    : "memory", "scc", "vcc", "s90", "s91", "s72", "s73", "s74", "s75", "s76", "s77", "s78", "s79", "s80", "s81", "s82", "s83", "s84", "s85", "s86", "s87", "v0", "v1", "v2", "v3", "v4", "v5", "v6", "v7", "v212", "v213", "v214", "v215", "v216", "v217", "v218", "v219", "v228", "v229", "v230", "v231", "v232", "v233", "v234", "v235", "v236", "v237", "v238", "v239", "v240", "v241", "v242", "v243", "v244", "v245", "v246", "v247", "v248", "v249", "v250", "v251", "v252", "v253", "v254", "v255")

#define T_WAIT() \
  asm volatile( \
    "s_getpc_b64 s[92:93]\n\t" \
    "TPC_%=:\n\t" \
    "s_add_u32 s92, s92, TEC_%=-TPC_%=\n\t" \
    "s_addc_u32 s93, s93, 0\n\t" \
    "s_lshr_b32 s91, %[tgt], 2\n\t" \
    "s_sub_u32 s91, s91, 3\n\t" \
    "TWC_%=:\n\t" \
    "s_memrealtime s[88:89]\n\t" \
    DGE \
    "s_waitcnt lgkmcnt(0)\n\t" \
    "s_sub_u32 s89, s91, s88\n\t" \
    "s_lshl_b32 s89, s89, 2\n\t" \
    "s_cmp_gt_i32 s89, 0\n\t" \
    "s_cbranch_scc1 TWC_%=\n\t" \
    "s_lshl_b32 s88, s88, 2\n\t" \
    "s_sub_u32 s89, %[tgt], s88\n\t" \
    "s_max_i32 s89, s89, 0\n\t" \
    "s_mul_i32 s89, s89, 11\n\t" \
    "s_lshr_b32 s89, s89, 1\n\t" \
    "s_and_b32 s89, s89, 0x7c\n\t" \
    "s_sub_u32 s92, s92, s89\n\t" \
    "s_subb_u32 s93, s93, 0\n\t" \
    "s_setpc_b64 s[92:93]\n\t" \
    "s_nop 0\n\t" \
    "s_nop 0\n\t" \
    "s_nop 0\n\t" \
    "s_nop 0\n\t" \
    "s_nop 0\n\t" \
    "s_nop 0\n\t" \
    "s_nop 0\n\t" \
    "s_nop 0\n\t" \
    "s_nop 0\n\t" \
    "s_nop 0\n\t" \
    "s_nop 0\n\t" \
    "s_nop 0\n\t" \
    "s_nop 0\n\t" \
    "s_nop 0\n\t" \
    "s_nop 0\n\t" \
    "s_nop 0\n\t" \
    "s_nop 0\n\t" \
    "s_nop 0\n\t" \
    "s_nop 0\n\t" \
    "s_nop 0\n\t" \
    "s_nop 0\n\t" \
    "s_nop 0\n\t" \
    "TEC_%=:\n\t" \
    :  \
    : [tgt] "s"(tgt) \
    : "s88", "s89", "s90", "s91", "s92", "s93", "s94", "s95", "memory", "scc")

#define V_PROJ() \
  asm volatile( \
    "v_mov_b32 v0, 0\n\t" \
    "v_mov_b32 v1, 0\n\t" \
    "v_mov_b32 v2, 0\n\t" \
    "v_mov_b32 v3, 0\n\t" \
    "v_mov_b32 v4, 0\n\t" \
    "v_mov_b32 v5, 0\n\t" \
    "v_mov_b32 v6, 0\n\t" \
    "v_mov_b32 v7, 0\n\t" \
    "v_mov_b32 v8, 0\n\t" \
    "v_mov_b32 v9, 0\n\t" \
    "v_mov_b32 v10, 0\n\t" \
    "v_mov_b32 v11, 0\n\t" \
    "v_mov_b32 v12, 0\n\t" \
    "v_mov_b32 v13, 0\n\t" \
    "v_mov_b32 v14, 0\n\t" \
    "v_mov_b32 v15, 0\n\t" \
    "v_mov_b32 v252, 0\n\t" \
    "s_getpc_b64 s[92:93]\n\t" \
    "TPB_%=:\n\t" \
    "s_add_u32 s92, s92, TEB_%=-TPB_%=\n\t" \
    "s_addc_u32 s93, s93, 0\n\t" \
    "s_lshr_b32 s91, %[tgt], 2\n\t" \
    "s_sub_u32 s91, s91, 3\n\t" \
    "TWB_%=:\n\t" \
    "s_memrealtime s[88:89]\n\t" \
    DGE \
    "s_waitcnt lgkmcnt(0)\n\t" \
    "s_sub_u32 s89, s91, s88\n\t" \
    "s_lshl_b32 s89, s89, 2\n\t" \
    "s_cmp_gt_i32 s89, 0\n\t" \
    "s_cbranch_scc1 TWB_%=\n\t" \
    "s_lshl_b32 s88, s88, 2\n\t" \
    "s_sub_u32 s89, %[tgt], s88\n\t" \
    "s_max_i32 s89, s89, 0\n\t" \
    "s_mul_i32 s89, s89, 11\n\t" \
    "s_lshr_b32 s89, s89, 1\n\t" \
    "s_and_b32 s89, s89, 0x7c\n\t" \
    "s_sub_u32 s92, s92, s89\n\t" \
    "s_subb_u32 s93, s93, 0\n\t" \
    "s_setpc_b64 s[92:93]\n\t" \
    "s_nop 0\n\t" \
    "s_nop 0\n\t" \
    "s_nop 0\n\t" \
    "s_nop 0\n\t" \
    "s_nop 0\n\t" \
    "s_nop 0\n\t" \
    "s_nop 0\n\t" \
    "s_nop 0\n\t" \
    "s_nop 0\n\t" \
    "s_nop 0\n\t" \
    "s_nop 0\n\t" \
    "s_nop 0\n\t" \
    "s_nop 0\n\t" \
    "s_nop 0\n\t" \
    "s_nop 0\n\t" \
    "s_nop 0\n\t" \
    "s_nop 0\n\t" \
    "s_nop 0\n\t" \
    "s_nop 0\n\t" \
    "s_nop 0\n\t" \
    "s_nop 0\n\t" \
    "s_nop 0\n\t" \
    "TEB_%=:\n\t" \
    "buffer_load_dwordx4 v[220:223], %[l16], %[rs], %[mo] offen offset:0 sc1\n\t" \
    "s_nop 11\n\t" \
    "buffer_load_dwordx4 v[224:227], %[l16], %[rs], %[mo] offen offset:1024 sc1\n\t" \
    "s_nop 11\n\t" \
    "buffer_load_dwordx4 v[228:231], %[l16], %[rs], %[mo] offen offset:2048 sc1\n\t" \
    "s_nop 11\n\t" \
    "buffer_load_dwordx4 v[232:235], %[l16], %[rs], %[mo] offen offset:3072 sc1\n\t" \
    "A_%=:\n\t" \
    "s_waitcnt vmcnt(3)\n\t" \
    "v_smfmac_f32_16x16x64_bf16 v[0:3], v[220:223], v[28:35], %[idx]\n\t" \
    "v_max3_u32 v252, v252, v220, v221\n\t" \
    "v_smfmac_f32_16x16x64_bf16 v[4:7], v[220:223], v[36:43], %[idx]\n\t" \
    "v_max3_u32 v252, v252, v222, v223\n\t" \
    "v_smfmac_f32_16x16x64_bf16 v[8:11], v[220:223], v[44:51], %[idx]\n\t" \
    "v_smfmac_f32_16x16x64_bf16 v[12:15], v[220:223], v[52:59], %[idx]\n\t" \
    "s_waitcnt vmcnt(2)\n\t" \
    "v_smfmac_f32_16x16x64_bf16 v[0:3], v[224:227], v[60:67], %[idx]\n\t" \
    "v_max3_u32 v252, v252, v224, v225\n\t" \
    "v_smfmac_f32_16x16x64_bf16 v[4:7], v[224:227], v[68:75], %[idx]\n\t" \
    "v_max3_u32 v252, v252, v226, v227\n\t" \
    "v_smfmac_f32_16x16x64_bf16 v[8:11], v[224:227], v[76:83], %[idx]\n\t" \
    "v_smfmac_f32_16x16x64_bf16 v[12:15], v[224:227], v[84:91], %[idx]\n\t" \
    "s_waitcnt vmcnt(1)\n\t" \
    "v_smfmac_f32_16x16x64_bf16 v[0:3], v[228:231], v[92:99], %[idx]\n\t" \
    "v_max3_u32 v252, v252, v228, v229\n\t" \
    "v_smfmac_f32_16x16x64_bf16 v[4:7], v[228:231], v[100:107], %[idx]\n\t" \
    "v_max3_u32 v252, v252, v230, v231\n\t" \
    "v_smfmac_f32_16x16x64_bf16 v[8:11], v[228:231], v[108:115], %[idx]\n\t" \
    "v_smfmac_f32_16x16x64_bf16 v[12:15], v[228:231], v[116:123], %[idx]\n\t" \
    "s_waitcnt vmcnt(0)\n\t" \
    "v_smfmac_f32_16x16x64_bf16 v[0:3], v[232:235], v[124:131], %[idx]\n\t" \
    "v_max3_u32 v252, v252, v232, v233\n\t" \
    "v_smfmac_f32_16x16x64_bf16 v[4:7], v[232:235], v[132:139], %[idx]\n\t" \
    "v_max3_u32 v252, v252, v234, v235\n\t" \
    "v_smfmac_f32_16x16x64_bf16 v[8:11], v[232:235], v[140:147], %[idx]\n\t" \
    "v_smfmac_f32_16x16x64_bf16 v[12:15], v[232:235], v[148:155], %[idx]\n\t" \
    "v_cmp_eq_u32 vcc, -1, v252\n\t" \
    "s_cbranch_vccnz SL_%=\n\t" \
    "D_%=:\n\t" \
    GSA(3) \
    "s_nop 0\n\t" \
    "v_add_f32 v0, v0, v1\n\t" \
    "v_add_f32 v1, v2, v3\n\t" \
    "v_add_f32 v4, v4, v5\n\t" \
    "v_add_f32 v5, v6, v7\n\t" \
    "ds_write2st64_b64 %[pw], v[0:1], v[4:5] offset0:0 offset1:4\n\t" \
    "v_add_f32 v8, v8, v9\n\t" \
    "v_add_f32 v9, v10, v11\n\t" \
    "v_add_f32 v12, v12, v13\n\t" \
    "v_add_f32 v13, v14, v15\n\t" \
    "ds_write2st64_b64 %[pw], v[8:9], v[12:13] offset0:8 offset1:12\n\t" \
    "s_waitcnt lgkmcnt(0)\n\t" \
    "s_barrier\n\t" \
    "ds_read2st64_b64 v[0:3], %[pr] offset0:0 offset1:1\n\t" \
    "ds_read2st64_b64 v[4:7], %[pr] offset0:2 offset1:3\n\t" \
    "s_waitcnt lgkmcnt(1)\n\t" \
    "v_pk_add_f32 v[0:1], v[0:1], v[2:3]\n\t" \
    "s_waitcnt lgkmcnt(0)\n\t" \
    "v_pk_add_f32 v[4:5], v[4:5], v[6:7]\n\t" \
    "s_nop 0\n\t" \
    "v_pk_add_f32 v[0:1], v[0:1], v[4:5]\n\t" \
    "s_nop 0\n\t" \
    "v_cvt_pk_bf16_f32 v0, v0, v1\n\t" \
    "v_min_u32 v0, -2, v0\n\t" \
    "s_nop 1\n\t" \
    "v_mov_b32_dpp v1, v0 quad_perm:[1,1,1,1] row_mask:0xf bank_mask:0xf\n\t" \
    "v_mov_b32_dpp v2, v0 quad_perm:[2,2,2,2] row_mask:0xf bank_mask:0xf\n\t" \
    "v_mov_b32_dpp v3, v0 quad_perm:[3,3,3,3] row_mask:0xf bank_mask:0xf\n\t" \
    "s_mov_b64 exec, %[qm]\n\t" \
    "buffer_store_dwordx4 v[0:3], %[l4], %[rs], %[vo] offen sc1\n\t" \
    "s_mov_b64 exec, -1\n\t" \
    "s_branch E_%=\n\t" \
    "SL_%=:\n\t" \
    "buffer_load_dwordx4 v[220:223], %[l16], %[rs], %[mo] offen offset:0 sc1\n\t" \
    "buffer_load_dwordx4 v[224:227], %[l16], %[rs], %[mo] offen offset:1024 sc1\n\t" \
    "buffer_load_dwordx4 v[228:231], %[l16], %[rs], %[mo] offen offset:2048 sc1\n\t" \
    "buffer_load_dwordx4 v[232:235], %[l16], %[rs], %[mo] offen offset:3072 sc1\n\t" \
    "v_mov_b32 v252, %[rt]\n\t" \
    "v_mov_b32 v253, %[tgt]\n\t" \
    "ds_write_b32 v252, v253\n\t" \
    "s_sub_u32 %[lim], %[lim], 1\n\t" \
    "s_cmp_gt_i32 %[lim], 0\n\t" \
    "s_cbranch_scc0 G_%=\n\t" \
    "s_nop 3\n\t" \
    "v_mov_b32 v0, 0\n\t" \
    "v_mov_b32 v1, 0\n\t" \
    "v_mov_b32 v2, 0\n\t" \
    "v_mov_b32 v3, 0\n\t" \
    "v_mov_b32 v4, 0\n\t" \
    "v_mov_b32 v5, 0\n\t" \
    "v_mov_b32 v6, 0\n\t" \
    "v_mov_b32 v7, 0\n\t" \
    "v_mov_b32 v8, 0\n\t" \
    "v_mov_b32 v9, 0\n\t" \
    "v_mov_b32 v10, 0\n\t" \
    "v_mov_b32 v11, 0\n\t" \
    "v_mov_b32 v12, 0\n\t" \
    "v_mov_b32 v13, 0\n\t" \
    "v_mov_b32 v14, 0\n\t" \
    "v_mov_b32 v15, 0\n\t" \
    "v_mov_b32 v252, 0\n\t" \
    "s_branch A_%=\n\t" \
    "G_%=:\n\t" \
    "s_waitcnt vmcnt(0)\n\t" \
    "s_branch D_%=\n\t" \
    "E_%=:\n\t" \
    GSW \
    "s_nop 1\n\t" \
    : [lim] "+s"(lim) GSO(3) \
    : [l16] "v"(l16), [pw] "v"(pw_a), [pr] "v"(pr_a), [idx] "v"(idx), [l4] "v"(ln * 4), [rs] "s"(rs), [mo] "s"(mo), [tgt] "s"(tgt), [rt] "s"(lbase + LB_RED + 4096 + 12), [vo] "s"(sb + vo), [qm] "s"(0x1111111111111111ull), "{v[28:35]}"(wv[0]), "{v[36:43]}"(wv[1]), "{v[44:51]}"(wv[2]), "{v[52:59]}"(wv[3]), "{v[60:67]}"(wv[4]), "{v[68:75]}"(wv[5]), "{v[76:83]}"(wv[6]), "{v[84:91]}"(wv[7]), "{v[92:99]}"(wv[8]), "{v[100:107]}"(wv[9]), "{v[108:115]}"(wv[10]), "{v[116:123]}"(wv[11]), "{v[124:131]}"(wv[12]), "{v[132:139]}"(wv[13]), "{v[140:147]}"(wv[14]), "{v[148:155]}"(wv[15]) \
    : "memory", "vcc", "scc", "v252", "v253", "s88", "s89", "s90", "s91", "s92", "s93", "s94", "s95", "v0", "v1", "v2", "v3", "v4", "v5", "v6", "v7", "v8", "v9", "v10", "v11", "v12", "v13", "v14", "v15", "v220", "v221", "v222", "v223", "v224", "v225", "v226", "v227", "v228", "v229", "v230", "v231", "v232", "v233", "v234", "v235")

#define WO_V() \
  asm volatile( \
    "v_mov_b32 v0, 0\n\t" \
    "v_mov_b32 v1, 0\n\t" \
    "v_mov_b32 v2, 0\n\t" \
    "v_mov_b32 v3, 0\n\t" \
    "v_mov_b32 v4, 0\n\t" \
    "v_mov_b32 v5, 0\n\t" \
    "v_mov_b32 v6, 0\n\t" \
    "v_mov_b32 v7, 0\n\t" \
    "v_mov_b32 v8, 0\n\t" \
    "v_mov_b32 v9, 0\n\t" \
    "v_mov_b32 v10, 0\n\t" \
    "v_mov_b32 v11, 0\n\t" \
    "v_mov_b32 v12, 0\n\t" \
    "v_mov_b32 v13, 0\n\t" \
    "v_mov_b32 v14, 0\n\t" \
    "v_mov_b32 v15, 0\n\t" \
    "v_mov_b32 v252, 0\n\t" \
    "s_barrier\n\t" \
    GSA(4) \
    "buffer_load_dwordx4 v[220:223], %[l16], %[rs], %[s0] offen sc1\n\t" \
    "s_nop 7\n\t" \
    "buffer_load_dwordx4 v[224:227], %[l16], %[rs], %[s1] offen sc1\n\t" \
    "A_%=:\n\t" \
    "s_waitcnt vmcnt(1)\n\t" \
    "v_smfmac_f32_16x16x64_bf16 v[0:3], v[220:223], v[156:163], %[idx]\n\t" \
    "v_max3_u32 v252, v252, v220, v221\n\t" \
    "v_smfmac_f32_16x16x64_bf16 v[4:7], v[220:223], v[164:171], %[idx]\n\t" \
    "v_max3_u32 v252, v252, v222, v223\n\t" \
    "v_smfmac_f32_16x16x64_bf16 v[8:11], v[220:223], v[172:179], %[idx]\n\t" \
    "v_smfmac_f32_16x16x64_bf16 v[12:15], v[220:223], v[180:187], %[idx]\n\t" \
    "s_waitcnt vmcnt(0)\n\t" \
    "v_smfmac_f32_16x16x64_bf16 v[0:3], v[224:227], v[188:195], %[idx]\n\t" \
    "v_max3_u32 v252, v252, v224, v225\n\t" \
    "v_smfmac_f32_16x16x64_bf16 v[4:7], v[224:227], v[196:203], %[idx]\n\t" \
    "v_max3_u32 v252, v252, v226, v227\n\t" \
    "v_smfmac_f32_16x16x64_bf16 v[8:11], v[224:227], v[204:211], %[idx]\n\t" \
    "v_smfmac_f32_16x16x64_bf16 v[12:15], v[224:227], v[212:219], %[idx]\n\t" \
    "s_setprio 0\n\t" \
    "v_cmp_eq_u32 vcc, -1, v252\n\t" \
    "s_cbranch_vccnz SL_%=\n\t" \
    "D_%=:\n\t" \
    GSA(1) \
    "s_nop 2\n\t" \
    "v_add_f32 v0, v0, v1\n\t" \
    "v_add_f32 v2, v2, v3\n\t" \
    "v_add_f32 v4, v4, v5\n\t" \
    "v_add_f32 v6, v6, v7\n\t" \
    "v_add_f32 v8, v8, v9\n\t" \
    "v_add_f32 v10, v10, v11\n\t" \
    "v_add_f32 v12, v12, v13\n\t" \
    "v_add_f32 v14, v14, v15\n\t" \
    "v_permlane32_swap_b32 v0, v4\n\t" \
    "v_permlane32_swap_b32 v2, v6\n\t" \
    "v_permlane32_swap_b32 v8, v12\n\t" \
    "v_permlane32_swap_b32 v10, v14\n\t" \
    "v_add_f32 v0, v0, v12\n\t" \
    "v_add_f32 v2, v2, v14\n\t" \
    "ds_write2_b32 %[rw], v0, v2 offset0:0 offset1:32\n\t" \
    "s_setprio 2\n\t" \
    "s_waitcnt lgkmcnt(0)\n\t" \
    "s_barrier\n\t" \
    "s_branch E_%=\n\t" \
    "SL_%=:\n\t" \
    "s_sleep 1\n\t" \
    "buffer_load_dwordx4 v[220:223], %[l16], %[rs], %[s0] offen sc1\n\t" \
    "buffer_load_dwordx4 v[224:227], %[l16], %[rs], %[s1] offen sc1\n\t" \
    "s_sub_u32 %[lim], %[lim], " STR(DEC2) "\n\t" \
    "s_cmp_gt_i32 %[lim], 0\n\t" \
    "s_cbranch_scc0 G_%=\n\t" \
    "s_nop 3\n\t" \
    "v_mov_b32 v0, 0\n\t" \
    "v_mov_b32 v1, 0\n\t" \
    "v_mov_b32 v2, 0\n\t" \
    "v_mov_b32 v3, 0\n\t" \
    "v_mov_b32 v4, 0\n\t" \
    "v_mov_b32 v5, 0\n\t" \
    "v_mov_b32 v6, 0\n\t" \
    "v_mov_b32 v7, 0\n\t" \
    "v_mov_b32 v8, 0\n\t" \
    "v_mov_b32 v9, 0\n\t" \
    "v_mov_b32 v10, 0\n\t" \
    "v_mov_b32 v11, 0\n\t" \
    "v_mov_b32 v12, 0\n\t" \
    "v_mov_b32 v13, 0\n\t" \
    "v_mov_b32 v14, 0\n\t" \
    "v_mov_b32 v15, 0\n\t" \
    "v_mov_b32 v252, 0\n\t" \
    "s_branch A_%=\n\t" \
    "G_%=:\n\t" \
    "s_waitcnt vmcnt(0)\n\t" \
    "s_branch D_%=\n\t" \
    "E_%=:\n\t" \
    GSW \
    : [lim] "+s"(lim) GSO(1) GSO(4) \
    : [l16] "v"(l16), [rs] "s"(rs), [idx] "v"(idx), [rw] "v"(rw_a), [s0] "s"(so[0]), [s1] "s"(so[1]), "{v[156:163]}"(wo[0][0][0]), "{v[164:171]}"(wo[0][0][1]), "{v[172:179]}"(wo[0][1][0]), "{v[180:187]}"(wo[0][1][1]), "{v[188:195]}"(wo[1][0][0]), "{v[196:203]}"(wo[1][0][1]), "{v[204:211]}"(wo[1][1][0]), "{v[212:219]}"(wo[1][1][1]) \
    : "memory", "vcc", "scc", "v252", "v0", "v1", "v2", "v3", "v4", "v5", "v6", "v7", "v8", "v9", "v10", "v11", "v12", "v13", "v14", "v15", "v220", "v221", "v222", "v223", "v224", "v225", "v226", "v227")

#define WO_W() \
  asm volatile( \
    "v_mov_b32 v0, 0\n\t" \
    "v_mov_b32 v1, 0\n\t" \
    "v_mov_b32 v2, 0\n\t" \
    "v_mov_b32 v3, 0\n\t" \
    "v_mov_b32 v4, 0\n\t" \
    "v_mov_b32 v5, 0\n\t" \
    "v_mov_b32 v6, 0\n\t" \
    "v_mov_b32 v7, 0\n\t" \
    "v_mov_b32 v8, 0\n\t" \
    "v_mov_b32 v9, 0\n\t" \
    "v_mov_b32 v10, 0\n\t" \
    "v_mov_b32 v11, 0\n\t" \
    "v_mov_b32 v12, 0\n\t" \
    "v_mov_b32 v13, 0\n\t" \
    "v_mov_b32 v14, 0\n\t" \
    "v_mov_b32 v15, 0\n\t" \
    "v_mov_b32 v252, 0\n\t" \
    "s_setprio 1\n\t" \
    "s_barrier\n\t" \
    GSA(4) \
    DGC \
    "buffer_load_dwordx4 v[220:223], %[l16], %[rs], %[s0] offen sc1\n\t" \
    "s_nop 15\n\t" \
    "buffer_load_dwordx4 v[224:227], %[l16], %[rs], %[s1] offen sc1\n\t" \
    "s_nop 15\n\t" \
    "buffer_load_dwordx4 v[228:231], %[l16], %[rs], %[s2] offen sc1\n\t" \
    "s_nop 15\n\t" \
    "buffer_load_dwordx4 v[232:235], %[l16], %[rs], %[s3] offen sc1\n\t" \
    "s_nop 15\n\t" \
    "buffer_load_dwordx4 v[236:239], %[l16], %[rs], %[s4] offen sc1\n\t" \
    "s_nop 15\n\t" \
    "buffer_load_dwordx4 v[240:243], %[l16], %[rs], %[s5] offen sc1\n\t" \
    "A_%=:\n\t" \
    "s_waitcnt vmcnt(5)\n\t" \
    "v_smfmac_f32_16x16x64_bf16 v[0:3], v[220:223], v[28:35], %[idx]\n\t" \
    "v_max3_u32 v252, v252, v220, v221\n\t" \
    "v_smfmac_f32_16x16x64_bf16 v[4:7], v[220:223], v[36:43], %[idx]\n\t" \
    "v_max3_u32 v252, v252, v222, v223\n\t" \
    "v_smfmac_f32_16x16x64_bf16 v[8:11], v[220:223], v[44:51], %[idx]\n\t" \
    "v_smfmac_f32_16x16x64_bf16 v[12:15], v[220:223], v[52:59], %[idx]\n\t" \
    "s_waitcnt vmcnt(4)\n\t" \
    "v_smfmac_f32_16x16x64_bf16 v[0:3], v[224:227], v[60:67], %[idx]\n\t" \
    "v_max3_u32 v252, v252, v224, v225\n\t" \
    "v_smfmac_f32_16x16x64_bf16 v[4:7], v[224:227], v[68:75], %[idx]\n\t" \
    "v_max3_u32 v252, v252, v226, v227\n\t" \
    "v_smfmac_f32_16x16x64_bf16 v[8:11], v[224:227], v[76:83], %[idx]\n\t" \
    "v_smfmac_f32_16x16x64_bf16 v[12:15], v[224:227], v[84:91], %[idx]\n\t" \
    "s_waitcnt vmcnt(3)\n\t" \
    "v_smfmac_f32_16x16x64_bf16 v[0:3], v[228:231], v[92:99], %[idx]\n\t" \
    "v_max3_u32 v252, v252, v228, v229\n\t" \
    "v_smfmac_f32_16x16x64_bf16 v[4:7], v[228:231], v[100:107], %[idx]\n\t" \
    "v_max3_u32 v252, v252, v230, v231\n\t" \
    "v_smfmac_f32_16x16x64_bf16 v[8:11], v[228:231], v[108:115], %[idx]\n\t" \
    "v_smfmac_f32_16x16x64_bf16 v[12:15], v[228:231], v[116:123], %[idx]\n\t" \
    "s_waitcnt vmcnt(2)\n\t" \
    "v_smfmac_f32_16x16x64_bf16 v[0:3], v[232:235], v[124:131], %[idx]\n\t" \
    "v_max3_u32 v252, v252, v232, v233\n\t" \
    "v_smfmac_f32_16x16x64_bf16 v[4:7], v[232:235], v[132:139], %[idx]\n\t" \
    "v_max3_u32 v252, v252, v234, v235\n\t" \
    "v_smfmac_f32_16x16x64_bf16 v[8:11], v[232:235], v[140:147], %[idx]\n\t" \
    "v_smfmac_f32_16x16x64_bf16 v[12:15], v[232:235], v[148:155], %[idx]\n\t" \
    "s_waitcnt vmcnt(1)\n\t" \
    "v_smfmac_f32_16x16x64_bf16 v[0:3], v[236:239], v[156:163], %[idx]\n\t" \
    "v_max3_u32 v252, v252, v236, v237\n\t" \
    "v_smfmac_f32_16x16x64_bf16 v[4:7], v[236:239], v[164:171], %[idx]\n\t" \
    "v_max3_u32 v252, v252, v238, v239\n\t" \
    "v_smfmac_f32_16x16x64_bf16 v[8:11], v[236:239], v[172:179], %[idx]\n\t" \
    "v_smfmac_f32_16x16x64_bf16 v[12:15], v[236:239], v[180:187], %[idx]\n\t" \
    "s_waitcnt vmcnt(0)\n\t" \
    "v_smfmac_f32_16x16x64_bf16 v[0:3], v[240:243], v[188:195], %[idx]\n\t" \
    "v_max3_u32 v252, v252, v240, v241\n\t" \
    "v_smfmac_f32_16x16x64_bf16 v[4:7], v[240:243], v[196:203], %[idx]\n\t" \
    "v_max3_u32 v252, v252, v242, v243\n\t" \
    "v_smfmac_f32_16x16x64_bf16 v[8:11], v[240:243], v[204:211], %[idx]\n\t" \
    "v_smfmac_f32_16x16x64_bf16 v[12:15], v[240:243], v[212:219], %[idx]\n\t" \
    "v_cmp_eq_u32 vcc, -1, v252\n\t" \
    "s_cbranch_vccnz SL_%=\n\t" \
    "D_%=:\n\t" \
    GSA(1) \
    "s_nop 2\n\t" \
    "v_add_f32 v0, v0, v1\n\t" \
    "v_add_f32 v2, v2, v3\n\t" \
    "v_add_f32 v4, v4, v5\n\t" \
    "v_add_f32 v6, v6, v7\n\t" \
    "v_add_f32 v8, v8, v9\n\t" \
    "v_add_f32 v10, v10, v11\n\t" \
    "v_add_f32 v12, v12, v13\n\t" \
    "v_add_f32 v14, v14, v15\n\t" \
    "v_permlane32_swap_b32 v0, v4\n\t" \
    "v_permlane32_swap_b32 v2, v6\n\t" \
    "v_permlane32_swap_b32 v8, v12\n\t" \
    "v_permlane32_swap_b32 v10, v14\n\t" \
    "v_add_f32 v0, v0, v12\n\t" \
    "v_add_f32 v2, v2, v14\n\t" \
    "ds_write2_b32 %[rw], v0, v2 offset0:0 offset1:32\n\t" \
    "s_setprio 3\n\t" \
    "s_cmp_eq_u32 %[red], 4\n\t" \
    "s_waitcnt lgkmcnt(0)\n\t" \
    "s_barrier\n\t" \
    "s_cbranch_scc0 E_%=\n\t" \
    "ds_read2st64_b64 v[0:3], %[rr] offset0:0 offset1:1\n\t" \
    "ds_read2st64_b64 v[4:7], %[rr] offset0:2 offset1:3\n\t" \
    "ds_read2st64_b64 v[8:11], %[rr] offset0:4 offset1:5\n\t" \
    "ds_read2st64_b64 v[12:15], %[rr] offset0:6 offset1:7\n\t" \
    "s_waitcnt lgkmcnt(3)\n\t" \
    "v_pk_add_f32 v[0:1], v[0:1], v[2:3]\n\t" \
    "s_waitcnt lgkmcnt(2)\n\t" \
    "v_pk_add_f32 v[4:5], v[4:5], v[6:7]\n\t" \
    "s_waitcnt lgkmcnt(1)\n\t" \
    "v_pk_add_f32 v[8:9], v[8:9], v[10:11]\n\t" \
    "v_pk_add_f32 v[0:1], v[0:1], v[4:5]\n\t" \
    "s_waitcnt lgkmcnt(0)\n\t" \
    "v_pk_add_f32 v[12:13], v[12:13], v[14:15]\n\t" \
    "s_nop 0\n\t" \
    "v_pk_add_f32 v[8:9], v[8:9], v[12:13]\n\t" \
    "s_nop 0\n\t" \
    "v_pk_add_f32 v[0:1], v[0:1], v[8:9]\n\t" \
    "s_nop 0\n\t" \
    "v_cvt_pk_bf16_f32 v0, v0, v1\n\t" \
    "v_min_u32 v0, -2, v0\n\t" \
    "buffer_store_dword v0, %[mo], %[rs], %[sn] offen sc1\n\t" \
    "s_memrealtime %[t1]\n\t" \
    DGA \
    "s_waitcnt lgkmcnt(0)\n\t" \
    GSA(2) \
    "s_branch E_%=\n\t" \
    "SL_%=:\n\t" \
    "s_sleep 1\n\t" \
    "buffer_load_dwordx4 v[220:223], %[l16], %[rs], %[s0] offen sc1\n\t" \
    "buffer_load_dwordx4 v[224:227], %[l16], %[rs], %[s1] offen sc1\n\t" \
    "buffer_load_dwordx4 v[228:231], %[l16], %[rs], %[s2] offen sc1\n\t" \
    "buffer_load_dwordx4 v[232:235], %[l16], %[rs], %[s3] offen sc1\n\t" \
    "buffer_load_dwordx4 v[236:239], %[l16], %[rs], %[s4] offen sc1\n\t" \
    "buffer_load_dwordx4 v[240:243], %[l16], %[rs], %[s5] offen sc1\n\t" \
    "s_sub_u32 %[lim], %[lim], " STR(DEC2) "\n\t" \
    "s_cmp_gt_i32 %[lim], 0\n\t" \
    "s_cbranch_scc0 G_%=\n\t" \
    "s_nop 3\n\t" \
    "v_mov_b32 v0, 0\n\t" \
    "v_mov_b32 v1, 0\n\t" \
    "v_mov_b32 v2, 0\n\t" \
    "v_mov_b32 v3, 0\n\t" \
    "v_mov_b32 v4, 0\n\t" \
    "v_mov_b32 v5, 0\n\t" \
    "v_mov_b32 v6, 0\n\t" \
    "v_mov_b32 v7, 0\n\t" \
    "v_mov_b32 v8, 0\n\t" \
    "v_mov_b32 v9, 0\n\t" \
    "v_mov_b32 v10, 0\n\t" \
    "v_mov_b32 v11, 0\n\t" \
    "v_mov_b32 v12, 0\n\t" \
    "v_mov_b32 v13, 0\n\t" \
    "v_mov_b32 v14, 0\n\t" \
    "v_mov_b32 v15, 0\n\t" \
    "v_mov_b32 v252, 0\n\t" \
    "s_branch A_%=\n\t" \
    "G_%=:\n\t" \
    "s_waitcnt vmcnt(0)\n\t" \
    "s_branch D_%=\n\t" \
    "E_%=:\n\t" \
    "s_setprio 0\n\t" \
    GSW \
    : [lim] "+s"(lim) GSO(1) GSO(4), [t1] "=&s"(t1) DGO DGQ GSO(2) \
    : [l16] "v"(l16), [rs] "s"(rs), [idx] "v"(idx), [rw] "v"(rw_a), [s0] "s"(so[0]), [s1] "s"(so[1]), [s2] "s"(so[2]), [s3] "s"(so[3]), [s4] "s"(so[4]), [s5] "s"(so[5]), [rr] "v"(rr_a), [mo] "v"(mo_v), [sn] "s"(sn), [red] "s"(wid), "{v[28:35]}"(wo[0][0][0]), "{v[36:43]}"(wo[0][0][1]), "{v[44:51]}"(wo[0][1][0]), "{v[52:59]}"(wo[0][1][1]), "{v[60:67]}"(wo[1][0][0]), "{v[68:75]}"(wo[1][0][1]), "{v[76:83]}"(wo[1][1][0]), "{v[84:91]}"(wo[1][1][1]), "{v[92:99]}"(wo[2][0][0]), "{v[100:107]}"(wo[2][0][1]), "{v[108:115]}"(wo[2][1][0]), "{v[116:123]}"(wo[2][1][1]), "{v[124:131]}"(wo[3][0][0]), "{v[132:139]}"(wo[3][0][1]), "{v[140:147]}"(wo[3][1][0]), "{v[148:155]}"(wo[3][1][1]), "{v[156:163]}"(wo[4][0][0]), "{v[164:171]}"(wo[4][0][1]), "{v[172:179]}"(wo[4][1][0]), "{v[180:187]}"(wo[4][1][1]), "{v[188:195]}"(wo[5][0][0]), "{v[196:203]}"(wo[5][0][1]), "{v[204:211]}"(wo[5][1][0]), "{v[212:219]}"(wo[5][1][1]) \
    : "memory", "vcc", "scc", "v252", "v0", "v1", "v2", "v3", "v4", "v5", "v6", "v7", "v8", "v9", "v10", "v11", "v12", "v13", "v14", "v15", "v220", "v221", "v222", "v223", "v224", "v225", "v226", "v227", "v228", "v229", "v230", "v231", "v232", "v233", "v234", "v235", "v236", "v237", "v238", "v239", "v240", "v241", "v242", "v243")

#define S_LOOP_K(TE_, FIN_) \
  asm volatile( \
    "s_cmp_eq_u32 %[kh], 0\n\t" \
    "s_cbranch_scc0 L1_%=\n\t" \
    "L0_%=:\n\t" \
    "s_setprio 0\n\t" \
    "s_nop 3\n\t" \
    "ds_write_b128 %[pa], v[196:199] offset:0\n\t" \
    "ds_write_b128 %[pa], v[200:203] offset:1024\n\t" \
    "ds_write_b128 %[pa], v[204:207] offset:2048\n\t" \
    "ds_write_b128 %[pa], v[208:211] offset:3072\n\t" \
    "s_sub_u32 %[a], %[t], 1\n\t" \
    "v_mov_b32 v53, %[tag]\n\t" \
    "v_mov_b32 v52, %[a]\n\t" \
    "s_sub_u32 %[b], %[t], 2\n\t" \
    "s_cmp_lt_u32 %[b], " STR(SQUICK) "\n\t" \
    "s_cselect_b64 exec, %[fm], 0\n\t" \
    "buffer_store_dwordx2 v[52:53], %[l16], %[rs], %[fo] offen sc1\n\t" \
    "s_mov_b64 exec, -1\n\t" \
    "s_add_u32 m0, %[sd], 49152\n\t" \
    "s_add_u32 %[a], %[sx], 81920\n\t" \
    "s_waitcnt lgkmcnt(0)\n\t" \
    "buffer_load_dwordx4 %[l16], %[rs], %[a] offen offset:0 sc1 lds\n\t" \
    "buffer_load_dwordx4 %[l16], %[rs], %[a] offen offset:1024 sc1 lds\n\t" \
    "buffer_load_dwordx4 %[l16], %[rs], %[a] offen offset:2048 sc1 lds\n\t" \
    "buffer_load_dwordx4 %[l16], %[rs], %[a] offen offset:3072 sc1 lds\n\t" \
    "v_mfma_f32_16x16x32_bf16 v[0:3], v[68:71], v[20:23], 0\n\t" \
    "ds_read_b128 v[44:47], %[xp] offset:6144\n\t" \
    "v_mfma_f32_16x16x32_bf16 v[4:7], v[76:79], v[20:23], 0\n\t" \
    "ds_read_b128 v[48:51], %[xp] offset:7168\n\t" \
    "v_mfma_f32_16x16x32_bf16 v[8:11], v[68:71], v[24:27], 0\n\t" \
    "v_mfma_f32_16x16x32_bf16 v[12:15], v[76:79], v[24:27], 0\n\t" \
    "v_mfma_f32_16x16x32_bf16 v[0:3], v[72:75], v[28:31], v[0:3]\n\t" \
    "ds_read_b128 v[20:23], %[xp] offset:8192\n\t" \
    "v_mfma_f32_16x16x32_bf16 v[4:7], v[80:83], v[28:31], v[4:7]\n\t" \
    "ds_read_b128 v[24:27], %[xp] offset:9216\n\t" \
    "v_mfma_f32_16x16x32_bf16 v[8:11], v[72:75], v[32:35], v[8:11]\n\t" \
    "v_mfma_f32_16x16x32_bf16 v[12:15], v[80:83], v[32:35], v[12:15]\n\t" \
    "v_mfma_f32_16x16x32_bf16 v[0:3], v[84:87], v[36:39], v[0:3]\n\t" \
    "ds_read_b128 v[28:31], %[xp] offset:10240\n\t" \
    "v_mfma_f32_16x16x32_bf16 v[4:7], v[92:95], v[36:39], v[4:7]\n\t" \
    "ds_read_b128 v[32:35], %[xp] offset:11264\n\t" \
    "v_mfma_f32_16x16x32_bf16 v[8:11], v[84:87], v[40:43], v[8:11]\n\t" \
    "s_waitcnt lgkmcnt(4)\n\t" \
    "v_mfma_f32_16x16x32_bf16 v[12:15], v[92:95], v[40:43], v[12:15]\n\t" \
    "v_mfma_f32_16x16x32_bf16 v[0:3], v[88:91], v[44:47], v[0:3]\n\t" \
    "ds_read_b128 v[36:39], %[xp] offset:12288\n\t" \
    "v_mfma_f32_16x16x32_bf16 v[4:7], v[96:99], v[44:47], v[4:7]\n\t" \
    "ds_read_b128 v[40:43], %[xp] offset:13312\n\t" \
    "v_mfma_f32_16x16x32_bf16 v[8:11], v[88:91], v[48:51], v[8:11]\n\t" \
    "s_waitcnt lgkmcnt(4)\n\t" \
    "v_mfma_f32_16x16x32_bf16 v[12:15], v[96:99], v[48:51], v[12:15]\n\t" \
    "v_mfma_f32_16x16x32_bf16 v[0:3], v[100:103], v[20:23], v[0:3]\n\t" \
    "ds_read_b128 v[44:47], %[xp] offset:14336\n\t" \
    "v_mfma_f32_16x16x32_bf16 v[4:7], v[108:111], v[20:23], v[4:7]\n\t" \
    "ds_read_b128 v[48:51], %[xp] offset:15360\n\t" \
    "v_mfma_f32_16x16x32_bf16 v[8:11], v[100:103], v[24:27], v[8:11]\n\t" \
    "s_waitcnt lgkmcnt(4)\n\t" \
    "v_mfma_f32_16x16x32_bf16 v[12:15], v[108:111], v[24:27], v[12:15]\n\t" \
    "v_mfma_f32_16x16x32_bf16 v[0:3], v[104:107], v[28:31], v[0:3]\n\t" \
    "ds_read_b128 v[20:23], %[xp] offset:16384\n\t" \
    "v_mfma_f32_16x16x32_bf16 v[4:7], v[112:115], v[28:31], v[4:7]\n\t" \
    "ds_read_b128 v[24:27], %[xp] offset:17408\n\t" \
    "v_mfma_f32_16x16x32_bf16 v[8:11], v[104:107], v[32:35], v[8:11]\n\t" \
    "s_waitcnt lgkmcnt(4)\n\t" \
    "v_mfma_f32_16x16x32_bf16 v[12:15], v[112:115], v[32:35], v[12:15]\n\t" \
    "v_mfma_f32_16x16x32_bf16 v[0:3], v[116:119], v[36:39], v[0:3]\n\t" \
    "ds_read_b128 v[28:31], %[xp] offset:18432\n\t" \
    "v_mfma_f32_16x16x32_bf16 v[4:7], v[124:127], v[36:39], v[4:7]\n\t" \
    "ds_read_b128 v[32:35], %[xp] offset:19456\n\t" \
    "v_mfma_f32_16x16x32_bf16 v[8:11], v[116:119], v[40:43], v[8:11]\n\t" \
    "s_waitcnt lgkmcnt(4)\n\t" \
    "v_mfma_f32_16x16x32_bf16 v[12:15], v[124:127], v[40:43], v[12:15]\n\t" \
    "v_mfma_f32_16x16x32_bf16 v[0:3], v[120:123], v[44:47], v[0:3]\n\t" \
    "ds_read_b128 v[36:39], %[xp] offset:20480\n\t" \
    "v_mfma_f32_16x16x32_bf16 v[4:7], v[128:131], v[44:47], v[4:7]\n\t" \
    "ds_read_b128 v[40:43], %[xp] offset:21504\n\t" \
    "v_mfma_f32_16x16x32_bf16 v[8:11], v[120:123], v[48:51], v[8:11]\n\t" \
    "s_waitcnt lgkmcnt(4)\n\t" \
    "v_mfma_f32_16x16x32_bf16 v[12:15], v[128:131], v[48:51], v[12:15]\n\t" \
    "s_waitcnt vmcnt(4)\n\t" \
    "s_barrier\n\t" \
    "s_setprio 0\n\t" \
    "s_sub_u32 %[a], %[t], 1\n\t" \
    "v_mov_b32 v53, %[tag]\n\t" \
    "v_mov_b32 v52, %[a]\n\t" \
    "s_cmp_gt_u32 %[t], 1\n\t" \
    "s_cselect_b64 exec, %[fm], 0\n\t" \
    "buffer_store_dwordx2 v[52:53], %[l16], %[rs], %[fo] offen sc1\n\t" \
    "s_mov_b64 exec, %[pm]\n\t" \
    "s_mov_b32 m0, %[pd]\n\t" \
    "s_add_u32 %[a], %[sx], %[po]\n\t" \
    "buffer_load_dwordx4 %[l16], %[rs], %[a] offen sc1 lds\n\t" \
    "s_mov_b64 exec, -1\n\t" \
    "s_add_u32 m0, %[sd], 0\n\t" \
    "s_add_u32 %[a], %[sx], 131072\n\t" \
    "buffer_load_dwordx4 %[l16], %[rs], %[a] offen offset:0 sc1 lds\n\t" \
    "buffer_load_dwordx4 %[l16], %[rs], %[a] offen offset:1024 sc1 lds\n\t" \
    "buffer_load_dwordx4 %[l16], %[rs], %[a] offen offset:2048 sc1 lds\n\t" \
    "buffer_load_dwordx4 %[l16], %[rs], %[a] offen offset:3072 sc1 lds\n\t" \
    "v_mfma_f32_16x16x32_bf16 v[0:3], v[132:135], v[20:23], v[0:3]\n\t" \
    "ds_read_b128 v[44:47], %[xp] offset:22528\n\t" \
    "v_mfma_f32_16x16x32_bf16 v[4:7], v[140:143], v[20:23], v[4:7]\n\t" \
    "ds_read_b128 v[48:51], %[xp] offset:23552\n\t" \
    "v_mfma_f32_16x16x32_bf16 v[8:11], v[132:135], v[24:27], v[8:11]\n\t" \
    "s_waitcnt lgkmcnt(4)\n\t" \
    "v_mfma_f32_16x16x32_bf16 v[12:15], v[140:143], v[24:27], v[12:15]\n\t" \
    "v_mfma_f32_16x16x32_bf16 v[0:3], v[136:139], v[28:31], v[0:3]\n\t" \
    "ds_read_b128 v[20:23], %[xp] offset:24576\n\t" \
    "v_mfma_f32_16x16x32_bf16 v[4:7], v[144:147], v[28:31], v[4:7]\n\t" \
    "ds_read_b128 v[24:27], %[xp] offset:25600\n\t" \
    "v_mfma_f32_16x16x32_bf16 v[8:11], v[136:139], v[32:35], v[8:11]\n\t" \
    "s_waitcnt lgkmcnt(4)\n\t" \
    "v_mfma_f32_16x16x32_bf16 v[12:15], v[144:147], v[32:35], v[12:15]\n\t" \
    "v_mfma_f32_16x16x32_bf16 v[0:3], v[148:151], v[36:39], v[0:3]\n\t" \
    "ds_read_b128 v[28:31], %[xp] offset:26624\n\t" \
    "v_mfma_f32_16x16x32_bf16 v[4:7], v[156:159], v[36:39], v[4:7]\n\t" \
    "ds_read_b128 v[32:35], %[xp] offset:27648\n\t" \
    "v_mfma_f32_16x16x32_bf16 v[8:11], v[148:151], v[40:43], v[8:11]\n\t" \
    "s_waitcnt lgkmcnt(4)\n\t" \
    "v_mfma_f32_16x16x32_bf16 v[12:15], v[156:159], v[40:43], v[12:15]\n\t" \
    "v_mfma_f32_16x16x32_bf16 v[0:3], v[152:155], v[44:47], v[0:3]\n\t" \
    "ds_read_b128 v[36:39], %[xp] offset:28672\n\t" \
    "v_mfma_f32_16x16x32_bf16 v[4:7], v[160:163], v[44:47], v[4:7]\n\t" \
    "ds_read_b128 v[40:43], %[xp] offset:29696\n\t" \
    "v_mfma_f32_16x16x32_bf16 v[8:11], v[152:155], v[48:51], v[8:11]\n\t" \
    "s_waitcnt lgkmcnt(4)\n\t" \
    "v_mfma_f32_16x16x32_bf16 v[12:15], v[160:163], v[48:51], v[12:15]\n\t" \
    "v_mfma_f32_16x16x32_bf16 v[0:3], v[164:167], v[20:23], v[0:3]\n\t" \
    "ds_read_b128 v[44:47], %[xp] offset:30720\n\t" \
    "v_mfma_f32_16x16x32_bf16 v[4:7], v[172:175], v[20:23], v[4:7]\n\t" \
    "ds_read_b128 v[48:51], %[xp] offset:31744\n\t" \
    "v_mfma_f32_16x16x32_bf16 v[8:11], v[164:167], v[24:27], v[8:11]\n\t" \
    "s_waitcnt lgkmcnt(4)\n\t" \
    "v_mfma_f32_16x16x32_bf16 v[12:15], v[172:175], v[24:27], v[12:15]\n\t" \
    "v_mfma_f32_16x16x32_bf16 v[0:3], v[168:171], v[28:31], v[0:3]\n\t" \
    "ds_read_b128 v[20:23], %[xp] offset:32768\n\t" \
    "v_mfma_f32_16x16x32_bf16 v[4:7], v[176:179], v[28:31], v[4:7]\n\t" \
    "ds_read_b128 v[24:27], %[xp] offset:33792\n\t" \
    "v_mfma_f32_16x16x32_bf16 v[8:11], v[168:171], v[32:35], v[8:11]\n\t" \
    "s_waitcnt lgkmcnt(4)\n\t" \
    "v_mfma_f32_16x16x32_bf16 v[12:15], v[176:179], v[32:35], v[12:15]\n\t" \
    "v_mfma_f32_16x16x32_bf16 v[0:3], v[180:183], v[36:39], v[0:3]\n\t" \
    "ds_read_b128 v[28:31], %[xp] offset:34816\n\t" \
    "v_mfma_f32_16x16x32_bf16 v[4:7], v[188:191], v[36:39], v[4:7]\n\t" \
    "ds_read_b128 v[32:35], %[xp] offset:35840\n\t" \
    "v_mfma_f32_16x16x32_bf16 v[8:11], v[180:183], v[40:43], v[8:11]\n\t" \
    "s_waitcnt lgkmcnt(4)\n\t" \
    "v_mfma_f32_16x16x32_bf16 v[12:15], v[188:191], v[40:43], v[12:15]\n\t" \
    "v_mfma_f32_16x16x32_bf16 v[0:3], v[184:187], v[44:47], v[0:3]\n\t" \
    "ds_read_b128 v[36:39], %[xp] offset:36864\n\t" \
    "v_mfma_f32_16x16x32_bf16 v[4:7], v[192:195], v[44:47], v[4:7]\n\t" \
    "ds_read_b128 v[40:43], %[xp] offset:37888\n\t" \
    "v_mfma_f32_16x16x32_bf16 v[8:11], v[184:187], v[48:51], v[8:11]\n\t" \
    "s_waitcnt lgkmcnt(4)\n\t" \
    "v_mfma_f32_16x16x32_bf16 v[12:15], v[192:195], v[48:51], v[12:15]\n\t" \
    "s_add_u32 %[sx], %[sx], 65536\n\t" \
    "s_add_u32 %[t], %[t], 1\n\t" \
    "s_waitcnt vmcnt(4)\n\t" \
    "s_barrier\n\t" \
    "s_setprio 0\n\t" \
    "s_nop 3\n\t" \
    "ds_write_b128 %[pa], v[0:3] offset:0\n\t" \
    "ds_write_b128 %[pa], v[4:7] offset:1024\n\t" \
    "ds_write_b128 %[pa], v[8:11] offset:2048\n\t" \
    "ds_write_b128 %[pa], v[12:15] offset:3072\n\t" \
    "s_sub_u32 %[a], %[t], 1\n\t" \
    "v_mov_b32 v53, %[tag]\n\t" \
    "v_mov_b32 v52, %[a]\n\t" \
    "s_sub_u32 %[b], %[t], 2\n\t" \
    "s_cmp_lt_u32 %[b], " STR(SQUICK) "\n\t" \
    "s_cselect_b64 exec, %[fm], 0\n\t" \
    "buffer_store_dwordx2 v[52:53], %[l16], %[rs], %[fo] offen sc1\n\t" \
    "s_mov_b64 exec, -1\n\t" \
    "s_add_u32 m0, %[sd], 16384\n\t" \
    "s_add_u32 %[a], %[sx], 81920\n\t" \
    "s_waitcnt lgkmcnt(0)\n\t" \
    "buffer_load_dwordx4 %[l16], %[rs], %[a] offen offset:0 sc1 lds\n\t" \
    "buffer_load_dwordx4 %[l16], %[rs], %[a] offen offset:1024 sc1 lds\n\t" \
    "buffer_load_dwordx4 %[l16], %[rs], %[a] offen offset:2048 sc1 lds\n\t" \
    "buffer_load_dwordx4 %[l16], %[rs], %[a] offen offset:3072 sc1 lds\n\t" \
    "v_mfma_f32_16x16x32_bf16 v[196:199], v[68:71], v[20:23], 0\n\t" \
    "ds_read_b128 v[44:47], %[xp] offset:38912\n\t" \
    "v_mfma_f32_16x16x32_bf16 v[200:203], v[76:79], v[20:23], 0\n\t" \
    "ds_read_b128 v[48:51], %[xp] offset:39936\n\t" \
    "v_mfma_f32_16x16x32_bf16 v[204:207], v[68:71], v[24:27], 0\n\t" \
    "v_mfma_f32_16x16x32_bf16 v[208:211], v[76:79], v[24:27], 0\n\t" \
    "v_mfma_f32_16x16x32_bf16 v[196:199], v[72:75], v[28:31], v[196:199]\n\t" \
    "ds_read_b128 v[20:23], %[xp] offset:40960\n\t" \
    "v_mfma_f32_16x16x32_bf16 v[200:203], v[80:83], v[28:31], v[200:203]\n\t" \
    "ds_read_b128 v[24:27], %[xp] offset:41984\n\t" \
    "v_mfma_f32_16x16x32_bf16 v[204:207], v[72:75], v[32:35], v[204:207]\n\t" \
    "v_mfma_f32_16x16x32_bf16 v[208:211], v[80:83], v[32:35], v[208:211]\n\t" \
    "v_mfma_f32_16x16x32_bf16 v[196:199], v[84:87], v[36:39], v[196:199]\n\t" \
    "ds_read_b128 v[28:31], %[xp] offset:43008\n\t" \
    "v_mfma_f32_16x16x32_bf16 v[200:203], v[92:95], v[36:39], v[200:203]\n\t" \
    "ds_read_b128 v[32:35], %[xp] offset:44032\n\t" \
    "v_mfma_f32_16x16x32_bf16 v[204:207], v[84:87], v[40:43], v[204:207]\n\t" \
    "s_waitcnt lgkmcnt(4)\n\t" \
    "v_mfma_f32_16x16x32_bf16 v[208:211], v[92:95], v[40:43], v[208:211]\n\t" \
    "v_mfma_f32_16x16x32_bf16 v[196:199], v[88:91], v[44:47], v[196:199]\n\t" \
    "ds_read_b128 v[36:39], %[xp] offset:45056\n\t" \
    "v_mfma_f32_16x16x32_bf16 v[200:203], v[96:99], v[44:47], v[200:203]\n\t" \
    "ds_read_b128 v[40:43], %[xp] offset:46080\n\t" \
    "v_mfma_f32_16x16x32_bf16 v[204:207], v[88:91], v[48:51], v[204:207]\n\t" \
    "s_waitcnt lgkmcnt(4)\n\t" \
    "v_mfma_f32_16x16x32_bf16 v[208:211], v[96:99], v[48:51], v[208:211]\n\t" \
    "v_mfma_f32_16x16x32_bf16 v[196:199], v[100:103], v[20:23], v[196:199]\n\t" \
    "ds_read_b128 v[44:47], %[xp] offset:47104\n\t" \
    "v_mfma_f32_16x16x32_bf16 v[200:203], v[108:111], v[20:23], v[200:203]\n\t" \
    "ds_read_b128 v[48:51], %[xp] offset:48128\n\t" \
    "v_mfma_f32_16x16x32_bf16 v[204:207], v[100:103], v[24:27], v[204:207]\n\t" \
    "s_waitcnt lgkmcnt(4)\n\t" \
    "v_mfma_f32_16x16x32_bf16 v[208:211], v[108:111], v[24:27], v[208:211]\n\t" \
    "v_mfma_f32_16x16x32_bf16 v[196:199], v[104:107], v[28:31], v[196:199]\n\t" \
    "ds_read_b128 v[20:23], %[xp] offset:49152\n\t" \
    "v_mfma_f32_16x16x32_bf16 v[200:203], v[112:115], v[28:31], v[200:203]\n\t" \
    "ds_read_b128 v[24:27], %[xp] offset:50176\n\t" \
    "v_mfma_f32_16x16x32_bf16 v[204:207], v[104:107], v[32:35], v[204:207]\n\t" \
    "s_waitcnt lgkmcnt(4)\n\t" \
    "v_mfma_f32_16x16x32_bf16 v[208:211], v[112:115], v[32:35], v[208:211]\n\t" \
    "v_mfma_f32_16x16x32_bf16 v[196:199], v[116:119], v[36:39], v[196:199]\n\t" \
    "ds_read_b128 v[28:31], %[xp] offset:51200\n\t" \
    "v_mfma_f32_16x16x32_bf16 v[200:203], v[124:127], v[36:39], v[200:203]\n\t" \
    "ds_read_b128 v[32:35], %[xp] offset:52224\n\t" \
    "v_mfma_f32_16x16x32_bf16 v[204:207], v[116:119], v[40:43], v[204:207]\n\t" \
    "s_waitcnt lgkmcnt(4)\n\t" \
    "v_mfma_f32_16x16x32_bf16 v[208:211], v[124:127], v[40:43], v[208:211]\n\t" \
    "v_mfma_f32_16x16x32_bf16 v[196:199], v[120:123], v[44:47], v[196:199]\n\t" \
    "ds_read_b128 v[36:39], %[xp] offset:53248\n\t" \
    "v_mfma_f32_16x16x32_bf16 v[200:203], v[128:131], v[44:47], v[200:203]\n\t" \
    "ds_read_b128 v[40:43], %[xp] offset:54272\n\t" \
    "v_mfma_f32_16x16x32_bf16 v[204:207], v[120:123], v[48:51], v[204:207]\n\t" \
    "s_waitcnt lgkmcnt(4)\n\t" \
    "v_mfma_f32_16x16x32_bf16 v[208:211], v[128:131], v[48:51], v[208:211]\n\t" \
    "s_waitcnt vmcnt(4)\n\t" \
    "s_barrier\n\t" \
    "s_setprio 0\n\t" \
    "s_sub_u32 %[a], %[t], 1\n\t" \
    "v_mov_b32 v53, %[tag]\n\t" \
    "v_mov_b32 v52, %[a]\n\t" \
    "s_cmp_gt_u32 %[t], 1\n\t" \
    "s_cselect_b64 exec, %[fm], 0\n\t" \
    "buffer_store_dwordx2 v[52:53], %[l16], %[rs], %[fo] offen sc1\n\t" \
    "s_mov_b64 exec, %[pm]\n\t" \
    "s_mov_b32 m0, %[pd]\n\t" \
    "s_add_u32 %[a], %[sx], %[po]\n\t" \
    "buffer_load_dwordx4 %[l16], %[rs], %[a] offen sc1 lds\n\t" \
    "s_mov_b64 exec, -1\n\t" \
    "s_add_u32 m0, %[sd], 32768\n\t" \
    "s_add_u32 %[a], %[sx], 131072\n\t" \
    "buffer_load_dwordx4 %[l16], %[rs], %[a] offen offset:0 sc1 lds\n\t" \
    "buffer_load_dwordx4 %[l16], %[rs], %[a] offen offset:1024 sc1 lds\n\t" \
    "buffer_load_dwordx4 %[l16], %[rs], %[a] offen offset:2048 sc1 lds\n\t" \
    "buffer_load_dwordx4 %[l16], %[rs], %[a] offen offset:3072 sc1 lds\n\t" \
    "v_mfma_f32_16x16x32_bf16 v[196:199], v[132:135], v[20:23], v[196:199]\n\t" \
    "ds_read_b128 v[44:47], %[xp] offset:55296\n\t" \
    "v_mfma_f32_16x16x32_bf16 v[200:203], v[140:143], v[20:23], v[200:203]\n\t" \
    "ds_read_b128 v[48:51], %[xp] offset:56320\n\t" \
    "v_mfma_f32_16x16x32_bf16 v[204:207], v[132:135], v[24:27], v[204:207]\n\t" \
    "s_waitcnt lgkmcnt(4)\n\t" \
    "v_mfma_f32_16x16x32_bf16 v[208:211], v[140:143], v[24:27], v[208:211]\n\t" \
    "v_mfma_f32_16x16x32_bf16 v[196:199], v[136:139], v[28:31], v[196:199]\n\t" \
    "ds_read_b128 v[20:23], %[xp] offset:57344\n\t" \
    "v_mfma_f32_16x16x32_bf16 v[200:203], v[144:147], v[28:31], v[200:203]\n\t" \
    "ds_read_b128 v[24:27], %[xp] offset:58368\n\t" \
    "v_mfma_f32_16x16x32_bf16 v[204:207], v[136:139], v[32:35], v[204:207]\n\t" \
    "s_waitcnt lgkmcnt(4)\n\t" \
    "v_mfma_f32_16x16x32_bf16 v[208:211], v[144:147], v[32:35], v[208:211]\n\t" \
    "v_mfma_f32_16x16x32_bf16 v[196:199], v[148:151], v[36:39], v[196:199]\n\t" \
    "ds_read_b128 v[28:31], %[xp] offset:59392\n\t" \
    "v_mfma_f32_16x16x32_bf16 v[200:203], v[156:159], v[36:39], v[200:203]\n\t" \
    "ds_read_b128 v[32:35], %[xp] offset:60416\n\t" \
    "v_mfma_f32_16x16x32_bf16 v[204:207], v[148:151], v[40:43], v[204:207]\n\t" \
    "s_waitcnt lgkmcnt(4)\n\t" \
    "v_mfma_f32_16x16x32_bf16 v[208:211], v[156:159], v[40:43], v[208:211]\n\t" \
    "v_mfma_f32_16x16x32_bf16 v[196:199], v[152:155], v[44:47], v[196:199]\n\t" \
    "ds_read_b128 v[36:39], %[xp] offset:61440\n\t" \
    "v_mfma_f32_16x16x32_bf16 v[200:203], v[160:163], v[44:47], v[200:203]\n\t" \
    "ds_read_b128 v[40:43], %[xp] offset:62464\n\t" \
    "v_mfma_f32_16x16x32_bf16 v[204:207], v[152:155], v[48:51], v[204:207]\n\t" \
    "s_waitcnt lgkmcnt(4)\n\t" \
    "v_mfma_f32_16x16x32_bf16 v[208:211], v[160:163], v[48:51], v[208:211]\n\t" \
    "v_mfma_f32_16x16x32_bf16 v[196:199], v[164:167], v[20:23], v[196:199]\n\t" \
    "ds_read_b128 v[44:47], %[xp] offset:63488\n\t" \
    "v_mfma_f32_16x16x32_bf16 v[200:203], v[172:175], v[20:23], v[200:203]\n\t" \
    "ds_read_b128 v[48:51], %[xp] offset:64512\n\t" \
    "v_mfma_f32_16x16x32_bf16 v[204:207], v[164:167], v[24:27], v[204:207]\n\t" \
    "s_waitcnt lgkmcnt(4)\n\t" \
    "v_mfma_f32_16x16x32_bf16 v[208:211], v[172:175], v[24:27], v[208:211]\n\t" \
    "v_mfma_f32_16x16x32_bf16 v[196:199], v[168:171], v[28:31], v[196:199]\n\t" \
    "ds_read_b128 v[20:23], %[xp] offset:0\n\t" \
    "v_mfma_f32_16x16x32_bf16 v[200:203], v[176:179], v[28:31], v[200:203]\n\t" \
    "ds_read_b128 v[24:27], %[xp] offset:1024\n\t" \
    "v_mfma_f32_16x16x32_bf16 v[204:207], v[168:171], v[32:35], v[204:207]\n\t" \
    "s_waitcnt lgkmcnt(4)\n\t" \
    "v_mfma_f32_16x16x32_bf16 v[208:211], v[176:179], v[32:35], v[208:211]\n\t" \
    "v_mfma_f32_16x16x32_bf16 v[196:199], v[180:183], v[36:39], v[196:199]\n\t" \
    "ds_read_b128 v[28:31], %[xp] offset:2048\n\t" \
    "v_mfma_f32_16x16x32_bf16 v[200:203], v[188:191], v[36:39], v[200:203]\n\t" \
    "ds_read_b128 v[32:35], %[xp] offset:3072\n\t" \
    "v_mfma_f32_16x16x32_bf16 v[204:207], v[180:183], v[40:43], v[204:207]\n\t" \
    "s_waitcnt lgkmcnt(4)\n\t" \
    "v_mfma_f32_16x16x32_bf16 v[208:211], v[188:191], v[40:43], v[208:211]\n\t" \
    "v_mfma_f32_16x16x32_bf16 v[196:199], v[184:187], v[44:47], v[196:199]\n\t" \
    "ds_read_b128 v[36:39], %[xp] offset:4096\n\t" \
    "v_mfma_f32_16x16x32_bf16 v[200:203], v[192:195], v[44:47], v[200:203]\n\t" \
    "ds_read_b128 v[40:43], %[xp] offset:5120\n\t" \
    "v_mfma_f32_16x16x32_bf16 v[204:207], v[184:187], v[48:51], v[204:207]\n\t" \
    "s_waitcnt lgkmcnt(4)\n\t" \
    "v_mfma_f32_16x16x32_bf16 v[208:211], v[192:195], v[48:51], v[208:211]\n\t" \
    "s_add_u32 %[sx], %[sx], 65536\n\t" \
    "s_add_u32 %[t], %[t], 1\n\t" \
    "s_waitcnt vmcnt(4)\n\t" \
    "s_barrier\n\t" \
    "s_cmp_lt_u32 %[t], %[te]\n\t" \
    "s_cbranch_scc1 L0_%=\n\t" \
    "s_cmp_eq_u32 %[fin], %[te]\n\t" \
    "s_cbranch_scc0 E_%=\n\t" \
    "s_nop 4\n\t" \
    "ds_write_b128 %[pa], v[196:199] offset:0\n\t" \
    "ds_write_b128 %[pa], v[200:203] offset:1024\n\t" \
    "ds_write_b128 %[pa], v[204:207] offset:2048\n\t" \
    "ds_write_b128 %[pa], v[208:211] offset:3072\n\t" \
    "s_waitcnt lgkmcnt(0)\n\t" \
    "s_barrier\n\t" \
    "s_branch E_%=\n\t" \
    "L1_%=:\n\t" \
    "s_setprio 1\n\t" \
    "s_add_u32 m0, %[sd], 49152\n\t" \
    "s_add_u32 %[a], %[sx], 81920\n\t" \
    "v_mfma_f32_16x16x32_bf16 v[0:3], v[68:71], v[20:23], 0\n\t" \
    "ds_read_b128 v[44:47], %[xp] offset:6144\n\t" \
    "v_mfma_f32_16x16x32_bf16 v[4:7], v[76:79], v[20:23], 0\n\t" \
    "ds_read_b128 v[48:51], %[xp] offset:7168\n\t" \
    "v_mfma_f32_16x16x32_bf16 v[8:11], v[68:71], v[24:27], 0\n\t" \
    "s_waitcnt lgkmcnt(4)\n\t" \
    "v_mfma_f32_16x16x32_bf16 v[12:15], v[76:79], v[24:27], 0\n\t" \
    "v_mfma_f32_16x16x32_bf16 v[0:3], v[72:75], v[28:31], v[0:3]\n\t" \
    "ds_read_b128 v[20:23], %[xp] offset:8192\n\t" \
    "v_mfma_f32_16x16x32_bf16 v[4:7], v[80:83], v[28:31], v[4:7]\n\t" \
    "ds_read_b128 v[24:27], %[xp] offset:9216\n\t" \
    "v_mfma_f32_16x16x32_bf16 v[8:11], v[72:75], v[32:35], v[8:11]\n\t" \
    "s_waitcnt lgkmcnt(4)\n\t" \
    "v_mfma_f32_16x16x32_bf16 v[12:15], v[80:83], v[32:35], v[12:15]\n\t" \
    "v_mfma_f32_16x16x32_bf16 v[0:3], v[84:87], v[36:39], v[0:3]\n\t" \
    "ds_read_b128 v[28:31], %[xp] offset:10240\n\t" \
    "v_mfma_f32_16x16x32_bf16 v[4:7], v[92:95], v[36:39], v[4:7]\n\t" \
    "ds_read_b128 v[32:35], %[xp] offset:11264\n\t" \
    "v_mfma_f32_16x16x32_bf16 v[8:11], v[84:87], v[40:43], v[8:11]\n\t" \
    "s_waitcnt lgkmcnt(4)\n\t" \
    "v_mfma_f32_16x16x32_bf16 v[12:15], v[92:95], v[40:43], v[12:15]\n\t" \
    "v_mfma_f32_16x16x32_bf16 v[0:3], v[88:91], v[44:47], v[0:3]\n\t" \
    "ds_read_b128 v[36:39], %[xp] offset:12288\n\t" \
    "v_mfma_f32_16x16x32_bf16 v[4:7], v[96:99], v[44:47], v[4:7]\n\t" \
    "ds_read_b128 v[40:43], %[xp] offset:13312\n\t" \
    "v_mfma_f32_16x16x32_bf16 v[8:11], v[88:91], v[48:51], v[8:11]\n\t" \
    "s_waitcnt lgkmcnt(4)\n\t" \
    "v_mfma_f32_16x16x32_bf16 v[12:15], v[96:99], v[48:51], v[12:15]\n\t" \
    "v_mfma_f32_16x16x32_bf16 v[0:3], v[100:103], v[20:23], v[0:3]\n\t" \
    "ds_read_b128 v[44:47], %[xp] offset:14336\n\t" \
    "v_mfma_f32_16x16x32_bf16 v[4:7], v[108:111], v[20:23], v[4:7]\n\t" \
    "ds_read_b128 v[48:51], %[xp] offset:15360\n\t" \
    "v_mfma_f32_16x16x32_bf16 v[8:11], v[100:103], v[24:27], v[8:11]\n\t" \
    "s_waitcnt lgkmcnt(4)\n\t" \
    "v_mfma_f32_16x16x32_bf16 v[12:15], v[108:111], v[24:27], v[12:15]\n\t" \
    "buffer_load_dwordx4 %[l16], %[rs], %[a] offen offset:0 sc1 lds\n\t" \
    "v_mfma_f32_16x16x32_bf16 v[0:3], v[104:107], v[28:31], v[0:3]\n\t" \
    "ds_read_b128 v[20:23], %[xp] offset:16384\n\t" \
    "v_mfma_f32_16x16x32_bf16 v[4:7], v[112:115], v[28:31], v[4:7]\n\t" \
    "ds_read_b128 v[24:27], %[xp] offset:17408\n\t" \
    "v_mfma_f32_16x16x32_bf16 v[8:11], v[104:107], v[32:35], v[8:11]\n\t" \
    "s_waitcnt lgkmcnt(4)\n\t" \
    "v_mfma_f32_16x16x32_bf16 v[12:15], v[112:115], v[32:35], v[12:15]\n\t" \
    "buffer_load_dwordx4 %[l16], %[rs], %[a] offen offset:1024 sc1 lds\n\t" \
    "v_mfma_f32_16x16x32_bf16 v[0:3], v[116:119], v[36:39], v[0:3]\n\t" \
    "ds_read_b128 v[28:31], %[xp] offset:18432\n\t" \
    "v_mfma_f32_16x16x32_bf16 v[4:7], v[124:127], v[36:39], v[4:7]\n\t" \
    "ds_read_b128 v[32:35], %[xp] offset:19456\n\t" \
    "v_mfma_f32_16x16x32_bf16 v[8:11], v[116:119], v[40:43], v[8:11]\n\t" \
    "s_waitcnt lgkmcnt(4)\n\t" \
    "v_mfma_f32_16x16x32_bf16 v[12:15], v[124:127], v[40:43], v[12:15]\n\t" \
    "buffer_load_dwordx4 %[l16], %[rs], %[a] offen offset:2048 sc1 lds\n\t" \
    "v_mfma_f32_16x16x32_bf16 v[0:3], v[120:123], v[44:47], v[0:3]\n\t" \
    "ds_read_b128 v[36:39], %[xp] offset:20480\n\t" \
    "v_mfma_f32_16x16x32_bf16 v[4:7], v[128:131], v[44:47], v[4:7]\n\t" \
    "ds_read_b128 v[40:43], %[xp] offset:21504\n\t" \
    "v_mfma_f32_16x16x32_bf16 v[8:11], v[120:123], v[48:51], v[8:11]\n\t" \
    "s_waitcnt lgkmcnt(4)\n\t" \
    "v_mfma_f32_16x16x32_bf16 v[12:15], v[128:131], v[48:51], v[12:15]\n\t" \
    "buffer_load_dwordx4 %[l16], %[rs], %[a] offen offset:3072 sc1 lds\n\t" \
    "s_waitcnt vmcnt(4)\n\t" \
    "s_barrier\n\t" \
    "s_setprio 1\n\t" \
    "s_add_u32 m0, %[sd], 0\n\t" \
    "s_add_u32 %[a], %[sx], 131072\n\t" \
    "s_add_u32 %[b], %[oo], 0x1000\n\t" \
    "s_cmp_lg_u32 %[t], 1\n\t" \
    "s_cselect_b64 %[em], -1, 0\n\t" \
    "v_mfma_f32_16x16x32_bf16 v[0:3], v[132:135], v[20:23], v[0:3]\n\t" \
    "ds_read_b128 v[44:47], %[xp] offset:22528\n\t" \
    "v_mfma_f32_16x16x32_bf16 v[4:7], v[140:143], v[20:23], v[4:7]\n\t" \
    "ds_read_b128 v[48:51], %[xp] offset:23552\n\t" \
    "v_mfma_f32_16x16x32_bf16 v[8:11], v[132:135], v[24:27], v[8:11]\n\t" \
    "ds_read_b128 v[52:55], %[pa] offset:0\n\t" \
    "s_waitcnt lgkmcnt(5)\n\t" \
    "v_mfma_f32_16x16x32_bf16 v[12:15], v[140:143], v[24:27], v[12:15]\n\t" \
    "buffer_load_dwordx4 %[l16], %[rs], %[a] offen offset:0 sc1 lds\n\t" \
    "v_mfma_f32_16x16x32_bf16 v[0:3], v[136:139], v[28:31], v[0:3]\n\t" \
    "ds_read_b128 v[20:23], %[xp] offset:24576\n\t" \
    "v_mfma_f32_16x16x32_bf16 v[4:7], v[144:147], v[28:31], v[4:7]\n\t" \
    "ds_read_b128 v[24:27], %[xp] offset:25600\n\t" \
    "v_mfma_f32_16x16x32_bf16 v[8:11], v[136:139], v[32:35], v[8:11]\n\t" \
    "ds_read_b128 v[56:59], %[pa] offset:1024\n\t" \
    "s_waitcnt lgkmcnt(6)\n\t" \
    "v_mfma_f32_16x16x32_bf16 v[12:15], v[144:147], v[32:35], v[12:15]\n\t" \
    "buffer_load_dwordx4 %[l16], %[rs], %[a] offen offset:1024 sc1 lds\n\t" \
    "v_mfma_f32_16x16x32_bf16 v[0:3], v[148:151], v[36:39], v[0:3]\n\t" \
    "ds_read_b128 v[28:31], %[xp] offset:26624\n\t" \
    "v_mfma_f32_16x16x32_bf16 v[4:7], v[156:159], v[36:39], v[4:7]\n\t" \
    "ds_read_b128 v[32:35], %[xp] offset:27648\n\t" \
    "v_mfma_f32_16x16x32_bf16 v[8:11], v[148:151], v[40:43], v[8:11]\n\t" \
    "ds_read_b128 v[60:63], %[pa] offset:2048\n\t" \
    "s_waitcnt lgkmcnt(7)\n\t" \
    "v_mfma_f32_16x16x32_bf16 v[12:15], v[156:159], v[40:43], v[12:15]\n\t" \
    "buffer_load_dwordx4 %[l16], %[rs], %[a] offen offset:2048 sc1 lds\n\t" \
    "v_mfma_f32_16x16x32_bf16 v[0:3], v[152:155], v[44:47], v[0:3]\n\t" \
    "ds_read_b128 v[36:39], %[xp] offset:28672\n\t" \
    "s_waitcnt lgkmcnt(7)\n\t" \
    "v_add_f32 v196, v196, v52\n\t" \
    "v_mfma_f32_16x16x32_bf16 v[4:7], v[160:163], v[44:47], v[4:7]\n\t" \
    "ds_read_b128 v[40:43], %[xp] offset:29696\n\t" \
    "v_add_f32 v197, v197, v53\n\t" \
    "v_mfma_f32_16x16x32_bf16 v[8:11], v[152:155], v[48:51], v[8:11]\n\t" \
    "ds_read_b128 v[64:67], %[pa] offset:3072\n\t" \
    "s_waitcnt lgkmcnt(7)\n\t" \
    "v_mfma_f32_16x16x32_bf16 v[12:15], v[160:163], v[48:51], v[12:15]\n\t" \
    "buffer_load_dwordx4 %[l16], %[rs], %[a] offen offset:3072 sc1 lds\n\t" \
    "v_add_f32 v198, v198, v54\n\t" \
    "v_add_f32 v199, v199, v55\n\t" \
    "v_mfma_f32_16x16x32_bf16 v[0:3], v[164:167], v[20:23], v[0:3]\n\t" \
    "ds_read_b128 v[44:47], %[xp] offset:30720\n\t" \
    "s_waitcnt lgkmcnt(7)\n\t" \
    "v_add_f32 v200, v200, v56\n\t" \
    "v_mfma_f32_16x16x32_bf16 v[4:7], v[172:175], v[20:23], v[4:7]\n\t" \
    "ds_read_b128 v[48:51], %[xp] offset:31744\n\t" \
    "v_add_f32 v201, v201, v57\n\t" \
    "v_mfma_f32_16x16x32_bf16 v[8:11], v[164:167], v[24:27], v[8:11]\n\t" \
    "v_add_f32 v202, v202, v58\n\t" \
    "s_waitcnt lgkmcnt(6)\n\t" \
    "v_mfma_f32_16x16x32_bf16 v[12:15], v[172:175], v[24:27], v[12:15]\n\t" \
    "v_add_f32 v203, v203, v59\n\t" \
    "s_waitcnt lgkmcnt(5)\n\t" \
    "v_add_f32 v204, v204, v60\n\t" \
    "v_add_f32 v205, v205, v61\n\t" \
    "v_mfma_f32_16x16x32_bf16 v[0:3], v[168:171], v[28:31], v[0:3]\n\t" \
    "ds_read_b128 v[20:23], %[xp] offset:32768\n\t" \
    "v_add_f32 v206, v206, v62\n\t" \
    "v_mfma_f32_16x16x32_bf16 v[4:7], v[176:179], v[28:31], v[4:7]\n\t" \
    "ds_read_b128 v[24:27], %[xp] offset:33792\n\t" \
    "v_add_f32 v207, v207, v63\n\t" \
    "v_mfma_f32_16x16x32_bf16 v[8:11], v[168:171], v[32:35], v[8:11]\n\t" \
    "s_waitcnt lgkmcnt(4)\n\t" \
    "v_add_f32 v208, v208, v64\n\t" \
    "v_mfma_f32_16x16x32_bf16 v[12:15], v[176:179], v[32:35], v[12:15]\n\t" \
    "v_add_f32 v209, v209, v65\n\t" \
    "v_add_f32 v210, v210, v66\n\t" \
    "v_add_f32 v211, v211, v67\n\t" \
    "v_mfma_f32_16x16x32_bf16 v[0:3], v[180:183], v[36:39], v[0:3]\n\t" \
    "ds_read_b128 v[28:31], %[xp] offset:34816\n\t" \
    "v_cvt_pk_bf16_f32 v52, v196, v200\n\t" \
    "v_mfma_f32_16x16x32_bf16 v[4:7], v[188:191], v[36:39], v[4:7]\n\t" \
    "ds_read_b128 v[32:35], %[xp] offset:35840\n\t" \
    "v_cvt_pk_bf16_f32 v53, v197, v201\n\t" \
    "v_mfma_f32_16x16x32_bf16 v[8:11], v[180:183], v[40:43], v[8:11]\n\t" \
    "v_cvt_pk_bf16_f32 v54, v198, v202\n\t" \
    "s_waitcnt lgkmcnt(4)\n\t" \
    "v_mfma_f32_16x16x32_bf16 v[12:15], v[188:191], v[40:43], v[12:15]\n\t" \
    "v_cvt_pk_bf16_f32 v55, v199, v203\n\t" \
    "v_cvt_pk_bf16_f32 v56, v204, v208\n\t" \
    "v_cvt_pk_bf16_f32 v57, v205, v209\n\t" \
    "v_mfma_f32_16x16x32_bf16 v[0:3], v[184:187], v[44:47], v[0:3]\n\t" \
    "ds_read_b128 v[36:39], %[xp] offset:36864\n\t" \
    "v_cvt_pk_bf16_f32 v58, v206, v210\n\t" \
    "v_mfma_f32_16x16x32_bf16 v[4:7], v[192:195], v[44:47], v[4:7]\n\t" \
    "ds_read_b128 v[40:43], %[xp] offset:37888\n\t" \
    "v_cvt_pk_bf16_f32 v59, v207, v211\n\t" \
    "v_mfma_f32_16x16x32_bf16 v[8:11], v[184:187], v[48:51], v[8:11]\n\t" \
    "s_waitcnt lgkmcnt(4)\n\t" \
    "v_mfma_f32_16x16x32_bf16 v[12:15], v[192:195], v[48:51], v[12:15]\n\t" \
    "s_mov_b64 exec, %[em]\n\t" \
    "buffer_store_dwordx4 v[52:55], %[l16], %[rs], %[oo] offen sc1\n\t" \
    "buffer_store_dwordx4 v[56:59], %[l16], %[rs], %[b] offen sc1\n\t" \
    "s_mov_b64 exec, -1\n\t" \
    "s_add_u32 %[sx], %[sx], 65536\n\t" \
    "s_add_u32 %[t], %[t], 1\n\t" \
    "s_add_u32 %[oo], %[oo], 262144\n\t" \
    "s_waitcnt vmcnt(6)\n\t" \
    "s_cmp_gt_u32 %[t], " STR(SQUICK) " + 1\n\t" \
    "s_cbranch_scc1 B0_%=\n\t" \
    "s_waitcnt vmcnt(0)\n\t" \
    "B0_%=:\n\t" \
    "s_barrier\n\t" \
    "s_setprio 1\n\t" \
    "s_add_u32 m0, %[sd], 16384\n\t" \
    "s_add_u32 %[a], %[sx], 81920\n\t" \
    "v_mfma_f32_16x16x32_bf16 v[196:199], v[68:71], v[20:23], 0\n\t" \
    "ds_read_b128 v[44:47], %[xp] offset:38912\n\t" \
    "v_mfma_f32_16x16x32_bf16 v[200:203], v[76:79], v[20:23], 0\n\t" \
    "ds_read_b128 v[48:51], %[xp] offset:39936\n\t" \
    "v_mfma_f32_16x16x32_bf16 v[204:207], v[68:71], v[24:27], 0\n\t" \
    "s_waitcnt lgkmcnt(4)\n\t" \
    "v_mfma_f32_16x16x32_bf16 v[208:211], v[76:79], v[24:27], 0\n\t" \
    "v_mfma_f32_16x16x32_bf16 v[196:199], v[72:75], v[28:31], v[196:199]\n\t" \
    "ds_read_b128 v[20:23], %[xp] offset:40960\n\t" \
    "v_mfma_f32_16x16x32_bf16 v[200:203], v[80:83], v[28:31], v[200:203]\n\t" \
    "ds_read_b128 v[24:27], %[xp] offset:41984\n\t" \
    "v_mfma_f32_16x16x32_bf16 v[204:207], v[72:75], v[32:35], v[204:207]\n\t" \
    "s_waitcnt lgkmcnt(4)\n\t" \
    "v_mfma_f32_16x16x32_bf16 v[208:211], v[80:83], v[32:35], v[208:211]\n\t" \
    "v_mfma_f32_16x16x32_bf16 v[196:199], v[84:87], v[36:39], v[196:199]\n\t" \
    "ds_read_b128 v[28:31], %[xp] offset:43008\n\t" \
    "v_mfma_f32_16x16x32_bf16 v[200:203], v[92:95], v[36:39], v[200:203]\n\t" \
    "ds_read_b128 v[32:35], %[xp] offset:44032\n\t" \
    "v_mfma_f32_16x16x32_bf16 v[204:207], v[84:87], v[40:43], v[204:207]\n\t" \
    "s_waitcnt lgkmcnt(4)\n\t" \
    "v_mfma_f32_16x16x32_bf16 v[208:211], v[92:95], v[40:43], v[208:211]\n\t" \
    "v_mfma_f32_16x16x32_bf16 v[196:199], v[88:91], v[44:47], v[196:199]\n\t" \
    "ds_read_b128 v[36:39], %[xp] offset:45056\n\t" \
    "v_mfma_f32_16x16x32_bf16 v[200:203], v[96:99], v[44:47], v[200:203]\n\t" \
    "ds_read_b128 v[40:43], %[xp] offset:46080\n\t" \
    "v_mfma_f32_16x16x32_bf16 v[204:207], v[88:91], v[48:51], v[204:207]\n\t" \
    "s_waitcnt lgkmcnt(4)\n\t" \
    "v_mfma_f32_16x16x32_bf16 v[208:211], v[96:99], v[48:51], v[208:211]\n\t" \
    "v_mfma_f32_16x16x32_bf16 v[196:199], v[100:103], v[20:23], v[196:199]\n\t" \
    "ds_read_b128 v[44:47], %[xp] offset:47104\n\t" \
    "v_mfma_f32_16x16x32_bf16 v[200:203], v[108:111], v[20:23], v[200:203]\n\t" \
    "ds_read_b128 v[48:51], %[xp] offset:48128\n\t" \
    "v_mfma_f32_16x16x32_bf16 v[204:207], v[100:103], v[24:27], v[204:207]\n\t" \
    "s_waitcnt lgkmcnt(4)\n\t" \
    "v_mfma_f32_16x16x32_bf16 v[208:211], v[108:111], v[24:27], v[208:211]\n\t" \
    "buffer_load_dwordx4 %[l16], %[rs], %[a] offen offset:0 sc1 lds\n\t" \
    "v_mfma_f32_16x16x32_bf16 v[196:199], v[104:107], v[28:31], v[196:199]\n\t" \
    "ds_read_b128 v[20:23], %[xp] offset:49152\n\t" \
    "v_mfma_f32_16x16x32_bf16 v[200:203], v[112:115], v[28:31], v[200:203]\n\t" \
    "ds_read_b128 v[24:27], %[xp] offset:50176\n\t" \
    "v_mfma_f32_16x16x32_bf16 v[204:207], v[104:107], v[32:35], v[204:207]\n\t" \
    "s_waitcnt lgkmcnt(4)\n\t" \
    "v_mfma_f32_16x16x32_bf16 v[208:211], v[112:115], v[32:35], v[208:211]\n\t" \
    "buffer_load_dwordx4 %[l16], %[rs], %[a] offen offset:1024 sc1 lds\n\t" \
    "v_mfma_f32_16x16x32_bf16 v[196:199], v[116:119], v[36:39], v[196:199]\n\t" \
    "ds_read_b128 v[28:31], %[xp] offset:51200\n\t" \
    "v_mfma_f32_16x16x32_bf16 v[200:203], v[124:127], v[36:39], v[200:203]\n\t" \
    "ds_read_b128 v[32:35], %[xp] offset:52224\n\t" \
    "v_mfma_f32_16x16x32_bf16 v[204:207], v[116:119], v[40:43], v[204:207]\n\t" \
    "s_waitcnt lgkmcnt(4)\n\t" \
    "v_mfma_f32_16x16x32_bf16 v[208:211], v[124:127], v[40:43], v[208:211]\n\t" \
    "buffer_load_dwordx4 %[l16], %[rs], %[a] offen offset:2048 sc1 lds\n\t" \
    "v_mfma_f32_16x16x32_bf16 v[196:199], v[120:123], v[44:47], v[196:199]\n\t" \
    "ds_read_b128 v[36:39], %[xp] offset:53248\n\t" \
    "v_mfma_f32_16x16x32_bf16 v[200:203], v[128:131], v[44:47], v[200:203]\n\t" \
    "ds_read_b128 v[40:43], %[xp] offset:54272\n\t" \
    "v_mfma_f32_16x16x32_bf16 v[204:207], v[120:123], v[48:51], v[204:207]\n\t" \
    "s_waitcnt lgkmcnt(4)\n\t" \
    "v_mfma_f32_16x16x32_bf16 v[208:211], v[128:131], v[48:51], v[208:211]\n\t" \
    "buffer_load_dwordx4 %[l16], %[rs], %[a] offen offset:3072 sc1 lds\n\t" \
    "s_waitcnt vmcnt(4)\n\t" \
    "s_barrier\n\t" \
    "s_setprio 1\n\t" \
    "s_add_u32 m0, %[sd], 32768\n\t" \
    "s_add_u32 %[a], %[sx], 131072\n\t" \
    "s_add_u32 %[b], %[oo], 0x1000\n\t" \
    "s_cmp_lg_u32 %[t], 1\n\t" \
    "s_cselect_b64 %[em], -1, 0\n\t" \
    "v_mfma_f32_16x16x32_bf16 v[196:199], v[132:135], v[20:23], v[196:199]\n\t" \
    "ds_read_b128 v[44:47], %[xp] offset:55296\n\t" \
    "v_mfma_f32_16x16x32_bf16 v[200:203], v[140:143], v[20:23], v[200:203]\n\t" \
    "ds_read_b128 v[48:51], %[xp] offset:56320\n\t" \
    "v_mfma_f32_16x16x32_bf16 v[204:207], v[132:135], v[24:27], v[204:207]\n\t" \
    "ds_read_b128 v[52:55], %[pa] offset:0\n\t" \
    "s_waitcnt lgkmcnt(5)\n\t" \
    "v_mfma_f32_16x16x32_bf16 v[208:211], v[140:143], v[24:27], v[208:211]\n\t" \
    "buffer_load_dwordx4 %[l16], %[rs], %[a] offen offset:0 sc1 lds\n\t" \
    "v_mfma_f32_16x16x32_bf16 v[196:199], v[136:139], v[28:31], v[196:199]\n\t" \
    "ds_read_b128 v[20:23], %[xp] offset:57344\n\t" \
    "v_mfma_f32_16x16x32_bf16 v[200:203], v[144:147], v[28:31], v[200:203]\n\t" \
    "ds_read_b128 v[24:27], %[xp] offset:58368\n\t" \
    "v_mfma_f32_16x16x32_bf16 v[204:207], v[136:139], v[32:35], v[204:207]\n\t" \
    "ds_read_b128 v[56:59], %[pa] offset:1024\n\t" \
    "s_waitcnt lgkmcnt(6)\n\t" \
    "v_mfma_f32_16x16x32_bf16 v[208:211], v[144:147], v[32:35], v[208:211]\n\t" \
    "buffer_load_dwordx4 %[l16], %[rs], %[a] offen offset:1024 sc1 lds\n\t" \
    "v_mfma_f32_16x16x32_bf16 v[196:199], v[148:151], v[36:39], v[196:199]\n\t" \
    "ds_read_b128 v[28:31], %[xp] offset:59392\n\t" \
    "v_mfma_f32_16x16x32_bf16 v[200:203], v[156:159], v[36:39], v[200:203]\n\t" \
    "ds_read_b128 v[32:35], %[xp] offset:60416\n\t" \
    "v_mfma_f32_16x16x32_bf16 v[204:207], v[148:151], v[40:43], v[204:207]\n\t" \
    "ds_read_b128 v[60:63], %[pa] offset:2048\n\t" \
    "s_waitcnt lgkmcnt(7)\n\t" \
    "v_mfma_f32_16x16x32_bf16 v[208:211], v[156:159], v[40:43], v[208:211]\n\t" \
    "buffer_load_dwordx4 %[l16], %[rs], %[a] offen offset:2048 sc1 lds\n\t" \
    "v_mfma_f32_16x16x32_bf16 v[196:199], v[152:155], v[44:47], v[196:199]\n\t" \
    "ds_read_b128 v[36:39], %[xp] offset:61440\n\t" \
    "s_waitcnt lgkmcnt(7)\n\t" \
    "v_add_f32 v0, v0, v52\n\t" \
    "v_mfma_f32_16x16x32_bf16 v[200:203], v[160:163], v[44:47], v[200:203]\n\t" \
    "ds_read_b128 v[40:43], %[xp] offset:62464\n\t" \
    "v_add_f32 v1, v1, v53\n\t" \
    "v_mfma_f32_16x16x32_bf16 v[204:207], v[152:155], v[48:51], v[204:207]\n\t" \
    "ds_read_b128 v[64:67], %[pa] offset:3072\n\t" \
    "s_waitcnt lgkmcnt(7)\n\t" \
    "v_mfma_f32_16x16x32_bf16 v[208:211], v[160:163], v[48:51], v[208:211]\n\t" \
    "buffer_load_dwordx4 %[l16], %[rs], %[a] offen offset:3072 sc1 lds\n\t" \
    "v_add_f32 v2, v2, v54\n\t" \
    "v_add_f32 v3, v3, v55\n\t" \
    "v_mfma_f32_16x16x32_bf16 v[196:199], v[164:167], v[20:23], v[196:199]\n\t" \
    "ds_read_b128 v[44:47], %[xp] offset:63488\n\t" \
    "s_waitcnt lgkmcnt(7)\n\t" \
    "v_add_f32 v4, v4, v56\n\t" \
    "v_mfma_f32_16x16x32_bf16 v[200:203], v[172:175], v[20:23], v[200:203]\n\t" \
    "ds_read_b128 v[48:51], %[xp] offset:64512\n\t" \
    "v_add_f32 v5, v5, v57\n\t" \
    "v_mfma_f32_16x16x32_bf16 v[204:207], v[164:167], v[24:27], v[204:207]\n\t" \
    "v_add_f32 v6, v6, v58\n\t" \
    "s_waitcnt lgkmcnt(6)\n\t" \
    "v_mfma_f32_16x16x32_bf16 v[208:211], v[172:175], v[24:27], v[208:211]\n\t" \
    "v_add_f32 v7, v7, v59\n\t" \
    "s_waitcnt lgkmcnt(5)\n\t" \
    "v_add_f32 v8, v8, v60\n\t" \
    "v_add_f32 v9, v9, v61\n\t" \
    "v_mfma_f32_16x16x32_bf16 v[196:199], v[168:171], v[28:31], v[196:199]\n\t" \
    "ds_read_b128 v[20:23], %[xp] offset:0\n\t" \
    "v_add_f32 v10, v10, v62\n\t" \
    "v_mfma_f32_16x16x32_bf16 v[200:203], v[176:179], v[28:31], v[200:203]\n\t" \
    "ds_read_b128 v[24:27], %[xp] offset:1024\n\t" \
    "v_add_f32 v11, v11, v63\n\t" \
    "v_mfma_f32_16x16x32_bf16 v[204:207], v[168:171], v[32:35], v[204:207]\n\t" \
    "s_waitcnt lgkmcnt(4)\n\t" \
    "v_add_f32 v12, v12, v64\n\t" \
    "v_mfma_f32_16x16x32_bf16 v[208:211], v[176:179], v[32:35], v[208:211]\n\t" \
    "v_add_f32 v13, v13, v65\n\t" \
    "v_add_f32 v14, v14, v66\n\t" \
    "v_add_f32 v15, v15, v67\n\t" \
    "v_mfma_f32_16x16x32_bf16 v[196:199], v[180:183], v[36:39], v[196:199]\n\t" \
    "ds_read_b128 v[28:31], %[xp] offset:2048\n\t" \
    "v_cvt_pk_bf16_f32 v52, v0, v4\n\t" \
    "v_mfma_f32_16x16x32_bf16 v[200:203], v[188:191], v[36:39], v[200:203]\n\t" \
    "ds_read_b128 v[32:35], %[xp] offset:3072\n\t" \
    "v_cvt_pk_bf16_f32 v53, v1, v5\n\t" \
    "v_mfma_f32_16x16x32_bf16 v[204:207], v[180:183], v[40:43], v[204:207]\n\t" \
    "v_cvt_pk_bf16_f32 v54, v2, v6\n\t" \
    "s_waitcnt lgkmcnt(4)\n\t" \
    "v_mfma_f32_16x16x32_bf16 v[208:211], v[188:191], v[40:43], v[208:211]\n\t" \
    "v_cvt_pk_bf16_f32 v55, v3, v7\n\t" \
    "v_cvt_pk_bf16_f32 v56, v8, v12\n\t" \
    "v_cvt_pk_bf16_f32 v57, v9, v13\n\t" \
    "v_mfma_f32_16x16x32_bf16 v[196:199], v[184:187], v[44:47], v[196:199]\n\t" \
    "ds_read_b128 v[36:39], %[xp] offset:4096\n\t" \
    "v_cvt_pk_bf16_f32 v58, v10, v14\n\t" \
    "v_mfma_f32_16x16x32_bf16 v[200:203], v[192:195], v[44:47], v[200:203]\n\t" \
    "ds_read_b128 v[40:43], %[xp] offset:5120\n\t" \
    "v_cvt_pk_bf16_f32 v59, v11, v15\n\t" \
    "v_mfma_f32_16x16x32_bf16 v[204:207], v[184:187], v[48:51], v[204:207]\n\t" \
    "s_waitcnt lgkmcnt(4)\n\t" \
    "v_mfma_f32_16x16x32_bf16 v[208:211], v[192:195], v[48:51], v[208:211]\n\t" \
    "s_mov_b64 exec, %[em]\n\t" \
    "buffer_store_dwordx4 v[52:55], %[l16], %[rs], %[oo] offen sc1\n\t" \
    "buffer_store_dwordx4 v[56:59], %[l16], %[rs], %[b] offen sc1\n\t" \
    "s_mov_b64 exec, -1\n\t" \
    "s_add_u32 %[sx], %[sx], 65536\n\t" \
    "s_add_u32 %[t], %[t], 1\n\t" \
    "s_add_u32 %[oo], %[oo], 262144\n\t" \
    "s_waitcnt vmcnt(6)\n\t" \
    "s_cmp_gt_u32 %[t], " STR(SQUICK) " + 1\n\t" \
    "s_cbranch_scc1 B1_%=\n\t" \
    "s_waitcnt vmcnt(0)\n\t" \
    "B1_%=:\n\t" \
    "s_barrier\n\t" \
    "s_cmp_lt_u32 %[t], %[te]\n\t" \
    "s_cbranch_scc1 L1_%=\n\t" \
    "s_cmp_eq_u32 %[fin], %[te]\n\t" \
    "s_cbranch_scc0 E_%=\n\t" \
    "s_nop 4\n\t" \
    "s_barrier\n\t" \
    "ds_read_b128 v[52:55], %[pa] offset:0\n\t" \
    "ds_read_b128 v[56:59], %[pa] offset:1024\n\t" \
    "ds_read_b128 v[60:63], %[pa] offset:2048\n\t" \
    "ds_read_b128 v[64:67], %[pa] offset:3072\n\t" \
    "s_add_u32 %[b], %[oo], 0x1000\n\t" \
    "s_waitcnt lgkmcnt(0)\n\t" \
    "v_add_f32 v196, v196, v52\n\t" \
    "v_add_f32 v197, v197, v53\n\t" \
    "v_add_f32 v198, v198, v54\n\t" \
    "v_add_f32 v199, v199, v55\n\t" \
    "v_add_f32 v200, v200, v56\n\t" \
    "v_add_f32 v201, v201, v57\n\t" \
    "v_add_f32 v202, v202, v58\n\t" \
    "v_add_f32 v203, v203, v59\n\t" \
    "v_add_f32 v204, v204, v60\n\t" \
    "v_add_f32 v205, v205, v61\n\t" \
    "v_add_f32 v206, v206, v62\n\t" \
    "v_add_f32 v207, v207, v63\n\t" \
    "v_add_f32 v208, v208, v64\n\t" \
    "v_add_f32 v209, v209, v65\n\t" \
    "v_add_f32 v210, v210, v66\n\t" \
    "v_add_f32 v211, v211, v67\n\t" \
    "v_cvt_pk_bf16_f32 v52, v196, v200\n\t" \
    "v_cvt_pk_bf16_f32 v53, v197, v201\n\t" \
    "v_cvt_pk_bf16_f32 v54, v198, v202\n\t" \
    "v_cvt_pk_bf16_f32 v55, v199, v203\n\t" \
    "v_cvt_pk_bf16_f32 v56, v204, v208\n\t" \
    "v_cvt_pk_bf16_f32 v57, v205, v209\n\t" \
    "v_cvt_pk_bf16_f32 v58, v206, v210\n\t" \
    "v_cvt_pk_bf16_f32 v59, v207, v211\n\t" \
    "buffer_store_dwordx4 v[52:55], %[l16], %[rs], %[oo] offen sc1\n\t" \
    "buffer_store_dwordx4 v[56:59], %[l16], %[rs], %[b] offen sc1\n\t" \
    "E_%=:\n\t" \
    "s_setprio 0\n\t" \
    "s_waitcnt lgkmcnt(0)\n\t" \
    "s_nop 7\n\t" \
    : [t] "+s"(t), [sx] "+s"(sx), [oo] "+s"(oo), [a] "=&s"(tmp), [b] "=&s"(tmp2), [em] "=&s"(em), "+{v[20:27]}"(xf[0]), "+{v[28:35]}"(xf[1]), "+{v[36:43]}"(xf[2]), "+{v[196:203]}"(ac[0]), "+{v[204:211]}"(ac[1]) \
    : [te] "s"(TE_), [fin] "s"(FIN_), [kh] "s"(kh), [sd] "s"(sd), [rs] "s"(rs), [l16] "v"(l16), [xp] "v"(xp), [pa] "v"(pa), [fm] "s"(fm), [pm] "s"(pm), [fo] "s"(fo), [po] "s"(po), [pd] "s"(pd), [tag] "s"(P.tag), "{v[68:75]}"(w[0][0]), "{v[76:83]}"(w[0][1]), "{v[84:91]}"(w[1][0]), "{v[92:99]}"(w[1][1]), "{v[100:107]}"(w[2][0]), "{v[108:115]}"(w[2][1]), "{v[116:123]}"(w[3][0]), "{v[124:131]}"(w[3][1]), "{v[132:139]}"(w[4][0]), "{v[140:147]}"(w[4][1]), "{v[148:155]}"(w[5][0]), "{v[156:163]}"(w[5][1]), "{v[164:171]}"(w[6][0]), "{v[172:179]}"(w[6][1]), "{v[180:187]}"(w[7][0]), "{v[188:195]}"(w[7][1]) \
    : "memory", "scc", "v0", "v1", "v2", "v3", "v4", "v5", "v6", "v7", "v8", "v9", "v10", "v11", "v12", "v13", "v14", "v15", "v44", "v45", "v46", "v47", "v48", "v49", "v50", "v51", "v52", "v53", "v54", "v55", "v56", "v57", "v58", "v59", "v60", "v61", "v62", "v63", "v64", "v65", "v66", "v67")

#define S_LOOP_V(TE_, FIN_) \
  asm volatile( \
    "s_cmp_eq_u32 %[kh], 0\n\t" \
    "s_cbranch_scc0 L1_%=\n\t" \
    "L0_%=:\n\t" \
    "s_setprio 0\n\t" \
    "s_nop 3\n\t" \
    "ds_write_b128 %[pa], v[196:199] offset:0\n\t" \
    "ds_write_b128 %[pa], v[200:203] offset:1024\n\t" \
    "ds_write_b128 %[pa], v[204:207] offset:2048\n\t" \
    "ds_write_b128 %[pa], v[208:211] offset:3072\n\t" \
    "s_sub_u32 %[a], %[t], 1\n\t" \
    "v_mov_b32 v53, %[tag]\n\t" \
    "v_mov_b32 v52, %[a]\n\t" \
    "s_sub_u32 %[b], %[t], 2\n\t" \
    "s_cmp_lt_u32 %[b], " STR(SQUICK) "\n\t" \
    "s_cselect_b64 exec, %[fm], 0\n\t" \
    "buffer_store_dwordx2 v[52:53], %[l16], %[rs], %[fo] offen sc1\n\t" \
    "s_mov_b64 exec, -1\n\t" \
    "s_add_u32 m0, %[sd], 49152\n\t" \
    "s_add_u32 %[a], %[sx], 81920\n\t" \
    "s_waitcnt lgkmcnt(0)\n\t" \
    "buffer_load_dwordx4 %[l16], %[rs], %[a] offen offset:0 sc1 lds\n\t" \
    "buffer_load_dwordx4 %[l16], %[rs], %[a] offen offset:1024 sc1 lds\n\t" \
    "buffer_load_dwordx4 %[l16], %[rs], %[a] offen offset:2048 sc1 lds\n\t" \
    "buffer_load_dwordx4 %[l16], %[rs], %[a] offen offset:3072 sc1 lds\n\t" \
    "v_mfma_f32_16x16x32_bf16 v[0:3], v[20:23], v[68:71], 0\n\t" \
    "ds_read_b128 v[44:47], %[xp] offset:6144\n\t" \
    "v_mfma_f32_16x16x32_bf16 v[4:7], v[20:23], v[76:79], 0\n\t" \
    "ds_read_b128 v[48:51], %[xp] offset:7168\n\t" \
    "v_mfma_f32_16x16x32_bf16 v[8:11], v[24:27], v[68:71], 0\n\t" \
    "v_mfma_f32_16x16x32_bf16 v[12:15], v[24:27], v[76:79], 0\n\t" \
    "v_mfma_f32_16x16x32_bf16 v[0:3], v[28:31], v[72:75], v[0:3]\n\t" \
    "ds_read_b128 v[20:23], %[xp] offset:8192\n\t" \
    "v_mfma_f32_16x16x32_bf16 v[4:7], v[28:31], v[80:83], v[4:7]\n\t" \
    "ds_read_b128 v[24:27], %[xp] offset:9216\n\t" \
    "v_mfma_f32_16x16x32_bf16 v[8:11], v[32:35], v[72:75], v[8:11]\n\t" \
    "v_mfma_f32_16x16x32_bf16 v[12:15], v[32:35], v[80:83], v[12:15]\n\t" \
    "v_mfma_f32_16x16x32_bf16 v[0:3], v[36:39], v[84:87], v[0:3]\n\t" \
    "ds_read_b128 v[28:31], %[xp] offset:10240\n\t" \
    "v_mfma_f32_16x16x32_bf16 v[4:7], v[36:39], v[92:95], v[4:7]\n\t" \
    "ds_read_b128 v[32:35], %[xp] offset:11264\n\t" \
    "v_mfma_f32_16x16x32_bf16 v[8:11], v[40:43], v[84:87], v[8:11]\n\t" \
    "s_waitcnt lgkmcnt(4)\n\t" \
    "v_mfma_f32_16x16x32_bf16 v[12:15], v[40:43], v[92:95], v[12:15]\n\t" \
    "v_mfma_f32_16x16x32_bf16 v[0:3], v[44:47], v[88:91], v[0:3]\n\t" \
    "ds_read_b128 v[36:39], %[xp] offset:12288\n\t" \
    "v_mfma_f32_16x16x32_bf16 v[4:7], v[44:47], v[96:99], v[4:7]\n\t" \
    "ds_read_b128 v[40:43], %[xp] offset:13312\n\t" \
    "v_mfma_f32_16x16x32_bf16 v[8:11], v[48:51], v[88:91], v[8:11]\n\t" \
    "s_waitcnt lgkmcnt(4)\n\t" \
    "v_mfma_f32_16x16x32_bf16 v[12:15], v[48:51], v[96:99], v[12:15]\n\t" \
    "v_mfma_f32_16x16x32_bf16 v[0:3], v[20:23], v[100:103], v[0:3]\n\t" \
    "ds_read_b128 v[44:47], %[xp] offset:14336\n\t" \
    "v_mfma_f32_16x16x32_bf16 v[4:7], v[20:23], v[108:111], v[4:7]\n\t" \
    "ds_read_b128 v[48:51], %[xp] offset:15360\n\t" \
    "v_mfma_f32_16x16x32_bf16 v[8:11], v[24:27], v[100:103], v[8:11]\n\t" \
    "s_waitcnt lgkmcnt(4)\n\t" \
    "v_mfma_f32_16x16x32_bf16 v[12:15], v[24:27], v[108:111], v[12:15]\n\t" \
    "v_mfma_f32_16x16x32_bf16 v[0:3], v[28:31], v[104:107], v[0:3]\n\t" \
    "ds_read_b128 v[20:23], %[xp] offset:16384\n\t" \
    "v_mfma_f32_16x16x32_bf16 v[4:7], v[28:31], v[112:115], v[4:7]\n\t" \
    "ds_read_b128 v[24:27], %[xp] offset:17408\n\t" \
    "v_mfma_f32_16x16x32_bf16 v[8:11], v[32:35], v[104:107], v[8:11]\n\t" \
    "s_waitcnt lgkmcnt(4)\n\t" \
    "v_mfma_f32_16x16x32_bf16 v[12:15], v[32:35], v[112:115], v[12:15]\n\t" \
    "v_mfma_f32_16x16x32_bf16 v[0:3], v[36:39], v[116:119], v[0:3]\n\t" \
    "ds_read_b128 v[28:31], %[xp] offset:18432\n\t" \
    "v_mfma_f32_16x16x32_bf16 v[4:7], v[36:39], v[124:127], v[4:7]\n\t" \
    "ds_read_b128 v[32:35], %[xp] offset:19456\n\t" \
    "v_mfma_f32_16x16x32_bf16 v[8:11], v[40:43], v[116:119], v[8:11]\n\t" \
    "s_waitcnt lgkmcnt(4)\n\t" \
    "v_mfma_f32_16x16x32_bf16 v[12:15], v[40:43], v[124:127], v[12:15]\n\t" \
    "v_mfma_f32_16x16x32_bf16 v[0:3], v[44:47], v[120:123], v[0:3]\n\t" \
    "ds_read_b128 v[36:39], %[xp] offset:20480\n\t" \
    "v_mfma_f32_16x16x32_bf16 v[4:7], v[44:47], v[128:131], v[4:7]\n\t" \
    "ds_read_b128 v[40:43], %[xp] offset:21504\n\t" \
    "v_mfma_f32_16x16x32_bf16 v[8:11], v[48:51], v[120:123], v[8:11]\n\t" \
    "s_waitcnt lgkmcnt(4)\n\t" \
    "v_mfma_f32_16x16x32_bf16 v[12:15], v[48:51], v[128:131], v[12:15]\n\t" \
    "s_waitcnt vmcnt(4)\n\t" \
    "s_barrier\n\t" \
    "s_setprio 0\n\t" \
    "s_sub_u32 %[a], %[t], 1\n\t" \
    "v_mov_b32 v53, %[tag]\n\t" \
    "v_mov_b32 v52, %[a]\n\t" \
    "s_cmp_gt_u32 %[t], 1\n\t" \
    "s_cselect_b64 exec, %[fm], 0\n\t" \
    "buffer_store_dwordx2 v[52:53], %[l16], %[rs], %[fo] offen sc1\n\t" \
    "s_mov_b64 exec, %[pm]\n\t" \
    "s_mov_b32 m0, %[pd]\n\t" \
    "s_add_u32 %[a], %[sx], %[po]\n\t" \
    "buffer_load_dwordx4 %[l16], %[rs], %[a] offen sc1 lds\n\t" \
    "s_mov_b64 exec, -1\n\t" \
    "s_add_u32 m0, %[sd], 0\n\t" \
    "s_add_u32 %[a], %[sx], 131072\n\t" \
    "buffer_load_dwordx4 %[l16], %[rs], %[a] offen offset:0 sc1 lds\n\t" \
    "buffer_load_dwordx4 %[l16], %[rs], %[a] offen offset:1024 sc1 lds\n\t" \
    "buffer_load_dwordx4 %[l16], %[rs], %[a] offen offset:2048 sc1 lds\n\t" \
    "buffer_load_dwordx4 %[l16], %[rs], %[a] offen offset:3072 sc1 lds\n\t" \
    "v_mfma_f32_16x16x32_bf16 v[0:3], v[20:23], v[132:135], v[0:3]\n\t" \
    "ds_read_b128 v[44:47], %[xp] offset:22528\n\t" \
    "v_mfma_f32_16x16x32_bf16 v[4:7], v[20:23], v[140:143], v[4:7]\n\t" \
    "ds_read_b128 v[48:51], %[xp] offset:23552\n\t" \
    "v_mfma_f32_16x16x32_bf16 v[8:11], v[24:27], v[132:135], v[8:11]\n\t" \
    "s_waitcnt lgkmcnt(4)\n\t" \
    "v_mfma_f32_16x16x32_bf16 v[12:15], v[24:27], v[140:143], v[12:15]\n\t" \
    "v_mfma_f32_16x16x32_bf16 v[0:3], v[28:31], v[136:139], v[0:3]\n\t" \
    "ds_read_b128 v[20:23], %[xp] offset:24576\n\t" \
    "v_mfma_f32_16x16x32_bf16 v[4:7], v[28:31], v[144:147], v[4:7]\n\t" \
    "ds_read_b128 v[24:27], %[xp] offset:25600\n\t" \
    "v_mfma_f32_16x16x32_bf16 v[8:11], v[32:35], v[136:139], v[8:11]\n\t" \
    "s_waitcnt lgkmcnt(4)\n\t" \
    "v_mfma_f32_16x16x32_bf16 v[12:15], v[32:35], v[144:147], v[12:15]\n\t" \
    "v_mfma_f32_16x16x32_bf16 v[0:3], v[36:39], v[148:151], v[0:3]\n\t" \
    "ds_read_b128 v[28:31], %[xp] offset:26624\n\t" \
    "v_mfma_f32_16x16x32_bf16 v[4:7], v[36:39], v[156:159], v[4:7]\n\t" \
    "ds_read_b128 v[32:35], %[xp] offset:27648\n\t" \
    "v_mfma_f32_16x16x32_bf16 v[8:11], v[40:43], v[148:151], v[8:11]\n\t" \
    "s_waitcnt lgkmcnt(4)\n\t" \
    "v_mfma_f32_16x16x32_bf16 v[12:15], v[40:43], v[156:159], v[12:15]\n\t" \
    "v_mfma_f32_16x16x32_bf16 v[0:3], v[44:47], v[152:155], v[0:3]\n\t" \
    "ds_read_b128 v[36:39], %[xp] offset:28672\n\t" \
    "v_mfma_f32_16x16x32_bf16 v[4:7], v[44:47], v[160:163], v[4:7]\n\t" \
    "ds_read_b128 v[40:43], %[xp] offset:29696\n\t" \
    "v_mfma_f32_16x16x32_bf16 v[8:11], v[48:51], v[152:155], v[8:11]\n\t" \
    "s_waitcnt lgkmcnt(4)\n\t" \
    "v_mfma_f32_16x16x32_bf16 v[12:15], v[48:51], v[160:163], v[12:15]\n\t" \
    "v_mfma_f32_16x16x32_bf16 v[0:3], v[20:23], v[164:167], v[0:3]\n\t" \
    "ds_read_b128 v[44:47], %[xp] offset:30720\n\t" \
    "v_mfma_f32_16x16x32_bf16 v[4:7], v[20:23], v[172:175], v[4:7]\n\t" \
    "ds_read_b128 v[48:51], %[xp] offset:31744\n\t" \
    "v_mfma_f32_16x16x32_bf16 v[8:11], v[24:27], v[164:167], v[8:11]\n\t" \
    "s_waitcnt lgkmcnt(4)\n\t" \
    "v_mfma_f32_16x16x32_bf16 v[12:15], v[24:27], v[172:175], v[12:15]\n\t" \
    "v_mfma_f32_16x16x32_bf16 v[0:3], v[28:31], v[168:171], v[0:3]\n\t" \
    "ds_read_b128 v[20:23], %[xp] offset:32768\n\t" \
    "v_mfma_f32_16x16x32_bf16 v[4:7], v[28:31], v[176:179], v[4:7]\n\t" \
    "ds_read_b128 v[24:27], %[xp] offset:33792\n\t" \
    "v_mfma_f32_16x16x32_bf16 v[8:11], v[32:35], v[168:171], v[8:11]\n\t" \
    "s_waitcnt lgkmcnt(4)\n\t" \
    "v_mfma_f32_16x16x32_bf16 v[12:15], v[32:35], v[176:179], v[12:15]\n\t" \
    "v_mfma_f32_16x16x32_bf16 v[0:3], v[36:39], v[180:183], v[0:3]\n\t" \
    "ds_read_b128 v[28:31], %[xp] offset:34816\n\t" \
    "v_mfma_f32_16x16x32_bf16 v[4:7], v[36:39], v[188:191], v[4:7]\n\t" \
    "ds_read_b128 v[32:35], %[xp] offset:35840\n\t" \
    "v_mfma_f32_16x16x32_bf16 v[8:11], v[40:43], v[180:183], v[8:11]\n\t" \
    "s_waitcnt lgkmcnt(4)\n\t" \
    "v_mfma_f32_16x16x32_bf16 v[12:15], v[40:43], v[188:191], v[12:15]\n\t" \
    "v_mfma_f32_16x16x32_bf16 v[0:3], v[44:47], v[184:187], v[0:3]\n\t" \
    "ds_read_b128 v[36:39], %[xp] offset:36864\n\t" \
    "v_mfma_f32_16x16x32_bf16 v[4:7], v[44:47], v[192:195], v[4:7]\n\t" \
    "ds_read_b128 v[40:43], %[xp] offset:37888\n\t" \
    "v_mfma_f32_16x16x32_bf16 v[8:11], v[48:51], v[184:187], v[8:11]\n\t" \
    "s_waitcnt lgkmcnt(4)\n\t" \
    "v_mfma_f32_16x16x32_bf16 v[12:15], v[48:51], v[192:195], v[12:15]\n\t" \
    "s_add_u32 %[sx], %[sx], 65536\n\t" \
    "s_add_u32 %[t], %[t], 1\n\t" \
    "s_waitcnt vmcnt(4)\n\t" \
    "s_barrier\n\t" \
    "s_setprio 0\n\t" \
    "s_nop 3\n\t" \
    "ds_write_b128 %[pa], v[0:3] offset:0\n\t" \
    "ds_write_b128 %[pa], v[4:7] offset:1024\n\t" \
    "ds_write_b128 %[pa], v[8:11] offset:2048\n\t" \
    "ds_write_b128 %[pa], v[12:15] offset:3072\n\t" \
    "s_sub_u32 %[a], %[t], 1\n\t" \
    "v_mov_b32 v53, %[tag]\n\t" \
    "v_mov_b32 v52, %[a]\n\t" \
    "s_sub_u32 %[b], %[t], 2\n\t" \
    "s_cmp_lt_u32 %[b], " STR(SQUICK) "\n\t" \
    "s_cselect_b64 exec, %[fm], 0\n\t" \
    "buffer_store_dwordx2 v[52:53], %[l16], %[rs], %[fo] offen sc1\n\t" \
    "s_mov_b64 exec, -1\n\t" \
    "s_add_u32 m0, %[sd], 16384\n\t" \
    "s_add_u32 %[a], %[sx], 81920\n\t" \
    "s_waitcnt lgkmcnt(0)\n\t" \
    "buffer_load_dwordx4 %[l16], %[rs], %[a] offen offset:0 sc1 lds\n\t" \
    "buffer_load_dwordx4 %[l16], %[rs], %[a] offen offset:1024 sc1 lds\n\t" \
    "buffer_load_dwordx4 %[l16], %[rs], %[a] offen offset:2048 sc1 lds\n\t" \
    "buffer_load_dwordx4 %[l16], %[rs], %[a] offen offset:3072 sc1 lds\n\t" \
    "v_mfma_f32_16x16x32_bf16 v[196:199], v[20:23], v[68:71], 0\n\t" \
    "ds_read_b128 v[44:47], %[xp] offset:38912\n\t" \
    "v_mfma_f32_16x16x32_bf16 v[200:203], v[20:23], v[76:79], 0\n\t" \
    "ds_read_b128 v[48:51], %[xp] offset:39936\n\t" \
    "v_mfma_f32_16x16x32_bf16 v[204:207], v[24:27], v[68:71], 0\n\t" \
    "v_mfma_f32_16x16x32_bf16 v[208:211], v[24:27], v[76:79], 0\n\t" \
    "v_mfma_f32_16x16x32_bf16 v[196:199], v[28:31], v[72:75], v[196:199]\n\t" \
    "ds_read_b128 v[20:23], %[xp] offset:40960\n\t" \
    "v_mfma_f32_16x16x32_bf16 v[200:203], v[28:31], v[80:83], v[200:203]\n\t" \
    "ds_read_b128 v[24:27], %[xp] offset:41984\n\t" \
    "v_mfma_f32_16x16x32_bf16 v[204:207], v[32:35], v[72:75], v[204:207]\n\t" \
    "v_mfma_f32_16x16x32_bf16 v[208:211], v[32:35], v[80:83], v[208:211]\n\t" \
    "v_mfma_f32_16x16x32_bf16 v[196:199], v[36:39], v[84:87], v[196:199]\n\t" \
    "ds_read_b128 v[28:31], %[xp] offset:43008\n\t" \
    "v_mfma_f32_16x16x32_bf16 v[200:203], v[36:39], v[92:95], v[200:203]\n\t" \
    "ds_read_b128 v[32:35], %[xp] offset:44032\n\t" \
    "v_mfma_f32_16x16x32_bf16 v[204:207], v[40:43], v[84:87], v[204:207]\n\t" \
    "s_waitcnt lgkmcnt(4)\n\t" \
    "v_mfma_f32_16x16x32_bf16 v[208:211], v[40:43], v[92:95], v[208:211]\n\t" \
    "v_mfma_f32_16x16x32_bf16 v[196:199], v[44:47], v[88:91], v[196:199]\n\t" \
    "ds_read_b128 v[36:39], %[xp] offset:45056\n\t" \
    "v_mfma_f32_16x16x32_bf16 v[200:203], v[44:47], v[96:99], v[200:203]\n\t" \
    "ds_read_b128 v[40:43], %[xp] offset:46080\n\t" \
    "v_mfma_f32_16x16x32_bf16 v[204:207], v[48:51], v[88:91], v[204:207]\n\t" \
    "s_waitcnt lgkmcnt(4)\n\t" \
    "v_mfma_f32_16x16x32_bf16 v[208:211], v[48:51], v[96:99], v[208:211]\n\t" \
    "v_mfma_f32_16x16x32_bf16 v[196:199], v[20:23], v[100:103], v[196:199]\n\t" \
    "ds_read_b128 v[44:47], %[xp] offset:47104\n\t" \
    "v_mfma_f32_16x16x32_bf16 v[200:203], v[20:23], v[108:111], v[200:203]\n\t" \
    "ds_read_b128 v[48:51], %[xp] offset:48128\n\t" \
    "v_mfma_f32_16x16x32_bf16 v[204:207], v[24:27], v[100:103], v[204:207]\n\t" \
    "s_waitcnt lgkmcnt(4)\n\t" \
    "v_mfma_f32_16x16x32_bf16 v[208:211], v[24:27], v[108:111], v[208:211]\n\t" \
    "v_mfma_f32_16x16x32_bf16 v[196:199], v[28:31], v[104:107], v[196:199]\n\t" \
    "ds_read_b128 v[20:23], %[xp] offset:49152\n\t" \
    "v_mfma_f32_16x16x32_bf16 v[200:203], v[28:31], v[112:115], v[200:203]\n\t" \
    "ds_read_b128 v[24:27], %[xp] offset:50176\n\t" \
    "v_mfma_f32_16x16x32_bf16 v[204:207], v[32:35], v[104:107], v[204:207]\n\t" \
    "s_waitcnt lgkmcnt(4)\n\t" \
    "v_mfma_f32_16x16x32_bf16 v[208:211], v[32:35], v[112:115], v[208:211]\n\t" \
    "v_mfma_f32_16x16x32_bf16 v[196:199], v[36:39], v[116:119], v[196:199]\n\t" \
    "ds_read_b128 v[28:31], %[xp] offset:51200\n\t" \
    "v_mfma_f32_16x16x32_bf16 v[200:203], v[36:39], v[124:127], v[200:203]\n\t" \
    "ds_read_b128 v[32:35], %[xp] offset:52224\n\t" \
    "v_mfma_f32_16x16x32_bf16 v[204:207], v[40:43], v[116:119], v[204:207]\n\t" \
    "s_waitcnt lgkmcnt(4)\n\t" \
    "v_mfma_f32_16x16x32_bf16 v[208:211], v[40:43], v[124:127], v[208:211]\n\t" \
    "v_mfma_f32_16x16x32_bf16 v[196:199], v[44:47], v[120:123], v[196:199]\n\t" \
    "ds_read_b128 v[36:39], %[xp] offset:53248\n\t" \
    "v_mfma_f32_16x16x32_bf16 v[200:203], v[44:47], v[128:131], v[200:203]\n\t" \
    "ds_read_b128 v[40:43], %[xp] offset:54272\n\t" \
    "v_mfma_f32_16x16x32_bf16 v[204:207], v[48:51], v[120:123], v[204:207]\n\t" \
    "s_waitcnt lgkmcnt(4)\n\t" \
    "v_mfma_f32_16x16x32_bf16 v[208:211], v[48:51], v[128:131], v[208:211]\n\t" \
    "s_waitcnt vmcnt(4)\n\t" \
    "s_barrier\n\t" \
    "s_setprio 0\n\t" \
    "s_sub_u32 %[a], %[t], 1\n\t" \
    "v_mov_b32 v53, %[tag]\n\t" \
    "v_mov_b32 v52, %[a]\n\t" \
    "s_cmp_gt_u32 %[t], 1\n\t" \
    "s_cselect_b64 exec, %[fm], 0\n\t" \
    "buffer_store_dwordx2 v[52:53], %[l16], %[rs], %[fo] offen sc1\n\t" \
    "s_mov_b64 exec, %[pm]\n\t" \
    "s_mov_b32 m0, %[pd]\n\t" \
    "s_add_u32 %[a], %[sx], %[po]\n\t" \
    "buffer_load_dwordx4 %[l16], %[rs], %[a] offen sc1 lds\n\t" \
    "s_mov_b64 exec, -1\n\t" \
    "s_add_u32 m0, %[sd], 32768\n\t" \
    "s_add_u32 %[a], %[sx], 131072\n\t" \
    "buffer_load_dwordx4 %[l16], %[rs], %[a] offen offset:0 sc1 lds\n\t" \
    "buffer_load_dwordx4 %[l16], %[rs], %[a] offen offset:1024 sc1 lds\n\t" \
    "buffer_load_dwordx4 %[l16], %[rs], %[a] offen offset:2048 sc1 lds\n\t" \
    "buffer_load_dwordx4 %[l16], %[rs], %[a] offen offset:3072 sc1 lds\n\t" \
    "v_mfma_f32_16x16x32_bf16 v[196:199], v[20:23], v[132:135], v[196:199]\n\t" \
    "ds_read_b128 v[44:47], %[xp] offset:55296\n\t" \
    "v_mfma_f32_16x16x32_bf16 v[200:203], v[20:23], v[140:143], v[200:203]\n\t" \
    "ds_read_b128 v[48:51], %[xp] offset:56320\n\t" \
    "v_mfma_f32_16x16x32_bf16 v[204:207], v[24:27], v[132:135], v[204:207]\n\t" \
    "s_waitcnt lgkmcnt(4)\n\t" \
    "v_mfma_f32_16x16x32_bf16 v[208:211], v[24:27], v[140:143], v[208:211]\n\t" \
    "v_mfma_f32_16x16x32_bf16 v[196:199], v[28:31], v[136:139], v[196:199]\n\t" \
    "ds_read_b128 v[20:23], %[xp] offset:57344\n\t" \
    "v_mfma_f32_16x16x32_bf16 v[200:203], v[28:31], v[144:147], v[200:203]\n\t" \
    "ds_read_b128 v[24:27], %[xp] offset:58368\n\t" \
    "v_mfma_f32_16x16x32_bf16 v[204:207], v[32:35], v[136:139], v[204:207]\n\t" \
    "s_waitcnt lgkmcnt(4)\n\t" \
    "v_mfma_f32_16x16x32_bf16 v[208:211], v[32:35], v[144:147], v[208:211]\n\t" \
    "v_mfma_f32_16x16x32_bf16 v[196:199], v[36:39], v[148:151], v[196:199]\n\t" \
    "ds_read_b128 v[28:31], %[xp] offset:59392\n\t" \
    "v_mfma_f32_16x16x32_bf16 v[200:203], v[36:39], v[156:159], v[200:203]\n\t" \
    "ds_read_b128 v[32:35], %[xp] offset:60416\n\t" \
    "v_mfma_f32_16x16x32_bf16 v[204:207], v[40:43], v[148:151], v[204:207]\n\t" \
    "s_waitcnt lgkmcnt(4)\n\t" \
    "v_mfma_f32_16x16x32_bf16 v[208:211], v[40:43], v[156:159], v[208:211]\n\t" \
    "v_mfma_f32_16x16x32_bf16 v[196:199], v[44:47], v[152:155], v[196:199]\n\t" \
    "ds_read_b128 v[36:39], %[xp] offset:61440\n\t" \
    "v_mfma_f32_16x16x32_bf16 v[200:203], v[44:47], v[160:163], v[200:203]\n\t" \
    "ds_read_b128 v[40:43], %[xp] offset:62464\n\t" \
    "v_mfma_f32_16x16x32_bf16 v[204:207], v[48:51], v[152:155], v[204:207]\n\t" \
    "s_waitcnt lgkmcnt(4)\n\t" \
    "v_mfma_f32_16x16x32_bf16 v[208:211], v[48:51], v[160:163], v[208:211]\n\t" \
    "v_mfma_f32_16x16x32_bf16 v[196:199], v[20:23], v[164:167], v[196:199]\n\t" \
    "ds_read_b128 v[44:47], %[xp] offset:63488\n\t" \
    "v_mfma_f32_16x16x32_bf16 v[200:203], v[20:23], v[172:175], v[200:203]\n\t" \
    "ds_read_b128 v[48:51], %[xp] offset:64512\n\t" \
    "v_mfma_f32_16x16x32_bf16 v[204:207], v[24:27], v[164:167], v[204:207]\n\t" \
    "s_waitcnt lgkmcnt(4)\n\t" \
    "v_mfma_f32_16x16x32_bf16 v[208:211], v[24:27], v[172:175], v[208:211]\n\t" \
    "v_mfma_f32_16x16x32_bf16 v[196:199], v[28:31], v[168:171], v[196:199]\n\t" \
    "ds_read_b128 v[20:23], %[xp] offset:0\n\t" \
    "v_mfma_f32_16x16x32_bf16 v[200:203], v[28:31], v[176:179], v[200:203]\n\t" \
    "ds_read_b128 v[24:27], %[xp] offset:1024\n\t" \
    "v_mfma_f32_16x16x32_bf16 v[204:207], v[32:35], v[168:171], v[204:207]\n\t" \
    "s_waitcnt lgkmcnt(4)\n\t" \
    "v_mfma_f32_16x16x32_bf16 v[208:211], v[32:35], v[176:179], v[208:211]\n\t" \
    "v_mfma_f32_16x16x32_bf16 v[196:199], v[36:39], v[180:183], v[196:199]\n\t" \
    "ds_read_b128 v[28:31], %[xp] offset:2048\n\t" \
    "v_mfma_f32_16x16x32_bf16 v[200:203], v[36:39], v[188:191], v[200:203]\n\t" \
    "ds_read_b128 v[32:35], %[xp] offset:3072\n\t" \
    "v_mfma_f32_16x16x32_bf16 v[204:207], v[40:43], v[180:183], v[204:207]\n\t" \
    "s_waitcnt lgkmcnt(4)\n\t" \
    "v_mfma_f32_16x16x32_bf16 v[208:211], v[40:43], v[188:191], v[208:211]\n\t" \
    "v_mfma_f32_16x16x32_bf16 v[196:199], v[44:47], v[184:187], v[196:199]\n\t" \
    "ds_read_b128 v[36:39], %[xp] offset:4096\n\t" \
    "v_mfma_f32_16x16x32_bf16 v[200:203], v[44:47], v[192:195], v[200:203]\n\t" \
    "ds_read_b128 v[40:43], %[xp] offset:5120\n\t" \
    "v_mfma_f32_16x16x32_bf16 v[204:207], v[48:51], v[184:187], v[204:207]\n\t" \
    "s_waitcnt lgkmcnt(4)\n\t" \
    "v_mfma_f32_16x16x32_bf16 v[208:211], v[48:51], v[192:195], v[208:211]\n\t" \
    "s_add_u32 %[sx], %[sx], 65536\n\t" \
    "s_add_u32 %[t], %[t], 1\n\t" \
    "s_waitcnt vmcnt(4)\n\t" \
    "s_barrier\n\t" \
    "s_cmp_lt_u32 %[t], %[te]\n\t" \
    "s_cbranch_scc1 L0_%=\n\t" \
    "s_cmp_eq_u32 %[fin], %[te]\n\t" \
    "s_cbranch_scc0 E_%=\n\t" \
    "s_nop 4\n\t" \
    "ds_write_b128 %[pa], v[196:199] offset:0\n\t" \
    "ds_write_b128 %[pa], v[200:203] offset:1024\n\t" \
    "ds_write_b128 %[pa], v[204:207] offset:2048\n\t" \
    "ds_write_b128 %[pa], v[208:211] offset:3072\n\t" \
    "s_waitcnt lgkmcnt(0)\n\t" \
    "s_barrier\n\t" \
    "s_branch E_%=\n\t" \
    "L1_%=:\n\t" \
    "s_setprio 1\n\t" \
    "s_add_u32 m0, %[sd], 49152\n\t" \
    "s_add_u32 %[a], %[sx], 81920\n\t" \
    "v_mfma_f32_16x16x32_bf16 v[0:3], v[20:23], v[68:71], 0\n\t" \
    "ds_read_b128 v[44:47], %[xp] offset:6144\n\t" \
    "v_mfma_f32_16x16x32_bf16 v[4:7], v[20:23], v[76:79], 0\n\t" \
    "ds_read_b128 v[48:51], %[xp] offset:7168\n\t" \
    "v_mfma_f32_16x16x32_bf16 v[8:11], v[24:27], v[68:71], 0\n\t" \
    "s_waitcnt lgkmcnt(4)\n\t" \
    "v_mfma_f32_16x16x32_bf16 v[12:15], v[24:27], v[76:79], 0\n\t" \
    "v_mfma_f32_16x16x32_bf16 v[0:3], v[28:31], v[72:75], v[0:3]\n\t" \
    "ds_read_b128 v[20:23], %[xp] offset:8192\n\t" \
    "v_mfma_f32_16x16x32_bf16 v[4:7], v[28:31], v[80:83], v[4:7]\n\t" \
    "ds_read_b128 v[24:27], %[xp] offset:9216\n\t" \
    "v_mfma_f32_16x16x32_bf16 v[8:11], v[32:35], v[72:75], v[8:11]\n\t" \
    "s_waitcnt lgkmcnt(4)\n\t" \
    "v_mfma_f32_16x16x32_bf16 v[12:15], v[32:35], v[80:83], v[12:15]\n\t" \
    "v_mfma_f32_16x16x32_bf16 v[0:3], v[36:39], v[84:87], v[0:3]\n\t" \
    "ds_read_b128 v[28:31], %[xp] offset:10240\n\t" \
    "v_mfma_f32_16x16x32_bf16 v[4:7], v[36:39], v[92:95], v[4:7]\n\t" \
    "ds_read_b128 v[32:35], %[xp] offset:11264\n\t" \
    "v_mfma_f32_16x16x32_bf16 v[8:11], v[40:43], v[84:87], v[8:11]\n\t" \
    "s_waitcnt lgkmcnt(4)\n\t" \
    "v_mfma_f32_16x16x32_bf16 v[12:15], v[40:43], v[92:95], v[12:15]\n\t" \
    "v_mfma_f32_16x16x32_bf16 v[0:3], v[44:47], v[88:91], v[0:3]\n\t" \
    "ds_read_b128 v[36:39], %[xp] offset:12288\n\t" \
    "v_mfma_f32_16x16x32_bf16 v[4:7], v[44:47], v[96:99], v[4:7]\n\t" \
    "ds_read_b128 v[40:43], %[xp] offset:13312\n\t" \
    "v_mfma_f32_16x16x32_bf16 v[8:11], v[48:51], v[88:91], v[8:11]\n\t" \
    "s_waitcnt lgkmcnt(4)\n\t" \
    "v_mfma_f32_16x16x32_bf16 v[12:15], v[48:51], v[96:99], v[12:15]\n\t" \
    "v_mfma_f32_16x16x32_bf16 v[0:3], v[20:23], v[100:103], v[0:3]\n\t" \
    "ds_read_b128 v[44:47], %[xp] offset:14336\n\t" \
    "v_mfma_f32_16x16x32_bf16 v[4:7], v[20:23], v[108:111], v[4:7]\n\t" \
    "ds_read_b128 v[48:51], %[xp] offset:15360\n\t" \
    "v_mfma_f32_16x16x32_bf16 v[8:11], v[24:27], v[100:103], v[8:11]\n\t" \
    "s_waitcnt lgkmcnt(4)\n\t" \
    "v_mfma_f32_16x16x32_bf16 v[12:15], v[24:27], v[108:111], v[12:15]\n\t" \
    "buffer_load_dwordx4 %[l16], %[rs], %[a] offen offset:0 sc1 lds\n\t" \
    "v_mfma_f32_16x16x32_bf16 v[0:3], v[28:31], v[104:107], v[0:3]\n\t" \
    "ds_read_b128 v[20:23], %[xp] offset:16384\n\t" \
    "v_mfma_f32_16x16x32_bf16 v[4:7], v[28:31], v[112:115], v[4:7]\n\t" \
    "ds_read_b128 v[24:27], %[xp] offset:17408\n\t" \
    "v_mfma_f32_16x16x32_bf16 v[8:11], v[32:35], v[104:107], v[8:11]\n\t" \
    "s_waitcnt lgkmcnt(4)\n\t" \
    "v_mfma_f32_16x16x32_bf16 v[12:15], v[32:35], v[112:115], v[12:15]\n\t" \
    "buffer_load_dwordx4 %[l16], %[rs], %[a] offen offset:1024 sc1 lds\n\t" \
    "v_mfma_f32_16x16x32_bf16 v[0:3], v[36:39], v[116:119], v[0:3]\n\t" \
    "ds_read_b128 v[28:31], %[xp] offset:18432\n\t" \
    "v_mfma_f32_16x16x32_bf16 v[4:7], v[36:39], v[124:127], v[4:7]\n\t" \
    "ds_read_b128 v[32:35], %[xp] offset:19456\n\t" \
    "v_mfma_f32_16x16x32_bf16 v[8:11], v[40:43], v[116:119], v[8:11]\n\t" \
    "s_waitcnt lgkmcnt(4)\n\t" \
    "v_mfma_f32_16x16x32_bf16 v[12:15], v[40:43], v[124:127], v[12:15]\n\t" \
    "buffer_load_dwordx4 %[l16], %[rs], %[a] offen offset:2048 sc1 lds\n\t" \
    "v_mfma_f32_16x16x32_bf16 v[0:3], v[44:47], v[120:123], v[0:3]\n\t" \
    "ds_read_b128 v[36:39], %[xp] offset:20480\n\t" \
    "v_mfma_f32_16x16x32_bf16 v[4:7], v[44:47], v[128:131], v[4:7]\n\t" \
    "ds_read_b128 v[40:43], %[xp] offset:21504\n\t" \
    "v_mfma_f32_16x16x32_bf16 v[8:11], v[48:51], v[120:123], v[8:11]\n\t" \
    "s_waitcnt lgkmcnt(4)\n\t" \
    "v_mfma_f32_16x16x32_bf16 v[12:15], v[48:51], v[128:131], v[12:15]\n\t" \
    "buffer_load_dwordx4 %[l16], %[rs], %[a] offen offset:3072 sc1 lds\n\t" \
    "s_waitcnt vmcnt(4)\n\t" \
    "s_barrier\n\t" \
    "s_setprio 1\n\t" \
    "s_add_u32 m0, %[sd], 0\n\t" \
    "s_add_u32 %[a], %[sx], 131072\n\t" \
    "s_add_u32 %[b], %[oo], 0x1000\n\t" \
    "s_cmp_lg_u32 %[t], 1\n\t" \
    "s_cselect_b64 %[em], -1, 0\n\t" \
    "v_mfma_f32_16x16x32_bf16 v[0:3], v[20:23], v[132:135], v[0:3]\n\t" \
    "ds_read_b128 v[44:47], %[xp] offset:22528\n\t" \
    "v_mfma_f32_16x16x32_bf16 v[4:7], v[20:23], v[140:143], v[4:7]\n\t" \
    "ds_read_b128 v[48:51], %[xp] offset:23552\n\t" \
    "v_mfma_f32_16x16x32_bf16 v[8:11], v[24:27], v[132:135], v[8:11]\n\t" \
    "ds_read_b128 v[52:55], %[pa] offset:0\n\t" \
    "s_waitcnt lgkmcnt(5)\n\t" \
    "v_mfma_f32_16x16x32_bf16 v[12:15], v[24:27], v[140:143], v[12:15]\n\t" \
    "buffer_load_dwordx4 %[l16], %[rs], %[a] offen offset:0 sc1 lds\n\t" \
    "v_mfma_f32_16x16x32_bf16 v[0:3], v[28:31], v[136:139], v[0:3]\n\t" \
    "ds_read_b128 v[20:23], %[xp] offset:24576\n\t" \
    "v_mfma_f32_16x16x32_bf16 v[4:7], v[28:31], v[144:147], v[4:7]\n\t" \
    "ds_read_b128 v[24:27], %[xp] offset:25600\n\t" \
    "v_mfma_f32_16x16x32_bf16 v[8:11], v[32:35], v[136:139], v[8:11]\n\t" \
    "ds_read_b128 v[56:59], %[pa] offset:1024\n\t" \
    "s_waitcnt lgkmcnt(6)\n\t" \
    "v_mfma_f32_16x16x32_bf16 v[12:15], v[32:35], v[144:147], v[12:15]\n\t" \
    "buffer_load_dwordx4 %[l16], %[rs], %[a] offen offset:1024 sc1 lds\n\t" \
    "v_mfma_f32_16x16x32_bf16 v[0:3], v[36:39], v[148:151], v[0:3]\n\t" \
    "ds_read_b128 v[28:31], %[xp] offset:26624\n\t" \
    "v_mfma_f32_16x16x32_bf16 v[4:7], v[36:39], v[156:159], v[4:7]\n\t" \
    "ds_read_b128 v[32:35], %[xp] offset:27648\n\t" \
    "v_mfma_f32_16x16x32_bf16 v[8:11], v[40:43], v[148:151], v[8:11]\n\t" \
    "ds_read_b128 v[60:63], %[pa] offset:2048\n\t" \
    "s_waitcnt lgkmcnt(7)\n\t" \
    "v_mfma_f32_16x16x32_bf16 v[12:15], v[40:43], v[156:159], v[12:15]\n\t" \
    "buffer_load_dwordx4 %[l16], %[rs], %[a] offen offset:2048 sc1 lds\n\t" \
    "v_mfma_f32_16x16x32_bf16 v[0:3], v[44:47], v[152:155], v[0:3]\n\t" \
    "ds_read_b128 v[36:39], %[xp] offset:28672\n\t" \
    "s_waitcnt lgkmcnt(7)\n\t" \
    "v_add_f32 v196, v196, v52\n\t" \
    "v_mfma_f32_16x16x32_bf16 v[4:7], v[44:47], v[160:163], v[4:7]\n\t" \
    "ds_read_b128 v[40:43], %[xp] offset:29696\n\t" \
    "v_add_f32 v197, v197, v53\n\t" \
    "v_mfma_f32_16x16x32_bf16 v[8:11], v[48:51], v[152:155], v[8:11]\n\t" \
    "ds_read_b128 v[64:67], %[pa] offset:3072\n\t" \
    "s_waitcnt lgkmcnt(7)\n\t" \
    "v_mfma_f32_16x16x32_bf16 v[12:15], v[48:51], v[160:163], v[12:15]\n\t" \
    "buffer_load_dwordx4 %[l16], %[rs], %[a] offen offset:3072 sc1 lds\n\t" \
    "v_add_f32 v198, v198, v54\n\t" \
    "v_add_f32 v199, v199, v55\n\t" \
    "v_mfma_f32_16x16x32_bf16 v[0:3], v[20:23], v[164:167], v[0:3]\n\t" \
    "ds_read_b128 v[44:47], %[xp] offset:30720\n\t" \
    "s_waitcnt lgkmcnt(7)\n\t" \
    "v_add_f32 v200, v200, v56\n\t" \
    "v_mfma_f32_16x16x32_bf16 v[4:7], v[20:23], v[172:175], v[4:7]\n\t" \
    "ds_read_b128 v[48:51], %[xp] offset:31744\n\t" \
    "v_add_f32 v201, v201, v57\n\t" \
    "v_mfma_f32_16x16x32_bf16 v[8:11], v[24:27], v[164:167], v[8:11]\n\t" \
    "v_add_f32 v202, v202, v58\n\t" \
    "s_waitcnt lgkmcnt(6)\n\t" \
    "v_mfma_f32_16x16x32_bf16 v[12:15], v[24:27], v[172:175], v[12:15]\n\t" \
    "v_add_f32 v203, v203, v59\n\t" \
    "s_waitcnt lgkmcnt(5)\n\t" \
    "v_add_f32 v204, v204, v60\n\t" \
    "v_add_f32 v205, v205, v61\n\t" \
    "v_mfma_f32_16x16x32_bf16 v[0:3], v[28:31], v[168:171], v[0:3]\n\t" \
    "ds_read_b128 v[20:23], %[xp] offset:32768\n\t" \
    "v_add_f32 v206, v206, v62\n\t" \
    "v_mfma_f32_16x16x32_bf16 v[4:7], v[28:31], v[176:179], v[4:7]\n\t" \
    "ds_read_b128 v[24:27], %[xp] offset:33792\n\t" \
    "v_add_f32 v207, v207, v63\n\t" \
    "v_mfma_f32_16x16x32_bf16 v[8:11], v[32:35], v[168:171], v[8:11]\n\t" \
    "s_waitcnt lgkmcnt(4)\n\t" \
    "v_add_f32 v208, v208, v64\n\t" \
    "v_mfma_f32_16x16x32_bf16 v[12:15], v[32:35], v[176:179], v[12:15]\n\t" \
    "v_add_f32 v209, v209, v65\n\t" \
    "v_add_f32 v210, v210, v66\n\t" \
    "v_add_f32 v211, v211, v67\n\t" \
    "v_mfma_f32_16x16x32_bf16 v[0:3], v[36:39], v[180:183], v[0:3]\n\t" \
    "ds_read_b128 v[28:31], %[xp] offset:34816\n\t" \
    "v_cvt_pk_bf16_f32 v52, v196, v197\n\t" \
    "v_mfma_f32_16x16x32_bf16 v[4:7], v[36:39], v[188:191], v[4:7]\n\t" \
    "ds_read_b128 v[32:35], %[xp] offset:35840\n\t" \
    "v_cvt_pk_bf16_f32 v53, v198, v199\n\t" \
    "v_mfma_f32_16x16x32_bf16 v[8:11], v[40:43], v[180:183], v[8:11]\n\t" \
    "v_cvt_pk_bf16_f32 v54, v204, v205\n\t" \
    "s_waitcnt lgkmcnt(4)\n\t" \
    "v_mfma_f32_16x16x32_bf16 v[12:15], v[40:43], v[188:191], v[12:15]\n\t" \
    "v_cvt_pk_bf16_f32 v55, v206, v207\n\t" \
    "v_cvt_pk_bf16_f32 v56, v200, v201\n\t" \
    "v_cvt_pk_bf16_f32 v57, v202, v203\n\t" \
    "v_mfma_f32_16x16x32_bf16 v[0:3], v[44:47], v[184:187], v[0:3]\n\t" \
    "ds_read_b128 v[36:39], %[xp] offset:36864\n\t" \
    "v_cvt_pk_bf16_f32 v58, v208, v209\n\t" \
    "v_mfma_f32_16x16x32_bf16 v[4:7], v[44:47], v[192:195], v[4:7]\n\t" \
    "ds_read_b128 v[40:43], %[xp] offset:37888\n\t" \
    "v_cvt_pk_bf16_f32 v59, v210, v211\n\t" \
    "v_mfma_f32_16x16x32_bf16 v[8:11], v[48:51], v[184:187], v[8:11]\n\t" \
    "s_waitcnt lgkmcnt(4)\n\t" \
    "v_mfma_f32_16x16x32_bf16 v[12:15], v[48:51], v[192:195], v[12:15]\n\t" \
    "s_mov_b64 exec, %[em]\n\t" \
    "buffer_store_dwordx4 v[52:55], %[l16], %[rs], %[oo] offen sc1\n\t" \
    "buffer_store_dwordx4 v[56:59], %[l16], %[rs], %[oo] offen offset:1024 sc1\n\t" \
    "s_mov_b64 exec, -1\n\t" \
    "s_add_u32 %[sx], %[sx], 65536\n\t" \
    "s_add_u32 %[t], %[t], 1\n\t" \
    "s_add_u32 %[oo], %[oo], 262144\n\t" \
    "s_waitcnt vmcnt(6)\n\t" \
    "s_cmp_gt_u32 %[t], " STR(SQUICK) " + 1\n\t" \
    "s_cbranch_scc1 B0_%=\n\t" \
    "s_waitcnt vmcnt(0)\n\t" \
    "B0_%=:\n\t" \
    "s_barrier\n\t" \
    "s_setprio 1\n\t" \
    "s_add_u32 m0, %[sd], 16384\n\t" \
    "s_add_u32 %[a], %[sx], 81920\n\t" \
    "v_mfma_f32_16x16x32_bf16 v[196:199], v[20:23], v[68:71], 0\n\t" \
    "ds_read_b128 v[44:47], %[xp] offset:38912\n\t" \
    "v_mfma_f32_16x16x32_bf16 v[200:203], v[20:23], v[76:79], 0\n\t" \
    "ds_read_b128 v[48:51], %[xp] offset:39936\n\t" \
    "v_mfma_f32_16x16x32_bf16 v[204:207], v[24:27], v[68:71], 0\n\t" \
    "s_waitcnt lgkmcnt(4)\n\t" \
    "v_mfma_f32_16x16x32_bf16 v[208:211], v[24:27], v[76:79], 0\n\t" \
    "v_mfma_f32_16x16x32_bf16 v[196:199], v[28:31], v[72:75], v[196:199]\n\t" \
    "ds_read_b128 v[20:23], %[xp] offset:40960\n\t" \
    "v_mfma_f32_16x16x32_bf16 v[200:203], v[28:31], v[80:83], v[200:203]\n\t" \
    "ds_read_b128 v[24:27], %[xp] offset:41984\n\t" \
    "v_mfma_f32_16x16x32_bf16 v[204:207], v[32:35], v[72:75], v[204:207]\n\t" \
    "s_waitcnt lgkmcnt(4)\n\t" \
    "v_mfma_f32_16x16x32_bf16 v[208:211], v[32:35], v[80:83], v[208:211]\n\t" \
    "v_mfma_f32_16x16x32_bf16 v[196:199], v[36:39], v[84:87], v[196:199]\n\t" \
    "ds_read_b128 v[28:31], %[xp] offset:43008\n\t" \
    "v_mfma_f32_16x16x32_bf16 v[200:203], v[36:39], v[92:95], v[200:203]\n\t" \
    "ds_read_b128 v[32:35], %[xp] offset:44032\n\t" \
    "v_mfma_f32_16x16x32_bf16 v[204:207], v[40:43], v[84:87], v[204:207]\n\t" \
    "s_waitcnt lgkmcnt(4)\n\t" \
    "v_mfma_f32_16x16x32_bf16 v[208:211], v[40:43], v[92:95], v[208:211]\n\t" \
    "v_mfma_f32_16x16x32_bf16 v[196:199], v[44:47], v[88:91], v[196:199]\n\t" \
    "ds_read_b128 v[36:39], %[xp] offset:45056\n\t" \
    "v_mfma_f32_16x16x32_bf16 v[200:203], v[44:47], v[96:99], v[200:203]\n\t" \
    "ds_read_b128 v[40:43], %[xp] offset:46080\n\t" \
    "v_mfma_f32_16x16x32_bf16 v[204:207], v[48:51], v[88:91], v[204:207]\n\t" \
    "s_waitcnt lgkmcnt(4)\n\t" \
    "v_mfma_f32_16x16x32_bf16 v[208:211], v[48:51], v[96:99], v[208:211]\n\t" \
    "v_mfma_f32_16x16x32_bf16 v[196:199], v[20:23], v[100:103], v[196:199]\n\t" \
    "ds_read_b128 v[44:47], %[xp] offset:47104\n\t" \
    "v_mfma_f32_16x16x32_bf16 v[200:203], v[20:23], v[108:111], v[200:203]\n\t" \
    "ds_read_b128 v[48:51], %[xp] offset:48128\n\t" \
    "v_mfma_f32_16x16x32_bf16 v[204:207], v[24:27], v[100:103], v[204:207]\n\t" \
    "s_waitcnt lgkmcnt(4)\n\t" \
    "v_mfma_f32_16x16x32_bf16 v[208:211], v[24:27], v[108:111], v[208:211]\n\t" \
    "buffer_load_dwordx4 %[l16], %[rs], %[a] offen offset:0 sc1 lds\n\t" \
    "v_mfma_f32_16x16x32_bf16 v[196:199], v[28:31], v[104:107], v[196:199]\n\t" \
    "ds_read_b128 v[20:23], %[xp] offset:49152\n\t" \
    "v_mfma_f32_16x16x32_bf16 v[200:203], v[28:31], v[112:115], v[200:203]\n\t" \
    "ds_read_b128 v[24:27], %[xp] offset:50176\n\t" \
    "v_mfma_f32_16x16x32_bf16 v[204:207], v[32:35], v[104:107], v[204:207]\n\t" \
    "s_waitcnt lgkmcnt(4)\n\t" \
    "v_mfma_f32_16x16x32_bf16 v[208:211], v[32:35], v[112:115], v[208:211]\n\t" \
    "buffer_load_dwordx4 %[l16], %[rs], %[a] offen offset:1024 sc1 lds\n\t" \
    "v_mfma_f32_16x16x32_bf16 v[196:199], v[36:39], v[116:119], v[196:199]\n\t" \
    "ds_read_b128 v[28:31], %[xp] offset:51200\n\t" \
    "v_mfma_f32_16x16x32_bf16 v[200:203], v[36:39], v[124:127], v[200:203]\n\t" \
    "ds_read_b128 v[32:35], %[xp] offset:52224\n\t" \
    "v_mfma_f32_16x16x32_bf16 v[204:207], v[40:43], v[116:119], v[204:207]\n\t" \
    "s_waitcnt lgkmcnt(4)\n\t" \
    "v_mfma_f32_16x16x32_bf16 v[208:211], v[40:43], v[124:127], v[208:211]\n\t" \
    "buffer_load_dwordx4 %[l16], %[rs], %[a] offen offset:2048 sc1 lds\n\t" \
    "v_mfma_f32_16x16x32_bf16 v[196:199], v[44:47], v[120:123], v[196:199]\n\t" \
    "ds_read_b128 v[36:39], %[xp] offset:53248\n\t" \
    "v_mfma_f32_16x16x32_bf16 v[200:203], v[44:47], v[128:131], v[200:203]\n\t" \
    "ds_read_b128 v[40:43], %[xp] offset:54272\n\t" \
    "v_mfma_f32_16x16x32_bf16 v[204:207], v[48:51], v[120:123], v[204:207]\n\t" \
    "s_waitcnt lgkmcnt(4)\n\t" \
    "v_mfma_f32_16x16x32_bf16 v[208:211], v[48:51], v[128:131], v[208:211]\n\t" \
    "buffer_load_dwordx4 %[l16], %[rs], %[a] offen offset:3072 sc1 lds\n\t" \
    "s_waitcnt vmcnt(4)\n\t" \
    "s_barrier\n\t" \
    "s_setprio 1\n\t" \
    "s_add_u32 m0, %[sd], 32768\n\t" \
    "s_add_u32 %[a], %[sx], 131072\n\t" \
    "s_add_u32 %[b], %[oo], 0x1000\n\t" \
    "s_cmp_lg_u32 %[t], 1\n\t" \
    "s_cselect_b64 %[em], -1, 0\n\t" \
    "v_mfma_f32_16x16x32_bf16 v[196:199], v[20:23], v[132:135], v[196:199]\n\t" \
    "ds_read_b128 v[44:47], %[xp] offset:55296\n\t" \
    "v_mfma_f32_16x16x32_bf16 v[200:203], v[20:23], v[140:143], v[200:203]\n\t" \
    "ds_read_b128 v[48:51], %[xp] offset:56320\n\t" \
    "v_mfma_f32_16x16x32_bf16 v[204:207], v[24:27], v[132:135], v[204:207]\n\t" \
    "ds_read_b128 v[52:55], %[pa] offset:0\n\t" \
    "s_waitcnt lgkmcnt(5)\n\t" \
    "v_mfma_f32_16x16x32_bf16 v[208:211], v[24:27], v[140:143], v[208:211]\n\t" \
    "buffer_load_dwordx4 %[l16], %[rs], %[a] offen offset:0 sc1 lds\n\t" \
    "v_mfma_f32_16x16x32_bf16 v[196:199], v[28:31], v[136:139], v[196:199]\n\t" \
    "ds_read_b128 v[20:23], %[xp] offset:57344\n\t" \
    "v_mfma_f32_16x16x32_bf16 v[200:203], v[28:31], v[144:147], v[200:203]\n\t" \
    "ds_read_b128 v[24:27], %[xp] offset:58368\n\t" \
    "v_mfma_f32_16x16x32_bf16 v[204:207], v[32:35], v[136:139], v[204:207]\n\t" \
    "ds_read_b128 v[56:59], %[pa] offset:1024\n\t" \
    "s_waitcnt lgkmcnt(6)\n\t" \
    "v_mfma_f32_16x16x32_bf16 v[208:211], v[32:35], v[144:147], v[208:211]\n\t" \
    "buffer_load_dwordx4 %[l16], %[rs], %[a] offen offset:1024 sc1 lds\n\t" \
    "v_mfma_f32_16x16x32_bf16 v[196:199], v[36:39], v[148:151], v[196:199]\n\t" \
    "ds_read_b128 v[28:31], %[xp] offset:59392\n\t" \
    "v_mfma_f32_16x16x32_bf16 v[200:203], v[36:39], v[156:159], v[200:203]\n\t" \
    "ds_read_b128 v[32:35], %[xp] offset:60416\n\t" \
    "v_mfma_f32_16x16x32_bf16 v[204:207], v[40:43], v[148:151], v[204:207]\n\t" \
    "ds_read_b128 v[60:63], %[pa] offset:2048\n\t" \
    "s_waitcnt lgkmcnt(7)\n\t" \
    "v_mfma_f32_16x16x32_bf16 v[208:211], v[40:43], v[156:159], v[208:211]\n\t" \
    "buffer_load_dwordx4 %[l16], %[rs], %[a] offen offset:2048 sc1 lds\n\t" \
    "v_mfma_f32_16x16x32_bf16 v[196:199], v[44:47], v[152:155], v[196:199]\n\t" \
    "ds_read_b128 v[36:39], %[xp] offset:61440\n\t" \
    "s_waitcnt lgkmcnt(7)\n\t" \
    "v_add_f32 v0, v0, v52\n\t" \
    "v_mfma_f32_16x16x32_bf16 v[200:203], v[44:47], v[160:163], v[200:203]\n\t" \
    "ds_read_b128 v[40:43], %[xp] offset:62464\n\t" \
    "v_add_f32 v1, v1, v53\n\t" \
    "v_mfma_f32_16x16x32_bf16 v[204:207], v[48:51], v[152:155], v[204:207]\n\t" \
    "ds_read_b128 v[64:67], %[pa] offset:3072\n\t" \
    "s_waitcnt lgkmcnt(7)\n\t" \
    "v_mfma_f32_16x16x32_bf16 v[208:211], v[48:51], v[160:163], v[208:211]\n\t" \
    "buffer_load_dwordx4 %[l16], %[rs], %[a] offen offset:3072 sc1 lds\n\t" \
    "v_add_f32 v2, v2, v54\n\t" \
    "v_add_f32 v3, v3, v55\n\t" \
    "v_mfma_f32_16x16x32_bf16 v[196:199], v[20:23], v[164:167], v[196:199]\n\t" \
    "ds_read_b128 v[44:47], %[xp] offset:63488\n\t" \
    "s_waitcnt lgkmcnt(7)\n\t" \
    "v_add_f32 v4, v4, v56\n\t" \
    "v_mfma_f32_16x16x32_bf16 v[200:203], v[20:23], v[172:175], v[200:203]\n\t" \
    "ds_read_b128 v[48:51], %[xp] offset:64512\n\t" \
    "v_add_f32 v5, v5, v57\n\t" \
    "v_mfma_f32_16x16x32_bf16 v[204:207], v[24:27], v[164:167], v[204:207]\n\t" \
    "v_add_f32 v6, v6, v58\n\t" \
    "s_waitcnt lgkmcnt(6)\n\t" \
    "v_mfma_f32_16x16x32_bf16 v[208:211], v[24:27], v[172:175], v[208:211]\n\t" \
    "v_add_f32 v7, v7, v59\n\t" \
    "s_waitcnt lgkmcnt(5)\n\t" \
    "v_add_f32 v8, v8, v60\n\t" \
    "v_add_f32 v9, v9, v61\n\t" \
    "v_mfma_f32_16x16x32_bf16 v[196:199], v[28:31], v[168:171], v[196:199]\n\t" \
    "ds_read_b128 v[20:23], %[xp] offset:0\n\t" \
    "v_add_f32 v10, v10, v62\n\t" \
    "v_mfma_f32_16x16x32_bf16 v[200:203], v[28:31], v[176:179], v[200:203]\n\t" \
    "ds_read_b128 v[24:27], %[xp] offset:1024\n\t" \
    "v_add_f32 v11, v11, v63\n\t" \
    "v_mfma_f32_16x16x32_bf16 v[204:207], v[32:35], v[168:171], v[204:207]\n\t" \
    "s_waitcnt lgkmcnt(4)\n\t" \
    "v_add_f32 v12, v12, v64\n\t" \
    "v_mfma_f32_16x16x32_bf16 v[208:211], v[32:35], v[176:179], v[208:211]\n\t" \
    "v_add_f32 v13, v13, v65\n\t" \
    "v_add_f32 v14, v14, v66\n\t" \
    "v_add_f32 v15, v15, v67\n\t" \
    "v_mfma_f32_16x16x32_bf16 v[196:199], v[36:39], v[180:183], v[196:199]\n\t" \
    "ds_read_b128 v[28:31], %[xp] offset:2048\n\t" \
    "v_cvt_pk_bf16_f32 v52, v0, v1\n\t" \
    "v_mfma_f32_16x16x32_bf16 v[200:203], v[36:39], v[188:191], v[200:203]\n\t" \
    "ds_read_b128 v[32:35], %[xp] offset:3072\n\t" \
    "v_cvt_pk_bf16_f32 v53, v2, v3\n\t" \
    "v_mfma_f32_16x16x32_bf16 v[204:207], v[40:43], v[180:183], v[204:207]\n\t" \
    "v_cvt_pk_bf16_f32 v54, v8, v9\n\t" \
    "s_waitcnt lgkmcnt(4)\n\t" \
    "v_mfma_f32_16x16x32_bf16 v[208:211], v[40:43], v[188:191], v[208:211]\n\t" \
    "v_cvt_pk_bf16_f32 v55, v10, v11\n\t" \
    "v_cvt_pk_bf16_f32 v56, v4, v5\n\t" \
    "v_cvt_pk_bf16_f32 v57, v6, v7\n\t" \
    "v_mfma_f32_16x16x32_bf16 v[196:199], v[44:47], v[184:187], v[196:199]\n\t" \
    "ds_read_b128 v[36:39], %[xp] offset:4096\n\t" \
    "v_cvt_pk_bf16_f32 v58, v12, v13\n\t" \
    "v_mfma_f32_16x16x32_bf16 v[200:203], v[44:47], v[192:195], v[200:203]\n\t" \
    "ds_read_b128 v[40:43], %[xp] offset:5120\n\t" \
    "v_cvt_pk_bf16_f32 v59, v14, v15\n\t" \
    "v_mfma_f32_16x16x32_bf16 v[204:207], v[48:51], v[184:187], v[204:207]\n\t" \
    "s_waitcnt lgkmcnt(4)\n\t" \
    "v_mfma_f32_16x16x32_bf16 v[208:211], v[48:51], v[192:195], v[208:211]\n\t" \
    "s_mov_b64 exec, %[em]\n\t" \
    "buffer_store_dwordx4 v[52:55], %[l16], %[rs], %[oo] offen sc1\n\t" \
    "buffer_store_dwordx4 v[56:59], %[l16], %[rs], %[oo] offen offset:1024 sc1\n\t" \
    "s_mov_b64 exec, -1\n\t" \
    "s_add_u32 %[sx], %[sx], 65536\n\t" \
    "s_add_u32 %[t], %[t], 1\n\t" \
    "s_add_u32 %[oo], %[oo], 262144\n\t" \
    "s_waitcnt vmcnt(6)\n\t" \
    "s_cmp_gt_u32 %[t], " STR(SQUICK) " + 1\n\t" \
    "s_cbranch_scc1 B1_%=\n\t" \
    "s_waitcnt vmcnt(0)\n\t" \
    "B1_%=:\n\t" \
    "s_barrier\n\t" \
    "s_cmp_lt_u32 %[t], %[te]\n\t" \
    "s_cbranch_scc1 L1_%=\n\t" \
    "s_cmp_eq_u32 %[fin], %[te]\n\t" \
    "s_cbranch_scc0 E_%=\n\t" \
    "s_nop 4\n\t" \
    "s_barrier\n\t" \
    "ds_read_b128 v[52:55], %[pa] offset:0\n\t" \
    "ds_read_b128 v[56:59], %[pa] offset:1024\n\t" \
    "ds_read_b128 v[60:63], %[pa] offset:2048\n\t" \
    "ds_read_b128 v[64:67], %[pa] offset:3072\n\t" \
    "s_add_u32 %[b], %[oo], 0x1000\n\t" \
    "s_waitcnt lgkmcnt(0)\n\t" \
    "v_add_f32 v196, v196, v52\n\t" \
    "v_add_f32 v197, v197, v53\n\t" \
    "v_add_f32 v198, v198, v54\n\t" \
    "v_add_f32 v199, v199, v55\n\t" \
    "v_add_f32 v200, v200, v56\n\t" \
    "v_add_f32 v201, v201, v57\n\t" \
    "v_add_f32 v202, v202, v58\n\t" \
    "v_add_f32 v203, v203, v59\n\t" \
    "v_add_f32 v204, v204, v60\n\t" \
    "v_add_f32 v205, v205, v61\n\t" \
    "v_add_f32 v206, v206, v62\n\t" \
    "v_add_f32 v207, v207, v63\n\t" \
    "v_add_f32 v208, v208, v64\n\t" \
    "v_add_f32 v209, v209, v65\n\t" \
    "v_add_f32 v210, v210, v66\n\t" \
    "v_add_f32 v211, v211, v67\n\t" \
    "v_cvt_pk_bf16_f32 v52, v196, v197\n\t" \
    "v_cvt_pk_bf16_f32 v53, v198, v199\n\t" \
    "v_cvt_pk_bf16_f32 v54, v204, v205\n\t" \
    "v_cvt_pk_bf16_f32 v55, v206, v207\n\t" \
    "v_cvt_pk_bf16_f32 v56, v200, v201\n\t" \
    "v_cvt_pk_bf16_f32 v57, v202, v203\n\t" \
    "v_cvt_pk_bf16_f32 v58, v208, v209\n\t" \
    "v_cvt_pk_bf16_f32 v59, v210, v211\n\t" \
    "buffer_store_dwordx4 v[52:55], %[l16], %[rs], %[oo] offen sc1\n\t" \
    "buffer_store_dwordx4 v[56:59], %[l16], %[rs], %[oo] offen offset:1024 sc1\n\t" \
    "E_%=:\n\t" \
    "s_setprio 0\n\t" \
    "s_waitcnt lgkmcnt(0)\n\t" \
    "s_nop 7\n\t" \
    : [t] "+s"(t), [sx] "+s"(sx), [oo] "+s"(oo), [a] "=&s"(tmp), [b] "=&s"(tmp2), [em] "=&s"(em), "+{v[20:27]}"(xf[0]), "+{v[28:35]}"(xf[1]), "+{v[36:43]}"(xf[2]), "+{v[196:203]}"(ac[0]), "+{v[204:211]}"(ac[1]) \
    : [te] "s"(TE_), [fin] "s"(FIN_), [kh] "s"(kh), [sd] "s"(sd), [rs] "s"(rs), [l16] "v"(l16), [xp] "v"(xp), [pa] "v"(pa), [fm] "s"(fm), [pm] "s"(pm), [fo] "s"(fo), [po] "s"(po), [pd] "s"(pd), [tag] "s"(P.tag), "{v[68:75]}"(w[0][0]), "{v[76:83]}"(w[0][1]), "{v[84:91]}"(w[1][0]), "{v[92:99]}"(w[1][1]), "{v[100:107]}"(w[2][0]), "{v[108:115]}"(w[2][1]), "{v[116:123]}"(w[3][0]), "{v[124:131]}"(w[3][1]), "{v[132:139]}"(w[4][0]), "{v[140:147]}"(w[4][1]), "{v[148:155]}"(w[5][0]), "{v[156:163]}"(w[5][1]), "{v[164:171]}"(w[6][0]), "{v[172:179]}"(w[6][1]), "{v[180:187]}"(w[7][0]), "{v[188:195]}"(w[7][1]) \
    : "memory", "scc", "v0", "v1", "v2", "v3", "v4", "v5", "v6", "v7", "v8", "v9", "v10", "v11", "v12", "v13", "v14", "v15", "v44", "v45", "v46", "v47", "v48", "v49", "v50", "v51", "v52", "v53", "v54", "v55", "v56", "v57", "v58", "v59", "v60", "v61", "v62", "v63", "v64", "v65", "v66", "v67")

#define S_LOOP_X(TE_, FIN_) \
  asm volatile( \
    "s_cmp_eq_u32 %[kh], 0\n\t" \
    "s_cbranch_scc0 L1_%=\n\t" \
    "L0_%=:\n\t" \
    "s_setprio 0\n\t" \
    "s_nop 3\n\t" \
    "ds_write_b128 %[pa], v[196:199] offset:0\n\t" \
    "ds_write_b128 %[pa], v[200:203] offset:1024\n\t" \
    "ds_write_b128 %[pa], v[204:207] offset:2048\n\t" \
    "ds_write_b128 %[pa], v[208:211] offset:3072\n\t" \
    "s_sub_u32 %[a], %[t], 1\n\t" \
    "v_mov_b32 v53, %[tag]\n\t" \
    "v_mov_b32 v52, %[a]\n\t" \
    "s_sub_u32 %[b], %[t], 2\n\t" \
    "s_cmp_lt_u32 %[b], " STR(SQUICK) "\n\t" \
    "s_cselect_b64 exec, %[fm], 0\n\t" \
    "buffer_store_dwordx2 v[52:53], %[l16], %[rs], %[fo] offen sc1\n\t" \
    "s_mov_b64 exec, -1\n\t" \
    "s_add_u32 m0, %[sd], 49152\n\t" \
    "s_add_u32 %[a], %[sx], 278528\n\t" \
    "s_waitcnt lgkmcnt(0)\n\t" \
    "buffer_load_dwordx4 %[l16], %[rs], %[a] offen offset:0 sc1 lds\n\t" \
    "buffer_load_dwordx4 %[l16], %[rs], %[a] offen offset:1024 sc1 lds\n\t" \
    "buffer_load_dwordx4 %[l16], %[rs], %[a] offen offset:2048 sc1 lds\n\t" \
    "buffer_load_dwordx4 %[l16], %[rs], %[a] offen offset:3072 sc1 lds\n\t" \
    "v_mfma_f32_16x16x32_bf16 v[0:3], v[68:71], v[20:23], 0\n\t" \
    "ds_read_b128 v[44:47], %[xp] offset:6144\n\t" \
    "v_mfma_f32_16x16x32_bf16 v[4:7], v[76:79], v[20:23], 0\n\t" \
    "ds_read_b128 v[48:51], %[xp] offset:7168\n\t" \
    "v_mfma_f32_16x16x32_bf16 v[8:11], v[68:71], v[24:27], 0\n\t" \
    "v_mfma_f32_16x16x32_bf16 v[12:15], v[76:79], v[24:27], 0\n\t" \
    "v_mfma_f32_16x16x32_bf16 v[0:3], v[72:75], v[28:31], v[0:3]\n\t" \
    "ds_read_b128 v[20:23], %[xp] offset:8192\n\t" \
    "v_mfma_f32_16x16x32_bf16 v[4:7], v[80:83], v[28:31], v[4:7]\n\t" \
    "ds_read_b128 v[24:27], %[xp] offset:9216\n\t" \
    "v_mfma_f32_16x16x32_bf16 v[8:11], v[72:75], v[32:35], v[8:11]\n\t" \
    "v_mfma_f32_16x16x32_bf16 v[12:15], v[80:83], v[32:35], v[12:15]\n\t" \
    "v_mfma_f32_16x16x32_bf16 v[0:3], v[84:87], v[36:39], v[0:3]\n\t" \
    "ds_read_b128 v[28:31], %[xp] offset:10240\n\t" \
    "v_mfma_f32_16x16x32_bf16 v[4:7], v[92:95], v[36:39], v[4:7]\n\t" \
    "ds_read_b128 v[32:35], %[xp] offset:11264\n\t" \
    "v_mfma_f32_16x16x32_bf16 v[8:11], v[84:87], v[40:43], v[8:11]\n\t" \
    "s_waitcnt lgkmcnt(4)\n\t" \
    "v_mfma_f32_16x16x32_bf16 v[12:15], v[92:95], v[40:43], v[12:15]\n\t" \
    "v_mfma_f32_16x16x32_bf16 v[0:3], v[88:91], v[44:47], v[0:3]\n\t" \
    "ds_read_b128 v[36:39], %[xp] offset:12288\n\t" \
    "v_mfma_f32_16x16x32_bf16 v[4:7], v[96:99], v[44:47], v[4:7]\n\t" \
    "ds_read_b128 v[40:43], %[xp] offset:13312\n\t" \
    "v_mfma_f32_16x16x32_bf16 v[8:11], v[88:91], v[48:51], v[8:11]\n\t" \
    "s_waitcnt lgkmcnt(4)\n\t" \
    "v_mfma_f32_16x16x32_bf16 v[12:15], v[96:99], v[48:51], v[12:15]\n\t" \
    "v_mfma_f32_16x16x32_bf16 v[0:3], v[100:103], v[20:23], v[0:3]\n\t" \
    "ds_read_b128 v[44:47], %[xp] offset:14336\n\t" \
    "v_mfma_f32_16x16x32_bf16 v[4:7], v[108:111], v[20:23], v[4:7]\n\t" \
    "ds_read_b128 v[48:51], %[xp] offset:15360\n\t" \
    "v_mfma_f32_16x16x32_bf16 v[8:11], v[100:103], v[24:27], v[8:11]\n\t" \
    "s_waitcnt lgkmcnt(4)\n\t" \
    "v_mfma_f32_16x16x32_bf16 v[12:15], v[108:111], v[24:27], v[12:15]\n\t" \
    "v_mfma_f32_16x16x32_bf16 v[0:3], v[104:107], v[28:31], v[0:3]\n\t" \
    "ds_read_b128 v[20:23], %[xp] offset:16384\n\t" \
    "v_mfma_f32_16x16x32_bf16 v[4:7], v[112:115], v[28:31], v[4:7]\n\t" \
    "ds_read_b128 v[24:27], %[xp] offset:17408\n\t" \
    "v_mfma_f32_16x16x32_bf16 v[8:11], v[104:107], v[32:35], v[8:11]\n\t" \
    "s_waitcnt lgkmcnt(4)\n\t" \
    "v_mfma_f32_16x16x32_bf16 v[12:15], v[112:115], v[32:35], v[12:15]\n\t" \
    "v_mfma_f32_16x16x32_bf16 v[0:3], v[116:119], v[36:39], v[0:3]\n\t" \
    "ds_read_b128 v[28:31], %[xp] offset:18432\n\t" \
    "v_mfma_f32_16x16x32_bf16 v[4:7], v[124:127], v[36:39], v[4:7]\n\t" \
    "ds_read_b128 v[32:35], %[xp] offset:19456\n\t" \
    "v_mfma_f32_16x16x32_bf16 v[8:11], v[116:119], v[40:43], v[8:11]\n\t" \
    "s_waitcnt lgkmcnt(4)\n\t" \
    "v_mfma_f32_16x16x32_bf16 v[12:15], v[124:127], v[40:43], v[12:15]\n\t" \
    "v_mfma_f32_16x16x32_bf16 v[0:3], v[120:123], v[44:47], v[0:3]\n\t" \
    "ds_read_b128 v[36:39], %[xp] offset:20480\n\t" \
    "v_mfma_f32_16x16x32_bf16 v[4:7], v[128:131], v[44:47], v[4:7]\n\t" \
    "ds_read_b128 v[40:43], %[xp] offset:21504\n\t" \
    "v_mfma_f32_16x16x32_bf16 v[8:11], v[120:123], v[48:51], v[8:11]\n\t" \
    "s_waitcnt lgkmcnt(4)\n\t" \
    "v_mfma_f32_16x16x32_bf16 v[12:15], v[128:131], v[48:51], v[12:15]\n\t" \
    "s_waitcnt vmcnt(4)\n\t" \
    "s_barrier\n\t" \
    "s_setprio 0\n\t" \
    "s_sub_u32 %[a], %[t], 1\n\t" \
    "v_mov_b32 v53, %[tag]\n\t" \
    "v_mov_b32 v52, %[a]\n\t" \
    "s_cmp_gt_u32 %[t], 1\n\t" \
    "s_cselect_b64 exec, %[fm], 0\n\t" \
    "buffer_store_dwordx2 v[52:53], %[l16], %[rs], %[fo] offen sc1\n\t" \
    "s_mov_b64 exec, %[pm]\n\t" \
    "s_mov_b32 m0, %[pd]\n\t" \
    "s_add_u32 %[a], %[sx], %[po]\n\t" \
    "buffer_load_dwordx4 %[l16], %[rs], %[a] offen sc1 lds\n\t" \
    "s_mov_b64 exec, -1\n\t" \
    "s_add_u32 m0, %[sd], 0\n\t" \
    "s_add_u32 %[a], %[sx], 524288\n\t" \
    "buffer_load_dwordx4 %[l16], %[rs], %[a] offen offset:0 sc1 lds\n\t" \
    "buffer_load_dwordx4 %[l16], %[rs], %[a] offen offset:1024 sc1 lds\n\t" \
    "buffer_load_dwordx4 %[l16], %[rs], %[a] offen offset:2048 sc1 lds\n\t" \
    "buffer_load_dwordx4 %[l16], %[rs], %[a] offen offset:3072 sc1 lds\n\t" \
    "v_mfma_f32_16x16x32_bf16 v[0:3], v[132:135], v[20:23], v[0:3]\n\t" \
    "ds_read_b128 v[44:47], %[xp] offset:22528\n\t" \
    "v_mfma_f32_16x16x32_bf16 v[4:7], v[140:143], v[20:23], v[4:7]\n\t" \
    "ds_read_b128 v[48:51], %[xp] offset:23552\n\t" \
    "v_mfma_f32_16x16x32_bf16 v[8:11], v[132:135], v[24:27], v[8:11]\n\t" \
    "s_waitcnt lgkmcnt(4)\n\t" \
    "v_mfma_f32_16x16x32_bf16 v[12:15], v[140:143], v[24:27], v[12:15]\n\t" \
    "v_mfma_f32_16x16x32_bf16 v[0:3], v[136:139], v[28:31], v[0:3]\n\t" \
    "ds_read_b128 v[20:23], %[xp] offset:24576\n\t" \
    "v_mfma_f32_16x16x32_bf16 v[4:7], v[144:147], v[28:31], v[4:7]\n\t" \
    "ds_read_b128 v[24:27], %[xp] offset:25600\n\t" \
    "v_mfma_f32_16x16x32_bf16 v[8:11], v[136:139], v[32:35], v[8:11]\n\t" \
    "s_waitcnt lgkmcnt(4)\n\t" \
    "v_mfma_f32_16x16x32_bf16 v[12:15], v[144:147], v[32:35], v[12:15]\n\t" \
    "v_mfma_f32_16x16x32_bf16 v[0:3], v[148:151], v[36:39], v[0:3]\n\t" \
    "ds_read_b128 v[28:31], %[xp] offset:26624\n\t" \
    "v_mfma_f32_16x16x32_bf16 v[4:7], v[156:159], v[36:39], v[4:7]\n\t" \
    "ds_read_b128 v[32:35], %[xp] offset:27648\n\t" \
    "v_mfma_f32_16x16x32_bf16 v[8:11], v[148:151], v[40:43], v[8:11]\n\t" \
    "s_waitcnt lgkmcnt(4)\n\t" \
    "v_mfma_f32_16x16x32_bf16 v[12:15], v[156:159], v[40:43], v[12:15]\n\t" \
    "v_mfma_f32_16x16x32_bf16 v[0:3], v[152:155], v[44:47], v[0:3]\n\t" \
    "ds_read_b128 v[36:39], %[xp] offset:28672\n\t" \
    "v_mfma_f32_16x16x32_bf16 v[4:7], v[160:163], v[44:47], v[4:7]\n\t" \
    "ds_read_b128 v[40:43], %[xp] offset:29696\n\t" \
    "v_mfma_f32_16x16x32_bf16 v[8:11], v[152:155], v[48:51], v[8:11]\n\t" \
    "s_waitcnt lgkmcnt(4)\n\t" \
    "v_mfma_f32_16x16x32_bf16 v[12:15], v[160:163], v[48:51], v[12:15]\n\t" \
    "v_mfma_f32_16x16x32_bf16 v[0:3], v[164:167], v[20:23], v[0:3]\n\t" \
    "ds_read_b128 v[44:47], %[xp] offset:30720\n\t" \
    "v_mfma_f32_16x16x32_bf16 v[4:7], v[172:175], v[20:23], v[4:7]\n\t" \
    "ds_read_b128 v[48:51], %[xp] offset:31744\n\t" \
    "v_mfma_f32_16x16x32_bf16 v[8:11], v[164:167], v[24:27], v[8:11]\n\t" \
    "s_waitcnt lgkmcnt(4)\n\t" \
    "v_mfma_f32_16x16x32_bf16 v[12:15], v[172:175], v[24:27], v[12:15]\n\t" \
    "v_mfma_f32_16x16x32_bf16 v[0:3], v[168:171], v[28:31], v[0:3]\n\t" \
    "ds_read_b128 v[20:23], %[xp] offset:32768\n\t" \
    "v_mfma_f32_16x16x32_bf16 v[4:7], v[176:179], v[28:31], v[4:7]\n\t" \
    "ds_read_b128 v[24:27], %[xp] offset:33792\n\t" \
    "v_mfma_f32_16x16x32_bf16 v[8:11], v[168:171], v[32:35], v[8:11]\n\t" \
    "s_waitcnt lgkmcnt(4)\n\t" \
    "v_mfma_f32_16x16x32_bf16 v[12:15], v[176:179], v[32:35], v[12:15]\n\t" \
    "v_mfma_f32_16x16x32_bf16 v[0:3], v[180:183], v[36:39], v[0:3]\n\t" \
    "ds_read_b128 v[28:31], %[xp] offset:34816\n\t" \
    "v_mfma_f32_16x16x32_bf16 v[4:7], v[188:191], v[36:39], v[4:7]\n\t" \
    "ds_read_b128 v[32:35], %[xp] offset:35840\n\t" \
    "v_mfma_f32_16x16x32_bf16 v[8:11], v[180:183], v[40:43], v[8:11]\n\t" \
    "s_waitcnt lgkmcnt(4)\n\t" \
    "v_mfma_f32_16x16x32_bf16 v[12:15], v[188:191], v[40:43], v[12:15]\n\t" \
    "v_mfma_f32_16x16x32_bf16 v[0:3], v[184:187], v[44:47], v[0:3]\n\t" \
    "ds_read_b128 v[36:39], %[xp] offset:36864\n\t" \
    "v_mfma_f32_16x16x32_bf16 v[4:7], v[192:195], v[44:47], v[4:7]\n\t" \
    "ds_read_b128 v[40:43], %[xp] offset:37888\n\t" \
    "v_mfma_f32_16x16x32_bf16 v[8:11], v[184:187], v[48:51], v[8:11]\n\t" \
    "s_waitcnt lgkmcnt(4)\n\t" \
    "v_mfma_f32_16x16x32_bf16 v[12:15], v[192:195], v[48:51], v[12:15]\n\t" \
    "s_add_u32 %[sx], %[sx], 262144\n\t" \
    "s_add_u32 %[t], %[t], 1\n\t" \
    "s_waitcnt vmcnt(4)\n\t" \
    "s_barrier\n\t" \
    "s_setprio 0\n\t" \
    "s_nop 3\n\t" \
    "ds_write_b128 %[pa], v[0:3] offset:0\n\t" \
    "ds_write_b128 %[pa], v[4:7] offset:1024\n\t" \
    "ds_write_b128 %[pa], v[8:11] offset:2048\n\t" \
    "ds_write_b128 %[pa], v[12:15] offset:3072\n\t" \
    "s_sub_u32 %[a], %[t], 1\n\t" \
    "v_mov_b32 v53, %[tag]\n\t" \
    "v_mov_b32 v52, %[a]\n\t" \
    "s_sub_u32 %[b], %[t], 2\n\t" \
    "s_cmp_lt_u32 %[b], " STR(SQUICK) "\n\t" \
    "s_cselect_b64 exec, %[fm], 0\n\t" \
    "buffer_store_dwordx2 v[52:53], %[l16], %[rs], %[fo] offen sc1\n\t" \
    "s_mov_b64 exec, -1\n\t" \
    "s_add_u32 m0, %[sd], 16384\n\t" \
    "s_add_u32 %[a], %[sx], 278528\n\t" \
    "s_waitcnt lgkmcnt(0)\n\t" \
    "buffer_load_dwordx4 %[l16], %[rs], %[a] offen offset:0 sc1 lds\n\t" \
    "buffer_load_dwordx4 %[l16], %[rs], %[a] offen offset:1024 sc1 lds\n\t" \
    "buffer_load_dwordx4 %[l16], %[rs], %[a] offen offset:2048 sc1 lds\n\t" \
    "buffer_load_dwordx4 %[l16], %[rs], %[a] offen offset:3072 sc1 lds\n\t" \
    "v_mfma_f32_16x16x32_bf16 v[196:199], v[68:71], v[20:23], 0\n\t" \
    "ds_read_b128 v[44:47], %[xp] offset:38912\n\t" \
    "v_mfma_f32_16x16x32_bf16 v[200:203], v[76:79], v[20:23], 0\n\t" \
    "ds_read_b128 v[48:51], %[xp] offset:39936\n\t" \
    "v_mfma_f32_16x16x32_bf16 v[204:207], v[68:71], v[24:27], 0\n\t" \
    "v_mfma_f32_16x16x32_bf16 v[208:211], v[76:79], v[24:27], 0\n\t" \
    "v_mfma_f32_16x16x32_bf16 v[196:199], v[72:75], v[28:31], v[196:199]\n\t" \
    "ds_read_b128 v[20:23], %[xp] offset:40960\n\t" \
    "v_mfma_f32_16x16x32_bf16 v[200:203], v[80:83], v[28:31], v[200:203]\n\t" \
    "ds_read_b128 v[24:27], %[xp] offset:41984\n\t" \
    "v_mfma_f32_16x16x32_bf16 v[204:207], v[72:75], v[32:35], v[204:207]\n\t" \
    "v_mfma_f32_16x16x32_bf16 v[208:211], v[80:83], v[32:35], v[208:211]\n\t" \
    "v_mfma_f32_16x16x32_bf16 v[196:199], v[84:87], v[36:39], v[196:199]\n\t" \
    "ds_read_b128 v[28:31], %[xp] offset:43008\n\t" \
    "v_mfma_f32_16x16x32_bf16 v[200:203], v[92:95], v[36:39], v[200:203]\n\t" \
    "ds_read_b128 v[32:35], %[xp] offset:44032\n\t" \
    "v_mfma_f32_16x16x32_bf16 v[204:207], v[84:87], v[40:43], v[204:207]\n\t" \
    "s_waitcnt lgkmcnt(4)\n\t" \
    "v_mfma_f32_16x16x32_bf16 v[208:211], v[92:95], v[40:43], v[208:211]\n\t" \
    "v_mfma_f32_16x16x32_bf16 v[196:199], v[88:91], v[44:47], v[196:199]\n\t" \
    "ds_read_b128 v[36:39], %[xp] offset:45056\n\t" \
    "v_mfma_f32_16x16x32_bf16 v[200:203], v[96:99], v[44:47], v[200:203]\n\t" \
    "ds_read_b128 v[40:43], %[xp] offset:46080\n\t" \
    "v_mfma_f32_16x16x32_bf16 v[204:207], v[88:91], v[48:51], v[204:207]\n\t" \
    "s_waitcnt lgkmcnt(4)\n\t" \
    "v_mfma_f32_16x16x32_bf16 v[208:211], v[96:99], v[48:51], v[208:211]\n\t" \
    "v_mfma_f32_16x16x32_bf16 v[196:199], v[100:103], v[20:23], v[196:199]\n\t" \
    "ds_read_b128 v[44:47], %[xp] offset:47104\n\t" \
    "v_mfma_f32_16x16x32_bf16 v[200:203], v[108:111], v[20:23], v[200:203]\n\t" \
    "ds_read_b128 v[48:51], %[xp] offset:48128\n\t" \
    "v_mfma_f32_16x16x32_bf16 v[204:207], v[100:103], v[24:27], v[204:207]\n\t" \
    "s_waitcnt lgkmcnt(4)\n\t" \
    "v_mfma_f32_16x16x32_bf16 v[208:211], v[108:111], v[24:27], v[208:211]\n\t" \
    "v_mfma_f32_16x16x32_bf16 v[196:199], v[104:107], v[28:31], v[196:199]\n\t" \
    "ds_read_b128 v[20:23], %[xp] offset:49152\n\t" \
    "v_mfma_f32_16x16x32_bf16 v[200:203], v[112:115], v[28:31], v[200:203]\n\t" \
    "ds_read_b128 v[24:27], %[xp] offset:50176\n\t" \
    "v_mfma_f32_16x16x32_bf16 v[204:207], v[104:107], v[32:35], v[204:207]\n\t" \
    "s_waitcnt lgkmcnt(4)\n\t" \
    "v_mfma_f32_16x16x32_bf16 v[208:211], v[112:115], v[32:35], v[208:211]\n\t" \
    "v_mfma_f32_16x16x32_bf16 v[196:199], v[116:119], v[36:39], v[196:199]\n\t" \
    "ds_read_b128 v[28:31], %[xp] offset:51200\n\t" \
    "v_mfma_f32_16x16x32_bf16 v[200:203], v[124:127], v[36:39], v[200:203]\n\t" \
    "ds_read_b128 v[32:35], %[xp] offset:52224\n\t" \
    "v_mfma_f32_16x16x32_bf16 v[204:207], v[116:119], v[40:43], v[204:207]\n\t" \
    "s_waitcnt lgkmcnt(4)\n\t" \
    "v_mfma_f32_16x16x32_bf16 v[208:211], v[124:127], v[40:43], v[208:211]\n\t" \
    "v_mfma_f32_16x16x32_bf16 v[196:199], v[120:123], v[44:47], v[196:199]\n\t" \
    "ds_read_b128 v[36:39], %[xp] offset:53248\n\t" \
    "v_mfma_f32_16x16x32_bf16 v[200:203], v[128:131], v[44:47], v[200:203]\n\t" \
    "ds_read_b128 v[40:43], %[xp] offset:54272\n\t" \
    "v_mfma_f32_16x16x32_bf16 v[204:207], v[120:123], v[48:51], v[204:207]\n\t" \
    "s_waitcnt lgkmcnt(4)\n\t" \
    "v_mfma_f32_16x16x32_bf16 v[208:211], v[128:131], v[48:51], v[208:211]\n\t" \
    "s_waitcnt vmcnt(4)\n\t" \
    "s_barrier\n\t" \
    "s_setprio 0\n\t" \
    "s_sub_u32 %[a], %[t], 1\n\t" \
    "v_mov_b32 v53, %[tag]\n\t" \
    "v_mov_b32 v52, %[a]\n\t" \
    "s_cmp_gt_u32 %[t], 1\n\t" \
    "s_cselect_b64 exec, %[fm], 0\n\t" \
    "buffer_store_dwordx2 v[52:53], %[l16], %[rs], %[fo] offen sc1\n\t" \
    "s_mov_b64 exec, %[pm]\n\t" \
    "s_mov_b32 m0, %[pd]\n\t" \
    "s_add_u32 %[a], %[sx], %[po]\n\t" \
    "buffer_load_dwordx4 %[l16], %[rs], %[a] offen sc1 lds\n\t" \
    "s_mov_b64 exec, -1\n\t" \
    "s_add_u32 m0, %[sd], 32768\n\t" \
    "s_add_u32 %[a], %[sx], 524288\n\t" \
    "buffer_load_dwordx4 %[l16], %[rs], %[a] offen offset:0 sc1 lds\n\t" \
    "buffer_load_dwordx4 %[l16], %[rs], %[a] offen offset:1024 sc1 lds\n\t" \
    "buffer_load_dwordx4 %[l16], %[rs], %[a] offen offset:2048 sc1 lds\n\t" \
    "buffer_load_dwordx4 %[l16], %[rs], %[a] offen offset:3072 sc1 lds\n\t" \
    "v_mfma_f32_16x16x32_bf16 v[196:199], v[132:135], v[20:23], v[196:199]\n\t" \
    "ds_read_b128 v[44:47], %[xp] offset:55296\n\t" \
    "v_mfma_f32_16x16x32_bf16 v[200:203], v[140:143], v[20:23], v[200:203]\n\t" \
    "ds_read_b128 v[48:51], %[xp] offset:56320\n\t" \
    "v_mfma_f32_16x16x32_bf16 v[204:207], v[132:135], v[24:27], v[204:207]\n\t" \
    "s_waitcnt lgkmcnt(4)\n\t" \
    "v_mfma_f32_16x16x32_bf16 v[208:211], v[140:143], v[24:27], v[208:211]\n\t" \
    "v_mfma_f32_16x16x32_bf16 v[196:199], v[136:139], v[28:31], v[196:199]\n\t" \
    "ds_read_b128 v[20:23], %[xp] offset:57344\n\t" \
    "v_mfma_f32_16x16x32_bf16 v[200:203], v[144:147], v[28:31], v[200:203]\n\t" \
    "ds_read_b128 v[24:27], %[xp] offset:58368\n\t" \
    "v_mfma_f32_16x16x32_bf16 v[204:207], v[136:139], v[32:35], v[204:207]\n\t" \
    "s_waitcnt lgkmcnt(4)\n\t" \
    "v_mfma_f32_16x16x32_bf16 v[208:211], v[144:147], v[32:35], v[208:211]\n\t" \
    "v_mfma_f32_16x16x32_bf16 v[196:199], v[148:151], v[36:39], v[196:199]\n\t" \
    "ds_read_b128 v[28:31], %[xp] offset:59392\n\t" \
    "v_mfma_f32_16x16x32_bf16 v[200:203], v[156:159], v[36:39], v[200:203]\n\t" \
    "ds_read_b128 v[32:35], %[xp] offset:60416\n\t" \
    "v_mfma_f32_16x16x32_bf16 v[204:207], v[148:151], v[40:43], v[204:207]\n\t" \
    "s_waitcnt lgkmcnt(4)\n\t" \
    "v_mfma_f32_16x16x32_bf16 v[208:211], v[156:159], v[40:43], v[208:211]\n\t" \
    "v_mfma_f32_16x16x32_bf16 v[196:199], v[152:155], v[44:47], v[196:199]\n\t" \
    "ds_read_b128 v[36:39], %[xp] offset:61440\n\t" \
    "v_mfma_f32_16x16x32_bf16 v[200:203], v[160:163], v[44:47], v[200:203]\n\t" \
    "ds_read_b128 v[40:43], %[xp] offset:62464\n\t" \
    "v_mfma_f32_16x16x32_bf16 v[204:207], v[152:155], v[48:51], v[204:207]\n\t" \
    "s_waitcnt lgkmcnt(4)\n\t" \
    "v_mfma_f32_16x16x32_bf16 v[208:211], v[160:163], v[48:51], v[208:211]\n\t" \
    "v_mfma_f32_16x16x32_bf16 v[196:199], v[164:167], v[20:23], v[196:199]\n\t" \
    "ds_read_b128 v[44:47], %[xp] offset:63488\n\t" \
    "v_mfma_f32_16x16x32_bf16 v[200:203], v[172:175], v[20:23], v[200:203]\n\t" \
    "ds_read_b128 v[48:51], %[xp] offset:64512\n\t" \
    "v_mfma_f32_16x16x32_bf16 v[204:207], v[164:167], v[24:27], v[204:207]\n\t" \
    "s_waitcnt lgkmcnt(4)\n\t" \
    "v_mfma_f32_16x16x32_bf16 v[208:211], v[172:175], v[24:27], v[208:211]\n\t" \
    "v_mfma_f32_16x16x32_bf16 v[196:199], v[168:171], v[28:31], v[196:199]\n\t" \
    "ds_read_b128 v[20:23], %[xp] offset:0\n\t" \
    "v_mfma_f32_16x16x32_bf16 v[200:203], v[176:179], v[28:31], v[200:203]\n\t" \
    "ds_read_b128 v[24:27], %[xp] offset:1024\n\t" \
    "v_mfma_f32_16x16x32_bf16 v[204:207], v[168:171], v[32:35], v[204:207]\n\t" \
    "s_waitcnt lgkmcnt(4)\n\t" \
    "v_mfma_f32_16x16x32_bf16 v[208:211], v[176:179], v[32:35], v[208:211]\n\t" \
    "v_mfma_f32_16x16x32_bf16 v[196:199], v[180:183], v[36:39], v[196:199]\n\t" \
    "ds_read_b128 v[28:31], %[xp] offset:2048\n\t" \
    "v_mfma_f32_16x16x32_bf16 v[200:203], v[188:191], v[36:39], v[200:203]\n\t" \
    "ds_read_b128 v[32:35], %[xp] offset:3072\n\t" \
    "v_mfma_f32_16x16x32_bf16 v[204:207], v[180:183], v[40:43], v[204:207]\n\t" \
    "s_waitcnt lgkmcnt(4)\n\t" \
    "v_mfma_f32_16x16x32_bf16 v[208:211], v[188:191], v[40:43], v[208:211]\n\t" \
    "v_mfma_f32_16x16x32_bf16 v[196:199], v[184:187], v[44:47], v[196:199]\n\t" \
    "ds_read_b128 v[36:39], %[xp] offset:4096\n\t" \
    "v_mfma_f32_16x16x32_bf16 v[200:203], v[192:195], v[44:47], v[200:203]\n\t" \
    "ds_read_b128 v[40:43], %[xp] offset:5120\n\t" \
    "v_mfma_f32_16x16x32_bf16 v[204:207], v[184:187], v[48:51], v[204:207]\n\t" \
    "s_waitcnt lgkmcnt(4)\n\t" \
    "v_mfma_f32_16x16x32_bf16 v[208:211], v[192:195], v[48:51], v[208:211]\n\t" \
    "s_add_u32 %[sx], %[sx], 262144\n\t" \
    "s_add_u32 %[t], %[t], 1\n\t" \
    "s_waitcnt vmcnt(4)\n\t" \
    "s_barrier\n\t" \
    "s_cmp_lt_u32 %[t], %[te]\n\t" \
    "s_cbranch_scc1 L0_%=\n\t" \
    "s_cmp_eq_u32 %[fin], %[te]\n\t" \
    "s_cbranch_scc0 E_%=\n\t" \
    "s_nop 4\n\t" \
    "ds_write_b128 %[pa], v[196:199] offset:0\n\t" \
    "ds_write_b128 %[pa], v[200:203] offset:1024\n\t" \
    "ds_write_b128 %[pa], v[204:207] offset:2048\n\t" \
    "ds_write_b128 %[pa], v[208:211] offset:3072\n\t" \
    "s_waitcnt lgkmcnt(0)\n\t" \
    "s_barrier\n\t" \
    "s_branch E_%=\n\t" \
    "L1_%=:\n\t" \
    "s_setprio 1\n\t" \
    "s_add_u32 m0, %[sd], 49152\n\t" \
    "s_add_u32 %[a], %[sx], 278528\n\t" \
    "v_mfma_f32_16x16x32_bf16 v[0:3], v[68:71], v[20:23], 0\n\t" \
    "ds_read_b128 v[44:47], %[xp] offset:6144\n\t" \
    "v_mfma_f32_16x16x32_bf16 v[4:7], v[76:79], v[20:23], 0\n\t" \
    "ds_read_b128 v[48:51], %[xp] offset:7168\n\t" \
    "v_mfma_f32_16x16x32_bf16 v[8:11], v[68:71], v[24:27], 0\n\t" \
    "s_waitcnt lgkmcnt(4)\n\t" \
    "v_mfma_f32_16x16x32_bf16 v[12:15], v[76:79], v[24:27], 0\n\t" \
    "v_mfma_f32_16x16x32_bf16 v[0:3], v[72:75], v[28:31], v[0:3]\n\t" \
    "ds_read_b128 v[20:23], %[xp] offset:8192\n\t" \
    "v_mfma_f32_16x16x32_bf16 v[4:7], v[80:83], v[28:31], v[4:7]\n\t" \
    "ds_read_b128 v[24:27], %[xp] offset:9216\n\t" \
    "v_mfma_f32_16x16x32_bf16 v[8:11], v[72:75], v[32:35], v[8:11]\n\t" \
    "s_waitcnt lgkmcnt(4)\n\t" \
    "v_mfma_f32_16x16x32_bf16 v[12:15], v[80:83], v[32:35], v[12:15]\n\t" \
    "v_mfma_f32_16x16x32_bf16 v[0:3], v[84:87], v[36:39], v[0:3]\n\t" \
    "ds_read_b128 v[28:31], %[xp] offset:10240\n\t" \
    "v_mfma_f32_16x16x32_bf16 v[4:7], v[92:95], v[36:39], v[4:7]\n\t" \
    "ds_read_b128 v[32:35], %[xp] offset:11264\n\t" \
    "v_mfma_f32_16x16x32_bf16 v[8:11], v[84:87], v[40:43], v[8:11]\n\t" \
    "s_waitcnt lgkmcnt(4)\n\t" \
    "v_mfma_f32_16x16x32_bf16 v[12:15], v[92:95], v[40:43], v[12:15]\n\t" \
    "v_mfma_f32_16x16x32_bf16 v[0:3], v[88:91], v[44:47], v[0:3]\n\t" \
    "ds_read_b128 v[36:39], %[xp] offset:12288\n\t" \
    "v_mfma_f32_16x16x32_bf16 v[4:7], v[96:99], v[44:47], v[4:7]\n\t" \
    "ds_read_b128 v[40:43], %[xp] offset:13312\n\t" \
    "v_mfma_f32_16x16x32_bf16 v[8:11], v[88:91], v[48:51], v[8:11]\n\t" \
    "s_waitcnt lgkmcnt(4)\n\t" \
    "v_mfma_f32_16x16x32_bf16 v[12:15], v[96:99], v[48:51], v[12:15]\n\t" \
    "v_mfma_f32_16x16x32_bf16 v[0:3], v[100:103], v[20:23], v[0:3]\n\t" \
    "ds_read_b128 v[44:47], %[xp] offset:14336\n\t" \
    "v_mfma_f32_16x16x32_bf16 v[4:7], v[108:111], v[20:23], v[4:7]\n\t" \
    "ds_read_b128 v[48:51], %[xp] offset:15360\n\t" \
    "v_mfma_f32_16x16x32_bf16 v[8:11], v[100:103], v[24:27], v[8:11]\n\t" \
    "s_waitcnt lgkmcnt(4)\n\t" \
    "v_mfma_f32_16x16x32_bf16 v[12:15], v[108:111], v[24:27], v[12:15]\n\t" \
    "buffer_load_dwordx4 %[l16], %[rs], %[a] offen offset:0 sc1 lds\n\t" \
    "v_mfma_f32_16x16x32_bf16 v[0:3], v[104:107], v[28:31], v[0:3]\n\t" \
    "ds_read_b128 v[20:23], %[xp] offset:16384\n\t" \
    "v_mfma_f32_16x16x32_bf16 v[4:7], v[112:115], v[28:31], v[4:7]\n\t" \
    "ds_read_b128 v[24:27], %[xp] offset:17408\n\t" \
    "v_mfma_f32_16x16x32_bf16 v[8:11], v[104:107], v[32:35], v[8:11]\n\t" \
    "s_waitcnt lgkmcnt(4)\n\t" \
    "v_mfma_f32_16x16x32_bf16 v[12:15], v[112:115], v[32:35], v[12:15]\n\t" \
    "buffer_load_dwordx4 %[l16], %[rs], %[a] offen offset:1024 sc1 lds\n\t" \
    "v_mfma_f32_16x16x32_bf16 v[0:3], v[116:119], v[36:39], v[0:3]\n\t" \
    "ds_read_b128 v[28:31], %[xp] offset:18432\n\t" \
    "v_mfma_f32_16x16x32_bf16 v[4:7], v[124:127], v[36:39], v[4:7]\n\t" \
    "ds_read_b128 v[32:35], %[xp] offset:19456\n\t" \
    "v_mfma_f32_16x16x32_bf16 v[8:11], v[116:119], v[40:43], v[8:11]\n\t" \
    "s_waitcnt lgkmcnt(4)\n\t" \
    "v_mfma_f32_16x16x32_bf16 v[12:15], v[124:127], v[40:43], v[12:15]\n\t" \
    "buffer_load_dwordx4 %[l16], %[rs], %[a] offen offset:2048 sc1 lds\n\t" \
    "v_mfma_f32_16x16x32_bf16 v[0:3], v[120:123], v[44:47], v[0:3]\n\t" \
    "ds_read_b128 v[36:39], %[xp] offset:20480\n\t" \
    "v_mfma_f32_16x16x32_bf16 v[4:7], v[128:131], v[44:47], v[4:7]\n\t" \
    "ds_read_b128 v[40:43], %[xp] offset:21504\n\t" \
    "v_mfma_f32_16x16x32_bf16 v[8:11], v[120:123], v[48:51], v[8:11]\n\t" \
    "s_waitcnt lgkmcnt(4)\n\t" \
    "v_mfma_f32_16x16x32_bf16 v[12:15], v[128:131], v[48:51], v[12:15]\n\t" \
    "buffer_load_dwordx4 %[l16], %[rs], %[a] offen offset:3072 sc1 lds\n\t" \
    "s_waitcnt vmcnt(4)\n\t" \
    "s_barrier\n\t" \
    "s_setprio 1\n\t" \
    "s_add_u32 m0, %[sd], 0\n\t" \
    "s_add_u32 %[a], %[sx], 524288\n\t" \
    "s_add_u32 %[b], %[oo], 0x1000\n\t" \
    "s_cmp_lg_u32 %[t], 0\n\t" \
    "s_cselect_b64 %[em], -1, 0\n\t" \
    "v_mfma_f32_16x16x32_bf16 v[0:3], v[132:135], v[20:23], v[0:3]\n\t" \
    "ds_read_b128 v[44:47], %[xp] offset:22528\n\t" \
    "v_mfma_f32_16x16x32_bf16 v[4:7], v[140:143], v[20:23], v[4:7]\n\t" \
    "ds_read_b128 v[48:51], %[xp] offset:23552\n\t" \
    "v_mfma_f32_16x16x32_bf16 v[8:11], v[132:135], v[24:27], v[8:11]\n\t" \
    "ds_read_b128 v[52:55], %[pa] offset:0\n\t" \
    "s_waitcnt lgkmcnt(5)\n\t" \
    "v_mfma_f32_16x16x32_bf16 v[12:15], v[140:143], v[24:27], v[12:15]\n\t" \
    "buffer_load_dwordx4 %[l16], %[rs], %[a] offen offset:0 sc1 lds\n\t" \
    "v_mfma_f32_16x16x32_bf16 v[0:3], v[136:139], v[28:31], v[0:3]\n\t" \
    "ds_read_b128 v[20:23], %[xp] offset:24576\n\t" \
    "v_mfma_f32_16x16x32_bf16 v[4:7], v[144:147], v[28:31], v[4:7]\n\t" \
    "ds_read_b128 v[24:27], %[xp] offset:25600\n\t" \
    "v_mfma_f32_16x16x32_bf16 v[8:11], v[136:139], v[32:35], v[8:11]\n\t" \
    "ds_read_b128 v[56:59], %[pa] offset:1024\n\t" \
    "s_waitcnt lgkmcnt(6)\n\t" \
    "v_mfma_f32_16x16x32_bf16 v[12:15], v[144:147], v[32:35], v[12:15]\n\t" \
    "buffer_load_dwordx4 %[l16], %[rs], %[a] offen offset:1024 sc1 lds\n\t" \
    "v_mfma_f32_16x16x32_bf16 v[0:3], v[148:151], v[36:39], v[0:3]\n\t" \
    "ds_read_b128 v[28:31], %[xp] offset:26624\n\t" \
    "v_mfma_f32_16x16x32_bf16 v[4:7], v[156:159], v[36:39], v[4:7]\n\t" \
    "ds_read_b128 v[32:35], %[xp] offset:27648\n\t" \
    "v_mfma_f32_16x16x32_bf16 v[8:11], v[148:151], v[40:43], v[8:11]\n\t" \
    "ds_read_b128 v[60:63], %[pa] offset:2048\n\t" \
    "s_waitcnt lgkmcnt(7)\n\t" \
    "v_mfma_f32_16x16x32_bf16 v[12:15], v[156:159], v[40:43], v[12:15]\n\t" \
    "buffer_load_dwordx4 %[l16], %[rs], %[a] offen offset:2048 sc1 lds\n\t" \
    "v_mfma_f32_16x16x32_bf16 v[0:3], v[152:155], v[44:47], v[0:3]\n\t" \
    "ds_read_b128 v[36:39], %[xp] offset:28672\n\t" \
    "s_waitcnt lgkmcnt(7)\n\t" \
    "v_add_f32 v196, v196, v52\n\t" \
    "v_mfma_f32_16x16x32_bf16 v[4:7], v[160:163], v[44:47], v[4:7]\n\t" \
    "ds_read_b128 v[40:43], %[xp] offset:29696\n\t" \
    "v_add_f32 v197, v197, v53\n\t" \
    "v_mfma_f32_16x16x32_bf16 v[8:11], v[152:155], v[48:51], v[8:11]\n\t" \
    "ds_read_b128 v[64:67], %[pa] offset:3072\n\t" \
    "s_waitcnt lgkmcnt(7)\n\t" \
    "v_mfma_f32_16x16x32_bf16 v[12:15], v[160:163], v[48:51], v[12:15]\n\t" \
    "buffer_load_dwordx4 %[l16], %[rs], %[a] offen offset:3072 sc1 lds\n\t" \
    "v_add_f32 v198, v198, v54\n\t" \
    "v_add_f32 v199, v199, v55\n\t" \
    "v_mfma_f32_16x16x32_bf16 v[0:3], v[164:167], v[20:23], v[0:3]\n\t" \
    "ds_read_b128 v[44:47], %[xp] offset:30720\n\t" \
    "s_waitcnt lgkmcnt(7)\n\t" \
    "v_add_f32 v200, v200, v56\n\t" \
    "v_mfma_f32_16x16x32_bf16 v[4:7], v[172:175], v[20:23], v[4:7]\n\t" \
    "ds_read_b128 v[48:51], %[xp] offset:31744\n\t" \
    "v_add_f32 v201, v201, v57\n\t" \
    "v_mfma_f32_16x16x32_bf16 v[8:11], v[164:167], v[24:27], v[8:11]\n\t" \
    "v_add_f32 v202, v202, v58\n\t" \
    "s_waitcnt lgkmcnt(6)\n\t" \
    "v_mfma_f32_16x16x32_bf16 v[12:15], v[172:175], v[24:27], v[12:15]\n\t" \
    "v_add_f32 v203, v203, v59\n\t" \
    "s_waitcnt lgkmcnt(5)\n\t" \
    "v_add_f32 v204, v204, v60\n\t" \
    "v_add_f32 v205, v205, v61\n\t" \
    "v_mfma_f32_16x16x32_bf16 v[0:3], v[168:171], v[28:31], v[0:3]\n\t" \
    "ds_read_b128 v[20:23], %[xp] offset:32768\n\t" \
    "v_add_f32 v206, v206, v62\n\t" \
    "v_mfma_f32_16x16x32_bf16 v[4:7], v[176:179], v[28:31], v[4:7]\n\t" \
    "ds_read_b128 v[24:27], %[xp] offset:33792\n\t" \
    "v_add_f32 v207, v207, v63\n\t" \
    "v_mfma_f32_16x16x32_bf16 v[8:11], v[168:171], v[32:35], v[8:11]\n\t" \
    "s_waitcnt lgkmcnt(4)\n\t" \
    "v_add_f32 v208, v208, v64\n\t" \
    "v_mfma_f32_16x16x32_bf16 v[12:15], v[176:179], v[32:35], v[12:15]\n\t" \
    "v_add_f32 v209, v209, v65\n\t" \
    "v_add_f32 v210, v210, v66\n\t" \
    "v_add_f32 v211, v211, v67\n\t" \
    "v_mfma_f32_16x16x32_bf16 v[0:3], v[180:183], v[36:39], v[0:3]\n\t" \
    "ds_read_b128 v[28:31], %[xp] offset:34816\n\t" \
    "v_cvt_pk_bf16_f32 v52, v196, v200\n\t" \
    "v_mfma_f32_16x16x32_bf16 v[4:7], v[188:191], v[36:39], v[4:7]\n\t" \
    "ds_read_b128 v[32:35], %[xp] offset:35840\n\t" \
    "v_cvt_pk_bf16_f32 v53, v197, v201\n\t" \
    "v_mfma_f32_16x16x32_bf16 v[8:11], v[180:183], v[40:43], v[8:11]\n\t" \
    "v_cvt_pk_bf16_f32 v54, v198, v202\n\t" \
    "s_waitcnt lgkmcnt(4)\n\t" \
    "v_mfma_f32_16x16x32_bf16 v[12:15], v[188:191], v[40:43], v[12:15]\n\t" \
    "v_cvt_pk_bf16_f32 v55, v199, v203\n\t" \
    "v_cvt_pk_bf16_f32 v56, v204, v208\n\t" \
    "v_cvt_pk_bf16_f32 v57, v205, v209\n\t" \
    "v_mfma_f32_16x16x32_bf16 v[0:3], v[184:187], v[44:47], v[0:3]\n\t" \
    "ds_read_b128 v[36:39], %[xp] offset:36864\n\t" \
    "v_cvt_pk_bf16_f32 v58, v206, v210\n\t" \
    "v_mfma_f32_16x16x32_bf16 v[4:7], v[192:195], v[44:47], v[4:7]\n\t" \
    "ds_read_b128 v[40:43], %[xp] offset:37888\n\t" \
    "v_cvt_pk_bf16_f32 v59, v207, v211\n\t" \
    "v_mfma_f32_16x16x32_bf16 v[8:11], v[184:187], v[48:51], v[8:11]\n\t" \
    "s_waitcnt lgkmcnt(4)\n\t" \
    "v_mfma_f32_16x16x32_bf16 v[12:15], v[192:195], v[48:51], v[12:15]\n\t" \
    "s_mov_b64 exec, %[em]\n\t" \
    "buffer_store_dwordx4 v[52:55], %[l16], %[rs], %[oo] offen sc1\n\t" \
    "buffer_store_dwordx4 v[56:59], %[l16], %[rs], %[b] offen sc1\n\t" \
    "s_mov_b64 exec, -1\n\t" \
    "s_add_u32 %[sx], %[sx], 262144\n\t" \
    "s_add_u32 %[t], %[t], 1\n\t" \
    "s_add_u32 %[oo], %[oo], 262144\n\t" \
    "s_waitcnt vmcnt(6)\n\t" \
    "s_cmp_gt_u32 %[t], " STR(SQUICK) " + 1\n\t" \
    "s_cbranch_scc1 B0_%=\n\t" \
    "s_waitcnt vmcnt(0)\n\t" \
    "B0_%=:\n\t" \
    "s_barrier\n\t" \
    "s_setprio 1\n\t" \
    "s_add_u32 m0, %[sd], 16384\n\t" \
    "s_add_u32 %[a], %[sx], 278528\n\t" \
    "v_mfma_f32_16x16x32_bf16 v[196:199], v[68:71], v[20:23], 0\n\t" \
    "ds_read_b128 v[44:47], %[xp] offset:38912\n\t" \
    "v_mfma_f32_16x16x32_bf16 v[200:203], v[76:79], v[20:23], 0\n\t" \
    "ds_read_b128 v[48:51], %[xp] offset:39936\n\t" \
    "v_mfma_f32_16x16x32_bf16 v[204:207], v[68:71], v[24:27], 0\n\t" \
    "s_waitcnt lgkmcnt(4)\n\t" \
    "v_mfma_f32_16x16x32_bf16 v[208:211], v[76:79], v[24:27], 0\n\t" \
    "v_mfma_f32_16x16x32_bf16 v[196:199], v[72:75], v[28:31], v[196:199]\n\t" \
    "ds_read_b128 v[20:23], %[xp] offset:40960\n\t" \
    "v_mfma_f32_16x16x32_bf16 v[200:203], v[80:83], v[28:31], v[200:203]\n\t" \
    "ds_read_b128 v[24:27], %[xp] offset:41984\n\t" \
    "v_mfma_f32_16x16x32_bf16 v[204:207], v[72:75], v[32:35], v[204:207]\n\t" \
    "s_waitcnt lgkmcnt(4)\n\t" \
    "v_mfma_f32_16x16x32_bf16 v[208:211], v[80:83], v[32:35], v[208:211]\n\t" \
    "v_mfma_f32_16x16x32_bf16 v[196:199], v[84:87], v[36:39], v[196:199]\n\t" \
    "ds_read_b128 v[28:31], %[xp] offset:43008\n\t" \
    "v_mfma_f32_16x16x32_bf16 v[200:203], v[92:95], v[36:39], v[200:203]\n\t" \
    "ds_read_b128 v[32:35], %[xp] offset:44032\n\t" \
    "v_mfma_f32_16x16x32_bf16 v[204:207], v[84:87], v[40:43], v[204:207]\n\t" \
    "s_waitcnt lgkmcnt(4)\n\t" \
    "v_mfma_f32_16x16x32_bf16 v[208:211], v[92:95], v[40:43], v[208:211]\n\t" \
    "v_mfma_f32_16x16x32_bf16 v[196:199], v[88:91], v[44:47], v[196:199]\n\t" \
    "ds_read_b128 v[36:39], %[xp] offset:45056\n\t" \
    "v_mfma_f32_16x16x32_bf16 v[200:203], v[96:99], v[44:47], v[200:203]\n\t" \
    "ds_read_b128 v[40:43], %[xp] offset:46080\n\t" \
    "v_mfma_f32_16x16x32_bf16 v[204:207], v[88:91], v[48:51], v[204:207]\n\t" \
    "s_waitcnt lgkmcnt(4)\n\t" \
    "v_mfma_f32_16x16x32_bf16 v[208:211], v[96:99], v[48:51], v[208:211]\n\t" \
    "v_mfma_f32_16x16x32_bf16 v[196:199], v[100:103], v[20:23], v[196:199]\n\t" \
    "ds_read_b128 v[44:47], %[xp] offset:47104\n\t" \
    "v_mfma_f32_16x16x32_bf16 v[200:203], v[108:111], v[20:23], v[200:203]\n\t" \
    "ds_read_b128 v[48:51], %[xp] offset:48128\n\t" \
    "v_mfma_f32_16x16x32_bf16 v[204:207], v[100:103], v[24:27], v[204:207]\n\t" \
    "s_waitcnt lgkmcnt(4)\n\t" \
    "v_mfma_f32_16x16x32_bf16 v[208:211], v[108:111], v[24:27], v[208:211]\n\t" \
    "buffer_load_dwordx4 %[l16], %[rs], %[a] offen offset:0 sc1 lds\n\t" \
    "v_mfma_f32_16x16x32_bf16 v[196:199], v[104:107], v[28:31], v[196:199]\n\t" \
    "ds_read_b128 v[20:23], %[xp] offset:49152\n\t" \
    "v_mfma_f32_16x16x32_bf16 v[200:203], v[112:115], v[28:31], v[200:203]\n\t" \
    "ds_read_b128 v[24:27], %[xp] offset:50176\n\t" \
    "v_mfma_f32_16x16x32_bf16 v[204:207], v[104:107], v[32:35], v[204:207]\n\t" \
    "s_waitcnt lgkmcnt(4)\n\t" \
    "v_mfma_f32_16x16x32_bf16 v[208:211], v[112:115], v[32:35], v[208:211]\n\t" \
    "buffer_load_dwordx4 %[l16], %[rs], %[a] offen offset:1024 sc1 lds\n\t" \
    "v_mfma_f32_16x16x32_bf16 v[196:199], v[116:119], v[36:39], v[196:199]\n\t" \
    "ds_read_b128 v[28:31], %[xp] offset:51200\n\t" \
    "v_mfma_f32_16x16x32_bf16 v[200:203], v[124:127], v[36:39], v[200:203]\n\t" \
    "ds_read_b128 v[32:35], %[xp] offset:52224\n\t" \
    "v_mfma_f32_16x16x32_bf16 v[204:207], v[116:119], v[40:43], v[204:207]\n\t" \
    "s_waitcnt lgkmcnt(4)\n\t" \
    "v_mfma_f32_16x16x32_bf16 v[208:211], v[124:127], v[40:43], v[208:211]\n\t" \
    "buffer_load_dwordx4 %[l16], %[rs], %[a] offen offset:2048 sc1 lds\n\t" \
    "v_mfma_f32_16x16x32_bf16 v[196:199], v[120:123], v[44:47], v[196:199]\n\t" \
    "ds_read_b128 v[36:39], %[xp] offset:53248\n\t" \
    "v_mfma_f32_16x16x32_bf16 v[200:203], v[128:131], v[44:47], v[200:203]\n\t" \
    "ds_read_b128 v[40:43], %[xp] offset:54272\n\t" \
    "v_mfma_f32_16x16x32_bf16 v[204:207], v[120:123], v[48:51], v[204:207]\n\t" \
    "s_waitcnt lgkmcnt(4)\n\t" \
    "v_mfma_f32_16x16x32_bf16 v[208:211], v[128:131], v[48:51], v[208:211]\n\t" \
    "buffer_load_dwordx4 %[l16], %[rs], %[a] offen offset:3072 sc1 lds\n\t" \
    "s_waitcnt vmcnt(4)\n\t" \
    "s_barrier\n\t" \
    "s_setprio 1\n\t" \
    "s_add_u32 m0, %[sd], 32768\n\t" \
    "s_add_u32 %[a], %[sx], 524288\n\t" \
    "s_add_u32 %[b], %[oo], 0x1000\n\t" \
    "s_cmp_lg_u32 %[t], 0\n\t" \
    "s_cselect_b64 %[em], -1, 0\n\t" \
    "v_mfma_f32_16x16x32_bf16 v[196:199], v[132:135], v[20:23], v[196:199]\n\t" \
    "ds_read_b128 v[44:47], %[xp] offset:55296\n\t" \
    "v_mfma_f32_16x16x32_bf16 v[200:203], v[140:143], v[20:23], v[200:203]\n\t" \
    "ds_read_b128 v[48:51], %[xp] offset:56320\n\t" \
    "v_mfma_f32_16x16x32_bf16 v[204:207], v[132:135], v[24:27], v[204:207]\n\t" \
    "ds_read_b128 v[52:55], %[pa] offset:0\n\t" \
    "s_waitcnt lgkmcnt(5)\n\t" \
    "v_mfma_f32_16x16x32_bf16 v[208:211], v[140:143], v[24:27], v[208:211]\n\t" \
    "buffer_load_dwordx4 %[l16], %[rs], %[a] offen offset:0 sc1 lds\n\t" \
    "v_mfma_f32_16x16x32_bf16 v[196:199], v[136:139], v[28:31], v[196:199]\n\t" \
    "ds_read_b128 v[20:23], %[xp] offset:57344\n\t" \
    "v_mfma_f32_16x16x32_bf16 v[200:203], v[144:147], v[28:31], v[200:203]\n\t" \
    "ds_read_b128 v[24:27], %[xp] offset:58368\n\t" \
    "v_mfma_f32_16x16x32_bf16 v[204:207], v[136:139], v[32:35], v[204:207]\n\t" \
    "ds_read_b128 v[56:59], %[pa] offset:1024\n\t" \
    "s_waitcnt lgkmcnt(6)\n\t" \
    "v_mfma_f32_16x16x32_bf16 v[208:211], v[144:147], v[32:35], v[208:211]\n\t" \
    "buffer_load_dwordx4 %[l16], %[rs], %[a] offen offset:1024 sc1 lds\n\t" \
    "v_mfma_f32_16x16x32_bf16 v[196:199], v[148:151], v[36:39], v[196:199]\n\t" \
    "ds_read_b128 v[28:31], %[xp] offset:59392\n\t" \
    "v_mfma_f32_16x16x32_bf16 v[200:203], v[156:159], v[36:39], v[200:203]\n\t" \
    "ds_read_b128 v[32:35], %[xp] offset:60416\n\t" \
    "v_mfma_f32_16x16x32_bf16 v[204:207], v[148:151], v[40:43], v[204:207]\n\t" \
    "ds_read_b128 v[60:63], %[pa] offset:2048\n\t" \
    "s_waitcnt lgkmcnt(7)\n\t" \
    "v_mfma_f32_16x16x32_bf16 v[208:211], v[156:159], v[40:43], v[208:211]\n\t" \
    "buffer_load_dwordx4 %[l16], %[rs], %[a] offen offset:2048 sc1 lds\n\t" \
    "v_mfma_f32_16x16x32_bf16 v[196:199], v[152:155], v[44:47], v[196:199]\n\t" \
    "ds_read_b128 v[36:39], %[xp] offset:61440\n\t" \
    "s_waitcnt lgkmcnt(7)\n\t" \
    "v_add_f32 v0, v0, v52\n\t" \
    "v_mfma_f32_16x16x32_bf16 v[200:203], v[160:163], v[44:47], v[200:203]\n\t" \
    "ds_read_b128 v[40:43], %[xp] offset:62464\n\t" \
    "v_add_f32 v1, v1, v53\n\t" \
    "v_mfma_f32_16x16x32_bf16 v[204:207], v[152:155], v[48:51], v[204:207]\n\t" \
    "ds_read_b128 v[64:67], %[pa] offset:3072\n\t" \
    "s_waitcnt lgkmcnt(7)\n\t" \
    "v_mfma_f32_16x16x32_bf16 v[208:211], v[160:163], v[48:51], v[208:211]\n\t" \
    "buffer_load_dwordx4 %[l16], %[rs], %[a] offen offset:3072 sc1 lds\n\t" \
    "v_add_f32 v2, v2, v54\n\t" \
    "v_add_f32 v3, v3, v55\n\t" \
    "v_mfma_f32_16x16x32_bf16 v[196:199], v[164:167], v[20:23], v[196:199]\n\t" \
    "ds_read_b128 v[44:47], %[xp] offset:63488\n\t" \
    "s_waitcnt lgkmcnt(7)\n\t" \
    "v_add_f32 v4, v4, v56\n\t" \
    "v_mfma_f32_16x16x32_bf16 v[200:203], v[172:175], v[20:23], v[200:203]\n\t" \
    "ds_read_b128 v[48:51], %[xp] offset:64512\n\t" \
    "v_add_f32 v5, v5, v57\n\t" \
    "v_mfma_f32_16x16x32_bf16 v[204:207], v[164:167], v[24:27], v[204:207]\n\t" \
    "v_add_f32 v6, v6, v58\n\t" \
    "s_waitcnt lgkmcnt(6)\n\t" \
    "v_mfma_f32_16x16x32_bf16 v[208:211], v[172:175], v[24:27], v[208:211]\n\t" \
    "v_add_f32 v7, v7, v59\n\t" \
    "s_waitcnt lgkmcnt(5)\n\t" \
    "v_add_f32 v8, v8, v60\n\t" \
    "v_add_f32 v9, v9, v61\n\t" \
    "v_mfma_f32_16x16x32_bf16 v[196:199], v[168:171], v[28:31], v[196:199]\n\t" \
    "ds_read_b128 v[20:23], %[xp] offset:0\n\t" \
    "v_add_f32 v10, v10, v62\n\t" \
    "v_mfma_f32_16x16x32_bf16 v[200:203], v[176:179], v[28:31], v[200:203]\n\t" \
    "ds_read_b128 v[24:27], %[xp] offset:1024\n\t" \
    "v_add_f32 v11, v11, v63\n\t" \
    "v_mfma_f32_16x16x32_bf16 v[204:207], v[168:171], v[32:35], v[204:207]\n\t" \
    "s_waitcnt lgkmcnt(4)\n\t" \
    "v_add_f32 v12, v12, v64\n\t" \
    "v_mfma_f32_16x16x32_bf16 v[208:211], v[176:179], v[32:35], v[208:211]\n\t" \
    "v_add_f32 v13, v13, v65\n\t" \
    "v_add_f32 v14, v14, v66\n\t" \
    "v_add_f32 v15, v15, v67\n\t" \
    "v_mfma_f32_16x16x32_bf16 v[196:199], v[180:183], v[36:39], v[196:199]\n\t" \
    "ds_read_b128 v[28:31], %[xp] offset:2048\n\t" \
    "v_cvt_pk_bf16_f32 v52, v0, v4\n\t" \
    "v_mfma_f32_16x16x32_bf16 v[200:203], v[188:191], v[36:39], v[200:203]\n\t" \
    "ds_read_b128 v[32:35], %[xp] offset:3072\n\t" \
    "v_cvt_pk_bf16_f32 v53, v1, v5\n\t" \
    "v_mfma_f32_16x16x32_bf16 v[204:207], v[180:183], v[40:43], v[204:207]\n\t" \
    "v_cvt_pk_bf16_f32 v54, v2, v6\n\t" \
    "s_waitcnt lgkmcnt(4)\n\t" \
    "v_mfma_f32_16x16x32_bf16 v[208:211], v[188:191], v[40:43], v[208:211]\n\t" \
    "v_cvt_pk_bf16_f32 v55, v3, v7\n\t" \
    "v_cvt_pk_bf16_f32 v56, v8, v12\n\t" \
    "v_cvt_pk_bf16_f32 v57, v9, v13\n\t" \
    "v_mfma_f32_16x16x32_bf16 v[196:199], v[184:187], v[44:47], v[196:199]\n\t" \
    "ds_read_b128 v[36:39], %[xp] offset:4096\n\t" \
    "v_cvt_pk_bf16_f32 v58, v10, v14\n\t" \
    "v_mfma_f32_16x16x32_bf16 v[200:203], v[192:195], v[44:47], v[200:203]\n\t" \
    "ds_read_b128 v[40:43], %[xp] offset:5120\n\t" \
    "v_cvt_pk_bf16_f32 v59, v11, v15\n\t" \
    "v_mfma_f32_16x16x32_bf16 v[204:207], v[184:187], v[48:51], v[204:207]\n\t" \
    "s_waitcnt lgkmcnt(4)\n\t" \
    "v_mfma_f32_16x16x32_bf16 v[208:211], v[192:195], v[48:51], v[208:211]\n\t" \
    "s_mov_b64 exec, %[em]\n\t" \
    "buffer_store_dwordx4 v[52:55], %[l16], %[rs], %[oo] offen sc1\n\t" \
    "buffer_store_dwordx4 v[56:59], %[l16], %[rs], %[b] offen sc1\n\t" \
    "s_mov_b64 exec, -1\n\t" \
    "s_add_u32 %[sx], %[sx], 262144\n\t" \
    "s_add_u32 %[t], %[t], 1\n\t" \
    "s_add_u32 %[oo], %[oo], 262144\n\t" \
    "s_waitcnt vmcnt(6)\n\t" \
    "s_cmp_gt_u32 %[t], " STR(SQUICK) " + 1\n\t" \
    "s_cbranch_scc1 B1_%=\n\t" \
    "s_waitcnt vmcnt(0)\n\t" \
    "B1_%=:\n\t" \
    "s_barrier\n\t" \
    "s_cmp_lt_u32 %[t], %[te]\n\t" \
    "s_cbranch_scc1 L1_%=\n\t" \
    "s_cmp_eq_u32 %[fin], %[te]\n\t" \
    "s_cbranch_scc0 E_%=\n\t" \
    "s_nop 4\n\t" \
    "s_barrier\n\t" \
    "ds_read_b128 v[52:55], %[pa] offset:0\n\t" \
    "ds_read_b128 v[56:59], %[pa] offset:1024\n\t" \
    "ds_read_b128 v[60:63], %[pa] offset:2048\n\t" \
    "ds_read_b128 v[64:67], %[pa] offset:3072\n\t" \
    "s_add_u32 %[b], %[oo], 0x1000\n\t" \
    "s_waitcnt lgkmcnt(0)\n\t" \
    "v_add_f32 v196, v196, v52\n\t" \
    "v_add_f32 v197, v197, v53\n\t" \
    "v_add_f32 v198, v198, v54\n\t" \
    "v_add_f32 v199, v199, v55\n\t" \
    "v_add_f32 v200, v200, v56\n\t" \
    "v_add_f32 v201, v201, v57\n\t" \
    "v_add_f32 v202, v202, v58\n\t" \
    "v_add_f32 v203, v203, v59\n\t" \
    "v_add_f32 v204, v204, v60\n\t" \
    "v_add_f32 v205, v205, v61\n\t" \
    "v_add_f32 v206, v206, v62\n\t" \
    "v_add_f32 v207, v207, v63\n\t" \
    "v_add_f32 v208, v208, v64\n\t" \
    "v_add_f32 v209, v209, v65\n\t" \
    "v_add_f32 v210, v210, v66\n\t" \
    "v_add_f32 v211, v211, v67\n\t" \
    "v_cvt_pk_bf16_f32 v52, v196, v200\n\t" \
    "v_cvt_pk_bf16_f32 v53, v197, v201\n\t" \
    "v_cvt_pk_bf16_f32 v54, v198, v202\n\t" \
    "v_cvt_pk_bf16_f32 v55, v199, v203\n\t" \
    "v_cvt_pk_bf16_f32 v56, v204, v208\n\t" \
    "v_cvt_pk_bf16_f32 v57, v205, v209\n\t" \
    "v_cvt_pk_bf16_f32 v58, v206, v210\n\t" \
    "v_cvt_pk_bf16_f32 v59, v207, v211\n\t" \
    "buffer_store_dwordx4 v[52:55], %[l16], %[rs], %[oo] offen sc1\n\t" \
    "buffer_store_dwordx4 v[56:59], %[l16], %[rs], %[b] offen sc1\n\t" \
    "E_%=:\n\t" \
    "s_setprio 0\n\t" \
    "s_waitcnt lgkmcnt(0)\n\t" \
    "s_nop 7\n\t" \
    : [t] "+s"(t), [sx] "+s"(sx), [oo] "+s"(oo), [a] "=&s"(tmp), [b] "=&s"(tmp2), [em] "=&s"(em), "+{v[20:27]}"(xf[0]), "+{v[28:35]}"(xf[1]), "+{v[36:43]}"(xf[2]), "+{v[196:203]}"(ac[0]), "+{v[204:211]}"(ac[1]) \
    : [te] "s"(TE_), [fin] "s"(FIN_), [kh] "s"(kh), [sd] "s"(sd), [rs] "s"(rs), [l16] "v"(l16), [xp] "v"(xp), [pa] "v"(pa), [fm] "s"(fm), [pm] "s"(pm), [fo] "s"(fo), [po] "s"(po), [pd] "s"(pd), [tag] "s"(P.tag), "{v[68:75]}"(w[0][0]), "{v[76:83]}"(w[0][1]), "{v[84:91]}"(w[1][0]), "{v[92:99]}"(w[1][1]), "{v[100:107]}"(w[2][0]), "{v[108:115]}"(w[2][1]), "{v[116:123]}"(w[3][0]), "{v[124:131]}"(w[3][1]), "{v[132:139]}"(w[4][0]), "{v[140:147]}"(w[4][1]), "{v[148:155]}"(w[5][0]), "{v[156:163]}"(w[5][1]), "{v[164:171]}"(w[6][0]), "{v[172:179]}"(w[6][1]), "{v[180:187]}"(w[7][0]), "{v[188:195]}"(w[7][1]) \
    : "memory", "scc", "v0", "v1", "v2", "v3", "v4", "v5", "v6", "v7", "v8", "v9", "v10", "v11", "v12", "v13", "v14", "v15", "v44", "v45", "v46", "v47", "v48", "v49", "v50", "v51", "v52", "v53", "v54", "v55", "v56", "v57", "v58", "v59", "v60", "v61", "v62", "v63", "v64", "v65", "v66", "v67")

#define ONE_X(XP_) \
  asm volatile( \
    "ds_read_b128 v[20:23], %[xp] offset:0\n\t" \
    "ds_read_b128 v[24:27], %[xp] offset:1024\n\t" \
    "ds_read_b128 v[28:31], %[xp] offset:2048\n\t" \
    "ds_read_b128 v[32:35], %[xp] offset:3072\n\t" \
    "ds_read_b128 v[36:39], %[xp] offset:4096\n\t" \
    "ds_read_b128 v[40:43], %[xp] offset:5120\n\t" \
    "s_waitcnt lgkmcnt(4)\n\t" \
    "v_mfma_f32_16x16x32_bf16 v[0:3], v[68:71], v[20:23], 0\n\t" \
    "ds_read_b128 v[44:47], %[xp] offset:6144\n\t" \
    "v_mfma_f32_16x16x32_bf16 v[4:7], v[76:79], v[20:23], 0\n\t" \
    "ds_read_b128 v[48:51], %[xp] offset:7168\n\t" \
    "v_mfma_f32_16x16x32_bf16 v[8:11], v[68:71], v[24:27], 0\n\t" \
    "s_waitcnt lgkmcnt(4)\n\t" \
    "v_mfma_f32_16x16x32_bf16 v[12:15], v[76:79], v[24:27], 0\n\t" \
    "v_mfma_f32_16x16x32_bf16 v[0:3], v[72:75], v[28:31], v[0:3]\n\t" \
    "ds_read_b128 v[20:23], %[xp] offset:8192\n\t" \
    "v_mfma_f32_16x16x32_bf16 v[4:7], v[80:83], v[28:31], v[4:7]\n\t" \
    "ds_read_b128 v[24:27], %[xp] offset:9216\n\t" \
    "v_mfma_f32_16x16x32_bf16 v[8:11], v[72:75], v[32:35], v[8:11]\n\t" \
    "s_waitcnt lgkmcnt(4)\n\t" \
    "v_mfma_f32_16x16x32_bf16 v[12:15], v[80:83], v[32:35], v[12:15]\n\t" \
    "v_mfma_f32_16x16x32_bf16 v[0:3], v[84:87], v[36:39], v[0:3]\n\t" \
    "ds_read_b128 v[28:31], %[xp] offset:10240\n\t" \
    "v_mfma_f32_16x16x32_bf16 v[4:7], v[92:95], v[36:39], v[4:7]\n\t" \
    "ds_read_b128 v[32:35], %[xp] offset:11264\n\t" \
    "v_mfma_f32_16x16x32_bf16 v[8:11], v[84:87], v[40:43], v[8:11]\n\t" \
    "s_waitcnt lgkmcnt(4)\n\t" \
    "v_mfma_f32_16x16x32_bf16 v[12:15], v[92:95], v[40:43], v[12:15]\n\t" \
    "v_mfma_f32_16x16x32_bf16 v[0:3], v[88:91], v[44:47], v[0:3]\n\t" \
    "ds_read_b128 v[36:39], %[xp] offset:12288\n\t" \
    "v_mfma_f32_16x16x32_bf16 v[4:7], v[96:99], v[44:47], v[4:7]\n\t" \
    "ds_read_b128 v[40:43], %[xp] offset:13312\n\t" \
    "v_mfma_f32_16x16x32_bf16 v[8:11], v[88:91], v[48:51], v[8:11]\n\t" \
    "s_waitcnt lgkmcnt(4)\n\t" \
    "v_mfma_f32_16x16x32_bf16 v[12:15], v[96:99], v[48:51], v[12:15]\n\t" \
    "v_mfma_f32_16x16x32_bf16 v[0:3], v[100:103], v[20:23], v[0:3]\n\t" \
    "ds_read_b128 v[44:47], %[xp] offset:14336\n\t" \
    "v_mfma_f32_16x16x32_bf16 v[4:7], v[108:111], v[20:23], v[4:7]\n\t" \
    "ds_read_b128 v[48:51], %[xp] offset:15360\n\t" \
    "v_mfma_f32_16x16x32_bf16 v[8:11], v[100:103], v[24:27], v[8:11]\n\t" \
    "s_waitcnt lgkmcnt(4)\n\t" \
    "v_mfma_f32_16x16x32_bf16 v[12:15], v[108:111], v[24:27], v[12:15]\n\t" \
    "s_waitcnt vmcnt(0)\n\t" \
    "s_barrier\n\t" \
    "v_mfma_f32_16x16x32_bf16 v[0:3], v[104:107], v[28:31], v[0:3]\n\t" \
    "ds_read_b128 v[20:23], %[xp] offset:16384\n\t" \
    "v_mfma_f32_16x16x32_bf16 v[4:7], v[112:115], v[28:31], v[4:7]\n\t" \
    "ds_read_b128 v[24:27], %[xp] offset:17408\n\t" \
    "v_mfma_f32_16x16x32_bf16 v[8:11], v[104:107], v[32:35], v[8:11]\n\t" \
    "s_waitcnt lgkmcnt(4)\n\t" \
    "v_mfma_f32_16x16x32_bf16 v[12:15], v[112:115], v[32:35], v[12:15]\n\t" \
    "buffer_load_dword v52, %[pv], %[rs], 0 offen sc1\n\t" \
    "v_mfma_f32_16x16x32_bf16 v[0:3], v[116:119], v[36:39], v[0:3]\n\t" \
    "ds_read_b128 v[28:31], %[xp] offset:18432\n\t" \
    "v_mfma_f32_16x16x32_bf16 v[4:7], v[124:127], v[36:39], v[4:7]\n\t" \
    "ds_read_b128 v[32:35], %[xp] offset:19456\n\t" \
    "v_mfma_f32_16x16x32_bf16 v[8:11], v[116:119], v[40:43], v[8:11]\n\t" \
    "s_waitcnt lgkmcnt(4)\n\t" \
    "v_mfma_f32_16x16x32_bf16 v[12:15], v[124:127], v[40:43], v[12:15]\n\t" \
    "v_mfma_f32_16x16x32_bf16 v[0:3], v[120:123], v[44:47], v[0:3]\n\t" \
    "ds_read_b128 v[36:39], %[xp] offset:20480\n\t" \
    "v_mfma_f32_16x16x32_bf16 v[4:7], v[128:131], v[44:47], v[4:7]\n\t" \
    "ds_read_b128 v[40:43], %[xp] offset:21504\n\t" \
    "v_mfma_f32_16x16x32_bf16 v[8:11], v[120:123], v[48:51], v[8:11]\n\t" \
    "s_waitcnt lgkmcnt(4)\n\t" \
    "v_mfma_f32_16x16x32_bf16 v[12:15], v[128:131], v[48:51], v[12:15]\n\t" \
    "v_mfma_f32_16x16x32_bf16 v[0:3], v[132:135], v[20:23], v[0:3]\n\t" \
    "ds_read_b128 v[44:47], %[xp] offset:22528\n\t" \
    "v_mfma_f32_16x16x32_bf16 v[4:7], v[140:143], v[20:23], v[4:7]\n\t" \
    "ds_read_b128 v[48:51], %[xp] offset:23552\n\t" \
    "v_mfma_f32_16x16x32_bf16 v[8:11], v[132:135], v[24:27], v[8:11]\n\t" \
    "s_waitcnt lgkmcnt(4)\n\t" \
    "v_mfma_f32_16x16x32_bf16 v[12:15], v[140:143], v[24:27], v[12:15]\n\t" \
    "v_mfma_f32_16x16x32_bf16 v[0:3], v[136:139], v[28:31], v[0:3]\n\t" \
    "ds_read_b128 v[20:23], %[xp] offset:24576\n\t" \
    "v_mfma_f32_16x16x32_bf16 v[4:7], v[144:147], v[28:31], v[4:7]\n\t" \
    "ds_read_b128 v[24:27], %[xp] offset:25600\n\t" \
    "v_mfma_f32_16x16x32_bf16 v[8:11], v[136:139], v[32:35], v[8:11]\n\t" \
    "s_waitcnt lgkmcnt(4)\n\t" \
    "v_mfma_f32_16x16x32_bf16 v[12:15], v[144:147], v[32:35], v[12:15]\n\t" \
    "v_mfma_f32_16x16x32_bf16 v[0:3], v[148:151], v[36:39], v[0:3]\n\t" \
    "ds_read_b128 v[28:31], %[xp] offset:26624\n\t" \
    "v_mfma_f32_16x16x32_bf16 v[4:7], v[156:159], v[36:39], v[4:7]\n\t" \
    "ds_read_b128 v[32:35], %[xp] offset:27648\n\t" \
    "v_mfma_f32_16x16x32_bf16 v[8:11], v[148:151], v[40:43], v[8:11]\n\t" \
    "s_waitcnt lgkmcnt(4)\n\t" \
    "v_mfma_f32_16x16x32_bf16 v[12:15], v[156:159], v[40:43], v[12:15]\n\t" \
    "v_mfma_f32_16x16x32_bf16 v[0:3], v[152:155], v[44:47], v[0:3]\n\t" \
    "ds_read_b128 v[36:39], %[xp] offset:28672\n\t" \
    "v_mfma_f32_16x16x32_bf16 v[4:7], v[160:163], v[44:47], v[4:7]\n\t" \
    "ds_read_b128 v[40:43], %[xp] offset:29696\n\t" \
    "v_mfma_f32_16x16x32_bf16 v[8:11], v[152:155], v[48:51], v[8:11]\n\t" \
    "s_waitcnt lgkmcnt(4)\n\t" \
    "v_mfma_f32_16x16x32_bf16 v[12:15], v[160:163], v[48:51], v[12:15]\n\t" \
    "v_mfma_f32_16x16x32_bf16 v[0:3], v[164:167], v[20:23], v[0:3]\n\t" \
    "ds_read_b128 v[44:47], %[xp] offset:30720\n\t" \
    "v_mfma_f32_16x16x32_bf16 v[4:7], v[172:175], v[20:23], v[4:7]\n\t" \
    "ds_read_b128 v[48:51], %[xp] offset:31744\n\t" \
    "v_mfma_f32_16x16x32_bf16 v[8:11], v[164:167], v[24:27], v[8:11]\n\t" \
    "s_waitcnt lgkmcnt(4)\n\t" \
    "v_mfma_f32_16x16x32_bf16 v[12:15], v[172:175], v[24:27], v[12:15]\n\t" \
    "v_mfma_f32_16x16x32_bf16 v[0:3], v[168:171], v[28:31], v[0:3]\n\t" \
    "v_mfma_f32_16x16x32_bf16 v[4:7], v[176:179], v[28:31], v[4:7]\n\t" \
    "v_mfma_f32_16x16x32_bf16 v[8:11], v[168:171], v[32:35], v[8:11]\n\t" \
    "s_waitcnt lgkmcnt(2)\n\t" \
    "v_mfma_f32_16x16x32_bf16 v[12:15], v[176:179], v[32:35], v[12:15]\n\t" \
    "v_mfma_f32_16x16x32_bf16 v[0:3], v[180:183], v[36:39], v[0:3]\n\t" \
    "v_mfma_f32_16x16x32_bf16 v[4:7], v[188:191], v[36:39], v[4:7]\n\t" \
    "v_mfma_f32_16x16x32_bf16 v[8:11], v[180:183], v[40:43], v[8:11]\n\t" \
    "s_waitcnt lgkmcnt(0)\n\t" \
    "v_mfma_f32_16x16x32_bf16 v[12:15], v[188:191], v[40:43], v[12:15]\n\t" \
    "v_mfma_f32_16x16x32_bf16 v[0:3], v[184:187], v[44:47], v[0:3]\n\t" \
    "v_mfma_f32_16x16x32_bf16 v[4:7], v[192:195], v[44:47], v[4:7]\n\t" \
    "v_mfma_f32_16x16x32_bf16 v[8:11], v[184:187], v[48:51], v[8:11]\n\t" \
    "v_mfma_f32_16x16x32_bf16 v[12:15], v[192:195], v[48:51], v[12:15]\n\t" \
    "s_waitcnt vmcnt(0)\n\t" \
    "s_nop 7\n\t" \
    : "=&{v52}"(pg), "=&{v[0:3]}"(a[0]), "=&{v[4:7]}"(a[1]), "=&{v[8:11]}"(a[2]), "=&{v[12:15]}"(a[3]) \
    : [xp] "v"(XP_), [pv] "v"(pv), [rs] "s"(rs), "{v[68:75]}"(w[0][0]), "{v[76:83]}"(w[0][1]), "{v[84:91]}"(w[1][0]), "{v[92:99]}"(w[1][1]), "{v[100:107]}"(w[2][0]), "{v[108:115]}"(w[2][1]), "{v[116:123]}"(w[3][0]), "{v[124:131]}"(w[3][1]), "{v[132:139]}"(w[4][0]), "{v[140:147]}"(w[4][1]), "{v[148:155]}"(w[5][0]), "{v[156:163]}"(w[5][1]), "{v[164:171]}"(w[6][0]), "{v[172:179]}"(w[6][1]), "{v[180:187]}"(w[7][0]), "{v[188:195]}"(w[7][1]) \
    : "memory", "v20", "v21", "v22", "v23", "v24", "v25", "v26", "v27", "v28", "v29", "v30", "v31", "v32", "v33", "v34", "v35", "v36", "v37", "v38", "v39", "v40", "v41", "v42", "v43", "v44", "v45", "v46", "v47", "v48", "v49", "v50", "v51")

#define ONE_K(XP_) \
  asm volatile( \
    "ds_read_b128 v[20:23], %[xp] offset:0\n\t" \
    "ds_read_b128 v[24:27], %[xp] offset:1024\n\t" \
    "ds_read_b128 v[28:31], %[xp] offset:2048\n\t" \
    "ds_read_b128 v[32:35], %[xp] offset:3072\n\t" \
    "ds_read_b128 v[36:39], %[xp] offset:4096\n\t" \
    "ds_read_b128 v[40:43], %[xp] offset:5120\n\t" \
    "s_waitcnt lgkmcnt(4)\n\t" \
    "v_mfma_f32_16x16x32_bf16 v[0:3], v[68:71], v[20:23], 0\n\t" \
    "ds_read_b128 v[44:47], %[xp] offset:6144\n\t" \
    "v_mfma_f32_16x16x32_bf16 v[4:7], v[76:79], v[20:23], 0\n\t" \
    "ds_read_b128 v[48:51], %[xp] offset:7168\n\t" \
    "v_mfma_f32_16x16x32_bf16 v[8:11], v[68:71], v[24:27], 0\n\t" \
    "s_waitcnt lgkmcnt(4)\n\t" \
    "v_mfma_f32_16x16x32_bf16 v[12:15], v[76:79], v[24:27], 0\n\t" \
    "v_mfma_f32_16x16x32_bf16 v[0:3], v[72:75], v[28:31], v[0:3]\n\t" \
    "ds_read_b128 v[20:23], %[xp] offset:8192\n\t" \
    "v_mfma_f32_16x16x32_bf16 v[4:7], v[80:83], v[28:31], v[4:7]\n\t" \
    "ds_read_b128 v[24:27], %[xp] offset:9216\n\t" \
    "v_mfma_f32_16x16x32_bf16 v[8:11], v[72:75], v[32:35], v[8:11]\n\t" \
    "s_waitcnt lgkmcnt(4)\n\t" \
    "v_mfma_f32_16x16x32_bf16 v[12:15], v[80:83], v[32:35], v[12:15]\n\t" \
    "v_mfma_f32_16x16x32_bf16 v[0:3], v[84:87], v[36:39], v[0:3]\n\t" \
    "ds_read_b128 v[28:31], %[xp] offset:10240\n\t" \
    "v_mfma_f32_16x16x32_bf16 v[4:7], v[92:95], v[36:39], v[4:7]\n\t" \
    "ds_read_b128 v[32:35], %[xp] offset:11264\n\t" \
    "v_mfma_f32_16x16x32_bf16 v[8:11], v[84:87], v[40:43], v[8:11]\n\t" \
    "s_waitcnt lgkmcnt(4)\n\t" \
    "v_mfma_f32_16x16x32_bf16 v[12:15], v[92:95], v[40:43], v[12:15]\n\t" \
    "v_mfma_f32_16x16x32_bf16 v[0:3], v[88:91], v[44:47], v[0:3]\n\t" \
    "ds_read_b128 v[36:39], %[xp] offset:12288\n\t" \
    "v_mfma_f32_16x16x32_bf16 v[4:7], v[96:99], v[44:47], v[4:7]\n\t" \
    "ds_read_b128 v[40:43], %[xp] offset:13312\n\t" \
    "v_mfma_f32_16x16x32_bf16 v[8:11], v[88:91], v[48:51], v[8:11]\n\t" \
    "s_waitcnt lgkmcnt(4)\n\t" \
    "v_mfma_f32_16x16x32_bf16 v[12:15], v[96:99], v[48:51], v[12:15]\n\t" \
    "v_mfma_f32_16x16x32_bf16 v[0:3], v[100:103], v[20:23], v[0:3]\n\t" \
    "ds_read_b128 v[44:47], %[xp] offset:14336\n\t" \
    "v_mfma_f32_16x16x32_bf16 v[4:7], v[108:111], v[20:23], v[4:7]\n\t" \
    "ds_read_b128 v[48:51], %[xp] offset:15360\n\t" \
    "v_mfma_f32_16x16x32_bf16 v[8:11], v[100:103], v[24:27], v[8:11]\n\t" \
    "s_waitcnt lgkmcnt(4)\n\t" \
    "v_mfma_f32_16x16x32_bf16 v[12:15], v[108:111], v[24:27], v[12:15]\n\t" \
    "s_waitcnt vmcnt(8)\n\t" \
    "s_barrier\n\t" \
    "v_mfma_f32_16x16x32_bf16 v[0:3], v[104:107], v[28:31], v[0:3]\n\t" \
    "ds_read_b128 v[20:23], %[xp] offset:16384\n\t" \
    "v_mfma_f32_16x16x32_bf16 v[4:7], v[112:115], v[28:31], v[4:7]\n\t" \
    "ds_read_b128 v[24:27], %[xp] offset:17408\n\t" \
    "v_mfma_f32_16x16x32_bf16 v[8:11], v[104:107], v[32:35], v[8:11]\n\t" \
    "s_waitcnt lgkmcnt(4)\n\t" \
    "v_mfma_f32_16x16x32_bf16 v[12:15], v[112:115], v[32:35], v[12:15]\n\t" \
    "v_mfma_f32_16x16x32_bf16 v[0:3], v[116:119], v[36:39], v[0:3]\n\t" \
    "ds_read_b128 v[28:31], %[xp] offset:18432\n\t" \
    "v_mfma_f32_16x16x32_bf16 v[4:7], v[124:127], v[36:39], v[4:7]\n\t" \
    "ds_read_b128 v[32:35], %[xp] offset:19456\n\t" \
    "v_mfma_f32_16x16x32_bf16 v[8:11], v[116:119], v[40:43], v[8:11]\n\t" \
    "s_waitcnt lgkmcnt(4)\n\t" \
    "v_mfma_f32_16x16x32_bf16 v[12:15], v[124:127], v[40:43], v[12:15]\n\t" \
    "v_mfma_f32_16x16x32_bf16 v[0:3], v[120:123], v[44:47], v[0:3]\n\t" \
    "ds_read_b128 v[36:39], %[xp] offset:20480\n\t" \
    "v_mfma_f32_16x16x32_bf16 v[4:7], v[128:131], v[44:47], v[4:7]\n\t" \
    "ds_read_b128 v[40:43], %[xp] offset:21504\n\t" \
    "v_mfma_f32_16x16x32_bf16 v[8:11], v[120:123], v[48:51], v[8:11]\n\t" \
    "s_waitcnt lgkmcnt(4)\n\t" \
    "v_mfma_f32_16x16x32_bf16 v[12:15], v[128:131], v[48:51], v[12:15]\n\t" \
    "v_mfma_f32_16x16x32_bf16 v[0:3], v[132:135], v[20:23], v[0:3]\n\t" \
    "ds_read_b128 v[44:47], %[xp] offset:22528\n\t" \
    "v_mfma_f32_16x16x32_bf16 v[4:7], v[140:143], v[20:23], v[4:7]\n\t" \
    "ds_read_b128 v[48:51], %[xp] offset:23552\n\t" \
    "v_mfma_f32_16x16x32_bf16 v[8:11], v[132:135], v[24:27], v[8:11]\n\t" \
    "s_waitcnt lgkmcnt(4)\n\t" \
    "v_mfma_f32_16x16x32_bf16 v[12:15], v[140:143], v[24:27], v[12:15]\n\t" \
    "v_mfma_f32_16x16x32_bf16 v[0:3], v[136:139], v[28:31], v[0:3]\n\t" \
    "ds_read_b128 v[20:23], %[xp] offset:24576\n\t" \
    "v_mfma_f32_16x16x32_bf16 v[4:7], v[144:147], v[28:31], v[4:7]\n\t" \
    "ds_read_b128 v[24:27], %[xp] offset:25600\n\t" \
    "v_mfma_f32_16x16x32_bf16 v[8:11], v[136:139], v[32:35], v[8:11]\n\t" \
    "s_waitcnt lgkmcnt(4)\n\t" \
    "v_mfma_f32_16x16x32_bf16 v[12:15], v[144:147], v[32:35], v[12:15]\n\t" \
    "v_mfma_f32_16x16x32_bf16 v[0:3], v[148:151], v[36:39], v[0:3]\n\t" \
    "ds_read_b128 v[28:31], %[xp] offset:26624\n\t" \
    "v_mfma_f32_16x16x32_bf16 v[4:7], v[156:159], v[36:39], v[4:7]\n\t" \
    "ds_read_b128 v[32:35], %[xp] offset:27648\n\t" \
    "v_mfma_f32_16x16x32_bf16 v[8:11], v[148:151], v[40:43], v[8:11]\n\t" \
    "s_waitcnt lgkmcnt(4)\n\t" \
    "v_mfma_f32_16x16x32_bf16 v[12:15], v[156:159], v[40:43], v[12:15]\n\t" \
    "v_mfma_f32_16x16x32_bf16 v[0:3], v[152:155], v[44:47], v[0:3]\n\t" \
    "ds_read_b128 v[36:39], %[xp] offset:28672\n\t" \
    "v_mfma_f32_16x16x32_bf16 v[4:7], v[160:163], v[44:47], v[4:7]\n\t" \
    "ds_read_b128 v[40:43], %[xp] offset:29696\n\t" \
    "v_mfma_f32_16x16x32_bf16 v[8:11], v[152:155], v[48:51], v[8:11]\n\t" \
    "s_waitcnt lgkmcnt(4)\n\t" \
    "v_mfma_f32_16x16x32_bf16 v[12:15], v[160:163], v[48:51], v[12:15]\n\t" \
    "v_mfma_f32_16x16x32_bf16 v[0:3], v[164:167], v[20:23], v[0:3]\n\t" \
    "ds_read_b128 v[44:47], %[xp] offset:30720\n\t" \
    "v_mfma_f32_16x16x32_bf16 v[4:7], v[172:175], v[20:23], v[4:7]\n\t" \
    "ds_read_b128 v[48:51], %[xp] offset:31744\n\t" \
    "v_mfma_f32_16x16x32_bf16 v[8:11], v[164:167], v[24:27], v[8:11]\n\t" \
    "s_waitcnt lgkmcnt(4)\n\t" \
    "v_mfma_f32_16x16x32_bf16 v[12:15], v[172:175], v[24:27], v[12:15]\n\t" \
    "v_mfma_f32_16x16x32_bf16 v[0:3], v[168:171], v[28:31], v[0:3]\n\t" \
    "v_mfma_f32_16x16x32_bf16 v[4:7], v[176:179], v[28:31], v[4:7]\n\t" \
    "v_mfma_f32_16x16x32_bf16 v[8:11], v[168:171], v[32:35], v[8:11]\n\t" \
    "s_waitcnt lgkmcnt(2)\n\t" \
    "v_mfma_f32_16x16x32_bf16 v[12:15], v[176:179], v[32:35], v[12:15]\n\t" \
    "v_mfma_f32_16x16x32_bf16 v[0:3], v[180:183], v[36:39], v[0:3]\n\t" \
    "v_mfma_f32_16x16x32_bf16 v[4:7], v[188:191], v[36:39], v[4:7]\n\t" \
    "v_mfma_f32_16x16x32_bf16 v[8:11], v[180:183], v[40:43], v[8:11]\n\t" \
    "s_waitcnt lgkmcnt(0)\n\t" \
    "v_mfma_f32_16x16x32_bf16 v[12:15], v[188:191], v[40:43], v[12:15]\n\t" \
    "v_mfma_f32_16x16x32_bf16 v[0:3], v[184:187], v[44:47], v[0:3]\n\t" \
    "v_mfma_f32_16x16x32_bf16 v[4:7], v[192:195], v[44:47], v[4:7]\n\t" \
    "v_mfma_f32_16x16x32_bf16 v[8:11], v[184:187], v[48:51], v[8:11]\n\t" \
    "v_mfma_f32_16x16x32_bf16 v[12:15], v[192:195], v[48:51], v[12:15]\n\t" \
    "s_nop 7\n\t" \
    : "=&{v[0:3]}"(a[0]), "=&{v[4:7]}"(a[1]), "=&{v[8:11]}"(a[2]), "=&{v[12:15]}"(a[3]) \
    : [xp] "v"(XP_), "{v[68:75]}"(w[0][0]), "{v[76:83]}"(w[0][1]), "{v[84:91]}"(w[1][0]), "{v[92:99]}"(w[1][1]), "{v[100:107]}"(w[2][0]), "{v[108:115]}"(w[2][1]), "{v[116:123]}"(w[3][0]), "{v[124:131]}"(w[3][1]), "{v[132:139]}"(w[4][0]), "{v[140:147]}"(w[4][1]), "{v[148:155]}"(w[5][0]), "{v[156:163]}"(w[5][1]), "{v[164:171]}"(w[6][0]), "{v[172:179]}"(w[6][1]), "{v[180:187]}"(w[7][0]), "{v[188:195]}"(w[7][1]) \
    : "memory", "v20", "v21", "v22", "v23", "v24", "v25", "v26", "v27", "v28", "v29", "v30", "v31", "v32", "v33", "v34", "v35", "v36", "v37", "v38", "v39", "v40", "v41", "v42", "v43", "v44", "v45", "v46", "v47", "v48", "v49", "v50", "v51")

#define ONE_V(XP_) \
  asm volatile( \
    "ds_read_b128 v[20:23], %[xp] offset:0\n\t" \
    "ds_read_b128 v[24:27], %[xp] offset:1024\n\t" \
    "ds_read_b128 v[28:31], %[xp] offset:2048\n\t" \
    "ds_read_b128 v[32:35], %[xp] offset:3072\n\t" \
    "ds_read_b128 v[36:39], %[xp] offset:4096\n\t" \
    "ds_read_b128 v[40:43], %[xp] offset:5120\n\t" \
    "s_waitcnt lgkmcnt(4)\n\t" \
    "v_mfma_f32_16x16x32_bf16 v[0:3], v[20:23], v[68:71], 0\n\t" \
    "ds_read_b128 v[44:47], %[xp] offset:6144\n\t" \
    "v_mfma_f32_16x16x32_bf16 v[4:7], v[20:23], v[76:79], 0\n\t" \
    "ds_read_b128 v[48:51], %[xp] offset:7168\n\t" \
    "v_mfma_f32_16x16x32_bf16 v[8:11], v[24:27], v[68:71], 0\n\t" \
    "s_waitcnt lgkmcnt(4)\n\t" \
    "v_mfma_f32_16x16x32_bf16 v[12:15], v[24:27], v[76:79], 0\n\t" \
    "v_mfma_f32_16x16x32_bf16 v[0:3], v[28:31], v[72:75], v[0:3]\n\t" \
    "ds_read_b128 v[20:23], %[xp] offset:8192\n\t" \
    "v_mfma_f32_16x16x32_bf16 v[4:7], v[28:31], v[80:83], v[4:7]\n\t" \
    "ds_read_b128 v[24:27], %[xp] offset:9216\n\t" \
    "v_mfma_f32_16x16x32_bf16 v[8:11], v[32:35], v[72:75], v[8:11]\n\t" \
    "s_waitcnt lgkmcnt(4)\n\t" \
    "v_mfma_f32_16x16x32_bf16 v[12:15], v[32:35], v[80:83], v[12:15]\n\t" \
    "v_mfma_f32_16x16x32_bf16 v[0:3], v[36:39], v[84:87], v[0:3]\n\t" \
    "ds_read_b128 v[28:31], %[xp] offset:10240\n\t" \
    "v_mfma_f32_16x16x32_bf16 v[4:7], v[36:39], v[92:95], v[4:7]\n\t" \
    "ds_read_b128 v[32:35], %[xp] offset:11264\n\t" \
    "v_mfma_f32_16x16x32_bf16 v[8:11], v[40:43], v[84:87], v[8:11]\n\t" \
    "s_waitcnt lgkmcnt(4)\n\t" \
    "v_mfma_f32_16x16x32_bf16 v[12:15], v[40:43], v[92:95], v[12:15]\n\t" \
    "v_mfma_f32_16x16x32_bf16 v[0:3], v[44:47], v[88:91], v[0:3]\n\t" \
    "ds_read_b128 v[36:39], %[xp] offset:12288\n\t" \
    "v_mfma_f32_16x16x32_bf16 v[4:7], v[44:47], v[96:99], v[4:7]\n\t" \
    "ds_read_b128 v[40:43], %[xp] offset:13312\n\t" \
    "v_mfma_f32_16x16x32_bf16 v[8:11], v[48:51], v[88:91], v[8:11]\n\t" \
    "s_waitcnt lgkmcnt(4)\n\t" \
    "v_mfma_f32_16x16x32_bf16 v[12:15], v[48:51], v[96:99], v[12:15]\n\t" \
    "v_mfma_f32_16x16x32_bf16 v[0:3], v[20:23], v[100:103], v[0:3]\n\t" \
    "ds_read_b128 v[44:47], %[xp] offset:14336\n\t" \
    "v_mfma_f32_16x16x32_bf16 v[4:7], v[20:23], v[108:111], v[4:7]\n\t" \
    "ds_read_b128 v[48:51], %[xp] offset:15360\n\t" \
    "v_mfma_f32_16x16x32_bf16 v[8:11], v[24:27], v[100:103], v[8:11]\n\t" \
    "s_waitcnt lgkmcnt(4)\n\t" \
    "v_mfma_f32_16x16x32_bf16 v[12:15], v[24:27], v[108:111], v[12:15]\n\t" \
    "s_waitcnt vmcnt(8)\n\t" \
    "s_barrier\n\t" \
    "v_mfma_f32_16x16x32_bf16 v[0:3], v[28:31], v[104:107], v[0:3]\n\t" \
    "ds_read_b128 v[20:23], %[xp] offset:16384\n\t" \
    "v_mfma_f32_16x16x32_bf16 v[4:7], v[28:31], v[112:115], v[4:7]\n\t" \
    "ds_read_b128 v[24:27], %[xp] offset:17408\n\t" \
    "v_mfma_f32_16x16x32_bf16 v[8:11], v[32:35], v[104:107], v[8:11]\n\t" \
    "s_waitcnt lgkmcnt(4)\n\t" \
    "v_mfma_f32_16x16x32_bf16 v[12:15], v[32:35], v[112:115], v[12:15]\n\t" \
    "v_mfma_f32_16x16x32_bf16 v[0:3], v[36:39], v[116:119], v[0:3]\n\t" \
    "ds_read_b128 v[28:31], %[xp] offset:18432\n\t" \
    "v_mfma_f32_16x16x32_bf16 v[4:7], v[36:39], v[124:127], v[4:7]\n\t" \
    "ds_read_b128 v[32:35], %[xp] offset:19456\n\t" \
    "v_mfma_f32_16x16x32_bf16 v[8:11], v[40:43], v[116:119], v[8:11]\n\t" \
    "s_waitcnt lgkmcnt(4)\n\t" \
    "v_mfma_f32_16x16x32_bf16 v[12:15], v[40:43], v[124:127], v[12:15]\n\t" \
    "v_mfma_f32_16x16x32_bf16 v[0:3], v[44:47], v[120:123], v[0:3]\n\t" \
    "ds_read_b128 v[36:39], %[xp] offset:20480\n\t" \
    "v_mfma_f32_16x16x32_bf16 v[4:7], v[44:47], v[128:131], v[4:7]\n\t" \
    "ds_read_b128 v[40:43], %[xp] offset:21504\n\t" \
    "v_mfma_f32_16x16x32_bf16 v[8:11], v[48:51], v[120:123], v[8:11]\n\t" \
    "s_waitcnt lgkmcnt(4)\n\t" \
    "v_mfma_f32_16x16x32_bf16 v[12:15], v[48:51], v[128:131], v[12:15]\n\t" \
    "v_mfma_f32_16x16x32_bf16 v[0:3], v[20:23], v[132:135], v[0:3]\n\t" \
    "ds_read_b128 v[44:47], %[xp] offset:22528\n\t" \
    "v_mfma_f32_16x16x32_bf16 v[4:7], v[20:23], v[140:143], v[4:7]\n\t" \
    "ds_read_b128 v[48:51], %[xp] offset:23552\n\t" \
    "v_mfma_f32_16x16x32_bf16 v[8:11], v[24:27], v[132:135], v[8:11]\n\t" \
    "s_waitcnt lgkmcnt(4)\n\t" \
    "v_mfma_f32_16x16x32_bf16 v[12:15], v[24:27], v[140:143], v[12:15]\n\t" \
    "v_mfma_f32_16x16x32_bf16 v[0:3], v[28:31], v[136:139], v[0:3]\n\t" \
    "ds_read_b128 v[20:23], %[xp] offset:24576\n\t" \
    "v_mfma_f32_16x16x32_bf16 v[4:7], v[28:31], v[144:147], v[4:7]\n\t" \
    "ds_read_b128 v[24:27], %[xp] offset:25600\n\t" \
    "v_mfma_f32_16x16x32_bf16 v[8:11], v[32:35], v[136:139], v[8:11]\n\t" \
    "s_waitcnt lgkmcnt(4)\n\t" \
    "v_mfma_f32_16x16x32_bf16 v[12:15], v[32:35], v[144:147], v[12:15]\n\t" \
    "v_mfma_f32_16x16x32_bf16 v[0:3], v[36:39], v[148:151], v[0:3]\n\t" \
    "ds_read_b128 v[28:31], %[xp] offset:26624\n\t" \
    "v_mfma_f32_16x16x32_bf16 v[4:7], v[36:39], v[156:159], v[4:7]\n\t" \
    "ds_read_b128 v[32:35], %[xp] offset:27648\n\t" \
    "v_mfma_f32_16x16x32_bf16 v[8:11], v[40:43], v[148:151], v[8:11]\n\t" \
    "s_waitcnt lgkmcnt(4)\n\t" \
    "v_mfma_f32_16x16x32_bf16 v[12:15], v[40:43], v[156:159], v[12:15]\n\t" \
    "v_mfma_f32_16x16x32_bf16 v[0:3], v[44:47], v[152:155], v[0:3]\n\t" \
    "ds_read_b128 v[36:39], %[xp] offset:28672\n\t" \
    "v_mfma_f32_16x16x32_bf16 v[4:7], v[44:47], v[160:163], v[4:7]\n\t" \
    "ds_read_b128 v[40:43], %[xp] offset:29696\n\t" \
    "v_mfma_f32_16x16x32_bf16 v[8:11], v[48:51], v[152:155], v[8:11]\n\t" \
    "s_waitcnt lgkmcnt(4)\n\t" \
    "v_mfma_f32_16x16x32_bf16 v[12:15], v[48:51], v[160:163], v[12:15]\n\t" \
    "v_mfma_f32_16x16x32_bf16 v[0:3], v[20:23], v[164:167], v[0:3]\n\t" \
    "ds_read_b128 v[44:47], %[xp] offset:30720\n\t" \
    "v_mfma_f32_16x16x32_bf16 v[4:7], v[20:23], v[172:175], v[4:7]\n\t" \
    "ds_read_b128 v[48:51], %[xp] offset:31744\n\t" \
    "v_mfma_f32_16x16x32_bf16 v[8:11], v[24:27], v[164:167], v[8:11]\n\t" \
    "s_waitcnt lgkmcnt(4)\n\t" \
    "v_mfma_f32_16x16x32_bf16 v[12:15], v[24:27], v[172:175], v[12:15]\n\t" \
    "v_mfma_f32_16x16x32_bf16 v[0:3], v[28:31], v[168:171], v[0:3]\n\t" \
    "v_mfma_f32_16x16x32_bf16 v[4:7], v[28:31], v[176:179], v[4:7]\n\t" \
    "v_mfma_f32_16x16x32_bf16 v[8:11], v[32:35], v[168:171], v[8:11]\n\t" \
    "s_waitcnt lgkmcnt(2)\n\t" \
    "v_mfma_f32_16x16x32_bf16 v[12:15], v[32:35], v[176:179], v[12:15]\n\t" \
    "v_mfma_f32_16x16x32_bf16 v[0:3], v[36:39], v[180:183], v[0:3]\n\t" \
    "v_mfma_f32_16x16x32_bf16 v[4:7], v[36:39], v[188:191], v[4:7]\n\t" \
    "v_mfma_f32_16x16x32_bf16 v[8:11], v[40:43], v[180:183], v[8:11]\n\t" \
    "s_waitcnt lgkmcnt(0)\n\t" \
    "v_mfma_f32_16x16x32_bf16 v[12:15], v[40:43], v[188:191], v[12:15]\n\t" \
    "v_mfma_f32_16x16x32_bf16 v[0:3], v[44:47], v[184:187], v[0:3]\n\t" \
    "v_mfma_f32_16x16x32_bf16 v[4:7], v[44:47], v[192:195], v[4:7]\n\t" \
    "v_mfma_f32_16x16x32_bf16 v[8:11], v[48:51], v[184:187], v[8:11]\n\t" \
    "v_mfma_f32_16x16x32_bf16 v[12:15], v[48:51], v[192:195], v[12:15]\n\t" \
    "s_nop 7\n\t" \
    : "=&{v[0:3]}"(a[0]), "=&{v[4:7]}"(a[1]), "=&{v[8:11]}"(a[2]), "=&{v[12:15]}"(a[3]) \
    : [xp] "v"(XP_), "{v[68:75]}"(w[0][0]), "{v[76:83]}"(w[0][1]), "{v[84:91]}"(w[1][0]), "{v[92:99]}"(w[1][1]), "{v[100:107]}"(w[2][0]), "{v[108:115]}"(w[2][1]), "{v[116:123]}"(w[3][0]), "{v[124:131]}"(w[3][1]), "{v[132:139]}"(w[4][0]), "{v[140:147]}"(w[4][1]), "{v[148:155]}"(w[5][0]), "{v[156:163]}"(w[5][1]), "{v[164:171]}"(w[6][0]), "{v[172:179]}"(w[6][1]), "{v[180:187]}"(w[7][0]), "{v[188:195]}"(w[7][1]) \
    : "memory", "v20", "v21", "v22", "v23", "v24", "v25", "v26", "v27", "v28", "v29", "v30", "v31", "v32", "v33", "v34", "v35", "v36", "v37", "v38", "v39", "v40", "v41", "v42", "v43", "v44", "v45", "v46", "v47", "v48", "v49", "v50", "v51")

#define H_LOOP1() \
  asm volatile( \
    "v_mov_b32 v20, 0\n\t" \
    "v_mov_b32 v21, 0\n\t" \
    "v_mov_b32 v22, 0\n\t" \
    "v_mov_b32 v23, 0\n\t" \
    "v_mov_b32 v24, 0\n\t" \
    "v_mov_b32 v25, 0\n\t" \
    "v_mov_b32 v26, 0\n\t" \
    "v_mov_b32 v27, 0\n\t" \
    "v_mov_b32 v28, 0\n\t" \
    "v_mov_b32 v29, 0\n\t" \
    "v_mov_b32 v30, 0\n\t" \
    "v_mov_b32 v31, 0\n\t" \
    "v_mov_b32 v32, 0\n\t" \
    "v_mov_b32 v33, 0\n\t" \
    "v_mov_b32 v34, 0\n\t" \
    "v_mov_b32 v35, 0\n\t" \
    "v_mov_b32 v36, 0\n\t" \
    "v_mov_b32 v37, 0\n\t" \
    "v_mov_b32 v38, 0\n\t" \
    "v_mov_b32 v39, 0\n\t" \
    "v_mov_b32 v40, 0\n\t" \
    "v_mov_b32 v41, 0\n\t" \
    "v_mov_b32 v42, 0\n\t" \
    "v_mov_b32 v43, 0\n\t" \
    "v_mov_b32 v44, 0\n\t" \
    "v_mov_b32 v45, 0\n\t" \
    "v_mov_b32 v46, 0\n\t" \
    "v_mov_b32 v47, 0\n\t" \
    "v_mov_b32 v48, 0\n\t" \
    "v_mov_b32 v49, 0\n\t" \
    "v_mov_b32 v50, 0\n\t" \
    "v_mov_b32 v51, 0\n\t" \
    "v_mov_b32 v52, 0\n\t" \
    "v_mov_b32 v53, 0\n\t" \
    "v_mov_b32 v54, 0\n\t" \
    "v_mov_b32 v55, 0\n\t" \
    "v_mov_b32 v56, 0\n\t" \
    "v_mov_b32 v57, 0\n\t" \
    "v_mov_b32 v58, 0\n\t" \
    "v_mov_b32 v59, 0\n\t" \
    "v_mov_b32 v60, 0\n\t" \
    "v_mov_b32 v61, 0\n\t" \
    "v_mov_b32 v62, 0\n\t" \
    "v_mov_b32 v63, 0\n\t" \
    "v_mov_b32 v64, 0\n\t" \
    "v_mov_b32 v65, 0\n\t" \
    "v_mov_b32 v66, 0\n\t" \
    "v_mov_b32 v67, 0\n\t" \
    "v_mov_b32 v68, 0\n\t" \
    "v_mov_b32 v69, 0\n\t" \
    "v_mov_b32 v70, 0\n\t" \
    "v_mov_b32 v71, 0\n\t" \
    "v_mov_b32 v72, 0\n\t" \
    "v_mov_b32 v73, 0\n\t" \
    "v_mov_b32 v74, 0\n\t" \
    "v_mov_b32 v75, 0\n\t" \
    "v_mov_b32 v76, 0\n\t" \
    "v_mov_b32 v77, 0\n\t" \
    "v_mov_b32 v78, 0\n\t" \
    "v_mov_b32 v79, 0\n\t" \
    "v_mov_b32 v80, 0\n\t" \
    "v_mov_b32 v81, 0\n\t" \
    "v_mov_b32 v82, 0\n\t" \
    "v_mov_b32 v83, 0\n\t" \
    "s_waitcnt vmcnt(8)\n\t" \
    "s_barrier\n\t" \
    "ds_read_b64_tr_b16 v[116:117], %[b0] offset:0\n\t" \
    "ds_read_b64_tr_b16 v[118:119], %[b0] offset:1024\n\t" \
    "ds_read_b64_tr_b16 v[124:125], %[b1] offset:0\n\t" \
    "ds_read_b64_tr_b16 v[126:127], %[b1] offset:1024\n\t" \
    "ds_read_b64_tr_b16 v[120:121], %[b0] offset:8192\n\t" \
    "ds_read_b64_tr_b16 v[122:123], %[b0] offset:9216\n\t" \
    "ds_read_b64_tr_b16 v[128:129], %[b1] offset:8192\n\t" \
    "ds_read_b64_tr_b16 v[130:131], %[b1] offset:9216\n\t" \
    "ds_read_b128 v[84:87], %[a] offset:0\n\t" \
    "ds_read_b128 v[88:91], %[a] offset:1024\n\t" \
    "ds_read_b128 v[92:95], %[a] offset:2048\n\t" \
    "ds_read_b128 v[96:99], %[a] offset:3072\n\t" \
    "ds_read_b128 v[100:103], %[a] offset:4096\n\t" \
    "ds_read_b128 v[104:107], %[a] offset:5120\n\t" \
    "ds_read_b128 v[108:111], %[a] offset:6144\n\t" \
    "ds_read_b128 v[112:115], %[a] offset:7168\n\t" \
    "s_add_u32 %[d], %[lw], %[c]\n\t" \
    "s_add_u32 %[y], %[c], 49152\n\t" \
    "s_cmp_eq_u32 %[y], 147456\n\t" \
    "s_cselect_b32 %[y], 0, %[y]\n\t" \
    "s_sub_u32 %[z], %[y], %[c]\n\t" \
    "s_mov_b32 %[c], %[y]\n\t" \
    "s_add_u32 %[x], %[t], 3\n\t" \
    "s_sub_u32 %[y], %[x], %[nt]\n\t" \
    "s_cmp_lt_u32 %[x], %[nt]\n\t" \
    "s_cselect_b32 %[x], %[x], %[y]\n\t" \
    "s_cselect_b32 m0, %[cb], %[cn]\n\t" \
    "s_mul_i32 %[y], %[x], %[kb]\n\t" \
    "s_add_u32 %[y], %[y], m0\n\t" \
    "s_lshl_b32 %[x], %[x], 11\n\t" \
    "s_add_u32 %[x], %[x], %[ca]\n\t" \
    "v_add_u32 %[a], %[z], %[a]\n\t" \
    "v_add_u32 %[b0], %[z], %[b0]\n\t" \
    "v_add_u32 %[b1], %[z], %[b1]\n\t" \
    "L_%=:\n\t" \
    "s_waitcnt vmcnt(4) lgkmcnt(0)\n\t" \
    "s_barrier\n\t" \
    "v_mfma_f32_16x16x32_bf16 v[20:23], v[116:119], v[84:87], v[20:23]\n\t" \
    "ds_read_b64_tr_b16 v[180:181], %[b0] offset:0\n\t" \
    "ds_read_b64_tr_b16 v[182:183], %[b0] offset:1024\n\t" \
    "v_mfma_f32_16x16x32_bf16 v[24:27], v[124:127], v[84:87], v[24:27]\n\t" \
    "ds_read_b64_tr_b16 v[188:189], %[b1] offset:0\n\t" \
    "ds_read_b64_tr_b16 v[190:191], %[b1] offset:1024\n\t" \
    "s_add_u32 m0, %[d], 0\n\t" \
    "v_mfma_f32_16x16x32_bf16 v[20:23], v[120:123], v[88:91], v[20:23]\n\t" \
    "buffer_load_dwordx4 %[vb0], %[rb], %[y] offen lds\n\t" \
    "v_mfma_f32_16x16x32_bf16 v[24:27], v[128:131], v[88:91], v[24:27]\n\t" \
    "ds_read_b64_tr_b16 v[184:185], %[b0] offset:8192\n\t" \
    "ds_read_b64_tr_b16 v[186:187], %[b0] offset:9216\n\t" \
    "s_add_u32 m0, %[d], 8192\n\t" \
    "v_mfma_f32_16x16x32_bf16 v[28:31], v[116:119], v[92:95], v[28:31]\n\t" \
    "buffer_load_dwordx4 %[vb1], %[rb], %[y] offen lds\n\t" \
    "v_mfma_f32_16x16x32_bf16 v[32:35], v[124:127], v[92:95], v[32:35]\n\t" \
    "ds_read_b64_tr_b16 v[192:193], %[b1] offset:8192\n\t" \
    "ds_read_b64_tr_b16 v[194:195], %[b1] offset:9216\n\t" \
    "s_add_u32 m0, %[d], 32768\n\t" \
    "v_mfma_f32_16x16x32_bf16 v[28:31], v[120:123], v[96:99], v[28:31]\n\t" \
    "buffer_load_dwordx4 %[va0], %[ra], %[x] offen sc1 lds\n\t" \
    "v_mfma_f32_16x16x32_bf16 v[32:35], v[128:131], v[96:99], v[32:35]\n\t" \
    "ds_read_b128 v[148:151], %[a] offset:0\n\t" \
    "s_add_u32 m0, %[d], 40960\n\t" \
    "v_mfma_f32_16x16x32_bf16 v[36:39], v[116:119], v[100:103], v[36:39]\n\t" \
    "buffer_load_dwordx4 %[va1], %[ra], %[x] offen sc1 lds\n\t" \
    "v_mfma_f32_16x16x32_bf16 v[40:43], v[124:127], v[100:103], v[40:43]\n\t" \
    "ds_read_b128 v[152:155], %[a] offset:1024\n\t" \
    "ds_read_b128 v[156:159], %[a] offset:2048\n\t" \
    "s_add_u32 %[t], %[t], 1\n\t" \
    "s_add_u32 %[d], %[lw], %[c]\n\t" \
    "s_add_u32 %[y], %[c], 49152\n\t" \
    "v_mfma_f32_16x16x32_bf16 v[36:39], v[120:123], v[104:107], v[36:39]\n\t" \
    "ds_read_b128 v[160:163], %[a] offset:3072\n\t" \
    "ds_read_b128 v[164:167], %[a] offset:4096\n\t" \
    "s_cmp_eq_u32 %[y], 147456\n\t" \
    "s_cselect_b32 %[y], 0, %[y]\n\t" \
    "s_sub_u32 %[z], %[y], %[c]\n\t" \
    "v_mfma_f32_16x16x32_bf16 v[40:43], v[128:131], v[104:107], v[40:43]\n\t" \
    "ds_read_b128 v[168:171], %[a] offset:5120\n\t" \
    "ds_read_b128 v[172:175], %[a] offset:6144\n\t" \
    "s_mov_b32 %[c], %[y]\n\t" \
    "s_add_u32 %[x], %[t], 3\n\t" \
    "s_sub_u32 %[y], %[x], %[nt]\n\t" \
    "v_mfma_f32_16x16x32_bf16 v[44:47], v[116:119], v[108:111], v[44:47]\n\t" \
    "ds_read_b128 v[176:179], %[a] offset:7168\n\t" \
    "s_cmp_lt_u32 %[x], %[nt]\n\t" \
    "s_cselect_b32 %[x], %[x], %[y]\n\t" \
    "s_cselect_b32 m0, %[cb], %[cn]\n\t" \
    "v_mfma_f32_16x16x32_bf16 v[48:51], v[124:127], v[108:111], v[48:51]\n\t" \
    "s_mul_i32 %[y], %[x], %[kb]\n\t" \
    "s_add_u32 %[y], %[y], m0\n\t" \
    "s_lshl_b32 %[x], %[x], 11\n\t" \
    "v_mfma_f32_16x16x32_bf16 v[44:47], v[120:123], v[112:115], v[44:47]\n\t" \
    "s_add_u32 %[x], %[x], %[ca]\n\t" \
    "v_mfma_f32_16x16x32_bf16 v[48:51], v[128:131], v[112:115], v[48:51]\n\t" \
    "v_add_u32 %[a], %[z], %[a]\n\t" \
    "v_add_u32 %[b0], %[z], %[b0]\n\t" \
    "v_add_u32 %[b1], %[z], %[b1]\n\t" \
    "s_waitcnt vmcnt(4) lgkmcnt(0)\n\t" \
    "s_barrier\n\t" \
    "v_mfma_f32_16x16x32_bf16 v[20:23], v[180:183], v[148:151], v[20:23]\n\t" \
    "ds_read_b64_tr_b16 v[116:117], %[b0] offset:0\n\t" \
    "ds_read_b64_tr_b16 v[118:119], %[b0] offset:1024\n\t" \
    "v_mfma_f32_16x16x32_bf16 v[24:27], v[188:191], v[148:151], v[24:27]\n\t" \
    "ds_read_b64_tr_b16 v[124:125], %[b1] offset:0\n\t" \
    "ds_read_b64_tr_b16 v[126:127], %[b1] offset:1024\n\t" \
    "s_add_u32 m0, %[d], 0\n\t" \
    "v_mfma_f32_16x16x32_bf16 v[20:23], v[184:187], v[152:155], v[20:23]\n\t" \
    "buffer_load_dwordx4 %[vb0], %[rb], %[y] offen lds\n\t" \
    "v_mfma_f32_16x16x32_bf16 v[24:27], v[192:195], v[152:155], v[24:27]\n\t" \
    "ds_read_b64_tr_b16 v[120:121], %[b0] offset:8192\n\t" \
    "ds_read_b64_tr_b16 v[122:123], %[b0] offset:9216\n\t" \
    "s_add_u32 m0, %[d], 8192\n\t" \
    "v_mfma_f32_16x16x32_bf16 v[28:31], v[180:183], v[156:159], v[28:31]\n\t" \
    "buffer_load_dwordx4 %[vb1], %[rb], %[y] offen lds\n\t" \
    "v_mfma_f32_16x16x32_bf16 v[32:35], v[188:191], v[156:159], v[32:35]\n\t" \
    "ds_read_b64_tr_b16 v[128:129], %[b1] offset:8192\n\t" \
    "ds_read_b64_tr_b16 v[130:131], %[b1] offset:9216\n\t" \
    "s_add_u32 m0, %[d], 32768\n\t" \
    "v_mfma_f32_16x16x32_bf16 v[28:31], v[184:187], v[160:163], v[28:31]\n\t" \
    "buffer_load_dwordx4 %[va0], %[ra], %[x] offen sc1 lds\n\t" \
    "v_mfma_f32_16x16x32_bf16 v[32:35], v[192:195], v[160:163], v[32:35]\n\t" \
    "ds_read_b128 v[84:87], %[a] offset:0\n\t" \
    "s_add_u32 m0, %[d], 40960\n\t" \
    "v_mfma_f32_16x16x32_bf16 v[36:39], v[180:183], v[164:167], v[36:39]\n\t" \
    "buffer_load_dwordx4 %[va1], %[ra], %[x] offen sc1 lds\n\t" \
    "v_mfma_f32_16x16x32_bf16 v[40:43], v[188:191], v[164:167], v[40:43]\n\t" \
    "ds_read_b128 v[88:91], %[a] offset:1024\n\t" \
    "ds_read_b128 v[92:95], %[a] offset:2048\n\t" \
    "s_add_u32 %[t], %[t], 1\n\t" \
    "s_add_u32 %[d], %[lw], %[c]\n\t" \
    "s_add_u32 %[y], %[c], 49152\n\t" \
    "v_mfma_f32_16x16x32_bf16 v[36:39], v[184:187], v[168:171], v[36:39]\n\t" \
    "ds_read_b128 v[96:99], %[a] offset:3072\n\t" \
    "ds_read_b128 v[100:103], %[a] offset:4096\n\t" \
    "s_cmp_eq_u32 %[y], 147456\n\t" \
    "s_cselect_b32 %[y], 0, %[y]\n\t" \
    "s_sub_u32 %[z], %[y], %[c]\n\t" \
    "v_mfma_f32_16x16x32_bf16 v[40:43], v[192:195], v[168:171], v[40:43]\n\t" \
    "ds_read_b128 v[104:107], %[a] offset:5120\n\t" \
    "ds_read_b128 v[108:111], %[a] offset:6144\n\t" \
    "s_mov_b32 %[c], %[y]\n\t" \
    "s_add_u32 %[x], %[t], 3\n\t" \
    "s_sub_u32 %[y], %[x], %[nt]\n\t" \
    "v_mfma_f32_16x16x32_bf16 v[44:47], v[180:183], v[172:175], v[44:47]\n\t" \
    "ds_read_b128 v[112:115], %[a] offset:7168\n\t" \
    "s_cmp_lt_u32 %[x], %[nt]\n\t" \
    "s_cselect_b32 %[x], %[x], %[y]\n\t" \
    "s_cselect_b32 m0, %[cb], %[cn]\n\t" \
    "v_mfma_f32_16x16x32_bf16 v[48:51], v[188:191], v[172:175], v[48:51]\n\t" \
    "s_mul_i32 %[y], %[x], %[kb]\n\t" \
    "s_add_u32 %[y], %[y], m0\n\t" \
    "s_lshl_b32 %[x], %[x], 11\n\t" \
    "v_mfma_f32_16x16x32_bf16 v[44:47], v[184:187], v[176:179], v[44:47]\n\t" \
    "s_add_u32 %[x], %[x], %[ca]\n\t" \
    "s_cmp_lt_u32 %[t], %[nt]\n\t" \
    "v_mfma_f32_16x16x32_bf16 v[48:51], v[192:195], v[176:179], v[48:51]\n\t" \
    "v_add_u32 %[a], %[z], %[a]\n\t" \
    "v_add_u32 %[b0], %[z], %[b0]\n\t" \
    "v_add_u32 %[b1], %[z], %[b1]\n\t" \
    "s_cbranch_scc1 L_%=\n\t" \
    "s_waitcnt lgkmcnt(0)\n\t" \
    "s_nop 7\n\t" \
    : "=&{v[20:23]}"(acc[0][0][0][0]), "=&{v[24:27]}"(acc[0][0][0][1]), "=&{v[28:31]}"(acc[0][0][1][0]), "=&{v[32:35]}"(acc[0][0][1][1]), "=&{v[36:39]}"(acc[0][0][2][0]), "=&{v[40:43]}"(acc[0][0][2][1]), "=&{v[44:47]}"(acc[0][0][3][0]), "=&{v[48:51]}"(acc[0][0][3][1]), "=&{v[52:55]}"(acc[0][1][0][0]), "=&{v[56:59]}"(acc[0][1][0][1]), "=&{v[60:63]}"(acc[0][1][1][0]), "=&{v[64:67]}"(acc[0][1][1][1]), "=&{v[68:71]}"(acc[0][1][2][0]), "=&{v[72:75]}"(acc[0][1][2][1]), "=&{v[76:79]}"(acc[0][1][3][0]), "=&{v[80:83]}"(acc[0][1][3][1]), [a] "+v"(aoff), [b0] "+v"(boff0), [b1] "+v"(boff1), [c] "+s"(cur), [t] "+s"(t), [d] "=&s"(sd), [x] "=&s"(sx), [y] "=&s"(sy), [z] "=&s"(sz) \
    : [va0] "v"(voffA[0]), [va1] "v"(voffA[1]), [vb0] "v"(voffB[0]), [vb1] "v"(voffB[1]), [ra] "s"(ra), [rb] "s"(rb), [lw] "s"(ldsw), [nt] "s"(nt), [kb] "s"(kB), [ca] "s"(cA), [cb] "s"(cB), [cn] "s"(cN) \
    : "memory", "scc", "v84", "v85", "v86", "v87", "v88", "v89", "v90", "v91", "v92", "v93", "v94", "v95", "v96", "v97", "v98", "v99", "v100", "v101", "v102", "v103", "v104", "v105", "v106", "v107", "v108", "v109", "v110", "v111", "v112", "v113", "v114", "v115", "v116", "v117", "v118", "v119", "v120", "v121", "v122", "v123", "v124", "v125", "v126", "v127", "v128", "v129", "v130", "v131", "v132", "v133", "v134", "v135", "v136", "v137", "v138", "v139", "v140", "v141", "v142", "v143", "v144", "v145", "v146", "v147", "v148", "v149", "v150", "v151", "v152", "v153", "v154", "v155", "v156", "v157", "v158", "v159", "v160", "v161", "v162", "v163", "v164", "v165", "v166", "v167", "v168", "v169", "v170", "v171", "v172", "v173", "v174", "v175", "v176", "v177", "v178", "v179", "v180", "v181", "v182", "v183", "v184", "v185", "v186", "v187", "v188", "v189", "v190", "v191", "v192", "v193", "v194", "v195", "v196", "v197", "v198", "v199", "v200", "v201", "v202", "v203", "v204", "v205", "v206", "v207", "v208", "v209", "v210", "v211")

#define H_LOOP2() \
  asm volatile( \
    "v_mov_b32 v20, 0\n\t" \
    "v_mov_b32 v21, 0\n\t" \
    "v_mov_b32 v22, 0\n\t" \
    "v_mov_b32 v23, 0\n\t" \
    "v_mov_b32 v24, 0\n\t" \
    "v_mov_b32 v25, 0\n\t" \
    "v_mov_b32 v26, 0\n\t" \
    "v_mov_b32 v27, 0\n\t" \
    "v_mov_b32 v28, 0\n\t" \
    "v_mov_b32 v29, 0\n\t" \
    "v_mov_b32 v30, 0\n\t" \
    "v_mov_b32 v31, 0\n\t" \
    "v_mov_b32 v32, 0\n\t" \
    "v_mov_b32 v33, 0\n\t" \
    "v_mov_b32 v34, 0\n\t" \
    "v_mov_b32 v35, 0\n\t" \
    "v_mov_b32 v36, 0\n\t" \
    "v_mov_b32 v37, 0\n\t" \
    "v_mov_b32 v38, 0\n\t" \
    "v_mov_b32 v39, 0\n\t" \
    "v_mov_b32 v40, 0\n\t" \
    "v_mov_b32 v41, 0\n\t" \
    "v_mov_b32 v42, 0\n\t" \
    "v_mov_b32 v43, 0\n\t" \
    "v_mov_b32 v44, 0\n\t" \
    "v_mov_b32 v45, 0\n\t" \
    "v_mov_b32 v46, 0\n\t" \
    "v_mov_b32 v47, 0\n\t" \
    "v_mov_b32 v48, 0\n\t" \
    "v_mov_b32 v49, 0\n\t" \
    "v_mov_b32 v50, 0\n\t" \
    "v_mov_b32 v51, 0\n\t" \
    "v_mov_b32 v52, 0\n\t" \
    "v_mov_b32 v53, 0\n\t" \
    "v_mov_b32 v54, 0\n\t" \
    "v_mov_b32 v55, 0\n\t" \
    "v_mov_b32 v56, 0\n\t" \
    "v_mov_b32 v57, 0\n\t" \
    "v_mov_b32 v58, 0\n\t" \
    "v_mov_b32 v59, 0\n\t" \
    "v_mov_b32 v60, 0\n\t" \
    "v_mov_b32 v61, 0\n\t" \
    "v_mov_b32 v62, 0\n\t" \
    "v_mov_b32 v63, 0\n\t" \
    "v_mov_b32 v64, 0\n\t" \
    "v_mov_b32 v65, 0\n\t" \
    "v_mov_b32 v66, 0\n\t" \
    "v_mov_b32 v67, 0\n\t" \
    "v_mov_b32 v68, 0\n\t" \
    "v_mov_b32 v69, 0\n\t" \
    "v_mov_b32 v70, 0\n\t" \
    "v_mov_b32 v71, 0\n\t" \
    "v_mov_b32 v72, 0\n\t" \
    "v_mov_b32 v73, 0\n\t" \
    "v_mov_b32 v74, 0\n\t" \
    "v_mov_b32 v75, 0\n\t" \
    "v_mov_b32 v76, 0\n\t" \
    "v_mov_b32 v77, 0\n\t" \
    "v_mov_b32 v78, 0\n\t" \
    "v_mov_b32 v79, 0\n\t" \
    "v_mov_b32 v80, 0\n\t" \
    "v_mov_b32 v81, 0\n\t" \
    "v_mov_b32 v82, 0\n\t" \
    "v_mov_b32 v83, 0\n\t" \
    "s_waitcnt vmcnt(12)\n\t" \
    "s_barrier\n\t" \
    "ds_read_b64_tr_b16 v[116:117], %[b0] offset:0\n\t" \
    "ds_read_b64_tr_b16 v[118:119], %[b0] offset:1024\n\t" \
    "ds_read_b64_tr_b16 v[124:125], %[b1] offset:0\n\t" \
    "ds_read_b64_tr_b16 v[126:127], %[b1] offset:1024\n\t" \
    "ds_read_b64_tr_b16 v[120:121], %[b0] offset:8192\n\t" \
    "ds_read_b64_tr_b16 v[122:123], %[b0] offset:9216\n\t" \
    "ds_read_b64_tr_b16 v[128:129], %[b1] offset:8192\n\t" \
    "ds_read_b64_tr_b16 v[130:131], %[b1] offset:9216\n\t" \
    "ds_read_b128 v[84:87], %[a] offset:0\n\t" \
    "ds_read_b128 v[88:91], %[a] offset:1024\n\t" \
    "ds_read_b128 v[92:95], %[a] offset:2048\n\t" \
    "ds_read_b128 v[96:99], %[a] offset:3072\n\t" \
    "ds_read_b128 v[100:103], %[a] offset:4096\n\t" \
    "ds_read_b128 v[104:107], %[a] offset:5120\n\t" \
    "ds_read_b128 v[108:111], %[a] offset:6144\n\t" \
    "ds_read_b128 v[112:115], %[a] offset:7168\n\t" \
    "ds_read_b64_tr_b16 v[132:133], %[b0] offset:16384\n\t" \
    "ds_read_b64_tr_b16 v[134:135], %[b0] offset:17408\n\t" \
    "ds_read_b64_tr_b16 v[140:141], %[b1] offset:16384\n\t" \
    "ds_read_b64_tr_b16 v[142:143], %[b1] offset:17408\n\t" \
    "ds_read_b64_tr_b16 v[136:137], %[b0] offset:24576\n\t" \
    "ds_read_b64_tr_b16 v[138:139], %[b0] offset:25600\n\t" \
    "ds_read_b64_tr_b16 v[144:145], %[b1] offset:24576\n\t" \
    "ds_read_b64_tr_b16 v[146:147], %[b1] offset:25600\n\t" \
    "s_add_u32 %[d], %[lw], %[c]\n\t" \
    "s_add_u32 %[y], %[c], 49152\n\t" \
    "s_cmp_eq_u32 %[y], 147456\n\t" \
    "s_cselect_b32 %[y], 0, %[y]\n\t" \
    "s_sub_u32 %[z], %[y], %[c]\n\t" \
    "s_mov_b32 %[c], %[y]\n\t" \
    "s_add_u32 %[x], %[t], 3\n\t" \
    "s_sub_u32 %[y], %[x], %[nt]\n\t" \
    "s_cmp_lt_u32 %[x], %[nt]\n\t" \
    "s_cselect_b32 %[x], %[x], %[y]\n\t" \
    "s_cselect_b32 m0, %[cb], %[cn]\n\t" \
    "s_mul_i32 %[y], %[x], %[kb]\n\t" \
    "s_add_u32 %[y], %[y], m0\n\t" \
    "s_lshl_b32 %[x], %[x], 11\n\t" \
    "s_add_u32 %[x], %[x], %[ca]\n\t" \
    "v_add_u32 %[a], %[z], %[a]\n\t" \
    "v_add_u32 %[b0], %[z], %[b0]\n\t" \
    "v_add_u32 %[b1], %[z], %[b1]\n\t" \
    "L_%=:\n\t" \
    "s_waitcnt vmcnt(6) lgkmcnt(0)\n\t" \
    "s_barrier\n\t" \
    "v_mfma_f32_16x16x32_bf16 v[20:23], v[116:119], v[84:87], v[20:23]\n\t" \
    "ds_read_b64_tr_b16 v[180:181], %[b0] offset:0\n\t" \
    "ds_read_b64_tr_b16 v[182:183], %[b0] offset:1024\n\t" \
    "v_mfma_f32_16x16x32_bf16 v[24:27], v[124:127], v[84:87], v[24:27]\n\t" \
    "ds_read_b64_tr_b16 v[188:189], %[b1] offset:0\n\t" \
    "ds_read_b64_tr_b16 v[190:191], %[b1] offset:1024\n\t" \
    "v_mfma_f32_16x16x32_bf16 v[20:23], v[120:123], v[88:91], v[20:23]\n\t" \
    "ds_read_b64_tr_b16 v[184:185], %[b0] offset:8192\n\t" \
    "ds_read_b64_tr_b16 v[186:187], %[b0] offset:9216\n\t" \
    "s_add_u32 m0, %[d], 0\n\t" \
    "v_mfma_f32_16x16x32_bf16 v[24:27], v[128:131], v[88:91], v[24:27]\n\t" \
    "buffer_load_dwordx4 %[vb0], %[rb], %[y] offen lds\n\t" \
    "v_mfma_f32_16x16x32_bf16 v[28:31], v[116:119], v[92:95], v[28:31]\n\t" \
    "ds_read_b64_tr_b16 v[192:193], %[b1] offset:8192\n\t" \
    "v_mfma_f32_16x16x32_bf16 v[32:35], v[124:127], v[92:95], v[32:35]\n\t" \
    "ds_read_b64_tr_b16 v[194:195], %[b1] offset:9216\n\t" \
    "v_mfma_f32_16x16x32_bf16 v[28:31], v[120:123], v[96:99], v[28:31]\n\t" \
    "ds_read_b128 v[148:151], %[a] offset:0\n\t" \
    "s_add_u32 m0, %[d], 8192\n\t" \
    "v_mfma_f32_16x16x32_bf16 v[32:35], v[128:131], v[96:99], v[32:35]\n\t" \
    "buffer_load_dwordx4 %[vb1], %[rb], %[y] offen lds\n\t" \
    "v_mfma_f32_16x16x32_bf16 v[36:39], v[116:119], v[100:103], v[36:39]\n\t" \
    "ds_read_b128 v[152:155], %[a] offset:1024\n\t" \
    "v_mfma_f32_16x16x32_bf16 v[40:43], v[124:127], v[100:103], v[40:43]\n\t" \
    "ds_read_b128 v[156:159], %[a] offset:2048\n\t" \
    "v_mfma_f32_16x16x32_bf16 v[36:39], v[120:123], v[104:107], v[36:39]\n\t" \
    "ds_read_b128 v[160:163], %[a] offset:3072\n\t" \
    "s_add_u32 m0, %[d], 32768\n\t" \
    "v_mfma_f32_16x16x32_bf16 v[40:43], v[128:131], v[104:107], v[40:43]\n\t" \
    "buffer_load_dwordx4 %[va0], %[ra], %[x] offen sc1 lds\n\t" \
    "v_mfma_f32_16x16x32_bf16 v[44:47], v[116:119], v[108:111], v[44:47]\n\t" \
    "ds_read_b128 v[164:167], %[a] offset:4096\n\t" \
    "v_mfma_f32_16x16x32_bf16 v[48:51], v[124:127], v[108:111], v[48:51]\n\t" \
    "ds_read_b128 v[168:171], %[a] offset:5120\n\t" \
    "v_mfma_f32_16x16x32_bf16 v[44:47], v[120:123], v[112:115], v[44:47]\n\t" \
    "ds_read_b128 v[172:175], %[a] offset:6144\n\t" \
    "s_add_u32 m0, %[d], 40960\n\t" \
    "v_mfma_f32_16x16x32_bf16 v[48:51], v[128:131], v[112:115], v[48:51]\n\t" \
    "buffer_load_dwordx4 %[va1], %[ra], %[x] offen sc1 lds\n\t" \
    "v_mfma_f32_16x16x32_bf16 v[52:55], v[132:135], v[84:87], v[52:55]\n\t" \
    "ds_read_b128 v[176:179], %[a] offset:7168\n\t" \
    "v_mfma_f32_16x16x32_bf16 v[56:59], v[140:143], v[84:87], v[56:59]\n\t" \
    "ds_read_b64_tr_b16 v[196:197], %[b0] offset:16384\n\t" \
    "v_mfma_f32_16x16x32_bf16 v[52:55], v[136:139], v[88:91], v[52:55]\n\t" \
    "ds_read_b64_tr_b16 v[198:199], %[b0] offset:17408\n\t" \
    "s_add_u32 m0, %[d], 16128\n\t" \
    "v_mfma_f32_16x16x32_bf16 v[56:59], v[144:147], v[88:91], v[56:59]\n\t" \
    "buffer_load_dwordx4 %[vb0], %[rb], %[y] offen offset:256 lds\n\t" \
    "v_mfma_f32_16x16x32_bf16 v[60:63], v[132:135], v[92:95], v[60:63]\n\t" \
    "ds_read_b64_tr_b16 v[204:205], %[b1] offset:16384\n\t" \
    "v_mfma_f32_16x16x32_bf16 v[64:67], v[140:143], v[92:95], v[64:67]\n\t" \
    "ds_read_b64_tr_b16 v[206:207], %[b1] offset:17408\n\t" \
    "v_mfma_f32_16x16x32_bf16 v[60:63], v[136:139], v[96:99], v[60:63]\n\t" \
    "ds_read_b64_tr_b16 v[200:201], %[b0] offset:24576\n\t" \
    "s_add_u32 m0, %[d], 24320\n\t" \
    "v_mfma_f32_16x16x32_bf16 v[64:67], v[144:147], v[96:99], v[64:67]\n\t" \
    "buffer_load_dwordx4 %[vb1], %[rb], %[y] offen offset:256 lds\n\t" \
    "v_mfma_f32_16x16x32_bf16 v[68:71], v[132:135], v[100:103], v[68:71]\n\t" \
    "ds_read_b64_tr_b16 v[202:203], %[b0] offset:25600\n\t" \
    "s_add_u32 %[t], %[t], 1\n\t" \
    "s_add_u32 %[d], %[lw], %[c]\n\t" \
    "v_mfma_f32_16x16x32_bf16 v[72:75], v[140:143], v[100:103], v[72:75]\n\t" \
    "ds_read_b64_tr_b16 v[208:209], %[b1] offset:24576\n\t" \
    "s_add_u32 %[y], %[c], 49152\n\t" \
    "s_cmp_eq_u32 %[y], 147456\n\t" \
    "v_mfma_f32_16x16x32_bf16 v[68:71], v[136:139], v[104:107], v[68:71]\n\t" \
    "ds_read_b64_tr_b16 v[210:211], %[b1] offset:25600\n\t" \
    "s_cselect_b32 %[y], 0, %[y]\n\t" \
    "s_sub_u32 %[z], %[y], %[c]\n\t" \
    "v_mfma_f32_16x16x32_bf16 v[72:75], v[144:147], v[104:107], v[72:75]\n\t" \
    "s_mov_b32 %[c], %[y]\n\t" \
    "s_add_u32 %[x], %[t], 3\n\t" \
    "v_mfma_f32_16x16x32_bf16 v[76:79], v[132:135], v[108:111], v[76:79]\n\t" \
    "s_sub_u32 %[y], %[x], %[nt]\n\t" \
    "s_cmp_lt_u32 %[x], %[nt]\n\t" \
    "v_mfma_f32_16x16x32_bf16 v[80:83], v[140:143], v[108:111], v[80:83]\n\t" \
    "s_cselect_b32 %[x], %[x], %[y]\n\t" \
    "s_cselect_b32 m0, %[cb], %[cn]\n\t" \
    "v_mfma_f32_16x16x32_bf16 v[76:79], v[136:139], v[112:115], v[76:79]\n\t" \
    "s_mul_i32 %[y], %[x], %[kb]\n\t" \
    "s_add_u32 %[y], %[y], m0\n\t" \
    "v_mfma_f32_16x16x32_bf16 v[80:83], v[144:147], v[112:115], v[80:83]\n\t" \
    "s_lshl_b32 %[x], %[x], 11\n\t" \
    "s_add_u32 %[x], %[x], %[ca]\n\t" \
    "v_add_u32 %[a], %[z], %[a]\n\t" \
    "v_add_u32 %[b0], %[z], %[b0]\n\t" \
    "v_add_u32 %[b1], %[z], %[b1]\n\t" \
    "s_waitcnt vmcnt(6) lgkmcnt(0)\n\t" \
    "s_barrier\n\t" \
    "v_mfma_f32_16x16x32_bf16 v[20:23], v[180:183], v[148:151], v[20:23]\n\t" \
    "ds_read_b64_tr_b16 v[116:117], %[b0] offset:0\n\t" \
    "ds_read_b64_tr_b16 v[118:119], %[b0] offset:1024\n\t" \
    "v_mfma_f32_16x16x32_bf16 v[24:27], v[188:191], v[148:151], v[24:27]\n\t" \
    "ds_read_b64_tr_b16 v[124:125], %[b1] offset:0\n\t" \
    "ds_read_b64_tr_b16 v[126:127], %[b1] offset:1024\n\t" \
    "v_mfma_f32_16x16x32_bf16 v[20:23], v[184:187], v[152:155], v[20:23]\n\t" \
    "ds_read_b64_tr_b16 v[120:121], %[b0] offset:8192\n\t" \
    "ds_read_b64_tr_b16 v[122:123], %[b0] offset:9216\n\t" \
    "s_add_u32 m0, %[d], 0\n\t" \
    "v_mfma_f32_16x16x32_bf16 v[24:27], v[192:195], v[152:155], v[24:27]\n\t" \
    "buffer_load_dwordx4 %[vb0], %[rb], %[y] offen lds\n\t" \
    "v_mfma_f32_16x16x32_bf16 v[28:31], v[180:183], v[156:159], v[28:31]\n\t" \
    "ds_read_b64_tr_b16 v[128:129], %[b1] offset:8192\n\t" \
    "v_mfma_f32_16x16x32_bf16 v[32:35], v[188:191], v[156:159], v[32:35]\n\t" \
    "ds_read_b64_tr_b16 v[130:131], %[b1] offset:9216\n\t" \
    "v_mfma_f32_16x16x32_bf16 v[28:31], v[184:187], v[160:163], v[28:31]\n\t" \
    "ds_read_b128 v[84:87], %[a] offset:0\n\t" \
    "s_add_u32 m0, %[d], 8192\n\t" \
    "v_mfma_f32_16x16x32_bf16 v[32:35], v[192:195], v[160:163], v[32:35]\n\t" \
    "buffer_load_dwordx4 %[vb1], %[rb], %[y] offen lds\n\t" \
    "v_mfma_f32_16x16x32_bf16 v[36:39], v[180:183], v[164:167], v[36:39]\n\t" \
    "ds_read_b128 v[88:91], %[a] offset:1024\n\t" \
    "v_mfma_f32_16x16x32_bf16 v[40:43], v[188:191], v[164:167], v[40:43]\n\t" \
    "ds_read_b128 v[92:95], %[a] offset:2048\n\t" \
    "v_mfma_f32_16x16x32_bf16 v[36:39], v[184:187], v[168:171], v[36:39]\n\t" \
    "ds_read_b128 v[96:99], %[a] offset:3072\n\t" \
    "s_add_u32 m0, %[d], 32768\n\t" \
    "v_mfma_f32_16x16x32_bf16 v[40:43], v[192:195], v[168:171], v[40:43]\n\t" \
    "buffer_load_dwordx4 %[va0], %[ra], %[x] offen sc1 lds\n\t" \
    "v_mfma_f32_16x16x32_bf16 v[44:47], v[180:183], v[172:175], v[44:47]\n\t" \
    "ds_read_b128 v[100:103], %[a] offset:4096\n\t" \
    "v_mfma_f32_16x16x32_bf16 v[48:51], v[188:191], v[172:175], v[48:51]\n\t" \
    "ds_read_b128 v[104:107], %[a] offset:5120\n\t" \
    "v_mfma_f32_16x16x32_bf16 v[44:47], v[184:187], v[176:179], v[44:47]\n\t" \
    "ds_read_b128 v[108:111], %[a] offset:6144\n\t" \
    "s_add_u32 m0, %[d], 40960\n\t" \
    "v_mfma_f32_16x16x32_bf16 v[48:51], v[192:195], v[176:179], v[48:51]\n\t" \
    "buffer_load_dwordx4 %[va1], %[ra], %[x] offen sc1 lds\n\t" \
    "v_mfma_f32_16x16x32_bf16 v[52:55], v[196:199], v[148:151], v[52:55]\n\t" \
    "ds_read_b128 v[112:115], %[a] offset:7168\n\t" \
    "v_mfma_f32_16x16x32_bf16 v[56:59], v[204:207], v[148:151], v[56:59]\n\t" \
    "ds_read_b64_tr_b16 v[132:133], %[b0] offset:16384\n\t" \
    "v_mfma_f32_16x16x32_bf16 v[52:55], v[200:203], v[152:155], v[52:55]\n\t" \
    "ds_read_b64_tr_b16 v[134:135], %[b0] offset:17408\n\t" \
    "s_add_u32 m0, %[d], 16128\n\t" \
    "v_mfma_f32_16x16x32_bf16 v[56:59], v[208:211], v[152:155], v[56:59]\n\t" \
    "buffer_load_dwordx4 %[vb0], %[rb], %[y] offen offset:256 lds\n\t" \
    "v_mfma_f32_16x16x32_bf16 v[60:63], v[196:199], v[156:159], v[60:63]\n\t" \
    "ds_read_b64_tr_b16 v[140:141], %[b1] offset:16384\n\t" \
    "v_mfma_f32_16x16x32_bf16 v[64:67], v[204:207], v[156:159], v[64:67]\n\t" \
    "ds_read_b64_tr_b16 v[142:143], %[b1] offset:17408\n\t" \
    "v_mfma_f32_16x16x32_bf16 v[60:63], v[200:203], v[160:163], v[60:63]\n\t" \
    "ds_read_b64_tr_b16 v[136:137], %[b0] offset:24576\n\t" \
    "s_add_u32 m0, %[d], 24320\n\t" \
    "v_mfma_f32_16x16x32_bf16 v[64:67], v[208:211], v[160:163], v[64:67]\n\t" \
    "buffer_load_dwordx4 %[vb1], %[rb], %[y] offen offset:256 lds\n\t" \
    "v_mfma_f32_16x16x32_bf16 v[68:71], v[196:199], v[164:167], v[68:71]\n\t" \
    "ds_read_b64_tr_b16 v[138:139], %[b0] offset:25600\n\t" \
    "s_add_u32 %[t], %[t], 1\n\t" \
    "s_add_u32 %[d], %[lw], %[c]\n\t" \
    "s_add_u32 %[y], %[c], 49152\n\t" \
    "v_mfma_f32_16x16x32_bf16 v[72:75], v[204:207], v[164:167], v[72:75]\n\t" \
    "ds_read_b64_tr_b16 v[144:145], %[b1] offset:24576\n\t" \
    "s_cmp_eq_u32 %[y], 147456\n\t" \
    "s_cselect_b32 %[y], 0, %[y]\n\t" \
    "s_sub_u32 %[z], %[y], %[c]\n\t" \
    "v_mfma_f32_16x16x32_bf16 v[68:71], v[200:203], v[168:171], v[68:71]\n\t" \
    "ds_read_b64_tr_b16 v[146:147], %[b1] offset:25600\n\t" \
    "s_mov_b32 %[c], %[y]\n\t" \
    "s_add_u32 %[x], %[t], 3\n\t" \
    "s_sub_u32 %[y], %[x], %[nt]\n\t" \
    "v_mfma_f32_16x16x32_bf16 v[72:75], v[208:211], v[168:171], v[72:75]\n\t" \
    "s_cmp_lt_u32 %[x], %[nt]\n\t" \
    "s_cselect_b32 %[x], %[x], %[y]\n\t" \
    "s_cselect_b32 m0, %[cb], %[cn]\n\t" \
    "v_mfma_f32_16x16x32_bf16 v[76:79], v[196:199], v[172:175], v[76:79]\n\t" \
    "s_mul_i32 %[y], %[x], %[kb]\n\t" \
    "s_add_u32 %[y], %[y], m0\n\t" \
    "s_lshl_b32 %[x], %[x], 11\n\t" \
    "v_mfma_f32_16x16x32_bf16 v[80:83], v[204:207], v[172:175], v[80:83]\n\t" \
    "s_add_u32 %[x], %[x], %[ca]\n\t" \
    "s_cmp_lt_u32 %[t], %[nt]\n\t" \
    "v_mfma_f32_16x16x32_bf16 v[76:79], v[200:203], v[176:179], v[76:79]\n\t" \
    "v_mfma_f32_16x16x32_bf16 v[80:83], v[208:211], v[176:179], v[80:83]\n\t" \
    "v_add_u32 %[a], %[z], %[a]\n\t" \
    "v_add_u32 %[b0], %[z], %[b0]\n\t" \
    "v_add_u32 %[b1], %[z], %[b1]\n\t" \
    "s_cbranch_scc1 L_%=\n\t" \
    "s_waitcnt lgkmcnt(0)\n\t" \
    "s_nop 7\n\t" \
    : "=&{v[20:23]}"(acc[0][0][0][0]), "=&{v[24:27]}"(acc[0][0][0][1]), "=&{v[28:31]}"(acc[0][0][1][0]), "=&{v[32:35]}"(acc[0][0][1][1]), "=&{v[36:39]}"(acc[0][0][2][0]), "=&{v[40:43]}"(acc[0][0][2][1]), "=&{v[44:47]}"(acc[0][0][3][0]), "=&{v[48:51]}"(acc[0][0][3][1]), "=&{v[52:55]}"(acc[0][1][0][0]), "=&{v[56:59]}"(acc[0][1][0][1]), "=&{v[60:63]}"(acc[0][1][1][0]), "=&{v[64:67]}"(acc[0][1][1][1]), "=&{v[68:71]}"(acc[0][1][2][0]), "=&{v[72:75]}"(acc[0][1][2][1]), "=&{v[76:79]}"(acc[0][1][3][0]), "=&{v[80:83]}"(acc[0][1][3][1]), [a] "+v"(aoff), [b0] "+v"(boff0), [b1] "+v"(boff1), [c] "+s"(cur), [t] "+s"(t), [d] "=&s"(sd), [x] "=&s"(sx), [y] "=&s"(sy), [z] "=&s"(sz) \
    : [va0] "v"(voffA[0]), [va1] "v"(voffA[1]), [vb0] "v"(voffB[0]), [vb1] "v"(voffB[1]), [ra] "s"(ra), [rb] "s"(rb), [lw] "s"(ldsw), [nt] "s"(nt), [kb] "s"(kB), [ca] "s"(cA), [cb] "s"(cB), [cn] "s"(cN) \
    : "memory", "scc", "v84", "v85", "v86", "v87", "v88", "v89", "v90", "v91", "v92", "v93", "v94", "v95", "v96", "v97", "v98", "v99", "v100", "v101", "v102", "v103", "v104", "v105", "v106", "v107", "v108", "v109", "v110", "v111", "v112", "v113", "v114", "v115", "v116", "v117", "v118", "v119", "v120", "v121", "v122", "v123", "v124", "v125", "v126", "v127", "v128", "v129", "v130", "v131", "v132", "v133", "v134", "v135", "v136", "v137", "v138", "v139", "v140", "v141", "v142", "v143", "v144", "v145", "v146", "v147", "v148", "v149", "v150", "v151", "v152", "v153", "v154", "v155", "v156", "v157", "v158", "v159", "v160", "v161", "v162", "v163", "v164", "v165", "v166", "v167", "v168", "v169", "v170", "v171", "v172", "v173", "v174", "v175", "v176", "v177", "v178", "v179", "v180", "v181", "v182", "v183", "v184", "v185", "v186", "v187", "v188", "v189", "v190", "v191", "v192", "v193", "v194", "v195", "v196", "v197", "v198", "v199", "v200", "v201", "v202", "v203", "v204", "v205", "v206", "v207", "v208", "v209", "v210", "v211")

#define H_LOOP2S() \
  asm volatile( \
    "v_mov_b32 v20, 0\n\t" \
    "v_mov_b32 v21, 0\n\t" \
    "v_mov_b32 v22, 0\n\t" \
    "v_mov_b32 v23, 0\n\t" \
    "v_mov_b32 v24, 0\n\t" \
    "v_mov_b32 v25, 0\n\t" \
    "v_mov_b32 v26, 0\n\t" \
    "v_mov_b32 v27, 0\n\t" \
    "v_mov_b32 v28, 0\n\t" \
    "v_mov_b32 v29, 0\n\t" \
    "v_mov_b32 v30, 0\n\t" \
    "v_mov_b32 v31, 0\n\t" \
    "v_mov_b32 v32, 0\n\t" \
    "v_mov_b32 v33, 0\n\t" \
    "v_mov_b32 v34, 0\n\t" \
    "v_mov_b32 v35, 0\n\t" \
    "v_mov_b32 v36, 0\n\t" \
    "v_mov_b32 v37, 0\n\t" \
    "v_mov_b32 v38, 0\n\t" \
    "v_mov_b32 v39, 0\n\t" \
    "v_mov_b32 v40, 0\n\t" \
    "v_mov_b32 v41, 0\n\t" \
    "v_mov_b32 v42, 0\n\t" \
    "v_mov_b32 v43, 0\n\t" \
    "v_mov_b32 v44, 0\n\t" \
    "v_mov_b32 v45, 0\n\t" \
    "v_mov_b32 v46, 0\n\t" \
    "v_mov_b32 v47, 0\n\t" \
    "v_mov_b32 v48, 0\n\t" \
    "v_mov_b32 v49, 0\n\t" \
    "v_mov_b32 v50, 0\n\t" \
    "v_mov_b32 v51, 0\n\t" \
    "v_mov_b32 v52, 0\n\t" \
    "v_mov_b32 v53, 0\n\t" \
    "v_mov_b32 v54, 0\n\t" \
    "v_mov_b32 v55, 0\n\t" \
    "v_mov_b32 v56, 0\n\t" \
    "v_mov_b32 v57, 0\n\t" \
    "v_mov_b32 v58, 0\n\t" \
    "v_mov_b32 v59, 0\n\t" \
    "v_mov_b32 v60, 0\n\t" \
    "v_mov_b32 v61, 0\n\t" \
    "v_mov_b32 v62, 0\n\t" \
    "v_mov_b32 v63, 0\n\t" \
    "v_mov_b32 v64, 0\n\t" \
    "v_mov_b32 v65, 0\n\t" \
    "v_mov_b32 v66, 0\n\t" \
    "v_mov_b32 v67, 0\n\t" \
    "v_mov_b32 v68, 0\n\t" \
    "v_mov_b32 v69, 0\n\t" \
    "v_mov_b32 v70, 0\n\t" \
    "v_mov_b32 v71, 0\n\t" \
    "v_mov_b32 v72, 0\n\t" \
    "v_mov_b32 v73, 0\n\t" \
    "v_mov_b32 v74, 0\n\t" \
    "v_mov_b32 v75, 0\n\t" \
    "v_mov_b32 v76, 0\n\t" \
    "v_mov_b32 v77, 0\n\t" \
    "v_mov_b32 v78, 0\n\t" \
    "v_mov_b32 v79, 0\n\t" \
    "v_mov_b32 v80, 0\n\t" \
    "v_mov_b32 v81, 0\n\t" \
    "v_mov_b32 v82, 0\n\t" \
    "v_mov_b32 v83, 0\n\t" \
    "s_waitcnt vmcnt(12)\n\t" \
    "s_barrier\n\t" \
    "ds_read_b64_tr_b16 v[116:117], %[b0] offset:0\n\t" \
    "ds_read_b64_tr_b16 v[118:119], %[b0] offset:1024\n\t" \
    "ds_read_b64_tr_b16 v[124:125], %[b1] offset:0\n\t" \
    "ds_read_b64_tr_b16 v[126:127], %[b1] offset:1024\n\t" \
    "ds_read_b64_tr_b16 v[120:121], %[b0] offset:8192\n\t" \
    "ds_read_b64_tr_b16 v[122:123], %[b0] offset:9216\n\t" \
    "ds_read_b64_tr_b16 v[128:129], %[b1] offset:8192\n\t" \
    "ds_read_b64_tr_b16 v[130:131], %[b1] offset:9216\n\t" \
    "ds_read_b128 v[84:87], %[a] offset:0\n\t" \
    "ds_read_b128 v[88:91], %[a] offset:1024\n\t" \
    "ds_read_b128 v[92:95], %[a] offset:2048\n\t" \
    "ds_read_b128 v[96:99], %[a] offset:3072\n\t" \
    "ds_read_b128 v[100:103], %[a] offset:4096\n\t" \
    "ds_read_b128 v[104:107], %[a] offset:5120\n\t" \
    "ds_read_b128 v[108:111], %[a] offset:6144\n\t" \
    "ds_read_b128 v[112:115], %[a] offset:7168\n\t" \
    "ds_read_b64_tr_b16 v[132:133], %[b0] offset:16384\n\t" \
    "ds_read_b64_tr_b16 v[134:135], %[b0] offset:17408\n\t" \
    "ds_read_b64_tr_b16 v[140:141], %[b1] offset:16384\n\t" \
    "ds_read_b64_tr_b16 v[142:143], %[b1] offset:17408\n\t" \
    "ds_read_b64_tr_b16 v[136:137], %[b0] offset:24576\n\t" \
    "ds_read_b64_tr_b16 v[138:139], %[b0] offset:25600\n\t" \
    "ds_read_b64_tr_b16 v[144:145], %[b1] offset:24576\n\t" \
    "ds_read_b64_tr_b16 v[146:147], %[b1] offset:25600\n\t" \
    "s_add_u32 %[d], %[lw], %[c]\n\t" \
    "s_add_u32 %[y], %[c], 49152\n\t" \
    "s_cmp_eq_u32 %[y], 147456\n\t" \
    "s_cselect_b32 %[y], 0, %[y]\n\t" \
    "s_sub_u32 %[z], %[y], %[c]\n\t" \
    "s_mov_b32 %[c], %[y]\n\t" \
    "s_add_u32 %[x], %[t], 3\n\t" \
    "s_sub_u32 %[y], %[x], %[nt]\n\t" \
    "s_cmp_lt_u32 %[x], %[nt]\n\t" \
    "s_cselect_b32 %[x], %[x], %[y]\n\t" \
    "s_cselect_b32 m0, %[cb], %[cn]\n\t" \
    "s_mul_i32 %[y], %[x], %[kb]\n\t" \
    "s_add_u32 %[y], %[y], m0\n\t" \
    "s_lshl_b32 %[x], %[x], 11\n\t" \
    "s_add_u32 %[x], %[x], %[ca]\n\t" \
    "v_add_u32 %[a], %[z], %[a]\n\t" \
    "v_add_u32 %[b0], %[z], %[b0]\n\t" \
    "v_add_u32 %[b1], %[z], %[b1]\n\t" \
    "L_%=:\n\t" \
    "s_waitcnt vmcnt(6) lgkmcnt(0)\n\t" \
    "s_barrier\n\t" \
    "s_sleep 2\n\t" \
    "s_nop 5\n\t" \
    "v_mfma_f32_16x16x32_bf16 v[20:23], v[116:119], v[84:87], v[20:23]\n\t" \
    "ds_read_b64_tr_b16 v[180:181], %[b0] offset:0\n\t" \
    "ds_read_b64_tr_b16 v[182:183], %[b0] offset:1024\n\t" \
    "v_mfma_f32_16x16x32_bf16 v[24:27], v[124:127], v[84:87], v[24:27]\n\t" \
    "ds_read_b64_tr_b16 v[188:189], %[b1] offset:0\n\t" \
    "ds_read_b64_tr_b16 v[190:191], %[b1] offset:1024\n\t" \
    "v_mfma_f32_16x16x32_bf16 v[20:23], v[120:123], v[88:91], v[20:23]\n\t" \
    "ds_read_b64_tr_b16 v[184:185], %[b0] offset:8192\n\t" \
    "ds_read_b64_tr_b16 v[186:187], %[b0] offset:9216\n\t" \
    "s_add_u32 m0, %[d], 0\n\t" \
    "v_mfma_f32_16x16x32_bf16 v[24:27], v[128:131], v[88:91], v[24:27]\n\t" \
    "buffer_load_dwordx4 %[vb0], %[rb], %[y] offen lds\n\t" \
    "v_mfma_f32_16x16x32_bf16 v[28:31], v[116:119], v[92:95], v[28:31]\n\t" \
    "ds_read_b64_tr_b16 v[192:193], %[b1] offset:8192\n\t" \
    "v_mfma_f32_16x16x32_bf16 v[32:35], v[124:127], v[92:95], v[32:35]\n\t" \
    "ds_read_b64_tr_b16 v[194:195], %[b1] offset:9216\n\t" \
    "v_mfma_f32_16x16x32_bf16 v[28:31], v[120:123], v[96:99], v[28:31]\n\t" \
    "ds_read_b128 v[148:151], %[a] offset:0\n\t" \
    "s_add_u32 m0, %[d], 8192\n\t" \
    "v_mfma_f32_16x16x32_bf16 v[32:35], v[128:131], v[96:99], v[32:35]\n\t" \
    "buffer_load_dwordx4 %[vb1], %[rb], %[y] offen lds\n\t" \
    "v_mfma_f32_16x16x32_bf16 v[36:39], v[116:119], v[100:103], v[36:39]\n\t" \
    "ds_read_b128 v[152:155], %[a] offset:1024\n\t" \
    "v_mfma_f32_16x16x32_bf16 v[40:43], v[124:127], v[100:103], v[40:43]\n\t" \
    "ds_read_b128 v[156:159], %[a] offset:2048\n\t" \
    "v_mfma_f32_16x16x32_bf16 v[36:39], v[120:123], v[104:107], v[36:39]\n\t" \
    "ds_read_b128 v[160:163], %[a] offset:3072\n\t" \
    "s_add_u32 m0, %[d], 32768\n\t" \
    "v_mfma_f32_16x16x32_bf16 v[40:43], v[128:131], v[104:107], v[40:43]\n\t" \
    "buffer_load_dwordx4 %[va0], %[ra], %[x] offen sc1 lds\n\t" \
    "v_mfma_f32_16x16x32_bf16 v[44:47], v[116:119], v[108:111], v[44:47]\n\t" \
    "ds_read_b128 v[164:167], %[a] offset:4096\n\t" \
    "v_mfma_f32_16x16x32_bf16 v[48:51], v[124:127], v[108:111], v[48:51]\n\t" \
    "ds_read_b128 v[168:171], %[a] offset:5120\n\t" \
    "v_mfma_f32_16x16x32_bf16 v[44:47], v[120:123], v[112:115], v[44:47]\n\t" \
    "ds_read_b128 v[172:175], %[a] offset:6144\n\t" \
    "s_add_u32 m0, %[d], 40960\n\t" \
    "v_mfma_f32_16x16x32_bf16 v[48:51], v[128:131], v[112:115], v[48:51]\n\t" \
    "buffer_load_dwordx4 %[va1], %[ra], %[x] offen sc1 lds\n\t" \
    "v_mfma_f32_16x16x32_bf16 v[52:55], v[132:135], v[84:87], v[52:55]\n\t" \
    "ds_read_b128 v[176:179], %[a] offset:7168\n\t" \
    "v_mfma_f32_16x16x32_bf16 v[56:59], v[140:143], v[84:87], v[56:59]\n\t" \
    "ds_read_b64_tr_b16 v[196:197], %[b0] offset:16384\n\t" \
    "v_mfma_f32_16x16x32_bf16 v[52:55], v[136:139], v[88:91], v[52:55]\n\t" \
    "ds_read_b64_tr_b16 v[198:199], %[b0] offset:17408\n\t" \
    "s_add_u32 m0, %[d], 16128\n\t" \
    "v_mfma_f32_16x16x32_bf16 v[56:59], v[144:147], v[88:91], v[56:59]\n\t" \
    "buffer_load_dwordx4 %[vb0], %[rb], %[y] offen offset:256 lds\n\t" \
    "v_mfma_f32_16x16x32_bf16 v[60:63], v[132:135], v[92:95], v[60:63]\n\t" \
    "ds_read_b64_tr_b16 v[204:205], %[b1] offset:16384\n\t" \
    "v_mfma_f32_16x16x32_bf16 v[64:67], v[140:143], v[92:95], v[64:67]\n\t" \
    "ds_read_b64_tr_b16 v[206:207], %[b1] offset:17408\n\t" \
    "v_mfma_f32_16x16x32_bf16 v[60:63], v[136:139], v[96:99], v[60:63]\n\t" \
    "ds_read_b64_tr_b16 v[200:201], %[b0] offset:24576\n\t" \
    "s_add_u32 m0, %[d], 24320\n\t" \
    "v_mfma_f32_16x16x32_bf16 v[64:67], v[144:147], v[96:99], v[64:67]\n\t" \
    "buffer_load_dwordx4 %[vb1], %[rb], %[y] offen offset:256 lds\n\t" \
    "v_mfma_f32_16x16x32_bf16 v[68:71], v[132:135], v[100:103], v[68:71]\n\t" \
    "ds_read_b64_tr_b16 v[202:203], %[b0] offset:25600\n\t" \
    "s_add_u32 %[t], %[t], 1\n\t" \
    "s_add_u32 %[d], %[lw], %[c]\n\t" \
    "v_mfma_f32_16x16x32_bf16 v[72:75], v[140:143], v[100:103], v[72:75]\n\t" \
    "ds_read_b64_tr_b16 v[208:209], %[b1] offset:24576\n\t" \
    "s_add_u32 %[y], %[c], 49152\n\t" \
    "s_cmp_eq_u32 %[y], 147456\n\t" \
    "v_mfma_f32_16x16x32_bf16 v[68:71], v[136:139], v[104:107], v[68:71]\n\t" \
    "ds_read_b64_tr_b16 v[210:211], %[b1] offset:25600\n\t" \
    "s_cselect_b32 %[y], 0, %[y]\n\t" \
    "s_sub_u32 %[z], %[y], %[c]\n\t" \
    "v_mfma_f32_16x16x32_bf16 v[72:75], v[144:147], v[104:107], v[72:75]\n\t" \
    "s_mov_b32 %[c], %[y]\n\t" \
    "s_add_u32 %[x], %[t], 3\n\t" \
    "v_mfma_f32_16x16x32_bf16 v[76:79], v[132:135], v[108:111], v[76:79]\n\t" \
    "s_sub_u32 %[y], %[x], %[nt]\n\t" \
    "s_cmp_lt_u32 %[x], %[nt]\n\t" \
    "v_mfma_f32_16x16x32_bf16 v[80:83], v[140:143], v[108:111], v[80:83]\n\t" \
    "s_cselect_b32 %[x], %[x], %[y]\n\t" \
    "s_cselect_b32 m0, %[cb], %[cn]\n\t" \
    "v_mfma_f32_16x16x32_bf16 v[76:79], v[136:139], v[112:115], v[76:79]\n\t" \
    "s_mul_i32 %[y], %[x], %[kb]\n\t" \
    "s_add_u32 %[y], %[y], m0\n\t" \
    "v_mfma_f32_16x16x32_bf16 v[80:83], v[144:147], v[112:115], v[80:83]\n\t" \
    "s_lshl_b32 %[x], %[x], 11\n\t" \
    "s_add_u32 %[x], %[x], %[ca]\n\t" \
    "v_add_u32 %[a], %[z], %[a]\n\t" \
    "v_add_u32 %[b0], %[z], %[b0]\n\t" \
    "v_add_u32 %[b1], %[z], %[b1]\n\t" \
    "s_waitcnt vmcnt(6) lgkmcnt(0)\n\t" \
    "s_barrier\n\t" \
    "s_sleep 2\n\t" \
    "s_nop 5\n\t" \
    "v_mfma_f32_16x16x32_bf16 v[20:23], v[180:183], v[148:151], v[20:23]\n\t" \
    "ds_read_b64_tr_b16 v[116:117], %[b0] offset:0\n\t" \
    "ds_read_b64_tr_b16 v[118:119], %[b0] offset:1024\n\t" \
    "v_mfma_f32_16x16x32_bf16 v[24:27], v[188:191], v[148:151], v[24:27]\n\t" \
    "ds_read_b64_tr_b16 v[124:125], %[b1] offset:0\n\t" \
    "ds_read_b64_tr_b16 v[126:127], %[b1] offset:1024\n\t" \
    "v_mfma_f32_16x16x32_bf16 v[20:23], v[184:187], v[152:155], v[20:23]\n\t" \
    "ds_read_b64_tr_b16 v[120:121], %[b0] offset:8192\n\t" \
    "ds_read_b64_tr_b16 v[122:123], %[b0] offset:9216\n\t" \
    "s_add_u32 m0, %[d], 0\n\t" \
    "v_mfma_f32_16x16x32_bf16 v[24:27], v[192:195], v[152:155], v[24:27]\n\t" \
    "buffer_load_dwordx4 %[vb0], %[rb], %[y] offen lds\n\t" \
    "v_mfma_f32_16x16x32_bf16 v[28:31], v[180:183], v[156:159], v[28:31]\n\t" \
    "ds_read_b64_tr_b16 v[128:129], %[b1] offset:8192\n\t" \
    "v_mfma_f32_16x16x32_bf16 v[32:35], v[188:191], v[156:159], v[32:35]\n\t" \
    "ds_read_b64_tr_b16 v[130:131], %[b1] offset:9216\n\t" \
    "v_mfma_f32_16x16x32_bf16 v[28:31], v[184:187], v[160:163], v[28:31]\n\t" \
    "ds_read_b128 v[84:87], %[a] offset:0\n\t" \
    "s_add_u32 m0, %[d], 8192\n\t" \
    "v_mfma_f32_16x16x32_bf16 v[32:35], v[192:195], v[160:163], v[32:35]\n\t" \
    "buffer_load_dwordx4 %[vb1], %[rb], %[y] offen lds\n\t" \
    "v_mfma_f32_16x16x32_bf16 v[36:39], v[180:183], v[164:167], v[36:39]\n\t" \
    "ds_read_b128 v[88:91], %[a] offset:1024\n\t" \
    "v_mfma_f32_16x16x32_bf16 v[40:43], v[188:191], v[164:167], v[40:43]\n\t" \
    "ds_read_b128 v[92:95], %[a] offset:2048\n\t" \
    "v_mfma_f32_16x16x32_bf16 v[36:39], v[184:187], v[168:171], v[36:39]\n\t" \
    "ds_read_b128 v[96:99], %[a] offset:3072\n\t" \
    "s_add_u32 m0, %[d], 32768\n\t" \
    "v_mfma_f32_16x16x32_bf16 v[40:43], v[192:195], v[168:171], v[40:43]\n\t" \
    "buffer_load_dwordx4 %[va0], %[ra], %[x] offen sc1 lds\n\t" \
    "v_mfma_f32_16x16x32_bf16 v[44:47], v[180:183], v[172:175], v[44:47]\n\t" \
    "ds_read_b128 v[100:103], %[a] offset:4096\n\t" \
    "v_mfma_f32_16x16x32_bf16 v[48:51], v[188:191], v[172:175], v[48:51]\n\t" \
    "ds_read_b128 v[104:107], %[a] offset:5120\n\t" \
    "v_mfma_f32_16x16x32_bf16 v[44:47], v[184:187], v[176:179], v[44:47]\n\t" \
    "ds_read_b128 v[108:111], %[a] offset:6144\n\t" \
    "s_add_u32 m0, %[d], 40960\n\t" \
    "v_mfma_f32_16x16x32_bf16 v[48:51], v[192:195], v[176:179], v[48:51]\n\t" \
    "buffer_load_dwordx4 %[va1], %[ra], %[x] offen sc1 lds\n\t" \
    "v_mfma_f32_16x16x32_bf16 v[52:55], v[196:199], v[148:151], v[52:55]\n\t" \
    "ds_read_b128 v[112:115], %[a] offset:7168\n\t" \
    "v_mfma_f32_16x16x32_bf16 v[56:59], v[204:207], v[148:151], v[56:59]\n\t" \
    "ds_read_b64_tr_b16 v[132:133], %[b0] offset:16384\n\t" \
    "v_mfma_f32_16x16x32_bf16 v[52:55], v[200:203], v[152:155], v[52:55]\n\t" \
    "ds_read_b64_tr_b16 v[134:135], %[b0] offset:17408\n\t" \
    "s_add_u32 m0, %[d], 16128\n\t" \
    "v_mfma_f32_16x16x32_bf16 v[56:59], v[208:211], v[152:155], v[56:59]\n\t" \
    "buffer_load_dwordx4 %[vb0], %[rb], %[y] offen offset:256 lds\n\t" \
    "v_mfma_f32_16x16x32_bf16 v[60:63], v[196:199], v[156:159], v[60:63]\n\t" \
    "ds_read_b64_tr_b16 v[140:141], %[b1] offset:16384\n\t" \
    "v_mfma_f32_16x16x32_bf16 v[64:67], v[204:207], v[156:159], v[64:67]\n\t" \
    "ds_read_b64_tr_b16 v[142:143], %[b1] offset:17408\n\t" \
    "v_mfma_f32_16x16x32_bf16 v[60:63], v[200:203], v[160:163], v[60:63]\n\t" \
    "ds_read_b64_tr_b16 v[136:137], %[b0] offset:24576\n\t" \
    "s_add_u32 m0, %[d], 24320\n\t" \
    "v_mfma_f32_16x16x32_bf16 v[64:67], v[208:211], v[160:163], v[64:67]\n\t" \
    "buffer_load_dwordx4 %[vb1], %[rb], %[y] offen offset:256 lds\n\t" \
    "v_mfma_f32_16x16x32_bf16 v[68:71], v[196:199], v[164:167], v[68:71]\n\t" \
    "ds_read_b64_tr_b16 v[138:139], %[b0] offset:25600\n\t" \
    "s_add_u32 %[t], %[t], 1\n\t" \
    "s_add_u32 %[d], %[lw], %[c]\n\t" \
    "s_add_u32 %[y], %[c], 49152\n\t" \
    "v_mfma_f32_16x16x32_bf16 v[72:75], v[204:207], v[164:167], v[72:75]\n\t" \
    "ds_read_b64_tr_b16 v[144:145], %[b1] offset:24576\n\t" \
    "s_cmp_eq_u32 %[y], 147456\n\t" \
    "s_cselect_b32 %[y], 0, %[y]\n\t" \
    "s_sub_u32 %[z], %[y], %[c]\n\t" \
    "v_mfma_f32_16x16x32_bf16 v[68:71], v[200:203], v[168:171], v[68:71]\n\t" \
    "ds_read_b64_tr_b16 v[146:147], %[b1] offset:25600\n\t" \
    "s_mov_b32 %[c], %[y]\n\t" \
    "s_add_u32 %[x], %[t], 3\n\t" \
    "s_sub_u32 %[y], %[x], %[nt]\n\t" \
    "v_mfma_f32_16x16x32_bf16 v[72:75], v[208:211], v[168:171], v[72:75]\n\t" \
    "s_cmp_lt_u32 %[x], %[nt]\n\t" \
    "s_cselect_b32 %[x], %[x], %[y]\n\t" \
    "s_cselect_b32 m0, %[cb], %[cn]\n\t" \
    "v_mfma_f32_16x16x32_bf16 v[76:79], v[196:199], v[172:175], v[76:79]\n\t" \
    "s_mul_i32 %[y], %[x], %[kb]\n\t" \
    "s_add_u32 %[y], %[y], m0\n\t" \
    "s_lshl_b32 %[x], %[x], 11\n\t" \
    "v_mfma_f32_16x16x32_bf16 v[80:83], v[204:207], v[172:175], v[80:83]\n\t" \
    "s_add_u32 %[x], %[x], %[ca]\n\t" \
    "s_cmp_lt_u32 %[t], %[nt]\n\t" \
    "v_mfma_f32_16x16x32_bf16 v[76:79], v[200:203], v[176:179], v[76:79]\n\t" \
    "v_mfma_f32_16x16x32_bf16 v[80:83], v[208:211], v[176:179], v[80:83]\n\t" \
    "v_add_u32 %[a], %[z], %[a]\n\t" \
    "v_add_u32 %[b0], %[z], %[b0]\n\t" \
    "v_add_u32 %[b1], %[z], %[b1]\n\t" \
    "s_cbranch_scc1 L_%=\n\t" \
    "s_waitcnt lgkmcnt(0)\n\t" \
    "s_nop 7\n\t" \
    : "=&{v[20:23]}"(acc[0][0][0][0]), "=&{v[24:27]}"(acc[0][0][0][1]), "=&{v[28:31]}"(acc[0][0][1][0]), "=&{v[32:35]}"(acc[0][0][1][1]), "=&{v[36:39]}"(acc[0][0][2][0]), "=&{v[40:43]}"(acc[0][0][2][1]), "=&{v[44:47]}"(acc[0][0][3][0]), "=&{v[48:51]}"(acc[0][0][3][1]), "=&{v[52:55]}"(acc[0][1][0][0]), "=&{v[56:59]}"(acc[0][1][0][1]), "=&{v[60:63]}"(acc[0][1][1][0]), "=&{v[64:67]}"(acc[0][1][1][1]), "=&{v[68:71]}"(acc[0][1][2][0]), "=&{v[72:75]}"(acc[0][1][2][1]), "=&{v[76:79]}"(acc[0][1][3][0]), "=&{v[80:83]}"(acc[0][1][3][1]), [a] "+v"(aoff), [b0] "+v"(boff0), [b1] "+v"(boff1), [c] "+s"(cur), [t] "+s"(t), [d] "=&s"(sd), [x] "=&s"(sx), [y] "=&s"(sy), [z] "=&s"(sz) \
    : [va0] "v"(voffA[0]), [va1] "v"(voffA[1]), [vb0] "v"(voffB[0]), [vb1] "v"(voffB[1]), [ra] "s"(ra), [rb] "s"(rb), [lw] "s"(ldsw), [nt] "s"(nt), [kb] "s"(kB), [ca] "s"(cA), [cb] "s"(cB), [cn] "s"(cN) \
    : "memory", "scc", "v84", "v85", "v86", "v87", "v88", "v89", "v90", "v91", "v92", "v93", "v94", "v95", "v96", "v97", "v98", "v99", "v100", "v101", "v102", "v103", "v104", "v105", "v106", "v107", "v108", "v109", "v110", "v111", "v112", "v113", "v114", "v115", "v116", "v117", "v118", "v119", "v120", "v121", "v122", "v123", "v124", "v125", "v126", "v127", "v128", "v129", "v130", "v131", "v132", "v133", "v134", "v135", "v136", "v137", "v138", "v139", "v140", "v141", "v142", "v143", "v144", "v145", "v146", "v147", "v148", "v149", "v150", "v151", "v152", "v153", "v154", "v155", "v156", "v157", "v158", "v159", "v160", "v161", "v162", "v163", "v164", "v165", "v166", "v167", "v168", "v169", "v170", "v171", "v172", "v173", "v174", "v175", "v176", "v177", "v178", "v179", "v180", "v181", "v182", "v183", "v184", "v185", "v186", "v187", "v188", "v189", "v190", "v191", "v192", "v193", "v194", "v195", "v196", "v197", "v198", "v199", "v200", "v201", "v202", "v203", "v204", "v205", "v206", "v207", "v208", "v209", "v210", "v211")
#define AQ_PIN0() asm volatile("" : "+{v[28:35]}"(wr[0][0]), "+{v[36:43]}"(wr[0][1]))
#define AQ_PIN1() asm volatile("" : "+{v[44:51]}"(wr[1][0]), "+{v[52:59]}"(wr[1][1]))
#define AQ_PIN2() asm volatile("" : "+{v[60:67]}"(wr[2][0]), "+{v[68:75]}"(wr[2][1]))
#define AQ_PIN3() asm volatile("" : "+{v[76:83]}"(wr[3][0]), "+{v[84:91]}"(wr[3][1]))
#define AQ_PIN4() asm volatile("" : "+{v[92:99]}"(wr[4][0]), "+{v[100:107]}"(wr[4][1]))
#define AQ_PIN5() asm volatile("" : "+{v[108:115]}"(wr[5][0]), "+{v[116:123]}"(wr[5][1]))
#define AQ_PIN6() asm volatile("" : "+{v[124:131]}"(wr[6][0]), "+{v[132:139]}"(wr[6][1]))
#define AQ_PIN7() asm volatile("" : "+{v[140:147]}"(wr[7][0]), "+{v[148:155]}"(wr[7][1]))
#define AQ_PIN8() asm volatile("" : "+{v[156:163]}"(wr[8][0]), "+{v[164:171]}"(wr[8][1]))
#define AQ_PIN9() asm volatile("" : "+{v[172:179]}"(wr[9][0]), "+{v[180:187]}"(wr[9][1]))
#define AQ_PIN10() asm volatile("" : "+{v[188:195]}"(wr[10][0]), "+{v[196:203]}"(wr[10][1]))
#define AQ_PIN11() asm volatile("" : "+{v[204:211]}"(wr[11][0]), "+{v[212:219]}"(wr[11][1]))
#define AK_PIN0() asm volatile("" : "+{v[20:27]}"(wr[0][0]), "+{v[28:35]}"(wr[0][1]))
#define AK_PIN1() asm volatile("" : "+{v[36:43]}"(wr[1][0]), "+{v[44:51]}"(wr[1][1]))
#define AK_PIN2() asm volatile("" : "+{v[52:59]}"(wr[2][0]), "+{v[60:67]}"(wr[2][1]))
#define AK_PIN3() asm volatile("" : "+{v[68:75]}"(wr[3][0]), "+{v[76:83]}"(wr[3][1]))
#define AK_PIN4() asm volatile("" : "+{v[84:91]}"(wr[4][0]), "+{v[92:99]}"(wr[4][1]))
#define AK_PIN5() asm volatile("" : "+{v[100:107]}"(wr[5][0]), "+{v[108:115]}"(wr[5][1]))
#define AK_PIN6() asm volatile("" : "+{v[116:123]}"(wr[6][0]), "+{v[124:131]}"(wr[6][1]))
#define AK_PIN7() asm volatile("" : "+{v[132:139]}"(wr[7][0]), "+{v[140:147]}"(wr[7][1]))
#define AK_PIN8() asm volatile("" : "+{v[148:155]}"(wr[8][0]), "+{v[156:163]}"(wr[8][1]))
#define AK_PIN9() asm volatile("" : "+{v[164:171]}"(wr[9][0]), "+{v[172:179]}"(wr[9][1]))
#define AK_PIN10() asm volatile("" : "+{v[180:187]}"(wr[10][0]), "+{v[188:195]}"(wr[10][1]))
#define AK_PIN11() asm volatile("" : "+{v[196:203]}"(wr[11][0]), "+{v[204:211]}"(wr[11][1]))
#define V_PIN() asm volatile("" : "+{v[28:35]}"(wv[0]), "+{v[36:43]}"(wv[1]), "+{v[44:51]}"(wv[2]), "+{v[52:59]}"(wv[3]), "+{v[60:67]}"(wv[4]), "+{v[68:75]}"(wv[5]), "+{v[76:83]}"(wv[6]), "+{v[84:91]}"(wv[7]), "+{v[92:99]}"(wv[8]), "+{v[100:107]}"(wv[9]), "+{v[108:115]}"(wv[10]), "+{v[116:123]}"(wv[11]), "+{v[124:131]}"(wv[12]), "+{v[132:139]}"(wv[13]), "+{v[140:147]}"(wv[14]), "+{v[148:155]}"(wv[15]))
#define WOV_PIN0() asm volatile("" : "+{v[156:163]}"(wo[0][0][0]), "+{v[164:171]}"(wo[0][0][1]), "+{v[172:179]}"(wo[0][1][0]), "+{v[180:187]}"(wo[0][1][1]))
#define WOV_PIN1() asm volatile("" : "+{v[188:195]}"(wo[1][0][0]), "+{v[196:203]}"(wo[1][0][1]), "+{v[204:211]}"(wo[1][1][0]), "+{v[212:219]}"(wo[1][1][1]))
#define WOW_PIN0() asm volatile("" : "+{v[28:35]}"(wo[0][0][0]), "+{v[36:43]}"(wo[0][0][1]), "+{v[44:51]}"(wo[0][1][0]), "+{v[52:59]}"(wo[0][1][1]))
#define WOW_PIN1() asm volatile("" : "+{v[60:67]}"(wo[1][0][0]), "+{v[68:75]}"(wo[1][0][1]), "+{v[76:83]}"(wo[1][1][0]), "+{v[84:91]}"(wo[1][1][1]))
#define WOW_PIN2() asm volatile("" : "+{v[92:99]}"(wo[2][0][0]), "+{v[100:107]}"(wo[2][0][1]), "+{v[108:115]}"(wo[2][1][0]), "+{v[116:123]}"(wo[2][1][1]))
#define WOW_PIN3() asm volatile("" : "+{v[124:131]}"(wo[3][0][0]), "+{v[132:139]}"(wo[3][0][1]), "+{v[140:147]}"(wo[3][1][0]), "+{v[148:155]}"(wo[3][1][1]))
#define WOW_PIN4() asm volatile("" : "+{v[156:163]}"(wo[4][0][0]), "+{v[164:171]}"(wo[4][0][1]), "+{v[172:179]}"(wo[4][1][0]), "+{v[180:187]}"(wo[4][1][1]))
#define WOW_PIN5() asm volatile("" : "+{v[188:195]}"(wo[5][0][0]), "+{v[196:203]}"(wo[5][0][1]), "+{v[204:211]}"(wo[5][1][0]), "+{v[212:219]}"(wo[5][1][1]))
#define A_PIN(QW, i) do { switch (i) { case 0: if (QW) AQ_PIN0(); else AK_PIN0(); break; case 1: if (QW) AQ_PIN1(); else AK_PIN1(); break; case 2: if (QW) AQ_PIN2(); else AK_PIN2(); break; case 3: if (QW) AQ_PIN3(); else AK_PIN3(); break; case 4: if (QW) AQ_PIN4(); else AK_PIN4(); break; case 5: if (QW) AQ_PIN5(); else AK_PIN5(); break; case 6: if (QW) AQ_PIN6(); else AK_PIN6(); break; case 7: if (QW) AQ_PIN7(); else AK_PIN7(); break; case 8: if (QW) AQ_PIN8(); else AK_PIN8(); break; case 9: if (QW) AQ_PIN9(); else AK_PIN9(); break; case 10: if (QW) AQ_PIN10(); else AK_PIN10(); break; case 11: if (QW) AQ_PIN11(); else AK_PIN11(); break; } } while (0)
#define WO_PIN(VW, i) do { switch (i) { case 0: if (VW) WOV_PIN0(); else WOW_PIN0(); break; case 1: if (VW) WOV_PIN1(); else WOW_PIN1(); break; case 2: if (!VW) WOW_PIN2(); break; case 3: if (!VW) WOW_PIN3(); break; case 4: if (!VW) WOW_PIN4(); break; case 5: if (!VW) WOW_PIN5(); break; } } while (0)

__device__ __forceinline__ int lds_byte(int r, int c) {
  const int st = (r >> 4) * 2 + (c >> 5), rr = r & 15, cc = c & 31, ob = rr * 64 + cc * 2;
  return st * 1024 + (ob ^ (((ob >> 9) & 1) << 5));
}
#define G_DvoffB dmaw
#define G_ST1(bufoff, r, so, voff, i) G_D##voff(r, (voff)[i], so, ldsw + (bufoff) + (i) * 8192)
#define G_DvoffA dmas
#define G_STAGE(bufoff, so, voff) do { G_ST1(bufoff, G_R##voff, so, voff, 0); G_ST1(bufoff, G_R##voff, so, voff, 1); } while (0)
#define G_RvoffA ra
#define G_RvoffB rb
#define G_WAIT_V(n) asm volatile("s_waitcnt vmcnt(%0)" :: "n"(n) : "memory")
__device__ __forceinline__ void gemm_h(LAS unsigned char* lds, rsrc_t ra, rsrc_t rb, unsigned cA, unsigned cB, int lda, int ldb, int nt,
                                       bool wide, bool slow, unsigned cN, bool cont, unsigned cur, f32x4 (&acc)[1][2][4][2]) {
  const int wid = __builtin_amdgcn_readfirstlane(threadIdx.x >> 6), lane = opq(0), tid = wid * 64 + lane;
  const int wr = wid >> 2, wc = wid & 3, fr = lane & 15, fq = lane >> 4;
  unsigned voffA[2], voffB[2];
  #pragma unroll
  for (int i = 0; i < 2; ++i) {
    voffA[i] = (unsigned)(((wid >> 1) + 4 * i) * 16 * lda + (wid & 1) * 1024 + lane * 16);
    const int b = tid * 16 + i * 8192, r = b >> 8, pos = (b >> 4) & 15;
    const int c = pos ^ (((r & 3) << 1) | (((r >> 3) & 1) << 3));
    voffB[i] = (unsigned)(r * ldb + c * 16);
  }
  const unsigned kB = 64 * ldb;
  const unsigned ldsw = (unsigned)(size_t)lds + (unsigned)wid * 1024u;
  const int q = (lane & 15) >> 2, p = lane & 3;
  unsigned aoff = (unsigned)(size_t)lds + cur + 32768 + lds_byte(wr * 64 + fr, fq * 8);
  unsigned boff0 = (unsigned)(size_t)lds + cur + (8 * fq + q) * 256 + (((wc * 4 + (p >> 1)) ^ ((q << 1) | ((fq & 1) << 3))) << 4) + 8 * (p & 1), boff1 = boff0 ^ 32;
#define H_STAGE(so, t) do { G_STAGE(so, cB + (t) * kB, voffB); G_STAGE((so) + 32768, cA + (t) * 2048, voffA); if (wide) G_STAGE((so) + 16384, cB + (t) * kB + 256, voffB); } while (0)
  if (!cont) { H_STAGE(0, 0); H_STAGE(49152, 1); H_STAGE(98304, 2); }
  unsigned t = 0, sd, sx, sy, sz;
  if (slow) H_LOOP2S(); else if (wide) H_LOOP2(); else H_LOOP1();
}

__device__ __forceinline__ u32x4 epi_pack(const f32x4 a0, const f32x4 a1) {
  const unsigned x0 = pack2(a0[0], a0[1]), x1 = pack2(a0[2], a0[3]);
  const unsigned y0 = pack2(a1[0], a1[1]), y1 = pack2(a1[2], a1[3]);
  auto s0 = __builtin_amdgcn_permlane16_swap(x0, y0, false, false);
  auto s1 = __builtin_amdgcn_permlane16_swap(x1, y1, false, false);
  return (u32x4){s0[0], s1[0], s0[1], s1[1]};
}
__device__ __forceinline__ unsigned gelu2(unsigned w) {
  const f32x2 x = {lo16(w), hi16(w)};
  const float c0 = -2.f * 1.4426950408889634f * 0.7978845608028654f, c1 = c0 * 0.044715f;
  const f32x2 a = x * (x * x * c1 + c0);
  const f32x2 d = (f32x2){__builtin_amdgcn_exp2f(a[0]), __builtin_amdgcn_exp2f(a[1])} + 1.f;
  const f32x2 y = x * (f32x2){__builtin_amdgcn_rcpf(d[0]), __builtin_amdgcn_rcpf(d[1])};
  return pack2(y[0], y[1]);
}
__device__ __forceinline__ float silu_f(float b) { return b * __builtin_amdgcn_rcpf(1.f + __expf(-b)); }

__device__ __forceinline__ unsigned blk_in(int r, int c) { const int ob = r * 64 + c * 2; return ob ^ (((ob >> 9) & 1) << 5); }
__device__ __forceinline__ unsigned xn_blk(int rb, int cb) { return (unsigned)(((rb >> 1) * 32 + cb) * 2 + (rb & 1)) * 1024u; }

template <int EPI>
__device__ __forceinline__ void epi_rows(const f32x4 (&acc)[1][2][4][2], rsrc_t rs, unsigned obase, int ldc, int pm2, int pn2, int sh, bool wide, int wr, int wc, int ln) {
  const int fq = ln >> 4, cb = 16 * (fq & 1) + 8 * (fq >> 1), fr = (ln & 15) + sh;
  const unsigned lo = (unsigned)((fr >> 4) * 16 * ldc) + blk_in(fr & 15, cb);
  const unsigned ob = (unsigned)(pm2 * 128 + 64 * wr) * (unsigned)ldc + (unsigned)(pn2 * 4 + wc) * 1024u + lo;
  u32x4 r[8];
  if (EPI >= 2) {
    #pragma unroll
    for (int i = 0; i < 8; ++i)
      r[i] = ld16(rs, EPI == 2 ? WS_XN + xn_blk(pm2 * 8 + 4 * wr + (i & 3), pn2 * 4 + 4 * (i >> 2) + wc) + lo : WS_X1 + ob + (i & 3) * 32768 + (i >> 2) * 4096);
  }
  #pragma unroll
  for (int i = 0; i < 8; ++i) {
    if (i == 4 && !wide) break;
    u32x4 w = epi_pack(acc[0][i >> 2][i & 3][0], acc[0][i >> 2][i & 3][1]);
    #pragma unroll
    for (int k = 0; k < 4; ++k) {
      w[k] = EPI < 2 ? gelu2(w[k]) : pack2(lo16(r[i][k]) + lo16(w[k]), hi16(r[i][k]) + hi16(w[k]));
      if (EPI == 3) w[k] = pack2(silu_f(lo16(w[k])), silu_f(hi16(w[k])));
    }
    st16(rs, obase + ob + (unsigned)(16 * (i & 3) * ldc) + (i >> 2) * 4096, w);
  }
}

__device__ __forceinline__ float row_sum16(float v) {
  v += __uint_as_float(__builtin_amdgcn_update_dpp(0, (int)__float_as_uint(v), 0x128, 0xf, 0xf, false));
  v += __uint_as_float(__builtin_amdgcn_update_dpp(0, (int)__float_as_uint(v), 0x124, 0xf, 0xf, false));
  v += __uint_as_float(__builtin_amdgcn_update_dpp(0, (int)__float_as_uint(v), 0x122, 0xf, 0xf, false));
  v += __uint_as_float(__builtin_amdgcn_update_dpp(0, (int)__float_as_uint(v), 0x121, 0xf, 0xf, false));
  return v;
}
__device__ __forceinline__ void ln_rows(u32x4 (&a)[8]) {
  f32x2 v[32];
  #pragma unroll
  for (int i = 0; i < 32; ++i) v[i] = (f32x2){lo16(a[i >> 2][i & 3]), hi16(a[i >> 2][i & 3])};
  f32x2 s[4] = {v[0], v[1], v[2], v[3]};
  #pragma unroll
  for (int i = 4; i < 32; ++i) s[i & 3] += v[i];
  s[0] = (s[0] + s[1]) + (s[2] + s[3]);
  const float mean = row_sum16(s[0][0] + s[0][1]) * (1.f / DM);
  #pragma unroll
  for (int i = 0; i < 32; ++i) { const f32x2 d = v[i] - mean; s[i & 3] = i < 4 ? d * d : s[i & 3] + d * d; }
  s[0] = (s[0] + s[1]) + (s[2] + s[3]);
  const float var = row_sum16(s[0][0] + s[0][1]) * (1.f / DM);
  const float mb = rb1(mean), vb = rb1(var);
  const float rden = __builtin_amdgcn_rcpf(rb1(__builtin_amdgcn_sqrtf(rb1(vb + 1e-5f))));
  #pragma unroll
  for (int i = 0; i < 32; ++i) {
    const f32x2 d = v[i] - mb;
    const unsigned w = pack2(d[0], d[1]);
    const f32x2 y = (f32x2){lo16(w), hi16(w)} * rden;
    a[i >> 2][i & 3] = pack2(y[0], y[1]);
  }
}

template <int N>
__device__ __forceinline__ void fin_sum(rsrc_t rs, unsigned po, u32x4* v) {
  u32x4 pv[N][4];
  #pragma unroll
  for (int i = 0; i < 4 * N; ++i) pv[i >> 2][i & 3] = ld16(rs, po + (i >> 2) * 8192 + (i & 3) * 65536);
  #pragma unroll
  for (int c = 0; c < N; ++c) {
    f32x4 a0, a1;
    #pragma unroll
    for (int k = 0; k < 4; ++k) {
      a0[k] = (lo16(pv[c][0][k]) + lo16(pv[c][1][k])) + (lo16(pv[c][2][k]) + lo16(pv[c][3][k]));
      a1[k] = (hi16(pv[c][0][k]) + hi16(pv[c][1][k])) + (hi16(pv[c][2][k]) + hi16(pv[c][3][k]));
    }
    v[c] = epi_pack(a0, a1);
  }
}
__device__ __forceinline__ void fin_out(const Ptrs& P, unsigned eo, u32x4 v, u32x4 gv, u32x4 xv) {
  #pragma unroll
  for (int k = 0; k < 4; ++k) {
    v[k] = pack2(lo16(v[k]) * lo16(gv[k]), hi16(v[k]) * hi16(gv[k]));
    v[k] = pack2(lo16(xv[k]) + lo16(v[k]), hi16(xv[k]) + hi16(v[k]));
  }
  *(u32x4*)((char*)P.out + eo) = v;
}

#define STG 56
#define SLOWC 6
__device__ __forceinline__ void mlp_role(LAS unsigned char* lds, const Ptrs& P, int hf, int slot, int wid) {
  const int wr = wid >> 2, wc = wid & 3;
  const int p2 = hf * 8 + (slot >> 2), pn = slot & 3;
  char* ws = P.ws;
  const rsrc_t rs = WS_RSRC(P);
  const u16* const p1 = P.w1; const u16* const p2w = P.w2; const u16* const p3 = P.w3; const u16* const p4 = P.w4;
#define M_DONE(c) do { VMWAIT(); BARRIER(); if (wid == 0) cnt_add(ws, c, opq(0)); } while (0)
  BARRIER();
  #pragma unroll
  for (int i = 0; i < 4; ++i) touch(i < 2 ? p3 : p4, DM * FF * 2, (((i & 1) * 64 + hf * 32 + slot) * 512 + wid * 64 + opq(0)) * 128, lds);
  if (hf == 0) touch(p2w, DM * DM * 2, (slot * 512 + wid * 64 + opq(0)) * 128, lds);
  if (STG && hf == 0) { VMWAIT(); for (int i = 0; i < 7 - (slot >> 2); ++i) __builtin_amdgcn_s_sleep(STG); }
  #pragma unroll 1
  for (int code = 0; code < 7; ++code) {
    int ln = opq(0);
    if (wid == 0) {
      if (code == 0) flag_wait(P, F_LN1(p2 * 4 + (ln & 3)), 0, ln);
      else if (code < 3 || code == 6) {
        const unsigned co = CNT_OFF + (code == 1 ? C_W1(min(max(p2 - 1 + min(ln, 2), 0), 15)) : code == 2 ? C_LN(p2) : C_W3(p2)) * 128;
        for (unsigned spins = 0; __any(ld4(rs, co) < 4u);) {
          __builtin_amdgcn_s_sleep(2);
          if (spin_fail(spins, P, ln)) break;
        }
      }
    }
    if (code < 3 || code == 6) BARRIER();
    TLW(3 * code);
    const bool w3 = code >= 2 && code < 6, wide = code != 5;
    const int lda = code == 6 ? 7168 : 2048, ldb = w3 ? 7168 : 2048, c2 = w3 ? (code - 2) * 8 + (wide ? 2 * pn : pn) : 2 * pn;
    const rsrc_t rb = __builtin_amdgcn_make_buffer_rsrc((void*)(code == 0 ? p1 : code == 1 ? p2w : w3 ? p3 : p4), 0, code < 2 ? DM * DM * 2 : DM * FF * 2, RSRC_W3);
    const unsigned cA = (code == 0 ? WS_XN2 : code == 1 ? WS_H1 : w3 ? WS_X1N : WS_H3) + p2 * 128 * lda, cB = c2 * 256;
    f32x4 acq[1][2][4][2];
    gemm_h(lds, rs, rb, cA, cB, lda, ldb, code == 6 ? 56 : 16, wide, hf == 0 && wide && code >= SLOWC, code == 4 ? (24 + pn) * 256 : w3 && wide ? cB + 2048 : cB, w3 && code > 2, code == 3 || code == 4 ? (code - 2) * 49152 : 0, acq);
    TLW(3 * code + 1);
    ln = opq(0);
    if (code == 1) {
      epi_rows<2>(acq, rs, WS_X1, 2048, p2, c2, 0, true, wr, wc, ln);
      M_DONE(C_W2(p2));
      if (wid == 0) cnt_wait(P, C_W2(p2), 4, ln);
      BARRIER();
      const unsigned ro = (unsigned)(p2 * 8 + pn * 2 + wr) * 32768u + ((ln >> 2) & 3) * 1024 + blk_in(wc * 4 + (ln >> 4), (ln & 3) * 8);
      u32x4 v[8];
      #pragma unroll
      for (int i = 0; i < 8; ++i) v[i] = ld16(rs, WS_X1 + ro + i * 4096);
      ln_rows(v);
      #pragma unroll
      for (int i = 0; i < 8; ++i) st16(rs, WS_X1N + ro + i * 4096, v[i]);
      M_DONE(C_LN(p2));
    } else if (code == 6) {
      epi_rows<3>(acq, rs, WS_G, 2048, p2, c2, 0, true, wr, wc, ln);
      M_DONE(C_W4(p2));
    } else {
      VMWAIT();
      epi_rows<1>(acq, rs, code ? WS_H3 : WS_H1, ldb, p2, c2, code ? 0 : (pn & 1) ? -1 - (pn >> 1) : 1 + (pn >> 1), wide, wr, wc, ln);
      if (code == 0 && wid == 0) setup_wait(P, ln);
      if (code == 0 || code == 5) M_DONE(code ? C_W3(p2) : C_W1(p2));
    }
    TLW(3 * code + 2);
  }
}

#define FPC 40
#define FINE 52
__device__ __forceinline__ void fin_role(LAS unsigned char* lds, const Ptrs& P, int wid) {
  const int wr = wid >> 2, wc = wid & 3;
  const rsrc_t rs = WS_RSRC(P);
  volatile LAS int* tk = (volatile LAS int*)(lds + 147456);
  unsigned tks = 0;
#define POP() do { if (wid == 0 && opq(0) == 0) tks = __hip_atomic_fetch_add((gu32*)(P.ws + CNT_OFF + C_HEAD * 128), 1u, RLX_AGENT); } while (0)
  POP();
  for (int round = 0;; ++round) {
    if (wid == 0 && opq(0) == 0) tk[round & 1] = tks;
    LBARRIER();
    const int u = __builtin_amdgcn_readfirstlane(tk[round & 1]);
    if (u >= 2 * FINE + 8 * (XS3 - FINE)) break;
    const bool big = u < 2 * FINE;
    const int sg = big ? u >> 1 : FINE + ((u - 2 * FINE) >> 3), cc0 = big ? (u & 1) * 4 : (u - 2 * FINE) & 7;
    const int ln = opq(0), fq = ln >> 4, cb = 16 * (fq & 1) + 8 * (fq >> 1);
    const unsigned eo = (unsigned)(sg * 32 + wr * 16 + (ln & 15)) * 2048u + (unsigned)(cc0 * 128 + wc * 32 + cb) * 2u;
    const unsigned go = WS_G + (unsigned)(sg * 2 + wr) * 32768u + (cc0 * 4 + wc) * 1024 + blk_in(ln & 15, cb);
    const unsigned po = WS_PT + (unsigned)sg * 262144u + cc0 * 8192 + wr * 4096 + wc * 1024 + ln * 16;
    const int xg = sg < XS0 || sg >= XS2 ? 2 : sg >= XS1, want = sg + 1 - (sg < XS0 ? 0 : xg == 2 ? XS2 - XS0 : xg ? XS1 : XS0);
    u32x4 v[4], gv[4], xv[4];
    if (big) {
      if (wid == 2) flag_wait(P, F_PRG(5 + xg, ln & 31), want, ln);
      BARRIER();
      fin_sum<2>(rs, po, v);
      asm volatile("" : "+v"(v[0]), "+v"(v[1]) :: "memory");
      DELAY(FPC);
      #pragma unroll
      for (int c = 0; c < 4; ++c) xv[c] = __builtin_nontemporal_load((const u32x4*)((const char*)P.x + eo + c * 256));
      fin_sum<2>(rs, po + 16384, v + 2);
      if (wid == 1) cnt_wait(P, C_W4(sg >> 2), 4, ln);
      BARRIER();
      #pragma unroll
      for (int c = 0; c < 4; ++c) gv[c] = ld16(rs, go + c * 4096);
      #pragma unroll
      for (int c = 0; c < 4; ++c) fin_out(P, eo + c * 256, v[c], gv[c], xv[c]);
    } else {
      if (wid == 1) cnt_wait(P, C_W4(sg >> 2), 4, ln);
      BARRIER();
      gv[0] = ld16(rs, go);
      xv[0] = __builtin_nontemporal_load((const u32x4*)((const char*)P.x + eo));
      if (wid == 2) flag_wait<3>(P, F_PRG(5 + xg, (ln & 3) * 8 + cc0), want, ln);
      BARRIER();
      fin_sum<1>(rs, po, v);
      fin_out(P, eo, v[0], gv[0], xv[0]);
    }
    POP();
  }
}

#define DMA_A dman
#define DMA_V dman
#define DMA_O dmaw
#define CADM 1

#define RTC(id)
#define DCA 3
#define DCB 12
#define DCU 8
#define DL1 -6
#define DL2 7
#define DLB 9
#define SN0 65
#define MAP 0
#define HELP 256
__device__ __forceinline__ void track(int& est, int d, int gain) { const int e = d - (est >> 4); est += e > 44 ? DCB * 2 : e * (e > 0 ? max(gain, DCU) : gain); }
__device__ __forceinline__ unsigned rfl(unsigned v) { unsigned r; asm volatile("s_nop 0\n\tv_readfirstlane_b32 %0, %1\n\ts_nop 4" : "=s"(r) : "v"(v)); return r; }
__device__ __forceinline__ void plan_wait(rsrc_t rs, unsigned fo, int s, int& lim, unsigned& tm, unsigned& d) {
  while ((d & 255u) != (unsigned)s && (lim -= DEC1) > 0) {
    __builtin_amdgcn_s_sleep(1);
    const u32x2 v = __builtin_amdgcn_raw_buffer_load_b64(rs, 0, fo, AUX_SC1);
    tm = rfl(v.x); d = rfl(v.y);
  }
  const unsigned now = (unsigned)__builtin_amdgcn_s_memrealtime() * 4u;
  tm = now + min((int)(tm - now), 2000);
  d = min(d >> 8, 1000u);
}
#define SPIN_BUDGET (1 << 21)

__device__ __forceinline__ unsigned rec_go(LAS unsigned char* w, const Ptrs& P, u32x2 f, int wid, int lane) {
  if (wid >= 6) {
    if (__any(f.y != P.tag)) f.x = flag_wait(P, wid == 6 ? F_LN0(lane) : F_CAL(lane), 0, lane);
    if (wid == 7) { const unsigned m = near_half(f.x); if (lane == 0) *(volatile LAS unsigned*)w = m; }
  }
  LBARRIER();
  return __builtin_amdgcn_readfirstlane(*(volatile LAS unsigned*)w);
}

#define SLOT(i) ((i) * 16384)
#define LA_MEM SLOT(8)
#define LA_Q SLOT(9)
#define LA_K (SLOT(9) + 2048)
#define LA_P (SLOT(9) + 4096)
#define LA_KS (SLOT(9) + 6144)
#define LA_VM LA_MEM
#define LA_KX (SLOT(9) + 8192)

template <bool QW>
__device__ __forceinline__ void a_wave(LAS unsigned char* lds, Ptrs& P, int h, int wid) {
  char* ws = P.ws;
  const int lane = opq(0), tid = wid * 64 + lane;
  const int j = wid & 3, g = lane >> 4, q4 = (lane & 15) >> 2, p4 = lane & 3;
  const rsrc_t rs = WS_RSRC(P);
  const rsrc_t rq = __builtin_amdgcn_make_buffer_rsrc((void*)P.Wq, 0, HD * DM * 2, RSRC_W3);
  const rsrc_t rk = __builtin_amdgcn_make_buffer_rsrc((void*)P.Wk, 0, HD * DM * 2, RSRC_W3);
  const unsigned lbase = (unsigned)(size_t)lds;
  int lim = SPIN_BUDGET;
  u32x8 wr[12][2];
  const int trow = 16 * (g >> 1) + 4 * (g & 1) + 8 * (q4 >> 1) + (q4 & 1);
  const int tsw = ((q4 & 1) | ((g & 1) << 1) | ((q4 >> 1) << 2)) << 1;
  const int toff = trow * 256 + (((4 * j + (p4 >> 1)) ^ tsw) << 4) + 8 * (p4 & 1);
  {
    unsigned dv[2];
    #pragma unroll
    for (int x = 0; x < 2; ++x) {
      const int row = 8 * wid + 4 * x + (lane >> 4);
      const int sw = ((row & 1) | (((row >> 2) & 1) << 1) | (((row >> 3) & 1) << 2)) << 1;
      dv[x] = (unsigned)(row * (HD * 2) + h * 256 + (((lane & 15) ^ sw) << 4));
    }
#define A_GRP(n) do { _Pragma("unroll") for (int i = 0; i < 8; ++i) \
      DMA_A(((n) & 1) ? rk : rq, dv[i & 1], (4 * (((n) & 1) ? ((n) >> 1) ^ ((n) >> 2) : (((n) >> 1) + 1) & 3) + (i >> 1)) * 64 * HD * 2, lbase + SLOT(4 * ((n) & 1) + (i >> 1)) + wid * 2048 + (i & 1) * 1024); } while (0)
    A_GRP(0); A_GRP(1);
    #pragma unroll
    for (int n = 0; n < 6; ++n) {
      VMW(8);
      BARRIER();
      if ((n & 1) == (QW ? 0 : 1)) {
        #pragma unroll
        for (int b = 0; b < 4; ++b) {
          LAS unsigned char* t = lds + SLOT(QW ? 0 : 4) + ((b ^ j) << 14);
          wr[4 * (n >> 1) + b][0] = rd_frag<8192, 512>(t + toff);
          wr[4 * (n >> 1) + b][1] = rd_frag<8192, 512>(t + (toff ^ 32));
        }
        #pragma unroll
        for (int b = 0; b < 4; ++b) A_PIN(QW, 4 * (n >> 1) + b);
      }
      LBARRIER();
      A_GRP(n + 2);
    }
  }
  const unsigned cb = comm_base(ws);
  u32x2 f0 = {0u, 0u};
  #pragma unroll
  for (int rd = 0; rd < 2; ++rd) {
    if (rd == 0) VMW(8);
    else {
      if (wid >= 6) f0 = ld8(rs, FLG_OFF + (wid == 6 ? F_LN0(lane) : F_CAL(lane)) * 128);
      const unsigned fv = wid == 3 ? FLG_OFF + F_PRG(min((lane >> 1) & 3, 2), h) * 128 + (lane & 1) * 4 : WS_QF + j * (16u << 20) + h * 8192 + lane * 128;
      if (QW) asm volatile("s_mov_b32 m0, %2\n\ts_nop 0\n\tbuffer_load_dword %0, %1, 0 offen sc1 lds" :: "v"(fv), "s"(rs), "s"(lbase + LA_P + (wid == 3 ? 640 : 1024)) : "memory");
      get_tag(P);
      VMWAIT();
    }
    BARRIER();
    u32x8 f[2][2];
    #pragma unroll
    for (int i = 0; i < 2; ++i) {
      f[i][0] = rd_frag<8192, 512>(lds + SLOT((QW ? 0 : 2) + 4 * rd + i) + toff);
      f[i][1] = rd_frag<8192, 512>(lds + SLOT((QW ? 0 : 2) + 4 * rd + i) + (toff ^ 32));
    }
    asm volatile("" : "+v"(f[0][0]), "+v"(f[0][1]), "+v"(f[1][0]), "+v"(f[1][1]));
    LBARRIER();
    #pragma unroll
    for (int i = 0; i < 2; ++i)
      #pragma unroll
      for (int t = 0; t < 2; ++t) {
        LAS unsigned char* p = lds + SLOT((QW ? 0 : 2) + 4 * rd + i) + j * 4096 + t * 2048 + lane * 16;
        *(LAS u32x4*)p = (u32x4){f[i][t][0], f[i][t][1], f[i][t][2], f[i][t][3]};
        *(LAS u32x4*)(p + 1024) = (u32x4){f[i][t][4], f[i][t][5], f[i][t][6], f[i][t][7]};
      }
  }
  const unsigned mask = rec_go(lds + LA_P + 128, P, f0, wid, lane);
  const int idx = (lane & 1) ? 0xEEEE : 0x4444;
  const unsigned ab_a = lbase + LA_MEM + lane * 16, am_a = ab_a + j * 1024;
  const unsigned t0_a = lbase + SLOT((QW ? 0 : 4) + j) + j * 4096 + lane * 16;
  const unsigned iw_a = lbase + (QW ? LA_Q : LA_K) + j * 512 + q4 * 128 + g * 32 + p4 * 4;
  const unsigned sw_a = lbase + LA_KS + (j >> 1) * 1024 + ((j & 1) * 2 + (q4 >> 1)) * 256 + (4 * g + (p4 & 1)) * 16 + ((q4 & 1) * 2 + (p4 >> 1)) * 4;
  const unsigned qr_a = lbase + LA_Q + g * 128 + (lane & 7) * 16;
  const unsigned kr_a = lbase + LA_K + g * 128 + (2 * q4 + (lane & 1)) * 16;
  const unsigned fr_a = lbase + LA_P + p4 * 4;
  if (tid < 12) *(LAS unsigned*)(lds + LA_P + tid * 4) = 0u;
  const unsigned vo_v = VB_AT(h) + j * 512 + lane * 4, md = lbase + LA_VM - VB_AT(h);
  const unsigned oo_v = OB_AT((lane >> 2) & 1, h) + (j * 16 + (g >> 1) * 8 + p4 * 2 + (g & 1)) * 16;
  const unsigned long long onej = 1ull << j;
  const unsigned lkx = lbase + LA_KX + j * 2048;
  u32x8 oxr = {};
  const unsigned xo_v = (lane >> 5) * 2048 + (lane & 31) * 16, vl_a = lbase + LA_VM + (j & 1) * 1024 + lane * 4;
  const int dq = 16 * (j >> 1) + (lane & 15) - 4 * g;
  const unsigned fb = step_base(cb, mask, 0);
  unsigned tgt = (unsigned)__builtin_amdgcn_s_memrealtime() * 4u;
  unsigned long long tp = 0;
  unsigned pt = 0, pd = 0;
  const unsigned long long fmask = __builtin_amdgcn_readfirstlane((16u >> wid) & 1u);
  int known = 0;
  __builtin_amdgcn_s_setprio(QW ? 2 : 1);
  LBARRIER();
  report(P, wid);
  TL(72);
  for (int s = 0; s < NSEG; ++s) {
    const unsigned sb = step_base(cb, mask, s);
    const unsigned sbm = sb - (lbase + LA_MEM);
        const unsigned fo = (unsigned)(s * NH + h) * 8192u + j * 2048 - (lbase + LA_MEM);
    GSDECL;
    RTC(h);
    lim = __builtin_amdgcn_readfirstlane(lim);
    if (QW) {
      if (wid == 3 && s > 0) {
        if (opq(0) == 0) __builtin_amdgcn_raw_buffer_store_b32((unsigned)tp * 4u - tgt, rs, 0, fb + FL_O(h), AUX_SC1);
        plan_wait(rs, fb + FL_T(s), s, lim, pt, pd);
        tgt = pt;
      }
      if (s >= known) {
        const u32x2 v = *(volatile LAS u32x2*)(lds + LA_P + 640 + (opq(0) & 3) * 8);
        if (!__any(v.y != P.tag)) known = rfl(min(min(__builtin_amdgcn_readlane(v.x, 0), __builtin_amdgcn_readlane(v.x, 1)), __builtin_amdgcn_readlane(v.x, 2)));
      }
      kv_wait(P, s, h, known, opq(0));
      const unsigned kxo = WS_KF + fo, vxo = WS_VF + fo;
      dmas(rs, ab_a, kxo, lkx);
      dmas(rs, ab_a, kxo + 1024, lkx + 1024);
      dmas(rs, ab_a, WS_QF + fo, lbase + LA_P + 1024);
      dmas(rs, ab_a, WS_QF + fo + 1024, lbase + LA_P + 1024);
      if (wid == 2 && s + 1 >= known) {
        const int ln = opq(0);
        const unsigned fv = FLG_OFF + F_PRG(min((ln >> 1) & 3, 2), h) * 128 + (ln & 1) * 4;
        asm volatile("s_mov_b32 m0, %2\n\ts_nop 0\n\tbuffer_load_dword %0, %1, 0 offen sc1 lds" :: "v"(fv), "s"(rs), "s"(lbase + LA_P + 640) : "memory");
      }
      unsigned rg = 0x3c000000u;
      if (s == NSEG - 1) { A_LAST_Q(); break; }
      A_MAIN_Q();
      TL(s);
      GSFLUSH(0, 24 + wid); GSFLUSH(1, 4 + wid); GSFLUSH(2, 12 + j); GSFLUSH(3, 16 + j); GSFLUSH(4, 20 + j);
      if (s + PD < NSEG && !(opq(0) & 8)) __builtin_amdgcn_raw_buffer_store_b128(poison4(), rs, oo_v, step_base(cb, mask, s + PD), AUX_SC1);
    } else {
      const unsigned qxo = WS_QF + fo - (j & 1) * 2048, oxl = h * 8192 + (j & 1) * 4096 + (j >> 1) * 1024;
      const unsigned oxo = (s == 0 || s == NSEG - 1 ? WS_DUM : WS_OX + (unsigned)(s - 1) * 262144u) + oxl;
      A_MAIN_K();
      if (s >= NSEG - 2) {
        __builtin_amdgcn_raw_buffer_store_b128((u32x4){oxr[0], oxr[1], oxr[2], oxr[3]}, rs, xo_v, WS_OX + (unsigned)s * 262144u + oxl, AUX_SC1);
        __builtin_amdgcn_raw_buffer_store_b128((u32x4){oxr[4], oxr[5], oxr[6], oxr[7]}, rs, xo_v + 512, WS_OX + (unsigned)s * 262144u + oxl, AUX_SC1);
      }
      if (wid == 7) GSFLUSH(0, 28);
      GSFLUSH(1, 4 + wid);
    }
  }
  VMWAIT();
  BARRIER();
  if (wid == 4) flag_set(P, F_PRG(4, h), NSEG, opq(0));
}

#define TSL 4
#define LB_MEM 0
#define LB_RED 16384
template <bool VW>
__device__ __forceinline__ void b_wave(LAS unsigned char* lds, Ptrs& P, int r, int c, int wid, int bs, int xg) {
  constexpr int NHD = VW ? 2 : 6;
  char* ws = P.ws;
  const int lane = opq(0), tid = wid * 64 + lane;
  const int j = wid & 3, g = lane >> 4, q4 = (lane & 15) >> 2, p4 = lane & 3;
  const int b = r * 32 + c;
  const rsrc_t rs = WS_RSRC(P);
  const rsrc_t rv = __builtin_amdgcn_make_buffer_rsrc((void*)P.Wv, 0, HD * DM * 2, RSRC_W3);
  const rsrc_t ro = __builtin_amdgcn_make_buffer_rsrc((void*)P.Wo, 0, HD * DM * 2, RSRC_W3);
  const unsigned lbase = (unsigned)(size_t)lds;
  int lim = SPIN_BUDGET;
  const int h0 = ((VW ? 2 * j : 8 + 6 * j) + 5 * c) & 31;
  u32x8 wv[16], wo[6][2][2];
  {
    if ((wid & 3) < 2 + MAP && xg < 2) {
      const unsigned nk = 8 + 2 * xg, M = (bs * (2 + MAP) + (wid & 3)) * 64 + lane, row = M / nk, k = M - row * nk;
      if (row < HELP) touch(VW ? P.Wq : P.Wk, HD * DM * 2, (row < 256 ? row + (VW ? 512 : 256) : row + 512) * (HD * 2) + min(xg * 4096 + (k >> 1) * 1024 + 256, 7936u) + (k & 1) * 128, lds);
    }
    const int nl = MAP ? (xg == 2 ? 8 : 4) : xg == 2 ? 6 : 5;
    #pragma unroll
    for (int i = 0; i < 2; ++i) {
      const unsigned L = bs * 1024 + wid * 128 + i * 64 + lane, row = L / nl;
      touch(P.Wo, HD * DM * 2, row * (DM * 2) + (c - (bs >> 1)) * 64 + (L - row * nl) * 128, lds);
    }
    VMWAIT();
    BARRIER();
    const int pos = lane >> 2;
    const int prow = (pos & 9) | ((pos & 2) << 1) | ((pos & 4) >> 1);
    const unsigned dv0 = (unsigned)(prow * (DM * 2) + c * 64 + ((lane & 3) << 4));
    const unsigned dv1 = (unsigned)(prow * (DM * 2) + c * 64 + (((lane & 3) ^ 2) << 4));
    const int toff = wid * 16384 + (g * 16 + q4) * 64 + (((p4 >> 1) ^ ((g & 1) << 1)) << 4) + 8 * (p4 & 1);
    #pragma unroll
    for (int rd = 0; rd < NHD / 2; ++rd) {
      #pragma unroll
      for (int hh = 0; hh < 2; ++hh) {
        const int h = (h0 + 2 * rd + hh) & 31;
        #pragma unroll
        for (int i = 0; i < 8; ++i)
          DMA_O(ro, (i & 1) ? dv1 : dv0, (unsigned)(h * 128 + i * 16) * (DM * 2), lbase + wid * 16384 + hh * 8192 + i * 1024);
      }
      VMWAIT();
      #pragma unroll
      for (int hh = 0; hh < 2; ++hh)
        #pragma unroll
        for (int ks = 0; ks < 2; ++ks)
          #pragma unroll
          for (int t = 0; t < 2; ++t)
            wo[2 * rd + hh][ks][t] = rd_frag<4096, 256>(lds + (toff ^ (t * 32)) + hh * 8192 + ks * 512);
      #pragma unroll
      for (int hh = 0; hh < 2; ++hh)
        WO_PIN(VW, 2 * rd + hh);
      asm volatile("s_waitcnt lgkmcnt(0)" ::: "memory");
    }
    const int lr = lane >> 3;
    #pragma unroll
    for (int i = 0; i < 16; ++i) {
      const int sw = (((lr >> 2) & 1) | ((i & 1) << 1)) << 1;
      const unsigned dv = (unsigned)(lr * (HD * 2) + b * 128 + (((lane & 7) ^ sw) << 4));
      DMA_V(rv, dv, (unsigned)(wid * 128 + i * 8) * (HD * 2), lbase + wid * 16384 + i * 1024);
    }
    VMWAIT();
    BARRIER();
    if (VW) {
      const int trow = 16 * (g >> 1) + 4 * (g & 1) + 8 * (q4 >> 1) + (q4 & 1);
      const int tsw = ((g & 1) | ((q4 >> 1) << 1)) << 1;
      const int tv = j * 32768 + trow * 128 + (((p4 >> 1) ^ tsw) << 4) + 8 * (p4 & 1);
      #pragma unroll
      for (int i = 0; i < 16; ++i) wv[i] = rd_frag<4096, 256>(lds + (tv ^ ((i & 3) * 32)) + (i >> 2) * 8192);
      V_PIN();
    }
    LBARRIER();
  }
  const unsigned cb = comm_base(ws);
  get_tag(P);
  if (wid == 0) touch(P.x, SEQ * DM * 2, (SEQ - 64 + lane) * 2048 + c * 64, lds);
  const unsigned mask = rec_go(lds + LB_RED + 4096 + 64, P, (u32x2){0u, P.tag - 1u}, wid, lane);
  const int idx = (lane & 1) ? 0xEEEE : 0x4444;
  const unsigned fb = step_base(cb, mask, 0);
  volatile LAS unsigned* tsl = (volatile LAS unsigned*)(lds + LB_RED + 4096);
  if (tid < 8) tsl[tid] = tid ? 0u : (unsigned)__builtin_amdgcn_s_memrealtime() * 4u;
  unsigned dn = 600;
  int sn = 210;
  int da = 560 * 16, gb = 350 * 16;
  if (VW) __builtin_amdgcn_s_setprio(2);
  LBARRIER();
  report(P, wid);
  TL(72);
  for (int s = 0; s < NSEG; ++s) {
    const unsigned sb = step_base(cb, mask, s);
    const int ln = opq(lane), gg = ln >> 4;
    GSDECL;
    RTC(32 + b);
    const int l16 = ln * 16;
    const int xc = 32 * (c & 1) + 2 * (ln & 15);
    const unsigned mo_v = MF_AT(c >> 1) + ((xc >> 4) * 16 + (4 * r + gg) * 2 + ((xc >> 3) & 1)) * 16 + (xc & 7) * 2;
    const unsigned tm = __builtin_amdgcn_readfirstlane(*tsl), tgt = tm + DLB;
    if (!VW) {
      LBARRIER();
      if (wid == 7) {
        const int d = (int)((unsigned)__builtin_amdgcn_s_memrealtime() * 4u - tgt);
        const bool late = rfl(tsl[3]) == tgt || d > sn + 100;
        if (!late) sn = (sn + d) >> 1;
        const unsigned to = tm + dn + (late ? max(d - sn + SN0, 0) : 0);
        if (ln == 0) tsl[1] = to;
        if (b == 0) {
          dn = (da >> 4) + DL1;
          if (ln == 0) { *tsl = to + (gb >> 4) + DL2; st8(rs, fb + FL_T(s + 1), (u32x2){to + (gb >> 4) + DL2, (dn << 8) | (s + 1)}); }
          __builtin_amdgcn_s_sleep(4);
          const unsigned va = ld4(rs, fb + FL_O(ln & 31)), vb = ld4(rs, fb + FL_B(ln));
          if (s > 2) {
            track(da, rfl(wmax(va)), s < 8 ? 5 : DCA);
            track(gb, rfl(wmax(vb)), s < 8 ? 5 : DCA);
          }
        }
      }
      if (wid == 7 && s > 0 && s + 2 < NSEG) __builtin_amdgcn_raw_buffer_store_b32(~0u, rs, mo_v, step_base(cb, mask, s + 2), AUX_SC1);
    } else {
      const unsigned mo = sb + MF_AT(4 * j), pw_a = lbase + LB_MEM + j * 512 + ln * 8, pr_a = lbase + LB_MEM + j * 2048 + ln * 8;
      lim = __builtin_amdgcn_readfirstlane(lim);
      const unsigned vo = VB_AT(b >> 1) + ((b & 1) * 4 + j) * 256;
      V_PROJ();
      GS(0); GSFLUSH(0, 4 + j);
      if (s + PD < NSEG && ln < 16) st16(rs, step_base(cb, mask, s + PD) + vo + l16, poison4());
    }
    GSFLUSH(3, 24 + wid);
    if (s + 1 == NSEG) break;
    unsigned so[6];
    #pragma unroll
    for (int i = 0; i < NHD; ++i) { const int h = (h0 + i) & 31; so[i] = __builtin_amdgcn_readfirstlane(sb + OB_AT(r, h)); }
    const unsigned sn = __builtin_amdgcn_readfirstlane(step_base(cb, mask, s + 1));
    const unsigned rw_a = lbase + LB_RED + (wid * 128 + (gg & 1) * 64 + (gg >> 1) * 16 + (ln & 15)) * 4;
    const unsigned rr_a = lbase + LB_RED + ln * 8;
    if (wid == 7) {
      if (b != 0) {
        __builtin_amdgcn_s_sleep(4);
        unsigned tm = 0;
        dn = 0;
        plan_wait(rs, fb + FL_T(s + 1), s + 1, lim, tm, dn);
        if (ln == 0) *tsl = tm;
      }
      const unsigned tgt = rfl(tsl[1]);
      T_WAIT();
    }
    lim = __builtin_amdgcn_readfirstlane(lim);
    unsigned long long t1;
    if (VW) WO_V(); else WO_W();
    if (wid == 4) {
      if (ln == 0) st4(rs, fb + FL_B(b), (unsigned)t1 * 4u - tsl[1]);
    }
    if (wid == 4) GSFLUSH(2, 16);
    TL(s);
    GSFLUSH(1, 8 + wid);
    if (wid == 0 || wid == 4) { GSFLUSH(4, 17 + (wid >> 2)); GSFLUSH(0, 19 + (wid >> 2)); }
  }
  __builtin_amdgcn_s_setprio(0);
  const int ln = opq(0);
  TL(64);
  volatile LAS int* rdy = (volatile LAS int*)(lds + 98304 + 3 * 16384);
  if (tid == 0) *rdy = 61;
  LBARRIER();
#define T_SRC(sg, i) (WS_OX + (unsigned)((sg) * NH + ((h0 + (i)) & 31)) * 8192u + r * 1024)
#define T_MMA(i, k, f) do { const u32x8 w0 = wo[i][(k) & 1][0], w1 = wo[i][(k) & 1][1]; const int e = ((k) >> 1) * 4; \
    a0 = MFMA32(((u32x4){w0[e], w0[e + 1], w0[e + 2], w0[e + 3]}), f, a0); a1 = MFMA32(((u32x4){w1[e], w1[e + 1], w1[e + 2], w1[e + 3]}), f, a1); } while (0)
#define T_DMA(sg, i) do { _Pragma("unroll") for (int k = 0; k < 4; ++k) dmas(rs, ln * 16, T_SRC(sg, i) + k * 2048, lbase + j * 24576 + (i) * 4096 + k * 1024); } while (0)
  const int cb2 = 16 * ((ln >> 4) & 1) + 8 * (ln >> 5);
  const unsigned eo = (unsigned)((NSEG - 1 - j) * 32 + r * 16 + (ln & 15)) * 2048u + (unsigned)(c * 32 + cb2) * 2u;
  u32x4 gv, xv;
  bool ahead = false;
  f32x4 k0, k1;
  #pragma unroll 1
  for (int sg = XS3; sg < NSEG; ++sg) {
    f32x4 a0 = zero4(), a1 = zero4();
    if (VW) {
      if (wid == 3) {
        for (int w = __builtin_amdgcn_readfirstlane(*rdy); w <= sg;) {
          w = max(w + 1, -wmax(-(int)flag_wait<TSL>(P, F_PRG(4, ln & 31), w + 1, ln)));
          if (ln == 0) *rdy = w;
        }
      } else while (__builtin_amdgcn_readfirstlane(*rdy) <= sg) __builtin_amdgcn_s_sleep(1);
      u32x4 of[8];
      #pragma unroll
      for (int i = 0; i < 8; ++i) of[i] = __builtin_amdgcn_raw_buffer_load_b128(rs, ln * 16 + (i & 3) * 2048, T_SRC(sg, i >> 2), AUX_SC1);
      #pragma unroll
      for (int i = 0; i < 8; ++i) T_MMA(i >> 2, i & 3, of[i]);
      if (sg == XS3 && wid < NSEG - XS3) {
        xv = *(const u32x4*)((const char*)P.x + eo);
        cnt_wait(P, C_W4(15), 4, ln);
        gv = ld16(rs, WS_G + (unsigned)((NSEG - 1 - j) * 2 + r) * 32768u + c * 1024 + blk_in(ln & 15, cb2));
      }
    } else {
      if (!ahead) {
        while (__builtin_amdgcn_readfirstlane(*rdy) <= sg) __builtin_amdgcn_s_sleep(1);
        #pragma unroll
        for (int i = 0; i < NHD; ++i) T_DMA(sg, i);
      }
      VMW(20);
      ahead = __builtin_amdgcn_readfirstlane(*rdy) > sg + 1;
      u32x4 of[2][4];
#define T_RD(i) do { _Pragma("unroll") for (int k = 0; k < 4; ++k) of[(i) & 1][k] = *(const LAS u32x4*)(lds + j * 24576 + (i) * 4096 + k * 1024 + ln * 16); } while (0)
      T_RD(0);
      #pragma unroll
      for (int i = 0; i < NHD; ++i) {
        asm volatile("s_waitcnt lgkmcnt(0)" : "+v"(of[i & 1][0]), "+v"(of[i & 1][1]), "+v"(of[i & 1][2]), "+v"(of[i & 1][3]) :: "memory");
        if (i + 1 < NHD) {
          if (ahead) VMW(16); else G_WAIT_V(16 - 4 * (i < 4 ? i : 4));
          T_RD(i + 1);
        }
        #pragma unroll
        for (int k = 0; k < 4; ++k) {
          if (ahead) dmas(rs, ln * 16, T_SRC(sg + 1, i) + k * 2048, lbase + j * 24576 + i * 4096 + k * 1024);
          T_MMA(i, k, of[i & 1][k]);
          __builtin_amdgcn_sched_barrier(0);
        }
      }
    }
    LAS f32x4* pw = (LAS f32x4*)(lds + 98304 + ((sg & 3) * 8 + wid) * 2048) + ln;
    if (wid == 0 && sg == NSEG - 1) { k0 = a0; k1 = a1; } else { pw[0] = a0; pw[64] = a1; }
  }
  if (VW && wid < NSEG - XS3) {
    LBARRIER();
    const LAS f32x4* pr = (const LAS f32x4*)(lds + 98304 + (3 - j) * 16384) + ln;
    f32x4 t[2];
    #pragma unroll
    for (int k = 0; k < 2; ++k)
      t[k] = (((wid ? pr[k * 64] : k ? k1 : k0) + pr[128 + k * 64]) + (pr[256 + k * 64] + pr[384 + k * 64])) + ((pr[512 + k * 64] + pr[640 + k * 64]) + (pr[768 + k * 64] + pr[896 + k * 64]));
    fin_out(P, eo, epi_pack(t[0], t[1]), gv, xv);
  } else LBARRIER();
}

#define DMA_S dman
#define XPC 20
#define LS_P 131072
template <int MODE>
__device__ __forceinline__ void s_role(LAS unsigned char* lds, Ptrs& P, const u16* W, int h, unsigned obase, int nseg, int fl, int wid, int s0) {
  constexpr bool XM = MODE == 2;
  constexpr unsigned RS = XM ? DM * 2 : HD * 2, SS = XM ? 262144u : 65536u;
  const int lane = opq(0);
  const int kh = wid >> 2, tp = wid & 3;
  const rsrc_t rs = WS_RSRC(P);
  const rsrc_t rw = __builtin_amdgcn_make_buffer_rsrc((void*)W, 0, HD * DM * 2, RSRC_W3);
  const unsigned lbase = (unsigned)(size_t)lds;
  u32x8 w[8][2];
  unsigned f0 = 0;
  {
    const int g = lane >> 4, q4 = (lane & 15) >> 2, p4 = lane & 3;
    const int trow = XM ? 16 * g + 4 * (q4 >> 1) + (q4 & 1) : 8 * g + q4;
#define S_SW(row) ((((row) & 1) | ((((row) >> (XM ? 2 : 1)) & 1) << 1) | ((((row) >> (XM ? 4 : 3)) & 1) << 2)) << 1)
    const int toff = trow * 256 + (((4 * tp + (p4 >> 1)) ^ S_SW(trow)) << 4) + 8 * (p4 & 1);
    unsigned dv[2];
    #pragma unroll
    for (int x = 0; x < 2; ++x) {
      const int row = 8 * wid + 4 * x + (lane >> 4);
      dv[x] = (unsigned)row * RS + (XM ? (h >> 3) * 1024 * RS + (h & 7) * 256 : h * 256) + (((lane & 15) ^ S_SW(row)) << 4);
    }
#define S_IMG(i) do { DMA_S(rw, dv[0], (i) * 64 * RS, lbase + ((i) & 7) * 16384 + wid * 2048); \
                      DMA_S(rw, dv[1], (i) * 64 * RS, lbase + ((i) & 7) * 16384 + wid * 2048 + 1024); if (XM && XPC) __builtin_amdgcn_s_sleep(XPC); } while (0)
    #pragma unroll
    for (int i = 0; i < 8; ++i) S_IMG(i);
    #pragma unroll
    for (int b = 0; b < 4; ++b) {
      if (b == 3 && !XM) { f0 = ld4(rs, FLG_OFF + F_LN0(lane & 7) * 128 + 4); get_tag(P); }
      if (b < 3) VMW(8); else VMWAIT();
      BARRIER();
      if (kh == (b >> 1)) {
        #pragma unroll
        for (int i = 4 * (b & 1); i < 4 * (b & 1) + 4; ++i) {
          w[i][0] = rd_frag<XM ? 2048 : 8192, XM ? 512 : 1024>(lds + toff + i * 16384);
          w[i][1] = rd_frag<XM ? 2048 : 8192, XM ? 512 : 1024>(lds + (toff ^ 32) + i * 16384);
        }
      }
      LBARRIER();
      if (b < 2) {
        #pragma unroll
        for (int i = 4 * b + 8; i < 4 * b + 12; ++i) S_IMG(i);
      }
    }
  }
  TL(XM ? 74 : 72);
  const unsigned l16 = lane * 16;
  volatile LAS int* rd = (volatile LAS int*)(lds + LS_P + 20480);
#define X_READY(want) do { \
    if (wid == 0) { \
      const int n_ = -wmax(-(int)flag_wait(P, F_PRG(4, (h & 24) + (lane & 7)), s0 + (want), lane)) - s0; \
      if (lane == 0) *rd = max(n_, want); \
    } \
    LBARRIER(); } while (0)
  if (MODE == 2) X_READY(2);
  else if (!XM) {
    if (__any(f0 != P.tag)) flag_wait(P, F_LN0(lane & 7), 0, lane);
  }
  unsigned sx = (XM ? WS_OX + s0 * SS + (h >> 3) * 65536 : WS_XN) + kh * 32768 + tp * 4096;
  const unsigned sd = lbase + kh * 65536 + tp * 4096;
  const unsigned xl = kh * 65536 + (XM ? l16 : blk_in(lane & 15, (lane >> 4) * 8));
  const unsigned xp = lbase + xl, pa = lbase + LS_P + tp * 4096 + l16;
  LAS f32x4* const pq = (LAS f32x4*)(lds + LS_P + tp * 4096 + l16);
  if (XM) {
    #pragma unroll
    for (int i = 0; i < 12; ++i) dmas(rs, l16, sx + (i >> 3) * SS + ((i >> 2) & 1) * 16384 + (i & 3) * 1024, sd + (i >> 2) * 16384 + (i & 3) * 1024);
    VMWAIT();
    BARRIER();
  } else {
    #pragma unroll
    for (int i = 0; i < 16; ++i) dmas(rs, l16, sx + (i >> 3) * SS + ((i >> 2) & 1) * 16384 + (i & 3) * 1024, sd + ((i >> 2) ^ 2) * 16384 + (i & 3) * 1024);
    VMW(12);
    BARRIER();
    f32x4 a[4];
    if (MODE) ONE_V(xp + 32768); else ONE_K(xp + 32768);
    pq[kh * 128] = kh ? a[0] : a[MODE ? 1 : 2]; pq[kh * 128 + 64] = kh ? a[MODE ? 2 : 1] : a[3];
    LBARRIER();
    const f32x4 a0 = (kh ? a[MODE ? 1 : 2] : a[0]) + pq[128 - kh * 128], a1 = (kh ? a[3] : a[MODE ? 2 : 1]) + pq[192 - kh * 128];
    if (MODE) st16(rs, obase + h * 8192 + (2 * tp + kh) * 1024 + l16, (u32x4){pack2(a0[0], a0[1]), pack2(a0[2], a0[3]), pack2(a1[0], a1[1]), pack2(a1[2], a1[3])});
    else st16(rs, obase + h * 8192 + kh * 4096 + tp * 1024 + l16, (u32x4){pack2(a0[0], a1[0]), pack2(a0[1], a1[1]), pack2(a0[2], a1[2]), pack2(a0[3], a1[3])});
    sx += SS;
    #pragma unroll
    for (int i = 0; i < 4; ++i) dmas(rs, l16, sx + SS + i * 1024, sd + 32768 + i * 1024);
    VMW(4);
    BARRIER();
    if (wid == 1) flag_set(P, fl, 1, lane);
    TL(62);
  }
  u32x8 xf[3], ac[2] = {};
  #pragma unroll
  for (int i = 0; i < 3; ++i) {
    const u32x4 a = *(const LAS u32x4*)(lds + xl + i * 2048), b = *(const LAS u32x4*)(lds + xl + i * 2048 + 1024);
    xf[i] = (u32x8){a[0], a[1], a[2], a[3], b[0], b[1], b[2], b[3]};
  }
  unsigned long long fm, pm;
  asm volatile("s_cmp_eq_u32 %2, 1\n\ts_cselect_b64 %0, 1, 0\n\ts_cmp_eq_u32 %3, 1\n\ts_cselect_b64 %1, -1, 0" : "=&s"(fm), "=&s"(pm) : "s"(wid), "s"(XM ? 0 : wid >> 1) : "scc");
  const unsigned fo = FLG_OFF + fl * 128, pd = lbase + LS_P + 16384;
  const unsigned po = 4 * 65536 + h * 2048 + (wid & 1) * 1024 - tp * 4096 - kh * 32768;
  unsigned oo = obase + (unsigned)h * 8192u + tp * (MODE == 1 ? 2048 : 1024) + (XM ? (s0 - 1) * 0x40000u : 0u), tmp, tmp2;
  unsigned long long em;
  int t = XM ? 0 : 1;
  if (MODE < 2) {
    #pragma unroll 1
    for (int part = 0; part < 2; ++part) {
      if (part == 1) { TL(63); if (wid == 0) setup_wait(P, lane); }
      if (MODE) S_LOOP_V(part ? nseg + 1 : 3, 0); else S_LOOP_K(part ? nseg + 1 : 3, 0);
    }
  } else {
    #pragma unroll 1
    while (t < nseg) {
      X_READY(min(t + 4, nseg));
      const int n = __builtin_amdgcn_readfirstlane(*rd), te = n >= nseg ? nseg : (n - 2) & ~1;
      TL(t);
      S_LOOP_X(te, nseg);
    }
  }
  TL(XM ? 65 : 64);
  VMWAIT();
  BARRIER();
  if (wid == 5) flag_set(P, fl, nseg, lane);
  if (XM && s0 == 0) {
    sx = WS_OX + (h >> 3) * 65536 + tp * 4096;
#define X_GET(t) do { _Pragma("unroll") for (int i = 0; i < 16; ++i) \
      dmas(rs, l16, sx + (t) * SS + ((i >> 2) & 1) * 32768 + (i >> 3) * 16384 + (i & 3) * 1024, sd + ((i >> 2) & 1) * 65536 + ((t) & 1) * 32768 + (i >> 3) * 16384 + (i & 3) * 1024); } while (0)
    bool have = false;
    const unsigned pv = FLG_OFF + F_PRG(kh ? 7 : 4, kh ? h : (h & 24) + (lane & 7)) * 128;
    if (!kh) __builtin_amdgcn_s_setprio(1);
    #pragma unroll 1
    for (int t = XS2; t < XS3; ++t) {
      if (!have && !kh) {
        flag_wait<2>(P, F_PRG(4, (h & 24) + (lane & 7)), t + 1, lane);
        X_GET(t);
        VMW(8);
      }
      BARRIER();
      f32x4 a[4];
      unsigned pg;
      ONE_X(xp + (t & 1) * 32768);
      if (wid == 5) flag_set(P, fl, t - XS2 + XS0, lane);
      have = t + 1 < XS3 && !__any(pg < (unsigned)(t + 2));
      if (!kh && lane == 0) rd[tp] = have;
      pq[kh * 128] = kh ? a[0] : a[2]; pq[kh * 128 + 64] = kh ? a[1] : a[3];
      if (have && !kh) X_GET(t + 1);
      LBARRIER();
      const f32x4 a0 = (kh ? a[2] : a[0]) + pq[128 - kh * 128], a1 = (kh ? a[3] : a[1]) + pq[192 - kh * 128];
      st16(rs, obase + (unsigned)(t * NH + h) * 8192u + kh * 4096 + tp * 1024 + l16, (u32x4){pack2(a0[0], a1[0]), pack2(a0[1], a1[1]), pack2(a0[2], a1[2]), pack2(a0[3], a1[3])});
      if (__any(rd[lane & 3] == 0)) {
        VMWAIT();
        BARRIER();
        if (wid == 5) flag_set(P, fl, t + 1 - XS2 + XS0, lane);
      } else if (!kh) VMW(9);
      asm volatile("s_waitcnt lgkmcnt(0)" ::: "memory");
      TL(t - XS2 + XS0);
    }
    __builtin_amdgcn_s_setprio(0);
    TL(65);
  }
}

#define LND 1
#define CALN 6
__device__ __forceinline__ void setup_role(LAS unsigned char* lds, Ptrs& P, int cu, int wid) {
  const int lane = opq(0), tid = wid * 64 + lane;
  const rsrc_t rs = WS_RSRC(P);
  u32x4 v[8];
  const u32x4* px = (const u32x4*)(P.x + (size_t)(cu * 32 + wid * 4 + (lane >> 4)) * DM) + (lane & 15);
  #pragma unroll
  for (int i = 0; i < 8; ++i) v[i] = __builtin_nontemporal_load(px + i * 16);
  const unsigned epo = __hip_atomic_load((gu32*)(P.ws + EPO_OFF), RLX_AGENT);
  const unsigned cb = comm_base(P.ws);
  const u32x4 z = {0u, 0u, 0u, 0u};
  const int cr = 63 - cu;
  if (wid == 0 && cr < 16) st16(rs, CNT_OFF + cr * 1024 + lane * 16, z);
  if (cr < 4) st16(rs, cb + FL_B(0) + cr * 8192 + tid * 16, z);
  if (cr == 4 && tid < 256) {
    const int g4 = tid >> 6, k = (tid >> 5) & 1 & (g4 >> 1);
    st16(rs, WS_H1 + ((g4 & 1) ? 127u * 32768u : 0u) + (g4 * 8 + ((tid >> 2) & 7)) * 1024 + blk_in((g4 & 1) ? 15 - k : k, (tid & 3) * 8), z);
  }
  #pragma unroll
  for (int i = 0; i < 2; ++i) {
    const int f = i * 54 + cr;
    if (cr < PD * NCHUNK) {
      u32x4 m = {~0u, ~0u, ~0u, ~0u};
      if (f < 4) {
        const int ks = (f >> 1) * 8 + wid;
        m = *(const u32x4*)(P.mem0 + ((lane >> 1) & 7) * DM + 64 * ks + 16 * (lane >> 4) + 8 * (lane & 1));
        m.x = canon1(m.x); m.y = canon1(m.y); m.z = canon1(m.z); m.w = canon1(m.w);
      }
      st16(rs, cb + (unsigned)(f / (NCHUNK * 2)) * CB_STEP + (f % (NCHUNK * 2)) * 8192u + tid * 16, m);
    }
  }
  const unsigned bi = blk_in((wid & 3) * 4 + (lane >> 4), (lane & 3) * 8);
  const int c0 = (lane >> 2) & 3;
  ln_rows(v);
  TL(69);
  if (cu >= 8) for (int i = 0; i < LND; ++i) __builtin_amdgcn_s_sleep(32);
  #pragma unroll
  for (int i = 0; i < 8; ++i) st16(rs, WS_XN + xn_blk(cu * 2 + (wid >> 2), 4 * i + c0) + bi, v[i]);
  P.tag = __builtin_amdgcn_readfirstlane(epo) + 1u;
  ln_rows(v);
  if (cu < 8) {
    VMWAIT();
    BARRIER();
    if (wid == 0) flag_set(P, F_LN0(cu), 1, lane);
    TL(66);
  }
  asm volatile("" ::: "memory");
  #pragma unroll
  for (int i = 0; i < 8; ++i) st16(rs, WS_XN2 + (unsigned)(cu * 2 + (wid >> 2)) * 32768u + (4 * i + c0) * 1024 + bi, v[i]);
  touch(P.w1, DM * DM * 2, ((cu & 31) * 512 + tid) * 128, lds);
  VMW(9);
  BARRIER();
  if (wid == 0) flag_set(P, F_LN0(cu), 1, lane);
  TL(70);
  if (wid == 0) VMW(2); else VMW(1);
  BARRIER();
  if (wid == 0) flag_set(P, F_LN1(cu), 1, lane);
  TL(71);
  if (cu < 32 && (wid >> 1) == 1) {
    const int id = cu * 2 + (wid & 1);
    unsigned best[2] = {65535u, 65535u};
    #pragma unroll
    for (int it = 0; it < CALN; ++it) {
      const unsigned long long c0 = __builtin_amdgcn_s_memtime();
      asm volatile("s_waitcnt lgkmcnt(0)" ::: "memory");
      if (lane == 0) st4(rs, cb + (id & 3) * CB_STEP + (30 + ((id >> 2) & 1)) * 16384u + (it & 1) * 8192u + (id >> 3) * 1024u, 0u);
      VMWAIT();
      const unsigned d = (unsigned)(__builtin_amdgcn_s_memtime() - c0);
      if (it >= 2) best[it & 1] = min(best[it & 1], d);
    }
    flag_set(P, F_CAL(id), (best[0] << 16) | best[1], lane);
  }
}

#define CPF 1
__global__ __launch_bounds__(512, 2) void mega_kernel(const u16* x, const u16* mem0, const u16* Wq, const u16* Wk, const u16* Wv, const u16* Wo,
                                                     const u16* w1, const u16* w2, const u16* w3, const u16* w4, u16* out, char* ws) {
  __shared__ __attribute__((aligned(16))) unsigned char smem[163840];
  LAS unsigned char* lds = (LAS unsigned char*)smem;
  Ptrs P;
  P.x = x; P.mem0 = mem0; P.Wq = Wq; P.Wk = Wk; P.Wv = Wv; P.Wo = Wo; P.w1 = w1; P.w2 = w2; P.w3 = w3; P.w4 = w4; P.out = out; P.ws = ws;
  const int xcd = blockIdx.x & 7, slot = blockIdx.x >> 3;
  const int wid = __builtin_amdgcn_readfirstlane(threadIdx.x >> 6);
  TL(79);
  P.tag = 0;
  if (wid == 7) {
    unsigned long long pc;
    unsigned len;
    asm volatile("s_getpc_b64 %0\nMK_BEG:\n\ts_mov_b32 %1, MK_END-MK_BEG" : "=s"(pc), "=s"(len));
    touch((const void*)pc, len, (slot * 64 + opq(0)) * 128, lds);
  }
  const int na = xcd == 2 ? 8 : 12;
  const bool isa = xcd < 3 && slot < na;
  const int ah = xcd == 2 ? slot * 4 + 2 : xcd * 16 + (slot & 3) * 4 + (slot >> 2) + (slot >> 3), bs = slot - na, br = bs & 1, bc = (xcd == 1 ? 22 : xcd * 5) + (bs >> 1);
  if (isa) {
    if (wid < 4) a_wave<true>(lds, P, ah, wid); else a_wave<false>(lds, P, ah, wid);
  } else if (xcd < 3) {
    if (wid < 4) b_wave<true>(lds, P, br, bc, wid, bs, xcd); else b_wave<false>(lds, P, br, bc, wid, bs, xcd);
  } else {
    if ((xcd & 3) == 3) {
      setup_role(lds, P, (xcd >> 2) * 32 + slot, wid);
    } else if (xcd == 6) s_role<1>(lds, P, Wv, slot, WS_VF, NSEG, F_PRG(2, slot), wid, 0);
    else s_role<0>(lds, P, xcd == 4 ? Wk : Wq, slot, xcd == 4 ? WS_KF : WS_QF, NSEG, F_PRG(xcd - 4, slot), wid, 0);
    if ((xcd & 3) == 3) mlp_role(lds, P, xcd >> 2, slot, wid);
    else s_role<2>(lds, P, Wo, slot, WS_PT, xcd == 4 ? XS1 - XS0 : xcd == 5 ? XS2 - XS1 : XS0, F_PRG(xcd + 1, slot), wid, xcd == 4 ? XS0 : xcd == 5 ? XS1 : 0);
    report(P, wid);
    fin_role(lds, P, wid);
    if (blockIdx.x == 3 && wid == 0) {
      cnt_wait(P, C_ARR, 256, threadIdx.x);
      if (threadIdx.x == 0) __hip_atomic_store((gu32*)(ws + EPO_OFF), P.tag, RLX_AGENT);
    }
  }
  asm volatile("MK_END:");
  TL(78);
}

extern "C" void kernel_launch(void* const* d_in, const int* in_sizes, int n_in, void* d_out, int out_size,
                              void* d_ws, size_t ws_size, hipStream_t stream) {
  mega_kernel<<<256, 512, 0, stream>>>((const u16*)d_in[0], (const u16*)d_in[1], (const u16*)d_in[2], (const u16*)d_in[3], (const u16*)d_in[4],
                                       (const u16*)d_in[5], (const u16*)d_in[8], (const u16*)d_in[9], (const u16*)d_in[10], (const u16*)d_in[11],
                                       (u16*)d_out, (char*)d_ws);
}
```

```cpp
#include <hip/hip_runtime.h>
#include <hip/hip_bf16.h>
#include <stdint.h>

#define SEQ 2048
#define DM 1024
#define NH 32
#define DH 128
#define HD (NH * DH)
#define MM 8
#define SEG 32
#define NSEG (SEQ / SEG)
#define FF 3584

typedef unsigned short u16;
typedef __attribute__((ext_vector_type(8))) short bf16x8;
typedef __attribute__((ext_vector_type(4))) short bf16x4;
typedef __attribute__((ext_vector_type(4))) float f32x4;
typedef __attribute__((ext_vector_type(4))) unsigned u32x4;
typedef __attribute__((ext_vector_type(2))) unsigned u32x2;
typedef __attribute__((ext_vector_type(2))) float f32x2;
typedef __attribute__((ext_vector_type(2))) __bf16 bf16v2;
typedef __attribute__((ext_vector_type(8))) unsigned u32x8;
#define LAS __attribute__((address_space(3)))
#define GAS __attribute__((address_space(1)))

__device__ __forceinline__ unsigned pack2(float lo, float hi) {
  return __builtin_bit_cast(unsigned, __builtin_convertvector((f32x2){lo, hi}, bf16v2));
}
__device__ __forceinline__ float rb1(float x) { return __uint_as_float(pack2(0.f, x)); }

typedef GAS unsigned gu32;
#define RLX_AGENT __ATOMIC_RELAXED, __HIP_MEMORY_SCOPE_AGENT
#define SPIN_CAP (1u << 20)
#define AUX_SC1 16
#define RSRC_W3 0x00020000
typedef __amdgpu_buffer_rsrc_t rsrc_t;

__device__ __forceinline__ u32x4 ld16(rsrc_t rs, unsigned off) { return __builtin_amdgcn_raw_buffer_load_b128(rs, off, 0, AUX_SC1); }
__device__ __forceinline__ u32x2 ld8(rsrc_t rs, unsigned off) { return __builtin_amdgcn_raw_buffer_load_b64(rs, off, 0, AUX_SC1); }
__device__ __forceinline__ unsigned ld4(rsrc_t rs, unsigned off) { return __builtin_amdgcn_raw_buffer_load_b32(rs, off, 0, AUX_SC1); }
__device__ __forceinline__ void st16(rsrc_t rs, unsigned off, u32x4 v) { __builtin_amdgcn_raw_buffer_store_b128(v, rs, off, 0, AUX_SC1); }
__device__ __forceinline__ void st8(rsrc_t rs, unsigned off, u32x2 v) { __builtin_amdgcn_raw_buffer_store_b64(v, rs, off, 0, AUX_SC1); }
__device__ __forceinline__ void st4(rsrc_t rs, unsigned off, unsigned v) { __builtin_amdgcn_raw_buffer_store_b32(v, rs, off, 0, AUX_SC1); }
__device__ __forceinline__ float lo16(unsigned w) { return __uint_as_float(w << 16); }
__device__ __forceinline__ float hi16(unsigned w) { return __uint_as_float(w & 0xffff0000u); }

__device__ __forceinline__ unsigned canon1(unsigned w) { return min(w, 0xfffffffeu); }
__device__ __forceinline__ unsigned max4(u32x4 v) { return max(max(v.x, v.y), max(v.z, v.w)); }
__device__ __forceinline__ int opq(int) { int v; asm volatile("v_mbcnt_lo_u32_b32 %0, -1, 0\n\tv_mbcnt_hi_u32_b32 %0, -1, %0" : "=v"(v)); return v; }
__device__ __forceinline__ u32x4 poison4() { unsigned m = ~0u; asm volatile("" : "+v"(m)); return (u32x4){m, m, m, m}; }
__device__ __forceinline__ f32x4 zero4() { float m = 0.f; asm volatile("" : "+v"(m)); return (f32x4){m, m, m, m}; }
#define MFMA32(a, b, c) __builtin_amdgcn_mfma_f32_16x16x32_bf16(__builtin_bit_cast(bf16x8, a), __builtin_bit_cast(bf16x8, b), c, 0, 0, 0)
#define MFMA16(a, b, c) __builtin_amdgcn_mfma_f32_16x16x16bf16_1k(__builtin_bit_cast(bf16x4, a), __builtin_bit_cast(bf16x4, b), c, 0, 0, 0)
#define LBARRIER() do { asm volatile("s_waitcnt lgkmcnt(0)" ::: "memory"); __builtin_amdgcn_s_barrier(); asm volatile("" ::: "memory"); } while (0)
#define BARRIER() do { asm volatile("" ::: "memory"); __builtin_amdgcn_s_barrier(); asm volatile("" ::: "memory"); } while (0)
#define VMWAIT() asm volatile("s_waitcnt vmcnt(0)" ::: "memory")
#define VMW(n) asm volatile("s_waitcnt vmcnt(" #n ")" ::: "memory")
#define DELAY(n) do { if ((n) > 0) __builtin_amdgcn_s_sleep(n); } while (0)

#define EPO_OFF 0
#define TMO_OFF 128
#define FLG_OFF 256
#define F_LN0(i) (i)
#define F_LN1(i) (64 + (i))
#define F_CAL(i) (128 + (i))
#define F_PRG(t, h) (192 + (t) * 32 + (h))
#define CNT_OFF 65536
#define C_W1(p2) (p2)
#define C_W2(p2) (16 + (p2))
#define C_LN(p2) (32 + (p2))
#define C_W3(p2) (48 + (p2))
#define C_W4(p2) (64 + (p2))
#define C_HEAD 80
#define C_ARR 81
#define NCNT 128
#define WS_XN (1u << 20)
#define WS_XN2 (WS_XN + (4u << 20))
#define WS_QF (WS_XN2 + (4u << 20))
#define WS_KF (WS_QF + (16u << 20))
#define WS_VF (WS_KF + (16u << 20))
#define WS_DUM (WS_VF + (16u << 20))
#define WS_H1 (WS_DUM + (1u << 20))
#define WS_X1 (WS_H1 + (4u << 20) + 65536u)
#define WS_H3 (WS_X1 + (4u << 20))
#define WS_OX WS_QF
#define WS_X1N WS_H1
#define WS_G WS_XN2
#define WS_PT WS_KF
#define WS_COMM (WS_H3 + (14u << 20))
#define WS_END (WS_COMM + (4u << 20))
#define CB_STEP 524288u
#define PD 3
__device__ __forceinline__ unsigned comm_base(const char* ws) { return WS_COMM + ((0u - ((unsigned)(size_t)ws + WS_COMM)) & 0x1fffffu); }
__device__ __forceinline__ unsigned step_base(unsigned cb, unsigned mask, int s) { return cb + (unsigned)(s & 3) * CB_STEP + mask; }
#define MF_AT(ks) (((ks) >> 3) * 16384u + ((ks) & 7) * 1024u)
#define OB_AT(r, h) ((2u + (r) * 4u + ((h) >> 3)) * 16384u + ((h) & 7) * 1024u)
#define VB_AT(h) ((10u + ((h) >> 2)) * 16384u + ((h) & 3) * 2048u)
#define XS0 4
#define XS1 24
#define XS2 44
#define XS3 62
#define CSL 8
#define FSL 6
#define SQUICK 3
#define NCHUNK 18
#define FL_O(h) (29u * 16384u + (h) * 128u)
#define FL_B(b) (28u * 16384u + (b) * 128u)
#define FL_T(s) FL_O(32 + ((s) & 3))

struct Ptrs { const u16 *x, *mem0, *Wq, *Wk, *Wv, *Wo, *w1, *w2, *w3, *w4; u16* out; char* ws; unsigned tag; };
#define WS_RSRC(P) __builtin_amdgcn_make_buffer_rsrc((void*)(P).ws, 0, WS_END, RSRC_W3)

__device__ __forceinline__ bool spin_fail(unsigned& spins, const Ptrs& P, int lane) {
  asm volatile("" ::: "memory");
  if (++spins > SPIN_CAP) {
    unsigned tv;
    asm volatile("v_mov_b32 %0, %1" : "=v"(tv) : "s"(P.tag));
    if (lane == 0) __builtin_amdgcn_raw_buffer_store_b32(tv, WS_RSRC(P), 0, TMO_OFF, AUX_SC1);
    return true;
  }
  return (spins & 255u) == 0u && (unsigned)__builtin_amdgcn_readfirstlane(__builtin_amdgcn_raw_buffer_load_b32(WS_RSRC(P), 0, TMO_OFF, AUX_SC1)) == P.tag;
}
__device__ __forceinline__ unsigned cnt_load(char* ws, int idx) {
  return __builtin_amdgcn_readfirstlane(__hip_atomic_load((gu32*)(ws + CNT_OFF + idx * 128), RLX_AGENT));
}
__device__ __forceinline__ void cnt_add(char* ws, int idx, int lane) {
  asm volatile("s_waitcnt vmcnt(0)" ::: "memory");
  if (lane == 0) __hip_atomic_fetch_add((gu32*)(ws + CNT_OFF + idx * 128), 1u, RLX_AGENT);
}
__device__ __forceinline__ void cnt_wait(const Ptrs& P, int idx, unsigned want, int lane) {
  for (unsigned spins = 0;;) {
    if (cnt_load(P.ws, idx) >= want) break;
    __builtin_amdgcn_s_sleep(CSL);
    if (spin_fail(spins, P, lane)) break;
  }
}
template <int SL = FSL>
__device__ __forceinline__ unsigned flag_wait(const Ptrs& P, int f, unsigned want, int lane) {
  const rsrc_t rs = WS_RSRC(P);
  u32x2 v;
  for (unsigned spins = 0;;) {
    v = ld8(rs, FLG_OFF + f * 128);
    if (!__any(v.y != P.tag || v.x < want)) break;
    __builtin_amdgcn_s_sleep(SL);
    if (spin_fail(spins, P, lane)) break;
  }
  asm volatile("" ::: "memory");
  return v.x;
}
__device__ __forceinline__ void flag_set(const Ptrs& P, int f, unsigned val, int lane) {
  if (lane == 0) st8(WS_RSRC(P), FLG_OFF + f * 128, (u32x2){val, P.tag});
}
__device__ __forceinline__ void get_tag(Ptrs& P) { P.tag = __builtin_amdgcn_readfirstlane(__hip_atomic_load((gu32*)(P.ws + EPO_OFF), RLX_AGENT)) + 1u; }
__device__ __forceinline__ void setup_wait(const Ptrs& P, int lane) { flag_wait(P, F_LN0(lane), 0, lane); }
__device__ __forceinline__ void report(const Ptrs& P, int wid) {
  if (wid == 0 && opq(0) == 0) __hip_atomic_fetch_add((gu32*)(P.ws + CNT_OFF + C_ARR * 128), 1u, RLX_AGENT);
}
__device__ __forceinline__ void kv_wait(const Ptrs& P, int s, int h, int& known, int lane) {
  if (s < known) return;
  const unsigned v = flag_wait(P, F_PRG(min(lane, 2), h), s + 1, lane);
  known = __builtin_amdgcn_readfirstlane(min(min(__builtin_amdgcn_readlane(v, 0), __builtin_amdgcn_readlane(v, 1)), __builtin_amdgcn_readlane(v, 2)));
}
__device__ __forceinline__ int wmax(int v) {
  v = max(v, __builtin_amdgcn_update_dpp(0, v, 0x128, 0xf, 0xf, false));
  v = max(v, __builtin_amdgcn_update_dpp(0, v, 0x124, 0xf, 0xf, false));
  v = max(v, __builtin_amdgcn_update_dpp(0, v, 0x122, 0xf, 0xf, false));
  v = max(v, __builtin_amdgcn_update_dpp(0, v, 0x121, 0xf, 0xf, false));
  return max(max(__builtin_amdgcn_readlane(v, 0), __builtin_amdgcn_readlane(v, 16)), max(__builtin_amdgcn_readlane(v, 32), __builtin_amdgcn_readlane(v, 48)));
}
__device__ __forceinline__ unsigned near_half(unsigned v) { return wmax(-(int)(v & 65535u)) > wmax(-(int)(v >> 16)) ? 8192u : 0u; }
__device__ __forceinline__ void dmaw(rsrc_t rs, unsigned voff, unsigned soff, unsigned ldsaddr) {
  asm volatile("s_mov_b32 m0, %3\n\ts_nop 0\n\tbuffer_load_dwordx4 %0, %1, %2 offen lds" :: "v"(voff), "s"(rs), "s"(soff), "s"(ldsaddr) : "memory");
}
__device__ __forceinline__ void dman(rsrc_t rs, unsigned voff, unsigned soff, unsigned ldsaddr) {
  asm volatile("s_mov_b32 m0, %3\n\ts_nop 0\n\tbuffer_load_dwordx4 %0, %1, %2 offen nt lds" :: "v"(voff), "s"(rs), "s"(soff), "s"(ldsaddr) : "memory");
}
__device__ __forceinline__ void dmas(rsrc_t rs, unsigned voff, unsigned soff, unsigned ldsaddr) {
  asm volatile("s_mov_b32 m0, %3\n\ts_nop 0\n\tbuffer_load_dwordx4 %0, %1, %2 offen sc1 lds" :: "v"(voff), "s"(rs), "s"(soff), "s"(ldsaddr) : "memory");
}
__device__ __forceinline__ void touch(const void* w, unsigned bytes, unsigned voff, LAS unsigned char* lds) {
  asm volatile("s_mov_b32 m0, %2\n\ts_nop 0\n\tbuffer_load_dword %0, %1, 0 offen lds"
               :: "v"(voff), "s"(__builtin_amdgcn_make_buffer_rsrc((void*)w, 0, bytes, RSRC_W3)), "s"((unsigned)(size_t)lds + 148480) : "memory");
}
__device__ __forceinline__ u32x2 trd(LAS unsigned char* p) {
  return __builtin_bit_cast(u32x2, __builtin_amdgcn_ds_read_tr16_b64_v4i16((LAS bf16x4*)p));
}
template <int BO, int UO>
__device__ __forceinline__ u32x8 rd_frag(LAS unsigned char* p) {
  const u32x2 a = trd(p), b = trd(p + UO), c = trd(p + BO), d = trd(p + BO + UO);
  return (u32x8){a.x, a.y, b.x, b.y, c.x, c.y, d.x, d.y};
}

#define TL(i) do {} while (0)
#define TLW(i) do {} while (0)
#define GSDECL
#define GS(i) do {} while (0)
#define GSFLUSH(k, i) do {} while (0)

#define QPRIO 0
#define VPOS 3
#define VLDI "buffer_load_dword v254, v17, %[rs], %[sb] offen sc1\n\tbuffer_load_dword v255, v17, %[rs], %[sb] offen offset:256 sc1\n\t"
#define VLD(p) VLD##p
#define VLD0
#define VLD1
#define VLD2
#define VLD3 VLDI
#define VLD4
#define DEC1 1
#define DEC2 1
#define DGA
#define DGB
#define DGC
#define DGE
#define DGO
#define DGP
#define DGQ
#define PACE "s_sleep 1\n\t"
#define STR_(x) #x
#define STR(x) STR_(x)
#define GSW
#define GSA(k)
#define GSO(k)
#define A_LAST_Q() \
  asm volatile( \
    "v_mov_b32 v253, %[tag]\n\t" \
    "buffer_load_dwordx4 v[20:23], v12, %[rs], %[vx] offen sc1\n\t" \
    "buffer_load_dwordx4 v[24:27], v12, %[rs], %[vx] offen offset:1024 sc1\n\t" \
    "v_mov_b32 v0, 0\n\t" \
    "v_mov_b32 v1, 0\n\t" \
    "v_mov_b32 v2, 0\n\t" \
    "v_mov_b32 v3, 0\n\t" \
    "v_mov_b32 v4, 0\n\t" \
    "v_mov_b32 v5, 0\n\t" \
    "v_mov_b32 v6, 0\n\t" \
    "v_mov_b32 v7, 0\n\t" \
    "s_cmp_lg_u32 %[sc], 3\n\t" \
    "s_cbranch_scc1 PB_%=\n\t" \
    "s_getpc_b64 s[92:93]\n\t" \
    "TPA_%=:\n\t" \
    "s_add_u32 s92, s92, TEA_%=-TPA_%=\n\t" \
    "s_addc_u32 s93, s93, 0\n\t" \
    "s_lshr_b32 s91, %[tgt], 2\n\t" \
    "s_sub_u32 s91, s91, 3\n\t" \
    "TWA_%=:\n\t" \
    "s_memrealtime s[88:89]\n\t" \
    "s_waitcnt lgkmcnt(0)\n\t" \
    "s_sub_u32 s89, s91, s88\n\t" \
    "s_lshl_b32 s89, s89, 2\n\t" \
    "s_cmp_gt_i32 s89, 0\n\t" \
    "s_cbranch_scc1 TWA_%=\n\t" \
    "s_lshl_b32 s88, s88, 2\n\t" \
    "s_sub_u32 s89, %[tgt], s88\n\t" \
    "s_max_i32 s89, s89, 0\n\t" \
    "s_mul_i32 s89, s89, 11\n\t" \
    "s_lshr_b32 s89, s89, 1\n\t" \
    "s_and_b32 s89, s89, 0x7c\n\t" \
    "s_sub_u32 s92, s92, s89\n\t" \
    "s_subb_u32 s93, s93, 0\n\t" \
    "s_setpc_b64 s[92:93]\n\t" \
    "s_nop 0\n\t" \
    "s_nop 0\n\t" \
    "s_nop 0\n\t" \
    "s_nop 0\n\t" \
    "s_nop 0\n\t" \
    "s_nop 0\n\t" \
    "s_nop 0\n\t" \
    "s_nop 0\n\t" \
    "s_nop 0\n\t" \
    "s_nop 0\n\t" \
    "s_nop 0\n\t" \
    "s_nop 0\n\t" \
    "s_nop 0\n\t" \
    "s_nop 0\n\t" \
    "s_nop 0\n\t" \
    "s_nop 0\n\t" \
    "s_nop 0\n\t" \
    "s_nop 0\n\t" \
    "s_nop 0\n\t" \
    "s_nop 0\n\t" \
    "s_nop 0\n\t" \
    "s_nop 0\n\t" \
    "TEA_%=:\n\t" \
    "PB_%=:\n\t" \
    "s_waitcnt lgkmcnt(0)\n\t" \
    "s_barrier\n\t" \
    "buffer_load_dwordx4 v[220:223], v8, %[rs], %[m0] offen sc1\n\t" \
    "buffer_load_dwordx4 v[224:227], v8, %[rs], %[m1] offen sc1\n\t" \
    "PM_%=:\n\t" \
    "s_waitcnt vmcnt(1)\n\t" \
    "ds_write_b128 v8, v[220:223] offset:0\n\t" \
    "v_max3_u32 v252, v220, v221, v222\n\t" \
    "s_waitcnt vmcnt(0)\n\t" \
    "ds_write_b128 v8, v[224:227] offset:4096\n\t" \
    "v_max3_u32 v252, v252, v223, v224\n\t" \
    "v_max3_u32 v252, v252, v225, v226\n\t" \
    "v_max_u32 v252, v252, v227\n\t" \
    "v_cmp_eq_u32 vcc, -1, v252\n\t" \
    "s_cbranch_vccz PD_%=\n\t" \
    "s_sub_u32 %[lim], %[lim], 1\n\t" \
    "s_cmp_gt_i32 %[lim], 0\n\t" \
    "s_cbranch_scc0 PD_%=\n\t" \
    "s_sleep " STR(CADM) "\n\t" \
    "buffer_load_dwordx4 v[220:223], v8, %[rs], %[m0] offen sc1\n\t" \
    "buffer_load_dwordx4 v[224:227], v8, %[rs], %[m1] offen sc1\n\t" \
    "s_branch PM_%=\n\t" \
    ".p2align 6\n\t" \
    ".long 0xbf800000\n\t" \
    ".long 0xbf800000\n\t" \
    ".long 0xbf800000\n\t" \
    ".long 0xbf800000\n\t" \
    "PD_%=:\n\t" \
    "s_waitcnt lgkmcnt(0)\n\t" \
    "s_barrier\n\t" \
    "s_sleep 6\n\t" \
    "LV_%=:\n\t" \
    "buffer_load_dword v254, v17, %[rs], %[sb] offen sc1\n\t" \
    "buffer_load_dword v255, v17, %[rs], %[sb] offen offset:256 sc1\n\t" \
    "s_sub_u32 %[lim], %[lim], 1\n\t" \
    "s_cmp_gt_i32 %[lim], 0\n\t" \
    "s_waitcnt vmcnt(0)\n\t" \
    "ds_read_b32 v252, v16 offset:16\n\t" \
    "s_cbranch_scc0 LD_%=\n\t" \
    "v_max_u32 v7, v254, v255\n\t" \
    "v_cmp_eq_u32 s[88:89], -1, v7\n\t" \
    "s_waitcnt lgkmcnt(0)\n\t" \
    "v_cmp_ne_u32 vcc, v253, v252\n\t" \
    "s_nop 3\n\t" \
    "s_or_b64 vcc, vcc, s[88:89]\n\t" \
    "s_cbranch_scc1 LV_%=\n\t" \
    "LD_%=:\n\t" \
    "s_waitcnt lgkmcnt(0)\n\t" \
    "v_add_u32 v252, %[vd], v12\n\t" \
    "v_add_u32 v253, %[md], v17\n\t" \
    "ds_write_b128 v252, v[20:23]\n\t" \
    "ds_write_b128 v252, v[24:27] offset:1024\n\t" \
    "ds_write2st64_b32 v253, v254, v255 offset0:0 offset1:1\n\t" \
    "s_waitcnt lgkmcnt(0)\n\t" \
    "s_barrier\n\t" \
    : [lim] "+s"(lim) \
    : "{v8}"(am_a), "{v12}"(ab_a), "{v16}"(fr_a), "{v17}"(vo_v), [rs] "s"(rs), [sb] "s"(sb), [tag] "s"(0xffffff81u + s), [tgt] "s"(tgt), [sc] "s"(wid), [m0] "s"(sbm), [m1] "s"(sbm + 4096u), [vx] "s"(vxo), [vd] "s"(8192 + j * 2048), [md] "s"(md) \
    : "memory", "vcc", "scc", "s88", "s89", "s90", "s91", "s92", "s93", "s94", "s95", "v0", "v1", "v2", "v3", "v4", "v5", "v6", "v7", "v20", "v21", "v22", "v23", "v24", "v25", "v26", "v27", "v220", "v221", "v222", "v223", "v224", "v225", "v226", "v227", "v228", "v229", "v230", "v231", "v232", "v233", "v234", "v235", "v236", "v237", "v238", "v239", "v240", "v241", "v242", "v243", "v244", "v245", "v246", "v247", "v248", "v249", "v250", "v251", "v252", "v253", "v254", "v255")
#define A_MAIN_Q() \
  asm volatile( \
    "v_mov_b32 v253, %[tag]\n\t" \
    "buffer_load_dwordx4 v[20:23], v12, %[rs], %[vx] offen sc1\n\t" \
    "buffer_load_dwordx4 v[24:27], v12, %[rs], %[vx] offen offset:1024 sc1\n\t" \
    "ds_read_b128 v[236:239], v9 offset:0\n\t" \
    "ds_read_b128 v[240:243], v9 offset:1024\n\t" \
    "ds_read_b128 v[244:247], v9 offset:2048\n\t" \
    "ds_read_b128 v[248:251], v9 offset:3072\n\t" \
    "v_mov_b32 v0, 0\n\t" \
    "v_mov_b32 v1, 0\n\t" \
    "v_mov_b32 v2, 0\n\t" \
    "v_mov_b32 v3, 0\n\t" \
    "v_mov_b32 v4, 0\n\t" \
    "v_mov_b32 v5, 0\n\t" \
    "v_mov_b32 v6, 0\n\t" \
    "v_mov_b32 v7, 0\n\t" \
    "s_cmp_lg_u32 %[sc], 3\n\t" \
    "s_cbranch_scc1 PB_%=\n\t" \
    "s_getpc_b64 s[92:93]\n\t" \
    "TPA_%=:\n\t" \
    "s_add_u32 s92, s92, TEA_%=-TPA_%=\n\t" \
    "s_addc_u32 s93, s93, 0\n\t" \
    "s_lshr_b32 s91, %[tgt], 2\n\t" \
    "s_sub_u32 s91, s91, 3\n\t" \
    "TWA_%=:\n\t" \
    "s_memrealtime s[88:89]\n\t" \
    DGE \
    "s_waitcnt lgkmcnt(0)\n\t" \
    "s_sub_u32 s89, s91, s88\n\t" \
    "s_lshl_b32 s89, s89, 2\n\t" \
    "s_cmp_gt_i32 s89, 0\n\t" \
    "s_cbranch_scc1 TWA_%=\n\t" \
    "s_lshl_b32 s88, s88, 2\n\t" \
    "s_sub_u32 s89, %[tgt], s88\n\t" \
    "s_max_i32 s89, s89, 0\n\t" \
    "s_mul_i32 s89, s89, 11\n\t" \
    "s_lshr_b32 s89, s89, 1\n\t" \
    DGB \
    "s_and_b32 s89, s89, 0x7c\n\t" \
    "s_sub_u32 s92, s92, s89\n\t" \
    "s_subb_u32 s93, s93, 0\n\t" \
    "s_setpc_b64 s[92:93]\n\t" \
    "s_nop 0\n\t" \
    "s_nop 0\n\t" \
    "s_nop 0\n\t" \
    "s_nop 0\n\t" \
    "s_nop 0\n\t" \
    "s_nop 0\n\t" \
    "s_nop 0\n\t" \
    "s_nop 0\n\t" \
    "s_nop 0\n\t" \
    "s_nop 0\n\t" \
    "s_nop 0\n\t" \
    "s_nop 0\n\t" \
    "s_nop 0\n\t" \
    "s_nop 0\n\t" \
    "s_nop 0\n\t" \
    "s_nop 0\n\t" \
    "s_nop 0\n\t" \
    "s_nop 0\n\t" \
    "s_nop 0\n\t" \
    "s_nop 0\n\t" \
    "s_nop 0\n\t" \
    "s_nop 0\n\t" \
    "TEA_%=:\n\t" \
    "PB_%=:\n\t" \
    "s_waitcnt lgkmcnt(0)\n\t" \
    "s_barrier\n\t" \
    "buffer_load_dwordx4 v[220:223], v8, %[rs], %[m0] offen sc1\n\t" \
    "buffer_load_dwordx4 v[224:227], v8, %[rs], %[m1] offen sc1\n\t" \
    "PM_%=:\n\t" \
    "s_waitcnt vmcnt(1)\n\t" \
    "ds_write_b128 v8, v[220:223] offset:0\n\t" \
    "v_max3_u32 v252, v220, v221, v222\n\t" \
    "s_waitcnt vmcnt(0)\n\t" \
    "ds_write_b128 v8, v[224:227] offset:4096\n\t" \
    "v_max3_u32 v252, v252, v223, v224\n\t" \
    "v_max3_u32 v252, v252, v225, v226\n\t" \
    "v_max_u32 v252, v252, v227\n\t" \
    "v_cmp_eq_u32 vcc, -1, v252\n\t" \
    "s_cbranch_vccz PD_%=\n\t" \
    "s_sub_u32 %[lim], %[lim], 1\n\t" \
    "s_cmp_gt_i32 %[lim], 0\n\t" \
    "s_cbranch_scc0 PD_%=\n\t" \
    "s_sleep " STR(CADM) "\n\t" \
    "buffer_load_dwordx4 v[220:223], v8, %[rs], %[m0] offen sc1\n\t" \
    "buffer_load_dwordx4 v[224:227], v8, %[rs], %[m1] offen sc1\n\t" \
    "s_branch PM_%=\n\t" \
    ".p2align 6\n\t" \
    ".long 0xbf800000\n\t" \
    ".long 0xbf800000\n\t" \
    ".long 0xbf800000\n\t" \
    ".long 0xbf800000\n\t" \
    "PD_%=:\n\t" \
    GSA(0) \
    "v_smfmac_f32_16x16x64_bf16 v[0:3], v[220:223], v[236:243], v10\n\t" \
    "v_xor_b32 v9, 16384, v9\n\t" \
    "ds_read_b128 v[236:239], v9 offset:0\n\t" \
    "ds_read_b128 v[240:243], v9 offset:1024\n\t" \
    "v_smfmac_f32_16x16x64_bf16 v[4:7], v[220:223], v[244:251], v10\n\t" \
    "ds_read_b128 v[244:247], v9 offset:2048\n\t" \
    "ds_read_b128 v[248:251], v9 offset:3072\n\t" \
    "s_waitcnt lgkmcnt(4)\n\t" \
    "s_barrier\n\t" \
    "ds_read_b128 v[228:231], v8 offset:8192\n\t" \
    "ds_read_b128 v[232:235], v8 offset:12288\n\t" \
    "v_xor_b32 v8, 1024, v8\n\t" \
    "ds_read_b128 v[220:223], v8 offset:4096\n\t" \
    "v_smfmac_f32_16x16x64_bf16 v[0:3], v[224:227], v[28:35], v10\n\t" \
    "v_smfmac_f32_16x16x64_bf16 v[4:7], v[224:227], v[36:43], v10\n\t" \
    "ds_read_b128 v[224:227], v8 offset:0\n\t" \
    "s_waitcnt lgkmcnt(3)\n\t" \
    "v_smfmac_f32_16x16x64_bf16 v[0:3], v[228:231], v[92:99], v10\n\t" \
    "v_smfmac_f32_16x16x64_bf16 v[4:7], v[228:231], v[100:107], v10\n\t" \
    "ds_read_b128 v[228:231], v8 offset:8192\n\t" \
    "s_waitcnt lgkmcnt(3)\n\t" \
    "v_smfmac_f32_16x16x64_bf16 v[0:3], v[232:235], v[156:163], v10\n\t" \
    "v_smfmac_f32_16x16x64_bf16 v[4:7], v[232:235], v[164:171], v10\n\t" \
    "ds_read_b128 v[232:235], v8 offset:12288\n\t" \
    "s_waitcnt lgkmcnt(3)\n\t" \
    "v_smfmac_f32_16x16x64_bf16 v[0:3], v[220:223], v[44:51], v10\n\t" \
    "v_smfmac_f32_16x16x64_bf16 v[4:7], v[220:223], v[52:59], v10\n\t" \
    "v_xor_b32 v8, 3072, v8\n\t" \
    "ds_read_b128 v[220:223], v8 offset:4096\n\t" \
    "s_waitcnt lgkmcnt(3)\n\t" \
    "v_smfmac_f32_16x16x64_bf16 v[0:3], v[224:227], v[236:243], v10\n\t" \
    "v_xor_b32 v9, 49152, v9\n\t" \
    "ds_read_b128 v[236:239], v9 offset:0\n\t" \
    "v_smfmac_f32_16x16x64_bf16 v[4:7], v[224:227], v[244:251], v10\n\t" \
    "ds_read_b128 v[240:243], v9 offset:1024\n\t" \
    "s_waitcnt lgkmcnt(4)\n\t" \
    "v_smfmac_f32_16x16x64_bf16 v[0:3], v[228:231], v[108:115], v10\n\t" \
    "ds_read_b128 v[224:227], v8 offset:8192\n\t" \
    "v_smfmac_f32_16x16x64_bf16 v[4:7], v[228:231], v[116:123], v10\n\t" \
    "ds_read_b128 v[244:247], v9 offset:2048\n\t" \
    "s_waitcnt lgkmcnt(5)\n\t" \
    "v_smfmac_f32_16x16x64_bf16 v[0:3], v[232:235], v[172:179], v10\n\t" \
    "ds_read_b128 v[228:231], v8 offset:0\n\t" \
    "v_smfmac_f32_16x16x64_bf16 v[4:7], v[232:235], v[180:187], v10\n\t" \
    "ds_read_b128 v[248:251], v9 offset:3072\n\t" \
    "s_waitcnt lgkmcnt(6)\n\t" \
    "v_smfmac_f32_16x16x64_bf16 v[0:3], v[220:223], v[60:67], v10\n\t" \
    "ds_read_b128 v[232:235], v8 offset:12288\n\t" \
    "v_smfmac_f32_16x16x64_bf16 v[4:7], v[220:223], v[68:75], v10\n\t" \
    "v_xor_b32 v8, 1024, v8\n\t" \
    "ds_read_b128 v[220:223], v8 offset:4096\n\t" \
    "s_waitcnt lgkmcnt(5)\n\t" \
    "v_smfmac_f32_16x16x64_bf16 v[0:3], v[224:227], v[124:131], v10\n\t" \
    "v_smfmac_f32_16x16x64_bf16 v[4:7], v[224:227], v[132:139], v10\n\t" \
    "ds_read_b128 v[224:227], v8 offset:8192\n\t" \
    "s_waitcnt lgkmcnt(3)\n\t" \
    "v_smfmac_f32_16x16x64_bf16 v[0:3], v[228:231], v[236:243], v10\n\t" \
    "v_xor_b32 v9, 16384, v9\n\t" \
    "ds_read_b128 v[236:239], v9 offset:0\n\t" \
    "v_smfmac_f32_16x16x64_bf16 v[4:7], v[228:231], v[244:251], v10\n\t" \
    "ds_read_b128 v[240:243], v9 offset:1024\n\t" \
    "s_waitcnt lgkmcnt(4)\n\t" \
    "v_smfmac_f32_16x16x64_bf16 v[0:3], v[232:235], v[188:195], v10\n\t" \
    "ds_read_b128 v[228:231], v8 offset:12288\n\t" \
    "v_smfmac_f32_16x16x64_bf16 v[4:7], v[232:235], v[196:203], v10\n\t" \
    "ds_read_b128 v[244:247], v9 offset:2048\n\t" \
    "s_waitcnt lgkmcnt(5)\n\t" \
    "v_smfmac_f32_16x16x64_bf16 v[0:3], v[220:223], v[76:83], v10\n\t" \
    "ds_read_b128 v[232:235], v8 offset:0\n\t" \
    "v_xor_b32 v8, 3072, v8\n\t" \
    "v_smfmac_f32_16x16x64_bf16 v[4:7], v[220:223], v[84:91], v10\n\t" \
    "ds_read_b128 v[248:251], v9 offset:3072\n\t" \
    "s_waitcnt lgkmcnt(6)\n\t" \
    "v_smfmac_f32_16x16x64_bf16 v[0:3], v[224:227], v[140:147], v10\n\t" \
    "ds_read_b128 v[220:223], v12 offset:24576\n\t" \
    "v_smfmac_f32_16x16x64_bf16 v[4:7], v[224:227], v[148:155], v10\n\t" \
    "ds_read_b128 v[224:227], v12 offset:28672\n\t" \
    "s_waitcnt lgkmcnt(5)\n\t" \
    "v_smfmac_f32_16x16x64_bf16 v[0:3], v[228:231], v[204:211], v10\n\t" \
    "v_smfmac_f32_16x16x64_bf16 v[4:7], v[228:231], v[212:219], v10\n\t" \
    "ds_read_b128 v[228:231], v12 offset:25600\n\t" \
    "s_waitcnt lgkmcnt(3)\n\t" \
    "v_smfmac_f32_16x16x64_bf16 v[0:3], v[232:235], v[236:243], v10\n\t" \
    "v_xor_b32 v9, 49152, v9\n\t" \
    "v_smfmac_f32_16x16x64_bf16 v[4:7], v[232:235], v[244:251], v10\n\t" \
    VLD(4) \
    "s_nop 5\n\t" \
    "v_add_f32 v0, v0, v1\n\t" \
    "v_add_f32 v2, v2, v3\n\t" \
    "v_add_f32 v4, v4, v5\n\t" \
    "v_add_f32 v6, v6, v7\n\t" \
    "v_mul_f32 v0, %[sl], v0\n\t" \
    "v_mul_f32 v2, %[sl], v2\n\t" \
    "v_mul_f32 v4, %[sl], v4\n\t" \
    "v_mul_f32 v6, %[sl], v6\n\t" \
    "v_cvt_pk_bf16_f32 v0, v0, v4\n\t" \
    "v_cvt_pk_bf16_f32 v2, v2, v6\n\t" \
    "ds_write2_b32 v11, v0, v2 offset0:0 offset1:4\n\t" \
    "s_mov_b64 exec, %[onej]\n\t" \
    "ds_write_b32 v16, v253\n\t" \
    "s_mov_b64 exec, -1\n\t" \
    GSA(1) \
    "s_setprio 0\n\t" \
    "ds_read_b128 v[232:235], v12 offset:29696\n\t" \
    "s_nop 2\n\t" \
    "ds_read_b128 v[0:3], v12 offset:26624\n\t" \
    "s_nop 2\n\t" \
    "ds_read_b128 v[4:7], v12 offset:27648\n\t" \
    "s_nop 2\n\t" \
    "ds_read_b32 v252, v16\n\t" \
    "ds_read_b128 v[236:239], v14 offset:0\n\t" \
    "s_nop 2\n\t" \
    "s_setprio 2\n\t" \
    VLD(3) \
    "s_setprio 0\n\t" \
    "ds_read_b128 v[240:243], v14 offset:512\n\t" \
    "s_nop 2\n\t" \
    "ds_read_b128 v[244:247], v14 offset:1024\n\t" \
    "s_nop 2\n\t" \
    "ds_read_b128 v[248:251], v14 offset:1536\n\t" \
    "s_nop 2\n\t" \
    "s_setprio 2\n\t" \
    "s_waitcnt lgkmcnt(4)\n\t" \
    "v_cmp_ne_u32 vcc, v253, v252\n\t" \
    "s_cbranch_vccnz RP_%=\n\t" \
    "CP_%=:\n\t" \
    "s_setprio 2\n\t" \
    VLD(0) \
    "s_waitcnt lgkmcnt(2)\n\t" \
    "v_mfma_f32_16x16x32_bf16 v[220:223], v[220:223], v[236:239], 0\n\t" \
    "v_mfma_f32_16x16x32_bf16 v[224:227], v[224:227], v[236:239], 0\n\t" \
    "v_mfma_f32_16x16x32_bf16 v[220:223], v[228:231], v[240:243], v[220:223]\n\t" \
    "ds_read_b128 v[228:231], v12 offset:30720\n\t" \
    "v_mfma_f32_16x16x32_bf16 v[224:227], v[232:235], v[240:243], v[224:227]\n\t" \
    "ds_read_b128 v[232:235], v12 offset:31744\n\t" \
    "s_setprio " STR(QPRIO) "\n\t" \
    VLD(1) \
    "s_waitcnt lgkmcnt(2)\n\t" \
    "v_mfma_f32_16x16x32_bf16 v[220:223], v[0:3], v[244:247], v[220:223]\n\t" \
    "v_mfma_f32_16x16x32_bf16 v[220:223], v[4:7], v[248:251], v[220:223]\n\t" \
    "s_waitcnt lgkmcnt(1)\n\t" \
    "v_mfma_f32_16x16x32_bf16 v[224:227], v[228:231], v[244:247], v[224:227]\n\t" \
    "s_waitcnt lgkmcnt(0)\n\t" \
    "v_mfma_f32_16x16x32_bf16 v[224:227], v[232:235], v[248:251], v[224:227]\n\t" \
    GSA(2) \
    "s_setprio 2\n\t" \
    "s_nop 2\n\t" \
    "v_exp_f32 v220, v220\n\t" \
    "v_exp_f32 v221, v221\n\t" \
    "v_exp_f32 v222, v222\n\t" \
    "v_exp_f32 v223, v223\n\t" \
    "v_exp_f32 v224, v224\n\t" \
    "v_exp_f32 v225, v225\n\t" \
    "v_exp_f32 v226, v226\n\t" \
    "v_exp_f32 v227, v227\n\t" \
    "v_cvt_pk_bf16_f32 v220, v220, v221\n\t" \
    "v_cvt_pk_bf16_f32 v221, v222, v223\n\t" \
    "ds_read_b32 v252, v16 offset:16\n\t" \
    "ds_read_b128 v[0:3], v12 offset:22528\n\t" \
    "ds_read_b128 v[4:7], v12 offset:23552\n\t" \
    VLD(2) \
    "v_cvt_pk_bf16_f32 v222, v224, v225\n\t" \
    "v_cvt_pk_bf16_f32 v223, v226, v227\n\t" \
    "v_mov_b32 v224, 0\n\t" \
    "v_mov_b32 v225, 0\n\t" \
    "v_mfma_f32_16x16x32_bf16 v[228:231], v[20:23], v[220:223], 0\n\t" \
    "v_mov_b32 v226, 0\n\t" \
    "v_mov_b32 v227, 0\n\t" \
    "v_mfma_f32_16x16x32_bf16 v[232:235], v[24:27], v[220:223], 0\n\t" \
    "s_waitcnt lgkmcnt(2)\n\t" \
    "v_cmp_ne_u32 vcc, %[tag], v252\n\t" \
    "s_cbranch_vccnz RK_%=\n\t" \
    "CK_%=:\n\t" \
    "s_waitcnt lgkmcnt(1)\n\t" \
    "v_smfmac_f32_16x16x64_bf16 v[224:227], v[0:3], v[236:243], v10\n\t" \
    "s_waitcnt lgkmcnt(0)\n\t" \
    "v_smfmac_f32_16x16x64_bf16 v[224:227], v[4:7], v[244:251], v10\n\t" \
    "v_mov_b32 v236, %[one]\n\t" \
    "v_mov_b32 v237, %[one]\n\t" \
    "v_mov_b32 v238, %[one]\n\t" \
    "v_mov_b32 v239, %[one]\n\t" \
    "v_mov_b32 v1, 0\n\t" \
    "v_mov_b32 v2, 0\n\t" \
    "v_mfma_f32_16x16x32_bf16 v[240:243], v[236:239], v[220:223], 0\n\t" \
    "s_nop 0\n\t" \
    "v_add_f32 v4, v224, v225\n\t" \
    "v_add_f32 v5, v226, v227\n\t" \
    "v_exp_f32 v4, v4\n\t" \
    "v_exp_f32 v5, v5\n\t" \
    "s_nop 0\n\t" \
    "v_cvt_pk_bf16_f32 v0, v4, v5\n\t" \
    "v_cvt_pk_bf16_f32 v3, v4, v5\n\t" \
    GSA(3) \
    "CM_%=:\n\t" \
    "s_nop 0\n\t" \
    "v_mfma_f32_16x16x16_bf16 v[240:243], v[236:237], v[0:1], v[240:243]\n\t" \
    "s_waitcnt vmcnt(0)\n\t" \
    "v_mfma_f32_16x16x16_bf16 v[228:231], v[254:255], v[0:1], v[228:231]\n\t" \
    "v_max_u32 v7, v254, v255\n\t" \
    "v_mfma_f32_16x16x16_bf16 v[232:235], v[254:255], v[2:3], v[232:235]\n\t" \
    "v_cmp_eq_u32 s[88:89], -1, v7\n\t" \
    "s_not_b64 exec, %[hi]\n\t" \
    "s_nop 1\n\t" \
    "v_rcp_f32 v252, v240\n\t" \
    "v_subrev_u32 v253, 0x21800000, v240\n\t" \
    "v_cmp_gt_u32 vcc, v253, %[rg]\n\t" \
    "v_pk_mul_f32 v[228:229], v[228:229], v[252:253] op_sel_hi:[1,0]\n\t" \
    "v_pk_mul_f32 v[230:231], v[230:231], v[252:253] op_sel_hi:[1,0]\n\t" \
    "v_pk_mul_f32 v[232:233], v[232:233], v[252:253] op_sel_hi:[1,0]\n\t" \
    "v_pk_mul_f32 v[234:235], v[234:235], v[252:253] op_sel_hi:[1,0]\n\t" \
    "s_or_b64 vcc, vcc, s[88:89]\n\t" \
    "v_cvt_pk_bf16_f32 v4, v228, v229\n\t" \
    "v_cvt_pk_bf16_f32 v5, v230, v231\n\t" \
    "v_cvt_pk_bf16_f32 v6, v232, v233\n\t" \
    "v_cvt_pk_bf16_f32 v7, v234, v235\n\t" \
    "s_cbranch_scc1 FX_%=\n\t" \
    "buffer_store_dwordx4 v[4:7], v18, %[rs], %[sb] offen sc1\n\t" \
    "s_memrealtime %[tp]\n\t" \
    DGA \
    GSA(4) \
    "v_max3_u32 v252, v4, v5, v6\n\t" \
    "v_max_u32 v252, v252, v7\n\t" \
    "v_cmp_eq_u32 vcc, -1, v252\n\t" \
    "s_cbranch_vccnz RC_%=\n\t" \
    "CC_%=:\n\t" \
    "s_mov_b64 exec, -1\n\t" \
    "v_add_u32 v252, %[vd], v12\n\t" \
    "v_add_u32 v253, %[md], v17\n\t" \
    "ds_write_b128 v252, v[20:23]\n\t" \
    "ds_write_b128 v252, v[24:27] offset:1024\n\t" \
    "ds_write2st64_b32 v253, v254, v255 offset0:0 offset1:1\n\t" \
    "s_waitcnt lgkmcnt(0)\n\t" \
    "s_cmp_lg_u32 %[sc], 3\n\t" \
    "s_cbranch_scc1 NH_%=\n\t" \
    "buffer_load_dwordx2 v[0:1], off, %[rs], %[fl] sc1\n\t" \
    "s_barrier\n\t" \
    "s_waitcnt vmcnt(0)\n\t" \
    "v_readfirstlane_b32 %[pt], v0\n\t" \
    "v_readfirstlane_b32 %[pd], v1\n\t" \
    "s_nop 3\n\t" \
    "s_branch E_%=\n\t" \
    "NH_%=:\n\t" \
    "s_barrier\n\t" \
    "s_branch E_%=\n\t" \
    "RP_%=:\n\t" \
    "s_waitcnt lgkmcnt(0)\n\t" \
    "ds_read_b32 v252, v16\n\t" \
    "ds_read_b128 v[236:239], v14 offset:0\n\t" \
    "ds_read_b128 v[240:243], v14 offset:512\n\t" \
    "ds_read_b128 v[244:247], v14 offset:1024\n\t" \
    "ds_read_b128 v[248:251], v14 offset:1536\n\t" \
    "s_sub_u32 %[lim], %[lim], " STR(DEC1) "\n\t" \
    "s_cmp_gt_i32 %[lim], 0\n\t" \
    "s_waitcnt lgkmcnt(4)\n\t" \
    "s_cbranch_scc0 CP_%=\n\t" \
    "v_cmp_ne_u32 vcc, %[tag], v252\n\t" \
    "s_cbranch_vccnz RP_%=\n\t" \
    "s_branch CP_%=\n\t" \
    "RK_%=:\n\t" \
    "s_waitcnt lgkmcnt(0)\n\t" \
    "ds_read_b32 v252, v16 offset:16\n\t" \
    "ds_read_b128 v[0:3], v12 offset:22528\n\t" \
    "ds_read_b128 v[4:7], v12 offset:23552\n\t" \
    "s_sub_u32 %[lim], %[lim], " STR(DEC1) "\n\t" \
    "s_cmp_gt_i32 %[lim], 0\n\t" \
    "s_waitcnt lgkmcnt(2)\n\t" \
    "s_cbranch_scc0 CK_%=\n\t" \
    "v_cmp_ne_u32 vcc, %[tag], v252\n\t" \
    "s_cbranch_vccnz RK_%=\n\t" \
    "s_branch CK_%=\n\t" \
    "RC_%=:\n\t" \
    "v_min_u32 v4, -2, v4\n\t" \
    "v_min_u32 v5, -2, v5\n\t" \
    "v_min_u32 v6, -2, v6\n\t" \
    "v_min_u32 v7, -2, v7\n\t" \
    "buffer_store_dwordx4 v[4:7], v18, %[rs], %[sb] offen sc1\n\t" \
    "s_branch CC_%=\n\t" \
    "FX_%=:\n\t" \
    "s_mov_b64 exec, -1\n\t" \
    "s_cmp_eq_u64 s[88:89], 0\n\t" \
    "s_cbranch_scc1 RM_%=\n\t" \
    "buffer_load_dword v254, v17, %[rs], %[sb] offen sc1\n\t" \
    "buffer_load_dword v255, v17, %[rs], %[sb] offen offset:256 sc1\n\t" \
    "v_mfma_f32_16x16x32_bf16 v[228:231], v[20:23], v[220:223], 0\n\t" \
    "v_mfma_f32_16x16x32_bf16 v[232:235], v[24:27], v[220:223], 0\n\t" \
    "v_mfma_f32_16x16x32_bf16 v[240:243], v[236:239], v[220:223], 0\n\t" \
    "s_sub_u32 %[lim], %[lim], " STR(DEC2) "\n\t" \
    "s_cmp_gt_i32 %[lim], 0\n\t" \
    "s_cbranch_scc1 CM_%=\n\t" \
    "s_waitcnt vmcnt(0)\n\t" \
    "v_mov_b32 v254, 0\n\t" \
    "v_mov_b32 v255, 0\n\t" \
    "s_branch CM_%=\n\t" \
    "RM_%=:\n\t" \
    "s_mov_b32 %[rg], -1\n\t" \
    "s_nop 7\n\t" \
    "v_add_f32 v4, v224, v225\n\t" \
    "v_add_f32 v5, v226, v227\n\t" \
    "ds_read_b128 v[236:239], v14 offset:0\n\t" \
    "ds_read_b128 v[240:243], v14 offset:512\n\t" \
    "ds_read_b128 v[244:247], v14 offset:1024\n\t" \
    "ds_read_b128 v[248:251], v14 offset:1536\n\t" \
    "ds_read_b128 v[232:235], v12 offset:24576\n\t" \
    "s_waitcnt lgkmcnt(0)\n\t" \
    "v_mfma_f32_16x16x32_bf16 v[220:223], v[232:235], v[236:239], 0\n\t" \
    "ds_read_b128 v[232:235], v12 offset:25600\n\t" \
    "s_waitcnt lgkmcnt(0)\n\t" \
    "v_mfma_f32_16x16x32_bf16 v[220:223], v[232:235], v[240:243], v[220:223]\n\t" \
    "ds_read_b128 v[232:235], v12 offset:26624\n\t" \
    "s_waitcnt lgkmcnt(0)\n\t" \
    "v_mfma_f32_16x16x32_bf16 v[220:223], v[232:235], v[244:247], v[220:223]\n\t" \
    "ds_read_b128 v[232:235], v12 offset:27648\n\t" \
    "s_waitcnt lgkmcnt(0)\n\t" \
    "v_mfma_f32_16x16x32_bf16 v[220:223], v[232:235], v[248:251], v[220:223]\n\t" \
    "ds_read_b128 v[232:235], v12 offset:28672\n\t" \
    "s_waitcnt lgkmcnt(0)\n\t" \
    "v_mfma_f32_16x16x32_bf16 v[224:227], v[232:235], v[236:239], 0\n\t" \
    "ds_read_b128 v[232:235], v12 offset:29696\n\t" \
    "s_waitcnt lgkmcnt(0)\n\t" \
    "v_mfma_f32_16x16x32_bf16 v[224:227], v[232:235], v[240:243], v[224:227]\n\t" \
    "ds_read_b128 v[232:235], v12 offset:30720\n\t" \
    "s_waitcnt lgkmcnt(0)\n\t" \
    "v_mfma_f32_16x16x32_bf16 v[224:227], v[232:235], v[244:247], v[224:227]\n\t" \
    "ds_read_b128 v[232:235], v12 offset:31744\n\t" \
    "s_waitcnt lgkmcnt(0)\n\t" \
    "v_mfma_f32_16x16x32_bf16 v[224:227], v[232:235], v[248:251], v[224:227]\n\t" \
    "v_max3_f32 v252, v220, v221, v222\n\t" \
    "s_nop 6\n\t" \
    "v_max3_f32 v253, v223, v224, v225\n\t" \
    "v_max3_f32 v252, v252, v226, v227\n\t" \
    "v_max3_f32 v253, v253, v4, v5\n\t" \
    "v_max_f32 v252, v252, v253\n\t" \
    "v_mov_b32 v253, v252\n\t" \
    "v_mov_b32 v6, v252\n\t" \
    "s_nop 1\n\t" \
    "v_permlane16_swap_b32 v253, v6\n\t" \
    "v_max_f32 v252, v253, v6\n\t" \
    "v_max_f32 v253, v253, v6\n\t" \
    "s_nop 1\n\t" \
    "v_permlane32_swap_b32 v252, v253\n\t" \
    "v_max_f32 v252, v252, v253\n\t" \
    "v_sub_f32 v220, v220, v252\n\t" \
    "v_sub_f32 v221, v221, v252\n\t" \
    "v_sub_f32 v222, v222, v252\n\t" \
    "v_sub_f32 v223, v223, v252\n\t" \
    "v_sub_f32 v224, v224, v252\n\t" \
    "v_sub_f32 v225, v225, v252\n\t" \
    "v_sub_f32 v226, v226, v252\n\t" \
    "v_sub_f32 v227, v227, v252\n\t" \
    "v_sub_f32 v4, v4, v252\n\t" \
    "v_sub_f32 v5, v5, v252\n\t" \
    "v_exp_f32 v220, v220\n\t" \
    "v_exp_f32 v221, v221\n\t" \
    "v_exp_f32 v222, v222\n\t" \
    "v_exp_f32 v223, v223\n\t" \
    "v_exp_f32 v224, v224\n\t" \
    "v_exp_f32 v225, v225\n\t" \
    "v_exp_f32 v226, v226\n\t" \
    "v_exp_f32 v227, v227\n\t" \
    "v_cvt_pk_bf16_f32 v220, v220, v221\n\t" \
    "v_cvt_pk_bf16_f32 v221, v222, v223\n\t" \
    "v_cvt_pk_bf16_f32 v222, v224, v225\n\t" \
    "v_cvt_pk_bf16_f32 v223, v226, v227\n\t" \
    "v_mov_b32 v236, %[one]\n\t" \
    "v_mov_b32 v237, %[one]\n\t" \
    "v_mov_b32 v238, %[one]\n\t" \
    "v_mov_b32 v239, %[one]\n\t" \
    "v_mfma_f32_16x16x32_bf16 v[228:231], v[20:23], v[220:223], 0\n\t" \
    "v_mfma_f32_16x16x32_bf16 v[232:235], v[24:27], v[220:223], 0\n\t" \
    "v_mfma_f32_16x16x32_bf16 v[240:243], v[236:239], v[220:223], 0\n\t" \
    "v_exp_f32 v4, v4\n\t" \
    "v_exp_f32 v5, v5\n\t" \
    "s_nop 0\n\t" \
    "v_cvt_pk_bf16_f32 v0, v4, v5\n\t" \
    "v_cvt_pk_bf16_f32 v3, v4, v5\n\t" \
    "s_nop 1\n\t" \
    "s_branch CM_%=\n\t" \
    "E_%=:\n\t" \
    GSW \
    "s_nop 1\n\t" \
    : [lim] "+s"(lim), [rg] "+s"(rg), [tp] "=&s"(tp), [pt] "=&s"(pt), [pd] "=&s"(pd) DGO DGP GSO(0) GSO(1) GSO(2) GSO(3) GSO(4) \
    : "{v8}"(am_a), "{v9}"(t0_a), "{v10}"(idx), "{v11}"(iw_a), "{v12}"(ab_a), "{v14}"(qr_a), "{v16}"(fr_a), "{v17}"(vo_v), "{v18}"(oo_v), [rs] "s"(rs), [sb] "s"(sb), [tag] "s"(0xffffff81u + s), [hi] "s"(0xff00ff00ff00ff00ull), [tgt] "s"(tgt), [sc] "s"(wid), [onej] "s"(onej), [sl] "s"(0x3e0293ee), [one] "s"(0x3f803f80), [m0] "s"(sbm), [m1] "s"(sbm + 4096u), [vx] "s"(vxo), [fl] "s"(fb + FL_T(s + 1)), [vd] "s"(8192 + j * 2048), [md] "s"(md), "{v[28:35]}"(wr[0][0]), "{v[36:43]}"(wr[0][1]), "{v[44:51]}"(wr[1][0]), "{v[52:59]}"(wr[1][1]), "{v[60:67]}"(wr[2][0]), "{v[68:75]}"(wr[2][1]), "{v[76:83]}"(wr[3][0]), "{v[84:91]}"(wr[3][1]), "{v[92:99]}"(wr[4][0]), "{v[100:107]}"(wr[4][1]), "{v[108:115]}"(wr[5][0]), "{v[116:123]}"(wr[5][1]), "{v[124:131]}"(wr[6][0]), "{v[132:139]}"(wr[6][1]), "{v[140:147]}"(wr[7][0]), "{v[148:155]}"(wr[7][1]), "{v[156:163]}"(wr[8][0]), "{v[164:171]}"(wr[8][1]), "{v[172:179]}"(wr[9][0]), "{v[180:187]}"(wr[9][1]), "{v[188:195]}"(wr[10][0]), "{v[196:203]}"(wr[10][1]), "{v[204:211]}"(wr[11][0]), "{v[212:219]}"(wr[11][1]) \
    : "memory", "vcc", "scc", "s88", "s89", "s90", "s91", "s92", "s93", "s94", "s95", "v0", "v1", "v2", "v3", "v4", "v5", "v6", "v7", "v20", "v21", "v22", "v23", "v24", "v25", "v26", "v27", "v220", "v221", "v222", "v223", "v224", "v225", "v226", "v227", "v228", "v229", "v230", "v231", "v232", "v233", "v234", "v235", "v236", "v237", "v238", "v239", "v240", "v241", "v242", "v243", "v244", "v245", "v246", "v247", "v248", "v249", "v250", "v251", "v252", "v253", "v254", "v255")

#define A_MAIN_K() \
  asm volatile( \
    "ds_read_b128 v[236:239], v9 offset:0\n\t" \
    "ds_read_b128 v[240:243], v9 offset:1024\n\t" \
    "ds_read_b128 v[244:247], v9 offset:2048\n\t" \
    "ds_read_b128 v[248:251], v9 offset:3072\n\t" \
    "v_mov_b32 v0, 0\n\t" \
    "v_mov_b32 v1, 0\n\t" \
    "v_mov_b32 v2, 0\n\t" \
    "v_mov_b32 v3, 0\n\t" \
    "v_mov_b32 v4, 0\n\t" \
    "v_mov_b32 v5, 0\n\t" \
    "v_mov_b32 v6, 0\n\t" \
    "v_mov_b32 v7, 0\n\t" \
    "s_waitcnt lgkmcnt(0)\n\t" \
    "s_barrier\n\t" \
    "buffer_load_dwordx4 v[212:215], v8, %[rs], %[m0] offen sc1\n\t" \
    "buffer_load_dwordx4 v[216:219], v8, %[rs], %[m1] offen sc1\n\t" \
    DGA \
    "v_mov_b32 v252, v252\n\t" \
    "s_nop 2\n\t" \
    "v_mov_b32 v252, v252\n\t" \
    "s_nop 2\n\t" \
    "v_mov_b32 v252, v252\n\t" \
    "s_nop 2\n\t" \
    "buffer_store_dwordx4 v[220:223], v14, %[rs], %[ox] offen offset:0 sc1\n\t" \
    "buffer_store_dwordx4 v[224:227], v14, %[rs], %[ox] offen offset:512 sc1\n\t" \
    GSA(3) \
    "v_mov_b32 v252, v252\n\t" \
    "s_nop 2\n\t" \
    "v_mov_b32 v252, v252\n\t" \
    "s_nop 2\n\t" \
    "v_mov_b32 v252, v252\n\t" \
    "s_nop 2\n\t" \
    "v_mov_b32 v252, v252\n\t" \
    "s_nop 2\n\t" \
    "v_mov_b32 v252, v252\n\t" \
    "s_nop 2\n\t" \
    "v_mov_b32 v252, v252\n\t" \
    "s_nop 2\n\t" \
    "v_mov_b32 v252, v252\n\t" \
    "s_nop 2\n\t" \
    "v_mov_b32 v252, v252\n\t" \
    "s_nop 2\n\t" \
    "v_mov_b32 v252, v252\n\t" \
    "s_nop 2\n\t" \
    "v_mov_b32 v252, v252\n\t" \
    "s_nop 2\n\t" \
    "v_mov_b32 v252, v252\n\t" \
    "s_nop 2\n\t" \
    "v_mov_b32 v252, v252\n\t" \
    "s_nop 2\n\t" \
    "v_mov_b32 v252, v252\n\t" \
    "s_nop 2\n\t" \
    "v_mov_b32 v252, v252\n\t" \
    "s_nop 2\n\t" \
    "v_mov_b32 v252, v252\n\t" \
    "s_nop 2\n\t" \
    "v_mov_b32 v252, v252\n\t" \
    "s_nop 2\n\t" \
    "v_mov_b32 v252, v252\n\t" \
    "s_nop 2\n\t" \
    "v_mov_b32 v252, v252\n\t" \
    "s_nop 2\n\t" \
    "v_mov_b32 v252, v252\n\t" \
    "s_nop 2\n\t" \
    "v_mov_b32 v252, v252\n\t" \
    "s_nop 2\n\t" \
    "v_mov_b32 v252, v252\n\t" \
    "s_nop 2\n\t" \
    "v_mov_b32 v252, v252\n\t" \
    "s_nop 2\n\t" \
    "v_mov_b32 v252, v252\n\t" \
    "s_nop 2\n\t" \
    "v_mov_b32 v252, v252\n\t" \
    "s_nop 2\n\t" \
    "PM_%=:\n\t" \
    "s_waitcnt vmcnt(3)\n\t" \
    "ds_write_b128 v8, v[212:215] offset:8192\n\t" \
    "v_max3_u32 v252, v212, v213, v214\n\t" \
    "s_waitcnt vmcnt(2)\n\t" \
    "ds_write_b128 v8, v[216:219] offset:12288\n\t" \
    "v_max3_u32 v252, v252, v215, v216\n\t" \
    "v_max3_u32 v252, v252, v217, v218\n\t" \
    "v_max_u32 v252, v252, v219\n\t" \
    "v_cmp_eq_u32 vcc, -1, v252\n\t" \
    "s_cbranch_vccz PD_%=\n\t" \
    "s_sub_u32 %[lim], %[lim], 1\n\t" \
    "s_cmp_gt_i32 %[lim], 0\n\t" \
    "s_cbranch_scc0 PD_%=\n\t" \
    "s_sleep " STR(CADM) "\n\t" \
    "buffer_load_dwordx4 v[212:215], v8, %[rs], %[m0] offen sc1\n\t" \
    "buffer_load_dwordx4 v[216:219], v8, %[rs], %[m1] offen sc1\n\t" \
    "s_waitcnt vmcnt(0)\n\t" \
    "s_branch PM_%=\n\t" \
    ".p2align 6\n\t" \
    "PD_%=:\n\t" \
    GSA(0) \
    "v_smfmac_f32_16x16x64_bf16 v[0:3], v[212:215], v[236:243], v10\n\t" \
    "v_xor_b32 v9, 16384, v9\n\t" \
    "v_smfmac_f32_16x16x64_bf16 v[4:7], v[212:215], v[244:251], v10\n\t" \
    "s_waitcnt lgkmcnt(0)\n\t" \
    "s_barrier\n\t" \
    "s_cmp_lg_u32 %[st], 63\n\t" \
    "s_cbranch_scc1 NL_%=\n\t" \
    "s_mov_b64 exec, %[fm]\n\t" \
    "v_mov_b32 v252, %[st]\n\t" \
    "v_mov_b32 v253, %[tag]\n\t" \
    "buffer_store_dwordx2 v[252:253], off, %[rs], %[po] sc1\n\t" \
    "s_mov_b64 exec, -1\n\t" \
    "NL_%=:\n\t" \
    "ds_read_b128 v[236:239], v9 offset:0\n\t" \
    "ds_read_b128 v[240:243], v9 offset:1024\n\t" \
    "ds_read_b128 v[244:247], v9 offset:2048\n\t" \
    "ds_read_b128 v[248:251], v9 offset:3072\n\t" \
    "ds_read_b128 v[220:223], v8 offset:0\n\t" \
    "ds_read_b128 v[224:227], v8 offset:4096\n\t" \
    "v_xor_b32 v8, 1024, v8\n\t" \
    "ds_read_b128 v[228:231], v8 offset:8192\n\t" \
    "v_smfmac_f32_16x16x64_bf16 v[0:3], v[216:219], v[148:155], v10\n\t" \
    "ds_read_b128 v[232:235], v8 offset:12288\n\t" \
    "v_smfmac_f32_16x16x64_bf16 v[4:7], v[216:219], v[156:163], v10\n\t" \
    "ds_read_b128 v[212:215], v8 offset:0\n\t" \
    "s_waitcnt lgkmcnt(4)\n\t" \
    "v_smfmac_f32_16x16x64_bf16 v[0:3], v[220:223], v[20:27], v10\n\t" \
    "ds_read_b128 v[216:219], v8 offset:4096\n\t" \
    "v_smfmac_f32_16x16x64_bf16 v[4:7], v[220:223], v[28:35], v10\n\t" \
    "v_xor_b32 v8, 3072, v8\n\t" \
    "ds_read_b128 v[220:223], v8 offset:12288\n\t" \
    "s_waitcnt lgkmcnt(5)\n\t" \
    "v_smfmac_f32_16x16x64_bf16 v[0:3], v[224:227], v[84:91], v10\n\t" \
    "v_smfmac_f32_16x16x64_bf16 v[4:7], v[224:227], v[92:99], v10\n\t" \
    "ds_read_b128 v[224:227], v8 offset:0\n\t" \
    "s_waitcnt lgkmcnt(5)\n\t" \
    "v_smfmac_f32_16x16x64_bf16 v[0:3], v[228:231], v[236:243], v10\n\t" \
    "v_xor_b32 v9, 49152, v9\n\t" \
    "ds_read_b128 v[236:239], v9 offset:0\n\t" \
    "v_smfmac_f32_16x16x64_bf16 v[4:7], v[228:231], v[244:251], v10\n\t" \
    "ds_read_b128 v[240:243], v9 offset:1024\n\t" \
    "s_waitcnt lgkmcnt(6)\n\t" \
    "v_smfmac_f32_16x16x64_bf16 v[0:3], v[232:235], v[164:171], v10\n\t" \
    "ds_read_b128 v[244:247], v9 offset:2048\n\t" \
    "v_smfmac_f32_16x16x64_bf16 v[4:7], v[232:235], v[172:179], v10\n\t" \
    "ds_read_b128 v[248:251], v9 offset:3072\n\t" \
    "s_waitcnt lgkmcnt(7)\n\t" \
    "v_smfmac_f32_16x16x64_bf16 v[0:3], v[212:215], v[36:43], v10\n\t" \
    "ds_read_b128 v[228:231], v8 offset:8192\n\t" \
    "v_smfmac_f32_16x16x64_bf16 v[4:7], v[212:215], v[44:51], v10\n\t" \
    "ds_read_b128 v[232:235], v8 offset:4096\n\t" \
    "s_waitcnt lgkmcnt(8)\n\t" \
    "v_smfmac_f32_16x16x64_bf16 v[0:3], v[216:219], v[100:107], v10\n\t" \
    "v_xor_b32 v8, 1024, v8\n\t" \
    "ds_read_b128 v[212:215], v8 offset:12288\n\t" \
    "v_smfmac_f32_16x16x64_bf16 v[4:7], v[216:219], v[108:115], v10\n\t" \
    "ds_read_b128 v[216:219], v8 offset:0\n\t" \
    "s_waitcnt lgkmcnt(9)\n\t" \
    "v_smfmac_f32_16x16x64_bf16 v[0:3], v[220:223], v[180:187], v10\n\t" \
    "v_smfmac_f32_16x16x64_bf16 v[4:7], v[220:223], v[188:195], v10\n\t" \
    "ds_read_b128 v[220:223], v8 offset:4096\n\t" \
    "s_waitcnt lgkmcnt(9)\n\t" \
    "v_smfmac_f32_16x16x64_bf16 v[0:3], v[224:227], v[52:59], v10\n\t" \
    "v_smfmac_f32_16x16x64_bf16 v[4:7], v[224:227], v[60:67], v10\n\t" \
    "ds_read_b128 v[224:227], v8 offset:8192\n\t" \
    "v_xor_b32 v8, 3072, v8\n\t" \
    "s_waitcnt lgkmcnt(5)\n\t" \
    "v_smfmac_f32_16x16x64_bf16 v[0:3], v[228:231], v[236:243], v10\n\t" \
    "v_xor_b32 v9, 16384, v9\n\t" \
    "ds_read_b128 v[236:239], v9 offset:0\n\t" \
    "v_smfmac_f32_16x16x64_bf16 v[4:7], v[228:231], v[244:251], v10\n\t" \
    "ds_read_b128 v[240:243], v9 offset:1024\n\t" \
    "s_waitcnt lgkmcnt(6)\n\t" \
    "v_smfmac_f32_16x16x64_bf16 v[0:3], v[232:235], v[116:123], v10\n\t" \
    "ds_read_b128 v[244:247], v9 offset:2048\n\t" \
    "v_smfmac_f32_16x16x64_bf16 v[4:7], v[232:235], v[124:131], v10\n\t" \
    "ds_read_b128 v[248:251], v9 offset:3072\n\t" \
    "s_waitcnt lgkmcnt(7)\n\t" \
    "v_smfmac_f32_16x16x64_bf16 v[0:3], v[212:215], v[196:203], v10\n\t" \
    "v_smfmac_f32_16x16x64_bf16 v[4:7], v[212:215], v[204:211], v10\n\t" \
    "s_waitcnt lgkmcnt(6)\n\t" \
    "v_smfmac_f32_16x16x64_bf16 v[0:3], v[216:219], v[68:75], v10\n\t" \
    "v_smfmac_f32_16x16x64_bf16 v[4:7], v[216:219], v[76:83], v10\n\t" \
    "s_waitcnt lgkmcnt(5)\n\t" \
    "v_smfmac_f32_16x16x64_bf16 v[0:3], v[220:223], v[132:139], v10\n\t" \
    "v_smfmac_f32_16x16x64_bf16 v[4:7], v[220:223], v[140:147], v10\n\t" \
    "s_waitcnt lgkmcnt(0)\n\t" \
    "v_smfmac_f32_16x16x64_bf16 v[0:3], v[224:227], v[236:243], v10\n\t" \
    "v_xor_b32 v9, 49152, v9\n\t" \
    "v_smfmac_f32_16x16x64_bf16 v[4:7], v[224:227], v[244:251], v10\n\t" \
    "s_setprio 3\n\t" \
    "v_mov_b32 v253, %[stag]\n\t" \
    "s_nop 3\n\t" \
    "v_add_f32 v0, v0, v1\n\t" \
    "v_add_f32 v2, v2, v3\n\t" \
    "v_add_f32 v4, v4, v5\n\t" \
    "v_add_f32 v6, v6, v7\n\t" \
    "v_cvt_pk_bf16_f32 v0, v0, v4\n\t" \
    "v_cvt_pk_bf16_f32 v2, v2, v6\n\t" \
    "ds_write2_b32 v13, v0, v2 offset0:0 offset1:8\n\t" \
    "s_mov_b64 exec, %[onej]\n\t" \
    "ds_write_b32 v18, v253 offset:16\n\t" \
    "s_mov_b64 exec, -1\n\t" \
    "s_setprio 0\n\t" \
    "ds_write2_b32 v11, v0, v2 offset0:0 offset1:4\n\t" \
    "s_mov_b64 exec, %[onej]\n\t" \
    "ds_write_b32 v18, v253 offset:32\n\t" \
    "s_mov_b64 exec, -1\n\t" \
    GSA(1) \
    "s_setprio 0\n\t" \
    "buffer_load_dwordx4 v[236:239], v12, %[rs], %[qx] offen offset:0 sc1\n\t" \
    "buffer_load_dwordx4 v[240:243], v12, %[rs], %[qx] offen offset:1024 sc1\n\t" \
    "buffer_load_dwordx4 v[244:247], v12, %[rs], %[qx] offen offset:2048 sc1\n\t" \
    "buffer_load_dwordx4 v[248:251], v12, %[rs], %[qx] offen offset:3072 sc1\n\t" \
    "v_cmp_le_i32 s[72:73], 0, v16\n\t" \
    "v_cmp_le_i32 s[74:75], 1, v16\n\t" \
    "v_cmp_le_i32 s[76:77], 2, v16\n\t" \
    "v_cmp_le_i32 s[78:79], 3, v16\n\t" \
    "v_cmp_le_i32 s[80:81], 16, v16\n\t" \
    "v_cmp_le_i32 s[82:83], 17, v16\n\t" \
    "v_cmp_le_i32 s[84:85], 18, v16\n\t" \
    "v_cmp_le_i32 s[86:87], 19, v16\n\t" \
    "s_cmp_eq_u32 %[st], 63\n\t" \
    "s_cbranch_scc1 LW_%=\n\t" \
    "v_mov_b32 v254, v254\n\t" \
    "s_nop 2\n\t" \
    "v_mov_b32 v254, v254\n\t" \
    "s_nop 2\n\t" \
    "v_mov_b32 v254, v254\n\t" \
    "s_nop 2\n\t" \
    "v_mov_b32 v254, v254\n\t" \
    "s_nop 2\n\t" \
    "v_mov_b32 v254, v254\n\t" \
    "s_nop 2\n\t" \
    "v_mov_b32 v254, v254\n\t" \
    "s_nop 2\n\t" \
    "v_mov_b32 v254, v254\n\t" \
    "s_nop 2\n\t" \
    "v_mov_b32 v254, v254\n\t" \
    "s_nop 2\n\t" \
    "v_mov_b32 v254, v254\n\t" \
    "s_nop 2\n\t" \
    "v_mov_b32 v254, v254\n\t" \
    "s_nop 2\n\t" \
    "v_mov_b32 v254, v254\n\t" \
    "s_nop 2\n\t" \
    "v_mov_b32 v254, v254\n\t" \
    "s_nop 2\n\t" \
    "v_mov_b32 v254, v254\n\t" \
    "s_nop 2\n\t" \
    "v_mov_b32 v254, v254\n\t" \
    "s_nop 2\n\t" \
    "v_mov_b32 v254, v254\n\t" \
    "s_nop 2\n\t" \
    "v_mov_b32 v254, v254\n\t" \
    "s_nop 2\n\t" \
    "v_mov_b32 v254, v254\n\t" \
    "s_nop 2\n\t" \
    "v_mov_b32 v254, v254\n\t" \
    "s_nop 2\n\t" \
    "v_mov_b32 v254, v254\n\t" \
    "s_nop 2\n\t" \
    "LW_%=:\n\t" \
    "ds_read_b32 v0, v18 offset:32\n\t" \
    "s_sub_u32 %[lim], %[lim], " STR(DEC1) "\n\t" \
    "s_cmp_gt_i32 %[lim], 0\n\t" \
    "v_mov_b32 v254, v254\n\t" \
    "s_nop 2\n\t" \
    "v_mov_b32 v254, v254\n\t" \
    "s_nop 2\n\t" \
    "v_mov_b32 v254, v254\n\t" \
    "s_nop 2\n\t" \
    "v_mov_b32 v254, v254\n\t" \
    "s_nop 2\n\t" \
    "v_mov_b32 v254, v254\n\t" \
    "s_nop 2\n\t" \
    "s_waitcnt lgkmcnt(0)\n\t" \
    "s_cbranch_scc0 CW_%=\n\t" \
    "v_cmp_ne_u32 vcc, v0, v253\n\t" \
    "s_cbranch_vccnz LW_%=\n\t" \
    "CW_%=:\n\t" \
    "ds_read_b128 v[212:215], v12 offset:24576\n\t" \
    "ds_read_b128 v[216:219], v12 offset:28672\n\t" \
    "ds_read_b128 v[220:223], v15 offset:0\n\t" \
    "ds_read_b128 v[224:227], v12 offset:25600\n\t" \
    "ds_read_b128 v[228:231], v12 offset:29696\n\t" \
    "v_add_u32 v253, %[se], v12\n\t" \
    "v_mov_b32 v254, v254\n\t" \
    "s_nop 2\n\t" \
    "v_mov_b32 v254, v254\n\t" \
    "s_nop 2\n\t" \
    "v_mov_b32 v254, v254\n\t" \
    "s_nop 2\n\t" \
    "v_mov_b32 v254, v254\n\t" \
    "s_nop 2\n\t" \
    "s_waitcnt vmcnt(0)\n\t" \
    "s_waitcnt lgkmcnt(4)\n\t" \
    "v_mfma_f32_16x16x32_bf16 v[0:3], v[212:215], v[236:239], 0\n\t" \
    "ds_read_b128 v[212:215], v15 offset:512\n\t" \
    "s_waitcnt lgkmcnt(4)\n\t" \
    "v_mfma_f32_16x16x32_bf16 v[4:7], v[216:219], v[236:239], 0\n\t" \
    "ds_read_b128 v[216:219], v12 offset:26624\n\t" \
    "s_waitcnt lgkmcnt(4)\n\t" \
    "v_mfma_f32_16x16x32_bf16 v[232:235], v[220:223], v[236:239], 0\n\t" \
    "ds_read_b128 v[220:223], v12 offset:30720\n\t" \
    "s_waitcnt lgkmcnt(4)\n\t" \
    "v_mfma_f32_16x16x32_bf16 v[0:3], v[224:227], v[240:243], v[0:3]\n\t" \
    "ds_read_b128 v[224:227], v15 offset:1024\n\t" \
    "s_waitcnt lgkmcnt(4)\n\t" \
    "v_mfma_f32_16x16x32_bf16 v[4:7], v[228:231], v[240:243], v[4:7]\n\t" \
    "ds_read_b128 v[228:231], v12 offset:27648\n\t" \
    "s_waitcnt lgkmcnt(4)\n\t" \
    "v_mfma_f32_16x16x32_bf16 v[232:235], v[212:215], v[240:243], v[232:235]\n\t" \
    "ds_read_b128 v[212:215], v12 offset:31744\n\t" \
    "s_waitcnt lgkmcnt(4)\n\t" \
    "v_mfma_f32_16x16x32_bf16 v[0:3], v[216:219], v[244:247], v[0:3]\n\t" \
    "ds_read_b128 v[216:219], v15 offset:1536\n\t" \
    "s_waitcnt lgkmcnt(4)\n\t" \
    "v_mfma_f32_16x16x32_bf16 v[4:7], v[220:223], v[244:247], v[4:7]\n\t" \
    "s_waitcnt lgkmcnt(3)\n\t" \
    "v_mfma_f32_16x16x32_bf16 v[232:235], v[224:227], v[244:247], v[232:235]\n\t" \
    "s_waitcnt lgkmcnt(2)\n\t" \
    "v_mfma_f32_16x16x32_bf16 v[0:3], v[228:231], v[248:251], v[0:3]\n\t" \
    "s_waitcnt lgkmcnt(1)\n\t" \
    "v_mfma_f32_16x16x32_bf16 v[4:7], v[212:215], v[248:251], v[4:7]\n\t" \
    "s_waitcnt lgkmcnt(0)\n\t" \
    "v_mfma_f32_16x16x32_bf16 v[232:235], v[216:219], v[248:251], v[232:235]\n\t" \
    "s_barrier\n\t" \
    GSA(4) \
    "ds_read_b128 v[236:239], v253 offset:8192\n\t" \
    "ds_read_b128 v[240:243], v253 offset:9216\n\t" \
    "ds_read_b128 v[244:247], v253 offset:10240\n\t" \
    "ds_read_b128 v[248:251], v253 offset:11264\n\t" \
    "ds_read2st64_b32 v[228:229], v17 offset0:0 offset1:1\n\t" \
    "ds_read2st64_b32 v[230:231], v17 offset0:2 offset1:3\n\t" \
    "s_mov_b64 exec, %[fm]\n\t" \
    "v_mov_b32 v220, %[st]\n\t" \
    "v_mov_b32 v221, %[tag]\n\t" \
    "buffer_store_dwordx2 v[220:221], off, %[rs], %[po] sc1\n\t" \
    "s_mov_b64 exec, -1\n\t" \
    "v_mov_b32 v254, 0xff800000\n\t" \
    "v_cvt_pk_bf16_f32 v0, 0, v0\n\t" \
    "v_cvt_pk_bf16_f32 v1, 0, v1\n\t" \
    "v_cvt_pk_bf16_f32 v2, 0, v2\n\t" \
    "v_cvt_pk_bf16_f32 v3, 0, v3\n\t" \
    "v_cvt_pk_bf16_f32 v4, 0, v4\n\t" \
    "v_cvt_pk_bf16_f32 v5, 0, v5\n\t" \
    "v_cvt_pk_bf16_f32 v6, 0, v6\n\t" \
    "v_cvt_pk_bf16_f32 v7, 0, v7\n\t" \
    "v_cvt_pk_bf16_f32 v232, 0, v232\n\t" \
    "v_cvt_pk_bf16_f32 v233, 0, v233\n\t" \
    "v_mul_f32 v0, 0x3db504f3, v0\n\t" \
    "v_mul_f32 v1, 0x3db504f3, v1\n\t" \
    "v_mul_f32 v2, 0x3db504f3, v2\n\t" \
    "v_mul_f32 v3, 0x3db504f3, v3\n\t" \
    "v_mul_f32 v4, 0x3db504f3, v4\n\t" \
    "v_mul_f32 v5, 0x3db504f3, v5\n\t" \
    "v_mul_f32 v6, 0x3db504f3, v6\n\t" \
    "v_mul_f32 v7, 0x3db504f3, v7\n\t" \
    "v_mul_f32 v232, 0x3db504f3, v232\n\t" \
    "v_mul_f32 v233, 0x3db504f3, v233\n\t" \
    "v_cvt_pk_bf16_f32 v0, 0, v0\n\t" \
    "v_cvt_pk_bf16_f32 v1, 0, v1\n\t" \
    "v_cvt_pk_bf16_f32 v2, 0, v2\n\t" \
    "v_cvt_pk_bf16_f32 v3, 0, v3\n\t" \
    "v_cvt_pk_bf16_f32 v4, 0, v4\n\t" \
    "v_cvt_pk_bf16_f32 v5, 0, v5\n\t" \
    "v_cvt_pk_bf16_f32 v6, 0, v6\n\t" \
    "v_cvt_pk_bf16_f32 v7, 0, v7\n\t" \
    "v_cvt_pk_bf16_f32 v232, 0, v232\n\t" \
    "v_cvt_pk_bf16_f32 v233, 0, v233\n\t" \
    "v_cndmask_b32 v0, v254, v0, s[72:73]\n\t" \
    "v_cndmask_b32 v1, v254, v1, s[74:75]\n\t" \
    "v_cndmask_b32 v2, v254, v2, s[76:77]\n\t" \
    "v_cndmask_b32 v3, v254, v3, s[78:79]\n\t" \
    "v_cndmask_b32 v4, v254, v4, s[80:81]\n\t" \
    "v_cndmask_b32 v5, v254, v5, s[82:83]\n\t" \
    "v_cndmask_b32 v6, v254, v6, s[84:85]\n\t" \
    "v_cndmask_b32 v7, v254, v7, s[86:87]\n\t" \
    "v_max3_f32 v252, v0, v1, v2\n\t" \
    "v_max3_f32 v255, v3, v4, v5\n\t" \
    "v_max3_f32 v254, v6, v7, v232\n\t" \
    "v_max3_f32 v252, v252, v255, v254\n\t" \
    "v_max_f32 v252, v252, v233\n\t" \
    "v_mov_b32 v255, v252\n\t" \
    "v_mov_b32 v254, v252\n\t" \
    "s_nop 1\n\t" \
    "v_permlane16_swap_b32 v255, v254\n\t" \
    "v_max_f32 v252, v255, v254\n\t" \
    "v_max_f32 v255, v255, v254\n\t" \
    "s_nop 1\n\t" \
    "v_permlane32_swap_b32 v252, v255\n\t" \
    "v_max_f32 v252, v252, v255\n\t" \
    "v_mul_f32 v252, 0xbfb8aa3b, v252\n\t" \
    "v_fma_f32 v0, v0, %[l2e], v252\n\t" \
    "v_fma_f32 v1, v1, %[l2e], v252\n\t" \
    "v_fma_f32 v2, v2, %[l2e], v252\n\t" \
    "v_fma_f32 v3, v3, %[l2e], v252\n\t" \
    "v_fma_f32 v4, v4, %[l2e], v252\n\t" \
    "v_fma_f32 v5, v5, %[l2e], v252\n\t" \
    "v_fma_f32 v6, v6, %[l2e], v252\n\t" \
    "v_fma_f32 v7, v7, %[l2e], v252\n\t" \
    "v_fma_f32 v232, v232, %[l2e], v252\n\t" \
    "v_fma_f32 v233, v233, %[l2e], v252\n\t" \
    "v_exp_f32 v0, v0\n\t" \
    "v_exp_f32 v1, v1\n\t" \
    "v_exp_f32 v2, v2\n\t" \
    "v_exp_f32 v3, v3\n\t" \
    "v_exp_f32 v4, v4\n\t" \
    "v_exp_f32 v5, v5\n\t" \
    "v_exp_f32 v6, v6\n\t" \
    "v_exp_f32 v7, v7\n\t" \
    "v_exp_f32 v232, v232\n\t" \
    "v_exp_f32 v233, v233\n\t" \
    "v_add_f32 v252, v0, v1\n\t" \
    "v_add_f32 v235, v2, v3\n\t" \
    "v_add_f32 v254, v4, v5\n\t" \
    "v_add_f32 v255, v6, v7\n\t" \
    "v_add_f32 v234, v232, v233\n\t" \
    "v_add_f32 v252, v252, v235\n\t" \
    "v_add_f32 v254, v254, v255\n\t" \
    "v_add_f32 v252, v252, v254\n\t" \
    "v_add_f32 v252, v252, v234\n\t" \
    "v_mov_b32 v255, v252\n\t" \
    "v_mov_b32 v254, v252\n\t" \
    "s_nop 1\n\t" \
    "v_permlane16_swap_b32 v255, v254\n\t" \
    "v_add_f32 v252, v255, v254\n\t" \
    "v_add_f32 v255, v255, v254\n\t" \
    "s_nop 1\n\t" \
    "v_permlane32_swap_b32 v252, v255\n\t" \
    "v_add_f32 v252, v252, v255\n\t" \
    "v_rcp_f32 v252, v252\n\t" \
    "s_nop 0\n\t" \
    "v_mul_f32 v0, v0, v252\n\t" \
    "v_mul_f32 v1, v1, v252\n\t" \
    "v_mul_f32 v2, v2, v252\n\t" \
    "v_mul_f32 v3, v3, v252\n\t" \
    "v_mul_f32 v4, v4, v252\n\t" \
    "v_mul_f32 v5, v5, v252\n\t" \
    "v_mul_f32 v6, v6, v252\n\t" \
    "v_mul_f32 v7, v7, v252\n\t" \
    "v_mul_f32 v232, v232, v252\n\t" \
    "v_mul_f32 v233, v233, v252\n\t" \
    "v_cvt_pk_bf16_f32 v0, v0, v1\n\t" \
    "v_cvt_pk_bf16_f32 v1, v2, v3\n\t" \
    "v_cvt_pk_bf16_f32 v2, v4, v5\n\t" \
    "v_cvt_pk_bf16_f32 v3, v6, v7\n\t" \
    "v_cvt_pk_bf16_f32 v4, v232, v233\n\t" \
    "v_mov_b32 v5, 0\n\t" \
    "v_mov_b32 v6, 0\n\t" \
    "v_cvt_pk_bf16_f32 v7, v232, v233\n\t" \
    "s_waitcnt lgkmcnt(0)\n\t" \
    "v_mfma_f32_16x16x32_bf16 v[236:239], v[236:239], v[0:3], 0\n\t" \
    "v_mfma_f32_16x16x32_bf16 v[240:243], v[240:243], v[0:3], 0\n\t" \
    "v_mfma_f32_16x16x32_bf16 v[244:247], v[244:247], v[0:3], 0\n\t" \
    "v_mfma_f32_16x16x32_bf16 v[248:251], v[248:251], v[0:3], 0\n\t" \
    "v_mfma_f32_16x16x16_bf16 v[236:239], v[228:229], v[4:5], v[236:239]\n\t" \
    "v_mfma_f32_16x16x16_bf16 v[240:243], v[228:229], v[6:7], v[240:243]\n\t" \
    "v_mfma_f32_16x16x16_bf16 v[244:247], v[230:231], v[4:5], v[244:247]\n\t" \
    "v_mfma_f32_16x16x16_bf16 v[248:251], v[230:231], v[6:7], v[248:251]\n\t" \
    "s_nop 4\n\t" \
    "v_cvt_pk_bf16_f32 v220, v236, v237\n\t" \
    "v_cvt_pk_bf16_f32 v221, v240, v241\n\t" \
    "v_cvt_pk_bf16_f32 v222, v238, v239\n\t" \
    "v_cvt_pk_bf16_f32 v223, v242, v243\n\t" \
    "v_permlane16_swap_b32 v220, v221\n\t" \
    "s_nop 0\n\t" \
    "v_permlane16_swap_b32 v222, v223\n\t" \
    "v_cvt_pk_bf16_f32 v224, v244, v245\n\t" \
    "v_cvt_pk_bf16_f32 v225, v248, v249\n\t" \
    "v_cvt_pk_bf16_f32 v226, v246, v247\n\t" \
    "v_cvt_pk_bf16_f32 v227, v250, v251\n\t" \
    "v_permlane16_swap_b32 v224, v225\n\t" \
    "s_nop 0\n\t" \
    "v_permlane16_swap_b32 v226, v227\n\t" \
    "s_setprio 1\n\t" \
    GSA(2) \
    GSW \
    "s_nop 1\n\t" \
    : [lim] "+s"(lim), "+{v[220:227]}"(oxr) DGO GSO(0) GSO(1) GSO(2) GSO(3) GSO(4) \
    : "{v8}"(am_a), "{v9}"(t0_a), "{v10}"(idx), "{v11}"(iw_a), "{v12}"(ab_a), "{v13}"(sw_a), "{v14}"(xo_v), "{v15}"(kr_a), "{v16}"(dq), "{v17}"(vl_a), "{v18}"(fr_a), [stag] "s"(0xffffff81u + s), [onej] "s"(onej), [rs] "s"(rs), [m0] "s"(sbm + 16384u), [m1] "s"(sbm + 20480u), [st] "s"(s), [tag] "s"(P.tag), [po] "s"(FLG_OFF + F_PRG(4, h) * 128), [fm] "s"(fmask), [qx] "s"(qxo), [ox] "s"(oxo), [se] "s"((j & 1) * 4096), [l2e] "s"(0x3fb8aa3b), "{v[20:27]}"(wr[0][0]), "{v[28:35]}"(wr[0][1]), "{v[36:43]}"(wr[1][0]), "{v[44:51]}"(wr[1][1]), "{v[52:59]}"(wr[2][0]), "{v[60:67]}"(wr[2][1]), "{v[68:75]}"(wr[3][0]), "{v[76:83]}"(wr[3][1]), "{v[84:91]}"(wr[4][0]), "{v[92:99]}"(wr[4][1]), "{v[100:107]}"(wr[5][0]), "{v[108:115]}"(wr[5][1]), "{v[116:123]}"(wr[6][0]), "{v[124:131]}"(wr[6][1]), "{v[132:139]}"(wr[7][0]), "{v[140:147]}"(wr[7][1]), "{v[148:155]}"(wr[8][0]), "{v[156:163]}"(wr[8][1]), "{v[164:171]}"(wr[9][0]), "{v[172:179]}"(wr[9][1]), "{v[180:187]}"(wr[10][0]), "{v[188:195]}"(wr[10][1]), "{v[196:203]}"(wr[11][0]), "{v[204:211]}"(wr[11][1]) \
    : "memory", "scc", "vcc", "s90", "s91", "s72", "s73", "s74", "s75", "s76", "s77", "s78", "s79", "s80", "s81", "s82", "s83", "s84", "s85", "s86", "s87", "v0", "v1", "v2", "v3", "v4", "v5", "v6", "v7", "v212", "v213", "v214", "v215", "v216", "v217", "v218", "v219", "v228", "v229", "v230", "v231", "v232", "v233", "v234", "v235", "v236", "v237", "v238", "v239", "v240", "v241", "v242", "v243", "v244", "v245", "v246", "v247", "v248", "v249", "v250", "v251", "v252", "v253", "v254", "v255")

#define T_WAIT() \
  asm volatile( \
    "s_getpc_b64 s[92:93]\n\t" \
    "TPC_%=:\n\t" \
    "s_add_u32 s92, s92, TEC_%=-TPC_%=\n\t" \
    "s_addc_u32 s93, s93, 0\n\t" \
    "s_lshr_b32 s91, %[tgt], 2\n\t" \
    "s_sub_u32 s91, s91, 3\n\t" \
    "TWC_%=:\n\t" \
    "s_memrealtime s[88:89]\n\t" \
    DGE \
    "s_waitcnt lgkmcnt(0)\n\t" \
    "s_sub_u32 s89, s91, s88\n\t" \
    "s_lshl_b32 s89, s89, 2\n\t" \
    "s_cmp_gt_i32 s89, 0\n\t" \
    "s_cbranch_scc1 TWC_%=\n\t" \
    "s_lshl_b32 s88, s88, 2\n\t" \
    "s_sub_u32 s89, %[tgt], s88\n\t" \
    "s_max_i32 s89, s89, 0\n\t" \
    "s_mul_i32 s89, s89, 11\n\t" \
    "s_lshr_b32 s89, s89, 1\n\t" \
    "s_and_b32 s89, s89, 0x7c\n\t" \
    "s_sub_u32 s92, s92, s89\n\t" \
    "s_subb_u32 s93, s93, 0\n\t" \
    "s_setpc_b64 s[92:93]\n\t" \
    "s_nop 0\n\t" \
    "s_nop 0\n\t" \
    "s_nop 0\n\t" \
    "s_nop 0\n\t" \
    "s_nop 0\n\t" \
    "s_nop 0\n\t" \
    "s_nop 0\n\t" \
    "s_nop 0\n\t" \
    "s_nop 0\n\t" \
    "s_nop 0\n\t" \
    "s_nop 0\n\t" \
    "s_nop 0\n\t" \
    "s_nop 0\n\t" \
    "s_nop 0\n\t" \
    "s_nop 0\n\t" \
    "s_nop 0\n\t" \
    "s_nop 0\n\t" \
    "s_nop 0\n\t" \
    "s_nop 0\n\t" \
    "s_nop 0\n\t" \
    "s_nop 0\n\t" \
    "s_nop 0\n\t" \
    "TEC_%=:\n\t" \
    :  \
    : [tgt] "s"(tgt) \
    : "s88", "s89", "s90", "s91", "s92", "s93", "s94", "s95", "memory", "scc")

#define V_PROJ() \
  asm volatile( \
    "v_mov_b32 v0, 0\n\t" \
    "v_mov_b32 v1, 0\n\t" \
    "v_mov_b32 v2, 0\n\t" \
    "v_mov_b32 v3, 0\n\t" \
    "v_mov_b32 v4, 0\n\t" \
    "v_mov_b32 v5, 0\n\t" \
    "v_mov_b32 v6, 0\n\t" \
    "v_mov_b32 v7, 0\n\t" \
    "v_mov_b32 v8, 0\n\t" \
    "v_mov_b32 v9, 0\n\t" \
    "v_mov_b32 v10, 0\n\t" \
    "v_mov_b32 v11, 0\n\t" \
    "v_mov_b32 v12, 0\n\t" \
    "v_mov_b32 v13, 0\n\t" \
    "v_mov_b32 v14, 0\n\t" \
    "v_mov_b32 v15, 0\n\t" \
    "v_mov_b32 v252, 0\n\t" \
    "s_getpc_b64 s[92:93]\n\t" \
    "TPB_%=:\n\t" \
    "s_add_u32 s92, s92, TEB_%=-TPB_%=\n\t" \
    "s_addc_u32 s93, s93, 0\n\t" \
    "s_lshr_b32 s91, %[tgt], 2\n\t" \
    "s_sub_u32 s91, s91, 3\n\t" \
    "TWB_%=:\n\t" \
    "s_memrealtime s[88:89]\n\t" \
    DGE \
    "s_waitcnt lgkmcnt(0)\n\t" \
    "s_sub_u32 s89, s91, s88\n\t" \
    "s_lshl_b32 s89, s89, 2\n\t" \
    "s_cmp_gt_i32 s89, 0\n\t" \
    "s_cbranch_scc1 TWB_%=\n\t" \
    "s_lshl_b32 s88, s88, 2\n\t" \
    "s_sub_u32 s89, %[tgt], s88\n\t" \
    "s_max_i32 s89, s89, 0\n\t" \
    "s_mul_i32 s89, s89, 11\n\t" \
    "s_lshr_b32 s89, s89, 1\n\t" \
    "s_and_b32 s89, s89, 0x7c\n\t" \
    "s_sub_u32 s92, s92, s89\n\t" \
    "s_subb_u32 s93, s93, 0\n\t" \
    "s_setpc_b64 s[92:93]\n\t" \
    "s_nop 0\n\t" \
    "s_nop 0\n\t" \
    "s_nop 0\n\t" \
    "s_nop 0\n\t" \
    "s_nop 0\n\t" \
    "s_nop 0\n\t" \
    "s_nop 0\n\t" \
    "s_nop 0\n\t" \
    "s_nop 0\n\t" \
    "s_nop 0\n\t" \
    "s_nop 0\n\t" \
    "s_nop 0\n\t" \
    "s_nop 0\n\t" \
    "s_nop 0\n\t" \
    "s_nop 0\n\t" \
    "s_nop 0\n\t" \
    "s_nop 0\n\t" \
    "s_nop 0\n\t" \
    "s_nop 0\n\t" \
    "s_nop 0\n\t" \
    "s_nop 0\n\t" \
    "s_nop 0\n\t" \
    "TEB_%=:\n\t" \
    "buffer_load_dwordx4 v[220:223], %[l16], %[rs], %[mo] offen offset:0 sc1\n\t" \
    "s_nop 11\n\t" \
    "buffer_load_dwordx4 v[224:227], %[l16], %[rs], %[mo] offen offset:1024 sc1\n\t" \
    "s_nop 11\n\t" \
    "buffer_load_dwordx4 v[228:231], %[l16], %[rs], %[mo] offen offset:2048 sc1\n\t" \
    "s_nop 11\n\t" \
    "buffer_load_dwordx4 v[232:235], %[l16], %[rs], %[mo] offen offset:3072 sc1\n\t" \
    "A_%=:\n\t" \
    "s_waitcnt vmcnt(3)\n\t" \
    "v_smfmac_f32_16x16x64_bf16 v[0:3], v[220:223], v[28:35], %[idx]\n\t" \
    "v_max3_u32 v252, v252, v220, v221\n\t" \
    "v_smfmac_f32_16x16x64_bf16 v[4:7], v[220:223], v[36:43], %[idx]\n\t" \
    "v_max3_u32 v252, v252, v222, v223\n\t" \
    "v_smfmac_f32_16x16x64_bf16 v[8:11], v[220:223], v[44:51], %[idx]\n\t" \
    "v_smfmac_f32_16x16x64_bf16 v[12:15], v[220:223], v[52:59], %[idx]\n\t" \
    "s_waitcnt vmcnt(2)\n\t" \
    "v_smfmac_f32_16x16x64_bf16 v[0:3], v[224:227], v[60:67], %[idx]\n\t" \
    "v_max3_u32 v252, v252, v224, v225\n\t" \
    "v_smfmac_f32_16x16x64_bf16 v[4:7], v[224:227], v[68:75], %[idx]\n\t" \
    "v_max3_u32 v252, v252, v226, v227\n\t" \
    "v_smfmac_f32_16x16x64_bf16 v[8:11], v[224:227], v[76:83], %[idx]\n\t" \
    "v_smfmac_f32_16x16x64_bf16 v[12:15], v[224:227], v[84:91], %[idx]\n\t" \
    "s_waitcnt vmcnt(1)\n\t" \
    "v_smfmac_f32_16x16x64_bf16 v[0:3], v[228:231], v[92:99], %[idx]\n\t" \
    "v_max3_u32 v252, v252, v228, v229\n\t" \
    "v_smfmac_f32_16x16x64_bf16 v[4:7], v[228:231], v[100:107], %[idx]\n\t" \
    "v_max3_u32 v252, v252, v230, v231\n\t" \
    "v_smfmac_f32_16x16x64_bf16 v[8:11], v[228:231], v[108:115], %[idx]\n\t" \
    "v_smfmac_f32_16x16x64_bf16 v[12:15], v[228:231], v[116:123], %[idx]\n\t" \
    "s_waitcnt vmcnt(0)\n\t" \
    "v_smfmac_f32_16x16x64_bf16 v[0:3], v[232:235], v[124:131], %[idx]\n\t" \
    "v_max3_u32 v252, v252, v232, v233\n\t" \
    "v_smfmac_f32_16x16x64_bf16 v[4:7], v[232:235], v[132:139], %[idx]\n\t" \
    "v_max3_u32 v252, v252, v234, v235\n\t" \
    "v_smfmac_f32_16x16x64_bf16 v[8:11], v[232:235], v[140:147], %[idx]\n\t" \
    "v_smfmac_f32_16x16x64_bf16 v[12:15], v[232:235], v[148:155], %[idx]\n\t" \
    "v_cmp_eq_u32 vcc, -1, v252\n\t" \
    "s_cbranch_vccnz SL_%=\n\t" \
    "D_%=:\n\t" \
    GSA(3) \
    "s_nop 0\n\t" \
    "v_add_f32 v0, v0, v1\n\t" \
    "v_add_f32 v1, v2, v3\n\t" \
    "v_add_f32 v4, v4, v5\n\t" \
    "v_add_f32 v5, v6, v7\n\t" \
    "ds_write2st64_b64 %[pw], v[0:1], v[4:5] offset0:0 offset1:4\n\t" \
    "v_add_f32 v8, v8, v9\n\t" \
    "v_add_f32 v9, v10, v11\n\t" \
    "v_add_f32 v12, v12, v13\n\t" \
    "v_add_f32 v13, v14, v15\n\t" \
    "ds_write2st64_b64 %[pw], v[8:9], v[12:13] offset0:8 offset1:12\n\t" \
    "s_waitcnt lgkmcnt(0)\n\t" \
    "s_barrier\n\t" \
    "ds_read2st64_b64 v[0:3], %[pr] offset0:0 offset1:1\n\t" \
    "ds_read2st64_b64 v[4:7], %[pr] offset0:2 offset1:3\n\t" \
    "s_waitcnt lgkmcnt(1)\n\t" \
    "v_pk_add_f32 v[0:1], v[0:1], v[2:3]\n\t" \
    "s_waitcnt lgkmcnt(0)\n\t" \
    "v_pk_add_f32 v[4:5], v[4:5], v[6:7]\n\t" \
    "s_nop 0\n\t" \
    "v_pk_add_f32 v[0:1], v[0:1], v[4:5]\n\t" \
    "s_nop 0\n\t" \
    "v_cvt_pk_bf16_f32 v0, v0, v1\n\t" \
    "v_min_u32 v0, -2, v0\n\t" \
    "s_nop 1\n\t" \
    "v_mov_b32_dpp v1, v0 quad_perm:[1,1,1,1] row_mask:0xf bank_mask:0xf\n\t" \
    "v_mov_b32_dpp v2, v0 quad_perm:[2,2,2,2] row_mask:0xf bank_mask:0xf\n\t" \
    "v_mov_b32_dpp v3, v0 quad_perm:[3,3,3,3] row_mask:0xf bank_mask:0xf\n\t" \
    "s_mov_b64 exec, %[qm]\n\t" \
    "buffer_store_dwordx4 v[0:3], %[l4], %[rs], %[vo] offen sc1\n\t" \
    "s_mov_b64 exec, -1\n\t" \
    "s_branch E_%=\n\t" \
    "SL_%=:\n\t" \
    "buffer_load_dwordx4 v[220:223], %[l16], %[rs], %[mo] offen offset:0 sc1\n\t" \
    "buffer_load_dwordx4 v[224:227], %[l16], %[rs], %[mo] offen offset:1024 sc1\n\t" \
    "buffer_load_dwordx4 v[228:231], %[l16], %[rs], %[mo] offen offset:2048 sc1\n\t" \
    "buffer_load_dwordx4 v[232:235], %[l16], %[rs], %[mo] offen offset:3072 sc1\n\t" \
    "v_mov_b32 v252, %[rt]\n\t" \
    "v_mov_b32 v253, %[tgt]\n\t" \
    "ds_write_b32 v252, v253\n\t" \
    "s_sub_u32 %[lim], %[lim], 1\n\t" \
    "s_cmp_gt_i32 %[lim], 0\n\t" \
    "s_cbranch_scc0 G_%=\n\t" \
    "s_nop 3\n\t" \
    "v_mov_b32 v0, 0\n\t" \
    "v_mov_b32 v1, 0\n\t" \
    "v_mov_b32 v2, 0\n\t" \
    "v_mov_b32 v3, 0\n\t" \
    "v_mov_b32 v4, 0\n\t" \
    "v_mov_b32 v5, 0\n\t" \
    "v_mov_b32 v6, 0\n\t" \
    "v_mov_b32 v7, 0\n\t" \
    "v_mov_b32 v8, 0\n\t" \
    "v_mov_b32 v9, 0\n\t" \
    "v_mov_b32 v10, 0\n\t" \
    "v_mov_b32 v11, 0\n\t" \
    "v_mov_b32 v12, 0\n\t" \
    "v_mov_b32 v13, 0\n\t" \
    "v_mov_b32 v14, 0\n\t" \
    "v_mov_b32 v15, 0\n\t" \
    "v_mov_b32 v252, 0\n\t" \
    "s_branch A_%=\n\t" \
    "G_%=:\n\t" \
    "s_waitcnt vmcnt(0)\n\t" \
    "s_branch D_%=\n\t" \
    "E_%=:\n\t" \
    GSW \
    "s_nop 1\n\t" \
    : [lim] "+s"(lim) GSO(3) \
    : [l16] "v"(l16), [pw] "v"(pw_a), [pr] "v"(pr_a), [idx] "v"(idx), [l4] "v"(ln * 4), [rs] "s"(rs), [mo] "s"(mo), [tgt] "s"(tgt), [rt] "s"(lbase + LB_RED + 4096 + 12), [vo] "s"(sb + vo), [qm] "s"(0x1111111111111111ull), "{v[28:35]}"(wv[0]), "{v[36:43]}"(wv[1]), "{v[44:51]}"(wv[2]), "{v[52:59]}"(wv[3]), "{v[60:67]}"(wv[4]), "{v[68:75]}"(wv[5]), "{v[76:83]}"(wv[6]), "{v[84:91]}"(wv[7]), "{v[92:99]}"(wv[8]), "{v[100:107]}"(wv[9]), "{v[108:115]}"(wv[10]), "{v[116:123]}"(wv[11]), "{v[124:131]}"(wv[12]), "{v[132:139]}"(wv[13]), "{v[140:147]}"(wv[14]), "{v[148:155]}"(wv[15]) \
    : "memory", "vcc", "scc", "v252", "v253", "s88", "s89", "s90", "s91", "s92", "s93", "s94", "s95", "v0", "v1", "v2", "v3", "v4", "v5", "v6", "v7", "v8", "v9", "v10", "v11", "v12", "v13", "v14", "v15", "v220", "v221", "v222", "v223", "v224", "v225", "v226", "v227", "v228", "v229", "v230", "v231", "v232", "v233", "v234", "v235")

#define WO_V() \
  asm volatile( \
    "v_mov_b32 v0, 0\n\t" \
    "v_mov_b32 v1, 0\n\t" \
    "v_mov_b32 v2, 0\n\t" \
    "v_mov_b32 v3, 0\n\t" \
    "v_mov_b32 v4, 0\n\t" \
    "v_mov_b32 v5, 0\n\t" \
    "v_mov_b32 v6, 0\n\t" \
    "v_mov_b32 v7, 0\n\t" \
    "v_mov_b32 v8, 0\n\t" \
    "v_mov_b32 v9, 0\n\t" \
    "v_mov_b32 v10, 0\n\t" \
    "v_mov_b32 v11, 0\n\t" \
    "v_mov_b32 v12, 0\n\t" \
    "v_mov_b32 v13, 0\n\t" \
    "v_mov_b32 v14, 0\n\t" \
    "v_mov_b32 v15, 0\n\t" \
    "v_mov_b32 v252, 0\n\t" \
    "s_barrier\n\t" \
    GSA(4) \
    "buffer_load_dwordx4 v[220:223], %[l16], %[rs], %[s0] offen sc1\n\t" \
    "s_nop 7\n\t" \
    "buffer_load_dwordx4 v[224:227], %[l16], %[rs], %[s1] offen sc1\n\t" \
    ".p2align 5\n\t" \
    "s_nop 0\n\t" \
    "s_nop 0\n\t" \
    "s_nop 0\n\t" \
    "s_nop 0\n\t" \
    "s_nop 0\n\t" \
    "A_%=:\n\t" \
    "s_waitcnt vmcnt(1)\n\t" \
    "v_smfmac_f32_16x16x64_bf16 v[0:3], v[220:223], v[156:163], %[idx]\n\t" \
    "v_max3_u32 v252, v252, v220, v221\n\t" \
    "v_smfmac_f32_16x16x64_bf16 v[4:7], v[220:223], v[164:171], %[idx]\n\t" \
    "v_max3_u32 v252, v252, v222, v223\n\t" \
    "v_smfmac_f32_16x16x64_bf16 v[8:11], v[220:223], v[172:179], %[idx]\n\t" \
    "v_smfmac_f32_16x16x64_bf16 v[12:15], v[220:223], v[180:187], %[idx]\n\t" \
    "s_waitcnt vmcnt(0)\n\t" \
    "v_smfmac_f32_16x16x64_bf16 v[0:3], v[224:227], v[188:195], %[idx]\n\t" \
    "v_max3_u32 v252, v252, v224, v225\n\t" \
    "v_smfmac_f32_16x16x64_bf16 v[4:7], v[224:227], v[196:203], %[idx]\n\t" \
    "v_max3_u32 v252, v252, v226, v227\n\t" \
    "v_smfmac_f32_16x16x64_bf16 v[8:11], v[224:227], v[204:211], %[idx]\n\t" \
    "v_smfmac_f32_16x16x64_bf16 v[12:15], v[224:227], v[212:219], %[idx]\n\t" \
    "s_setprio 0\n\t" \
    "v_cmp_eq_u32 vcc, -1, v252\n\t" \
    "s_cbranch_vccnz SL_%=\n\t" \
    "D_%=:\n\t" \
    GSA(1) \
    "s_nop 2\n\t" \
    "v_add_f32 v0, v0, v1\n\t" \
    "v_add_f32 v2, v2, v3\n\t" \
    "v_add_f32 v4, v4, v5\n\t" \
    "v_add_f32 v6, v6, v7\n\t" \
    "v_add_f32 v8, v8, v9\n\t" \
    "v_add_f32 v10, v10, v11\n\t" \
    "v_add_f32 v12, v12, v13\n\t" \
    "v_add_f32 v14, v14, v15\n\t" \
    "v_permlane32_swap_b32 v0, v4\n\t" \
    "v_permlane32_swap_b32 v2, v6\n\t" \
    "v_permlane32_swap_b32 v8, v12\n\t" \
    "v_permlane32_swap_b32 v10, v14\n\t" \
    "v_add_f32 v0, v0, v12\n\t" \
    "v_add_f32 v2, v2, v14\n\t" \
    "ds_write2_b32 %[rw], v0, v2 offset0:0 offset1:32\n\t" \
    "s_setprio 2\n\t" \
    "s_waitcnt lgkmcnt(0)\n\t" \
    "s_barrier\n\t" \
    "s_branch E_%=\n\t" \
    "SL_%=:\n\t" \
    "s_sleep 1\n\t" \
    "buffer_load_dwordx4 v[220:223], %[l16], %[rs], %[s0] offen sc1\n\t" \
    "buffer_load_dwordx4 v[224:227], %[l16], %[rs], %[s1] offen sc1\n\t" \
    "s_sub_u32 %[lim], %[lim], " STR(DEC2) "\n\t" \
    "s_cmp_gt_i32 %[lim], 0\n\t" \
    "s_cbranch_scc0 G_%=\n\t" \
    "s_nop 3\n\t" \
    "v_mov_b32 v0, 0\n\t" \
    "v_mov_b32 v1, 0\n\t" \
    "v_mov_b32 v2, 0\n\t" \
    "v_mov_b32 v3, 0\n\t" \
    "v_mov_b32 v4, 0\n\t" \
    "v_mov_b32 v5, 0\n\t" \
    "v_mov_b32 v6, 0\n\t" \
    "v_mov_b32 v7, 0\n\t" \
    "v_mov_b32 v8, 0\n\t" \
    "v_mov_b32 v9, 0\n\t" \
    "v_mov_b32 v10, 0\n\t" \
    "v_mov_b32 v11, 0\n\t" \
    "v_mov_b32 v12, 0\n\t" \
    "v_mov_b32 v13, 0\n\t" \
    "v_mov_b32 v14, 0\n\t" \
    "v_mov_b32 v15, 0\n\t" \
    "v_mov_b32 v252, 0\n\t" \
    "s_branch A_%=\n\t" \
    "G_%=:\n\t" \
    "s_waitcnt vmcnt(0)\n\t" \
    "s_branch D_%=\n\t" \
    "E_%=:\n\t" \
    GSW \
    : [lim] "+s"(lim) GSO(1) GSO(4) \
    : [l16] "v"(l16), [rs] "s"(rs), [idx] "v"(idx), [rw] "v"(rw_a), [s0] "s"(so[0]), [s1] "s"(so[1]), "{v[156:163]}"(wo[0][0][0]), "{v[164:171]}"(wo[0][0][1]), "{v[172:179]}"(wo[0][1][0]), "{v[180:187]}"(wo[0][1][1]), "{v[188:195]}"(wo[1][0][0]), "{v[196:203]}"(wo[1][0][1]), "{v[204:211]}"(wo[1][1][0]), "{v[212:219]}"(wo[1][1][1]) \
    : "memory", "vcc", "scc", "v252", "v0", "v1", "v2", "v3", "v4", "v5", "v6", "v7", "v8", "v9", "v10", "v11", "v12", "v13", "v14", "v15", "v220", "v221", "v222", "v223", "v224", "v225", "v226", "v227")

#define WO_W() \
  asm volatile( \
    "v_mov_b32 v0, 0\n\t" \
    "v_mov_b32 v1, 0\n\t" \
    "v_mov_b32 v2, 0\n\t" \
    "v_mov_b32 v3, 0\n\t" \
    "v_mov_b32 v4, 0\n\t" \
    "v_mov_b32 v5, 0\n\t" \
    "v_mov_b32 v6, 0\n\t" \
    "v_mov_b32 v7, 0\n\t" \
    "v_mov_b32 v8, 0\n\t" \
    "v_mov_b32 v9, 0\n\t" \
    "v_mov_b32 v10, 0\n\t" \
    "v_mov_b32 v11, 0\n\t" \
    "v_mov_b32 v12, 0\n\t" \
    "v_mov_b32 v13, 0\n\t" \
    "v_mov_b32 v14, 0\n\t" \
    "v_mov_b32 v15, 0\n\t" \
    "v_mov_b32 v252, 0\n\t" \
    "s_setprio 1\n\t" \
    "s_barrier\n\t" \
    GSA(4) \
    DGC \
    "buffer_load_dwordx4 v[220:223], %[l16], %[rs], %[s0] offen sc1\n\t" \
    "s_nop 15\n\t" \
    "buffer_load_dwordx4 v[224:227], %[l16], %[rs], %[s1] offen sc1\n\t" \
    "s_nop 15\n\t" \
    "buffer_load_dwordx4 v[228:231], %[l16], %[rs], %[s2] offen sc1\n\t" \
    "s_nop 15\n\t" \
    "buffer_load_dwordx4 v[232:235], %[l16], %[rs], %[s3] offen sc1\n\t" \
    "s_nop 15\n\t" \
    "buffer_load_dwordx4 v[236:239], %[l16], %[rs], %[s4] offen sc1\n\t" \
    "s_nop 15\n\t" \
    "buffer_load_dwordx4 v[240:243], %[l16], %[rs], %[s5] offen sc1\n\t" \
    ".p2align 5\n\t" \
    "s_nop 0\n\t" \
    "s_nop 0\n\t" \
    "s_nop 0\n\t" \
    "s_nop 0\n\t" \
    "s_nop 0\n\t" \
    "A_%=:\n\t" \
    "s_waitcnt vmcnt(5)\n\t" \
    "v_smfmac_f32_16x16x64_bf16 v[0:3], v[220:223], v[28:35], %[idx]\n\t" \
    "v_max3_u32 v252, v252, v220, v221\n\t" \
    "v_smfmac_f32_16x16x64_bf16 v[4:7], v[220:223], v[36:43], %[idx]\n\t" \
    "v_max3_u32 v252, v252, v222, v223\n\t" \
    "v_smfmac_f32_16x16x64_bf16 v[8:11], v[220:223], v[44:51], %[idx]\n\t" \
    "v_smfmac_f32_16x16x64_bf16 v[12:15], v[220:223], v[52:59], %[idx]\n\t" \
    "s_waitcnt vmcnt(4)\n\t" \
    "v_smfmac_f32_16x16x64_bf16 v[0:3], v[224:227], v[60:67], %[idx]\n\t" \
    "v_max3_u32 v252, v252, v224, v225\n\t" \
    "v_smfmac_f32_16x16x64_bf16 v[4:7], v[224:227], v[68:75], %[idx]\n\t" \
    "v_max3_u32 v252, v252, v226, v227\n\t" \
    "v_smfmac_f32_16x16x64_bf16 v[8:11], v[224:227], v[76:83], %[idx]\n\t" \
    "v_smfmac_f32_16x16x64_bf16 v[12:15], v[224:227], v[84:91], %[idx]\n\t" \
    "s_waitcnt vmcnt(3)\n\t" \
    "v_smfmac_f32_16x16x64_bf16 v[0:3], v[228:231], v[92:99], %[idx]\n\t" \
    "v_max3_u32 v252, v252, v228, v229\n\t" \
    "v_smfmac_f32_16x16x64_bf16 v[4:7], v[228:231], v[100:107], %[idx]\n\t" \
    "v_max3_u32 v252, v252, v230, v231\n\t" \
    "v_smfmac_f32_16x16x64_bf16 v[8:11], v[228:231], v[108:115], %[idx]\n\t" \
    "v_smfmac_f32_16x16x64_bf16 v[12:15], v[228:231], v[116:123], %[idx]\n\t" \
    "s_waitcnt vmcnt(2)\n\t" \
    "v_smfmac_f32_16x16x64_bf16 v[0:3], v[232:235], v[124:131], %[idx]\n\t" \
    "v_max3_u32 v252, v252, v232, v233\n\t" \
    "v_smfmac_f32_16x16x64_bf16 v[4:7], v[232:235], v[132:139], %[idx]\n\t" \
    "v_max3_u32 v252, v252, v234, v235\n\t" \
    "v_smfmac_f32_16x16x64_bf16 v[8:11], v[232:235], v[140:147], %[idx]\n\t" \
    "v_smfmac_f32_16x16x64_bf16 v[12:15], v[232:235], v[148:155], %[idx]\n\t" \
    "s_waitcnt vmcnt(1)\n\t" \
    "v_smfmac_f32_16x16x64_bf16 v[0:3], v[236:239], v[156:163], %[idx]\n\t" \
    "v_max3_u32 v252, v252, v236, v237\n\t" \
    "v_smfmac_f32_16x16x64_bf16 v[4:7], v[236:239], v[164:171], %[idx]\n\t" \
    "v_max3_u32 v252, v252, v238, v239\n\t" \
    "v_smfmac_f32_16x16x64_bf16 v[8:11], v[236:239], v[172:179], %[idx]\n\t" \
    "v_smfmac_f32_16x16x64_bf16 v[12:15], v[236:239], v[180:187], %[idx]\n\t" \
    "s_waitcnt vmcnt(0)\n\t" \
    "v_smfmac_f32_16x16x64_bf16 v[0:3], v[240:243], v[188:195], %[idx]\n\t" \
    "v_max3_u32 v252, v252, v240, v241\n\t" \
    "v_smfmac_f32_16x16x64_bf16 v[4:7], v[240:243], v[196:203], %[idx]\n\t" \
    "v_max3_u32 v252, v252, v242, v243\n\t" \
    "v_smfmac_f32_16x16x64_bf16 v[8:11], v[240:243], v[204:211], %[idx]\n\t" \
    "v_smfmac_f32_16x16x64_bf16 v[12:15], v[240:243], v[212:219], %[idx]\n\t" \
    "v_cmp_eq_u32 vcc, -1, v252\n\t" \
    "s_cbranch_vccnz SL_%=\n\t" \
    "D_%=:\n\t" \
    GSA(1) \
    "s_nop 2\n\t" \
    "v_add_f32 v0, v0, v1\n\t" \
    "v_add_f32 v2, v2, v3\n\t" \
    "v_add_f32 v4, v4, v5\n\t" \
    "v_add_f32 v6, v6, v7\n\t" \
    "v_add_f32 v8, v8, v9\n\t" \
    "v_add_f32 v10, v10, v11\n\t" \
    "v_add_f32 v12, v12, v13\n\t" \
    "v_add_f32 v14, v14, v15\n\t" \
    "v_permlane32_swap_b32 v0, v4\n\t" \
    "v_permlane32_swap_b32 v2, v6\n\t" \
    "v_permlane32_swap_b32 v8, v12\n\t" \
    "v_permlane32_swap_b32 v10, v14\n\t" \
    "v_add_f32 v0, v0, v12\n\t" \
    "v_add_f32 v2, v2, v14\n\t" \
    "ds_write2_b32 %[rw], v0, v2 offset0:0 offset1:32\n\t" \
    "s_setprio 3\n\t" \
    "s_cmp_eq_u32 %[red], 4\n\t" \
    "s_waitcnt lgkmcnt(0)\n\t" \
    "s_barrier\n\t" \
    "s_cbranch_scc0 E_%=\n\t" \
    "ds_read2st64_b64 v[0:3], %[rr] offset0:0 offset1:1\n\t" \
    "ds_read2st64_b64 v[4:7], %[rr] offset0:2 offset1:3\n\t" \
    "ds_read2st64_b64 v[8:11], %[rr] offset0:4 offset1:5\n\t" \
    "ds_read2st64_b64 v[12:15], %[rr] offset0:6 offset1:7\n\t" \
    "s_waitcnt lgkmcnt(3)\n\t" \
    "v_pk_add_f32 v[0:1], v[0:1], v[2:3]\n\t" \
    "s_waitcnt lgkmcnt(2)\n\t" \
    "v_pk_add_f32 v[4:5], v[4:5], v[6:7]\n\t" \
    "s_waitcnt lgkmcnt(1)\n\t" \
    "v_pk_add_f32 v[8:9], v[8:9], v[10:11]\n\t" \
    "v_pk_add_f32 v[0:1], v[0:1], v[4:5]\n\t" \
    "s_waitcnt lgkmcnt(0)\n\t" \
    "v_pk_add_f32 v[12:13], v[12:13], v[14:15]\n\t" \
    "s_nop 0\n\t" \
    "v_pk_add_f32 v[8:9], v[8:9], v[12:13]\n\t" \
    "s_nop 0\n\t" \
    "v_pk_add_f32 v[0:1], v[0:1], v[8:9]\n\t" \
    "s_nop 0\n\t" \
    "v_cvt_pk_bf16_f32 v0, v0, v1\n\t" \
    "v_min_u32 v0, -2, v0\n\t" \
    "buffer_store_dword v0, %[mo], %[rs], %[sn] offen sc1\n\t" \
    "s_memrealtime %[t1]\n\t" \
    DGA \
    "s_waitcnt lgkmcnt(0)\n\t" \
    GSA(2) \
    "s_branch E_%=\n\t" \
    "SL_%=:\n\t" \
    "s_sleep 1\n\t" \
    "buffer_load_dwordx4 v[220:223], %[l16], %[rs], %[s0] offen sc1\n\t" \
    "buffer_load_dwordx4 v[224:227], %[l16], %[rs], %[s1] offen sc1\n\t" \
    "buffer_load_dwordx4 v[228:231], %[l16], %[rs], %[s2] offen sc1\n\t" \
    "buffer_load_dwordx4 v[232:235], %[l16], %[rs], %[s3] offen sc1\n\t" \
    "buffer_load_dwordx4 v[236:239], %[l16], %[rs], %[s4] offen sc1\n\t" \
    "buffer_load_dwordx4 v[240:243], %[l16], %[rs], %[s5] offen sc1\n\t" \
    "s_sub_u32 %[lim], %[lim], " STR(DEC2) "\n\t" \
    "s_cmp_gt_i32 %[lim], 0\n\t" \
    "s_cbranch_scc0 G_%=\n\t" \
    "s_nop 3\n\t" \
    "v_mov_b32 v0, 0\n\t" \
    "v_mov_b32 v1, 0\n\t" \
    "v_mov_b32 v2, 0\n\t" \
    "v_mov_b32 v3, 0\n\t" \
    "v_mov_b32 v4, 0\n\t" \
    "v_mov_b32 v5, 0\n\t" \
    "v_mov_b32 v6, 0\n\t" \
    "v_mov_b32 v7, 0\n\t" \
    "v_mov_b32 v8, 0\n\t" \
    "v_mov_b32 v9, 0\n\t" \
    "v_mov_b32 v10, 0\n\t" \
    "v_mov_b32 v11, 0\n\t" \
    "v_mov_b32 v12, 0\n\t" \
    "v_mov_b32 v13, 0\n\t" \
    "v_mov_b32 v14, 0\n\t" \
    "v_mov_b32 v15, 0\n\t" \
    "v_mov_b32 v252, 0\n\t" \
    "s_branch A_%=\n\t" \
    "G_%=:\n\t" \
    "s_waitcnt vmcnt(0)\n\t" \
    "s_branch D_%=\n\t" \
    "E_%=:\n\t" \
    "s_setprio 0\n\t" \
    GSW \
    : [lim] "+s"(lim) GSO(1) GSO(4), [t1] "=&s"(t1) DGO DGQ GSO(2) \
    : [l16] "v"(l16), [rs] "s"(rs), [idx] "v"(idx), [rw] "v"(rw_a), [s0] "s"(so[0]), [s1] "s"(so[1]), [s2] "s"(so[2]), [s3] "s"(so[3]), [s4] "s"(so[4]), [s5] "s"(so[5]), [rr] "v"(rr_a), [mo] "v"(mo_v), [sn] "s"(sn), [red] "s"(wid), "{v[28:35]}"(wo[0][0][0]), "{v[36:43]}"(wo[0][0][1]), "{v[44:51]}"(wo[0][1][0]), "{v[52:59]}"(wo[0][1][1]), "{v[60:67]}"(wo[1][0][0]), "{v[68:75]}"(wo[1][0][1]), "{v[76:83]}"(wo[1][1][0]), "{v[84:91]}"(wo[1][1][1]), "{v[92:99]}"(wo[2][0][0]), "{v[100:107]}"(wo[2][0][1]), "{v[108:115]}"(wo[2][1][0]), "{v[116:123]}"(wo[2][1][1]), "{v[124:131]}"(wo[3][0][0]), "{v[132:139]}"(wo[3][0][1]), "{v[140:147]}"(wo[3][1][0]), "{v[148:155]}"(wo[3][1][1]), "{v[156:163]}"(wo[4][0][0]), "{v[164:171]}"(wo[4][0][1]), "{v[172:179]}"(wo[4][1][0]), "{v[180:187]}"(wo[4][1][1]), "{v[188:195]}"(wo[5][0][0]), "{v[196:203]}"(wo[5][0][1]), "{v[204:211]}"(wo[5][1][0]), "{v[212:219]}"(wo[5][1][1]) \
    : "memory", "vcc", "scc", "v252", "v0", "v1", "v2", "v3", "v4", "v5", "v6", "v7", "v8", "v9", "v10", "v11", "v12", "v13", "v14", "v15", "v220", "v221", "v222", "v223", "v224", "v225", "v226", "v227", "v228", "v229", "v230", "v231", "v232", "v233", "v234", "v235", "v236", "v237", "v238", "v239", "v240", "v241", "v242", "v243")

#define S_LOOP_K(TE_, FIN_) \
  asm volatile( \
    "s_cmp_eq_u32 %[kh], 0\n\t" \
    "s_cbranch_scc0 L1_%=\n\t" \
    "L0_%=:\n\t" \
    "s_setprio 0\n\t" \
    "s_nop 3\n\t" \
    "ds_write_b128 %[pa], v[196:199] offset:0\n\t" \
    "ds_write_b128 %[pa], v[200:203] offset:1024\n\t" \
    "ds_write_b128 %[pa], v[204:207] offset:2048\n\t" \
    "ds_write_b128 %[pa], v[208:211] offset:3072\n\t" \
    "s_sub_u32 %[a], %[t], 1\n\t" \
    "v_mov_b32 v53, %[tag]\n\t" \
    "v_mov_b32 v52, %[a]\n\t" \
    "s_sub_u32 %[b], %[t], 2\n\t" \
    "s_cmp_lt_u32 %[b], " STR(SQUICK) "\n\t" \
    "s_cselect_b64 exec, %[fm], 0\n\t" \
    "buffer_store_dwordx2 v[52:53], %[l16], %[rs], %[fo] offen sc1\n\t" \
    "s_mov_b64 exec, -1\n\t" \
    "s_add_u32 m0, %[sd], 49152\n\t" \
    "s_add_u32 %[a], %[sx], 81920\n\t" \
    "s_waitcnt lgkmcnt(0)\n\t" \
    "buffer_load_dwordx4 %[l16], %[rs], %[a] offen offset:0 sc1 lds\n\t" \
    "buffer_load_dwordx4 %[l16], %[rs], %[a] offen offset:1024 sc1 lds\n\t" \
    "buffer_load_dwordx4 %[l16], %[rs], %[a] offen offset:2048 sc1 lds\n\t" \
    "buffer_load_dwordx4 %[l16], %[rs], %[a] offen offset:3072 sc1 lds\n\t" \
    "v_mfma_f32_16x16x32_bf16 v[0:3], v[68:71], v[20:23], 0\n\t" \
    "ds_read_b128 v[44:47], %[xp] offset:6144\n\t" \
    "v_mfma_f32_16x16x32_bf16 v[4:7], v[76:79], v[20:23], 0\n\t" \
    "ds_read_b128 v[48:51], %[xp] offset:7168\n\t" \
    "v_mfma_f32_16x16x32_bf16 v[8:11], v[68:71], v[24:27], 0\n\t" \
    "v_mfma_f32_16x16x32_bf16 v[12:15], v[76:79], v[24:27], 0\n\t" \
    "v_mfma_f32_16x16x32_bf16 v[0:3], v[72:75], v[28:31], v[0:3]\n\t" \
    "ds_read_b128 v[20:23], %[xp] offset:8192\n\t" \
    "v_mfma_f32_16x16x32_bf16 v[4:7], v[80:83], v[28:31], v[4:7]\n\t" \
    "ds_read_b128 v[24:27], %[xp] offset:9216\n\t" \
    "v_mfma_f32_16x16x32_bf16 v[8:11], v[72:75], v[32:35], v[8:11]\n\t" \
    "v_mfma_f32_16x16x32_bf16 v[12:15], v[80:83], v[32:35], v[12:15]\n\t" \
    "v_mfma_f32_16x16x32_bf16 v[0:3], v[84:87], v[36:39], v[0:3]\n\t" \
    "ds_read_b128 v[28:31], %[xp] offset:10240\n\t" \
    "v_mfma_f32_16x16x32_bf16 v[4:7], v[92:95], v[36:39], v[4:7]\n\t" \
    "ds_read_b128 v[32:35], %[xp] offset:11264\n\t" \
    "v_mfma_f32_16x16x32_bf16 v[8:11], v[84:87], v[40:43], v[8:11]\n\t" \
    "s_waitcnt lgkmcnt(4)\n\t" \
    "v_mfma_f32_16x16x32_bf16 v[12:15], v[92:95], v[40:43], v[12:15]\n\t" \
    "v_mfma_f32_16x16x32_bf16 v[0:3], v[88:91], v[44:47], v[0:3]\n\t" \
    "ds_read_b128 v[36:39], %[xp] offset:12288\n\t" \
    "v_mfma_f32_16x16x32_bf16 v[4:7], v[96:99], v[44:47], v[4:7]\n\t" \
    "ds_read_b128 v[40:43], %[xp] offset:13312\n\t" \
    "v_mfma_f32_16x16x32_bf16 v[8:11], v[88:91], v[48:51], v[8:11]\n\t" \
    "s_waitcnt lgkmcnt(4)\n\t" \
    "v_mfma_f32_16x16x32_bf16 v[12:15], v[96:99], v[48:51], v[12:15]\n\t" \
    "v_mfma_f32_16x16x32_bf16 v[0:3], v[100:103], v[20:23], v[0:3]\n\t" \
    "ds_read_b128 v[44:47], %[xp] offset:14336\n\t" \
    "v_mfma_f32_16x16x32_bf16 v[4:7], v[108:111], v[20:23], v[4:7]\n\t" \
    "ds_read_b128 v[48:51], %[xp] offset:15360\n\t" \
    "v_mfma_f32_16x16x32_bf16 v[8:11], v[100:103], v[24:27], v[8:11]\n\t" \
    "s_waitcnt lgkmcnt(4)\n\t" \
    "v_mfma_f32_16x16x32_bf16 v[12:15], v[108:111], v[24:27], v[12:15]\n\t" \
    "v_mfma_f32_16x16x32_bf16 v[0:3], v[104:107], v[28:31], v[0:3]\n\t" \
    "ds_read_b128 v[20:23], %[xp] offset:16384\n\t" \
    "v_mfma_f32_16x16x32_bf16 v[4:7], v[112:115], v[28:31], v[4:7]\n\t" \
    "ds_read_b128 v[24:27], %[xp] offset:17408\n\t" \
    "v_mfma_f32_16x16x32_bf16 v[8:11], v[104:107], v[32:35], v[8:11]\n\t" \
    "s_waitcnt lgkmcnt(4)\n\t" \
    "v_mfma_f32_16x16x32_bf16 v[12:15], v[112:115], v[32:35], v[12:15]\n\t" \
    "v_mfma_f32_16x16x32_bf16 v[0:3], v[116:119], v[36:39], v[0:3]\n\t" \
    "ds_read_b128 v[28:31], %[xp] offset:18432\n\t" \
    "v_mfma_f32_16x16x32_bf16 v[4:7], v[124:127], v[36:39], v[4:7]\n\t" \
    "ds_read_b128 v[32:35], %[xp] offset:19456\n\t" \
    "v_mfma_f32_16x16x32_bf16 v[8:11], v[116:119], v[40:43], v[8:11]\n\t" \
    "s_waitcnt lgkmcnt(4)\n\t" \
    "v_mfma_f32_16x16x32_bf16 v[12:15], v[124:127], v[40:43], v[12:15]\n\t" \
    "v_mfma_f32_16x16x32_bf16 v[0:3], v[120:123], v[44:47], v[0:3]\n\t" \
    "ds_read_b128 v[36:39], %[xp] offset:20480\n\t" \
    "v_mfma_f32_16x16x32_bf16 v[4:7], v[128:131], v[44:47], v[4:7]\n\t" \
    "ds_read_b128 v[40:43], %[xp] offset:21504\n\t" \
    "v_mfma_f32_16x16x32_bf16 v[8:11], v[120:123], v[48:51], v[8:11]\n\t" \
    "s_waitcnt lgkmcnt(4)\n\t" \
    "v_mfma_f32_16x16x32_bf16 v[12:15], v[128:131], v[48:51], v[12:15]\n\t" \
    "s_waitcnt vmcnt(4)\n\t" \
    "s_barrier\n\t" \
    "s_setprio 0\n\t" \
    "s_sub_u32 %[a], %[t], 1\n\t" \
    "v_mov_b32 v53, %[tag]\n\t" \
    "v_mov_b32 v52, %[a]\n\t" \
    "s_cmp_gt_u32 %[t], 1\n\t" \
    "s_cselect_b64 exec, %[fm], 0\n\t" \
    "buffer_store_dwordx2 v[52:53], %[l16], %[rs], %[fo] offen sc1\n\t" \
    "s_mov_b64 exec, %[pm]\n\t" \
    "s_mov_b32 m0, %[pd]\n\t" \
    "s_add_u32 %[a], %[sx], %[po]\n\t" \
    "buffer_load_dwordx4 %[l16], %[rs], %[a] offen sc1 lds\n\t" \
    "s_mov_b64 exec, -1\n\t" \
    "s_add_u32 m0, %[sd], 0\n\t" \
    "s_add_u32 %[a], %[sx], 131072\n\t" \
    "buffer_load_dwordx4 %[l16], %[rs], %[a] offen offset:0 sc1 lds\n\t" \
    "buffer_load_dwordx4 %[l16], %[rs], %[a] offen offset:1024 sc1 lds\n\t" \
    "buffer_load_dwordx4 %[l16], %[rs], %[a] offen offset:2048 sc1 lds\n\t" \
    "buffer_load_dwordx4 %[l16], %[rs], %[a] offen offset:3072 sc1 lds\n\t" \
    "v_mfma_f32_16x16x32_bf16 v[0:3], v[132:135], v[20:23], v[0:3]\n\t" \
    "ds_read_b128 v[44:47], %[xp] offset:22528\n\t" \
    "v_mfma_f32_16x16x32_bf16 v[4:7], v[140:143], v[20:23], v[4:7]\n\t" \
    "ds_read_b128 v[48:51], %[xp] offset:23552\n\t" \
    "v_mfma_f32_16x16x32_bf16 v[8:11], v[132:135], v[24:27], v[8:11]\n\t" \
    "s_waitcnt lgkmcnt(4)\n\t" \
    "v_mfma_f32_16x16x32_bf16 v[12:15], v[140:143], v[24:27], v[12:15]\n\t" \
    "v_mfma_f32_16x16x32_bf16 v[0:3], v[136:139], v[28:31], v[0:3]\n\t" \
    "ds_read_b128 v[20:23], %[xp] offset:24576\n\t" \
    "v_mfma_f32_16x16x32_bf16 v[4:7], v[144:147], v[28:31], v[4:7]\n\t" \
    "ds_read_b128 v[24:27], %[xp] offset:25600\n\t" \
    "v_mfma_f32_16x16x32_bf16 v[8:11], v[136:139], v[32:35], v[8:11]\n\t" \
    "s_waitcnt lgkmcnt(4)\n\t" \
    "v_mfma_f32_16x16x32_bf16 v[12:15], v[144:147], v[32:35], v[12:15]\n\t" \
    "v_mfma_f32_16x16x32_bf16 v[0:3], v[148:151], v[36:39], v[0:3]\n\t" \
    "ds_read_b128 v[28:31], %[xp] offset:26624\n\t" \
    "v_mfma_f32_16x16x32_bf16 v[4:7], v[156:159], v[36:39], v[4:7]\n\t" \
    "ds_read_b128 v[32:35], %[xp] offset:27648\n\t" \
    "v_mfma_f32_16x16x32_bf16 v[8:11], v[148:151], v[40:43], v[8:11]\n\t" \
    "s_waitcnt lgkmcnt(4)\n\t" \
    "v_mfma_f32_16x16x32_bf16 v[12:15], v[156:159], v[40:43], v[12:15]\n\t" \
    "v_mfma_f32_16x16x32_bf16 v[0:3], v[152:155], v[44:47], v[0:3]\n\t" \
    "ds_read_b128 v[36:39], %[xp] offset:28672\n\t" \
    "v_mfma_f32_16x16x32_bf16 v[4:7], v[160:163], v[44:47], v[4:7]\n\t" \
    "ds_read_b128 v[40:43], %[xp] offset:29696\n\t" \
    "v_mfma_f32_16x16x32_bf16 v[8:11], v[152:155], v[48:51], v[8:11]\n\t" \
    "s_waitcnt lgkmcnt(4)\n\t" \
    "v_mfma_f32_16x16x32_bf16 v[12:15], v[160:163], v[48:51], v[12:15]\n\t" \
    "v_mfma_f32_16x16x32_bf16 v[0:3], v[164:167], v[20:23], v[0:3]\n\t" \
    "ds_read_b128 v[44:47], %[xp] offset:30720\n\t" \
    "v_mfma_f32_16x16x32_bf16 v[4:7], v[172:175], v[20:23], v[4:7]\n\t" \
    "ds_read_b128 v[48:51], %[xp] offset:31744\n\t" \
    "v_mfma_f32_16x16x32_bf16 v[8:11], v[164:167], v[24:27], v[8:11]\n\t" \
    "s_waitcnt lgkmcnt(4)\n\t" \
    "v_mfma_f32_16x16x32_bf16 v[12:15], v[172:175], v[24:27], v[12:15]\n\t" \
    "v_mfma_f32_16x16x32_bf16 v[0:3], v[168:171], v[28:31], v[0:3]\n\t" \
    "ds_read_b128 v[20:23], %[xp] offset:32768\n\t" \
    "v_mfma_f32_16x16x32_bf16 v[4:7], v[176:179], v[28:31], v[4:7]\n\t" \
    "ds_read_b128 v[24:27], %[xp] offset:33792\n\t" \
    "v_mfma_f32_16x16x32_bf16 v[8:11], v[168:171], v[32:35], v[8:11]\n\t" \
    "s_waitcnt lgkmcnt(4)\n\t" \
    "v_mfma_f32_16x16x32_bf16 v[12:15], v[176:179], v[32:35], v[12:15]\n\t" \
    "v_mfma_f32_16x16x32_bf16 v[0:3], v[180:183], v[36:39], v[0:3]\n\t" \
    "ds_read_b128 v[28:31], %[xp] offset:34816\n\t" \
    "v_mfma_f32_16x16x32_bf16 v[4:7], v[188:191], v[36:39], v[4:7]\n\t" \
    "ds_read_b128 v[32:35], %[xp] offset:35840\n\t" \
    "v_mfma_f32_16x16x32_bf16 v[8:11], v[180:183], v[40:43], v[8:11]\n\t" \
    "s_waitcnt lgkmcnt(4)\n\t" \
    "v_mfma_f32_16x16x32_bf16 v[12:15], v[188:191], v[40:43], v[12:15]\n\t" \
    "v_mfma_f32_16x16x32_bf16 v[0:3], v[184:187], v[44:47], v[0:3]\n\t" \
    "ds_read_b128 v[36:39], %[xp] offset:36864\n\t" \
    "v_mfma_f32_16x16x32_bf16 v[4:7], v[192:195], v[44:47], v[4:7]\n\t" \
    "ds_read_b128 v[40:43], %[xp] offset:37888\n\t" \
    "v_mfma_f32_16x16x32_bf16 v[8:11], v[184:187], v[48:51], v[8:11]\n\t" \
    "s_waitcnt lgkmcnt(4)\n\t" \
    "v_mfma_f32_16x16x32_bf16 v[12:15], v[192:195], v[48:51], v[12:15]\n\t" \
    "s_add_u32 %[sx], %[sx], 65536\n\t" \
    "s_add_u32 %[t], %[t], 1\n\t" \
    "s_waitcnt vmcnt(4)\n\t" \
    "s_barrier\n\t" \
    "s_setprio 0\n\t" \
    "s_nop 3\n\t" \
    "ds_write_b128 %[pa], v[0:3] offset:0\n\t" \
    "ds_write_b128 %[pa], v[4:7] offset:1024\n\t" \
    "ds_write_b128 %[pa], v[8:11] offset:2048\n\t" \
    "ds_write_b128 %[pa], v[12:15] offset:3072\n\t" \
    "s_sub_u32 %[a], %[t], 1\n\t" \
    "v_mov_b32 v53, %[tag]\n\t" \
    "v_mov_b32 v52, %[a]\n\t" \
    "s_sub_u32 %[b], %[t], 2\n\t" \
    "s_cmp_lt_u32 %[b], " STR(SQUICK) "\n\t" \
    "s_cselect_b64 exec, %[fm], 0\n\t" \
    "buffer_store_dwordx2 v[52:53], %[l16], %[rs], %[fo] offen sc1\n\t" \
    "s_mov_b64 exec, -1\n\t" \
    "s_add_u32 m0, %[sd], 16384\n\t" \
    "s_add_u32 %[a], %[sx], 81920\n\t" \
    "s_waitcnt lgkmcnt(0)\n\t" \
    "buffer_load_dwordx4 %[l16], %[rs], %[a] offen offset:0 sc1 lds\n\t" \
    "buffer_load_dwordx4 %[l16], %[rs], %[a] offen offset:1024 sc1 lds\n\t" \
    "buffer_load_dwordx4 %[l16], %[rs], %[a] offen offset:2048 sc1 lds\n\t" \
    "buffer_load_dwordx4 %[l16], %[rs], %[a] offen offset:3072 sc1 lds\n\t" \
    "v_mfma_f32_16x16x32_bf16 v[196:199], v[68:71], v[20:23], 0\n\t" \
    "ds_read_b128 v[44:47], %[xp] offset:38912\n\t" \
    "v_mfma_f32_16x16x32_bf16 v[200:203], v[76:79], v[20:23], 0\n\t" \
    "ds_read_b128 v[48:51], %[xp] offset:39936\n\t" \
    "v_mfma_f32_16x16x32_bf16 v[204:207], v[68:71], v[24:27], 0\n\t" \
    "v_mfma_f32_16x16x32_bf16 v[208:211], v[76:79], v[24:27], 0\n\t" \
    "v_mfma_f32_16x16x32_bf16 v[196:199], v[72:75], v[28:31], v[196:199]\n\t" \
    "ds_read_b128 v[20:23], %[xp] offset:40960\n\t" \
    "v_mfma_f32_16x16x32_bf16 v[200:203], v[80:83], v[28:31], v[200:203]\n\t" \
    "ds_read_b128 v[24:27], %[xp] offset:41984\n\t" \
    "v_mfma_f32_16x16x32_bf16 v[204:207], v[72:75], v[32:35], v[204:207]\n\t" \
    "v_mfma_f32_16x16x32_bf16 v[208:211], v[80:83], v[32:35], v[208:211]\n\t" \
    "v_mfma_f32_16x16x32_bf16 v[196:199], v[84:87], v[36:39], v[196:199]\n\t" \
    "ds_read_b128 v[28:31], %[xp] offset:43008\n\t" \
    "v_mfma_f32_16x16x32_bf16 v[200:203], v[92:95], v[36:39], v[200:203]\n\t" \
    "ds_read_b128 v[32:35], %[xp] offset:44032\n\t" \
    "v_mfma_f32_16x16x32_bf16 v[204:207], v[84:87], v[40:43], v[204:207]\n\t" \
    "s_waitcnt lgkmcnt(4)\n\t" \
    "v_mfma_f32_16x16x32_bf16 v[208:211], v[92:95], v[40:43], v[208:211]\n\t" \
    "v_mfma_f32_16x16x32_bf16 v[196:199], v[88:91], v[44:47], v[196:199]\n\t" \
    "ds_read_b128 v[36:39], %[xp] offset:45056\n\t" \
    "v_mfma_f32_16x16x32_bf16 v[200:203], v[96:99], v[44:47], v[200:203]\n\t" \
    "ds_read_b128 v[40:43], %[xp] offset:46080\n\t" \
    "v_mfma_f32_16x16x32_bf16 v[204:207], v[88:91], v[48:51], v[204:207]\n\t" \
    "s_waitcnt lgkmcnt(4)\n\t" \
    "v_mfma_f32_16x16x32_bf16 v[208:211], v[96:99], v[48:51], v[208:211]\n\t" \
    "v_mfma_f32_16x16x32_bf16 v[196:199], v[100:103], v[20:23], v[196:199]\n\t" \
    "ds_read_b128 v[44:47], %[xp] offset:47104\n\t" \
    "v_mfma_f32_16x16x32_bf16 v[200:203], v[108:111], v[20:23], v[200:203]\n\t" \
    "ds_read_b128 v[48:51], %[xp] offset:48128\n\t" \
    "v_mfma_f32_16x16x32_bf16 v[204:207], v[100:103], v[24:27], v[204:207]\n\t" \
    "s_waitcnt lgkmcnt(4)\n\t" \
    "v_mfma_f32_16x16x32_bf16 v[208:211], v[108:111], v[24:27], v[208:211]\n\t" \
    "v_mfma_f32_16x16x32_bf16 v[196:199], v[104:107], v[28:31], v[196:199]\n\t" \
    "ds_read_b128 v[20:23], %[xp] offset:49152\n\t" \
    "v_mfma_f32_16x16x32_bf16 v[200:203], v[112:115], v[28:31], v[200:203]\n\t" \
    "ds_read_b128 v[24:27], %[xp] offset:50176\n\t" \
    "v_mfma_f32_16x16x32_bf16 v[204:207], v[104:107], v[32:35], v[204:207]\n\t" \
    "s_waitcnt lgkmcnt(4)\n\t" \
    "v_mfma_f32_16x16x32_bf16 v[208:211], v[112:115], v[32:35], v[208:211]\n\t" \
    "v_mfma_f32_16x16x32_bf16 v[196:199], v[116:119], v[36:39], v[196:199]\n\t" \
    "ds_read_b128 v[28:31], %[xp] offset:51200\n\t" \
    "v_mfma_f32_16x16x32_bf16 v[200:203], v[124:127], v[36:39], v[200:203]\n\t" \
    "ds_read_b128 v[32:35], %[xp] offset:52224\n\t" \
    "v_mfma_f32_16x16x32_bf16 v[204:207], v[116:119], v[40:43], v[204:207]\n\t" \
    "s_waitcnt lgkmcnt(4)\n\t" \
    "v_mfma_f32_16x16x32_bf16 v[208:211], v[124:127], v[40:43], v[208:211]\n\t" \
    "v_mfma_f32_16x16x32_bf16 v[196:199], v[120:123], v[44:47], v[196:199]\n\t" \
    "ds_read_b128 v[36:39], %[xp] offset:53248\n\t" \
    "v_mfma_f32_16x16x32_bf16 v[200:203], v[128:131], v[44:47], v[200:203]\n\t" \
    "ds_read_b128 v[40:43], %[xp] offset:54272\n\t" \
    "v_mfma_f32_16x16x32_bf16 v[204:207], v[120:123], v[48:51], v[204:207]\n\t" \
    "s_waitcnt lgkmcnt(4)\n\t" \
    "v_mfma_f32_16x16x32_bf16 v[208:211], v[128:131], v[48:51], v[208:211]\n\t" \
    "s_waitcnt vmcnt(4)\n\t" \
    "s_barrier\n\t" \
    "s_setprio 0\n\t" \
    "s_sub_u32 %[a], %[t], 1\n\t" \
    "v_mov_b32 v53, %[tag]\n\t" \
    "v_mov_b32 v52, %[a]\n\t" \
    "s_cmp_gt_u32 %[t], 1\n\t" \
    "s_cselect_b64 exec, %[fm], 0\n\t" \
    "buffer_store_dwordx2 v[52:53], %[l16], %[rs], %[fo] offen sc1\n\t" \
    "s_mov_b64 exec, %[pm]\n\t" \
    "s_mov_b32 m0, %[pd]\n\t" \
    "s_add_u32 %[a], %[sx], %[po]\n\t" \
    "buffer_load_dwordx4 %[l16], %[rs], %[a] offen sc1 lds\n\t" \
    "s_mov_b64 exec, -1\n\t" \
    "s_add_u32 m0, %[sd], 32768\n\t" \
    "s_add_u32 %[a], %[sx], 131072\n\t" \
    "buffer_load_dwordx4 %[l16], %[rs], %[a] offen offset:0 sc1 lds\n\t" \
    "buffer_load_dwordx4 %[l16], %[rs], %[a] offen offset:1024 sc1 lds\n\t" \
    "buffer_load_dwordx4 %[l16], %[rs], %[a] offen offset:2048 sc1 lds\n\t" \
    "buffer_load_dwordx4 %[l16], %[rs], %[a] offen offset:3072 sc1 lds\n\t" \
    "v_mfma_f32_16x16x32_bf16 v[196:199], v[132:135], v[20:23], v[196:199]\n\t" \
    "ds_read_b128 v[44:47], %[xp] offset:55296\n\t" \
    "v_mfma_f32_16x16x32_bf16 v[200:203], v[140:143], v[20:23], v[200:203]\n\t" \
    "ds_read_b128 v[48:51], %[xp] offset:56320\n\t" \
    "v_mfma_f32_16x16x32_bf16 v[204:207], v[132:135], v[24:27], v[204:207]\n\t" \
    "s_waitcnt lgkmcnt(4)\n\t" \
    "v_mfma_f32_16x16x32_bf16 v[208:211], v[140:143], v[24:27], v[208:211]\n\t" \
    "v_mfma_f32_16x16x32_bf16 v[196:199], v[136:139], v[28:31], v[196:199]\n\t" \
    "ds_read_b128 v[20:23], %[xp] offset:57344\n\t" \
    "v_mfma_f32_16x16x32_bf16 v[200:203], v[144:147], v[28:31], v[200:203]\n\t" \
    "ds_read_b128 v[24:27], %[xp] offset:58368\n\t" \
    "v_mfma_f32_16x16x32_bf16 v[204:207], v[136:139], v[32:35], v[204:207]\n\t" \
    "s_waitcnt lgkmcnt(4)\n\t" \
    "v_mfma_f32_16x16x32_bf16 v[208:211], v[144:147], v[32:35], v[208:211]\n\t" \
    "v_mfma_f32_16x16x32_bf16 v[196:199], v[148:151], v[36:39], v[196:199]\n\t" \
    "ds_read_b128 v[28:31], %[xp] offset:59392\n\t" \
    "v_mfma_f32_16x16x32_bf16 v[200:203], v[156:159], v[36:39], v[200:203]\n\t" \
    "ds_read_b128 v[32:35], %[xp] offset:60416\n\t" \
    "v_mfma_f32_16x16x32_bf16 v[204:207], v[148:151], v[40:43], v[204:207]\n\t" \
    "s_waitcnt lgkmcnt(4)\n\t" \
    "v_mfma_f32_16x16x32_bf16 v[208:211], v[156:159], v[40:43], v[208:211]\n\t" \
    "v_mfma_f32_16x16x32_bf16 v[196:199], v[152:155], v[44:47], v[196:199]\n\t" \
    "ds_read_b128 v[36:39], %[xp] offset:61440\n\t" \
    "v_mfma_f32_16x16x32_bf16 v[200:203], v[160:163], v[44:47], v[200:203]\n\t" \
    "ds_read_b128 v[40:43], %[xp] offset:62464\n\t" \
    "v_mfma_f32_16x16x32_bf16 v[204:207], v[152:155], v[48:51], v[204:207]\n\t" \
    "s_waitcnt lgkmcnt(4)\n\t" \
    "v_mfma_f32_16x16x32_bf16 v[208:211], v[160:163], v[48:51], v[208:211]\n\t" \
    "v_mfma_f32_16x16x32_bf16 v[196:199], v[164:167], v[20:23], v[196:199]\n\t" \
    "ds_read_b128 v[44:47], %[xp] offset:63488\n\t" \
    "v_mfma_f32_16x16x32_bf16 v[200:203], v[172:175], v[20:23], v[200:203]\n\t" \
    "ds_read_b128 v[48:51], %[xp] offset:64512\n\t" \
    "v_mfma_f32_16x16x32_bf16 v[204:207], v[164:167], v[24:27], v[204:207]\n\t" \
    "s_waitcnt lgkmcnt(4)\n\t" \
    "v_mfma_f32_16x16x32_bf16 v[208:211], v[172:175], v[24:27], v[208:211]\n\t" \
    "v_mfma_f32_16x16x32_bf16 v[196:199], v[168:171], v[28:31], v[196:199]\n\t" \
    "ds_read_b128 v[20:23], %[xp] offset:0\n\t" \
    "v_mfma_f32_16x16x32_bf16 v[200:203], v[176:179], v[28:31], v[200:203]\n\t" \
    "ds_read_b128 v[24:27], %[xp] offset:1024\n\t" \
    "v_mfma_f32_16x16x32_bf16 v[204:207], v[168:171], v[32:35], v[204:207]\n\t" \
    "s_waitcnt lgkmcnt(4)\n\t" \
    "v_mfma_f32_16x16x32_bf16 v[208:211], v[176:179], v[32:35], v[208:211]\n\t" \
    "v_mfma_f32_16x16x32_bf16 v[196:199], v[180:183], v[36:39], v[196:199]\n\t" \
    "ds_read_b128 v[28:31], %[xp] offset:2048\n\t" \
    "v_mfma_f32_16x16x32_bf16 v[200:203], v[188:191], v[36:39], v[200:203]\n\t" \
    "ds_read_b128 v[32:35], %[xp] offset:3072\n\t" \
    "v_mfma_f32_16x16x32_bf16 v[204:207], v[180:183], v[40:43], v[204:207]\n\t" \
    "s_waitcnt lgkmcnt(4)\n\t" \
    "v_mfma_f32_16x16x32_bf16 v[208:211], v[188:191], v[40:43], v[208:211]\n\t" \
    "v_mfma_f32_16x16x32_bf16 v[196:199], v[184:187], v[44:47], v[196:199]\n\t" \
    "ds_read_b128 v[36:39], %[xp] offset:4096\n\t" \
    "v_mfma_f32_16x16x32_bf16 v[200:203], v[192:195], v[44:47], v[200:203]\n\t" \
    "ds_read_b128 v[40:43], %[xp] offset:5120\n\t" \
    "v_mfma_f32_16x16x32_bf16 v[204:207], v[184:187], v[48:51], v[204:207]\n\t" \
    "s_waitcnt lgkmcnt(4)\n\t" \
    "v_mfma_f32_16x16x32_bf16 v[208:211], v[192:195], v[48:51], v[208:211]\n\t" \
    "s_add_u32 %[sx], %[sx], 65536\n\t" \
    "s_add_u32 %[t], %[t], 1\n\t" \
    "s_waitcnt vmcnt(4)\n\t" \
    "s_barrier\n\t" \
    "s_cmp_lt_u32 %[t], %[te]\n\t" \
    "s_cbranch_scc1 L0_%=\n\t" \
    "s_cmp_eq_u32 %[fin], %[te]\n\t" \
    "s_cbranch_scc0 E_%=\n\t" \
    "s_nop 4\n\t" \
    "ds_write_b128 %[pa], v[196:199] offset:0\n\t" \
    "ds_write_b128 %[pa], v[200:203] offset:1024\n\t" \
    "ds_write_b128 %[pa], v[204:207] offset:2048\n\t" \
    "ds_write_b128 %[pa], v[208:211] offset:3072\n\t" \
    "s_waitcnt lgkmcnt(0)\n\t" \
    "s_barrier\n\t" \
    "s_branch E_%=\n\t" \
    "L1_%=:\n\t" \
    "s_setprio 1\n\t" \
    "s_add_u32 m0, %[sd], 49152\n\t" \
    "s_add_u32 %[a], %[sx], 81920\n\t" \
    "v_mfma_f32_16x16x32_bf16 v[0:3], v[68:71], v[20:23], 0\n\t" \
    "ds_read_b128 v[44:47], %[xp] offset:6144\n\t" \
    "v_mfma_f32_16x16x32_bf16 v[4:7], v[76:79], v[20:23], 0\n\t" \
    "ds_read_b128 v[48:51], %[xp] offset:7168\n\t" \
    "v_mfma_f32_16x16x32_bf16 v[8:11], v[68:71], v[24:27], 0\n\t" \
    "s_waitcnt lgkmcnt(4)\n\t" \
    "v_mfma_f32_16x16x32_bf16 v[12:15], v[76:79], v[24:27], 0\n\t" \
    "v_mfma_f32_16x16x32_bf16 v[0:3], v[72:75], v[28:31], v[0:3]\n\t" \
    "ds_read_b128 v[20:23], %[xp] offset:8192\n\t" \
    "v_mfma_f32_16x16x32_bf16 v[4:7], v[80:83], v[28:31], v[4:7]\n\t" \
    "ds_read_b128 v[24:27], %[xp] offset:9216\n\t" \
    "v_mfma_f32_16x16x32_bf16 v[8:11], v[72:75], v[32:35], v[8:11]\n\t" \
    "s_waitcnt lgkmcnt(4)\n\t" \
    "v_mfma_f32_16x16x32_bf16 v[12:15], v[80:83], v[32:35], v[12:15]\n\t" \
    "v_mfma_f32_16x16x32_bf16 v[0:3], v[84:87], v[36:39], v[0:3]\n\t" \
    "ds_read_b128 v[28:31], %[xp] offset:10240\n\t" \
    "v_mfma_f32_16x16x32_bf16 v[4:7], v[92:95], v[36:39], v[4:7]\n\t" \
    "ds_read_b128 v[32:35], %[xp] offset:11264\n\t" \
    "v_mfma_f32_16x16x32_bf16 v[8:11], v[84:87], v[40:43], v[8:11]\n\t" \
    "s_waitcnt lgkmcnt(4)\n\t" \
    "v_mfma_f32_16x16x32_bf16 v[12:15], v[92:95], v[40:43], v[12:15]\n\t" \
    "v_mfma_f32_16x16x32_bf16 v[0:3], v[88:91], v[44:47], v[0:3]\n\t" \
    "ds_read_b128 v[36:39], %[xp] offset:12288\n\t" \
    "v_mfma_f32_16x16x32_bf16 v[4:7], v[96:99], v[44:47], v[4:7]\n\t" \
    "ds_read_b128 v[40:43], %[xp] offset:13312\n\t" \
    "v_mfma_f32_16x16x32_bf16 v[8:11], v[88:91], v[48:51], v[8:11]\n\t" \
    "s_waitcnt lgkmcnt(4)\n\t" \
    "v_mfma_f32_16x16x32_bf16 v[12:15], v[96:99], v[48:51], v[12:15]\n\t" \
    "v_mfma_f32_16x16x32_bf16 v[0:3], v[100:103], v[20:23], v[0:3]\n\t" \
    "ds_read_b128 v[44:47], %[xp] offset:14336\n\t" \
    "v_mfma_f32_16x16x32_bf16 v[4:7], v[108:111], v[20:23], v[4:7]\n\t" \
    "ds_read_b128 v[48:51], %[xp] offset:15360\n\t" \
    "v_mfma_f32_16x16x32_bf16 v[8:11], v[100:103], v[24:27], v[8:11]\n\t" \
    "s_waitcnt lgkmcnt(4)\n\t" \
    "v_mfma_f32_16x16x32_bf16 v[12:15], v[108:111], v[24:27], v[12:15]\n\t" \
    "buffer_load_dwordx4 %[l16], %[rs], %[a] offen offset:0 sc1 lds\n\t" \
    "v_mfma_f32_16x16x32_bf16 v[0:3], v[104:107], v[28:31], v[0:3]\n\t" \
    "ds_read_b128 v[20:23], %[xp] offset:16384\n\t" \
    "v_mfma_f32_16x16x32_bf16 v[4:7], v[112:115], v[28:31], v[4:7]\n\t" \
    "ds_read_b128 v[24:27], %[xp] offset:17408\n\t" \
    "v_mfma_f32_16x16x32_bf16 v[8:11], v[104:107], v[32:35], v[8:11]\n\t" \
    "s_waitcnt lgkmcnt(4)\n\t" \
    "v_mfma_f32_16x16x32_bf16 v[12:15], v[112:115], v[32:35], v[12:15]\n\t" \
    "buffer_load_dwordx4 %[l16], %[rs], %[a] offen offset:1024 sc1 lds\n\t" \
    "v_mfma_f32_16x16x32_bf16 v[0:3], v[116:119], v[36:39], v[0:3]\n\t" \
    "ds_read_b128 v[28:31], %[xp] offset:18432\n\t" \
    "v_mfma_f32_16x16x32_bf16 v[4:7], v[124:127], v[36:39], v[4:7]\n\t" \
    "ds_read_b128 v[32:35], %[xp] offset:19456\n\t" \
    "v_mfma_f32_16x16x32_bf16 v[8:11], v[116:119], v[40:43], v[8:11]\n\t" \
    "s_waitcnt lgkmcnt(4)\n\t" \
    "v_mfma_f32_16x16x32_bf16 v[12:15], v[124:127], v[40:43], v[12:15]\n\t" \
    "buffer_load_dwordx4 %[l16], %[rs], %[a] offen offset:2048 sc1 lds\n\t" \
    "v_mfma_f32_16x16x32_bf16 v[0:3], v[120:123], v[44:47], v[0:3]\n\t" \
    "ds_read_b128 v[36:39], %[xp] offset:20480\n\t" \
    "v_mfma_f32_16x16x32_bf16 v[4:7], v[128:131], v[44:47], v[4:7]\n\t" \
    "ds_read_b128 v[40:43], %[xp] offset:21504\n\t" \
    "v_mfma_f32_16x16x32_bf16 v[8:11], v[120:123], v[48:51], v[8:11]\n\t" \
    "s_waitcnt lgkmcnt(4)\n\t" \
    "v_mfma_f32_16x16x32_bf16 v[12:15], v[128:131], v[48:51], v[12:15]\n\t" \
    "buffer_load_dwordx4 %[l16], %[rs], %[a] offen offset:3072 sc1 lds\n\t" \
    "s_waitcnt vmcnt(4)\n\t" \
    "s_barrier\n\t" \
    "s_setprio 1\n\t" \
    "s_add_u32 m0, %[sd], 0\n\t" \
    "s_add_u32 %[a], %[sx], 131072\n\t" \
    "s_add_u32 %[b], %[oo], 0x1000\n\t" \
    "s_cmp_lg_u32 %[t], 1\n\t" \
    "s_cselect_b64 %[em], -1, 0\n\t" \
    "v_mfma_f32_16x16x32_bf16 v[0:3], v[132:135], v[20:23], v[0:3]\n\t" \
    "ds_read_b128 v[44:47], %[xp] offset:22528\n\t" \
    "v_mfma_f32_16x16x32_bf16 v[4:7], v[140:143], v[20:23], v[4:7]\n\t" \
    "ds_read_b128 v[48:51], %[xp] offset:23552\n\t" \
    "v_mfma_f32_16x16x32_bf16 v[8:11], v[132:135], v[24:27], v[8:11]\n\t" \
    "ds_read_b128 v[52:55], %[pa] offset:0\n\t" \
    "s_waitcnt lgkmcnt(5)\n\t" \
    "v_mfma_f32_16x16x32_bf16 v[12:15], v[140:143], v[24:27], v[12:15]\n\t" \
    "buffer_load_dwordx4 %[l16], %[rs], %[a] offen offset:0 sc1 lds\n\t" \
    "v_mfma_f32_16x16x32_bf16 v[0:3], v[136:139], v[28:31], v[0:3]\n\t" \
    "ds_read_b128 v[20:23], %[xp] offset:24576\n\t" \
    "v_mfma_f32_16x16x32_bf16 v[4:7], v[144:147], v[28:31], v[4:7]\n\t" \
    "ds_read_b128 v[24:27], %[xp] offset:25600\n\t" \
    "v_mfma_f32_16x16x32_bf16 v[8:11], v[136:139], v[32:35], v[8:11]\n\t" \
    "ds_read_b128 v[56:59], %[pa] offset:1024\n\t" \
    "s_waitcnt lgkmcnt(6)\n\t" \
    "v_mfma_f32_16x16x32_bf16 v[12:15], v[144:147], v[32:35], v[12:15]\n\t" \
    "buffer_load_dwordx4 %[l16], %[rs], %[a] offen offset:1024 sc1 lds\n\t" \
    "v_mfma_f32_16x16x32_bf16 v[0:3], v[148:151], v[36:39], v[0:3]\n\t" \
    "ds_read_b128 v[28:31], %[xp] offset:26624\n\t" \
    "v_mfma_f32_16x16x32_bf16 v[4:7], v[156:159], v[36:39], v[4:7]\n\t" \
    "ds_read_b128 v[32:35], %[xp] offset:27648\n\t" \
    "v_mfma_f32_16x16x32_bf16 v[8:11], v[148:151], v[40:43], v[8:11]\n\t" \
    "ds_read_b128 v[60:63], %[pa] offset:2048\n\t" \
    "s_waitcnt lgkmcnt(7)\n\t" \
    "v_mfma_f32_16x16x32_bf16 v[12:15], v[156:159], v[40:43], v[12:15]\n\t" \
    "buffer_load_dwordx4 %[l16], %[rs], %[a] offen offset:2048 sc1 lds\n\t" \
    "v_mfma_f32_16x16x32_bf16 v[0:3], v[152:155], v[44:47], v[0:3]\n\t" \
    "ds_read_b128 v[36:39], %[xp] offset:28672\n\t" \
    "s_waitcnt lgkmcnt(7)\n\t" \
    "v_add_f32 v196, v196, v52\n\t" \
    "v_mfma_f32_16x16x32_bf16 v[4:7], v[160:163], v[44:47], v[4:7]\n\t" \
    "ds_read_b128 v[40:43], %[xp] offset:29696\n\t" \
    "v_add_f32 v197, v197, v53\n\t" \
    "v_mfma_f32_16x16x32_bf16 v[8:11], v[152:155], v[48:51], v[8:11]\n\t" \
    "ds_read_b128 v[64:67], %[pa] offset:3072\n\t" \
    "s_waitcnt lgkmcnt(7)\n\t" \
    "v_mfma_f32_16x16x32_bf16 v[12:15], v[160:163], v[48:51], v[12:15]\n\t" \
    "buffer_load_dwordx4 %[l16], %[rs], %[a] offen offset:3072 sc1 lds\n\t" \
    "v_add_f32 v198, v198, v54\n\t" \
    "v_add_f32 v199, v199, v55\n\t" \
    "v_mfma_f32_16x16x32_bf16 v[0:3], v[164:167], v[20:23], v[0:3]\n\t" \
    "ds_read_b128 v[44:47], %[xp] offset:30720\n\t" \
    "s_waitcnt lgkmcnt(7)\n\t" \
    "v_add_f32 v200, v200, v56\n\t" \
    "v_mfma_f32_16x16x32_bf16 v[4:7], v[172:175], v[20:23], v[4:7]\n\t" \
    "ds_read_b128 v[48:51], %[xp] offset:31744\n\t" \
    "v_add_f32 v201, v201, v57\n\t" \
    "v_mfma_f32_16x16x32_bf16 v[8:11], v[164:167], v[24:27], v[8:11]\n\t" \
    "v_add_f32 v202, v202, v58\n\t" \
    "s_waitcnt lgkmcnt(6)\n\t" \
    "v_mfma_f32_16x16x32_bf16 v[12:15], v[172:175], v[24:27], v[12:15]\n\t" \
    "v_add_f32 v203, v203, v59\n\t" \
    "s_waitcnt lgkmcnt(5)\n\t" \
    "v_add_f32 v204, v204, v60\n\t" \
    "v_add_f32 v205, v205, v61\n\t" \
    "v_mfma_f32_16x16x32_bf16 v[0:3], v[168:171], v[28:31], v[0:3]\n\t" \
    "ds_read_b128 v[20:23], %[xp] offset:32768\n\t" \
    "v_add_f32 v206, v206, v62\n\t" \
    "v_mfma_f32_16x16x32_bf16 v[4:7], v[176:179], v[28:31], v[4:7]\n\t" \
    "ds_read_b128 v[24:27], %[xp] offset:33792\n\t" \
    "v_add_f32 v207, v207, v63\n\t" \
    "v_mfma_f32_16x16x32_bf16 v[8:11], v[168:171], v[32:35], v[8:11]\n\t" \
    "s_waitcnt lgkmcnt(4)\n\t" \
    "v_add_f32 v208, v208, v64\n\t" \
    "v_mfma_f32_16x16x32_bf16 v[12:15], v[176:179], v[32:35], v[12:15]\n\t" \
    "v_add_f32 v209, v209, v65\n\t" \
    "v_add_f32 v210, v210, v66\n\t" \
    "v_add_f32 v211, v211, v67\n\t" \
    "v_mfma_f32_16x16x32_bf16 v[0:3], v[180:183], v[36:39], v[0:3]\n\t" \
    "ds_read_b128 v[28:31], %[xp] offset:34816\n\t" \
    "v_cvt_pk_bf16_f32 v52, v196, v200\n\t" \
    "v_mfma_f32_16x16x32_bf16 v[4:7], v[188:191], v[36:39], v[4:7]\n\t" \
    "ds_read_b128 v[32:35], %[xp] offset:35840\n\t" \
    "v_cvt_pk_bf16_f32 v53, v197, v201\n\t" \
    "v_mfma_f32_16x16x32_bf16 v[8:11], v[180:183], v[40:43], v[8:11]\n\t" \
    "v_cvt_pk_bf16_f32 v54, v198, v202\n\t" \
    "s_waitcnt lgkmcnt(4)\n\t" \
    "v_mfma_f32_16x16x32_bf16 v[12:15], v[188:191], v[40:43], v[12:15]\n\t" \
    "v_cvt_pk_bf16_f32 v55, v199, v203\n\t" \
    "v_cvt_pk_bf16_f32 v56, v204, v208\n\t" \
    "v_cvt_pk_bf16_f32 v57, v205, v209\n\t" \
    "v_mfma_f32_16x16x32_bf16 v[0:3], v[184:187], v[44:47], v[0:3]\n\t" \
    "ds_read_b128 v[36:39], %[xp] offset:36864\n\t" \
    "v_cvt_pk_bf16_f32 v58, v206, v210\n\t" \
    "v_mfma_f32_16x16x32_bf16 v[4:7], v[192:195], v[44:47], v[4:7]\n\t" \
    "ds_read_b128 v[40:43], %[xp] offset:37888\n\t" \
    "v_cvt_pk_bf16_f32 v59, v207, v211\n\t" \
    "v_mfma_f32_16x16x32_bf16 v[8:11], v[184:187], v[48:51], v[8:11]\n\t" \
    "s_waitcnt lgkmcnt(4)\n\t" \
    "v_mfma_f32_16x16x32_bf16 v[12:15], v[192:195], v[48:51], v[12:15]\n\t" \
    "s_mov_b64 exec, %[em]\n\t" \
    "buffer_store_dwordx4 v[52:55], %[l16], %[rs], %[oo] offen sc1\n\t" \
    "buffer_store_dwordx4 v[56:59], %[l16], %[rs], %[b] offen sc1\n\t" \
    "s_mov_b64 exec, -1\n\t" \
    "s_add_u32 %[sx], %[sx], 65536\n\t" \
    "s_add_u32 %[t], %[t], 1\n\t" \
    "s_add_u32 %[oo], %[oo], 262144\n\t" \
    "s_waitcnt vmcnt(6)\n\t" \
    "s_cmp_gt_u32 %[t], " STR(SQUICK) " + 1\n\t" \
    "s_cbranch_scc1 B0_%=\n\t" \
    "s_waitcnt vmcnt(0)\n\t" \
    "B0_%=:\n\t" \
    "s_barrier\n\t" \
    "s_setprio 1\n\t" \
    "s_add_u32 m0, %[sd], 16384\n\t" \
    "s_add_u32 %[a], %[sx], 81920\n\t" \
    "v_mfma_f32_16x16x32_bf16 v[196:199], v[68:71], v[20:23], 0\n\t" \
    "ds_read_b128 v[44:47], %[xp] offset:38912\n\t" \
    "v_mfma_f32_16x16x32_bf16 v[200:203], v[76:79], v[20:23], 0\n\t" \
    "ds_read_b128 v[48:51], %[xp] offset:39936\n\t" \
    "v_mfma_f32_16x16x32_bf16 v[204:207], v[68:71], v[24:27], 0\n\t" \
    "s_waitcnt lgkmcnt(4)\n\t" \
    "v_mfma_f32_16x16x32_bf16 v[208:211], v[76:79], v[24:27], 0\n\t" \
    "v_mfma_f32_16x16x32_bf16 v[196:199], v[72:75], v[28:31], v[196:199]\n\t" \
    "ds_read_b128 v[20:23], %[xp] offset:40960\n\t" \
    "v_mfma_f32_16x16x32_bf16 v[200:203], v[80:83], v[28:31], v[200:203]\n\t" \
    "ds_read_b128 v[24:27], %[xp] offset:41984\n\t" \
    "v_mfma_f32_16x16x32_bf16 v[204:207], v[72:75], v[32:35], v[204:207]\n\t" \
    "s_waitcnt lgkmcnt(4)\n\t" \
    "v_mfma_f32_16x16x32_bf16 v[208:211], v[80:83], v[32:35], v[208:211]\n\t" \
    "v_mfma_f32_16x16x32_bf16 v[196:199], v[84:87], v[36:39], v[196:199]\n\t" \
    "ds_read_b128 v[28:31], %[xp] offset:43008\n\t" \
    "v_mfma_f32_16x16x32_bf16 v[200:203], v[92:95], v[36:39], v[200:203]\n\t" \
    "ds_read_b128 v[32:35], %[xp] offset:44032\n\t" \
    "v_mfma_f32_16x16x32_bf16 v[204:207], v[84:87], v[40:43], v[204:207]\n\t" \
    "s_waitcnt lgkmcnt(4)\n\t" \
    "v_mfma_f32_16x16x32_bf16 v[208:211], v[92:95], v[40:43], v[208:211]\n\t" \
    "v_mfma_f32_16x16x32_bf16 v[196:199], v[88:91], v[44:47], v[196:199]\n\t" \
    "ds_read_b128 v[36:39], %[xp] offset:45056\n\t" \
    "v_mfma_f32_16x16x32_bf16 v[200:203], v[96:99], v[44:47], v[200:203]\n\t" \
    "ds_read_b128 v[40:43], %[xp] offset:46080\n\t" \
    "v_mfma_f32_16x16x32_bf16 v[204:207], v[88:91], v[48:51], v[204:207]\n\t" \
    "s_waitcnt lgkmcnt(4)\n\t" \
    "v_mfma_f32_16x16x32_bf16 v[208:211], v[96:99], v[48:51], v[208:211]\n\t" \
    "v_mfma_f32_16x16x32_bf16 v[196:199], v[100:103], v[20:23], v[196:199]\n\t" \
    "ds_read_b128 v[44:47], %[xp] offset:47104\n\t" \
    "v_mfma_f32_16x16x32_bf16 v[200:203], v[108:111], v[20:23], v[200:203]\n\t" \
    "ds_read_b128 v[48:51], %[xp] offset:48128\n\t" \
    "v_mfma_f32_16x16x32_bf16 v[204:207], v[100:103], v[24:27], v[204:207]\n\t" \
    "s_waitcnt lgkmcnt(4)\n\t" \
    "v_mfma_f32_16x16x32_bf16 v[208:211], v[108:111], v[24:27], v[208:211]\n\t" \
    "buffer_load_dwordx4 %[l16], %[rs], %[a] offen offset:0 sc1 lds\n\t" \
    "v_mfma_f32_16x16x32_bf16 v[196:199], v[104:107], v[28:31], v[196:199]\n\t" \
    "ds_read_b128 v[20:23], %[xp] offset:49152\n\t" \
    "v_mfma_f32_16x16x32_bf16 v[200:203], v[112:115], v[28:31], v[200:203]\n\t" \
    "ds_read_b128 v[24:27], %[xp] offset:50176\n\t" \
    "v_mfma_f32_16x16x32_bf16 v[204:207], v[104:107], v[32:35], v[204:207]\n\t" \
    "s_waitcnt lgkmcnt(4)\n\t" \
    "v_mfma_f32_16x16x32_bf16 v[208:211], v[112:115], v[32:35], v[208:211]\n\t" \
    "buffer_load_dwordx4 %[l16], %[rs], %[a] offen offset:1024 sc1 lds\n\t" \
    "v_mfma_f32_16x16x32_bf16 v[196:199], v[116:119], v[36:39], v[196:199]\n\t" \
    "ds_read_b128 v[28:31], %[xp] offset:51200\n\t" \
    "v_mfma_f32_16x16x32_bf16 v[200:203], v[124:127], v[36:39], v[200:203]\n\t" \
    "ds_read_b128 v[32:35], %[xp] offset:52224\n\t" \
    "v_mfma_f32_16x16x32_bf16 v[204:207], v[116:119], v[40:43], v[204:207]\n\t" \
    "s_waitcnt lgkmcnt(4)\n\t" \
    "v_mfma_f32_16x16x32_bf16 v[208:211], v[124:127], v[40:43], v[208:211]\n\t" \
    "buffer_load_dwordx4 %[l16], %[rs], %[a] offen offset:2048 sc1 lds\n\t" \
    "v_mfma_f32_16x16x32_bf16 v[196:199], v[120:123], v[44:47], v[196:199]\n\t" \
    "ds_read_b128 v[36:39], %[xp] offset:53248\n\t" \
    "v_mfma_f32_16x16x32_bf16 v[200:203], v[128:131], v[44:47], v[200:203]\n\t" \
    "ds_read_b128 v[40:43], %[xp] offset:54272\n\t" \
    "v_mfma_f32_16x16x32_bf16 v[204:207], v[120:123], v[48:51], v[204:207]\n\t" \
    "s_waitcnt lgkmcnt(4)\n\t" \
    "v_mfma_f32_16x16x32_bf16 v[208:211], v[128:131], v[48:51], v[208:211]\n\t" \
    "buffer_load_dwordx4 %[l16], %[rs], %[a] offen offset:3072 sc1 lds\n\t" \
    "s_waitcnt vmcnt(4)\n\t" \
    "s_barrier\n\t" \
    "s_setprio 1\n\t" \
    "s_add_u32 m0, %[sd], 32768\n\t" \
    "s_add_u32 %[a], %[sx], 131072\n\t" \
    "s_add_u32 %[b], %[oo], 0x1000\n\t" \
    "s_cmp_lg_u32 %[t], 1\n\t" \
    "s_cselect_b64 %[em], -1, 0\n\t" \
    "v_mfma_f32_16x16x32_bf16 v[196:199], v[132:135], v[20:23], v[196:199]\n\t" \
    "ds_read_b128 v[44:47], %[xp] offset:55296\n\t" \
    "v_mfma_f32_16x16x32_bf16 v[200:203], v[140:143], v[20:23], v[200:203]\n\t" \
    "ds_read_b128 v[48:51], %[xp] offset:56320\n\t" \
    "v_mfma_f32_16x16x32_bf16 v[204:207], v[132:135], v[24:27], v[204:207]\n\t" \
    "ds_read_b128 v[52:55], %[pa] offset:0\n\t" \
    "s_waitcnt lgkmcnt(5)\n\t" \
    "v_mfma_f32_16x16x32_bf16 v[208:211], v[140:143], v[24:27], v[208:211]\n\t" \
    "buffer_load_dwordx4 %[l16], %[rs], %[a] offen offset:0 sc1 lds\n\t" \
    "v_mfma_f32_16x16x32_bf16 v[196:199], v[136:139], v[28:31], v[196:199]\n\t" \
    "ds_read_b128 v[20:23], %[xp] offset:57344\n\t" \
    "v_mfma_f32_16x16x32_bf16 v[200:203], v[144:147], v[28:31], v[200:203]\n\t" \
    "ds_read_b128 v[24:27], %[xp] offset:58368\n\t" \
    "v_mfma_f32_16x16x32_bf16 v[204:207], v[136:139], v[32:35], v[204:207]\n\t" \
    "ds_read_b128 v[56:59], %[pa] offset:1024\n\t" \
    "s_waitcnt lgkmcnt(6)\n\t" \
    "v_mfma_f32_16x16x32_bf16 v[208:211], v[144:147], v[32:35], v[208:211]\n\t" \
    "buffer_load_dwordx4 %[l16], %[rs], %[a] offen offset:1024 sc1 lds\n\t" \
    "v_mfma_f32_16x16x32_bf16 v[196:199], v[148:151], v[36:39], v[196:199]\n\t" \
    "ds_read_b128 v[28:31], %[xp] offset:59392\n\t" \
    "v_mfma_f32_16x16x32_bf16 v[200:203], v[156:159], v[36:39], v[200:203]\n\t" \
    "ds_read_b128 v[32:35], %[xp] offset:60416\n\t" \
    "v_mfma_f32_16x16x32_bf16 v[204:207], v[148:151], v[40:43], v[204:207]\n\t" \
    "ds_read_b128 v[60:63], %[pa] offset:2048\n\t" \
    "s_waitcnt lgkmcnt(7)\n\t" \
    "v_mfma_f32_16x16x32_bf16 v[208:211], v[156:159], v[40:43], v[208:211]\n\t" \
    "buffer_load_dwordx4 %[l16], %[rs], %[a] offen offset:2048 sc1 lds\n\t" \
    "v_mfma_f32_16x16x32_bf16 v[196:199], v[152:155], v[44:47], v[196:199]\n\t" \
    "ds_read_b128 v[36:39], %[xp] offset:61440\n\t" \
    "s_waitcnt lgkmcnt(7)\n\t" \
    "v_add_f32 v0, v0, v52\n\t" \
    "v_mfma_f32_16x16x32_bf16 v[200:203], v[160:163], v[44:47], v[200:203]\n\t" \
    "ds_read_b128 v[40:43], %[xp] offset:62464\n\t" \
    "v_add_f32 v1, v1, v53\n\t" \
    "v_mfma_f32_16x16x32_bf16 v[204:207], v[152:155], v[48:51], v[204:207]\n\t" \
    "ds_read_b128 v[64:67], %[pa] offset:3072\n\t" \
    "s_waitcnt lgkmcnt(7)\n\t" \
    "v_mfma_f32_16x16x32_bf16 v[208:211], v[160:163], v[48:51], v[208:211]\n\t" \
    "buffer_load_dwordx4 %[l16], %[rs], %[a] offen offset:3072 sc1 lds\n\t" \
    "v_add_f32 v2, v2, v54\n\t" \
    "v_add_f32 v3, v3, v55\n\t" \
    "v_mfma_f32_16x16x32_bf16 v[196:199], v[164:167], v[20:23], v[196:199]\n\t" \
    "ds_read_b128 v[44:47], %[xp] offset:63488\n\t" \
    "s_waitcnt lgkmcnt(7)\n\t" \
    "v_add_f32 v4, v4, v56\n\t" \
    "v_mfma_f32_16x16x32_bf16 v[200:203], v[172:175], v[20:23], v[200:203]\n\t" \
    "ds_read_b128 v[48:51], %[xp] offset:64512\n\t" \
    "v_add_f32 v5, v5, v57\n\t" \
    "v_mfma_f32_16x16x32_bf16 v[204:207], v[164:167], v[24:27], v[204:207]\n\t" \
    "v_add_f32 v6, v6, v58\n\t" \
    "s_waitcnt lgkmcnt(6)\n\t" \
    "v_mfma_f32_16x16x32_bf16 v[208:211], v[172:175], v[24:27], v[208:211]\n\t" \
    "v_add_f32 v7, v7, v59\n\t" \
    "s_waitcnt lgkmcnt(5)\n\t" \
    "v_add_f32 v8, v8, v60\n\t" \
    "v_add_f32 v9, v9, v61\n\t" \
    "v_mfma_f32_16x16x32_bf16 v[196:199], v[168:171], v[28:31], v[196:199]\n\t" \
    "ds_read_b128 v[20:23], %[xp] offset:0\n\t" \
    "v_add_f32 v10, v10, v62\n\t" \
    "v_mfma_f32_16x16x32_bf16 v[200:203], v[176:179], v[28:31], v[200:203]\n\t" \
    "ds_read_b128 v[24:27], %[xp] offset:1024\n\t" \
    "v_add_f32 v11, v11, v63\n\t" \
    "v_mfma_f32_16x16x32_bf16 v[204:207], v[168:171], v[32:35], v[204:207]\n\t" \
    "s_waitcnt lgkmcnt(4)\n\t" \
    "v_add_f32 v12, v12, v64\n\t" \
    "v_mfma_f32_16x16x32_bf16 v[208:211], v[176:179], v[32:35], v[208:211]\n\t" \
    "v_add_f32 v13, v13, v65\n\t" \
    "v_add_f32 v14, v14, v66\n\t" \
    "v_add_f32 v15, v15, v67\n\t" \
    "v_mfma_f32_16x16x32_bf16 v[196:199], v[180:183], v[36:39], v[196:199]\n\t" \
    "ds_read_b128 v[28:31], %[xp] offset:2048\n\t" \
    "v_cvt_pk_bf16_f32 v52, v0, v4\n\t" \
    "v_mfma_f32_16x16x32_bf16 v[200:203], v[188:191], v[36:39], v[200:203]\n\t" \
    "ds_read_b128 v[32:35], %[xp] offset:3072\n\t" \
    "v_cvt_pk_bf16_f32 v53, v1, v5\n\t" \
    "v_mfma_f32_16x16x32_bf16 v[204:207], v[180:183], v[40:43], v[204:207]\n\t" \
    "v_cvt_pk_bf16_f32 v54, v2, v6\n\t" \
    "s_waitcnt lgkmcnt(4)\n\t" \
    "v_mfma_f32_16x16x32_bf16 v[208:211], v[188:191], v[40:43], v[208:211]\n\t" \
    "v_cvt_pk_bf16_f32 v55, v3, v7\n\t" \
    "v_cvt_pk_bf16_f32 v56, v8, v12\n\t" \
    "v_cvt_pk_bf16_f32 v57, v9, v13\n\t" \
    "v_mfma_f32_16x16x32_bf16 v[196:199], v[184:187], v[44:47], v[196:199]\n\t" \
    "ds_read_b128 v[36:39], %[xp] offset:4096\n\t" \
    "v_cvt_pk_bf16_f32 v58, v10, v14\n\t" \
    "v_mfma_f32_16x16x32_bf16 v[200:203], v[192:195], v[44:47], v[200:203]\n\t" \
    "ds_read_b128 v[40:43], %[xp] offset:5120\n\t" \
    "v_cvt_pk_bf16_f32 v59, v11, v15\n\t" \
    "v_mfma_f32_16x16x32_bf16 v[204:207], v[184:187], v[48:51], v[204:207]\n\t" \
    "s_waitcnt lgkmcnt(4)\n\t" \
    "v_mfma_f32_16x16x32_bf16 v[208:211], v[192:195], v[48:51], v[208:211]\n\t" \
    "s_mov_b64 exec, %[em]\n\t" \
    "buffer_store_dwordx4 v[52:55], %[l16], %[rs], %[oo] offen sc1\n\t" \
    "buffer_store_dwordx4 v[56:59], %[l16], %[rs], %[b] offen sc1\n\t" \
    "s_mov_b64 exec, -1\n\t" \
    "s_add_u32 %[sx], %[sx], 65536\n\t" \
    "s_add_u32 %[t], %[t], 1\n\t" \
    "s_add_u32 %[oo], %[oo], 262144\n\t" \
    "s_waitcnt vmcnt(6)\n\t" \
    "s_cmp_gt_u32 %[t], " STR(SQUICK) " + 1\n\t" \
    "s_cbranch_scc1 B1_%=\n\t" \
    "s_waitcnt vmcnt(0)\n\t" \
    "B1_%=:\n\t" \
    "s_barrier\n\t" \
    "s_cmp_lt_u32 %[t], %[te]\n\t" \
    "s_cbranch_scc1 L1_%=\n\t" \
    "s_cmp_eq_u32 %[fin], %[te]\n\t" \
    "s_cbranch_scc0 E_%=\n\t" \
    "s_nop 4\n\t" \
    "s_barrier\n\t" \
    "ds_read_b128 v[52:55], %[pa] offset:0\n\t" \
    "ds_read_b128 v[56:59], %[pa] offset:1024\n\t" \
    "ds_read_b128 v[60:63], %[pa] offset:2048\n\t" \
    "ds_read_b128 v[64:67], %[pa] offset:3072\n\t" \
    "s_add_u32 %[b], %[oo], 0x1000\n\t" \
    "s_waitcnt lgkmcnt(0)\n\t" \
    "v_add_f32 v196, v196, v52\n\t" \
    "v_add_f32 v197, v197, v53\n\t" \
    "v_add_f32 v198, v198, v54\n\t" \
    "v_add_f32 v199, v199, v55\n\t" \
    "v_add_f32 v200, v200, v56\n\t" \
    "v_add_f32 v201, v201, v57\n\t" \
    "v_add_f32 v202, v202, v58\n\t" \
    "v_add_f32 v203, v203, v59\n\t" \
    "v_add_f32 v204, v204, v60\n\t" \
    "v_add_f32 v205, v205, v61\n\t" \
    "v_add_f32 v206, v206, v62\n\t" \
    "v_add_f32 v207, v207, v63\n\t" \
    "v_add_f32 v208, v208, v64\n\t" \
    "v_add_f32 v209, v209, v65\n\t" \
    "v_add_f32 v210, v210, v66\n\t" \
    "v_add_f32 v211, v211, v67\n\t" \
    "v_cvt_pk_bf16_f32 v52, v196, v200\n\t" \
    "v_cvt_pk_bf16_f32 v53, v197, v201\n\t" \
    "v_cvt_pk_bf16_f32 v54, v198, v202\n\t" \
    "v_cvt_pk_bf16_f32 v55, v199, v203\n\t" \
    "v_cvt_pk_bf16_f32 v56, v204, v208\n\t" \
    "v_cvt_pk_bf16_f32 v57, v205, v209\n\t" \
    "v_cvt_pk_bf16_f32 v58, v206, v210\n\t" \
    "v_cvt_pk_bf16_f32 v59, v207, v211\n\t" \
    "buffer_store_dwordx4 v[52:55], %[l16], %[rs], %[oo] offen sc1\n\t" \
    "buffer_store_dwordx4 v[56:59], %[l16], %[rs], %[b] offen sc1\n\t" \
    "E_%=:\n\t" \
    "s_setprio 0\n\t" \
    "s_waitcnt lgkmcnt(0)\n\t" \
    "s_nop 7\n\t" \
    : [t] "+s"(t), [sx] "+s"(sx), [oo] "+s"(oo), [a] "=&s"(tmp), [b] "=&s"(tmp2), [em] "=&s"(em), "+{v[20:27]}"(xf[0]), "+{v[28:35]}"(xf[1]), "+{v[36:43]}"(xf[2]), "+{v[196:203]}"(ac[0]), "+{v[204:211]}"(ac[1]) \
    : [te] "s"(TE_), [fin] "s"(FIN_), [kh] "s"(kh), [sd] "s"(sd), [rs] "s"(rs), [l16] "v"(l16), [xp] "v"(xp), [pa] "v"(pa), [fm] "s"(fm), [pm] "s"(pm), [fo] "s"(fo), [po] "s"(po), [pd] "s"(pd), [tag] "s"(P.tag), "{v[68:75]}"(w[0][0]), "{v[76:83]}"(w[0][1]), "{v[84:91]}"(w[1][0]), "{v[92:99]}"(w[1][1]), "{v[100:107]}"(w[2][0]), "{v[108:115]}"(w[2][1]), "{v[116:123]}"(w[3][0]), "{v[124:131]}"(w[3][1]), "{v[132:139]}"(w[4][0]), "{v[140:147]}"(w[4][1]), "{v[148:155]}"(w[5][0]), "{v[156:163]}"(w[5][1]), "{v[164:171]}"(w[6][0]), "{v[172:179]}"(w[6][1]), "{v[180:187]}"(w[7][0]), "{v[188:195]}"(w[7][1]) \
    : "memory", "scc", "v0", "v1", "v2", "v3", "v4", "v5", "v6", "v7", "v8", "v9", "v10", "v11", "v12", "v13", "v14", "v15", "v44", "v45", "v46", "v47", "v48", "v49", "v50", "v51", "v52", "v53", "v54", "v55", "v56", "v57", "v58", "v59", "v60", "v61", "v62", "v63", "v64", "v65", "v66", "v67")

#define S_LOOP_V(TE_, FIN_) \
  asm volatile( \
    "s_cmp_eq_u32 %[kh], 0\n\t" \
    "s_cbranch_scc0 L1_%=\n\t" \
    "L0_%=:\n\t" \
    "s_setprio 0\n\t" \
    "s_nop 3\n\t" \
    "ds_write_b128 %[pa], v[196:199] offset:0\n\t" \
    "ds_write_b128 %[pa], v[200:203] offset:1024\n\t" \
    "ds_write_b128 %[pa], v[204:207] offset:2048\n\t" \
    "ds_write_b128 %[pa], v[208:211] offset:3072\n\t" \
    "s_sub_u32 %[a], %[t], 1\n\t" \
    "v_mov_b32 v53, %[tag]\n\t" \
    "v_mov_b32 v52, %[a]\n\t" \
    "s_sub_u32 %[b], %[t], 2\n\t" \
    "s_cmp_lt_u32 %[b], " STR(SQUICK) "\n\t" \
    "s_cselect_b64 exec, %[fm], 0\n\t" \
    "buffer_store_dwordx2 v[52:53], %[l16], %[rs], %[fo] offen sc1\n\t" \
    "s_mov_b64 exec, -1\n\t" \
    "s_add_u32 m0, %[sd], 49152\n\t" \
    "s_add_u32 %[a], %[sx], 81920\n\t" \
    "s_waitcnt lgkmcnt(0)\n\t" \
    "buffer_load_dwordx4 %[l16], %[rs], %[a] offen offset:0 sc1 lds\n\t" \
    "buffer_load_dwordx4 %[l16], %[rs], %[a] offen offset:1024 sc1 lds\n\t" \
    "buffer_load_dwordx4 %[l16], %[rs], %[a] offen offset:2048 sc1 lds\n\t" \
    "buffer_load_dwordx4 %[l16], %[rs], %[a] offen offset:3072 sc1 lds\n\t" \
    "v_mfma_f32_16x16x32_bf16 v[0:3], v[20:23], v[68:71], 0\n\t" \
    "ds_read_b128 v[44:47], %[xp] offset:6144\n\t" \
    "v_mfma_f32_16x16x32_bf16 v[4:7], v[20:23], v[76:79], 0\n\t" \
    "ds_read_b128 v[48:51], %[xp] offset:7168\n\t" \
    "v_mfma_f32_16x16x32_bf16 v[8:11], v[24:27], v[68:71], 0\n\t" \
    "v_mfma_f32_16x16x32_bf16 v[12:15], v[24:27], v[76:79], 0\n\t" \
    "v_mfma_f32_16x16x32_bf16 v[0:3], v[28:31], v[72:75], v[0:3]\n\t" \
    "ds_read_b128 v[20:23], %[xp] offset:8192\n\t" \
    "v_mfma_f32_16x16x32_bf16 v[4:7], v[28:31], v[80:83], v[4:7]\n\t" \
    "ds_read_b128 v[24:27], %[xp] offset:9216\n\t" \
    "v_mfma_f32_16x16x32_bf16 v[8:11], v[32:35], v[72:75], v[8:11]\n\t" \
    "v_mfma_f32_16x16x32_bf16 v[12:15], v[32:35], v[80:83], v[12:15]\n\t" \
    "v_mfma_f32_16x16x32_bf16 v[0:3], v[36:39], v[84:87], v[0:3]\n\t" \
    "ds_read_b128 v[28:31], %[xp] offset:10240\n\t" \
    "v_mfma_f32_16x16x32_bf16 v[4:7], v[36:39], v[92:95], v[4:7]\n\t" \
    "ds_read_b128 v[32:35], %[xp] offset:11264\n\t" \
    "v_mfma_f32_16x16x32_bf16 v[8:11], v[40:43], v[84:87], v[8:11]\n\t" \
    "s_waitcnt lgkmcnt(4)\n\t" \
    "v_mfma_f32_16x16x32_bf16 v[12:15], v[40:43], v[92:95], v[12:15]\n\t" \
    "v_mfma_f32_16x16x32_bf16 v[0:3], v[44:47], v[88:91], v[0:3]\n\t" \
    "ds_read_b128 v[36:39], %[xp] offset:12288\n\t" \
    "v_mfma_f32_16x16x32_bf16 v[4:7], v[44:47], v[96:99], v[4:7]\n\t" \
    "ds_read_b128 v[40:43], %[xp] offset:13312\n\t" \
    "v_mfma_f32_16x16x32_bf16 v[8:11], v[48:51], v[88:91], v[8:11]\n\t" \
    "s_waitcnt lgkmcnt(4)\n\t" \
    "v_mfma_f32_16x16x32_bf16 v[12:15], v[48:51], v[96:99], v[12:15]\n\t" \
    "v_mfma_f32_16x16x32_bf16 v[0:3], v[20:23], v[100:103], v[0:3]\n\t" \
    "ds_read_b128 v[44:47], %[xp] offset:14336\n\t" \
    "v_mfma_f32_16x16x32_bf16 v[4:7], v[20:23], v[108:111], v[4:7]\n\t" \
    "ds_read_b128 v[48:51], %[xp] offset:15360\n\t" \
    "v_mfma_f32_16x16x32_bf16 v[8:11], v[24:27], v[100:103], v[8:11]\n\t" \
    "s_waitcnt lgkmcnt(4)\n\t" \
    "v_mfma_f32_16x16x32_bf16 v[12:15], v[24:27], v[108:111], v[12:15]\n\t" \
    "v_mfma_f32_16x16x32_bf16 v[0:3], v[28:31], v[104:107], v[0:3]\n\t" \
    "ds_read_b128 v[20:23], %[xp] offset:16384\n\t" \
    "v_mfma_f32_16x16x32_bf16 v[4:7], v[28:31], v[112:115], v[4:7]\n\t" \
    "ds_read_b128 v[24:27], %[xp] offset:17408\n\t" \
    "v_mfma_f32_16x16x32_bf16 v[8:11], v[32:35], v[104:107], v[8:11]\n\t" \
    "s_waitcnt lgkmcnt(4)\n\t" \
    "v_mfma_f32_16x16x32_bf16 v[12:15], v[32:35], v[112:115], v[12:15]\n\t" \
    "v_mfma_f32_16x16x32_bf16 v[0:3], v[36:39], v[116:119], v[0:3]\n\t" \
    "ds_read_b128 v[28:31], %[xp] offset:18432\n\t" \
    "v_mfma_f32_16x16x32_bf16 v[4:7], v[36:39], v[124:127], v[4:7]\n\t" \
    "ds_read_b128 v[32:35], %[xp] offset:19456\n\t" \
    "v_mfma_f32_16x16x32_bf16 v[8:11], v[40:43], v[116:119], v[8:11]\n\t" \
    "s_waitcnt lgkmcnt(4)\n\t" \
    "v_mfma_f32_16x16x32_bf16 v[12:15], v[40:43], v[124:127], v[12:15]\n\t" \
    "v_mfma_f32_16x16x32_bf16 v[0:3], v[44:47], v[120:123], v[0:3]\n\t" \
    "ds_read_b128 v[36:39], %[xp] offset:20480\n\t" \
    "v_mfma_f32_16x16x32_bf16 v[4:7], v[44:47], v[128:131], v[4:7]\n\t" \
    "ds_read_b128 v[40:43], %[xp] offset:21504\n\t" \
    "v_mfma_f32_16x16x32_bf16 v[8:11], v[48:51], v[120:123], v[8:11]\n\t" \
    "s_waitcnt lgkmcnt(4)\n\t" \
    "v_mfma_f32_16x16x32_bf16 v[12:15], v[48:51], v[128:131], v[12:15]\n\t" \
    "s_waitcnt vmcnt(4)\n\t" \
    "s_barrier\n\t" \
    "s_setprio 0\n\t" \
    "s_sub_u32 %[a], %[t], 1\n\t" \
    "v_mov_b32 v53, %[tag]\n\t" \
    "v_mov_b32 v52, %[a]\n\t" \
    "s_cmp_gt_u32 %[t], 1\n\t" \
    "s_cselect_b64 exec, %[fm], 0\n\t" \
    "buffer_store_dwordx2 v[52:53], %[l16], %[rs], %[fo] offen sc1\n\t" \
    "s_mov_b64 exec, %[pm]\n\t" \
    "s_mov_b32 m0, %[pd]\n\t" \
    "s_add_u32 %[a], %[sx], %[po]\n\t" \
    "buffer_load_dwordx4 %[l16], %[rs], %[a] offen sc1 lds\n\t" \
    "s_mov_b64 exec, -1\n\t" \
    "s_add_u32 m0, %[sd], 0\n\t" \
    "s_add_u32 %[a], %[sx], 131072\n\t" \
    "buffer_load_dwordx4 %[l16], %[rs], %[a] offen offset:0 sc1 lds\n\t" \
    "buffer_load_dwordx4 %[l16], %[rs], %[a] offen offset:1024 sc1 lds\n\t" \
    "buffer_load_dwordx4 %[l16], %[rs], %[a] offen offset:2048 sc1 lds\n\t" \
    "buffer_load_dwordx4 %[l16], %[rs], %[a] offen offset:3072 sc1 lds\n\t" \
    "v_mfma_f32_16x16x32_bf16 v[0:3], v[20:23], v[132:135], v[0:3]\n\t" \
    "ds_read_b128 v[44:47], %[xp] offset:22528\n\t" \
    "v_mfma_f32_16x16x32_bf16 v[4:7], v[20:23], v[140:143], v[4:7]\n\t" \
    "ds_read_b128 v[48:51], %[xp] offset:23552\n\t" \
    "v_mfma_f32_16x16x32_bf16 v[8:11], v[24:27], v[132:135], v[8:11]\n\t" \
    "s_waitcnt lgkmcnt(4)\n\t" \
    "v_mfma_f32_16x16x32_bf16 v[12:15], v[24:27], v[140:143], v[12:15]\n\t" \
    "v_mfma_f32_16x16x32_bf16 v[0:3], v[28:31], v[136:139], v[0:3]\n\t" \
    "ds_read_b128 v[20:23], %[xp] offset:24576\n\t" \
    "v_mfma_f32_16x16x32_bf16 v[4:7], v[28:31], v[144:147], v[4:7]\n\t" \
    "ds_read_b128 v[24:27], %[xp] offset:25600\n\t" \
    "v_mfma_f32_16x16x32_bf16 v[8:11], v[32:35], v[136:139], v[8:11]\n\t" \
    "s_waitcnt lgkmcnt(4)\n\t" \
    "v_mfma_f32_16x16x32_bf16 v[12:15], v[32:35], v[144:147], v[12:15]\n\t" \
    "v_mfma_f32_16x16x32_bf16 v[0:3], v[36:39], v[148:151], v[0:3]\n\t" \
    "ds_read_b128 v[28:31], %[xp] offset:26624\n\t" \
    "v_mfma_f32_16x16x32_bf16 v[4:7], v[36:39], v[156:159], v[4:7]\n\t" \
    "ds_read_b128 v[32:35], %[xp] offset:27648\n\t" \
    "v_mfma_f32_16x16x32_bf16 v[8:11], v[40:43], v[148:151], v[8:11]\n\t" \
    "s_waitcnt lgkmcnt(4)\n\t" \
    "v_mfma_f32_16x16x32_bf16 v[12:15], v[40:43], v[156:159], v[12:15]\n\t" \
    "v_mfma_f32_16x16x32_bf16 v[0:3], v[44:47], v[152:155], v[0:3]\n\t" \
    "ds_read_b128 v[36:39], %[xp] offset:28672\n\t" \
    "v_mfma_f32_16x16x32_bf16 v[4:7], v[44:47], v[160:163], v[4:7]\n\t" \
    "ds_read_b128 v[40:43], %[xp] offset:29696\n\t" \
    "v_mfma_f32_16x16x32_bf16 v[8:11], v[48:51], v[152:155], v[8:11]\n\t" \
    "s_waitcnt lgkmcnt(4)\n\t" \
    "v_mfma_f32_16x16x32_bf16 v[12:15], v[48:51], v[160:163], v[12:15]\n\t" \
    "v_mfma_f32_16x16x32_bf16 v[0:3], v[20:23], v[164:167], v[0:3]\n\t" \
    "ds_read_b128 v[44:47], %[xp] offset:30720\n\t" \
    "v_mfma_f32_16x16x32_bf16 v[4:7], v[20:23], v[172:175], v[4:7]\n\t" \
    "ds_read_b128 v[48:51], %[xp] offset:31744\n\t" \
    "v_mfma_f32_16x16x32_bf16 v[8:11], v[24:27], v[164:167], v[8:11]\n\t" \
    "s_waitcnt lgkmcnt(4)\n\t" \
    "v_mfma_f32_16x16x32_bf16 v[12:15], v[24:27], v[172:175], v[12:15]\n\t" \
    "v_mfma_f32_16x16x32_bf16 v[0:3], v[28:31], v[168:171], v[0:3]\n\t" \
    "ds_read_b128 v[20:23], %[xp] offset:32768\n\t" \
    "v_mfma_f32_16x16x32_bf16 v[4:7], v[28:31], v[176:179], v[4:7]\n\t" \
    "ds_read_b128 v[24:27], %[xp] offset:33792\n\t" \
    "v_mfma_f32_16x16x32_bf16 v[8:11], v[32:35], v[168:171], v[8:11]\n\t" \
    "s_waitcnt lgkmcnt(4)\n\t" \
    "v_mfma_f32_16x16x32_bf16 v[12:15], v[32:35], v[176:179], v[12:15]\n\t" \
    "v_mfma_f32_16x16x32_bf16 v[0:3], v[36:39], v[180:183], v[0:3]\n\t" \
    "ds_read_b128 v[28:31], %[xp] offset:34816\n\t" \
    "v_mfma_f32_16x16x32_bf16 v[4:7], v[36:39], v[188:191], v[4:7]\n\t" \
    "ds_read_b128 v[32:35], %[xp] offset:35840\n\t" \
    "v_mfma_f32_16x16x32_bf16 v[8:11], v[40:43], v[180:183], v[8:11]\n\t" \
    "s_waitcnt lgkmcnt(4)\n\t" \
    "v_mfma_f32_16x16x32_bf16 v[12:15], v[40:43], v[188:191], v[12:15]\n\t" \
    "v_mfma_f32_16x16x32_bf16 v[0:3], v[44:47], v[184:187], v[0:3]\n\t" \
    "ds_read_b128 v[36:39], %[xp] offset:36864\n\t" \
    "v_mfma_f32_16x16x32_bf16 v[4:7], v[44:47], v[192:195], v[4:7]\n\t" \
    "ds_read_b128 v[40:43], %[xp] offset:37888\n\t" \
    "v_mfma_f32_16x16x32_bf16 v[8:11], v[48:51], v[184:187], v[8:11]\n\t" \
    "s_waitcnt lgkmcnt(4)\n\t" \
    "v_mfma_f32_16x16x32_bf16 v[12:15], v[48:51], v[192:195], v[12:15]\n\t" \
    "s_add_u32 %[sx], %[sx], 65536\n\t" \
    "s_add_u32 %[t], %[t], 1\n\t" \
    "s_waitcnt vmcnt(4)\n\t" \
    "s_barrier\n\t" \
    "s_setprio 0\n\t" \
    "s_nop 3\n\t" \
    "ds_write_b128 %[pa], v[0:3] offset:0\n\t" \
    "ds_write_b128 %[pa], v[4:7] offset:1024\n\t" \
    "ds_write_b128 %[pa], v[8:11] offset:2048\n\t" \
    "ds_write_b128 %[pa], v[12:15] offset:3072\n\t" \
    "s_sub_u32 %[a], %[t], 1\n\t" \
    "v_mov_b32 v53, %[tag]\n\t" \
    "v_mov_b32 v52, %[a]\n\t" \
    "s_sub_u32 %[b], %[t], 2\n\t" \
    "s_cmp_lt_u32 %[b], " STR(SQUICK) "\n\t" \
    "s_cselect_b64 exec, %[fm], 0\n\t" \
    "buffer_store_dwordx2 v[52:53], %[l16], %[rs], %[fo] offen sc1\n\t" \
    "s_mov_b64 exec, -1\n\t" \
    "s_add_u32 m0, %[sd], 16384\n\t" \
    "s_add_u32 %[a], %[sx], 81920\n\t" \
    "s_waitcnt lgkmcnt(0)\n\t" \
    "buffer_load_dwordx4 %[l16], %[rs], %[a] offen offset:0 sc1 lds\n\t" \
    "buffer_load_dwordx4 %[l16], %[rs], %[a] offen offset:1024 sc1 lds\n\t" \
    "buffer_load_dwordx4 %[l16], %[rs], %[a] offen offset:2048 sc1 lds\n\t" \
    "buffer_load_dwordx4 %[l16], %[rs], %[a] offen offset:3072 sc1 lds\n\t" \
    "v_mfma_f32_16x16x32_bf16 v[196:199], v[20:23], v[68:71], 0\n\t" \
    "ds_read_b128 v[44:47], %[xp] offset:38912\n\t" \
    "v_mfma_f32_16x16x32_bf16 v[200:203], v[20:23], v[76:79], 0\n\t" \
    "ds_read_b128 v[48:51], %[xp] offset:39936\n\t" \
    "v_mfma_f32_16x16x32_bf16 v[204:207], v[24:27], v[68:71], 0\n\t" \
    "v_mfma_f32_16x16x32_bf16 v[208:211], v[24:27], v[76:79], 0\n\t" \
    "v_mfma_f32_16x16x32_bf16 v[196:199], v[28:31], v[72:75], v[196:199]\n\t" \
    "ds_read_b128 v[20:23], %[xp] offset:40960\n\t" \
    "v_mfma_f32_16x16x32_bf16 v[200:203], v[28:31], v[80:83], v[200:203]\n\t" \
    "ds_read_b128 v[24:27], %[xp] offset:41984\n\t" \
    "v_mfma_f32_16x16x32_bf16 v[204:207], v[32:35], v[72:75], v[204:207]\n\t" \
    "v_mfma_f32_16x16x32_bf16 v[208:211], v[32:35], v[80:83], v[208:211]\n\t" \
    "v_mfma_f32_16x16x32_bf16 v[196:199], v[36:39], v[84:87], v[196:199]\n\t" \
    "ds_read_b128 v[28:31], %[xp] offset:43008\n\t" \
    "v_mfma_f32_16x16x32_bf16 v[200:203], v[36:39], v[92:95], v[200:203]\n\t" \
    "ds_read_b128 v[32:35], %[xp] offset:44032\n\t" \
    "v_mfma_f32_16x16x32_bf16 v[204:207], v[40:43], v[84:87], v[204:207]\n\t" \
    "s_waitcnt lgkmcnt(4)\n\t" \
    "v_mfma_f32_16x16x32_bf16 v[208:211], v[40:43], v[92:95], v[208:211]\n\t" \
    "v_mfma_f32_16x16x32_bf16 v[196:199], v[44:47], v[88:91], v[196:199]\n\t" \
    "ds_read_b128 v[36:39], %[xp] offset:45056\n\t" \
    "v_mfma_f32_16x16x32_bf16 v[200:203], v[44:47], v[96:99], v[200:203]\n\t" \
    "ds_read_b128 v[40:43], %[xp] offset:46080\n\t" \
    "v_mfma_f32_16x16x32_bf16 v[204:207], v[48:51], v[88:91], v[204:207]\n\t" \
    "s_waitcnt lgkmcnt(4)\n\t" \
    "v_mfma_f32_16x16x32_bf16 v[208:211], v[48:51], v[96:99], v[208:211]\n\t" \
    "v_mfma_f32_16x16x32_bf16 v[196:199], v[20:23], v[100:103], v[196:199]\n\t" \
    "ds_read_b128 v[44:47], %[xp] offset:47104\n\t" \
    "v_mfma_f32_16x16x32_bf16 v[200:203], v[20:23], v[108:111], v[200:203]\n\t" \
    "ds_read_b128 v[48:51], %[xp] offset:48128\n\t" \
    "v_mfma_f32_16x16x32_bf16 v[204:207], v[24:27], v[100:103], v[204:207]\n\t" \
    "s_waitcnt lgkmcnt(4)\n\t" \
    "v_mfma_f32_16x16x32_bf16 v[208:211], v[24:27], v[108:111], v[208:211]\n\t" \
    "v_mfma_f32_16x16x32_bf16 v[196:199], v[28:31], v[104:107], v[196:199]\n\t" \
    "ds_read_b128 v[20:23], %[xp] offset:49152\n\t" \
    "v_mfma_f32_16x16x32_bf16 v[200:203], v[28:31], v[112:115], v[200:203]\n\t" \
    "ds_read_b128 v[24:27], %[xp] offset:50176\n\t" \
    "v_mfma_f32_16x16x32_bf16 v[204:207], v[32:35], v[104:107], v[204:207]\n\t" \
    "s_waitcnt lgkmcnt(4)\n\t" \
    "v_mfma_f32_16x16x32_bf16 v[208:211], v[32:35], v[112:115], v[208:211]\n\t" \
    "v_mfma_f32_16x16x32_bf16 v[196:199], v[36:39], v[116:119], v[196:199]\n\t" \
    "ds_read_b128 v[28:31], %[xp] offset:51200\n\t" \
    "v_mfma_f32_16x16x32_bf16 v[200:203], v[36:39], v[124:127], v[200:203]\n\t" \
    "ds_read_b128 v[32:35], %[xp] offset:52224\n\t" \
    "v_mfma_f32_16x16x32_bf16 v[204:207], v[40:43], v[116:119], v[204:207]\n\t" \
    "s_waitcnt lgkmcnt(4)\n\t" \
    "v_mfma_f32_16x16x32_bf16 v[208:211], v[40:43], v[124:127], v[208:211]\n\t" \
    "v_mfma_f32_16x16x32_bf16 v[196:199], v[44:47], v[120:123], v[196:199]\n\t" \
    "ds_read_b128 v[36:39], %[xp] offset:53248\n\t" \
    "v_mfma_f32_16x16x32_bf16 v[200:203], v[44:47], v[128:131], v[200:203]\n\t" \
    "ds_read_b128 v[40:43], %[xp] offset:54272\n\t" \
    "v_mfma_f32_16x16x32_bf16 v[204:207], v[48:51], v[120:123], v[204:207]\n\t" \
    "s_waitcnt lgkmcnt(4)\n\t" \
    "v_mfma_f32_16x16x32_bf16 v[208:211], v[48:51], v[128:131], v[208:211]\n\t" \
    "s_waitcnt vmcnt(4)\n\t" \
    "s_barrier\n\t" \
    "s_setprio 0\n\t" \
    "s_sub_u32 %[a], %[t], 1\n\t" \
    "v_mov_b32 v53, %[tag]\n\t" \
    "v_mov_b32 v52, %[a]\n\t" \
    "s_cmp_gt_u32 %[t], 1\n\t" \
    "s_cselect_b64 exec, %[fm], 0\n\t" \
    "buffer_store_dwordx2 v[52:53], %[l16], %[rs], %[fo] offen sc1\n\t" \
    "s_mov_b64 exec, %[pm]\n\t" \
    "s_mov_b32 m0, %[pd]\n\t" \
    "s_add_u32 %[a], %[sx], %[po]\n\t" \
    "buffer_load_dwordx4 %[l16], %[rs], %[a] offen sc1 lds\n\t" \
    "s_mov_b64 exec, -1\n\t" \
    "s_add_u32 m0, %[sd], 32768\n\t" \
    "s_add_u32 %[a], %[sx], 131072\n\t" \
    "buffer_load_dwordx4 %[l16], %[rs], %[a] offen offset:0 sc1 lds\n\t" \
    "buffer_load_dwordx4 %[l16], %[rs], %[a] offen offset:1024 sc1 lds\n\t" \
    "buffer_load_dwordx4 %[l16], %[rs], %[a] offen offset:2048 sc1 lds\n\t" \
    "buffer_load_dwordx4 %[l16], %[rs], %[a] offen offset:3072 sc1 lds\n\t" \
    "v_mfma_f32_16x16x32_bf16 v[196:199], v[20:23], v[132:135], v[196:199]\n\t" \
    "ds_read_b128 v[44:47], %[xp] offset:55296\n\t" \
    "v_mfma_f32_16x16x32_bf16 v[200:203], v[20:23], v[140:143], v[200:203]\n\t" \
    "ds_read_b128 v[48:51], %[xp] offset:56320\n\t" \
    "v_mfma_f32_16x16x32_bf16 v[204:207], v[24:27], v[132:135], v[204:207]\n\t" \
    "s_waitcnt lgkmcnt(4)\n\t" \
    "v_mfma_f32_16x16x32_bf16 v[208:211], v[24:27], v[140:143], v[208:211]\n\t" \
    "v_mfma_f32_16x16x32_bf16 v[196:199], v[28:31], v[136:139], v[196:199]\n\t" \
    "ds_read_b128 v[20:23], %[xp] offset:57344\n\t" \
    "v_mfma_f32_16x16x32_bf16 v[200:203], v[28:31], v[144:147], v[200:203]\n\t" \
    "ds_read_b128 v[24:27], %[xp] offset:58368\n\t" \
    "v_mfma_f32_16x16x32_bf16 v[204:207], v[32:35], v[136:139], v[204:207]\n\t" \
    "s_waitcnt lgkmcnt(4)\n\t" \
    "v_mfma_f32_16x16x32_bf16 v[208:211], v[32:35], v[144:147], v[208:211]\n\t" \
    "v_mfma_f32_16x16x32_bf16 v[196:199], v[36:39], v[148:151], v[196:199]\n\t" \
    "ds_read_b128 v[28:31], %[xp] offset:59392\n\t" \
    "v_mfma_f32_16x16x32_bf16 v[200:203], v[36:39], v[156:159], v[200:203]\n\t" \
    "ds_read_b128 v[32:35], %[xp] offset:60416\n\t" \
    "v_mfma_f32_16x16x32_bf16 v[204:207], v[40:43], v[148:151], v[204:207]\n\t" \
    "s_waitcnt lgkmcnt(4)\n\t" \
    "v_mfma_f32_16x16x32_bf16 v[208:211], v[40:43], v[156:159], v[208:211]\n\t" \
    "v_mfma_f32_16x16x32_bf16 v[196:199], v[44:47], v[152:155], v[196:199]\n\t" \
    "ds_read_b128 v[36:39], %[xp] offset:61440\n\t" \
    "v_mfma_f32_16x16x32_bf16 v[200:203], v[44:47], v[160:163], v[200:203]\n\t" \
    "ds_read_b128 v[40:43], %[xp] offset:62464\n\t" \
    "v_mfma_f32_16x16x32_bf16 v[204:207], v[48:51], v[152:155], v[204:207]\n\t" \
    "s_waitcnt lgkmcnt(4)\n\t" \
    "v_mfma_f32_16x16x32_bf16 v[208:211], v[48:51], v[160:163], v[208:211]\n\t" \
    "v_mfma_f32_16x16x32_bf16 v[196:199], v[20:23], v[164:167], v[196:199]\n\t" \
    "ds_read_b128 v[44:47], %[xp] offset:63488\n\t" \
    "v_mfma_f32_16x16x32_bf16 v[200:203], v[20:23], v[172:175], v[200:203]\n\t" \
    "ds_read_b128 v[48:51], %[xp] offset:64512\n\t" \
    "v_mfma_f32_16x16x32_bf16 v[204:207], v[24:27], v[164:167], v[204:207]\n\t" \
    "s_waitcnt lgkmcnt(4)\n\t" \
    "v_mfma_f32_16x16x32_bf16 v[208:211], v[24:27], v[172:175], v[208:211]\n\t" \
    "v_mfma_f32_16x16x32_bf16 v[196:199], v[28:31], v[168:171], v[196:199]\n\t" \
    "ds_read_b128 v[20:23], %[xp] offset:0\n\t" \
    "v_mfma_f32_16x16x32_bf16 v[200:203], v[28:31], v[176:179], v[200:203]\n\t" \
    "ds_read_b128 v[24:27], %[xp] offset:1024\n\t" \
    "v_mfma_f32_16x16x32_bf16 v[204:207], v[32:35], v[168:171], v[204:207]\n\t" \
    "s_waitcnt lgkmcnt(4)\n\t" \
    "v_mfma_f32_16x16x32_bf16 v[208:211], v[32:35], v[176:179], v[208:211]\n\t" \
    "v_mfma_f32_16x16x32_bf16 v[196:199], v[36:39], v[180:183], v[196:199]\n\t" \
    "ds_read_b128 v[28:31], %[xp] offset:2048\n\t" \
    "v_mfma_f32_16x16x32_bf16 v[200:203], v[36:39], v[188:191], v[200:203]\n\t" \
    "ds_read_b128 v[32:35], %[xp] offset:3072\n\t" \
    "v_mfma_f32_16x16x32_bf16 v[204:207], v[40:43], v[180:183], v[204:207]\n\t" \
    "s_waitcnt lgkmcnt(4)\n\t" \
    "v_mfma_f32_16x16x32_bf16 v[208:211], v[40:43], v[188:191], v[208:211]\n\t" \
    "v_mfma_f32_16x16x32_bf16 v[196:199], v[44:47], v[184:187], v[196:199]\n\t" \
    "ds_read_b128 v[36:39], %[xp] offset:4096\n\t" \
    "v_mfma_f32_16x16x32_bf16 v[200:203], v[44:47], v[192:195], v[200:203]\n\t" \
    "ds_read_b128 v[40:43], %[xp] offset:5120\n\t" \
    "v_mfma_f32_16x16x32_bf16 v[204:207], v[48:51], v[184:187], v[204:207]\n\t" \
    "s_waitcnt lgkmcnt(4)\n\t" \
    "v_mfma_f32_16x16x32_bf16 v[208:211], v[48:51], v[192:195], v[208:211]\n\t" \
    "s_add_u32 %[sx], %[sx], 65536\n\t" \
    "s_add_u32 %[t], %[t], 1\n\t" \
    "s_waitcnt vmcnt(4)\n\t" \
    "s_barrier\n\t" \
    "s_cmp_lt_u32 %[t], %[te]\n\t" \
    "s_cbranch_scc1 L0_%=\n\t" \
    "s_cmp_eq_u32 %[fin], %[te]\n\t" \
    "s_cbranch_scc0 E_%=\n\t" \
    "s_nop 4\n\t" \
    "ds_write_b128 %[pa], v[196:199] offset:0\n\t" \
    "ds_write_b128 %[pa], v[200:203] offset:1024\n\t" \
    "ds_write_b128 %[pa], v[204:207] offset:2048\n\t" \
    "ds_write_b128 %[pa], v[208:211] offset:3072\n\t" \
    "s_waitcnt lgkmcnt(0)\n\t" \
    "s_barrier\n\t" \
    "s_branch E_%=\n\t" \
    "L1_%=:\n\t" \
    "s_setprio 1\n\t" \
    "s_add_u32 m0, %[sd], 49152\n\t" \
    "s_add_u32 %[a], %[sx], 81920\n\t" \
    "v_mfma_f32_16x16x32_bf16 v[0:3], v[20:23], v[68:71], 0\n\t" \
    "ds_read_b128 v[44:47], %[xp] offset:6144\n\t" \
    "v_mfma_f32_16x16x32_bf16 v[4:7], v[20:23], v[76:79], 0\n\t" \
    "ds_read_b128 v[48:51], %[xp] offset:7168\n\t" \
    "v_mfma_f32_16x16x32_bf16 v[8:11], v[24:27], v[68:71], 0\n\t" \
    "s_waitcnt lgkmcnt(4)\n\t" \
    "v_mfma_f32_16x16x32_bf16 v[12:15], v[24:27], v[76:79], 0\n\t" \
    "v_mfma_f32_16x16x32_bf16 v[0:3], v[28:31], v[72:75], v[0:3]\n\t" \
    "ds_read_b128 v[20:23], %[xp] offset:8192\n\t" \
    "v_mfma_f32_16x16x32_bf16 v[4:7], v[28:31], v[80:83], v[4:7]\n\t" \
    "ds_read_b128 v[24:27], %[xp] offset:9216\n\t" \
    "v_mfma_f32_16x16x32_bf16 v[8:11], v[32:35], v[72:75], v[8:11]\n\t" \
    "s_waitcnt lgkmcnt(4)\n\t" \
    "v_mfma_f32_16x16x32_bf16 v[12:15], v[32:35], v[80:83], v[12:15]\n\t" \
    "v_mfma_f32_16x16x32_bf16 v[0:3], v[36:39], v[84:87], v[0:3]\n\t" \
    "ds_read_b128 v[28:31], %[xp] offset:10240\n\t" \
    "v_mfma_f32_16x16x32_bf16 v[4:7], v[36:39], v[92:95], v[4:7]\n\t" \
    "ds_read_b128 v[32:35], %[xp] offset:11264\n\t" \
    "v_mfma_f32_16x16x32_bf16 v[8:11], v[40:43], v[84:87], v[8:11]\n\t" \
    "s_waitcnt lgkmcnt(4)\n\t" \
    "v_mfma_f32_16x16x32_bf16 v[12:15], v[40:43], v[92:95], v[12:15]\n\t" \
    "v_mfma_f32_16x16x32_bf16 v[0:3], v[44:47], v[88:91], v[0:3]\n\t" \
    "ds_read_b128 v[36:39], %[xp] offset:12288\n\t" \
    "v_mfma_f32_16x16x32_bf16 v[4:7], v[44:47], v[96:99], v[4:7]\n\t" \
    "ds_read_b128 v[40:43], %[xp] offset:13312\n\t" \
    "v_mfma_f32_16x16x32_bf16 v[8:11], v[48:51], v[88:91], v[8:11]\n\t" \
    "s_waitcnt lgkmcnt(4)\n\t" \
    "v_mfma_f32_16x16x32_bf16 v[12:15], v[48:51], v[96:99], v[12:15]\n\t" \
    "v_mfma_f32_16x16x32_bf16 v[0:3], v[20:23], v[100:103], v[0:3]\n\t" \
    "ds_read_b128 v[44:47], %[xp] offset:14336\n\t" \
    "v_mfma_f32_16x16x32_bf16 v[4:7], v[20:23], v[108:111], v[4:7]\n\t" \
    "ds_read_b128 v[48:51], %[xp] offset:15360\n\t" \
    "v_mfma_f32_16x16x32_bf16 v[8:11], v[24:27], v[100:103], v[8:11]\n\t" \
    "s_waitcnt lgkmcnt(4)\n\t" \
    "v_mfma_f32_16x16x32_bf16 v[12:15], v[24:27], v[108:111], v[12:15]\n\t" \
    "buffer_load_dwordx4 %[l16], %[rs], %[a] offen offset:0 sc1 lds\n\t" \
    "v_mfma_f32_16x16x32_bf16 v[0:3], v[28:31], v[104:107], v[0:3]\n\t" \
    "ds_read_b128 v[20:23], %[xp] offset:16384\n\t" \
    "v_mfma_f32_16x16x32_bf16 v[4:7], v[28:31], v[112:115], v[4:7]\n\t" \
    "ds_read_b128 v[24:27], %[xp] offset:17408\n\t" \
    "v_mfma_f32_16x16x32_bf16 v[8:11], v[32:35], v[104:107], v[8:11]\n\t" \
    "s_waitcnt lgkmcnt(4)\n\t" \
    "v_mfma_f32_16x16x32_bf16 v[12:15], v[32:35], v[112:115], v[12:15]\n\t" \
    "buffer_load_dwordx4 %[l16], %[rs], %[a] offen offset:1024 sc1 lds\n\t" \
    "v_mfma_f32_16x16x32_bf16 v[0:3], v[36:39], v[116:119], v[0:3]\n\t" \
    "ds_read_b128 v[28:31], %[xp] offset:18432\n\t" \
    "v_mfma_f32_16x16x32_bf16 v[4:7], v[36:39], v[124:127], v[4:7]\n\t" \
    "ds_read_b128 v[32:35], %[xp] offset:19456\n\t" \
    "v_mfma_f32_16x16x32_bf16 v[8:11], v[40:43], v[116:119], v[8:11]\n\t" \
    "s_waitcnt lgkmcnt(4)\n\t" \
    "v_mfma_f32_16x16x32_bf16 v[12:15], v[40:43], v[124:127], v[12:15]\n\t" \
    "buffer_load_dwordx4 %[l16], %[rs], %[a] offen offset:2048 sc1 lds\n\t" \
    "v_mfma_f32_16x16x32_bf16 v[0:3], v[44:47], v[120:123], v[0:3]\n\t" \
    "ds_read_b128 v[36:39], %[xp] offset:20480\n\t" \
    "v_mfma_f32_16x16x32_bf16 v[4:7], v[44:47], v[128:131], v[4:7]\n\t" \
    "ds_read_b128 v[40:43], %[xp] offset:21504\n\t" \
    "v_mfma_f32_16x16x32_bf16 v[8:11], v[48:51], v[120:123], v[8:11]\n\t" \
    "s_waitcnt lgkmcnt(4)\n\t" \
    "v_mfma_f32_16x16x32_bf16 v[12:15], v[48:51], v[128:131], v[12:15]\n\t" \
    "buffer_load_dwordx4 %[l16], %[rs], %[a] offen offset:3072 sc1 lds\n\t" \
    "s_waitcnt vmcnt(4)\n\t" \
    "s_barrier\n\t" \
    "s_setprio 1\n\t" \
    "s_add_u32 m0, %[sd], 0\n\t" \
    "s_add_u32 %[a], %[sx], 131072\n\t" \
    "s_add_u32 %[b], %[oo], 0x1000\n\t" \
    "s_cmp_lg_u32 %[t], 1\n\t" \
    "s_cselect_b64 %[em], -1, 0\n\t" \
    "v_mfma_f32_16x16x32_bf16 v[0:3], v[20:23], v[132:135], v[0:3]\n\t" \
    "ds_read_b128 v[44:47], %[xp] offset:22528\n\t" \
    "v_mfma_f32_16x16x32_bf16 v[4:7], v[20:23], v[140:143], v[4:7]\n\t" \
    "ds_read_b128 v[48:51], %[xp] offset:23552\n\t" \
    "v_mfma_f32_16x16x32_bf16 v[8:11], v[24:27], v[132:135], v[8:11]\n\t" \
    "ds_read_b128 v[52:55], %[pa] offset:0\n\t" \
    "s_waitcnt lgkmcnt(5)\n\t" \
    "v_mfma_f32_16x16x32_bf16 v[12:15], v[24:27], v[140:143], v[12:15]\n\t" \
    "buffer_load_dwordx4 %[l16], %[rs], %[a] offen offset:0 sc1 lds\n\t" \
    "v_mfma_f32_16x16x32_bf16 v[0:3], v[28:31], v[136:139], v[0:3]\n\t" \
    "ds_read_b128 v[20:23], %[xp] offset:24576\n\t" \
    "v_mfma_f32_16x16x32_bf16 v[4:7], v[28:31], v[144:147], v[4:7]\n\t" \
    "ds_read_b128 v[24:27], %[xp] offset:25600\n\t" \
    "v_mfma_f32_16x16x32_bf16 v[8:11], v[32:35], v[136:139], v[8:11]\n\t" \
    "ds_read_b128 v[56:59], %[pa] offset:1024\n\t" \
    "s_waitcnt lgkmcnt(6)\n\t" \
    "v_mfma_f32_16x16x32_bf16 v[12:15], v[32:35], v[144:147], v[12:15]\n\t" \
    "buffer_load_dwordx4 %[l16], %[rs], %[a] offen offset:1024 sc1 lds\n\t" \
    "v_mfma_f32_16x16x32_bf16 v[0:3], v[36:39], v[148:151], v[0:3]\n\t" \
    "ds_read_b128 v[28:31], %[xp] offset:26624\n\t" \
    "v_mfma_f32_16x16x32_bf16 v[4:7], v[36:39], v[156:159], v[4:7]\n\t" \
    "ds_read_b128 v[32:35], %[xp] offset:27648\n\t" \
    "v_mfma_f32_16x16x32_bf16 v[8:11], v[40:43], v[148:151], v[8:11]\n\t" \
    "ds_read_b128 v[60:63], %[pa] offset:2048\n\t" \
    "s_waitcnt lgkmcnt(7)\n\t" \
    "v_mfma_f32_16x16x32_bf16 v[12:15], v[40:43], v[156:159], v[12:15]\n\t" \
    "buffer_load_dwordx4 %[l16], %[rs], %[a] offen offset:2048 sc1 lds\n\t" \
    "v_mfma_f32_16x16x32_bf16 v[0:3], v[44:47], v[152:155], v[0:3]\n\t" \
    "ds_read_b128 v[36:39], %[xp] offset:28672\n\t" \
    "s_waitcnt lgkmcnt(7)\n\t" \
    "v_add_f32 v196, v196, v52\n\t" \
    "v_mfma_f32_16x16x32_bf16 v[4:7], v[44:47], v[160:163], v[4:7]\n\t" \
    "ds_read_b128 v[40:43], %[xp] offset:29696\n\t" \
    "v_add_f32 v197, v197, v53\n\t" \
    "v_mfma_f32_16x16x32_bf16 v[8:11], v[48:51], v[152:155], v[8:11]\n\t" \
    "ds_read_b128 v[64:67], %[pa] offset:3072\n\t" \
    "s_waitcnt lgkmcnt(7)\n\t" \
    "v_mfma_f32_16x16x32_bf16 v[12:15], v[48:51], v[160:163], v[12:15]\n\t" \
    "buffer_load_dwordx4 %[l16], %[rs], %[a] offen offset:3072 sc1 lds\n\t" \
    "v_add_f32 v198, v198, v54\n\t" \
    "v_add_f32 v199, v199, v55\n\t" \
    "v_mfma_f32_16x16x32_bf16 v[0:3], v[20:23], v[164:167], v[0:3]\n\t" \
    "ds_read_b128 v[44:47], %[xp] offset:30720\n\t" \
    "s_waitcnt lgkmcnt(7)\n\t" \
    "v_add_f32 v200, v200, v56\n\t" \
    "v_mfma_f32_16x16x32_bf16 v[4:7], v[20:23], v[172:175], v[4:7]\n\t" \
    "ds_read_b128 v[48:51], %[xp] offset:31744\n\t" \
    "v_add_f32 v201, v201, v57\n\t" \
    "v_mfma_f32_16x16x32_bf16 v[8:11], v[24:27], v[164:167], v[8:11]\n\t" \
    "v_add_f32 v202, v202, v58\n\t" \
    "s_waitcnt lgkmcnt(6)\n\t" \
    "v_mfma_f32_16x16x32_bf16 v[12:15], v[24:27], v[172:175], v[12:15]\n\t" \
    "v_add_f32 v203, v203, v59\n\t" \
    "s_waitcnt lgkmcnt(5)\n\t" \
    "v_add_f32 v204, v204, v60\n\t" \
    "v_add_f32 v205, v205, v61\n\t" \
    "v_mfma_f32_16x16x32_bf16 v[0:3], v[28:31], v[168:171], v[0:3]\n\t" \
    "ds_read_b128 v[20:23], %[xp] offset:32768\n\t" \
    "v_add_f32 v206, v206, v62\n\t" \
    "v_mfma_f32_16x16x32_bf16 v[4:7], v[28:31], v[176:179], v[4:7]\n\t" \
    "ds_read_b128 v[24:27], %[xp] offset:33792\n\t" \
    "v_add_f32 v207, v207, v63\n\t" \
    "v_mfma_f32_16x16x32_bf16 v[8:11], v[32:35], v[168:171], v[8:11]\n\t" \
    "s_waitcnt lgkmcnt(4)\n\t" \
    "v_add_f32 v208, v208, v64\n\t" \
    "v_mfma_f32_16x16x32_bf16 v[12:15], v[32:35], v[176:179], v[12:15]\n\t" \
    "v_add_f32 v209, v209, v65\n\t" \
    "v_add_f32 v210, v210, v66\n\t" \
    "v_add_f32 v211, v211, v67\n\t" \
    "v_mfma_f32_16x16x32_bf16 v[0:3], v[36:39], v[180:183], v[0:3]\n\t" \
    "ds_read_b128 v[28:31], %[xp] offset:34816\n\t" \
    "v_cvt_pk_bf16_f32 v52, v196, v197\n\t" \
    "v_mfma_f32_16x16x32_bf16 v[4:7], v[36:39], v[188:191], v[4:7]\n\t" \
    "ds_read_b128 v[32:35], %[xp] offset:35840\n\t" \
    "v_cvt_pk_bf16_f32 v53, v198, v199\n\t" \
    "v_mfma_f32_16x16x32_bf16 v[8:11], v[40:43], v[180:183], v[8:11]\n\t" \
    "v_cvt_pk_bf16_f32 v54, v204, v205\n\t" \
    "s_waitcnt lgkmcnt(4)\n\t" \
    "v_mfma_f32_16x16x32_bf16 v[12:15], v[40:43], v[188:191], v[12:15]\n\t" \
    "v_cvt_pk_bf16_f32 v55, v206, v207\n\t" \
    "v_cvt_pk_bf16_f32 v56, v200, v201\n\t" \
    "v_cvt_pk_bf16_f32 v57, v202, v203\n\t" \
    "v_mfma_f32_16x16x32_bf16 v[0:3], v[44:47], v[184:187], v[0:3]\n\t" \
    "ds_read_b128 v[36:39], %[xp] offset:36864\n\t" \
    "v_cvt_pk_bf16_f32 v58, v208, v209\n\t" \
    "v_mfma_f32_16x16x32_bf16 v[4:7], v[44:47], v[192:195], v[4:7]\n\t" \
    "ds_read_b128 v[40:43], %[xp] offset:37888\n\t" \
    "v_cvt_pk_bf16_f32 v59, v210, v211\n\t" \
    "v_mfma_f32_16x16x32_bf16 v[8:11], v[48:51], v[184:187], v[8:11]\n\t" \
    "s_waitcnt lgkmcnt(4)\n\t" \
    "v_mfma_f32_16x16x32_bf16 v[12:15], v[48:51], v[192:195], v[12:15]\n\t" \
    "s_mov_b64 exec, %[em]\n\t" \
    "buffer_store_dwordx4 v[52:55], %[l16], %[rs], %[oo] offen sc1\n\t" \
    "buffer_store_dwordx4 v[56:59], %[l16], %[rs], %[oo] offen offset:1024 sc1\n\t" \
    "s_mov_b64 exec, -1\n\t" \
    "s_add_u32 %[sx], %[sx], 65536\n\t" \
    "s_add_u32 %[t], %[t], 1\n\t" \
    "s_add_u32 %[oo], %[oo], 262144\n\t" \
    "s_waitcnt vmcnt(6)\n\t" \
    "s_cmp_gt_u32 %[t], " STR(SQUICK) " + 1\n\t" \
    "s_cbranch_scc1 B0_%=\n\t" \
    "s_waitcnt vmcnt(0)\n\t" \
    "B0_%=:\n\t" \
    "s_barrier\n\t" \
    "s_setprio 1\n\t" \
    "s_add_u32 m0, %[sd], 16384\n\t" \
    "s_add_u32 %[a], %[sx], 81920\n\t" \
    "v_mfma_f32_16x16x32_bf16 v[196:199], v[20:23], v[68:71], 0\n\t" \
    "ds_read_b128 v[44:47], %[xp] offset:38912\n\t" \
    "v_mfma_f32_16x16x32_bf16 v[200:203], v[20:23], v[76:79], 0\n\t" \
    "ds_read_b128 v[48:51], %[xp] offset:39936\n\t" \
    "v_mfma_f32_16x16x32_bf16 v[204:207], v[24:27], v[68:71], 0\n\t" \
    "s_waitcnt lgkmcnt(4)\n\t" \
    "v_mfma_f32_16x16x32_bf16 v[208:211], v[24:27], v[76:79], 0\n\t" \
    "v_mfma_f32_16x16x32_bf16 v[196:199], v[28:31], v[72:75], v[196:199]\n\t" \
    "ds_read_b128 v[20:23], %[xp] offset:40960\n\t" \
    "v_mfma_f32_16x16x32_bf16 v[200:203], v[28:31], v[80:83], v[200:203]\n\t" \
    "ds_read_b128 v[24:27], %[xp] offset:41984\n\t" \
    "v_mfma_f32_16x16x32_bf16 v[204:207], v[32:35], v[72:75], v[204:207]\n\t" \
    "s_waitcnt lgkmcnt(4)\n\t" \
    "v_mfma_f32_16x16x32_bf16 v[208:211], v[32:35], v[80:83], v[208:211]\n\t" \
    "v_mfma_f32_16x16x32_bf16 v[196:199], v[36:39], v[84:87], v[196:199]\n\t" \
    "ds_read_b128 v[28:31], %[xp] offset:43008\n\t" \
    "v_mfma_f32_16x16x32_bf16 v[200:203], v[36:39], v[92:95], v[200:203]\n\t" \
    "ds_read_b128 v[32:35], %[xp] offset:44032\n\t" \
    "v_mfma_f32_16x16x32_bf16 v[204:207], v[40:43], v[84:87], v[204:207]\n\t" \
    "s_waitcnt lgkmcnt(4)\n\t" \
    "v_mfma_f32_16x16x32_bf16 v[208:211], v[40:43], v[92:95], v[208:211]\n\t" \
    "v_mfma_f32_16x16x32_bf16 v[196:199], v[44:47], v[88:91], v[196:199]\n\t" \
    "ds_read_b128 v[36:39], %[xp] offset:45056\n\t" \
    "v_mfma_f32_16x16x32_bf16 v[200:203], v[44:47], v[96:99], v[200:203]\n\t" \
    "ds_read_b128 v[40:43], %[xp] offset:46080\n\t" \
    "v_mfma_f32_16x16x32_bf16 v[204:207], v[48:51], v[88:91], v[204:207]\n\t" \
    "s_waitcnt lgkmcnt(4)\n\t" \
    "v_mfma_f32_16x16x32_bf16 v[208:211], v[48:51], v[96:99], v[208:211]\n\t" \
    "v_mfma_f32_16x16x32_bf16 v[196:199], v[20:23], v[100:103], v[196:199]\n\t" \
    "ds_read_b128 v[44:47], %[xp] offset:47104\n\t" \
    "v_mfma_f32_16x16x32_bf16 v[200:203], v[20:23], v[108:111], v[200:203]\n\t" \
    "ds_read_b128 v[48:51], %[xp] offset:48128\n\t" \
    "v_mfma_f32_16x16x32_bf16 v[204:207], v[24:27], v[100:103], v[204:207]\n\t" \
    "s_waitcnt lgkmcnt(4)\n\t" \
    "v_mfma_f32_16x16x32_bf16 v[208:211], v[24:27], v[108:111], v[208:211]\n\t" \
    "buffer_load_dwordx4 %[l16], %[rs], %[a] offen offset:0 sc1 lds\n\t" \
    "v_mfma_f32_16x16x32_bf16 v[196:199], v[28:31], v[104:107], v[196:199]\n\t" \
    "ds_read_b128 v[20:23], %[xp] offset:49152\n\t" \
    "v_mfma_f32_16x16x32_bf16 v[200:203], v[28:31], v[112:115], v[200:203]\n\t" \
    "ds_read_b128 v[24:27], %[xp] offset:50176\n\t" \
    "v_mfma_f32_16x16x32_bf16 v[204:207], v[32:35], v[104:107], v[204:207]\n\t" \
    "s_waitcnt lgkmcnt(4)\n\t" \
    "v_mfma_f32_16x16x32_bf16 v[208:211], v[32:35], v[112:115], v[208:211]\n\t" \
    "buffer_load_dwordx4 %[l16], %[rs], %[a] offen offset:1024 sc1 lds\n\t" \
    "v_mfma_f32_16x16x32_bf16 v[196:199], v[36:39], v[116:119], v[196:199]\n\t" \
    "ds_read_b128 v[28:31], %[xp] offset:51200\n\t" \
    "v_mfma_f32_16x16x32_bf16 v[200:203], v[36:39], v[124:127], v[200:203]\n\t" \
    "ds_read_b128 v[32:35], %[xp] offset:52224\n\t" \
    "v_mfma_f32_16x16x32_bf16 v[204:207], v[40:43], v[116:119], v[204:207]\n\t" \
    "s_waitcnt lgkmcnt(4)\n\t" \
    "v_mfma_f32_16x16x32_bf16 v[208:211], v[40:43], v[124:127], v[208:211]\n\t" \
    "buffer_load_dwordx4 %[l16], %[rs], %[a] offen offset:2048 sc1 lds\n\t" \
    "v_mfma_f32_16x16x32_bf16 v[196:199], v[44:47], v[120:123], v[196:199]\n\t" \
    "ds_read_b128 v[36:39], %[xp] offset:53248\n\t" \
    "v_mfma_f32_16x16x32_bf16 v[200:203], v[44:47], v[128:131], v[200:203]\n\t" \
    "ds_read_b128 v[40:43], %[xp] offset:54272\n\t" \
    "v_mfma_f32_16x16x32_bf16 v[204:207], v[48:51], v[120:123], v[204:207]\n\t" \
    "s_waitcnt lgkmcnt(4)\n\t" \
    "v_mfma_f32_16x16x32_bf16 v[208:211], v[48:51], v[128:131], v[208:211]\n\t" \
    "buffer_load_dwordx4 %[l16], %[rs], %[a] offen offset:3072 sc1 lds\n\t" \
    "s_waitcnt vmcnt(4)\n\t" \
    "s_barrier\n\t" \
    "s_setprio 1\n\t" \
    "s_add_u32 m0, %[sd], 32768\n\t" \
    "s_add_u32 %[a], %[sx], 131072\n\t" \
    "s_add_u32 %[b], %[oo], 0x1000\n\t" \
    "s_cmp_lg_u32 %[t], 1\n\t" \
    "s_cselect_b64 %[em], -1, 0\n\t" \
    "v_mfma_f32_16x16x32_bf16 v[196:199], v[20:23], v[132:135], v[196:199]\n\t" \
    "ds_read_b128 v[44:47], %[xp] offset:55296\n\t" \
    "v_mfma_f32_16x16x32_bf16 v[200:203], v[20:23], v[140:143], v[200:203]\n\t" \
    "ds_read_b128 v[48:51], %[xp] offset:56320\n\t" \
    "v_mfma_f32_16x16x32_bf16 v[204:207], v[24:27], v[132:135], v[204:207]\n\t" \
    "ds_read_b128 v[52:55], %[pa] offset:0\n\t" \
    "s_waitcnt lgkmcnt(5)\n\t" \
    "v_mfma_f32_16x16x32_bf16 v[208:211], v[24:27], v[140:143], v[208:211]\n\t" \
    "buffer_load_dwordx4 %[l16], %[rs], %[a] offen offset:0 sc1 lds\n\t" \
    "v_mfma_f32_16x16x32_bf16 v[196:199], v[28:31], v[136:139], v[196:199]\n\t" \
    "ds_read_b128 v[20:23], %[xp] offset:57344\n\t" \
    "v_mfma_f32_16x16x32_bf16 v[200:203], v[28:31], v[144:147], v[200:203]\n\t" \
    "ds_read_b128 v[24:27], %[xp] offset:58368\n\t" \
    "v_mfma_f32_16x16x32_bf16 v[204:207], v[32:35], v[136:139], v[204:207]\n\t" \
    "ds_read_b128 v[56:59], %[pa] offset:1024\n\t" \
    "s_waitcnt lgkmcnt(6)\n\t" \
    "v_mfma_f32_16x16x32_bf16 v[208:211], v[32:35], v[144:147], v[208:211]\n\t" \
    "buffer_load_dwordx4 %[l16], %[rs], %[a] offen offset:1024 sc1 lds\n\t" \
    "v_mfma_f32_16x16x32_bf16 v[196:199], v[36:39], v[148:151], v[196:199]\n\t" \
    "ds_read_b128 v[28:31], %[xp] offset:59392\n\t" \
    "v_mfma_f32_16x16x32_bf16 v[200:203], v[36:39], v[156:159], v[200:203]\n\t" \
    "ds_read_b128 v[32:35], %[xp] offset:60416\n\t" \
    "v_mfma_f32_16x16x32_bf16 v[204:207], v[40:43], v[148:151], v[204:207]\n\t" \
    "ds_read_b128 v[60:63], %[pa] offset:2048\n\t" \
    "s_waitcnt lgkmcnt(7)\n\t" \
    "v_mfma_f32_16x16x32_bf16 v[208:211], v[40:43], v[156:159], v[208:211]\n\t" \
    "buffer_load_dwordx4 %[l16], %[rs], %[a] offen offset:2048 sc1 lds\n\t" \
    "v_mfma_f32_16x16x32_bf16 v[196:199], v[44:47], v[152:155], v[196:199]\n\t" \
    "ds_read_b128 v[36:39], %[xp] offset:61440\n\t" \
    "s_waitcnt lgkmcnt(7)\n\t" \
    "v_add_f32 v0, v0, v52\n\t" \
    "v_mfma_f32_16x16x32_bf16 v[200:203], v[44:47], v[160:163], v[200:203]\n\t" \
    "ds_read_b128 v[40:43], %[xp] offset:62464\n\t" \
    "v_add_f32 v1, v1, v53\n\t" \
    "v_mfma_f32_16x16x32_bf16 v[204:207], v[48:51], v[152:155], v[204:207]\n\t" \
    "ds_read_b128 v[64:67], %[pa] offset:3072\n\t" \
    "s_waitcnt lgkmcnt(7)\n\t" \
    "v_mfma_f32_16x16x32_bf16 v[208:211], v[48:51], v[160:163], v[208:211]\n\t" \
    "buffer_load_dwordx4 %[l16], %[rs], %[a] offen offset:3072 sc1 lds\n\t" \
    "v_add_f32 v2, v2, v54\n\t" \
    "v_add_f32 v3, v3, v55\n\t" \
    "v_mfma_f32_16x16x32_bf16 v[196:199], v[20:23], v[164:167], v[196:199]\n\t" \
    "ds_read_b128 v[44:47], %[xp] offset:63488\n\t" \
    "s_waitcnt lgkmcnt(7)\n\t" \
    "v_add_f32 v4, v4, v56\n\t" \
    "v_mfma_f32_16x16x32_bf16 v[200:203], v[20:23], v[172:175], v[200:203]\n\t" \
    "ds_read_b128 v[48:51], %[xp] offset:64512\n\t" \
    "v_add_f32 v5, v5, v57\n\t" \
    "v_mfma_f32_16x16x32_bf16 v[204:207], v[24:27], v[164:167], v[204:207]\n\t" \
    "v_add_f32 v6, v6, v58\n\t" \
    "s_waitcnt lgkmcnt(6)\n\t" \
    "v_mfma_f32_16x16x32_bf16 v[208:211], v[24:27], v[172:175], v[208:211]\n\t" \
    "v_add_f32 v7, v7, v59\n\t" \
    "s_waitcnt lgkmcnt(5)\n\t" \
    "v_add_f32 v8, v8, v60\n\t" \
    "v_add_f32 v9, v9, v61\n\t" \
    "v_mfma_f32_16x16x32_bf16 v[196:199], v[28:31], v[168:171], v[196:199]\n\t" \
    "ds_read_b128 v[20:23], %[xp] offset:0\n\t" \
    "v_add_f32 v10, v10, v62\n\t" \
    "v_mfma_f32_16x16x32_bf16 v[200:203], v[28:31], v[176:179], v[200:203]\n\t" \
    "ds_read_b128 v[24:27], %[xp] offset:1024\n\t" \
    "v_add_f32 v11, v11, v63\n\t" \
    "v_mfma_f32_16x16x32_bf16 v[204:207], v[32:35], v[168:171], v[204:207]\n\t" \
    "s_waitcnt lgkmcnt(4)\n\t" \
    "v_add_f32 v12, v12, v64\n\t" \
    "v_mfma_f32_16x16x32_bf16 v[208:211], v[32:35], v[176:179], v[208:211]\n\t" \
    "v_add_f32 v13, v13, v65\n\t" \
    "v_add_f32 v14, v14, v66\n\t" \
    "v_add_f32 v15, v15, v67\n\t" \
    "v_mfma_f32_16x16x32_bf16 v[196:199], v[36:39], v[180:183], v[196:199]\n\t" \
    "ds_read_b128 v[28:31], %[xp] offset:2048\n\t" \
    "v_cvt_pk_bf16_f32 v52, v0, v1\n\t" \
    "v_mfma_f32_16x16x32_bf16 v[200:203], v[36:39], v[188:191], v[200:203]\n\t" \
    "ds_read_b128 v[32:35], %[xp] offset:3072\n\t" \
    "v_cvt_pk_bf16_f32 v53, v2, v3\n\t" \
    "v_mfma_f32_16x16x32_bf16 v[204:207], v[40:43], v[180:183], v[204:207]\n\t" \
    "v_cvt_pk_bf16_f32 v54, v8, v9\n\t" \
    "s_waitcnt lgkmcnt(4)\n\t" \
    "v_mfma_f32_16x16x32_bf16 v[208:211], v[40:43], v[188:191], v[208:211]\n\t" \
    "v_cvt_pk_bf16_f32 v55, v10, v11\n\t" \
    "v_cvt_pk_bf16_f32 v56, v4, v5\n\t" \
    "v_cvt_pk_bf16_f32 v57, v6, v7\n\t" \
    "v_mfma_f32_16x16x32_bf16 v[196:199], v[44:47], v[184:187], v[196:199]\n\t" \
    "ds_read_b128 v[36:39], %[xp] offset:4096\n\t" \
    "v_cvt_pk_bf16_f32 v58, v12, v13\n\t" \
    "v_mfma_f32_16x16x32_bf16 v[200:203], v[44:47], v[192:195], v[200:203]\n\t" \
    "ds_read_b128 v[40:43], %[xp] offset:5120\n\t" \
    "v_cvt_pk_bf16_f32 v59, v14, v15\n\t" \
    "v_mfma_f32_16x16x32_bf16 v[204:207], v[48:51], v[184:187], v[204:207]\n\t" \
    "s_waitcnt lgkmcnt(4)\n\t" \
    "v_mfma_f32_16x16x32_bf16 v[208:211], v[48:51], v[192:195], v[208:211]\n\t" \
    "s_mov_b64 exec, %[em]\n\t" \
    "buffer_store_dwordx4 v[52:55], %[l16], %[rs], %[oo] offen sc1\n\t" \
    "buffer_store_dwordx4 v[56:59], %[l16], %[rs], %[oo] offen offset:1024 sc1\n\t" \
    "s_mov_b64 exec, -1\n\t" \
    "s_add_u32 %[sx], %[sx], 65536\n\t" \
    "s_add_u32 %[t], %[t], 1\n\t" \
    "s_add_u32 %[oo], %[oo], 262144\n\t" \
    "s_waitcnt vmcnt(6)\n\t" \
    "s_cmp_gt_u32 %[t], " STR(SQUICK) " + 1\n\t" \
    "s_cbranch_scc1 B1_%=\n\t" \
    "s_waitcnt vmcnt(0)\n\t" \
    "B1_%=:\n\t" \
    "s_barrier\n\t" \
    "s_cmp_lt_u32 %[t], %[te]\n\t" \
    "s_cbranch_scc1 L1_%=\n\t" \
    "s_cmp_eq_u32 %[fin], %[te]\n\t" \
    "s_cbranch_scc0 E_%=\n\t" \
    "s_nop 4\n\t" \
    "s_barrier\n\t" \
    "ds_read_b128 v[52:55], %[pa] offset:0\n\t" \
    "ds_read_b128 v[56:59], %[pa] offset:1024\n\t" \
    "ds_read_b128 v[60:63], %[pa] offset:2048\n\t" \
    "ds_read_b128 v[64:67], %[pa] offset:3072\n\t" \
    "s_add_u32 %[b], %[oo], 0x1000\n\t" \
    "s_waitcnt lgkmcnt(0)\n\t" \
    "v_add_f32 v196, v196, v52\n\t" \
    "v_add_f32 v197, v197, v53\n\t" \
    "v_add_f32 v198, v198, v54\n\t" \
    "v_add_f32 v199, v199, v55\n\t" \
    "v_add_f32 v200, v200, v56\n\t" \
    "v_add_f32 v201, v201, v57\n\t" \
    "v_add_f32 v202, v202, v58\n\t" \
    "v_add_f32 v203, v203, v59\n\t" \
    "v_add_f32 v204, v204, v60\n\t" \
    "v_add_f32 v205, v205, v61\n\t" \
    "v_add_f32 v206, v206, v62\n\t" \
    "v_add_f32 v207, v207, v63\n\t" \
    "v_add_f32 v208, v208, v64\n\t" \
    "v_add_f32 v209, v209, v65\n\t" \
    "v_add_f32 v210, v210, v66\n\t" \
    "v_add_f32 v211, v211, v67\n\t" \
    "v_cvt_pk_bf16_f32 v52, v196, v197\n\t" \
    "v_cvt_pk_bf16_f32 v53, v198, v199\n\t" \
    "v_cvt_pk_bf16_f32 v54, v204, v205\n\t" \
    "v_cvt_pk_bf16_f32 v55, v206, v207\n\t" \
    "v_cvt_pk_bf16_f32 v56, v200, v201\n\t" \
    "v_cvt_pk_bf16_f32 v57, v202, v203\n\t" \
    "v_cvt_pk_bf16_f32 v58, v208, v209\n\t" \
    "v_cvt_pk_bf16_f32 v59, v210, v211\n\t" \
    "buffer_store_dwordx4 v[52:55], %[l16], %[rs], %[oo] offen sc1\n\t" \
    "buffer_store_dwordx4 v[56:59], %[l16], %[rs], %[oo] offen offset:1024 sc1\n\t" \
    "E_%=:\n\t" \
    "s_setprio 0\n\t" \
    "s_waitcnt lgkmcnt(0)\n\t" \
    "s_nop 7\n\t" \
    : [t] "+s"(t), [sx] "+s"(sx), [oo] "+s"(oo), [a] "=&s"(tmp), [b] "=&s"(tmp2), [em] "=&s"(em), "+{v[20:27]}"(xf[0]), "+{v[28:35]}"(xf[1]), "+{v[36:43]}"(xf[2]), "+{v[196:203]}"(ac[0]), "+{v[204:211]}"(ac[1]) \
    : [te] "s"(TE_), [fin] "s"(FIN_), [kh] "s"(kh), [sd] "s"(sd), [rs] "s"(rs), [l16] "v"(l16), [xp] "v"(xp), [pa] "v"(pa), [fm] "s"(fm), [pm] "s"(pm), [fo] "s"(fo), [po] "s"(po), [pd] "s"(pd), [tag] "s"(P.tag), "{v[68:75]}"(w[0][0]), "{v[76:83]}"(w[0][1]), "{v[84:91]}"(w[1][0]), "{v[92:99]}"(w[1][1]), "{v[100:107]}"(w[2][0]), "{v[108:115]}"(w[2][1]), "{v[116:123]}"(w[3][0]), "{v[124:131]}"(w[3][1]), "{v[132:139]}"(w[4][0]), "{v[140:147]}"(w[4][1]), "{v[148:155]}"(w[5][0]), "{v[156:163]}"(w[5][1]), "{v[164:171]}"(w[6][0]), "{v[172:179]}"(w[6][1]), "{v[180:187]}"(w[7][0]), "{v[188:195]}"(w[7][1]) \
    : "memory", "scc", "v0", "v1", "v2", "v3", "v4", "v5", "v6", "v7", "v8", "v9", "v10", "v11", "v12", "v13", "v14", "v15", "v44", "v45", "v46", "v47", "v48", "v49", "v50", "v51", "v52", "v53", "v54", "v55", "v56", "v57", "v58", "v59", "v60", "v61", "v62", "v63", "v64", "v65", "v66", "v67")

#define S_LOOP_X(TE_, FIN_) \
  asm volatile( \
    "s_cmp_eq_u32 %[kh], 0\n\t" \
    "s_cbranch_scc0 L1_%=\n\t" \
    "L0_%=:\n\t" \
    "s_setprio 0\n\t" \
    "s_nop 3\n\t" \
    "ds_write_b128 %[pa], v[196:199] offset:0\n\t" \
    "ds_write_b128 %[pa], v[200:203] offset:1024\n\t" \
    "ds_write_b128 %[pa], v[204:207] offset:2048\n\t" \
    "ds_write_b128 %[pa], v[208:211] offset:3072\n\t" \
    "s_sub_u32 %[a], %[t], 1\n\t" \
    "v_mov_b32 v53, %[tag]\n\t" \
    "v_mov_b32 v52, %[a]\n\t" \
    "s_sub_u32 %[b], %[t], 2\n\t" \
    "s_cmp_lt_u32 %[b], " STR(SQUICK) "\n\t" \
    "s_cselect_b64 exec, %[fm], 0\n\t" \
    "buffer_store_dwordx2 v[52:53], %[l16], %[rs], %[fo] offen sc1\n\t" \
    "s_mov_b64 exec, -1\n\t" \
    "s_add_u32 m0, %[sd], 49152\n\t" \
    "s_add_u32 %[a], %[sx], 278528\n\t" \
    "s_waitcnt lgkmcnt(0)\n\t" \
    "buffer_load_dwordx4 %[l16], %[rs], %[a] offen offset:0 sc1 lds\n\t" \
    "buffer_load_dwordx4 %[l16], %[rs], %[a] offen offset:1024 sc1 lds\n\t" \
    "buffer_load_dwordx4 %[l16], %[rs], %[a] offen offset:2048 sc1 lds\n\t" \
    "buffer_load_dwordx4 %[l16], %[rs], %[a] offen offset:3072 sc1 lds\n\t" \
    "v_mfma_f32_16x16x32_bf16 v[0:3], v[68:71], v[20:23], 0\n\t" \
    "ds_read_b128 v[44:47], %[xp] offset:6144\n\t" \
    "v_mfma_f32_16x16x32_bf16 v[4:7], v[76:79], v[20:23], 0\n\t" \
    "ds_read_b128 v[48:51], %[xp] offset:7168\n\t" \
    "v_mfma_f32_16x16x32_bf16 v[8:11], v[68:71], v[24:27], 0\n\t" \
    "v_mfma_f32_16x16x32_bf16 v[12:15], v[76:79], v[24:27], 0\n\t" \
    "v_mfma_f32_16x16x32_bf16 v[0:3], v[72:75], v[28:31], v[0:3]\n\t" \
    "ds_read_b128 v[20:23], %[xp] offset:8192\n\t" \
    "v_mfma_f32_16x16x32_bf16 v[4:7], v[80:83], v[28:31], v[4:7]\n\t" \
    "ds_read_b128 v[24:27], %[xp] offset:9216\n\t" \
    "v_mfma_f32_16x16x32_bf16 v[8:11], v[72:75], v[32:35], v[8:11]\n\t" \
    "v_mfma_f32_16x16x32_bf16 v[12:15], v[80:83], v[32:35], v[12:15]\n\t" \
    "v_mfma_f32_16x16x32_bf16 v[0:3], v[84:87], v[36:39], v[0:3]\n\t" \
    "ds_read_b128 v[28:31], %[xp] offset:10240\n\t" \
    "v_mfma_f32_16x16x32_bf16 v[4:7], v[92:95], v[36:39], v[4:7]\n\t" \
    "ds_read_b128 v[32:35], %[xp] offset:11264\n\t" \
    "v_mfma_f32_16x16x32_bf16 v[8:11], v[84:87], v[40:43], v[8:11]\n\t" \
    "s_waitcnt lgkmcnt(4)\n\t" \
    "v_mfma_f32_16x16x32_bf16 v[12:15], v[92:95], v[40:43], v[12:15]\n\t" \
    "v_mfma_f32_16x16x32_bf16 v[0:3], v[88:91], v[44:47], v[0:3]\n\t" \
    "ds_read_b128 v[36:39], %[xp] offset:12288\n\t" \
    "v_mfma_f32_16x16x32_bf16 v[4:7], v[96:99], v[44:47], v[4:7]\n\t" \
    "ds_read_b128 v[40:43], %[xp] offset:13312\n\t" \
    "v_mfma_f32_16x16x32_bf16 v[8:11], v[88:91], v[48:51], v[8:11]\n\t" \
    "s_waitcnt lgkmcnt(4)\n\t" \
    "v_mfma_f32_16x16x32_bf16 v[12:15], v[96:99], v[48:51], v[12:15]\n\t" \
    "v_mfma_f32_16x16x32_bf16 v[0:3], v[100:103], v[20:23], v[0:3]\n\t" \
    "ds_read_b128 v[44:47], %[xp] offset:14336\n\t" \
    "v_mfma_f32_16x16x32_bf16 v[4:7], v[108:111], v[20:23], v[4:7]\n\t" \
    "ds_read_b128 v[48:51], %[xp] offset:15360\n\t" \
    "v_mfma_f32_16x16x32_bf16 v[8:11], v[100:103], v[24:27], v[8:11]\n\t" \
    "s_waitcnt lgkmcnt(4)\n\t" \
    "v_mfma_f32_16x16x32_bf16 v[12:15], v[108:111], v[24:27], v[12:15]\n\t" \
    "v_mfma_f32_16x16x32_bf16 v[0:3], v[104:107], v[28:31], v[0:3]\n\t" \
    "ds_read_b128 v[20:23], %[xp] offset:16384\n\t" \
    "v_mfma_f32_16x16x32_bf16 v[4:7], v[112:115], v[28:31], v[4:7]\n\t" \
    "ds_read_b128 v[24:27], %[xp] offset:17408\n\t" \
    "v_mfma_f32_16x16x32_bf16 v[8:11], v[104:107], v[32:35], v[8:11]\n\t" \
    "s_waitcnt lgkmcnt(4)\n\t" \
    "v_mfma_f32_16x16x32_bf16 v[12:15], v[112:115], v[32:35], v[12:15]\n\t" \
    "v_mfma_f32_16x16x32_bf16 v[0:3], v[116:119], v[36:39], v[0:3]\n\t" \
    "ds_read_b128 v[28:31], %[xp] offset:18432\n\t" \
    "v_mfma_f32_16x16x32_bf16 v[4:7], v[124:127], v[36:39], v[4:7]\n\t" \
    "ds_read_b128 v[32:35], %[xp] offset:19456\n\t" \
    "v_mfma_f32_16x16x32_bf16 v[8:11], v[116:119], v[40:43], v[8:11]\n\t" \
    "s_waitcnt lgkmcnt(4)\n\t" \
    "v_mfma_f32_16x16x32_bf16 v[12:15], v[124:127], v[40:43], v[12:15]\n\t" \
    "v_mfma_f32_16x16x32_bf16 v[0:3], v[120:123], v[44:47], v[0:3]\n\t" \
    "ds_read_b128 v[36:39], %[xp] offset:20480\n\t" \
    "v_mfma_f32_16x16x32_bf16 v[4:7], v[128:131], v[44:47], v[4:7]\n\t" \
    "ds_read_b128 v[40:43], %[xp] offset:21504\n\t" \
    "v_mfma_f32_16x16x32_bf16 v[8:11], v[120:123], v[48:51], v[8:11]\n\t" \
    "s_waitcnt lgkmcnt(4)\n\t" \
    "v_mfma_f32_16x16x32_bf16 v[12:15], v[128:131], v[48:51], v[12:15]\n\t" \
    "s_waitcnt vmcnt(4)\n\t" \
    "s_barrier\n\t" \
    "s_setprio 0\n\t" \
    "s_sub_u32 %[a], %[t], 1\n\t" \
    "v_mov_b32 v53, %[tag]\n\t" \
    "v_mov_b32 v52, %[a]\n\t" \
    "s_cmp_gt_u32 %[t], 1\n\t" \
    "s_cselect_b64 exec, %[fm], 0\n\t" \
    "buffer_store_dwordx2 v[52:53], %[l16], %[rs], %[fo] offen sc1\n\t" \
    "s_mov_b64 exec, %[pm]\n\t" \
    "s_mov_b32 m0, %[pd]\n\t" \
    "s_add_u32 %[a], %[sx], %[po]\n\t" \
    "buffer_load_dwordx4 %[l16], %[rs], %[a] offen sc1 lds\n\t" \
    "s_mov_b64 exec, -1\n\t" \
    "s_add_u32 m0, %[sd], 0\n\t" \
    "s_add_u32 %[a], %[sx], 524288\n\t" \
    "buffer_load_dwordx4 %[l16], %[rs], %[a] offen offset:0 sc1 lds\n\t" \
    "buffer_load_dwordx4 %[l16], %[rs], %[a] offen offset:1024 sc1 lds\n\t" \
    "buffer_load_dwordx4 %[l16], %[rs], %[a] offen offset:2048 sc1 lds\n\t" \
    "buffer_load_dwordx4 %[l16], %[rs], %[a] offen offset:3072 sc1 lds\n\t" \
    "v_mfma_f32_16x16x32_bf16 v[0:3], v[132:135], v[20:23], v[0:3]\n\t" \
    "ds_read_b128 v[44:47], %[xp] offset:22528\n\t" \
    "v_mfma_f32_16x16x32_bf16 v[4:7], v[140:143], v[20:23], v[4:7]\n\t" \
    "ds_read_b128 v[48:51], %[xp] offset:23552\n\t" \
    "v_mfma_f32_16x16x32_bf16 v[8:11], v[132:135], v[24:27], v[8:11]\n\t" \
    "s_waitcnt lgkmcnt(4)\n\t" \
    "v_mfma_f32_16x16x32_bf16 v[12:15], v[140:143], v[24:27], v[12:15]\n\t" \
    "v_mfma_f32_16x16x32_bf16 v[0:3], v[136:139], v[28:31], v[0:3]\n\t" \
    "ds_read_b128 v[20:23], %[xp] offset:24576\n\t" \
    "v_mfma_f32_16x16x32_bf16 v[4:7], v[144:147], v[28:31], v[4:7]\n\t" \
    "ds_read_b128 v[24:27], %[xp] offset:25600\n\t" \
    "v_mfma_f32_16x16x32_bf16 v[8:11], v[136:139], v[32:35], v[8:11]\n\t" \
    "s_waitcnt lgkmcnt(4)\n\t" \
    "v_mfma_f32_16x16x32_bf16 v[12:15], v[144:147], v[32:35], v[12:15]\n\t" \
    "v_mfma_f32_16x16x32_bf16 v[0:3], v[148:151], v[36:39], v[0:3]\n\t" \
    "ds_read_b128 v[28:31], %[xp] offset:26624\n\t" \
    "v_mfma_f32_16x16x32_bf16 v[4:7], v[156:159], v[36:39], v[4:7]\n\t" \
    "ds_read_b128 v[32:35], %[xp] offset:27648\n\t" \
    "v_mfma_f32_16x16x32_bf16 v[8:11], v[148:151], v[40:43], v[8:11]\n\t" \
    "s_waitcnt lgkmcnt(4)\n\t" \
    "v_mfma_f32_16x16x32_bf16 v[12:15], v[156:159], v[40:43], v[12:15]\n\t" \
    "v_mfma_f32_16x16x32_bf16 v[0:3], v[152:155], v[44:47], v[0:3]\n\t" \
    "ds_read_b128 v[36:39], %[xp] offset:28672\n\t" \
    "v_mfma_f32_16x16x32_bf16 v[4:7], v[160:163], v[44:47], v[4:7]\n\t" \
    "ds_read_b128 v[40:43], %[xp] offset:29696\n\t" \
    "v_mfma_f32_16x16x32_bf16 v[8:11], v[152:155], v[48:51], v[8:11]\n\t" \
    "s_waitcnt lgkmcnt(4)\n\t" \
    "v_mfma_f32_16x16x32_bf16 v[12:15], v[160:163], v[48:51], v[12:15]\n\t" \
    "v_mfma_f32_16x16x32_bf16 v[0:3], v[164:167], v[20:23], v[0:3]\n\t" \
    "ds_read_b128 v[44:47], %[xp] offset:30720\n\t" \
    "v_mfma_f32_16x16x32_bf16 v[4:7], v[172:175], v[20:23], v[4:7]\n\t" \
    "ds_read_b128 v[48:51], %[xp] offset:31744\n\t" \
    "v_mfma_f32_16x16x32_bf16 v[8:11], v[164:167], v[24:27], v[8:11]\n\t" \
    "s_waitcnt lgkmcnt(4)\n\t" \
    "v_mfma_f32_16x16x32_bf16 v[12:15], v[172:175], v[24:27], v[12:15]\n\t" \
    "v_mfma_f32_16x16x32_bf16 v[0:3], v[168:171], v[28:31], v[0:3]\n\t" \
    "ds_read_b128 v[20:23], %[xp] offset:32768\n\t" \
    "v_mfma_f32_16x16x32_bf16 v[4:7], v[176:179], v[28:31], v[4:7]\n\t" \
    "ds_read_b128 v[24:27], %[xp] offset:33792\n\t" \
    "v_mfma_f32_16x16x32_bf16 v[8:11], v[168:171], v[32:35], v[8:11]\n\t" \
    "s_waitcnt lgkmcnt(4)\n\t" \
    "v_mfma_f32_16x16x32_bf16 v[12:15], v[176:179], v[32:35], v[12:15]\n\t" \
    "v_mfma_f32_16x16x32_bf16 v[0:3], v[180:183], v[36:39], v[0:3]\n\t" \
    "ds_read_b128 v[28:31], %[xp] offset:34816\n\t" \
    "v_mfma_f32_16x16x32_bf16 v[4:7], v[188:191], v[36:39], v[4:7]\n\t" \
    "ds_read_b128 v[32:35], %[xp] offset:35840\n\t" \
    "v_mfma_f32_16x16x32_bf16 v[8:11], v[180:183], v[40:43], v[8:11]\n\t" \
    "s_waitcnt lgkmcnt(4)\n\t" \
    "v_mfma_f32_16x16x32_bf16 v[12:15], v[188:191], v[40:43], v[12:15]\n\t" \
    "v_mfma_f32_16x16x32_bf16 v[0:3], v[184:187], v[44:47], v[0:3]\n\t" \
    "ds_read_b128 v[36:39], %[xp] offset:36864\n\t" \
    "v_mfma_f32_16x16x32_bf16 v[4:7], v[192:195], v[44:47], v[4:7]\n\t" \
    "ds_read_b128 v[40:43], %[xp] offset:37888\n\t" \
    "v_mfma_f32_16x16x32_bf16 v[8:11], v[184:187], v[48:51], v[8:11]\n\t" \
    "s_waitcnt lgkmcnt(4)\n\t" \
    "v_mfma_f32_16x16x32_bf16 v[12:15], v[192:195], v[48:51], v[12:15]\n\t" \
    "s_add_u32 %[sx], %[sx], 262144\n\t" \
    "s_add_u32 %[t], %[t], 1\n\t" \
    "s_waitcnt vmcnt(4)\n\t" \
    "s_barrier\n\t" \
    "s_setprio 0\n\t" \
    "s_nop 3\n\t" \
    "ds_write_b128 %[pa], v[0:3] offset:0\n\t" \
    "ds_write_b128 %[pa], v[4:7] offset:1024\n\t" \
    "ds_write_b128 %[pa], v[8:11] offset:2048\n\t" \
    "ds_write_b128 %[pa], v[12:15] offset:3072\n\t" \
    "s_sub_u32 %[a], %[t], 1\n\t" \
    "v_mov_b32 v53, %[tag]\n\t" \
    "v_mov_b32 v52, %[a]\n\t" \
    "s_sub_u32 %[b], %[t], 2\n\t" \
    "s_cmp_lt_u32 %[b], " STR(SQUICK) "\n\t" \
    "s_cselect_b64 exec, %[fm], 0\n\t" \
    "buffer_store_dwordx2 v[52:53], %[l16], %[rs], %[fo] offen sc1\n\t" \
    "s_mov_b64 exec, -1\n\t" \
    "s_add_u32 m0, %[sd], 16384\n\t" \
    "s_add_u32 %[a], %[sx], 278528\n\t" \
    "s_waitcnt lgkmcnt(0)\n\t" \
    "buffer_load_dwordx4 %[l16], %[rs], %[a] offen offset:0 sc1 lds\n\t" \
    "buffer_load_dwordx4 %[l16], %[rs], %[a] offen offset:1024 sc1 lds\n\t" \
    "buffer_load_dwordx4 %[l16], %[rs], %[a] offen offset:2048 sc1 lds\n\t" \
    "buffer_load_dwordx4 %[l16], %[rs], %[a] offen offset:3072 sc1 lds\n\t" \
    "v_mfma_f32_16x16x32_bf16 v[196:199], v[68:71], v[20:23], 0\n\t" \
    "ds_read_b128 v[44:47], %[xp] offset:38912\n\t" \
    "v_mfma_f32_16x16x32_bf16 v[200:203], v[76:79], v[20:23], 0\n\t" \
    "ds_read_b128 v[48:51], %[xp] offset:39936\n\t" \
    "v_mfma_f32_16x16x32_bf16 v[204:207], v[68:71], v[24:27], 0\n\t" \
    "v_mfma_f32_16x16x32_bf16 v[208:211], v[76:79], v[24:27], 0\n\t" \
    "v_mfma_f32_16x16x32_bf16 v[196:199], v[72:75], v[28:31], v[196:199]\n\t" \
    "ds_read_b128 v[20:23], %[xp] offset:40960\n\t" \
    "v_mfma_f32_16x16x32_bf16 v[200:203], v[80:83], v[28:31], v[200:203]\n\t" \
    "ds_read_b128 v[24:27], %[xp] offset:41984\n\t" \
    "v_mfma_f32_16x16x32_bf16 v[204:207], v[72:75], v[32:35], v[204:207]\n\t" \
    "v_mfma_f32_16x16x32_bf16 v[208:211], v[80:83], v[32:35], v[208:211]\n\t" \
    "v_mfma_f32_16x16x32_bf16 v[196:199], v[84:87], v[36:39], v[196:199]\n\t" \
    "ds_read_b128 v[28:31], %[xp] offset:43008\n\t" \
    "v_mfma_f32_16x16x32_bf16 v[200:203], v[92:95], v[36:39], v[200:203]\n\t" \
    "ds_read_b128 v[32:35], %[xp] offset:44032\n\t" \
    "v_mfma_f32_16x16x32_bf16 v[204:207], v[84:87], v[40:43], v[204:207]\n\t" \
    "s_waitcnt lgkmcnt(4)\n\t" \
    "v_mfma_f32_16x16x32_bf16 v[208:211], v[92:95], v[40:43], v[208:211]\n\t" \
    "v_mfma_f32_16x16x32_bf16 v[196:199], v[88:91], v[44:47], v[196:199]\n\t" \
    "ds_read_b128 v[36:39], %[xp] offset:45056\n\t" \
    "v_mfma_f32_16x16x32_bf16 v[200:203], v[96:99], v[44:47], v[200:203]\n\t" \
    "ds_read_b128 v[40:43], %[xp] offset:46080\n\t" \
    "v_mfma_f32_16x16x32_bf16 v[204:207], v[88:91], v[48:51], v[204:207]\n\t" \
    "s_waitcnt lgkmcnt(4)\n\t" \
    "v_mfma_f32_16x16x32_bf16 v[208:211], v[96:99], v[48:51], v[208:211]\n\t" \
    "v_mfma_f32_16x16x32_bf16 v[196:199], v[100:103], v[20:23], v[196:199]\n\t" \
    "ds_read_b128 v[44:47], %[xp] offset:47104\n\t" \
    "v_mfma_f32_16x16x32_bf16 v[200:203], v[108:111], v[20:23], v[200:203]\n\t" \
    "ds_read_b128 v[48:51], %[xp] offset:48128\n\t" \
    "v_mfma_f32_16x16x32_bf16 v[204:207], v[100:103], v[24:27], v[204:207]\n\t" \
    "s_waitcnt lgkmcnt(4)\n\t" \
    "v_mfma_f32_16x16x32_bf16 v[208:211], v[108:111], v[24:27], v[208:211]\n\t" \
    "v_mfma_f32_16x16x32_bf16 v[196:199], v[104:107], v[28:31], v[196:199]\n\t" \
    "ds_read_b128 v[20:23], %[xp] offset:49152\n\t" \
    "v_mfma_f32_16x16x32_bf16 v[200:203], v[112:115], v[28:31], v[200:203]\n\t" \
    "ds_read_b128 v[24:27], %[xp] offset:50176\n\t" \
    "v_mfma_f32_16x16x32_bf16 v[204:207], v[104:107], v[32:35], v[204:207]\n\t" \
    "s_waitcnt lgkmcnt(4)\n\t" \
    "v_mfma_f32_16x16x32_bf16 v[208:211], v[112:115], v[32:35], v[208:211]\n\t" \
    "v_mfma_f32_16x16x32_bf16 v[196:199], v[116:119], v[36:39], v[196:199]\n\t" \
    "ds_read_b128 v[28:31], %[xp] offset:51200\n\t" \
    "v_mfma_f32_16x16x32_bf16 v[200:203], v[124:127], v[36:39], v[200:203]\n\t" \
    "ds_read_b128 v[32:35], %[xp] offset:52224\n\t" \
    "v_mfma_f32_16x16x32_bf16 v[204:207], v[116:119], v[40:43], v[204:207]\n\t" \
    "s_waitcnt lgkmcnt(4)\n\t" \
    "v_mfma_f32_16x16x32_bf16 v[208:211], v[124:127], v[40:43], v[208:211]\n\t" \
    "v_mfma_f32_16x16x32_bf16 v[196:199], v[120:123], v[44:47], v[196:199]\n\t" \
    "ds_read_b128 v[36:39], %[xp] offset:53248\n\t" \
    "v_mfma_f32_16x16x32_bf16 v[200:203], v[128:131], v[44:47], v[200:203]\n\t" \
    "ds_read_b128 v[40:43], %[xp] offset:54272\n\t" \
    "v_mfma_f32_16x16x32_bf16 v[204:207], v[120:123], v[48:51], v[204:207]\n\t" \
    "s_waitcnt lgkmcnt(4)\n\t" \
    "v_mfma_f32_16x16x32_bf16 v[208:211], v[128:131], v[48:51], v[208:211]\n\t" \
    "s_waitcnt vmcnt(4)\n\t" \
    "s_barrier\n\t" \
    "s_setprio 0\n\t" \
    "s_sub_u32 %[a], %[t], 1\n\t" \
    "v_mov_b32 v53, %[tag]\n\t" \
    "v_mov_b32 v52, %[a]\n\t" \
    "s_cmp_gt_u32 %[t], 1\n\t" \
    "s_cselect_b64 exec, %[fm], 0\n\t" \
    "buffer_store_dwordx2 v[52:53], %[l16], %[rs], %[fo] offen sc1\n\t" \
    "s_mov_b64 exec, %[pm]\n\t" \
    "s_mov_b32 m0, %[pd]\n\t" \
    "s_add_u32 %[a], %[sx], %[po]\n\t" \
    "buffer_load_dwordx4 %[l16], %[rs], %[a] offen sc1 lds\n\t" \
    "s_mov_b64 exec, -1\n\t" \
    "s_add_u32 m0, %[sd], 32768\n\t" \
    "s_add_u32 %[a], %[sx], 524288\n\t" \
    "buffer_load_dwordx4 %[l16], %[rs], %[a] offen offset:0 sc1 lds\n\t" \
    "buffer_load_dwordx4 %[l16], %[rs], %[a] offen offset:1024 sc1 lds\n\t" \
    "buffer_load_dwordx4 %[l16], %[rs], %[a] offen offset:2048 sc1 lds\n\t" \
    "buffer_load_dwordx4 %[l16], %[rs], %[a] offen offset:3072 sc1 lds\n\t" \
    "v_mfma_f32_16x16x32_bf16 v[196:199], v[132:135], v[20:23], v[196:199]\n\t" \
    "ds_read_b128 v[44:47], %[xp] offset:55296\n\t" \
    "v_mfma_f32_16x16x32_bf16 v[200:203], v[140:143], v[20:23], v[200:203]\n\t" \
    "ds_read_b128 v[48:51], %[xp] offset:56320\n\t" \
    "v_mfma_f32_16x16x32_bf16 v[204:207], v[132:135], v[24:27], v[204:207]\n\t" \
    "s_waitcnt lgkmcnt(4)\n\t" \
    "v_mfma_f32_16x16x32_bf16 v[208:211], v[140:143], v[24:27], v[208:211]\n\t" \
    "v_mfma_f32_16x16x32_bf16 v[196:199], v[136:139], v[28:31], v[196:199]\n\t" \
    "ds_read_b128 v[20:23], %[xp] offset:57344\n\t" \
    "v_mfma_f32_16x16x32_bf16 v[200:203], v[144:147], v[28:31], v[200:203]\n\t" \
    "ds_read_b128 v[24:27], %[xp] offset:58368\n\t" \
    "v_mfma_f32_16x16x32_bf16 v[204:207], v[136:139], v[32:35], v[204:207]\n\t" \
    "s_waitcnt lgkmcnt(4)\n\t" \
    "v_mfma_f32_16x16x32_bf16 v[208:211], v[144:147], v[32:35], v[208:211]\n\t" \
    "v_mfma_f32_16x16x32_bf16 v[196:199], v[148:151], v[36:39], v[196:199]\n\t" \
    "ds_read_b128 v[28:31], %[xp] offset:59392\n\t" \
    "v_mfma_f32_16x16x32_bf16 v[200:203], v[156:159], v[36:39], v[200:203]\n\t" \
    "ds_read_b128 v[32:35], %[xp] offset:60416\n\t" \
    "v_mfma_f32_16x16x32_bf16 v[204:207], v[148:151], v[40:43], v[204:207]\n\t" \
    "s_waitcnt lgkmcnt(4)\n\t" \
    "v_mfma_f32_16x16x32_bf16 v[208:211], v[156:159], v[40:43], v[208:211]\n\t" \
    "v_mfma_f32_16x16x32_bf16 v[196:199], v[152:155], v[44:47], v[196:199]\n\t" \
    "ds_read_b128 v[36:39], %[xp] offset:61440\n\t" \
    "v_mfma_f32_16x16x32_bf16 v[200:203], v[160:163], v[44:47], v[200:203]\n\t" \
    "ds_read_b128 v[40:43], %[xp] offset:62464\n\t" \
    "v_mfma_f32_16x16x32_bf16 v[204:207], v[152:155], v[48:51], v[204:207]\n\t" \
    "s_waitcnt lgkmcnt(4)\n\t" \
    "v_mfma_f32_16x16x32_bf16 v[208:211], v[160:163], v[48:51], v[208:211]\n\t" \
    "v_mfma_f32_16x16x32_bf16 v[196:199], v[164:167], v[20:23], v[196:199]\n\t" \
    "ds_read_b128 v[44:47], %[xp] offset:63488\n\t" \
    "v_mfma_f32_16x16x32_bf16 v[200:203], v[172:175], v[20:23], v[200:203]\n\t" \
    "ds_read_b128 v[48:51], %[xp] offset:64512\n\t" \
    "v_mfma_f32_16x16x32_bf16 v[204:207], v[164:167], v[24:27], v[204:207]\n\t" \
    "s_waitcnt lgkmcnt(4)\n\t" \
    "v_mfma_f32_16x16x32_bf16 v[208:211], v[172:175], v[24:27], v[208:211]\n\t" \
    "v_mfma_f32_16x16x32_bf16 v[196:199], v[168:171], v[28:31], v[196:199]\n\t" \
    "ds_read_b128 v[20:23], %[xp] offset:0\n\t" \
    "v_mfma_f32_16x16x32_bf16 v[200:203], v[176:179], v[28:31], v[200:203]\n\t" \
    "ds_read_b128 v[24:27], %[xp] offset:1024\n\t" \
    "v_mfma_f32_16x16x32_bf16 v[204:207], v[168:171], v[32:35], v[204:207]\n\t" \
    "s_waitcnt lgkmcnt(4)\n\t" \
    "v_mfma_f32_16x16x32_bf16 v[208:211], v[176:179], v[32:35], v[208:211]\n\t" \
    "v_mfma_f32_16x16x32_bf16 v[196:199], v[180:183], v[36:39], v[196:199]\n\t" \
    "ds_read_b128 v[28:31], %[xp] offset:2048\n\t" \
    "v_mfma_f32_16x16x32_bf16 v[200:203], v[188:191], v[36:39], v[200:203]\n\t" \
    "ds_read_b128 v[32:35], %[xp] offset:3072\n\t" \
    "v_mfma_f32_16x16x32_bf16 v[204:207], v[180:183], v[40:43], v[204:207]\n\t" \
    "s_waitcnt lgkmcnt(4)\n\t" \
    "v_mfma_f32_16x16x32_bf16 v[208:211], v[188:191], v[40:43], v[208:211]\n\t" \
    "v_mfma_f32_16x16x32_bf16 v[196:199], v[184:187], v[44:47], v[196:199]\n\t" \
    "ds_read_b128 v[36:39], %[xp] offset:4096\n\t" \
    "v_mfma_f32_16x16x32_bf16 v[200:203], v[192:195], v[44:47], v[200:203]\n\t" \
    "ds_read_b128 v[40:43], %[xp] offset:5120\n\t" \
    "v_mfma_f32_16x16x32_bf16 v[204:207], v[184:187], v[48:51], v[204:207]\n\t" \
    "s_waitcnt lgkmcnt(4)\n\t" \
    "v_mfma_f32_16x16x32_bf16 v[208:211], v[192:195], v[48:51], v[208:211]\n\t" \
    "s_add_u32 %[sx], %[sx], 262144\n\t" \
    "s_add_u32 %[t], %[t], 1\n\t" \
    "s_waitcnt vmcnt(4)\n\t" \
    "s_barrier\n\t" \
    "s_cmp_lt_u32 %[t], %[te]\n\t" \
    "s_cbranch_scc1 L0_%=\n\t" \
    "s_cmp_eq_u32 %[fin], %[te]\n\t" \
    "s_cbranch_scc0 E_%=\n\t" \
    "s_nop 4\n\t" \
    "ds_write_b128 %[pa], v[196:199] offset:0\n\t" \
    "ds_write_b128 %[pa], v[200:203] offset:1024\n\t" \
    "ds_write_b128 %[pa], v[204:207] offset:2048\n\t" \
    "ds_write_b128 %[pa], v[208:211] offset:3072\n\t" \
    "s_waitcnt lgkmcnt(0)\n\t" \
    "s_barrier\n\t" \
    "s_branch E_%=\n\t" \
    "L1_%=:\n\t" \
    "s_setprio 1\n\t" \
    "s_add_u32 m0, %[sd], 49152\n\t" \
    "s_add_u32 %[a], %[sx], 278528\n\t" \
    "v_mfma_f32_16x16x32_bf16 v[0:3], v[68:71], v[20:23], 0\n\t" \
    "ds_read_b128 v[44:47], %[xp] offset:6144\n\t" \
    "v_mfma_f32_16x16x32_bf16 v[4:7], v[76:79], v[20:23], 0\n\t" \
    "ds_read_b128 v[48:51], %[xp] offset:7168\n\t" \
    "v_mfma_f32_16x16x32_bf16 v[8:11], v[68:71], v[24:27], 0\n\t" \
    "s_waitcnt lgkmcnt(4)\n\t" \
    "v_mfma_f32_16x16x32_bf16 v[12:15], v[76:79], v[24:27], 0\n\t" \
    "v_mfma_f32_16x16x32_bf16 v[0:3], v[72:75], v[28:31], v[0:3]\n\t" \
    "ds_read_b128 v[20:23], %[xp] offset:8192\n\t" \
    "v_mfma_f32_16x16x32_bf16 v[4:7], v[80:83], v[28:31], v[4:7]\n\t" \
    "ds_read_b128 v[24:27], %[xp] offset:9216\n\t" \
    "v_mfma_f32_16x16x32_bf16 v[8:11], v[72:75], v[32:35], v[8:11]\n\t" \
    "s_waitcnt lgkmcnt(4)\n\t" \
    "v_mfma_f32_16x16x32_bf16 v[12:15], v[80:83], v[32:35], v[12:15]\n\t" \
    "v_mfma_f32_16x16x32_bf16 v[0:3], v[84:87], v[36:39], v[0:3]\n\t" \
    "ds_read_b128 v[28:31], %[xp] offset:10240\n\t" \
    "v_mfma_f32_16x16x32_bf16 v[4:7], v[92:95], v[36:39], v[4:7]\n\t" \
    "ds_read_b128 v[32:35], %[xp] offset:11264\n\t" \
    "v_mfma_f32_16x16x32_bf16 v[8:11], v[84:87], v[40:43], v[8:11]\n\t" \
    "s_waitcnt lgkmcnt(4)\n\t" \
    "v_mfma_f32_16x16x32_bf16 v[12:15], v[92:95], v[40:43], v[12:15]\n\t" \
    "v_mfma_f32_16x16x32_bf16 v[0:3], v[88:91], v[44:47], v[0:3]\n\t" \
    "ds_read_b128 v[36:39], %[xp] offset:12288\n\t" \
    "v_mfma_f32_16x16x32_bf16 v[4:7], v[96:99], v[44:47], v[4:7]\n\t" \
    "ds_read_b128 v[40:43], %[xp] offset:13312\n\t" \
    "v_mfma_f32_16x16x32_bf16 v[8:11], v[88:91], v[48:51], v[8:11]\n\t" \
    "s_waitcnt lgkmcnt(4)\n\t" \
    "v_mfma_f32_16x16x32_bf16 v[12:15], v[96:99], v[48:51], v[12:15]\n\t" \
    "v_mfma_f32_16x16x32_bf16 v[0:3], v[100:103], v[20:23], v[0:3]\n\t" \
    "ds_read_b128 v[44:47], %[xp] offset:14336\n\t" \
    "v_mfma_f32_16x16x32_bf16 v[4:7], v[108:111], v[20:23], v[4:7]\n\t" \
    "ds_read_b128 v[48:51], %[xp] offset:15360\n\t" \
    "v_mfma_f32_16x16x32_bf16 v[8:11], v[100:103], v[24:27], v[8:11]\n\t" \
    "s_waitcnt lgkmcnt(4)\n\t" \
    "v_mfma_f32_16x16x32_bf16 v[12:15], v[108:111], v[24:27], v[12:15]\n\t" \
    "buffer_load_dwordx4 %[l16], %[rs], %[a] offen offset:0 sc1 lds\n\t" \
    "v_mfma_f32_16x16x32_bf16 v[0:3], v[104:107], v[28:31], v[0:3]\n\t" \
    "ds_read_b128 v[20:23], %[xp] offset:16384\n\t" \
    "v_mfma_f32_16x16x32_bf16 v[4:7], v[112:115], v[28:31], v[4:7]\n\t" \
    "ds_read_b128 v[24:27], %[xp] offset:17408\n\t" \
    "v_mfma_f32_16x16x32_bf16 v[8:11], v[104:107], v[32:35], v[8:11]\n\t" \
    "s_waitcnt lgkmcnt(4)\n\t" \
    "v_mfma_f32_16x16x32_bf16 v[12:15], v[112:115], v[32:35], v[12:15]\n\t" \
    "buffer_load_dwordx4 %[l16], %[rs], %[a] offen offset:1024 sc1 lds\n\t" \
    "v_mfma_f32_16x16x32_bf16 v[0:3], v[116:119], v[36:39], v[0:3]\n\t" \
    "ds_read_b128 v[28:31], %[xp] offset:18432\n\t" \
    "v_mfma_f32_16x16x32_bf16 v[4:7], v[124:127], v[36:39], v[4:7]\n\t" \
    "ds_read_b128 v[32:35], %[xp] offset:19456\n\t" \
    "v_mfma_f32_16x16x32_bf16 v[8:11], v[116:119], v[40:43], v[8:11]\n\t" \
    "s_waitcnt lgkmcnt(4)\n\t" \
    "v_mfma_f32_16x16x32_bf16 v[12:15], v[124:127], v[40:43], v[12:15]\n\t" \
    "buffer_load_dwordx4 %[l16], %[rs], %[a] offen offset:2048 sc1 lds\n\t" \
    "v_mfma_f32_16x16x32_bf16 v[0:3], v[120:123], v[44:47], v[0:3]\n\t" \
    "ds_read_b128 v[36:39], %[xp] offset:20480\n\t" \
    "v_mfma_f32_16x16x32_bf16 v[4:7], v[128:131], v[44:47], v[4:7]\n\t" \
    "ds_read_b128 v[40:43], %[xp] offset:21504\n\t" \
    "v_mfma_f32_16x16x32_bf16 v[8:11], v[120:123], v[48:51], v[8:11]\n\t" \
    "s_waitcnt lgkmcnt(4)\n\t" \
    "v_mfma_f32_16x16x32_bf16 v[12:15], v[128:131], v[48:51], v[12:15]\n\t" \
    "buffer_load_dwordx4 %[l16], %[rs], %[a] offen offset:3072 sc1 lds\n\t" \
    "s_waitcnt vmcnt(4)\n\t" \
    "s_barrier\n\t" \
    "s_setprio 1\n\t" \
    "s_add_u32 m0, %[sd], 0\n\t" \
    "s_add_u32 %[a], %[sx], 524288\n\t" \
    "s_add_u32 %[b], %[oo], 0x1000\n\t" \
    "s_cmp_lg_u32 %[t], 0\n\t" \
    "s_cselect_b64 %[em], -1, 0\n\t" \
    "v_mfma_f32_16x16x32_bf16 v[0:3], v[132:135], v[20:23], v[0:3]\n\t" \
    "ds_read_b128 v[44:47], %[xp] offset:22528\n\t" \
    "v_mfma_f32_16x16x32_bf16 v[4:7], v[140:143], v[20:23], v[4:7]\n\t" \
    "ds_read_b128 v[48:51], %[xp] offset:23552\n\t" \
    "v_mfma_f32_16x16x32_bf16 v[8:11], v[132:135], v[24:27], v[8:11]\n\t" \
    "ds_read_b128 v[52:55], %[pa] offset:0\n\t" \
    "s_waitcnt lgkmcnt(5)\n\t" \
    "v_mfma_f32_16x16x32_bf16 v[12:15], v[140:143], v[24:27], v[12:15]\n\t" \
    "buffer_load_dwordx4 %[l16], %[rs], %[a] offen offset:0 sc1 lds\n\t" \
    "v_mfma_f32_16x16x32_bf16 v[0:3], v[136:139], v[28:31], v[0:3]\n\t" \
    "ds_read_b128 v[20:23], %[xp] offset:24576\n\t" \
    "v_mfma_f32_16x16x32_bf16 v[4:7], v[144:147], v[28:31], v[4:7]\n\t" \
    "ds_read_b128 v[24:27], %[xp] offset:25600\n\t" \
    "v_mfma_f32_16x16x32_bf16 v[8:11], v[136:139], v[32:35], v[8:11]\n\t" \
    "ds_read_b128 v[56:59], %[pa] offset:1024\n\t" \
    "s_waitcnt lgkmcnt(6)\n\t" \
    "v_mfma_f32_16x16x32_bf16 v[12:15], v[144:147], v[32:35], v[12:15]\n\t" \
    "buffer_load_dwordx4 %[l16], %[rs], %[a] offen offset:1024 sc1 lds\n\t" \
    "v_mfma_f32_16x16x32_bf16 v[0:3], v[148:151], v[36:39], v[0:3]\n\t" \
    "ds_read_b128 v[28:31], %[xp] offset:26624\n\t" \
    "v_mfma_f32_16x16x32_bf16 v[4:7], v[156:159], v[36:39], v[4:7]\n\t" \
    "ds_read_b128 v[32:35], %[xp] offset:27648\n\t" \
    "v_mfma_f32_16x16x32_bf16 v[8:11], v[148:151], v[40:43], v[8:11]\n\t" \
    "ds_read_b128 v[60:63], %[pa] offset:2048\n\t" \
    "s_waitcnt lgkmcnt(7)\n\t" \
    "v_mfma_f32_16x16x32_bf16 v[12:15], v[156:159], v[40:43], v[12:15]\n\t" \
    "buffer_load_dwordx4 %[l16], %[rs], %[a] offen offset:2048 sc1 lds\n\t" \
    "v_mfma_f32_16x16x32_bf16 v[0:3], v[152:155], v[44:47], v[0:3]\n\t" \
    "ds_read_b128 v[36:39], %[xp] offset:28672\n\t" \
    "s_waitcnt lgkmcnt(7)\n\t" \
    "v_add_f32 v196, v196, v52\n\t" \
    "v_mfma_f32_16x16x32_bf16 v[4:7], v[160:163], v[44:47], v[4:7]\n\t" \
    "ds_read_b128 v[40:43], %[xp] offset:29696\n\t" \
    "v_add_f32 v197, v197, v53\n\t" \
    "v_mfma_f32_16x16x32_bf16 v[8:11], v[152:155], v[48:51], v[8:11]\n\t" \
    "ds_read_b128 v[64:67], %[pa] offset:3072\n\t" \
    "s_waitcnt lgkmcnt(7)\n\t" \
    "v_mfma_f32_16x16x32_bf16 v[12:15], v[160:163], v[48:51], v[12:15]\n\t" \
    "buffer_load_dwordx4 %[l16], %[rs], %[a] offen offset:3072 sc1 lds\n\t" \
    "v_add_f32 v198, v198, v54\n\t" \
    "v_add_f32 v199, v199, v55\n\t" \
    "v_mfma_f32_16x16x32_bf16 v[0:3], v[164:167], v[20:23], v[0:3]\n\t" \
    "ds_read_b128 v[44:47], %[xp] offset:30720\n\t" \
    "s_waitcnt lgkmcnt(7)\n\t" \
    "v_add_f32 v200, v200, v56\n\t" \
    "v_mfma_f32_16x16x32_bf16 v[4:7], v[172:175], v[20:23], v[4:7]\n\t" \
    "ds_read_b128 v[48:51], %[xp] offset:31744\n\t" \
    "v_add_f32 v201, v201, v57\n\t" \
    "v_mfma_f32_16x16x32_bf16 v[8:11], v[164:167], v[24:27], v[8:11]\n\t" \
    "v_add_f32 v202, v202, v58\n\t" \
    "s_waitcnt lgkmcnt(6)\n\t" \
    "v_mfma_f32_16x16x32_bf16 v[12:15], v[172:175], v[24:27], v[12:15]\n\t" \
    "v_add_f32 v203, v203, v59\n\t" \
    "s_waitcnt lgkmcnt(5)\n\t" \
    "v_add_f32 v204, v204, v60\n\t" \
    "v_add_f32 v205, v205, v61\n\t" \
    "v_mfma_f32_16x16x32_bf16 v[0:3], v[168:171], v[28:31], v[0:3]\n\t" \
    "ds_read_b128 v[20:23], %[xp] offset:32768\n\t" \
    "v_add_f32 v206, v206, v62\n\t" \
    "v_mfma_f32_16x16x32_bf16 v[4:7], v[176:179], v[28:31], v[4:7]\n\t" \
    "ds_read_b128 v[24:27], %[xp] offset:33792\n\t" \
    "v_add_f32 v207, v207, v63\n\t" \
    "v_mfma_f32_16x16x32_bf16 v[8:11], v[168:171], v[32:35], v[8:11]\n\t" \
    "s_waitcnt lgkmcnt(4)\n\t" \
    "v_add_f32 v208, v208, v64\n\t" \
    "v_mfma_f32_16x16x32_bf16 v[12:15], v[176:179], v[32:35], v[12:15]\n\t" \
    "v_add_f32 v209, v209, v65\n\t" \
    "v_add_f32 v210, v210, v66\n\t" \
    "v_add_f32 v211, v211, v67\n\t" \
    "v_mfma_f32_16x16x32_bf16 v[0:3], v[180:183], v[36:39], v[0:3]\n\t" \
    "ds_read_b128 v[28:31], %[xp] offset:34816\n\t" \
    "v_cvt_pk_bf16_f32 v52, v196, v200\n\t" \
    "v_mfma_f32_16x16x32_bf16 v[4:7], v[188:191], v[36:39], v[4:7]\n\t" \
    "ds_read_b128 v[32:35], %[xp] offset:35840\n\t" \
    "v_cvt_pk_bf16_f32 v53, v197, v201\n\t" \
    "v_mfma_f32_16x16x32_bf16 v[8:11], v[180:183], v[40:43], v[8:11]\n\t" \
    "v_cvt_pk_bf16_f32 v54, v198, v202\n\t" \
    "s_waitcnt lgkmcnt(4)\n\t" \
    "v_mfma_f32_16x16x32_bf16 v[12:15], v[188:191], v[40:43], v[12:15]\n\t" \
    "v_cvt_pk_bf16_f32 v55, v199, v203\n\t" \
    "v_cvt_pk_bf16_f32 v56, v204, v208\n\t" \
    "v_cvt_pk_bf16_f32 v57, v205, v209\n\t" \
    "v_mfma_f32_16x16x32_bf16 v[0:3], v[184:187], v[44:47], v[0:3]\n\t" \
    "ds_read_b128 v[36:39], %[xp] offset:36864\n\t" \
    "v_cvt_pk_bf16_f32 v58, v206, v210\n\t" \
    "v_mfma_f32_16x16x32_bf16 v[4:7], v[192:195], v[44:47], v[4:7]\n\t" \
    "ds_read_b128 v[40:43], %[xp] offset:37888\n\t" \
    "v_cvt_pk_bf16_f32 v59, v207, v211\n\t" \
    "v_mfma_f32_16x16x32_bf16 v[8:11], v[184:187], v[48:51], v[8:11]\n\t" \
    "s_waitcnt lgkmcnt(4)\n\t" \
    "v_mfma_f32_16x16x32_bf16 v[12:15], v[192:195], v[48:51], v[12:15]\n\t" \
    "s_mov_b64 exec, %[em]\n\t" \
    "buffer_store_dwordx4 v[52:55], %[l16], %[rs], %[oo] offen sc1\n\t" \
    "buffer_store_dwordx4 v[56:59], %[l16], %[rs], %[b] offen sc1\n\t" \
    "s_mov_b64 exec, -1\n\t" \
    "s_add_u32 %[sx], %[sx], 262144\n\t" \
    "s_add_u32 %[t], %[t], 1\n\t" \
    "s_add_u32 %[oo], %[oo], 262144\n\t" \
    "s_waitcnt vmcnt(6)\n\t" \
    "s_cmp_gt_u32 %[t], " STR(SQUICK) " + 1\n\t" \
    "s_cbranch_scc1 B0_%=\n\t" \
    "s_waitcnt vmcnt(0)\n\t" \
    "B0_%=:\n\t" \
    "s_barrier\n\t" \
    "s_setprio 1\n\t" \
    "s_add_u32 m0, %[sd], 16384\n\t" \
    "s_add_u32 %[a], %[sx], 278528\n\t" \
    "v_mfma_f32_16x16x32_bf16 v[196:199], v[68:71], v[20:23], 0\n\t" \
    "ds_read_b128 v[44:47], %[xp] offset:38912\n\t" \
    "v_mfma_f32_16x16x32_bf16 v[200:203], v[76:79], v[20:23], 0\n\t" \
    "ds_read_b128 v[48:51], %[xp] offset:39936\n\t" \
    "v_mfma_f32_16x16x32_bf16 v[204:207], v[68:71], v[24:27], 0\n\t" \
    "s_waitcnt lgkmcnt(4)\n\t" \
    "v_mfma_f32_16x16x32_bf16 v[208:211], v[76:79], v[24:27], 0\n\t" \
    "v_mfma_f32_16x16x32_bf16 v[196:199], v[72:75], v[28:31], v[196:199]\n\t" \
    "ds_read_b128 v[20:23], %[xp] offset:40960\n\t" \
    "v_mfma_f32_16x16x32_bf16 v[200:203], v[80:83], v[28:31], v[200:203]\n\t" \
    "ds_read_b128 v[24:27], %[xp] offset:41984\n\t" \
    "v_mfma_f32_16x16x32_bf16 v[204:207], v[72:75], v[32:35], v[204:207]\n\t" \
    "s_waitcnt lgkmcnt(4)\n\t" \
    "v_mfma_f32_16x16x32_bf16 v[208:211], v[80:83], v[32:35], v[208:211]\n\t" \
    "v_mfma_f32_16x16x32_bf16 v[196:199], v[84:87], v[36:39], v[196:199]\n\t" \
    "ds_read_b128 v[28:31], %[xp] offset:43008\n\t" \
    "v_mfma_f32_16x16x32_bf16 v[200:203], v[92:95], v[36:39], v[200:203]\n\t" \
    "ds_read_b128 v[32:35], %[xp] offset:44032\n\t" \
    "v_mfma_f32_16x16x32_bf16 v[204:207], v[84:87], v[40:43], v[204:207]\n\t" \
    "s_waitcnt lgkmcnt(4)\n\t" \
    "v_mfma_f32_16x16x32_bf16 v[208:211], v[92:95], v[40:43], v[208:211]\n\t" \
    "v_mfma_f32_16x16x32_bf16 v[196:199], v[88:91], v[44:47], v[196:199]\n\t" \
    "ds_read_b128 v[36:39], %[xp] offset:45056\n\t" \
    "v_mfma_f32_16x16x32_bf16 v[200:203], v[96:99], v[44:47], v[200:203]\n\t" \
    "ds_read_b128 v[40:43], %[xp] offset:46080\n\t" \
    "v_mfma_f32_16x16x32_bf16 v[204:207], v[88:91], v[48:51], v[204:207]\n\t" \
    "s_waitcnt lgkmcnt(4)\n\t" \
    "v_mfma_f32_16x16x32_bf16 v[208:211], v[96:99], v[48:51], v[208:211]\n\t" \
    "v_mfma_f32_16x16x32_bf16 v[196:199], v[100:103], v[20:23], v[196:199]\n\t" \
    "ds_read_b128 v[44:47], %[xp] offset:47104\n\t" \
    "v_mfma_f32_16x16x32_bf16 v[200:203], v[108:111], v[20:23], v[200:203]\n\t" \
    "ds_read_b128 v[48:51], %[xp] offset:48128\n\t" \
    "v_mfma_f32_16x16x32_bf16 v[204:207], v[100:103], v[24:27], v[204:207]\n\t" \
    "s_waitcnt lgkmcnt(4)\n\t" \
    "v_mfma_f32_16x16x32_bf16 v[208:211], v[108:111], v[24:27], v[208:211]\n\t" \
    "buffer_load_dwordx4 %[l16], %[rs], %[a] offen offset:0 sc1 lds\n\t" \
    "v_mfma_f32_16x16x32_bf16 v[196:199], v[104:107], v[28:31], v[196:199]\n\t" \
    "ds_read_b128 v[20:23], %[xp] offset:49152\n\t" \
    "v_mfma_f32_16x16x32_bf16 v[200:203], v[112:115], v[28:31], v[200:203]\n\t" \
    "ds_read_b128 v[24:27], %[xp] offset:50176\n\t" \
    "v_mfma_f32_16x16x32_bf16 v[204:207], v[104:107], v[32:35], v[204:207]\n\t" \
    "s_waitcnt lgkmcnt(4)\n\t" \
    "v_mfma_f32_16x16x32_bf16 v[208:211], v[112:115], v[32:35], v[208:211]\n\t" \
    "buffer_load_dwordx4 %[l16], %[rs], %[a] offen offset:1024 sc1 lds\n\t" \
    "v_mfma_f32_16x16x32_bf16 v[196:199], v[116:119], v[36:39], v[196:199]\n\t" \
    "ds_read_b128 v[28:31], %[xp] offset:51200\n\t" \
    "v_mfma_f32_16x16x32_bf16 v[200:203], v[124:127], v[36:39], v[200:203]\n\t" \
    "ds_read_b128 v[32:35], %[xp] offset:52224\n\t" \
    "v_mfma_f32_16x16x32_bf16 v[204:207], v[116:119], v[40:43], v[204:207]\n\t" \
    "s_waitcnt lgkmcnt(4)\n\t" \
    "v_mfma_f32_16x16x32_bf16 v[208:211], v[124:127], v[40:43], v[208:211]\n\t" \
    "buffer_load_dwordx4 %[l16], %[rs], %[a] offen offset:2048 sc1 lds\n\t" \
    "v_mfma_f32_16x16x32_bf16 v[196:199], v[120:123], v[44:47], v[196:199]\n\t" \
    "ds_read_b128 v[36:39], %[xp] offset:53248\n\t" \
    "v_mfma_f32_16x16x32_bf16 v[200:203], v[128:131], v[44:47], v[200:203]\n\t" \
    "ds_read_b128 v[40:43], %[xp] offset:54272\n\t" \
    "v_mfma_f32_16x16x32_bf16 v[204:207], v[120:123], v[48:51], v[204:207]\n\t" \
    "s_waitcnt lgkmcnt(4)\n\t" \
    "v_mfma_f32_16x16x32_bf16 v[208:211], v[128:131], v[48:51], v[208:211]\n\t" \
    "buffer_load_dwordx4 %[l16], %[rs], %[a] offen offset:3072 sc1 lds\n\t" \
    "s_waitcnt vmcnt(4)\n\t" \
    "s_barrier\n\t" \
    "s_setprio 1\n\t" \
    "s_add_u32 m0, %[sd], 32768\n\t" \
    "s_add_u32 %[a], %[sx], 524288\n\t" \
    "s_add_u32 %[b], %[oo], 0x1000\n\t" \
    "s_cmp_lg_u32 %[t], 0\n\t" \
    "s_cselect_b64 %[em], -1, 0\n\t" \
    "v_mfma_f32_16x16x32_bf16 v[196:199], v[132:135], v[20:23], v[196:199]\n\t" \
    "ds_read_b128 v[44:47], %[xp] offset:55296\n\t" \
    "v_mfma_f32_16x16x32_bf16 v[200:203], v[140:143], v[20:23], v[200:203]\n\t" \
    "ds_read_b128 v[48:51], %[xp] offset:56320\n\t" \
    "v_mfma_f32_16x16x32_bf16 v[204:207], v[132:135], v[24:27], v[204:207]\n\t" \
    "ds_read_b128 v[52:55], %[pa] offset:0\n\t" \
    "s_waitcnt lgkmcnt(5)\n\t" \
    "v_mfma_f32_16x16x32_bf16 v[208:211], v[140:143], v[24:27], v[208:211]\n\t" \
    "buffer_load_dwordx4 %[l16], %[rs], %[a] offen offset:0 sc1 lds\n\t" \
    "v_mfma_f32_16x16x32_bf16 v[196:199], v[136:139], v[28:31], v[196:199]\n\t" \
    "ds_read_b128 v[20:23], %[xp] offset:57344\n\t" \
    "v_mfma_f32_16x16x32_bf16 v[200:203], v[144:147], v[28:31], v[200:203]\n\t" \
    "ds_read_b128 v[24:27], %[xp] offset:58368\n\t" \
    "v_mfma_f32_16x16x32_bf16 v[204:207], v[136:139], v[32:35], v[204:207]\n\t" \
    "ds_read_b128 v[56:59], %[pa] offset:1024\n\t" \
    "s_waitcnt lgkmcnt(6)\n\t" \
    "v_mfma_f32_16x16x32_bf16 v[208:211], v[144:147], v[32:35], v[208:211]\n\t" \
    "buffer_load_dwordx4 %[l16], %[rs], %[a] offen offset:1024 sc1 lds\n\t" \
    "v_mfma_f32_16x16x32_bf16 v[196:199], v[148:151], v[36:39], v[196:199]\n\t" \
    "ds_read_b128 v[28:31], %[xp] offset:59392\n\t" \
    "v_mfma_f32_16x16x32_bf16 v[200:203], v[156:159], v[36:39], v[200:203]\n\t" \
    "ds_read_b128 v[32:35], %[xp] offset:60416\n\t" \
    "v_mfma_f32_16x16x32_bf16 v[204:207], v[148:151], v[40:43], v[204:207]\n\t" \
    "ds_read_b128 v[60:63], %[pa] offset:2048\n\t" \
    "s_waitcnt lgkmcnt(7)\n\t" \
    "v_mfma_f32_16x16x32_bf16 v[208:211], v[156:159], v[40:43], v[208:211]\n\t" \
    "buffer_load_dwordx4 %[l16], %[rs], %[a] offen offset:2048 sc1 lds\n\t" \
    "v_mfma_f32_16x16x32_bf16 v[196:199], v[152:155], v[44:47], v[196:199]\n\t" \
    "ds_read_b128 v[36:39], %[xp] offset:61440\n\t" \
    "s_waitcnt lgkmcnt(7)\n\t" \
    "v_add_f32 v0, v0, v52\n\t" \
    "v_mfma_f32_16x16x32_bf16 v[200:203], v[160:163], v[44:47], v[200:203]\n\t" \
    "ds_read_b128 v[40:43], %[xp] offset:62464\n\t" \
    "v_add_f32 v1, v1, v53\n\t" \
    "v_mfma_f32_16x16x32_bf16 v[204:207], v[152:155], v[48:51], v[204:207]\n\t" \
    "ds_read_b128 v[64:67], %[pa] offset:3072\n\t" \
    "s_waitcnt lgkmcnt(7)\n\t" \
    "v_mfma_f32_16x16x32_bf16 v[208:211], v[160:163], v[48:51], v[208:211]\n\t" \
    "buffer_load_dwordx4 %[l16], %[rs], %[a] offen offset:3072 sc1 lds\n\t" \
    "v_add_f32 v2, v2, v54\n\t" \
    "v_add_f32 v3, v3, v55\n\t" \
    "v_mfma_f32_16x16x32_bf16 v[196:199], v[164:167], v[20:23], v[196:199]\n\t" \
    "ds_read_b128 v[44:47], %[xp] offset:63488\n\t" \
    "s_waitcnt lgkmcnt(7)\n\t" \
    "v_add_f32 v4, v4, v56\n\t" \
    "v_mfma_f32_16x16x32_bf16 v[200:203], v[172:175], v[20:23], v[200:203]\n\t" \
    "ds_read_b128 v[48:51], %[xp] offset:64512\n\t" \
    "v_add_f32 v5, v5, v57\n\t" \
    "v_mfma_f32_16x16x32_bf16 v[204:207], v[164:167], v[24:27], v[204:207]\n\t" \
    "v_add_f32 v6, v6, v58\n\t" \
    "s_waitcnt lgkmcnt(6)\n\t" \
    "v_mfma_f32_16x16x32_bf16 v[208:211], v[172:175], v[24:27], v[208:211]\n\t" \
    "v_add_f32 v7, v7, v59\n\t" \
    "s_waitcnt lgkmcnt(5)\n\t" \
    "v_add_f32 v8, v8, v60\n\t" \
    "v_add_f32 v9, v9, v61\n\t" \
    "v_mfma_f32_16x16x32_bf16 v[196:199], v[168:171], v[28:31], v[196:199]\n\t" \
    "ds_read_b128 v[20:23], %[xp] offset:0\n\t" \
    "v_add_f32 v10, v10, v62\n\t" \
    "v_mfma_f32_16x16x32_bf16 v[200:203], v[176:179], v[28:31], v[200:203]\n\t" \
    "ds_read_b128 v[24:27], %[xp] offset:1024\n\t" \
    "v_add_f32 v11, v11, v63\n\t" \
    "v_mfma_f32_16x16x32_bf16 v[204:207], v[168:171], v[32:35], v[204:207]\n\t" \
    "s_waitcnt lgkmcnt(4)\n\t" \
    "v_add_f32 v12, v12, v64\n\t" \
    "v_mfma_f32_16x16x32_bf16 v[208:211], v[176:179], v[32:35], v[208:211]\n\t" \
    "v_add_f32 v13, v13, v65\n\t" \
    "v_add_f32 v14, v14, v66\n\t" \
    "v_add_f32 v15, v15, v67\n\t" \
    "v_mfma_f32_16x16x32_bf16 v[196:199], v[180:183], v[36:39], v[196:199]\n\t" \
    "ds_read_b128 v[28:31], %[xp] offset:2048\n\t" \
    "v_cvt_pk_bf16_f32 v52, v0, v4\n\t" \
    "v_mfma_f32_16x16x32_bf16 v[200:203], v[188:191], v[36:39], v[200:203]\n\t" \
    "ds_read_b128 v[32:35], %[xp] offset:3072\n\t" \
    "v_cvt_pk_bf16_f32 v53, v1, v5\n\t" \
    "v_mfma_f32_16x16x32_bf16 v[204:207], v[180:183], v[40:43], v[204:207]\n\t" \
    "v_cvt_pk_bf16_f32 v54, v2, v6\n\t" \
    "s_waitcnt lgkmcnt(4)\n\t" \
    "v_mfma_f32_16x16x32_bf16 v[208:211], v[188:191], v[40:43], v[208:211]\n\t" \
    "v_cvt_pk_bf16_f32 v55, v3, v7\n\t" \
    "v_cvt_pk_bf16_f32 v56, v8, v12\n\t" \
    "v_cvt_pk_bf16_f32 v57, v9, v13\n\t" \
    "v_mfma_f32_16x16x32_bf16 v[196:199], v[184:187], v[44:47], v[196:199]\n\t" \
    "ds_read_b128 v[36:39], %[xp] offset:4096\n\t" \
    "v_cvt_pk_bf16_f32 v58, v10, v14\n\t" \
    "v_mfma_f32_16x16x32_bf16 v[200:203], v[192:195], v[44:47], v[200:203]\n\t" \
    "ds_read_b128 v[40:43], %[xp] offset:5120\n\t" \
    "v_cvt_pk_bf16_f32 v59, v11, v15\n\t" \
    "v_mfma_f32_16x16x32_bf16 v[204:207], v[184:187], v[48:51], v[204:207]\n\t" \
    "s_waitcnt lgkmcnt(4)\n\t" \
    "v_mfma_f32_16x16x32_bf16 v[208:211], v[192:195], v[48:51], v[208:211]\n\t" \
    "s_mov_b64 exec, %[em]\n\t" \
    "buffer_store_dwordx4 v[52:55], %[l16], %[rs], %[oo] offen sc1\n\t" \
    "buffer_store_dwordx4 v[56:59], %[l16], %[rs], %[b] offen sc1\n\t" \
    "s_mov_b64 exec, -1\n\t" \
    "s_add_u32 %[sx], %[sx], 262144\n\t" \
    "s_add_u32 %[t], %[t], 1\n\t" \
    "s_add_u32 %[oo], %[oo], 262144\n\t" \
    "s_waitcnt vmcnt(6)\n\t" \
    "s_cmp_gt_u32 %[t], " STR(SQUICK) " + 1\n\t" \
    "s_cbranch_scc1 B1_%=\n\t" \
    "s_waitcnt vmcnt(0)\n\t" \
    "B1_%=:\n\t" \
    "s_barrier\n\t" \
    "s_cmp_lt_u32 %[t], %[te]\n\t" \
    "s_cbranch_scc1 L1_%=\n\t" \
    "s_cmp_eq_u32 %[fin], %[te]\n\t" \
    "s_cbranch_scc0 E_%=\n\t" \
    "s_nop 4\n\t" \
    "s_barrier\n\t" \
    "ds_read_b128 v[52:55], %[pa] offset:0\n\t" \
    "ds_read_b128 v[56:59], %[pa] offset:1024\n\t" \
    "ds_read_b128 v[60:63], %[pa] offset:2048\n\t" \
    "ds_read_b128 v[64:67], %[pa] offset:3072\n\t" \
    "s_add_u32 %[b], %[oo], 0x1000\n\t" \
    "s_waitcnt lgkmcnt(0)\n\t" \
    "v_add_f32 v196, v196, v52\n\t" \
    "v_add_f32 v197, v197, v53\n\t" \
    "v_add_f32 v198, v198, v54\n\t" \
    "v_add_f32 v199, v199, v55\n\t" \
    "v_add_f32 v200, v200, v56\n\t" \
    "v_add_f32 v201, v201, v57\n\t" \
    "v_add_f32 v202, v202, v58\n\t" \
    "v_add_f32 v203, v203, v59\n\t" \
    "v_add_f32 v204, v204, v60\n\t" \
    "v_add_f32 v205, v205, v61\n\t" \
    "v_add_f32 v206, v206, v62\n\t" \
    "v_add_f32 v207, v207, v63\n\t" \
    "v_add_f32 v208, v208, v64\n\t" \
    "v_add_f32 v209, v209, v65\n\t" \
    "v_add_f32 v210, v210, v66\n\t" \
    "v_add_f32 v211, v211, v67\n\t" \
    "v_cvt_pk_bf16_f32 v52, v196, v200\n\t" \
    "v_cvt_pk_bf16_f32 v53, v197, v201\n\t" \
    "v_cvt_pk_bf16_f32 v54, v198, v202\n\t" \
    "v_cvt_pk_bf16_f32 v55, v199, v203\n\t" \
    "v_cvt_pk_bf16_f32 v56, v204, v208\n\t" \
    "v_cvt_pk_bf16_f32 v57, v205, v209\n\t" \
    "v_cvt_pk_bf16_f32 v58, v206, v210\n\t" \
    "v_cvt_pk_bf16_f32 v59, v207, v211\n\t" \
    "buffer_store_dwordx4 v[52:55], %[l16], %[rs], %[oo] offen sc1\n\t" \
    "buffer_store_dwordx4 v[56:59], %[l16], %[rs], %[b] offen sc1\n\t" \
    "E_%=:\n\t" \
    "s_setprio 0\n\t" \
    "s_waitcnt lgkmcnt(0)\n\t" \
    "s_nop 7\n\t" \
    : [t] "+s"(t), [sx] "+s"(sx), [oo] "+s"(oo), [a] "=&s"(tmp), [b] "=&s"(tmp2), [em] "=&s"(em), "+{v[20:27]}"(xf[0]), "+{v[28:35]}"(xf[1]), "+{v[36:43]}"(xf[2]), "+{v[196:203]}"(ac[0]), "+{v[204:211]}"(ac[1]) \
    : [te] "s"(TE_), [fin] "s"(FIN_), [kh] "s"(kh), [sd] "s"(sd), [rs] "s"(rs), [l16] "v"(l16), [xp] "v"(xp), [pa] "v"(pa), [fm] "s"(fm), [pm] "s"(pm), [fo] "s"(fo), [po] "s"(po), [pd] "s"(pd), [tag] "s"(P.tag), "{v[68:75]}"(w[0][0]), "{v[76:83]}"(w[0][1]), "{v[84:91]}"(w[1][0]), "{v[92:99]}"(w[1][1]), "{v[100:107]}"(w[2][0]), "{v[108:115]}"(w[2][1]), "{v[116:123]}"(w[3][0]), "{v[124:131]}"(w[3][1]), "{v[132:139]}"(w[4][0]), "{v[140:147]}"(w[4][1]), "{v[148:155]}"(w[5][0]), "{v[156:163]}"(w[5][1]), "{v[164:171]}"(w[6][0]), "{v[172:179]}"(w[6][1]), "{v[180:187]}"(w[7][0]), "{v[188:195]}"(w[7][1]) \
    : "memory", "scc", "v0", "v1", "v2", "v3", "v4", "v5", "v6", "v7", "v8", "v9", "v10", "v11", "v12", "v13", "v14", "v15", "v44", "v45", "v46", "v47", "v48", "v49", "v50", "v51", "v52", "v53", "v54", "v55", "v56", "v57", "v58", "v59", "v60", "v61", "v62", "v63", "v64", "v65", "v66", "v67")

#define ONE_X(XP_) \
  asm volatile( \
    "ds_read_b128 v[20:23], %[xp] offset:0\n\t" \
    "ds_read_b128 v[24:27], %[xp] offset:1024\n\t" \
    "ds_read_b128 v[28:31], %[xp] offset:2048\n\t" \
    "ds_read_b128 v[32:35], %[xp] offset:3072\n\t" \
    "ds_read_b128 v[36:39], %[xp] offset:4096\n\t" \
    "ds_read_b128 v[40:43], %[xp] offset:5120\n\t" \
    "s_waitcnt lgkmcnt(4)\n\t" \
    "v_mfma_f32_16x16x32_bf16 v[0:3], v[68:71], v[20:23], 0\n\t" \
    "ds_read_b128 v[44:47], %[xp] offset:6144\n\t" \
    "v_mfma_f32_16x16x32_bf16 v[4:7], v[76:79], v[20:23], 0\n\t" \
    "ds_read_b128 v[48:51], %[xp] offset:7168\n\t" \
    "v_mfma_f32_16x16x32_bf16 v[8:11], v[68:71], v[24:27], 0\n\t" \
    "s_waitcnt lgkmcnt(4)\n\t" \
    "v_mfma_f32_16x16x32_bf16 v[12:15], v[76:79], v[24:27], 0\n\t" \
    "v_mfma_f32_16x16x32_bf16 v[0:3], v[72:75], v[28:31], v[0:3]\n\t" \
    "ds_read_b128 v[20:23], %[xp] offset:8192\n\t" \
    "v_mfma_f32_16x16x32_bf16 v[4:7], v[80:83], v[28:31], v[4:7]\n\t" \
    "ds_read_b128 v[24:27], %[xp] offset:9216\n\t" \
    "v_mfma_f32_16x16x32_bf16 v[8:11], v[72:75], v[32:35], v[8:11]\n\t" \
    "s_waitcnt lgkmcnt(4)\n\t" \
    "v_mfma_f32_16x16x32_bf16 v[12:15], v[80:83], v[32:35], v[12:15]\n\t" \
    "v_mfma_f32_16x16x32_bf16 v[0:3], v[84:87], v[36:39], v[0:3]\n\t" \
    "ds_read_b128 v[28:31], %[xp] offset:10240\n\t" \
    "v_mfma_f32_16x16x32_bf16 v[4:7], v[92:95], v[36:39], v[4:7]\n\t" \
    "ds_read_b128 v[32:35], %[xp] offset:11264\n\t" \
    "v_mfma_f32_16x16x32_bf16 v[8:11], v[84:87], v[40:43], v[8:11]\n\t" \
    "s_waitcnt lgkmcnt(4)\n\t" \
    "v_mfma_f32_16x16x32_bf16 v[12:15], v[92:95], v[40:43], v[12:15]\n\t" \
    "v_mfma_f32_16x16x32_bf16 v[0:3], v[88:91], v[44:47], v[0:3]\n\t" \
    "ds_read_b128 v[36:39], %[xp] offset:12288\n\t" \
    "v_mfma_f32_16x16x32_bf16 v[4:7], v[96:99], v[44:47], v[4:7]\n\t" \
    "ds_read_b128 v[40:43], %[xp] offset:13312\n\t" \
    "v_mfma_f32_16x16x32_bf16 v[8:11], v[88:91], v[48:51], v[8:11]\n\t" \
    "s_waitcnt lgkmcnt(4)\n\t" \
    "v_mfma_f32_16x16x32_bf16 v[12:15], v[96:99], v[48:51], v[12:15]\n\t" \
    "v_mfma_f32_16x16x32_bf16 v[0:3], v[100:103], v[20:23], v[0:3]\n\t" \
    "ds_read_b128 v[44:47], %[xp] offset:14336\n\t" \
    "v_mfma_f32_16x16x32_bf16 v[4:7], v[108:111], v[20:23], v[4:7]\n\t" \
    "ds_read_b128 v[48:51], %[xp] offset:15360\n\t" \
    "v_mfma_f32_16x16x32_bf16 v[8:11], v[100:103], v[24:27], v[8:11]\n\t" \
    "s_waitcnt lgkmcnt(4)\n\t" \
    "v_mfma_f32_16x16x32_bf16 v[12:15], v[108:111], v[24:27], v[12:15]\n\t" \
    "s_waitcnt vmcnt(0)\n\t" \
    "s_barrier\n\t" \
    "v_mfma_f32_16x16x32_bf16 v[0:3], v[104:107], v[28:31], v[0:3]\n\t" \
    "ds_read_b128 v[20:23], %[xp] offset:16384\n\t" \
    "v_mfma_f32_16x16x32_bf16 v[4:7], v[112:115], v[28:31], v[4:7]\n\t" \
    "ds_read_b128 v[24:27], %[xp] offset:17408\n\t" \
    "v_mfma_f32_16x16x32_bf16 v[8:11], v[104:107], v[32:35], v[8:11]\n\t" \
    "s_waitcnt lgkmcnt(4)\n\t" \
    "v_mfma_f32_16x16x32_bf16 v[12:15], v[112:115], v[32:35], v[12:15]\n\t" \
    "buffer_load_dword v52, %[pv], %[rs], 0 offen sc1\n\t" \
    "v_mfma_f32_16x16x32_bf16 v[0:3], v[116:119], v[36:39], v[0:3]\n\t" \
    "ds_read_b128 v[28:31], %[xp] offset:18432\n\t" \
    "v_mfma_f32_16x16x32_bf16 v[4:7], v[124:127], v[36:39], v[4:7]\n\t" \
    "ds_read_b128 v[32:35], %[xp] offset:19456\n\t" \
    "v_mfma_f32_16x16x32_bf16 v[8:11], v[116:119], v[40:43], v[8:11]\n\t" \
    "s_waitcnt lgkmcnt(4)\n\t" \
    "v_mfma_f32_16x16x32_bf16 v[12:15], v[124:127], v[40:43], v[12:15]\n\t" \
    "v_mfma_f32_16x16x32_bf16 v[0:3], v[120:123], v[44:47], v[0:3]\n\t" \
    "ds_read_b128 v[36:39], %[xp] offset:20480\n\t" \
    "v_mfma_f32_16x16x32_bf16 v[4:7], v[128:131], v[44:47], v[4:7]\n\t" \
    "ds_read_b128 v[40:43], %[xp] offset:21504\n\t" \
    "v_mfma_f32_16x16x32_bf16 v[8:11], v[120:123], v[48:51], v[8:11]\n\t" \
    "s_waitcnt lgkmcnt(4)\n\t" \
    "v_mfma_f32_16x16x32_bf16 v[12:15], v[128:131], v[48:51], v[12:15]\n\t" \
    "v_mfma_f32_16x16x32_bf16 v[0:3], v[132:135], v[20:23], v[0:3]\n\t" \
    "ds_read_b128 v[44:47], %[xp] offset:22528\n\t" \
    "v_mfma_f32_16x16x32_bf16 v[4:7], v[140:143], v[20:23], v[4:7]\n\t" \
    "ds_read_b128 v[48:51], %[xp] offset:23552\n\t" \
    "v_mfma_f32_16x16x32_bf16 v[8:11], v[132:135], v[24:27], v[8:11]\n\t" \
    "s_waitcnt lgkmcnt(4)\n\t" \
    "v_mfma_f32_16x16x32_bf16 v[12:15], v[140:143], v[24:27], v[12:15]\n\t" \
    "v_mfma_f32_16x16x32_bf16 v[0:3], v[136:139], v[28:31], v[0:3]\n\t" \
    "ds_read_b128 v[20:23], %[xp] offset:24576\n\t" \
    "v_mfma_f32_16x16x32_bf16 v[4:7], v[144:147], v[28:31], v[4:7]\n\t" \
    "ds_read_b128 v[24:27], %[xp] offset:25600\n\t" \
    "v_mfma_f32_16x16x32_bf16 v[8:11], v[136:139], v[32:35], v[8:11]\n\t" \
    "s_waitcnt lgkmcnt(4)\n\t" \
    "v_mfma_f32_16x16x32_bf16 v[12:15], v[144:147], v[32:35], v[12:15]\n\t" \
    "v_mfma_f32_16x16x32_bf16 v[0:3], v[148:151], v[36:39], v[0:3]\n\t" \
    "ds_read_b128 v[28:31], %[xp] offset:26624\n\t" \
    "v_mfma_f32_16x16x32_bf16 v[4:7], v[156:159], v[36:39], v[4:7]\n\t" \
    "ds_read_b128 v[32:35], %[xp] offset:27648\n\t" \
    "v_mfma_f32_16x16x32_bf16 v[8:11], v[148:151], v[40:43], v[8:11]\n\t" \
    "s_waitcnt lgkmcnt(4)\n\t" \
    "v_mfma_f32_16x16x32_bf16 v[12:15], v[156:159], v[40:43], v[12:15]\n\t" \
    "v_mfma_f32_16x16x32_bf16 v[0:3], v[152:155], v[44:47], v[0:3]\n\t" \
    "ds_read_b128 v[36:39], %[xp] offset:28672\n\t" \
    "v_mfma_f32_16x16x32_bf16 v[4:7], v[160:163], v[44:47], v[4:7]\n\t" \
    "ds_read_b128 v[40:43], %[xp] offset:29696\n\t" \
    "v_mfma_f32_16x16x32_bf16 v[8:11], v[152:155], v[48:51], v[8:11]\n\t" \
    "s_waitcnt lgkmcnt(4)\n\t" \
    "v_mfma_f32_16x16x32_bf16 v[12:15], v[160:163], v[48:51], v[12:15]\n\t" \
    "v_mfma_f32_16x16x32_bf16 v[0:3], v[164:167], v[20:23], v[0:3]\n\t" \
    "ds_read_b128 v[44:47], %[xp] offset:30720\n\t" \
    "v_mfma_f32_16x16x32_bf16 v[4:7], v[172:175], v[20:23], v[4:7]\n\t" \
    "ds_read_b128 v[48:51], %[xp] offset:31744\n\t" \
    "v_mfma_f32_16x16x32_bf16 v[8:11], v[164:167], v[24:27], v[8:11]\n\t" \
    "s_waitcnt lgkmcnt(4)\n\t" \
    "v_mfma_f32_16x16x32_bf16 v[12:15], v[172:175], v[24:27], v[12:15]\n\t" \
    "v_mfma_f32_16x16x32_bf16 v[0:3], v[168:171], v[28:31], v[0:3]\n\t" \
    "v_mfma_f32_16x16x32_bf16 v[4:7], v[176:179], v[28:31], v[4:7]\n\t" \
    "v_mfma_f32_16x16x32_bf16 v[8:11], v[168:171], v[32:35], v[8:11]\n\t" \
    "s_waitcnt lgkmcnt(2)\n\t" \
    "v_mfma_f32_16x16x32_bf16 v[12:15], v[176:179], v[32:35], v[12:15]\n\t" \
    "v_mfma_f32_16x16x32_bf16 v[0:3], v[180:183], v[36:39], v[0:3]\n\t" \
    "v_mfma_f32_16x16x32_bf16 v[4:7], v[188:191], v[36:39], v[4:7]\n\t" \
    "v_mfma_f32_16x16x32_bf16 v[8:11], v[180:183], v[40:43], v[8:11]\n\t" \
    "s_waitcnt lgkmcnt(0)\n\t" \
    "v_mfma_f32_16x16x32_bf16 v[12:15], v[188:191], v[40:43], v[12:15]\n\t" \
    "v_mfma_f32_16x16x32_bf16 v[0:3], v[184:187], v[44:47], v[0:3]\n\t" \
    "v_mfma_f32_16x16x32_bf16 v[4:7], v[192:195], v[44:47], v[4:7]\n\t" \
    "v_mfma_f32_16x16x32_bf16 v[8:11], v[184:187], v[48:51], v[8:11]\n\t" \
    "v_mfma_f32_16x16x32_bf16 v[12:15], v[192:195], v[48:51], v[12:15]\n\t" \
    "s_waitcnt vmcnt(0)\n\t" \
    "s_nop 7\n\t" \
    : "=&{v52}"(pg), "=&{v[0:3]}"(a[0]), "=&{v[4:7]}"(a[1]), "=&{v[8:11]}"(a[2]), "=&{v[12:15]}"(a[3]) \
    : [xp] "v"(XP_), [pv] "v"(pv), [rs] "s"(rs), "{v[68:75]}"(w[0][0]), "{v[76:83]}"(w[0][1]), "{v[84:91]}"(w[1][0]), "{v[92:99]}"(w[1][1]), "{v[100:107]}"(w[2][0]), "{v[108:115]}"(w[2][1]), "{v[116:123]}"(w[3][0]), "{v[124:131]}"(w[3][1]), "{v[132:139]}"(w[4][0]), "{v[140:147]}"(w[4][1]), "{v[148:155]}"(w[5][0]), "{v[156:163]}"(w[5][1]), "{v[164:171]}"(w[6][0]), "{v[172:179]}"(w[6][1]), "{v[180:187]}"(w[7][0]), "{v[188:195]}"(w[7][1]) \
    : "memory", "v20", "v21", "v22", "v23", "v24", "v25", "v26", "v27", "v28", "v29", "v30", "v31", "v32", "v33", "v34", "v35", "v36", "v37", "v38", "v39", "v40", "v41", "v42", "v43", "v44", "v45", "v46", "v47", "v48", "v49", "v50", "v51")

#define ONE_K(XP_) \
  asm volatile( \
    "ds_read_b128 v[20:23], %[xp] offset:0\n\t" \
    "ds_read_b128 v[24:27], %[xp] offset:1024\n\t" \
    "ds_read_b128 v[28:31], %[xp] offset:2048\n\t" \
    "ds_read_b128 v[32:35], %[xp] offset:3072\n\t" \
    "ds_read_b128 v[36:39], %[xp] offset:4096\n\t" \
    "ds_read_b128 v[40:43], %[xp] offset:5120\n\t" \
    "s_waitcnt lgkmcnt(4)\n\t" \
    "v_mfma_f32_16x16x32_bf16 v[0:3], v[68:71], v[20:23], 0\n\t" \
    "ds_read_b128 v[44:47], %[xp] offset:6144\n\t" \
    "v_mfma_f32_16x16x32_bf16 v[4:7], v[76:79], v[20:23], 0\n\t" \
    "ds_read_b128 v[48:51], %[xp] offset:7168\n\t" \
    "v_mfma_f32_16x16x32_bf16 v[8:11], v[68:71], v[24:27], 0\n\t" \
    "s_waitcnt lgkmcnt(4)\n\t" \
    "v_mfma_f32_16x16x32_bf16 v[12:15], v[76:79], v[24:27], 0\n\t" \
    "v_mfma_f32_16x16x32_bf16 v[0:3], v[72:75], v[28:31], v[0:3]\n\t" \
    "ds_read_b128 v[20:23], %[xp] offset:8192\n\t" \
    "v_mfma_f32_16x16x32_bf16 v[4:7], v[80:83], v[28:31], v[4:7]\n\t" \
    "ds_read_b128 v[24:27], %[xp] offset:9216\n\t" \
    "v_mfma_f32_16x16x32_bf16 v[8:11], v[72:75], v[32:35], v[8:11]\n\t" \
    "s_waitcnt lgkmcnt(4)\n\t" \
    "v_mfma_f32_16x16x32_bf16 v[12:15], v[80:83], v[32:35], v[12:15]\n\t" \
    "v_mfma_f32_16x16x32_bf16 v[0:3], v[84:87], v[36:39], v[0:3]\n\t" \
    "ds_read_b128 v[28:31], %[xp] offset:10240\n\t" \
    "v_mfma_f32_16x16x32_bf16 v[4:7], v[92:95], v[36:39], v[4:7]\n\t" \
    "ds_read_b128 v[32:35], %[xp] offset:11264\n\t" \
    "v_mfma_f32_16x16x32_bf16 v[8:11], v[84:87], v[40:43], v[8:11]\n\t" \
    "s_waitcnt lgkmcnt(4)\n\t" \
    "v_mfma_f32_16x16x32_bf16 v[12:15], v[92:95], v[40:43], v[12:15]\n\t" \
    "v_mfma_f32_16x16x32_bf16 v[0:3], v[88:91], v[44:47], v[0:3]\n\t" \
    "ds_read_b128 v[36:39], %[xp] offset:12288\n\t" \
    "v_mfma_f32_16x16x32_bf16 v[4:7], v[96:99], v[44:47], v[4:7]\n\t" \
    "ds_read_b128 v[40:43], %[xp] offset:13312\n\t" \
    "v_mfma_f32_16x16x32_bf16 v[8:11], v[88:91], v[48:51], v[8:11]\n\t" \
    "s_waitcnt lgkmcnt(4)\n\t" \
    "v_mfma_f32_16x16x32_bf16 v[12:15], v[96:99], v[48:51], v[12:15]\n\t" \
    "v_mfma_f32_16x16x32_bf16 v[0:3], v[100:103], v[20:23], v[0:3]\n\t" \
    "ds_read_b128 v[44:47], %[xp] offset:14336\n\t" \
    "v_mfma_f32_16x16x32_bf16 v[4:7], v[108:111], v[20:23], v[4:7]\n\t" \
    "ds_read_b128 v[48:51], %[xp] offset:15360\n\t" \
    "v_mfma_f32_16x16x32_bf16 v[8:11], v[100:103], v[24:27], v[8:11]\n\t" \
    "s_waitcnt lgkmcnt(4)\n\t" \
    "v_mfma_f32_16x16x32_bf16 v[12:15], v[108:111], v[24:27], v[12:15]\n\t" \
    "s_waitcnt vmcnt(8)\n\t" \
    "s_barrier\n\t" \
    "v_mfma_f32_16x16x32_bf16 v[0:3], v[104:107], v[28:31], v[0:3]\n\t" \
    "ds_read_b128 v[20:23], %[xp] offset:16384\n\t" \
    "v_mfma_f32_16x16x32_bf16 v[4:7], v[112:115], v[28:31], v[4:7]\n\t" \
    "ds_read_b128 v[24:27], %[xp] offset:17408\n\t" \
    "v_mfma_f32_16x16x32_bf16 v[8:11], v[104:107], v[32:35], v[8:11]\n\t" \
    "s_waitcnt lgkmcnt(4)\n\t" \
    "v_mfma_f32_16x16x32_bf16 v[12:15], v[112:115], v[32:35], v[12:15]\n\t" \
    "v_mfma_f32_16x16x32_bf16 v[0:3], v[116:119], v[36:39], v[0:3]\n\t" \
    "ds_read_b128 v[28:31], %[xp] offset:18432\n\t" \
    "v_mfma_f32_16x16x32_bf16 v[4:7], v[124:127], v[36:39], v[4:7]\n\t" \
    "ds_read_b128 v[32:35], %[xp] offset:19456\n\t" \
    "v_mfma_f32_16x16x32_bf16 v[8:11], v[116:119], v[40:43], v[8:11]\n\t" \
    "s_waitcnt lgkmcnt(4)\n\t" \
    "v_mfma_f32_16x16x32_bf16 v[12:15], v[124:127], v[40:43], v[12:15]\n\t" \
    "v_mfma_f32_16x16x32_bf16 v[0:3], v[120:123], v[44:47], v[0:3]\n\t" \
    "ds_read_b128 v[36:39], %[xp] offset:20480\n\t" \
    "v_mfma_f32_16x16x32_bf16 v[4:7], v[128:131], v[44:47], v[4:7]\n\t" \
    "ds_read_b128 v[40:43], %[xp] offset:21504\n\t" \
    "v_mfma_f32_16x16x32_bf16 v[8:11], v[120:123], v[48:51], v[8:11]\n\t" \
    "s_waitcnt lgkmcnt(4)\n\t" \
    "v_mfma_f32_16x16x32_bf16 v[12:15], v[128:131], v[48:51], v[12:15]\n\t" \
    "v_mfma_f32_16x16x32_bf16 v[0:3], v[132:135], v[20:23], v[0:3]\n\t" \
    "ds_read_b128 v[44:47], %[xp] offset:22528\n\t" \
    "v_mfma_f32_16x16x32_bf16 v[4:7], v[140:143], v[20:23], v[4:7]\n\t" \
    "ds_read_b128 v[48:51], %[xp] offset:23552\n\t" \
    "v_mfma_f32_16x16x32_bf16 v[8:11], v[132:135], v[24:27], v[8:11]\n\t" \
    "s_waitcnt lgkmcnt(4)\n\t" \
    "v_mfma_f32_16x16x32_bf16 v[12:15], v[140:143], v[24:27], v[12:15]\n\t" \
    "v_mfma_f32_16x16x32_bf16 v[0:3], v[136:139], v[28:31], v[0:3]\n\t" \
    "ds_read_b128 v[20:23], %[xp] offset:24576\n\t" \
    "v_mfma_f32_16x16x32_bf16 v[4:7], v[144:147], v[28:31], v[4:7]\n\t" \
    "ds_read_b128 v[24:27], %[xp] offset:25600\n\t" \
    "v_mfma_f32_16x16x32_bf16 v[8:11], v[136:139], v[32:35], v[8:11]\n\t" \
    "s_waitcnt lgkmcnt(4)\n\t" \
    "v_mfma_f32_16x16x32_bf16 v[12:15], v[144:147], v[32:35], v[12:15]\n\t" \
    "v_mfma_f32_16x16x32_bf16 v[0:3], v[148:151], v[36:39], v[0:3]\n\t" \
    "ds_read_b128 v[28:31], %[xp] offset:26624\n\t" \
    "v_mfma_f32_16x16x32_bf16 v[4:7], v[156:159], v[36:39], v[4:7]\n\t" \
    "ds_read_b128 v[32:35], %[xp] offset:27648\n\t" \
    "v_mfma_f32_16x16x32_bf16 v[8:11], v[148:151], v[40:43], v[8:11]\n\t" \
    "s_waitcnt lgkmcnt(4)\n\t" \
    "v_mfma_f32_16x16x32_bf16 v[12:15], v[156:159], v[40:43], v[12:15]\n\t" \
    "v_mfma_f32_16x16x32_bf16 v[0:3], v[152:155], v[44:47], v[0:3]\n\t" \
    "ds_read_b128 v[36:39], %[xp] offset:28672\n\t" \
    "v_mfma_f32_16x16x32_bf16 v[4:7], v[160:163], v[44:47], v[4:7]\n\t" \
    "ds_read_b128 v[40:43], %[xp] offset:29696\n\t" \
    "v_mfma_f32_16x16x32_bf16 v[8:11], v[152:155], v[48:51], v[8:11]\n\t" \
    "s_waitcnt lgkmcnt(4)\n\t" \
    "v_mfma_f32_16x16x32_bf16 v[12:15], v[160:163], v[48:51], v[12:15]\n\t" \
    "v_mfma_f32_16x16x32_bf16 v[0:3], v[164:167], v[20:23], v[0:3]\n\t" \
    "ds_read_b128 v[44:47], %[xp] offset:30720\n\t" \
    "v_mfma_f32_16x16x32_bf16 v[4:7], v[172:175], v[20:23], v[4:7]\n\t" \
    "ds_read_b128 v[48:51], %[xp] offset:31744\n\t" \
    "v_mfma_f32_16x16x32_bf16 v[8:11], v[164:167], v[24:27], v[8:11]\n\t" \
    "s_waitcnt lgkmcnt(4)\n\t" \
    "v_mfma_f32_16x16x32_bf16 v[12:15], v[172:175], v[24:27], v[12:15]\n\t" \
    "v_mfma_f32_16x16x32_bf16 v[0:3], v[168:171], v[28:31], v[0:3]\n\t" \
    "v_mfma_f32_16x16x32_bf16 v[4:7], v[176:179], v[28:31], v[4:7]\n\t" \
    "v_mfma_f32_16x16x32_bf16 v[8:11], v[168:171], v[32:35], v[8:11]\n\t" \
    "s_waitcnt lgkmcnt(2)\n\t" \
    "v_mfma_f32_16x16x32_bf16 v[12:15], v[176:179], v[32:35], v[12:15]\n\t" \
    "v_mfma_f32_16x16x32_bf16 v[0:3], v[180:183], v[36:39], v[0:3]\n\t" \
    "v_mfma_f32_16x16x32_bf16 v[4:7], v[188:191], v[36:39], v[4:7]\n\t" \
    "v_mfma_f32_16x16x32_bf16 v[8:11], v[180:183], v[40:43], v[8:11]\n\t" \
    "s_waitcnt lgkmcnt(0)\n\t" \
    "v_mfma_f32_16x16x32_bf16 v[12:15], v[188:191], v[40:43], v[12:15]\n\t" \
    "v_mfma_f32_16x16x32_bf16 v[0:3], v[184:187], v[44:47], v[0:3]\n\t" \
    "v_mfma_f32_16x16x32_bf16 v[4:7], v[192:195], v[44:47], v[4:7]\n\t" \
    "v_mfma_f32_16x16x32_bf16 v[8:11], v[184:187], v[48:51], v[8:11]\n\t" \
    "v_mfma_f32_16x16x32_bf16 v[12:15], v[192:195], v[48:51], v[12:15]\n\t" \
    "s_nop 7\n\t" \
    : "=&{v[0:3]}"(a[0]), "=&{v[4:7]}"(a[1]), "=&{v[8:11]}"(a[2]), "=&{v[12:15]}"(a[3]) \
    : [xp] "v"(XP_), "{v[68:75]}"(w[0][0]), "{v[76:83]}"(w[0][1]), "{v[84:91]}"(w[1][0]), "{v[92:99]}"(w[1][1]), "{v[100:107]}"(w[2][0]), "{v[108:115]}"(w[2][1]), "{v[116:123]}"(w[3][0]), "{v[124:131]}"(w[3][1]), "{v[132:139]}"(w[4][0]), "{v[140:147]}"(w[4][1]), "{v[148:155]}"(w[5][0]), "{v[156:163]}"(w[5][1]), "{v[164:171]}"(w[6][0]), "{v[172:179]}"(w[6][1]), "{v[180:187]}"(w[7][0]), "{v[188:195]}"(w[7][1]) \
    : "memory", "v20", "v21", "v22", "v23", "v24", "v25", "v26", "v27", "v28", "v29", "v30", "v31", "v32", "v33", "v34", "v35", "v36", "v37", "v38", "v39", "v40", "v41", "v42", "v43", "v44", "v45", "v46", "v47", "v48", "v49", "v50", "v51")

#define ONE_V(XP_) \
  asm volatile( \
    "ds_read_b128 v[20:23], %[xp] offset:0\n\t" \
    "ds_read_b128 v[24:27], %[xp] offset:1024\n\t" \
    "ds_read_b128 v[28:31], %[xp] offset:2048\n\t" \
    "ds_read_b128 v[32:35], %[xp] offset:3072\n\t" \
    "ds_read_b128 v[36:39], %[xp] offset:4096\n\t" \
    "ds_read_b128 v[40:43], %[xp] offset:5120\n\t" \
    "s_waitcnt lgkmcnt(4)\n\t" \
    "v_mfma_f32_16x16x32_bf16 v[0:3], v[20:23], v[68:71], 0\n\t" \
    "ds_read_b128 v[44:47], %[xp] offset:6144\n\t" \
    "v_mfma_f32_16x16x32_bf16 v[4:7], v[20:23], v[76:79], 0\n\t" \
    "ds_read_b128 v[48:51], %[xp] offset:7168\n\t" \
    "v_mfma_f32_16x16x32_bf16 v[8:11], v[24:27], v[68:71], 0\n\t" \
    "s_waitcnt lgkmcnt(4)\n\t" \
    "v_mfma_f32_16x16x32_bf16 v[12:15], v[24:27], v[76:79], 0\n\t" \
    "v_mfma_f32_16x16x32_bf16 v[0:3], v[28:31], v[72:75], v[0:3]\n\t" \
    "ds_read_b128 v[20:23], %[xp] offset:8192\n\t" \
    "v_mfma_f32_16x16x32_bf16 v[4:7], v[28:31], v[80:83], v[4:7]\n\t" \
    "ds_read_b128 v[24:27], %[xp] offset:9216\n\t" \
    "v_mfma_f32_16x16x32_bf16 v[8:11], v[32:35], v[72:75], v[8:11]\n\t" \
    "s_waitcnt lgkmcnt(4)\n\t" \
    "v_mfma_f32_16x16x32_bf16 v[12:15], v[32:35], v[80:83], v[12:15]\n\t" \
    "v_mfma_f32_16x16x32_bf16 v[0:3], v[36:39], v[84:87], v[0:3]\n\t" \
    "ds_read_b128 v[28:31], %[xp] offset:10240\n\t" \
    "v_mfma_f32_16x16x32_bf16 v[4:7], v[36:39], v[92:95], v[4:7]\n\t" \
    "ds_read_b128 v[32:35], %[xp] offset:11264\n\t" \
    "v_mfma_f32_16x16x32_bf16 v[8:11], v[40:43], v[84:87], v[8:11]\n\t" \
    "s_waitcnt lgkmcnt(4)\n\t" \
    "v_mfma_f32_16x16x32_bf16 v[12:15], v[40:43], v[92:95], v[12:15]\n\t" \
    "v_mfma_f32_16x16x32_bf16 v[0:3], v[44:47], v[88:91], v[0:3]\n\t" \
    "ds_read_b128 v[36:39], %[xp] offset:12288\n\t" \
    "v_mfma_f32_16x16x32_bf16 v[4:7], v[44:47], v[96:99], v[4:7]\n\t" \
    "ds_read_b128 v[40:43], %[xp] offset:13312\n\t" \
    "v_mfma_f32_16x16x32_bf16 v[8:11], v[48:51], v[88:91], v[8:11]\n\t" \
    "s_waitcnt lgkmcnt(4)\n\t" \
    "v_mfma_f32_16x16x32_bf16 v[12:15], v[48:51], v[96:99], v[12:15]\n\t" \
    "v_mfma_f32_16x16x32_bf16 v[0:3], v[20:23], v[100:103], v[0:3]\n\t" \
    "ds_read_b128 v[44:47], %[xp] offset:14336\n\t" \
    "v_mfma_f32_16x16x32_bf16 v[4:7], v[20:23], v[108:111], v[4:7]\n\t" \
    "ds_read_b128 v[48:51], %[xp] offset:15360\n\t" \
    "v_mfma_f32_16x16x32_bf16 v[8:11], v[24:27], v[100:103], v[8:11]\n\t" \
    "s_waitcnt lgkmcnt(4)\n\t" \
    "v_mfma_f32_16x16x32_bf16 v[12:15], v[24:27], v[108:111], v[12:15]\n\t" \
    "s_waitcnt vmcnt(8)\n\t" \
    "s_barrier\n\t" \
    "v_mfma_f32_16x16x32_bf16 v[0:3], v[28:31], v[104:107], v[0:3]\n\t" \
    "ds_read_b128 v[20:23], %[xp] offset:16384\n\t" \
    "v_mfma_f32_16x16x32_bf16 v[4:7], v[28:31], v[112:115], v[4:7]\n\t" \
    "ds_read_b128 v[24:27], %[xp] offset:17408\n\t" \
    "v_mfma_f32_16x16x32_bf16 v[8:11], v[32:35], v[104:107], v[8:11]\n\t" \
    "s_waitcnt lgkmcnt(4)\n\t" \
    "v_mfma_f32_16x16x32_bf16 v[12:15], v[32:35], v[112:115], v[12:15]\n\t" \
    "v_mfma_f32_16x16x32_bf16 v[0:3], v[36:39], v[116:119], v[0:3]\n\t" \
    "ds_read_b128 v[28:31], %[xp] offset:18432\n\t" \
    "v_mfma_f32_16x16x32_bf16 v[4:7], v[36:39], v[124:127], v[4:7]\n\t" \
    "ds_read_b128 v[32:35], %[xp] offset:19456\n\t" \
    "v_mfma_f32_16x16x32_bf16 v[8:11], v[40:43], v[116:119], v[8:11]\n\t" \
    "s_waitcnt lgkmcnt(4)\n\t" \
    "v_mfma_f32_16x16x32_bf16 v[12:15], v[40:43], v[124:127], v[12:15]\n\t" \
    "v_mfma_f32_16x16x32_bf16 v[0:3], v[44:47], v[120:123], v[0:3]\n\t" \
    "ds_read_b128 v[36:39], %[xp] offset:20480\n\t" \
    "v_mfma_f32_16x16x32_bf16 v[4:7], v[44:47], v[128:131], v[4:7]\n\t" \
    "ds_read_b128 v[40:43], %[xp] offset:21504\n\t" \
    "v_mfma_f32_16x16x32_bf16 v[8:11], v[48:51], v[120:123], v[8:11]\n\t" \
    "s_waitcnt lgkmcnt(4)\n\t" \
    "v_mfma_f32_16x16x32_bf16 v[12:15], v[48:51], v[128:131], v[12:15]\n\t" \
    "v_mfma_f32_16x16x32_bf16 v[0:3], v[20:23], v[132:135], v[0:3]\n\t" \
    "ds_read_b128 v[44:47], %[xp] offset:22528\n\t" \
    "v_mfma_f32_16x16x32_bf16 v[4:7], v[20:23], v[140:143], v[4:7]\n\t" \
    "ds_read_b128 v[48:51], %[xp] offset:23552\n\t" \
    "v_mfma_f32_16x16x32_bf16 v[8:11], v[24:27], v[132:135], v[8:11]\n\t" \
    "s_waitcnt lgkmcnt(4)\n\t" \
    "v_mfma_f32_16x16x32_bf16 v[12:15], v[24:27], v[140:143], v[12:15]\n\t" \
    "v_mfma_f32_16x16x32_bf16 v[0:3], v[28:31], v[136:139], v[0:3]\n\t" \
    "ds_read_b128 v[20:23], %[xp] offset:24576\n\t" \
    "v_mfma_f32_16x16x32_bf16 v[4:7], v[28:31], v[144:147], v[4:7]\n\t" \
    "ds_read_b128 v[24:27], %[xp] offset:25600\n\t" \
    "v_mfma_f32_16x16x32_bf16 v[8:11], v[32:35], v[136:139], v[8:11]\n\t" \
    "s_waitcnt lgkmcnt(4)\n\t" \
    "v_mfma_f32_16x16x32_bf16 v[12:15], v[32:35], v[144:147], v[12:15]\n\t" \
    "v_mfma_f32_16x16x32_bf16 v[0:3], v[36:39], v[148:151], v[0:3]\n\t" \
    "ds_read_b128 v[28:31], %[xp] offset:26624\n\t" \
    "v_mfma_f32_16x16x32_bf16 v[4:7], v[36:39], v[156:159], v[4:7]\n\t" \
    "ds_read_b128 v[32:35], %[xp] offset:27648\n\t" \
    "v_mfma_f32_16x16x32_bf16 v[8:11], v[40:43], v[148:151], v[8:11]\n\t" \
    "s_waitcnt lgkmcnt(4)\n\t" \
    "v_mfma_f32_16x16x32_bf16 v[12:15], v[40:43], v[156:159], v[12:15]\n\t" \
    "v_mfma_f32_16x16x32_bf16 v[0:3], v[44:47], v[152:155], v[0:3]\n\t" \
    "ds_read_b128 v[36:39], %[xp] offset:28672\n\t" \
    "v_mfma_f32_16x16x32_bf16 v[4:7], v[44:47], v[160:163], v[4:7]\n\t" \
    "ds_read_b128 v[40:43], %[xp] offset:29696\n\t" \
    "v_mfma_f32_16x16x32_bf16 v[8:11], v[48:51], v[152:155], v[8:11]\n\t" \
    "s_waitcnt lgkmcnt(4)\n\t" \
    "v_mfma_f32_16x16x32_bf16 v[12:15], v[48:51], v[160:163], v[12:15]\n\t" \
    "v_mfma_f32_16x16x32_bf16 v[0:3], v[20:23], v[164:167], v[0:3]\n\t" \
    "ds_read_b128 v[44:47], %[xp] offset:30720\n\t" \
    "v_mfma_f32_16x16x32_bf16 v[4:7], v[20:23], v[172:175], v[4:7]\n\t" \
    "ds_read_b128 v[48:51], %[xp] offset:31744\n\t" \
    "v_mfma_f32_16x16x32_bf16 v[8:11], v[24:27], v[164:167], v[8:11]\n\t" \
    "s_waitcnt lgkmcnt(4)\n\t" \
    "v_mfma_f32_16x16x32_bf16 v[12:15], v[24:27], v[172:175], v[12:15]\n\t" \
    "v_mfma_f32_16x16x32_bf16 v[0:3], v[28:31], v[168:171], v[0:3]\n\t" \
    "v_mfma_f32_16x16x32_bf16 v[4:7], v[28:31], v[176:179], v[4:7]\n\t" \
    "v_mfma_f32_16x16x32_bf16 v[8:11], v[32:35], v[168:171], v[8:11]\n\t" \
    "s_waitcnt lgkmcnt(2)\n\t" \
    "v_mfma_f32_16x16x32_bf16 v[12:15], v[32:35], v[176:179], v[12:15]\n\t" \
    "v_mfma_f32_16x16x32_bf16 v[0:3], v[36:39], v[180:183], v[0:3]\n\t" \
    "v_mfma_f32_16x16x32_bf16 v[4:7], v[36:39], v[188:191], v[4:7]\n\t" \
    "v_mfma_f32_16x16x32_bf16 v[8:11], v[40:43], v[180:183], v[8:11]\n\t" \
    "s_waitcnt lgkmcnt(0)\n\t" \
    "v_mfma_f32_16x16x32_bf16 v[12:15], v[40:43], v[188:191], v[12:15]\n\t" \
    "v_mfma_f32_16x16x32_bf16 v[0:3], v[44:47], v[184:187], v[0:3]\n\t" \
    "v_mfma_f32_16x16x32_bf16 v[4:7], v[44:47], v[192:195], v[4:7]\n\t" \
    "v_mfma_f32_16x16x32_bf16 v[8:11], v[48:51], v[184:187], v[8:11]\n\t" \
    "v_mfma_f32_16x16x32_bf16 v[12:15], v[48:51], v[192:195], v[12:15]\n\t" \
    "s_nop 7\n\t" \
    : "=&{v[0:3]}"(a[0]), "=&{v[4:7]}"(a[1]), "=&{v[8:11]}"(a[2]), "=&{v[12:15]}"(a[3]) \
    : [xp] "v"(XP_), "{v[68:75]}"(w[0][0]), "{v[76:83]}"(w[0][1]), "{v[84:91]}"(w[1][0]), "{v[92:99]}"(w[1][1]), "{v[100:107]}"(w[2][0]), "{v[108:115]}"(w[2][1]), "{v[116:123]}"(w[3][0]), "{v[124:131]}"(w[3][1]), "{v[132:139]}"(w[4][0]), "{v[140:147]}"(w[4][1]), "{v[148:155]}"(w[5][0]), "{v[156:163]}"(w[5][1]), "{v[164:171]}"(w[6][0]), "{v[172:179]}"(w[6][1]), "{v[180:187]}"(w[7][0]), "{v[188:195]}"(w[7][1]) \
    : "memory", "v20", "v21", "v22", "v23", "v24", "v25", "v26", "v27", "v28", "v29", "v30", "v31", "v32", "v33", "v34", "v35", "v36", "v37", "v38", "v39", "v40", "v41", "v42", "v43", "v44", "v45", "v46", "v47", "v48", "v49", "v50", "v51")

#define H_LOOP1() \
  asm volatile( \
    "v_mov_b32 v20, 0\n\t" \
    "v_mov_b32 v21, 0\n\t" \
    "v_mov_b32 v22, 0\n\t" \
    "v_mov_b32 v23, 0\n\t" \
    "v_mov_b32 v24, 0\n\t" \
    "v_mov_b32 v25, 0\n\t" \
    "v_mov_b32 v26, 0\n\t" \
    "v_mov_b32 v27, 0\n\t" \
    "v_mov_b32 v28, 0\n\t" \
    "v_mov_b32 v29, 0\n\t" \
    "v_mov_b32 v30, 0\n\t" \
    "v_mov_b32 v31, 0\n\t" \
    "v_mov_b32 v32, 0\n\t" \
    "v_mov_b32 v33, 0\n\t" \
    "v_mov_b32 v34, 0\n\t" \
    "v_mov_b32 v35, 0\n\t" \
    "v_mov_b32 v36, 0\n\t" \
    "v_mov_b32 v37, 0\n\t" \
    "v_mov_b32 v38, 0\n\t" \
    "v_mov_b32 v39, 0\n\t" \
    "v_mov_b32 v40, 0\n\t" \
    "v_mov_b32 v41, 0\n\t" \
    "v_mov_b32 v42, 0\n\t" \
    "v_mov_b32 v43, 0\n\t" \
    "v_mov_b32 v44, 0\n\t" \
    "v_mov_b32 v45, 0\n\t" \
    "v_mov_b32 v46, 0\n\t" \
    "v_mov_b32 v47, 0\n\t" \
    "v_mov_b32 v48, 0\n\t" \
    "v_mov_b32 v49, 0\n\t" \
    "v_mov_b32 v50, 0\n\t" \
    "v_mov_b32 v51, 0\n\t" \
    "v_mov_b32 v52, 0\n\t" \
    "v_mov_b32 v53, 0\n\t" \
    "v_mov_b32 v54, 0\n\t" \
    "v_mov_b32 v55, 0\n\t" \
    "v_mov_b32 v56, 0\n\t" \
    "v_mov_b32 v57, 0\n\t" \
    "v_mov_b32 v58, 0\n\t" \
    "v_mov_b32 v59, 0\n\t" \
    "v_mov_b32 v60, 0\n\t" \
    "v_mov_b32 v61, 0\n\t" \
    "v_mov_b32 v62, 0\n\t" \
    "v_mov_b32 v63, 0\n\t" \
    "v_mov_b32 v64, 0\n\t" \
    "v_mov_b32 v65, 0\n\t" \
    "v_mov_b32 v66, 0\n\t" \
    "v_mov_b32 v67, 0\n\t" \
    "v_mov_b32 v68, 0\n\t" \
    "v_mov_b32 v69, 0\n\t" \
    "v_mov_b32 v70, 0\n\t" \
    "v_mov_b32 v71, 0\n\t" \
    "v_mov_b32 v72, 0\n\t" \
    "v_mov_b32 v73, 0\n\t" \
    "v_mov_b32 v74, 0\n\t" \
    "v_mov_b32 v75, 0\n\t" \
    "v_mov_b32 v76, 0\n\t" \
    "v_mov_b32 v77, 0\n\t" \
    "v_mov_b32 v78, 0\n\t" \
    "v_mov_b32 v79, 0\n\t" \
    "v_mov_b32 v80, 0\n\t" \
    "v_mov_b32 v81, 0\n\t" \
    "v_mov_b32 v82, 0\n\t" \
    "v_mov_b32 v83, 0\n\t" \
    "s_waitcnt vmcnt(8)\n\t" \
    "s_barrier\n\t" \
    "ds_read_b64_tr_b16 v[116:117], %[b0] offset:0\n\t" \
    "ds_read_b64_tr_b16 v[118:119], %[b0] offset:1024\n\t" \
    "ds_read_b64_tr_b16 v[124:125], %[b1] offset:0\n\t" \
    "ds_read_b64_tr_b16 v[126:127], %[b1] offset:1024\n\t" \
    "ds_read_b64_tr_b16 v[120:121], %[b0] offset:8192\n\t" \
    "ds_read_b64_tr_b16 v[122:123], %[b0] offset:9216\n\t" \
    "ds_read_b64_tr_b16 v[128:129], %[b1] offset:8192\n\t" \
    "ds_read_b64_tr_b16 v[130:131], %[b1] offset:9216\n\t" \
    "ds_read_b128 v[84:87], %[a] offset:0\n\t" \
    "ds_read_b128 v[88:91], %[a] offset:1024\n\t" \
    "ds_read_b128 v[92:95], %[a] offset:2048\n\t" \
    "ds_read_b128 v[96:99], %[a] offset:3072\n\t" \
    "ds_read_b128 v[100:103], %[a] offset:4096\n\t" \
    "ds_read_b128 v[104:107], %[a] offset:5120\n\t" \
    "ds_read_b128 v[108:111], %[a] offset:6144\n\t" \
    "ds_read_b128 v[112:115], %[a] offset:7168\n\t" \
    "s_add_u32 %[d], %[lw], %[c]\n\t" \
    "s_add_u32 %[y], %[c], 49152\n\t" \
    "s_cmp_eq_u32 %[y], 147456\n\t" \
    "s_cselect_b32 %[y], 0, %[y]\n\t" \
    "s_sub_u32 %[z], %[y], %[c]\n\t" \
    "s_mov_b32 %[c], %[y]\n\t" \
    "s_add_u32 %[x], %[t], 3\n\t" \
    "s_sub_u32 %[y], %[x], %[nt]\n\t" \
    "s_cmp_lt_u32 %[x], %[nt]\n\t" \
    "s_cselect_b32 %[x], %[x], %[y]\n\t" \
    "s_cselect_b32 m0, %[cb], %[cn]\n\t" \
    "s_mul_i32 %[y], %[x], %[kb]\n\t" \
    "s_add_u32 %[y], %[y], m0\n\t" \
    "s_lshl_b32 %[x], %[x], 11\n\t" \
    "s_add_u32 %[x], %[x], %[ca]\n\t" \
    "v_add_u32 %[a], %[z], %[a]\n\t" \
    "v_add_u32 %[b0], %[z], %[b0]\n\t" \
    "v_add_u32 %[b1], %[z], %[b1]\n\t" \
    "L_%=:\n\t" \
    "s_waitcnt vmcnt(4) lgkmcnt(0)\n\t" \
    "s_barrier\n\t" \
    "v_mfma_f32_16x16x32_bf16 v[20:23], v[116:119], v[84:87], v[20:23]\n\t" \
    "ds_read_b64_tr_b16 v[180:181], %[b0] offset:0\n\t" \
    "ds_read_b64_tr_b16 v[182:183], %[b0] offset:1024\n\t" \
    "v_mfma_f32_16x16x32_bf16 v[24:27], v[124:127], v[84:87], v[24:27]\n\t" \
    "ds_read_b64_tr_b16 v[188:189], %[b1] offset:0\n\t" \
    "ds_read_b64_tr_b16 v[190:191], %[b1] offset:1024\n\t" \
    "s_add_u32 m0, %[d], 0\n\t" \
    "v_mfma_f32_16x16x32_bf16 v[20:23], v[120:123], v[88:91], v[20:23]\n\t" \
    "buffer_load_dwordx4 %[vb0], %[rb], %[y] offen lds\n\t" \
    "v_mfma_f32_16x16x32_bf16 v[24:27], v[128:131], v[88:91], v[24:27]\n\t" \
    "ds_read_b64_tr_b16 v[184:185], %[b0] offset:8192\n\t" \
    "ds_read_b64_tr_b16 v[186:187], %[b0] offset:9216\n\t" \
    "s_add_u32 m0, %[d], 8192\n\t" \
    "v_mfma_f32_16x16x32_bf16 v[28:31], v[116:119], v[92:95], v[28:31]\n\t" \
    "buffer_load_dwordx4 %[vb1], %[rb], %[y] offen lds\n\t" \
    "v_mfma_f32_16x16x32_bf16 v[32:35], v[124:127], v[92:95], v[32:35]\n\t" \
    "ds_read_b64_tr_b16 v[192:193], %[b1] offset:8192\n\t" \
    "ds_read_b64_tr_b16 v[194:195], %[b1] offset:9216\n\t" \
    "s_add_u32 m0, %[d], 32768\n\t" \
    "v_mfma_f32_16x16x32_bf16 v[28:31], v[120:123], v[96:99], v[28:31]\n\t" \
    "buffer_load_dwordx4 %[va0], %[ra], %[x] offen sc1 lds\n\t" \
    "v_mfma_f32_16x16x32_bf16 v[32:35], v[128:131], v[96:99], v[32:35]\n\t" \
    "ds_read_b128 v[148:151], %[a] offset:0\n\t" \
    "s_add_u32 m0, %[d], 40960\n\t" \
    "v_mfma_f32_16x16x32_bf16 v[36:39], v[116:119], v[100:103], v[36:39]\n\t" \
    "buffer_load_dwordx4 %[va1], %[ra], %[x] offen sc1 lds\n\t" \
    "v_mfma_f32_16x16x32_bf16 v[40:43], v[124:127], v[100:103], v[40:43]\n\t" \
    "ds_read_b128 v[152:155], %[a] offset:1024\n\t" \
    "ds_read_b128 v[156:159], %[a] offset:2048\n\t" \
    "s_add_u32 %[t], %[t], 1\n\t" \
    "s_add_u32 %[d], %[lw], %[c]\n\t" \
    "s_add_u32 %[y], %[c], 49152\n\t" \
    "v_mfma_f32_16x16x32_bf16 v[36:39], v[120:123], v[104:107], v[36:39]\n\t" \
    "ds_read_b128 v[160:163], %[a] offset:3072\n\t" \
    "ds_read_b128 v[164:167], %[a] offset:4096\n\t" \
    "s_cmp_eq_u32 %[y], 147456\n\t" \
    "s_cselect_b32 %[y], 0, %[y]\n\t" \
    "s_sub_u32 %[z], %[y], %[c]\n\t" \
    "v_mfma_f32_16x16x32_bf16 v[40:43], v[128:131], v[104:107], v[40:43]\n\t" \
    "ds_read_b128 v[168:171], %[a] offset:5120\n\t" \
    "ds_read_b128 v[172:175], %[a] offset:6144\n\t" \
    "s_mov_b32 %[c], %[y]\n\t" \
    "s_add_u32 %[x], %[t], 3\n\t" \
    "s_sub_u32 %[y], %[x], %[nt]\n\t" \
    "v_mfma_f32_16x16x32_bf16 v[44:47], v[116:119], v[108:111], v[44:47]\n\t" \
    "ds_read_b128 v[176:179], %[a] offset:7168\n\t" \
    "s_cmp_lt_u32 %[x], %[nt]\n\t" \
    "s_cselect_b32 %[x], %[x], %[y]\n\t" \
    "s_cselect_b32 m0, %[cb], %[cn]\n\t" \
    "v_mfma_f32_16x16x32_bf16 v[48:51], v[124:127], v[108:111], v[48:51]\n\t" \
    "s_mul_i32 %[y], %[x], %[kb]\n\t" \
    "s_add_u32 %[y], %[y], m0\n\t" \
    "s_lshl_b32 %[x], %[x], 11\n\t" \
    "v_mfma_f32_16x16x32_bf16 v[44:47], v[120:123], v[112:115], v[44:47]\n\t" \
    "s_add_u32 %[x], %[x], %[ca]\n\t" \
    "v_mfma_f32_16x16x32_bf16 v[48:51], v[128:131], v[112:115], v[48:51]\n\t" \
    "v_add_u32 %[a], %[z], %[a]\n\t" \
    "v_add_u32 %[b0], %[z], %[b0]\n\t" \
    "v_add_u32 %[b1], %[z], %[b1]\n\t" \
    "s_waitcnt vmcnt(4) lgkmcnt(0)\n\t" \
    "s_barrier\n\t" \
    "v_mfma_f32_16x16x32_bf16 v[20:23], v[180:183], v[148:151], v[20:23]\n\t" \
    "ds_read_b64_tr_b16 v[116:117], %[b0] offset:0\n\t" \
    "ds_read_b64_tr_b16 v[118:119], %[b0] offset:1024\n\t" \
    "v_mfma_f32_16x16x32_bf16 v[24:27], v[188:191], v[148:151], v[24:27]\n\t" \
    "ds_read_b64_tr_b16 v[124:125], %[b1] offset:0\n\t" \
    "ds_read_b64_tr_b16 v[126:127], %[b1] offset:1024\n\t" \
    "s_add_u32 m0, %[d], 0\n\t" \
    "v_mfma_f32_16x16x32_bf16 v[20:23], v[184:187], v[152:155], v[20:23]\n\t" \
    "buffer_load_dwordx4 %[vb0], %[rb], %[y] offen lds\n\t" \
    "v_mfma_f32_16x16x32_bf16 v[24:27], v[192:195], v[152:155], v[24:27]\n\t" \
    "ds_read_b64_tr_b16 v[120:121], %[b0] offset:8192\n\t" \
    "ds_read_b64_tr_b16 v[122:123], %[b0] offset:9216\n\t" \
    "s_add_u32 m0, %[d], 8192\n\t" \
    "v_mfma_f32_16x16x32_bf16 v[28:31], v[180:183], v[156:159], v[28:31]\n\t" \
    "buffer_load_dwordx4 %[vb1], %[rb], %[y] offen lds\n\t" \
    "v_mfma_f32_16x16x32_bf16 v[32:35], v[188:191], v[156:159], v[32:35]\n\t" \
    "ds_read_b64_tr_b16 v[128:129], %[b1] offset:8192\n\t" \
    "ds_read_b64_tr_b16 v[130:131], %[b1] offset:9216\n\t" \
    "s_add_u32 m0, %[d], 32768\n\t" \
    "v_mfma_f32_16x16x32_bf16 v[28:31], v[184:187], v[160:163], v[28:31]\n\t" \
    "buffer_load_dwordx4 %[va0], %[ra], %[x] offen sc1 lds\n\t" \
    "v_mfma_f32_16x16x32_bf16 v[32:35], v[192:195], v[160:163], v[32:35]\n\t" \
    "ds_read_b128 v[84:87], %[a] offset:0\n\t" \
    "s_add_u32 m0, %[d], 40960\n\t" \
    "v_mfma_f32_16x16x32_bf16 v[36:39], v[180:183], v[164:167], v[36:39]\n\t" \
    "buffer_load_dwordx4 %[va1], %[ra], %[x] offen sc1 lds\n\t" \
    "v_mfma_f32_16x16x32_bf16 v[40:43], v[188:191], v[164:167], v[40:43]\n\t" \
    "ds_read_b128 v[88:91], %[a] offset:1024\n\t" \
    "ds_read_b128 v[92:95], %[a] offset:2048\n\t" \
    "s_add_u32 %[t], %[t], 1\n\t" \
    "s_add_u32 %[d], %[lw], %[c]\n\t" \
    "s_add_u32 %[y], %[c], 49152\n\t" \
    "v_mfma_f32_16x16x32_bf16 v[36:39], v[184:187], v[168:171], v[36:39]\n\t" \
    "ds_read_b128 v[96:99], %[a] offset:3072\n\t" \
    "ds_read_b128 v[100:103], %[a] offset:4096\n\t" \
    "s_cmp_eq_u32 %[y], 147456\n\t" \
    "s_cselect_b32 %[y], 0, %[y]\n\t" \
    "s_sub_u32 %[z], %[y], %[c]\n\t" \
    "v_mfma_f32_16x16x32_bf16 v[40:43], v[192:195], v[168:171], v[40:43]\n\t" \
    "ds_read_b128 v[104:107], %[a] offset:5120\n\t" \
    "ds_read_b128 v[108:111], %[a] offset:6144\n\t" \
    "s_mov_b32 %[c], %[y]\n\t" \
    "s_add_u32 %[x], %[t], 3\n\t" \
    "s_sub_u32 %[y], %[x], %[nt]\n\t" \
    "v_mfma_f32_16x16x32_bf16 v[44:47], v[180:183], v[172:175], v[44:47]\n\t" \
    "ds_read_b128 v[112:115], %[a] offset:7168\n\t" \
    "s_cmp_lt_u32 %[x], %[nt]\n\t" \
    "s_cselect_b32 %[x], %[x], %[y]\n\t" \
    "s_cselect_b32 m0, %[cb], %[cn]\n\t" \
    "v_mfma_f32_16x16x32_bf16 v[48:51], v[188:191], v[172:175], v[48:51]\n\t" \
    "s_mul_i32 %[y], %[x], %[kb]\n\t" \
    "s_add_u32 %[y], %[y], m0\n\t" \
    "s_lshl_b32 %[x], %[x], 11\n\t" \
    "v_mfma_f32_16x16x32_bf16 v[44:47], v[184:187], v[176:179], v[44:47]\n\t" \
    "s_add_u32 %[x], %[x], %[ca]\n\t" \
    "s_cmp_lt_u32 %[t], %[nt]\n\t" \
    "v_mfma_f32_16x16x32_bf16 v[48:51], v[192:195], v[176:179], v[48:51]\n\t" \
    "v_add_u32 %[a], %[z], %[a]\n\t" \
    "v_add_u32 %[b0], %[z], %[b0]\n\t" \
    "v_add_u32 %[b1], %[z], %[b1]\n\t" \
    "s_cbranch_scc1 L_%=\n\t" \
    "s_waitcnt lgkmcnt(0)\n\t" \
    "s_nop 7\n\t" \
    : "=&{v[20:23]}"(acc[0][0][0][0]), "=&{v[24:27]}"(acc[0][0][0][1]), "=&{v[28:31]}"(acc[0][0][1][0]), "=&{v[32:35]}"(acc[0][0][1][1]), "=&{v[36:39]}"(acc[0][0][2][0]), "=&{v[40:43]}"(acc[0][0][2][1]), "=&{v[44:47]}"(acc[0][0][3][0]), "=&{v[48:51]}"(acc[0][0][3][1]), "=&{v[52:55]}"(acc[0][1][0][0]), "=&{v[56:59]}"(acc[0][1][0][1]), "=&{v[60:63]}"(acc[0][1][1][0]), "=&{v[64:67]}"(acc[0][1][1][1]), "=&{v[68:71]}"(acc[0][1][2][0]), "=&{v[72:75]}"(acc[0][1][2][1]), "=&{v[76:79]}"(acc[0][1][3][0]), "=&{v[80:83]}"(acc[0][1][3][1]), [a] "+v"(aoff), [b0] "+v"(boff0), [b1] "+v"(boff1), [c] "+s"(cur), [t] "+s"(t), [d] "=&s"(sd), [x] "=&s"(sx), [y] "=&s"(sy), [z] "=&s"(sz) \
    : [va0] "v"(voffA[0]), [va1] "v"(voffA[1]), [vb0] "v"(voffB[0]), [vb1] "v"(voffB[1]), [ra] "s"(ra), [rb] "s"(rb), [lw] "s"(ldsw), [nt] "s"(nt), [kb] "s"(kB), [ca] "s"(cA), [cb] "s"(cB), [cn] "s"(cN) \
    : "memory", "scc", "v84", "v85", "v86", "v87", "v88", "v89", "v90", "v91", "v92", "v93", "v94", "v95", "v96", "v97", "v98", "v99", "v100", "v101", "v102", "v103", "v104", "v105", "v106", "v107", "v108", "v109", "v110", "v111", "v112", "v113", "v114", "v115", "v116", "v117", "v118", "v119", "v120", "v121", "v122", "v123", "v124", "v125", "v126", "v127", "v128", "v129", "v130", "v131", "v132", "v133", "v134", "v135", "v136", "v137", "v138", "v139", "v140", "v141", "v142", "v143", "v144", "v145", "v146", "v147", "v148", "v149", "v150", "v151", "v152", "v153", "v154", "v155", "v156", "v157", "v158", "v159", "v160", "v161", "v162", "v163", "v164", "v165", "v166", "v167", "v168", "v169", "v170", "v171", "v172", "v173", "v174", "v175", "v176", "v177", "v178", "v179", "v180", "v181", "v182", "v183", "v184", "v185", "v186", "v187", "v188", "v189", "v190", "v191", "v192", "v193", "v194", "v195", "v196", "v197", "v198", "v199", "v200", "v201", "v202", "v203", "v204", "v205", "v206", "v207", "v208", "v209", "v210", "v211")

#define H_LOOP2() \
  asm volatile( \
    "v_mov_b32 v20, 0\n\t" \
    "v_mov_b32 v21, 0\n\t" \
    "v_mov_b32 v22, 0\n\t" \
    "v_mov_b32 v23, 0\n\t" \
    "v_mov_b32 v24, 0\n\t" \
    "v_mov_b32 v25, 0\n\t" \
    "v_mov_b32 v26, 0\n\t" \
    "v_mov_b32 v27, 0\n\t" \
    "v_mov_b32 v28, 0\n\t" \
    "v_mov_b32 v29, 0\n\t" \
    "v_mov_b32 v30, 0\n\t" \
    "v_mov_b32 v31, 0\n\t" \
    "v_mov_b32 v32, 0\n\t" \
    "v_mov_b32 v33, 0\n\t" \
    "v_mov_b32 v34, 0\n\t" \
    "v_mov_b32 v35, 0\n\t" \
    "v_mov_b32 v36, 0\n\t" \
    "v_mov_b32 v37, 0\n\t" \
    "v_mov_b32 v38, 0\n\t" \
    "v_mov_b32 v39, 0\n\t" \
    "v_mov_b32 v40, 0\n\t" \
    "v_mov_b32 v41, 0\n\t" \
    "v_mov_b32 v42, 0\n\t" \
    "v_mov_b32 v43, 0\n\t" \
    "v_mov_b32 v44, 0\n\t" \
    "v_mov_b32 v45, 0\n\t" \
    "v_mov_b32 v46, 0\n\t" \
    "v_mov_b32 v47, 0\n\t" \
    "v_mov_b32 v48, 0\n\t" \
    "v_mov_b32 v49, 0\n\t" \
    "v_mov_b32 v50, 0\n\t" \
    "v_mov_b32 v51, 0\n\t" \
    "v_mov_b32 v52, 0\n\t" \
    "v_mov_b32 v53, 0\n\t" \
    "v_mov_b32 v54, 0\n\t" \
    "v_mov_b32 v55, 0\n\t" \
    "v_mov_b32 v56, 0\n\t" \
    "v_mov_b32 v57, 0\n\t" \
    "v_mov_b32 v58, 0\n\t" \
    "v_mov_b32 v59, 0\n\t" \
    "v_mov_b32 v60, 0\n\t" \
    "v_mov_b32 v61, 0\n\t" \
    "v_mov_b32 v62, 0\n\t" \
    "v_mov_b32 v63, 0\n\t" \
    "v_mov_b32 v64, 0\n\t" \
    "v_mov_b32 v65, 0\n\t" \
    "v_mov_b32 v66, 0\n\t" \
    "v_mov_b32 v67, 0\n\t" \
    "v_mov_b32 v68, 0\n\t" \
    "v_mov_b32 v69, 0\n\t" \
    "v_mov_b32 v70, 0\n\t" \
    "v_mov_b32 v71, 0\n\t" \
    "v_mov_b32 v72, 0\n\t" \
    "v_mov_b32 v73, 0\n\t" \
    "v_mov_b32 v74, 0\n\t" \
    "v_mov_b32 v75, 0\n\t" \
    "v_mov_b32 v76, 0\n\t" \
    "v_mov_b32 v77, 0\n\t" \
    "v_mov_b32 v78, 0\n\t" \
    "v_mov_b32 v79, 0\n\t" \
    "v_mov_b32 v80, 0\n\t" \
    "v_mov_b32 v81, 0\n\t" \
    "v_mov_b32 v82, 0\n\t" \
    "v_mov_b32 v83, 0\n\t" \
    "s_waitcnt vmcnt(12)\n\t" \
    "s_barrier\n\t" \
    "ds_read_b64_tr_b16 v[116:117], %[b0] offset:0\n\t" \
    "ds_read_b64_tr_b16 v[118:119], %[b0] offset:1024\n\t" \
    "ds_read_b64_tr_b16 v[124:125], %[b1] offset:0\n\t" \
    "ds_read_b64_tr_b16 v[126:127], %[b1] offset:1024\n\t" \
    "ds_read_b64_tr_b16 v[120:121], %[b0] offset:8192\n\t" \
    "ds_read_b64_tr_b16 v[122:123], %[b0] offset:9216\n\t" \
    "ds_read_b64_tr_b16 v[128:129], %[b1] offset:8192\n\t" \
    "ds_read_b64_tr_b16 v[130:131], %[b1] offset:9216\n\t" \
    "ds_read_b128 v[84:87], %[a] offset:0\n\t" \
    "ds_read_b128 v[88:91], %[a] offset:1024\n\t" \
    "ds_read_b128 v[92:95], %[a] offset:2048\n\t" \
    "ds_read_b128 v[96:99], %[a] offset:3072\n\t" \
    "ds_read_b128 v[100:103], %[a] offset:4096\n\t" \
    "ds_read_b128 v[104:107], %[a] offset:5120\n\t" \
    "ds_read_b128 v[108:111], %[a] offset:6144\n\t" \
    "ds_read_b128 v[112:115], %[a] offset:7168\n\t" \
    "ds_read_b64_tr_b16 v[132:133], %[b0] offset:16384\n\t" \
    "ds_read_b64_tr_b16 v[134:135], %[b0] offset:17408\n\t" \
    "ds_read_b64_tr_b16 v[140:141], %[b1] offset:16384\n\t" \
    "ds_read_b64_tr_b16 v[142:143], %[b1] offset:17408\n\t" \
    "ds_read_b64_tr_b16 v[136:137], %[b0] offset:24576\n\t" \
    "ds_read_b64_tr_b16 v[138:139], %[b0] offset:25600\n\t" \
    "ds_read_b64_tr_b16 v[144:145], %[b1] offset:24576\n\t" \
    "ds_read_b64_tr_b16 v[146:147], %[b1] offset:25600\n\t" \
    "s_add_u32 %[d], %[lw], %[c]\n\t" \
    "s_add_u32 %[y], %[c], 49152\n\t" \
    "s_cmp_eq_u32 %[y], 147456\n\t" \
    "s_cselect_b32 %[y], 0, %[y]\n\t" \
    "s_sub_u32 %[z], %[y], %[c]\n\t" \
    "s_mov_b32 %[c], %[y]\n\t" \
    "s_add_u32 %[x], %[t], 3\n\t" \
    "s_sub_u32 %[y], %[x], %[nt]\n\t" \
    "s_cmp_lt_u32 %[x], %[nt]\n\t" \
    "s_cselect_b32 %[x], %[x], %[y]\n\t" \
    "s_cselect_b32 m0, %[cb], %[cn]\n\t" \
    "s_mul_i32 %[y], %[x], %[kb]\n\t" \
    "s_add_u32 %[y], %[y], m0\n\t" \
    "s_lshl_b32 %[x], %[x], 11\n\t" \
    "s_add_u32 %[x], %[x], %[ca]\n\t" \
    "v_add_u32 %[a], %[z], %[a]\n\t" \
    "v_add_u32 %[b0], %[z], %[b0]\n\t" \
    "v_add_u32 %[b1], %[z], %[b1]\n\t" \
    "L_%=:\n\t" \
    "s_waitcnt vmcnt(6) lgkmcnt(0)\n\t" \
    "s_barrier\n\t" \
    "v_mfma_f32_16x16x32_bf16 v[20:23], v[116:119], v[84:87], v[20:23]\n\t" \
    "ds_read_b64_tr_b16 v[180:181], %[b0] offset:0\n\t" \
    "ds_read_b64_tr_b16 v[182:183], %[b0] offset:1024\n\t" \
    "v_mfma_f32_16x16x32_bf16 v[24:27], v[124:127], v[84:87], v[24:27]\n\t" \
    "ds_read_b64_tr_b16 v[188:189], %[b1] offset:0\n\t" \
    "ds_read_b64_tr_b16 v[190:191], %[b1] offset:1024\n\t" \
    "v_mfma_f32_16x16x32_bf16 v[20:23], v[120:123], v[88:91], v[20:23]\n\t" \
    "ds_read_b64_tr_b16 v[184:185], %[b0] offset:8192\n\t" \
    "ds_read_b64_tr_b16 v[186:187], %[b0] offset:9216\n\t" \
    "s_add_u32 m0, %[d], 0\n\t" \
    "v_mfma_f32_16x16x32_bf16 v[24:27], v[128:131], v[88:91], v[24:27]\n\t" \
    "buffer_load_dwordx4 %[vb0], %[rb], %[y] offen lds\n\t" \
    "v_mfma_f32_16x16x32_bf16 v[28:31], v[116:119], v[92:95], v[28:31]\n\t" \
    "ds_read_b64_tr_b16 v[192:193], %[b1] offset:8192\n\t" \
    "v_mfma_f32_16x16x32_bf16 v[32:35], v[124:127], v[92:95], v[32:35]\n\t" \
    "ds_read_b64_tr_b16 v[194:195], %[b1] offset:9216\n\t" \
    "v_mfma_f32_16x16x32_bf16 v[28:31], v[120:123], v[96:99], v[28:31]\n\t" \
    "ds_read_b128 v[148:151], %[a] offset:0\n\t" \
    "s_add_u32 m0, %[d], 8192\n\t" \
    "v_mfma_f32_16x16x32_bf16 v[32:35], v[128:131], v[96:99], v[32:35]\n\t" \
    "buffer_load_dwordx4 %[vb1], %[rb], %[y] offen lds\n\t" \
    "v_mfma_f32_16x16x32_bf16 v[36:39], v[116:119], v[100:103], v[36:39]\n\t" \
    "ds_read_b128 v[152:155], %[a] offset:1024\n\t" \
    "v_mfma_f32_16x16x32_bf16 v[40:43], v[124:127], v[100:103], v[40:43]\n\t" \
    "ds_read_b128 v[156:159], %[a] offset:2048\n\t" \
    "v_mfma_f32_16x16x32_bf16 v[36:39], v[120:123], v[104:107], v[36:39]\n\t" \
    "ds_read_b128 v[160:163], %[a] offset:3072\n\t" \
    "s_add_u32 m0, %[d], 32768\n\t" \
    "v_mfma_f32_16x16x32_bf16 v[40:43], v[128:131], v[104:107], v[40:43]\n\t" \
    "buffer_load_dwordx4 %[va0], %[ra], %[x] offen sc1 lds\n\t" \
    "v_mfma_f32_16x16x32_bf16 v[44:47], v[116:119], v[108:111], v[44:47]\n\t" \
    "ds_read_b128 v[164:167], %[a] offset:4096\n\t" \
    "v_mfma_f32_16x16x32_bf16 v[48:51], v[124:127], v[108:111], v[48:51]\n\t" \
    "ds_read_b128 v[168:171], %[a] offset:5120\n\t" \
    "v_mfma_f32_16x16x32_bf16 v[44:47], v[120:123], v[112:115], v[44:47]\n\t" \
    "ds_read_b128 v[172:175], %[a] offset:6144\n\t" \
    "s_add_u32 m0, %[d], 40960\n\t" \
    "v_mfma_f32_16x16x32_bf16 v[48:51], v[128:131], v[112:115], v[48:51]\n\t" \
    "buffer_load_dwordx4 %[va1], %[ra], %[x] offen sc1 lds\n\t" \
    "v_mfma_f32_16x16x32_bf16 v[52:55], v[132:135], v[84:87], v[52:55]\n\t" \
    "ds_read_b128 v[176:179], %[a] offset:7168\n\t" \
    "v_mfma_f32_16x16x32_bf16 v[56:59], v[140:143], v[84:87], v[56:59]\n\t" \
    "ds_read_b64_tr_b16 v[196:197], %[b0] offset:16384\n\t" \
    "v_mfma_f32_16x16x32_bf16 v[52:55], v[136:139], v[88:91], v[52:55]\n\t" \
    "ds_read_b64_tr_b16 v[198:199], %[b0] offset:17408\n\t" \
    "s_add_u32 m0, %[d], 16128\n\t" \
    "v_mfma_f32_16x16x32_bf16 v[56:59], v[144:147], v[88:91], v[56:59]\n\t" \
    "buffer_load_dwordx4 %[vb0], %[rb], %[y] offen offset:256 lds\n\t" \
    "v_mfma_f32_16x16x32_bf16 v[60:63], v[132:135], v[92:95], v[60:63]\n\t" \
    "ds_read_b64_tr_b16 v[204:205], %[b1] offset:16384\n\t" \
    "v_mfma_f32_16x16x32_bf16 v[64:67], v[140:143], v[92:95], v[64:67]\n\t" \
    "ds_read_b64_tr_b16 v[206:207], %[b1] offset:17408\n\t" \
    "v_mfma_f32_16x16x32_bf16 v[60:63], v[136:139], v[96:99], v[60:63]\n\t" \
    "ds_read_b64_tr_b16 v[200:201], %[b0] offset:24576\n\t" \
    "s_add_u32 m0, %[d], 24320\n\t" \
    "v_mfma_f32_16x16x32_bf16 v[64:67], v[144:147], v[96:99], v[64:67]\n\t" \
    "buffer_load_dwordx4 %[vb1], %[rb], %[y] offen offset:256 lds\n\t" \
    "v_mfma_f32_16x16x32_bf16 v[68:71], v[132:135], v[100:103], v[68:71]\n\t" \
    "ds_read_b64_tr_b16 v[202:203], %[b0] offset:25600\n\t" \
    "s_add_u32 %[t], %[t], 1\n\t" \
    "s_add_u32 %[d], %[lw], %[c]\n\t" \
    "v_mfma_f32_16x16x32_bf16 v[72:75], v[140:143], v[100:103], v[72:75]\n\t" \
    "ds_read_b64_tr_b16 v[208:209], %[b1] offset:24576\n\t" \
    "s_add_u32 %[y], %[c], 49152\n\t" \
    "s_cmp_eq_u32 %[y], 147456\n\t" \
    "v_mfma_f32_16x16x32_bf16 v[68:71], v[136:139], v[104:107], v[68:71]\n\t" \
    "ds_read_b64_tr_b16 v[210:211], %[b1] offset:25600\n\t" \
    "s_cselect_b32 %[y], 0, %[y]\n\t" \
    "s_sub_u32 %[z], %[y], %[c]\n\t" \
    "v_mfma_f32_16x16x32_bf16 v[72:75], v[144:147], v[104:107], v[72:75]\n\t" \
    "s_mov_b32 %[c], %[y]\n\t" \
    "s_add_u32 %[x], %[t], 3\n\t" \
    "v_mfma_f32_16x16x32_bf16 v[76:79], v[132:135], v[108:111], v[76:79]\n\t" \
    "s_sub_u32 %[y], %[x], %[nt]\n\t" \
    "s_cmp_lt_u32 %[x], %[nt]\n\t" \
    "v_mfma_f32_16x16x32_bf16 v[80:83], v[140:143], v[108:111], v[80:83]\n\t" \
    "s_cselect_b32 %[x], %[x], %[y]\n\t" \
    "s_cselect_b32 m0, %[cb], %[cn]\n\t" \
    "v_mfma_f32_16x16x32_bf16 v[76:79], v[136:139], v[112:115], v[76:79]\n\t" \
    "s_mul_i32 %[y], %[x], %[kb]\n\t" \
    "s_add_u32 %[y], %[y], m0\n\t" \
    "v_mfma_f32_16x16x32_bf16 v[80:83], v[144:147], v[112:115], v[80:83]\n\t" \
    "s_lshl_b32 %[x], %[x], 11\n\t" \
    "s_add_u32 %[x], %[x], %[ca]\n\t" \
    "v_add_u32 %[a], %[z], %[a]\n\t" \
    "v_add_u32 %[b0], %[z], %[b0]\n\t" \
    "v_add_u32 %[b1], %[z], %[b1]\n\t" \
    "s_waitcnt vmcnt(6) lgkmcnt(0)\n\t" \
    "s_barrier\n\t" \
    "v_mfma_f32_16x16x32_bf16 v[20:23], v[180:183], v[148:151], v[20:23]\n\t" \
    "ds_read_b64_tr_b16 v[116:117], %[b0] offset:0\n\t" \
    "ds_read_b64_tr_b16 v[118:119], %[b0] offset:1024\n\t" \
    "v_mfma_f32_16x16x32_bf16 v[24:27], v[188:191], v[148:151], v[24:27]\n\t" \
    "ds_read_b64_tr_b16 v[124:125], %[b1] offset:0\n\t" \
    "ds_read_b64_tr_b16 v[126:127], %[b1] offset:1024\n\t" \
    "v_mfma_f32_16x16x32_bf16 v[20:23], v[184:187], v[152:155], v[20:23]\n\t" \
    "ds_read_b64_tr_b16 v[120:121], %[b0] offset:8192\n\t" \
    "ds_read_b64_tr_b16 v[122:123], %[b0] offset:9216\n\t" \
    "s_add_u32 m0, %[d], 0\n\t" \
    "v_mfma_f32_16x16x32_bf16 v[24:27], v[192:195], v[152:155], v[24:27]\n\t" \
    "buffer_load_dwordx4 %[vb0], %[rb], %[y] offen lds\n\t" \
    "v_mfma_f32_16x16x32_bf16 v[28:31], v[180:183], v[156:159], v[28:31]\n\t" \
    "ds_read_b64_tr_b16 v[128:129], %[b1] offset:8192\n\t" \
    "v_mfma_f32_16x16x32_bf16 v[32:35], v[188:191], v[156:159], v[32:35]\n\t" \
    "ds_read_b64_tr_b16 v[130:131], %[b1] offset:9216\n\t" \
    "v_mfma_f32_16x16x32_bf16 v[28:31], v[184:187], v[160:163], v[28:31]\n\t" \
    "ds_read_b128 v[84:87], %[a] offset:0\n\t" \
    "s_add_u32 m0, %[d], 8192\n\t" \
    "v_mfma_f32_16x16x32_bf16 v[32:35], v[192:195], v[160:163], v[32:35]\n\t" \
    "buffer_load_dwordx4 %[vb1], %[rb], %[y] offen lds\n\t" \
    "v_mfma_f32_16x16x32_bf16 v[36:39], v[180:183], v[164:167], v[36:39]\n\t" \
    "ds_read_b128 v[88:91], %[a] offset:1024\n\t" \
    "v_mfma_f32_16x16x32_bf16 v[40:43], v[188:191], v[164:167], v[40:43]\n\t" \
    "ds_read_b128 v[92:95], %[a] offset:2048\n\t" \
    "v_mfma_f32_16x16x32_bf16 v[36:39], v[184:187], v[168:171], v[36:39]\n\t" \
    "ds_read_b128 v[96:99], %[a] offset:3072\n\t" \
    "s_add_u32 m0, %[d], 32768\n\t" \
    "v_mfma_f32_16x16x32_bf16 v[40:43], v[192:195], v[168:171], v[40:43]\n\t" \
    "buffer_load_dwordx4 %[va0], %[ra], %[x] offen sc1 lds\n\t" \
    "v_mfma_f32_16x16x32_bf16 v[44:47], v[180:183], v[172:175], v[44:47]\n\t" \
    "ds_read_b128 v[100:103], %[a] offset:4096\n\t" \
    "v_mfma_f32_16x16x32_bf16 v[48:51], v[188:191], v[172:175], v[48:51]\n\t" \
    "ds_read_b128 v[104:107], %[a] offset:5120\n\t" \
    "v_mfma_f32_16x16x32_bf16 v[44:47], v[184:187], v[176:179], v[44:47]\n\t" \
    "ds_read_b128 v[108:111], %[a] offset:6144\n\t" \
    "s_add_u32 m0, %[d], 40960\n\t" \
    "v_mfma_f32_16x16x32_bf16 v[48:51], v[192:195], v[176:179], v[48:51]\n\t" \
    "buffer_load_dwordx4 %[va1], %[ra], %[x] offen sc1 lds\n\t" \
    "v_mfma_f32_16x16x32_bf16 v[52:55], v[196:199], v[148:151], v[52:55]\n\t" \
    "ds_read_b128 v[112:115], %[a] offset:7168\n\t" \
    "v_mfma_f32_16x16x32_bf16 v[56:59], v[204:207], v[148:151], v[56:59]\n\t" \
    "ds_read_b64_tr_b16 v[132:133], %[b0] offset:16384\n\t" \
    "v_mfma_f32_16x16x32_bf16 v[52:55], v[200:203], v[152:155], v[52:55]\n\t" \
    "ds_read_b64_tr_b16 v[134:135], %[b0] offset:17408\n\t" \
    "s_add_u32 m0, %[d], 16128\n\t" \
    "v_mfma_f32_16x16x32_bf16 v[56:59], v[208:211], v[152:155], v[56:59]\n\t" \
    "buffer_load_dwordx4 %[vb0], %[rb], %[y] offen offset:256 lds\n\t" \
    "v_mfma_f32_16x16x32_bf16 v[60:63], v[196:199], v[156:159], v[60:63]\n\t" \
    "ds_read_b64_tr_b16 v[140:141], %[b1] offset:16384\n\t" \
    "v_mfma_f32_16x16x32_bf16 v[64:67], v[204:207], v[156:159], v[64:67]\n\t" \
    "ds_read_b64_tr_b16 v[142:143], %[b1] offset:17408\n\t" \
    "v_mfma_f32_16x16x32_bf16 v[60:63], v[200:203], v[160:163], v[60:63]\n\t" \
    "ds_read_b64_tr_b16 v[136:137], %[b0] offset:24576\n\t" \
    "s_add_u32 m0, %[d], 24320\n\t" \
    "v_mfma_f32_16x16x32_bf16 v[64:67], v[208:211], v[160:163], v[64:67]\n\t" \
    "buffer_load_dwordx4 %[vb1], %[rb], %[y] offen offset:256 lds\n\t" \
    "v_mfma_f32_16x16x32_bf16 v[68:71], v[196:199], v[164:167], v[68:71]\n\t" \
    "ds_read_b64_tr_b16 v[138:139], %[b0] offset:25600\n\t" \
    "s_add_u32 %[t], %[t], 1\n\t" \
    "s_add_u32 %[d], %[lw], %[c]\n\t" \
    "s_add_u32 %[y], %[c], 49152\n\t" \
    "v_mfma_f32_16x16x32_bf16 v[72:75], v[204:207], v[164:167], v[72:75]\n\t" \
    "ds_read_b64_tr_b16 v[144:145], %[b1] offset:24576\n\t" \
    "s_cmp_eq_u32 %[y], 147456\n\t" \
    "s_cselect_b32 %[y], 0, %[y]\n\t" \
    "s_sub_u32 %[z], %[y], %[c]\n\t" \
    "v_mfma_f32_16x16x32_bf16 v[68:71], v[200:203], v[168:171], v[68:71]\n\t" \
    "ds_read_b64_tr_b16 v[146:147], %[b1] offset:25600\n\t" \
    "s_mov_b32 %[c], %[y]\n\t" \
    "s_add_u32 %[x], %[t], 3\n\t" \
    "s_sub_u32 %[y], %[x], %[nt]\n\t" \
    "v_mfma_f32_16x16x32_bf16 v[72:75], v[208:211], v[168:171], v[72:75]\n\t" \
    "s_cmp_lt_u32 %[x], %[nt]\n\t" \
    "s_cselect_b32 %[x], %[x], %[y]\n\t" \
    "s_cselect_b32 m0, %[cb], %[cn]\n\t" \
    "v_mfma_f32_16x16x32_bf16 v[76:79], v[196:199], v[172:175], v[76:79]\n\t" \
    "s_mul_i32 %[y], %[x], %[kb]\n\t" \
    "s_add_u32 %[y], %[y], m0\n\t" \
    "s_lshl_b32 %[x], %[x], 11\n\t" \
    "v_mfma_f32_16x16x32_bf16 v[80:83], v[204:207], v[172:175], v[80:83]\n\t" \
    "s_add_u32 %[x], %[x], %[ca]\n\t" \
    "s_cmp_lt_u32 %[t], %[nt]\n\t" \
    "v_mfma_f32_16x16x32_bf16 v[76:79], v[200:203], v[176:179], v[76:79]\n\t" \
    "v_mfma_f32_16x16x32_bf16 v[80:83], v[208:211], v[176:179], v[80:83]\n\t" \
    "v_add_u32 %[a], %[z], %[a]\n\t" \
    "v_add_u32 %[b0], %[z], %[b0]\n\t" \
    "v_add_u32 %[b1], %[z], %[b1]\n\t" \
    "s_cbranch_scc1 L_%=\n\t" \
    "s_waitcnt lgkmcnt(0)\n\t" \
    "s_nop 7\n\t" \
    : "=&{v[20:23]}"(acc[0][0][0][0]), "=&{v[24:27]}"(acc[0][0][0][1]), "=&{v[28:31]}"(acc[0][0][1][0]), "=&{v[32:35]}"(acc[0][0][1][1]), "=&{v[36:39]}"(acc[0][0][2][0]), "=&{v[40:43]}"(acc[0][0][2][1]), "=&{v[44:47]}"(acc[0][0][3][0]), "=&{v[48:51]}"(acc[0][0][3][1]), "=&{v[52:55]}"(acc[0][1][0][0]), "=&{v[56:59]}"(acc[0][1][0][1]), "=&{v[60:63]}"(acc[0][1][1][0]), "=&{v[64:67]}"(acc[0][1][1][1]), "=&{v[68:71]}"(acc[0][1][2][0]), "=&{v[72:75]}"(acc[0][1][2][1]), "=&{v[76:79]}"(acc[0][1][3][0]), "=&{v[80:83]}"(acc[0][1][3][1]), [a] "+v"(aoff), [b0] "+v"(boff0), [b1] "+v"(boff1), [c] "+s"(cur), [t] "+s"(t), [d] "=&s"(sd), [x] "=&s"(sx), [y] "=&s"(sy), [z] "=&s"(sz) \
    : [va0] "v"(voffA[0]), [va1] "v"(voffA[1]), [vb0] "v"(voffB[0]), [vb1] "v"(voffB[1]), [ra] "s"(ra), [rb] "s"(rb), [lw] "s"(ldsw), [nt] "s"(nt), [kb] "s"(kB), [ca] "s"(cA), [cb] "s"(cB), [cn] "s"(cN) \
    : "memory", "scc", "v84", "v85", "v86", "v87", "v88", "v89", "v90", "v91", "v92", "v93", "v94", "v95", "v96", "v97", "v98", "v99", "v100", "v101", "v102", "v103", "v104", "v105", "v106", "v107", "v108", "v109", "v110", "v111", "v112", "v113", "v114", "v115", "v116", "v117", "v118", "v119", "v120", "v121", "v122", "v123", "v124", "v125", "v126", "v127", "v128", "v129", "v130", "v131", "v132", "v133", "v134", "v135", "v136", "v137", "v138", "v139", "v140", "v141", "v142", "v143", "v144", "v145", "v146", "v147", "v148", "v149", "v150", "v151", "v152", "v153", "v154", "v155", "v156", "v157", "v158", "v159", "v160", "v161", "v162", "v163", "v164", "v165", "v166", "v167", "v168", "v169", "v170", "v171", "v172", "v173", "v174", "v175", "v176", "v177", "v178", "v179", "v180", "v181", "v182", "v183", "v184", "v185", "v186", "v187", "v188", "v189", "v190", "v191", "v192", "v193", "v194", "v195", "v196", "v197", "v198", "v199", "v200", "v201", "v202", "v203", "v204", "v205", "v206", "v207", "v208", "v209", "v210", "v211")

#define H_LOOP2S() \
  asm volatile( \
    "v_mov_b32 v20, 0\n\t" \
    "v_mov_b32 v21, 0\n\t" \
    "v_mov_b32 v22, 0\n\t" \
    "v_mov_b32 v23, 0\n\t" \
    "v_mov_b32 v24, 0\n\t" \
    "v_mov_b32 v25, 0\n\t" \
    "v_mov_b32 v26, 0\n\t" \
    "v_mov_b32 v27, 0\n\t" \
    "v_mov_b32 v28, 0\n\t" \
    "v_mov_b32 v29, 0\n\t" \
    "v_mov_b32 v30, 0\n\t" \
    "v_mov_b32 v31, 0\n\t" \
    "v_mov_b32 v32, 0\n\t" \
    "v_mov_b32 v33, 0\n\t" \
    "v_mov_b32 v34, 0\n\t" \
    "v_mov_b32 v35, 0\n\t" \
    "v_mov_b32 v36, 0\n\t" \
    "v_mov_b32 v37, 0\n\t" \
    "v_mov_b32 v38, 0\n\t" \
    "v_mov_b32 v39, 0\n\t" \
    "v_mov_b32 v40, 0\n\t" \
    "v_mov_b32 v41, 0\n\t" \
    "v_mov_b32 v42, 0\n\t" \
    "v_mov_b32 v43, 0\n\t" \
    "v_mov_b32 v44, 0\n\t" \
    "v_mov_b32 v45, 0\n\t" \
    "v_mov_b32 v46, 0\n\t" \
    "v_mov_b32 v47, 0\n\t" \
    "v_mov_b32 v48, 0\n\t" \
    "v_mov_b32 v49, 0\n\t" \
    "v_mov_b32 v50, 0\n\t" \
    "v_mov_b32 v51, 0\n\t" \
    "v_mov_b32 v52, 0\n\t" \
    "v_mov_b32 v53, 0\n\t" \
    "v_mov_b32 v54, 0\n\t" \
    "v_mov_b32 v55, 0\n\t" \
    "v_mov_b32 v56, 0\n\t" \
    "v_mov_b32 v57, 0\n\t" \
    "v_mov_b32 v58, 0\n\t" \
    "v_mov_b32 v59, 0\n\t" \
    "v_mov_b32 v60, 0\n\t" \
    "v_mov_b32 v61, 0\n\t" \
    "v_mov_b32 v62, 0\n\t" \
    "v_mov_b32 v63, 0\n\t" \
    "v_mov_b32 v64, 0\n\t" \
    "v_mov_b32 v65, 0\n\t" \
    "v_mov_b32 v66, 0\n\t" \
    "v_mov_b32 v67, 0\n\t" \
    "v_mov_b32 v68, 0\n\t" \
    "v_mov_b32 v69, 0\n\t" \
    "v_mov_b32 v70, 0\n\t" \
    "v_mov_b32 v71, 0\n\t" \
    "v_mov_b32 v72, 0\n\t" \
    "v_mov_b32 v73, 0\n\t" \
    "v_mov_b32 v74, 0\n\t" \
    "v_mov_b32 v75, 0\n\t" \
    "v_mov_b32 v76, 0\n\t" \
    "v_mov_b32 v77, 0\n\t" \
    "v_mov_b32 v78, 0\n\t" \
    "v_mov_b32 v79, 0\n\t" \
    "v_mov_b32 v80, 0\n\t" \
    "v_mov_b32 v81, 0\n\t" \
    "v_mov_b32 v82, 0\n\t" \
    "v_mov_b32 v83, 0\n\t" \
    "s_waitcnt vmcnt(12)\n\t" \
    "s_barrier\n\t" \
    "ds_read_b64_tr_b16 v[116:117], %[b0] offset:0\n\t" \
    "ds_read_b64_tr_b16 v[118:119], %[b0] offset:1024\n\t" \
    "ds_read_b64_tr_b16 v[124:125], %[b1] offset:0\n\t" \
    "ds_read_b64_tr_b16 v[126:127], %[b1] offset:1024\n\t" \
    "ds_read_b64_tr_b16 v[120:121], %[b0] offset:8192\n\t" \
    "ds_read_b64_tr_b16 v[122:123], %[b0] offset:9216\n\t" \
    "ds_read_b64_tr_b16 v[128:129], %[b1] offset:8192\n\t" \
    "ds_read_b64_tr_b16 v[130:131], %[b1] offset:9216\n\t" \
    "ds_read_b128 v[84:87], %[a] offset:0\n\t" \
    "ds_read_b128 v[88:91], %[a] offset:1024\n\t" \
    "ds_read_b128 v[92:95], %[a] offset:2048\n\t" \
    "ds_read_b128 v[96:99], %[a] offset:3072\n\t" \
    "ds_read_b128 v[100:103], %[a] offset:4096\n\t" \
    "ds_read_b128 v[104:107], %[a] offset:5120\n\t" \
    "ds_read_b128 v[108:111], %[a] offset:6144\n\t" \
    "ds_read_b128 v[112:115], %[a] offset:7168\n\t" \
    "ds_read_b64_tr_b16 v[132:133], %[b0] offset:16384\n\t" \
    "ds_read_b64_tr_b16 v[134:135], %[b0] offset:17408\n\t" \
    "ds_read_b64_tr_b16 v[140:141], %[b1] offset:16384\n\t" \
    "ds_read_b64_tr_b16 v[142:143], %[b1] offset:17408\n\t" \
    "ds_read_b64_tr_b16 v[136:137], %[b0] offset:24576\n\t" \
    "ds_read_b64_tr_b16 v[138:139], %[b0] offset:25600\n\t" \
    "ds_read_b64_tr_b16 v[144:145], %[b1] offset:24576\n\t" \
    "ds_read_b64_tr_b16 v[146:147], %[b1] offset:25600\n\t" \
    "s_add_u32 %[d], %[lw], %[c]\n\t" \
    "s_add_u32 %[y], %[c], 49152\n\t" \
    "s_cmp_eq_u32 %[y], 147456\n\t" \
    "s_cselect_b32 %[y], 0, %[y]\n\t" \
    "s_sub_u32 %[z], %[y], %[c]\n\t" \
    "s_mov_b32 %[c], %[y]\n\t" \
    "s_add_u32 %[x], %[t], 3\n\t" \
    "s_sub_u32 %[y], %[x], %[nt]\n\t" \
    "s_cmp_lt_u32 %[x], %[nt]\n\t" \
    "s_cselect_b32 %[x], %[x], %[y]\n\t" \
    "s_cselect_b32 m0, %[cb], %[cn]\n\t" \
    "s_mul_i32 %[y], %[x], %[kb]\n\t" \
    "s_add_u32 %[y], %[y], m0\n\t" \
    "s_lshl_b32 %[x], %[x], 11\n\t" \
    "s_add_u32 %[x], %[x], %[ca]\n\t" \
    "v_add_u32 %[a], %[z], %[a]\n\t" \
    "v_add_u32 %[b0], %[z], %[b0]\n\t" \
    "v_add_u32 %[b1], %[z], %[b1]\n\t" \
    "L_%=:\n\t" \
    "s_waitcnt vmcnt(6) lgkmcnt(0)\n\t" \
    "s_barrier\n\t" \
    "s_sleep 2\n\t" \
    "s_nop 5\n\t" \
    "v_mfma_f32_16x16x32_bf16 v[20:23], v[116:119], v[84:87], v[20:23]\n\t" \
    "ds_read_b64_tr_b16 v[180:181], %[b0] offset:0\n\t" \
    "ds_read_b64_tr_b16 v[182:183], %[b0] offset:1024\n\t" \
    "v_mfma_f32_16x16x32_bf16 v[24:27], v[124:127], v[84:87], v[24:27]\n\t" \
    "ds_read_b64_tr_b16 v[188:189], %[b1] offset:0\n\t" \
    "ds_read_b64_tr_b16 v[190:191], %[b1] offset:1024\n\t" \
    "v_mfma_f32_16x16x32_bf16 v[20:23], v[120:123], v[88:91], v[20:23]\n\t" \
    "ds_read_b64_tr_b16 v[184:185], %[b0] offset:8192\n\t" \
    "ds_read_b64_tr_b16 v[186:187], %[b0] offset:9216\n\t" \
    "s_add_u32 m0, %[d], 0\n\t" \
    "v_mfma_f32_16x16x32_bf16 v[24:27], v[128:131], v[88:91], v[24:27]\n\t" \
    "buffer_load_dwordx4 %[vb0], %[rb], %[y] offen lds\n\t" \
    "v_mfma_f32_16x16x32_bf16 v[28:31], v[116:119], v[92:95], v[28:31]\n\t" \
    "ds_read_b64_tr_b16 v[192:193], %[b1] offset:8192\n\t" \
    "v_mfma_f32_16x16x32_bf16 v[32:35], v[124:127], v[92:95], v[32:35]\n\t" \
    "ds_read_b64_tr_b16 v[194:195], %[b1] offset:9216\n\t" \
    "v_mfma_f32_16x16x32_bf16 v[28:31], v[120:123], v[96:99], v[28:31]\n\t" \
    "ds_read_b128 v[148:151], %[a] offset:0\n\t" \
    "s_add_u32 m0, %[d], 8192\n\t" \
    "v_mfma_f32_16x16x32_bf16 v[32:35], v[128:131], v[96:99], v[32:35]\n\t" \
    "buffer_load_dwordx4 %[vb1], %[rb], %[y] offen lds\n\t" \
    "v_mfma_f32_16x16x32_bf16 v[36:39], v[116:119], v[100:103], v[36:39]\n\t" \
    "ds_read_b128 v[152:155], %[a] offset:1024\n\t" \
    "v_mfma_f32_16x16x32_bf16 v[40:43], v[124:127], v[100:103], v[40:43]\n\t" \
    "ds_read_b128 v[156:159], %[a] offset:2048\n\t" \
    "v_mfma_f32_16x16x32_bf16 v[36:39], v[120:123], v[104:107], v[36:39]\n\t" \
    "ds_read_b128 v[160:163], %[a] offset:3072\n\t" \
    "s_add_u32 m0, %[d], 32768\n\t" \
    "v_mfma_f32_16x16x32_bf16 v[40:43], v[128:131], v[104:107], v[40:43]\n\t" \
    "buffer_load_dwordx4 %[va0], %[ra], %[x] offen sc1 lds\n\t" \
    "v_mfma_f32_16x16x32_bf16 v[44:47], v[116:119], v[108:111], v[44:47]\n\t" \
    "ds_read_b128 v[164:167], %[a] offset:4096\n\t" \
    "v_mfma_f32_16x16x32_bf16 v[48:51], v[124:127], v[108:111], v[48:51]\n\t" \
    "ds_read_b128 v[168:171], %[a] offset:5120\n\t" \
    "v_mfma_f32_16x16x32_bf16 v[44:47], v[120:123], v[112:115], v[44:47]\n\t" \
    "ds_read_b128 v[172:175], %[a] offset:6144\n\t" \
    "s_add_u32 m0, %[d], 40960\n\t" \
    "v_mfma_f32_16x16x32_bf16 v[48:51], v[128:131], v[112:115], v[48:51]\n\t" \
    "buffer_load_dwordx4 %[va1], %[ra], %[x] offen sc1 lds\n\t" \
    "v_mfma_f32_16x16x32_bf16 v[52:55], v[132:135], v[84:87], v[52:55]\n\t" \
    "ds_read_b128 v[176:179], %[a] offset:7168\n\t" \
    "v_mfma_f32_16x16x32_bf16 v[56:59], v[140:143], v[84:87], v[56:59]\n\t" \
    "ds_read_b64_tr_b16 v[196:197], %[b0] offset:16384\n\t" \
    "v_mfma_f32_16x16x32_bf16 v[52:55], v[136:139], v[88:91], v[52:55]\n\t" \
    "ds_read_b64_tr_b16 v[198:199], %[b0] offset:17408\n\t" \
    "s_add_u32 m0, %[d], 16128\n\t" \
    "v_mfma_f32_16x16x32_bf16 v[56:59], v[144:147], v[88:91], v[56:59]\n\t" \
    "buffer_load_dwordx4 %[vb0], %[rb], %[y] offen offset:256 lds\n\t" \
    "v_mfma_f32_16x16x32_bf16 v[60:63], v[132:135], v[92:95], v[60:63]\n\t" \
    "ds_read_b64_tr_b16 v[204:205], %[b1] offset:16384\n\t" \
    "v_mfma_f32_16x16x32_bf16 v[64:67], v[140:143], v[92:95], v[64:67]\n\t" \
    "ds_read_b64_tr_b16 v[206:207], %[b1] offset:17408\n\t" \
    "v_mfma_f32_16x16x32_bf16 v[60:63], v[136:139], v[96:99], v[60:63]\n\t" \
    "ds_read_b64_tr_b16 v[200:201], %[b0] offset:24576\n\t" \
    "s_add_u32 m0, %[d], 24320\n\t" \
    "v_mfma_f32_16x16x32_bf16 v[64:67], v[144:147], v[96:99], v[64:67]\n\t" \
    "buffer_load_dwordx4 %[vb1], %[rb], %[y] offen offset:256 lds\n\t" \
    "v_mfma_f32_16x16x32_bf16 v[68:71], v[132:135], v[100:103], v[68:71]\n\t" \
    "ds_read_b64_tr_b16 v[202:203], %[b0] offset:25600\n\t" \
    "s_add_u32 %[t], %[t], 1\n\t" \
    "s_add_u32 %[d], %[lw], %[c]\n\t" \
    "v_mfma_f32_16x16x32_bf16 v[72:75], v[140:143], v[100:103], v[72:75]\n\t" \
    "ds_read_b64_tr_b16 v[208:209], %[b1] offset:24576\n\t" \
    "s_add_u32 %[y], %[c], 49152\n\t" \
    "s_cmp_eq_u32 %[y], 147456\n\t" \
    "v_mfma_f32_16x16x32_bf16 v[68:71], v[136:139], v[104:107], v[68:71]\n\t" \
    "ds_read_b64_tr_b16 v[210:211], %[b1] offset:25600\n\t" \
    "s_cselect_b32 %[y], 0, %[y]\n\t" \
    "s_sub_u32 %[z], %[y], %[c]\n\t" \
    "v_mfma_f32_16x16x32_bf16 v[72:75], v[144:147], v[104:107], v[72:75]\n\t" \
    "s_mov_b32 %[c], %[y]\n\t" \
    "s_add_u32 %[x], %[t], 3\n\t" \
    "v_mfma_f32_16x16x32_bf16 v[76:79], v[132:135], v[108:111], v[76:79]\n\t" \
    "s_sub_u32 %[y], %[x], %[nt]\n\t" \
    "s_cmp_lt_u32 %[x], %[nt]\n\t" \
    "v_mfma_f32_16x16x32_bf16 v[80:83], v[140:143], v[108:111], v[80:83]\n\t" \
    "s_cselect_b32 %[x], %[x], %[y]\n\t" \
    "s_cselect_b32 m0, %[cb], %[cn]\n\t" \
    "v_mfma_f32_16x16x32_bf16 v[76:79], v[136:139], v[112:115], v[76:79]\n\t" \
    "s_mul_i32 %[y], %[x], %[kb]\n\t" \
    "s_add_u32 %[y], %[y], m0\n\t" \
    "v_mfma_f32_16x16x32_bf16 v[80:83], v[144:147], v[112:115], v[80:83]\n\t" \
    "s_lshl_b32 %[x], %[x], 11\n\t" \
    "s_add_u32 %[x], %[x], %[ca]\n\t" \
    "v_add_u32 %[a], %[z], %[a]\n\t" \
    "v_add_u32 %[b0], %[z], %[b0]\n\t" \
    "v_add_u32 %[b1], %[z], %[b1]\n\t" \
    "s_waitcnt vmcnt(6) lgkmcnt(0)\n\t" \
    "s_barrier\n\t" \
    "s_sleep 2\n\t" \
    "s_nop 5\n\t" \
    "v_mfma_f32_16x16x32_bf16 v[20:23], v[180:183], v[148:151], v[20:23]\n\t" \
    "ds_read_b64_tr_b16 v[116:117], %[b0] offset:0\n\t" \
    "ds_read_b64_tr_b16 v[118:119], %[b0] offset:1024\n\t" \
    "v_mfma_f32_16x16x32_bf16 v[24:27], v[188:191], v[148:151], v[24:27]\n\t" \
    "ds_read_b64_tr_b16 v[124:125], %[b1] offset:0\n\t" \
    "ds_read_b64_tr_b16 v[126:127], %[b1] offset:1024\n\t" \
    "v_mfma_f32_16x16x32_bf16 v[20:23], v[184:187], v[152:155], v[20:23]\n\t" \
    "ds_read_b64_tr_b16 v[120:121], %[b0] offset:8192\n\t" \
    "ds_read_b64_tr_b16 v[122:123], %[b0] offset:9216\n\t" \
    "s_add_u32 m0, %[d], 0\n\t" \
    "v_mfma_f32_16x16x32_bf16 v[24:27], v[192:195], v[152:155], v[24:27]\n\t" \
    "buffer_load_dwordx4 %[vb0], %[rb], %[y] offen lds\n\t" \
    "v_mfma_f32_16x16x32_bf16 v[28:31], v[180:183], v[156:159], v[28:31]\n\t" \
    "ds_read_b64_tr_b16 v[128:129], %[b1] offset:8192\n\t" \
    "v_mfma_f32_16x16x32_bf16 v[32:35], v[188:191], v[156:159], v[32:35]\n\t" \
    "ds_read_b64_tr_b16 v[130:131], %[b1] offset:9216\n\t" \
    "v_mfma_f32_16x16x32_bf16 v[28:31], v[184:187], v[160:163], v[28:31]\n\t" \
    "ds_read_b128 v[84:87], %[a] offset:0\n\t" \
    "s_add_u32 m0, %[d], 8192\n\t" \
    "v_mfma_f32_16x16x32_bf16 v[32:35], v[192:195], v[160:163], v[32:35]\n\t" \
    "buffer_load_dwordx4 %[vb1], %[rb], %[y] offen lds\n\t" \
    "v_mfma_f32_16x16x32_bf16 v[36:39], v[180:183], v[164:167], v[36:39]\n\t" \
    "ds_read_b128 v[88:91], %[a] offset:1024\n\t" \
    "v_mfma_f32_16x16x32_bf16 v[40:43], v[188:191], v[164:167], v[40:43]\n\t" \
    "ds_read_b128 v[92:95], %[a] offset:2048\n\t" \
    "v_mfma_f32_16x16x32_bf16 v[36:39], v[184:187], v[168:171], v[36:39]\n\t" \
    "ds_read_b128 v[96:99], %[a] offset:3072\n\t" \
    "s_add_u32 m0, %[d], 32768\n\t" \
    "v_mfma_f32_16x16x32_bf16 v[40:43], v[192:195], v[168:171], v[40:43]\n\t" \
    "buffer_load_dwordx4 %[va0], %[ra], %[x] offen sc1 lds\n\t" \
    "v_mfma_f32_16x16x32_bf16 v[44:47], v[180:183], v[172:175], v[44:47]\n\t" \
    "ds_read_b128 v[100:103], %[a] offset:4096\n\t" \
    "v_mfma_f32_16x16x32_bf16 v[48:51], v[188:191], v[172:175], v[48:51]\n\t" \
    "ds_read_b128 v[104:107], %[a] offset:5120\n\t" \
    "v_mfma_f32_16x16x32_bf16 v[44:47], v[184:187], v[176:179], v[44:47]\n\t" \
    "ds_read_b128 v[108:111], %[a] offset:6144\n\t" \
    "s_add_u32 m0, %[d], 40960\n\t" \
    "v_mfma_f32_16x16x32_bf16 v[48:51], v[192:195], v[176:179], v[48:51]\n\t" \
    "buffer_load_dwordx4 %[va1], %[ra], %[x] offen sc1 lds\n\t" \
    "v_mfma_f32_16x16x32_bf16 v[52:55], v[196:199], v[148:151], v[52:55]\n\t" \
    "ds_read_b128 v[112:115], %[a] offset:7168\n\t" \
    "v_mfma_f32_16x16x32_bf16 v[56:59], v[204:207], v[148:151], v[56:59]\n\t" \
    "ds_read_b64_tr_b16 v[132:133], %[b0] offset:16384\n\t" \
    "v_mfma_f32_16x16x32_bf16 v[52:55], v[200:203], v[152:155], v[52:55]\n\t" \
    "ds_read_b64_tr_b16 v[134:135], %[b0] offset:17408\n\t" \
    "s_add_u32 m0, %[d], 16128\n\t" \
    "v_mfma_f32_16x16x32_bf16 v[56:59], v[208:211], v[152:155], v[56:59]\n\t" \
    "buffer_load_dwordx4 %[vb0], %[rb], %[y] offen offset:256 lds\n\t" \
    "v_mfma_f32_16x16x32_bf16 v[60:63], v[196:199], v[156:159], v[60:63]\n\t" \
    "ds_read_b64_tr_b16 v[140:141], %[b1] offset:16384\n\t" \
    "v_mfma_f32_16x16x32_bf16 v[64:67], v[204:207], v[156:159], v[64:67]\n\t" \
    "ds_read_b64_tr_b16 v[142:143], %[b1] offset:17408\n\t" \
    "v_mfma_f32_16x16x32_bf16 v[60:63], v[200:203], v[160:163], v[60:63]\n\t" \
    "ds_read_b64_tr_b16 v[136:137], %[b0] offset:24576\n\t" \
    "s_add_u32 m0, %[d], 24320\n\t" \
    "v_mfma_f32_16x16x32_bf16 v[64:67], v[208:211], v[160:163], v[64:67]\n\t" \
    "buffer_load_dwordx4 %[vb1], %[rb], %[y] offen offset:256 lds\n\t" \
    "v_mfma_f32_16x16x32_bf16 v[68:71], v[196:199], v[164:167], v[68:71]\n\t" \
    "ds_read_b64_tr_b16 v[138:139], %[b0] offset:25600\n\t" \
    "s_add_u32 %[t], %[t], 1\n\t" \
    "s_add_u32 %[d], %[lw], %[c]\n\t" \
    "s_add_u32 %[y], %[c], 49152\n\t" \
    "v_mfma_f32_16x16x32_bf16 v[72:75], v[204:207], v[164:167], v[72:75]\n\t" \
    "ds_read_b64_tr_b16 v[144:145], %[b1] offset:24576\n\t" \
    "s_cmp_eq_u32 %[y], 147456\n\t" \
    "s_cselect_b32 %[y], 0, %[y]\n\t" \
    "s_sub_u32 %[z], %[y], %[c]\n\t" \
    "v_mfma_f32_16x16x32_bf16 v[68:71], v[200:203], v[168:171], v[68:71]\n\t" \
    "ds_read_b64_tr_b16 v[146:147], %[b1] offset:25600\n\t" \
    "s_mov_b32 %[c], %[y]\n\t" \
    "s_add_u32 %[x], %[t], 3\n\t" \
    "s_sub_u32 %[y], %[x], %[nt]\n\t" \
    "v_mfma_f32_16x16x32_bf16 v[72:75], v[208:211], v[168:171], v[72:75]\n\t" \
    "s_cmp_lt_u32 %[x], %[nt]\n\t" \
    "s_cselect_b32 %[x], %[x], %[y]\n\t" \
    "s_cselect_b32 m0, %[cb], %[cn]\n\t" \
    "v_mfma_f32_16x16x32_bf16 v[76:79], v[196:199], v[172:175], v[76:79]\n\t" \
    "s_mul_i32 %[y], %[x], %[kb]\n\t" \
    "s_add_u32 %[y], %[y], m0\n\t" \
    "s_lshl_b32 %[x], %[x], 11\n\t" \
    "v_mfma_f32_16x16x32_bf16 v[80:83], v[204:207], v[172:175], v[80:83]\n\t" \
    "s_add_u32 %[x], %[x], %[ca]\n\t" \
    "s_cmp_lt_u32 %[t], %[nt]\n\t" \
    "v_mfma_f32_16x16x32_bf16 v[76:79], v[200:203], v[176:179], v[76:79]\n\t" \
    "v_mfma_f32_16x16x32_bf16 v[80:83], v[208:211], v[176:179], v[80:83]\n\t" \
    "v_add_u32 %[a], %[z], %[a]\n\t" \
    "v_add_u32 %[b0], %[z], %[b0]\n\t" \
    "v_add_u32 %[b1], %[z], %[b1]\n\t" \
    "s_cbranch_scc1 L_%=\n\t" \
    "s_waitcnt lgkmcnt(0)\n\t" \
    "s_nop 7\n\t" \
    : "=&{v[20:23]}"(acc[0][0][0][0]), "=&{v[24:27]}"(acc[0][0][0][1]), "=&{v[28:31]}"(acc[0][0][1][0]), "=&{v[32:35]}"(acc[0][0][1][1]), "=&{v[36:39]}"(acc[0][0][2][0]), "=&{v[40:43]}"(acc[0][0][2][1]), "=&{v[44:47]}"(acc[0][0][3][0]), "=&{v[48:51]}"(acc[0][0][3][1]), "=&{v[52:55]}"(acc[0][1][0][0]), "=&{v[56:59]}"(acc[0][1][0][1]), "=&{v[60:63]}"(acc[0][1][1][0]), "=&{v[64:67]}"(acc[0][1][1][1]), "=&{v[68:71]}"(acc[0][1][2][0]), "=&{v[72:75]}"(acc[0][1][2][1]), "=&{v[76:79]}"(acc[0][1][3][0]), "=&{v[80:83]}"(acc[0][1][3][1]), [a] "+v"(aoff), [b0] "+v"(boff0), [b1] "+v"(boff1), [c] "+s"(cur), [t] "+s"(t), [d] "=&s"(sd), [x] "=&s"(sx), [y] "=&s"(sy), [z] "=&s"(sz) \
    : [va0] "v"(voffA[0]), [va1] "v"(voffA[1]), [vb0] "v"(voffB[0]), [vb1] "v"(voffB[1]), [ra] "s"(ra), [rb] "s"(rb), [lw] "s"(ldsw), [nt] "s"(nt), [kb] "s"(kB), [ca] "s"(cA), [cb] "s"(cB), [cn] "s"(cN) \
    : "memory", "scc", "v84", "v85", "v86", "v87", "v88", "v89", "v90", "v91", "v92", "v93", "v94", "v95", "v96", "v97", "v98", "v99", "v100", "v101", "v102", "v103", "v104", "v105", "v106", "v107", "v108", "v109", "v110", "v111", "v112", "v113", "v114", "v115", "v116", "v117", "v118", "v119", "v120", "v121", "v122", "v123", "v124", "v125", "v126", "v127", "v128", "v129", "v130", "v131", "v132", "v133", "v134", "v135", "v136", "v137", "v138", "v139", "v140", "v141", "v142", "v143", "v144", "v145", "v146", "v147", "v148", "v149", "v150", "v151", "v152", "v153", "v154", "v155", "v156", "v157", "v158", "v159", "v160", "v161", "v162", "v163", "v164", "v165", "v166", "v167", "v168", "v169", "v170", "v171", "v172", "v173", "v174", "v175", "v176", "v177", "v178", "v179", "v180", "v181", "v182", "v183", "v184", "v185", "v186", "v187", "v188", "v189", "v190", "v191", "v192", "v193", "v194", "v195", "v196", "v197", "v198", "v199", "v200", "v201", "v202", "v203", "v204", "v205", "v206", "v207", "v208", "v209", "v210", "v211")
#define AQ_PIN0() asm volatile("" : "+{v[28:35]}"(wr[0][0]), "+{v[36:43]}"(wr[0][1]))
#define AQ_PIN1() asm volatile("" : "+{v[44:51]}"(wr[1][0]), "+{v[52:59]}"(wr[1][1]))
#define AQ_PIN2() asm volatile("" : "+{v[60:67]}"(wr[2][0]), "+{v[68:75]}"(wr[2][1]))
#define AQ_PIN3() asm volatile("" : "+{v[76:83]}"(wr[3][0]), "+{v[84:91]}"(wr[3][1]))
#define AQ_PIN4() asm volatile("" : "+{v[92:99]}"(wr[4][0]), "+{v[100:107]}"(wr[4][1]))
#define AQ_PIN5() asm volatile("" : "+{v[108:115]}"(wr[5][0]), "+{v[116:123]}"(wr[5][1]))
#define AQ_PIN6() asm volatile("" : "+{v[124:131]}"(wr[6][0]), "+{v[132:139]}"(wr[6][1]))
#define AQ_PIN7() asm volatile("" : "+{v[140:147]}"(wr[7][0]), "+{v[148:155]}"(wr[7][1]))
#define AQ_PIN8() asm volatile("" : "+{v[156:163]}"(wr[8][0]), "+{v[164:171]}"(wr[8][1]))
#define AQ_PIN9() asm volatile("" : "+{v[172:179]}"(wr[9][0]), "+{v[180:187]}"(wr[9][1]))
#define AQ_PIN10() asm volatile("" : "+{v[188:195]}"(wr[10][0]), "+{v[196:203]}"(wr[10][1]))
#define AQ_PIN11() asm volatile("" : "+{v[204:211]}"(wr[11][0]), "+{v[212:219]}"(wr[11][1]))
#define AK_PIN0() asm volatile("" : "+{v[20:27]}"(wr[0][0]), "+{v[28:35]}"(wr[0][1]))
#define AK_PIN1() asm volatile("" : "+{v[36:43]}"(wr[1][0]), "+{v[44:51]}"(wr[1][1]))
#define AK_PIN2() asm volatile("" : "+{v[52:59]}"(wr[2][0]), "+{v[60:67]}"(wr[2][1]))
#define AK_PIN3() asm volatile("" : "+{v[68:75]}"(wr[3][0]), "+{v[76:83]}"(wr[3][1]))
#define AK_PIN4() asm volatile("" : "+{v[84:91]}"(wr[4][0]), "+{v[92:99]}"(wr[4][1]))
#define AK_PIN5() asm volatile("" : "+{v[100:107]}"(wr[5][0]), "+{v[108:115]}"(wr[5][1]))
#define AK_PIN6() asm volatile("" : "+{v[116:123]}"(wr[6][0]), "+{v[124:131]}"(wr[6][1]))
#define AK_PIN7() asm volatile("" : "+{v[132:139]}"(wr[7][0]), "+{v[140:147]}"(wr[7][1]))
#define AK_PIN8() asm volatile("" : "+{v[148:155]}"(wr[8][0]), "+{v[156:163]}"(wr[8][1]))
#define AK_PIN9() asm volatile("" : "+{v[164:171]}"(wr[9][0]), "+{v[172:179]}"(wr[9][1]))
#define AK_PIN10() asm volatile("" : "+{v[180:187]}"(wr[10][0]), "+{v[188:195]}"(wr[10][1]))
#define AK_PIN11() asm volatile("" : "+{v[196:203]}"(wr[11][0]), "+{v[204:211]}"(wr[11][1]))
#define V_PIN() asm volatile("" : "+{v[28:35]}"(wv[0]), "+{v[36:43]}"(wv[1]), "+{v[44:51]}"(wv[2]), "+{v[52:59]}"(wv[3]), "+{v[60:67]}"(wv[4]), "+{v[68:75]}"(wv[5]), "+{v[76:83]}"(wv[6]), "+{v[84:91]}"(wv[7]), "+{v[92:99]}"(wv[8]), "+{v[100:107]}"(wv[9]), "+{v[108:115]}"(wv[10]), "+{v[116:123]}"(wv[11]), "+{v[124:131]}"(wv[12]), "+{v[132:139]}"(wv[13]), "+{v[140:147]}"(wv[14]), "+{v[148:155]}"(wv[15]))
#define WOV_PIN0() asm volatile("" : "+{v[156:163]}"(wo[0][0][0]), "+{v[164:171]}"(wo[0][0][1]), "+{v[172:179]}"(wo[0][1][0]), "+{v[180:187]}"(wo[0][1][1]))
#define WOV_PIN1() asm volatile("" : "+{v[188:195]}"(wo[1][0][0]), "+{v[196:203]}"(wo[1][0][1]), "+{v[204:211]}"(wo[1][1][0]), "+{v[212:219]}"(wo[1][1][1]))
#define WOW_PIN0() asm volatile("" : "+{v[28:35]}"(wo[0][0][0]), "+{v[36:43]}"(wo[0][0][1]), "+{v[44:51]}"(wo[0][1][0]), "+{v[52:59]}"(wo[0][1][1]))
#define WOW_PIN1() asm volatile("" : "+{v[60:67]}"(wo[1][0][0]), "+{v[68:75]}"(wo[1][0][1]), "+{v[76:83]}"(wo[1][1][0]), "+{v[84:91]}"(wo[1][1][1]))
#define WOW_PIN2() asm volatile("" : "+{v[92:99]}"(wo[2][0][0]), "+{v[100:107]}"(wo[2][0][1]), "+{v[108:115]}"(wo[2][1][0]), "+{v[116:123]}"(wo[2][1][1]))
#define WOW_PIN3() asm volatile("" : "+{v[124:131]}"(wo[3][0][0]), "+{v[132:139]}"(wo[3][0][1]), "+{v[140:147]}"(wo[3][1][0]), "+{v[148:155]}"(wo[3][1][1]))
#define WOW_PIN4() asm volatile("" : "+{v[156:163]}"(wo[4][0][0]), "+{v[164:171]}"(wo[4][0][1]), "+{v[172:179]}"(wo[4][1][0]), "+{v[180:187]}"(wo[4][1][1]))
#define WOW_PIN5() asm volatile("" : "+{v[188:195]}"(wo[5][0][0]), "+{v[196:203]}"(wo[5][0][1]), "+{v[204:211]}"(wo[5][1][0]), "+{v[212:219]}"(wo[5][1][1]))
#define A_PIN(QW, i) do { switch (i) { case 0: if (QW) AQ_PIN0(); else AK_PIN0(); break; case 1: if (QW) AQ_PIN1(); else AK_PIN1(); break; case 2: if (QW) AQ_PIN2(); else AK_PIN2(); break; case 3: if (QW) AQ_PIN3(); else AK_PIN3(); break; case 4: if (QW) AQ_PIN4(); else AK_PIN4(); break; case 5: if (QW) AQ_PIN5(); else AK_PIN5(); break; case 6: if (QW) AQ_PIN6(); else AK_PIN6(); break; case 7: if (QW) AQ_PIN7(); else AK_PIN7(); break; case 8: if (QW) AQ_PIN8(); else AK_PIN8(); break; case 9: if (QW) AQ_PIN9(); else AK_PIN9(); break; case 10: if (QW) AQ_PIN10(); else AK_PIN10(); break; case 11: if (QW) AQ_PIN11(); else AK_PIN11(); break; } } while (0)
#define WO_PIN(VW, i) do { switch (i) { case 0: if (VW) WOV_PIN0(); else WOW_PIN0(); break; case 1: if (VW) WOV_PIN1(); else WOW_PIN1(); break; case 2: if (!VW) WOW_PIN2(); break; case 3: if (!VW) WOW_PIN3(); break; case 4: if (!VW) WOW_PIN4(); break; case 5: if (!VW) WOW_PIN5(); break; } } while (0)

__device__ __forceinline__ int lds_byte(int r, int c) {
  const int st = (r >> 4) * 2 + (c >> 5), rr = r & 15, cc = c & 31, ob = rr * 64 + cc * 2;
  return st * 1024 + (ob ^ (((ob >> 9) & 1) << 5));
}
#define G_DvoffB dmaw
#define G_ST1(bufoff, r, so, voff, i) G_D##voff(r, (voff)[i], so, ldsw + (bufoff) + (i) * 8192)
#define G_DvoffA dmas
#define G_STAGE(bufoff, so, voff) do { G_ST1(bufoff, G_R##voff, so, voff, 0); G_ST1(bufoff, G_R##voff, so, voff, 1); } while (0)
#define G_RvoffA ra
#define G_RvoffB rb
#define G_WAIT_V(n) asm volatile("s_waitcnt vmcnt(%0)" :: "n"(n) : "memory")
__device__ __forceinline__ void gemm_h(LAS unsigned char* lds, rsrc_t ra, rsrc_t rb, unsigned cA, unsigned cB, int lda, int ldb, int nt,
                                       bool wide, bool slow, unsigned cN, bool cont, unsigned cur, f32x4 (&acc)[1][2][4][2]) {
  const int wid = __builtin_amdgcn_readfirstlane(threadIdx.x >> 6), lane = opq(0), tid = wid * 64 + lane;
  const int wr = wid >> 2, wc = wid & 3, fr = lane & 15, fq = lane >> 4;
  unsigned voffA[2], voffB[2];
  #pragma unroll
  for (int i = 0; i < 2; ++i) {
    voffA[i] = (unsigned)(((wid >> 1) + 4 * i) * 16 * lda + (wid & 1) * 1024 + lane * 16);
    const int b = tid * 16 + i * 8192, r = b >> 8, pos = (b >> 4) & 15;
    const int c = pos ^ (((r & 3) << 1) | (((r >> 3) & 1) << 3));
    voffB[i] = (unsigned)(r * ldb + c * 16);
  }
  const unsigned kB = 64 * ldb;
  const unsigned ldsw = (unsigned)(size_t)lds + (unsigned)wid * 1024u;
  const int q = (lane & 15) >> 2, p = lane & 3;
  unsigned aoff = (unsigned)(size_t)lds + cur + 32768 + lds_byte(wr * 64 + fr, fq * 8);
  unsigned boff0 = (unsigned)(size_t)lds + cur + (8 * fq + q) * 256 + (((wc * 4 + (p >> 1)) ^ ((q << 1) | ((fq & 1) << 3))) << 4) + 8 * (p & 1), boff1 = boff0 ^ 32;
#define H_STAGE(so, t) do { G_STAGE(so, cB + (t) * kB, voffB); G_STAGE((so) + 32768, cA + (t) * 2048, voffA); if (wide) G_STAGE((so) + 16384, cB + (t) * kB + 256, voffB); } while (0)
  if (!cont) { H_STAGE(0, 0); H_STAGE(49152, 1); H_STAGE(98304, 2); }
  unsigned t = 0, sd, sx, sy, sz;
  if (slow) H_LOOP2S(); else if (wide) H_LOOP2(); else H_LOOP1();
}

__device__ __forceinline__ u32x4 epi_pack(const f32x4 a0, const f32x4 a1) {
  const unsigned x0 = pack2(a0[0], a0[1]), x1 = pack2(a0[2], a0[3]);
  const unsigned y0 = pack2(a1[0], a1[1]), y1 = pack2(a1[2], a1[3]);
  auto s0 = __builtin_amdgcn_permlane16_swap(x0, y0, false, false);
  auto s1 = __builtin_amdgcn_permlane16_swap(x1, y1, false, false);
  return (u32x4){s0[0], s1[0], s0[1], s1[1]};
}
__device__ __forceinline__ unsigned gelu2(unsigned w) {
  const f32x2 x = {lo16(w), hi16(w)};
  const float c0 = -2.f * 1.4426950408889634f * 0.7978845608028654f, c1 = c0 * 0.044715f;
  const f32x2 a = x * (x * x * c1 + c0);
  const f32x2 d = (f32x2){__builtin_amdgcn_exp2f(a[0]), __builtin_amdgcn_exp2f(a[1])} + 1.f;
  const f32x2 y = x * (f32x2){__builtin_amdgcn_rcpf(d[0]), __builtin_amdgcn_rcpf(d[1])};
  return pack2(y[0], y[1]);
}
__device__ __forceinline__ float silu_f(float b) { return b * __builtin_amdgcn_rcpf(1.f + __expf(-b)); }

__device__ __forceinline__ unsigned blk_in(int r, int c) { const int ob = r * 64 + c * 2; return ob ^ (((ob >> 9) & 1) << 5); }
__device__ __forceinline__ unsigned xn_blk(int rb, int cb) { return (unsigned)(((rb >> 1) * 32 + cb) * 2 + (rb & 1)) * 1024u; }

template <int EPI>
__device__ __forceinline__ void epi_rows(const f32x4 (&acc)[1][2][4][2], rsrc_t rs, unsigned obase, int ldc, int pm2, int pn2, int sh, bool wide, int wr, int wc, int ln) {
  const int fq = ln >> 4, cb = 16 * (fq & 1) + 8 * (fq >> 1), fr = (ln & 15) + sh;
  const unsigned lo = (unsigned)((fr >> 4) * 16 * ldc) + blk_in(fr & 15, cb);
  const unsigned ob = (unsigned)(pm2 * 128 + 64 * wr) * (unsigned)ldc + (unsigned)(pn2 * 4 + wc) * 1024u + lo;
  u32x4 r[8];
  if (EPI >= 2) {
    #pragma unroll
    for (int i = 0; i < 8; ++i)
      r[i] = ld16(rs, EPI == 2 ? WS_XN + xn_blk(pm2 * 8 + 4 * wr + (i & 3), pn2 * 4 + 4 * (i >> 2) + wc) + lo : WS_X1 + ob + (i & 3) * 32768 + (i >> 2) * 4096);
  }
  #pragma unroll
  for (int i = 0; i < 8; ++i) {
    if (i == 4 && !wide) break;
    u32x4 w = epi_pack(acc[0][i >> 2][i & 3][0], acc[0][i >> 2][i & 3][1]);
    #pragma unroll
    for (int k = 0; k < 4; ++k) {
      w[k] = EPI < 2 ? gelu2(w[k]) : pack2(lo16(r[i][k]) + lo16(w[k]), hi16(r[i][k]) + hi16(w[k]));
      if (EPI == 3) w[k] = pack2(silu_f(lo16(w[k])), silu_f(hi16(w[k])));
    }
    st16(rs, obase + ob + (unsigned)(16 * (i & 3) * ldc) + (i >> 2) * 4096, w);
  }
}

__device__ __forceinline__ float row_sum16(float v) {
  v += __uint_as_float(__builtin_amdgcn_update_dpp(0, (int)__float_as_uint(v), 0x128, 0xf, 0xf, false));
  v += __uint_as_float(__builtin_amdgcn_update_dpp(0, (int)__float_as_uint(v), 0x124, 0xf, 0xf, false));
  v += __uint_as_float(__builtin_amdgcn_update_dpp(0, (int)__float_as_uint(v), 0x122, 0xf, 0xf, false));
  v += __uint_as_float(__builtin_amdgcn_update_dpp(0, (int)__float_as_uint(v), 0x121, 0xf, 0xf, false));
  return v;
}
__device__ __forceinline__ void ln_rows(u32x4 (&a)[8]) {
  f32x2 v[32];
  #pragma unroll
  for (int i = 0; i < 32; ++i) v[i] = (f32x2){lo16(a[i >> 2][i & 3]), hi16(a[i >> 2][i & 3])};
  f32x2 s[4] = {v[0], v[1], v[2], v[3]};
  #pragma unroll
  for (int i = 4; i < 32; ++i) s[i & 3] += v[i];
  s[0] = (s[0] + s[1]) + (s[2] + s[3]);
  const float mean = row_sum16(s[0][0] + s[0][1]) * (1.f / DM);
  #pragma unroll
  for (int i = 0; i < 32; ++i) { const f32x2 d = v[i] - mean; s[i & 3] = i < 4 ? d * d : s[i & 3] + d * d; }
  s[0] = (s[0] + s[1]) + (s[2] + s[3]);
  const float var = row_sum16(s[0][0] + s[0][1]) * (1.f / DM);
  const float mb = rb1(mean), vb = rb1(var);
  const float rden = __builtin_amdgcn_rcpf(rb1(__builtin_amdgcn_sqrtf(rb1(vb + 1e-5f))));
  #pragma unroll
  for (int i = 0; i < 32; ++i) {
    const f32x2 d = v[i] - mb;
    const unsigned w = pack2(d[0], d[1]);
    const f32x2 y = (f32x2){lo16(w), hi16(w)} * rden;
    a[i >> 2][i & 3] = pack2(y[0], y[1]);
  }
}

template <int N>
__device__ __forceinline__ void fin_sum(rsrc_t rs, unsigned po, u32x4* v) {
  u32x4 pv[N][4];
  #pragma unroll
  for (int i = 0; i < 4 * N; ++i) pv[i >> 2][i & 3] = ld16(rs, po + (i >> 2) * 8192 + (i & 3) * 65536);
  #pragma unroll
  for (int c = 0; c < N; ++c) {
    f32x4 a0, a1;
    #pragma unroll
    for (int k = 0; k < 4; ++k) {
      a0[k] = (lo16(pv[c][0][k]) + lo16(pv[c][1][k])) + (lo16(pv[c][2][k]) + lo16(pv[c][3][k]));
      a1[k] = (hi16(pv[c][0][k]) + hi16(pv[c][1][k])) + (hi16(pv[c][2][k]) + hi16(pv[c][3][k]));
    }
    v[c] = epi_pack(a0, a1);
  }
}
__device__ __forceinline__ void fin_out(const Ptrs& P, unsigned eo, u32x4 v, u32x4 gv, u32x4 xv) {
  #pragma unroll
  for (int k = 0; k < 4; ++k) {
    v[k] = pack2(lo16(v[k]) * lo16(gv[k]), hi16(v[k]) * hi16(gv[k]));
    v[k] = pack2(lo16(xv[k]) + lo16(v[k]), hi16(xv[k]) + hi16(v[k]));
  }
  *(u32x4*)((char*)P.out + eo) = v;
}

#define STG 56
#define SLOWC 6
__device__ __forceinline__ void mlp_role(LAS unsigned char* lds, const Ptrs& P, int hf, int slot, int wid) {
  const int wr = wid >> 2, wc = wid & 3;
  const int p2 = hf * 8 + (slot >> 2), pn = slot & 3;
  char* ws = P.ws;
  const rsrc_t rs = WS_RSRC(P);
  const u16* const p1 = P.w1; const u16* const p2w = P.w2; const u16* const p3 = P.w3; const u16* const p4 = P.w4;
#define M_DONE(c) do { VMWAIT(); BARRIER(); if (wid == 0) cnt_add(ws, c, opq(0)); } while (0)
  BARRIER();
  #pragma unroll
  for (int i = 0; i < 4; ++i) touch(i < 2 ? p3 : p4, DM * FF * 2, (((i & 1) * 64 + hf * 32 + slot) * 512 + wid * 64 + opq(0)) * 128, lds);
  if (hf == 0) touch(p2w, DM * DM * 2, (slot * 512 + wid * 64 + opq(0)) * 128, lds);
  if (STG && hf == 0) { VMWAIT(); for (int i = 0; i < 7 - (slot >> 2); ++i) __builtin_amdgcn_s_sleep(STG); }
  #pragma unroll 1
  for (int code = 0; code < 7; ++code) {
    int ln = opq(0);
    if (wid == 0) {
      if (code == 0) flag_wait(P, F_LN1(p2 * 4 + (ln & 3)), 0, ln);
      else if (code < 3 || code == 6) {
        const unsigned co = CNT_OFF + (code == 1 ? C_W1(min(max(p2 - 1 + min(ln, 2), 0), 15)) : code == 2 ? C_LN(p2) : C_W3(p2)) * 128;
        for (unsigned spins = 0; __any(ld4(rs, co) < 4u);) {
          __builtin_amdgcn_s_sleep(2);
          if (spin_fail(spins, P, ln)) break;
        }
      }
    }
    if (code < 3 || code == 6) BARRIER();
    TLW(3 * code);
    const bool w3 = code >= 2 && code < 6, wide = code != 5;
    const int lda = code == 6 ? 7168 : 2048, ldb = w3 ? 7168 : 2048, c2 = w3 ? (code - 2) * 8 + (wide ? 2 * pn : pn) : 2 * pn;
    const rsrc_t rb = __builtin_amdgcn_make_buffer_rsrc((void*)(code == 0 ? p1 : code == 1 ? p2w : w3 ? p3 : p4), 0, code < 2 ? DM * DM * 2 : DM * FF * 2, RSRC_W3);
    const unsigned cA = (code == 0 ? WS_XN2 : code == 1 ? WS_H1 : w3 ? WS_X1N : WS_H3) + p2 * 128 * lda, cB = c2 * 256;
    f32x4 acq[1][2][4][2];
    gemm_h(lds, rs, rb, cA, cB, lda, ldb, code == 6 ? 56 : 16, wide, hf == 0 && wide && code >= SLOWC, code == 4 ? (24 + pn) * 256 : w3 && wide ? cB + 2048 : cB, w3 && code > 2, code == 3 || code == 4 ? (code - 2) * 49152 : 0, acq);
    TLW(3 * code + 1);
    ln = opq(0);
    if (code == 1) {
      epi_rows<2>(acq, rs, WS_X1, 2048, p2, c2, 0, true, wr, wc, ln);
      M_DONE(C_W2(p2));
      if (wid == 0) cnt_wait(P, C_W2(p2), 4, ln);
      BARRIER();
      const unsigned ro = (unsigned)(p2 * 8 + pn * 2 + wr) * 32768u + ((ln >> 2) & 3) * 1024 + blk_in(wc * 4 + (ln >> 4), (ln & 3) * 8);
      u32x4 v[8];
      #pragma unroll
      for (int i = 0; i < 8; ++i) v[i] = ld16(rs, WS_X1 + ro + i * 4096);
      ln_rows(v);
      #pragma unroll
      for (int i = 0; i < 8; ++i) st16(rs, WS_X1N + ro + i * 4096, v[i]);
      M_DONE(C_LN(p2));
    } else if (code == 6) {
      epi_rows<3>(acq, rs, WS_G, 2048, p2, c2, 0, true, wr, wc, ln);
      M_DONE(C_W4(p2));
    } else {
      VMWAIT();
      epi_rows<1>(acq, rs, code ? WS_H3 : WS_H1, ldb, p2, c2, code ? 0 : (pn & 1) ? -1 - (pn >> 1) : 1 + (pn >> 1), wide, wr, wc, ln);
      if (code == 0 && wid == 0) setup_wait(P, ln);
      if (code == 0 || code == 5) M_DONE(code ? C_W3(p2) : C_W1(p2));
    }
    TLW(3 * code + 2);
  }
}

#define FPC 40
#define FINE 52
__device__ __forceinline__ void fin_role(LAS unsigned char* lds, const Ptrs& P, int wid) {
  const int wr = wid >> 2, wc = wid & 3;
  const rsrc_t rs = WS_RSRC(P);
  volatile LAS int* tk = (volatile LAS int*)(lds + 147456);
  unsigned tks = 0;
#define POP() do { if (wid == 0 && opq(0) == 0) tks = __hip_atomic_fetch_add((gu32*)(P.ws + CNT_OFF + C_HEAD * 128), 1u, RLX_AGENT); } while (0)
  POP();
  for (int round = 0;; ++round) {
    if (wid == 0 && opq(0) == 0) tk[round & 1] = tks;
    LBARRIER();
    const int u = __builtin_amdgcn_readfirstlane(tk[round & 1]);
    if (u >= 2 * FINE + 8 * (XS3 - FINE)) break;
    const bool big = u < 2 * FINE;
    const int sg = big ? u >> 1 : FINE + ((u - 2 * FINE) >> 3), cc0 = big ? (u & 1) * 4 : (u - 2 * FINE) & 7;
    const int ln = opq(0), fq = ln >> 4, cb = 16 * (fq & 1) + 8 * (fq >> 1);
    const unsigned eo = (unsigned)(sg * 32 + wr * 16 + (ln & 15)) * 2048u + (unsigned)(cc0 * 128 + wc * 32 + cb) * 2u;
    const unsigned go = WS_G + (unsigned)(sg * 2 + wr) * 32768u + (cc0 * 4 + wc) * 1024 + blk_in(ln & 15, cb);
    const unsigned po = WS_PT + (unsigned)sg * 262144u + cc0 * 8192 + wr * 4096 + wc * 1024 + ln * 16;
    const int xg = sg < XS0 || sg >= XS2 ? 2 : sg >= XS1, want = sg + 1 - (sg < XS0 ? 0 : xg == 2 ? XS2 - XS0 : xg ? XS1 : XS0);
    u32x4 v[4], gv[4], xv[4];
    if (big) {
      if (wid == 2) flag_wait(P, F_PRG(5 + xg, ln & 31), want, ln);
      BARRIER();
      fin_sum<2>(rs, po, v);
      asm volatile("" : "+v"(v[0]), "+v"(v[1]) :: "memory");
      DELAY(FPC);
      #pragma unroll
      for (int c = 0; c < 4; ++c) xv[c] = __builtin_nontemporal_load((const u32x4*)((const char*)P.x + eo + c * 256));
      fin_sum<2>(rs, po + 16384, v + 2);
      if (wid == 1) cnt_wait(P, C_W4(sg >> 2), 4, ln);
      BARRIER();
      #pragma unroll
      for (int c = 0; c < 4; ++c) gv[c] = ld16(rs, go + c * 4096);
      #pragma unroll
      for (int c = 0; c < 4; ++c) fin_out(P, eo + c * 256, v[c], gv[c], xv[c]);
    } else {
      if (wid == 1) cnt_wait(P, C_W4(sg >> 2), 4, ln);
      BARRIER();
      gv[0] = ld16(rs, go);
      xv[0] = __builtin_nontemporal_load((const u32x4*)((const char*)P.x + eo));
      if (wid == 2) flag_wait<3>(P, F_PRG(5 + xg, (ln & 3) * 8 + cc0), want, ln);
      BARRIER();
      fin_sum<1>(rs, po, v);
      fin_out(P, eo, v[0], gv[0], xv[0]);
    }
    POP();
  }
}

#define DMA_A dman
#define DMA_V dman
#define DMA_O dmaw
#define CADM 1

#define RTC(id)
#define DCA 3
#define DCB 12
#define DCU 8
#define DL1 -1
#define DL2 7
#define DLB 9
#define SN0 65
#define MAP 0
#define HELP 256
__device__ __forceinline__ void track(int& est, int d, int gain) { const int e = d - (est >> 4); est += e > 44 ? DCB * 2 : e * (e > 0 ? max(gain, DCU) : gain); }
__device__ __forceinline__ unsigned rfl(unsigned v) { unsigned r; asm volatile("s_nop 0\n\tv_readfirstlane_b32 %0, %1\n\ts_nop 4" : "=s"(r) : "v"(v)); return r; }
__device__ __forceinline__ void plan_wait(rsrc_t rs, unsigned fo, int s, int& lim, unsigned& tm, unsigned& d) {
  while ((d & 255u) != (unsigned)s && (lim -= DEC1) > 0) {
    __builtin_amdgcn_s_sleep(1);
    const u32x2 v = __builtin_amdgcn_raw_buffer_load_b64(rs, 0, fo, AUX_SC1);
    tm = rfl(v.x); d = rfl(v.y);
  }
  const unsigned now = (unsigned)__builtin_amdgcn_s_memrealtime() * 4u;
  tm = now + min((int)(tm - now), 2000);
  d = min(d >> 8, 1000u);
}
#define SPIN_BUDGET (1 << 21)

__device__ __forceinline__ unsigned rec_go(LAS unsigned char* w, const Ptrs& P, u32x2 f, int wid, int lane) {
  if (wid >= 6) {
    if (__any(f.y != P.tag)) f.x = flag_wait(P, wid == 6 ? F_LN0(lane) : F_CAL(lane), 0, lane);
    if (wid == 7) { const unsigned m = near_half(f.x); if (lane == 0) *(volatile LAS unsigned*)w = m; }
  }
  LBARRIER();
  return __builtin_amdgcn_readfirstlane(*(volatile LAS unsigned*)w);
}

#define SLOT(i) ((i) * 16384)
#define LA_MEM SLOT(8)
#define LA_Q SLOT(9)
#define LA_K (SLOT(9) + 2048)
#define LA_P (SLOT(9) + 4096)
#define LA_KS (SLOT(9) + 6144)
#define LA_VM LA_MEM
#define LA_KX (SLOT(9) + 8192)

template <bool QW>
__device__ __forceinline__ void a_wave(LAS unsigned char* lds, Ptrs& P, int h, int wid) {
  char* ws = P.ws;
  const int lane = opq(0), tid = wid * 64 + lane;
  const int j = wid & 3, g = lane >> 4, q4 = (lane & 15) >> 2, p4 = lane & 3;
  const rsrc_t rs = WS_RSRC(P);
  const rsrc_t rq = __builtin_amdgcn_make_buffer_rsrc((void*)P.Wq, 0, HD * DM * 2, RSRC_W3);
  const rsrc_t rk = __builtin_amdgcn_make_buffer_rsrc((void*)P.Wk, 0, HD * DM * 2, RSRC_W3);
  const unsigned lbase = (unsigned)(size_t)lds;
  int lim = SPIN_BUDGET;
  u32x8 wr[12][2];
  const int trow = 16 * (g >> 1) + 4 * (g & 1) + 8 * (q4 >> 1) + (q4 & 1);
  const int tsw = ((q4 & 1) | ((g & 1) << 1) | ((q4 >> 1) << 2)) << 1;
  const int toff = trow * 256 + (((4 * j + (p4 >> 1)) ^ tsw) << 4) + 8 * (p4 & 1);
  {
    unsigned dv[2];
    #pragma unroll
    for (int x = 0; x < 2; ++x) {
      const int row = 8 * wid + 4 * x + (lane >> 4);
      const int sw = ((row & 1) | (((row >> 2) & 1) << 1) | (((row >> 3) & 1) << 2)) << 1;
      dv[x] = (unsigned)(row * (HD * 2) + h * 256 + (((lane & 15) ^ sw) << 4));
    }
#define A_GRP(n) do { _Pragma("unroll") for (int i = 0; i < 8; ++i) \
      DMA_A(((n) & 1) ? rk : rq, dv[i & 1], (4 * (((n) & 1) ? ((n) >> 1) ^ ((n) >> 2) : (((n) >> 1) + 1) & 3) + (i >> 1)) * 64 * HD * 2, lbase + SLOT(4 * ((n) & 1) + (i >> 1)) + wid * 2048 + (i & 1) * 1024); } while (0)
    A_GRP(0); A_GRP(1);
    #pragma unroll
    for (int n = 0; n < 6; ++n) {
      VMW(8);
      BARRIER();
      if ((n & 1) == (QW ? 0 : 1)) {
        #pragma unroll
        for (int b = 0; b < 4; ++b) {
          LAS unsigned char* t = lds + SLOT(QW ? 0 : 4) + ((b ^ j) << 14);
          wr[4 * (n >> 1) + b][0] = rd_frag<8192, 512>(t + toff);
          wr[4 * (n >> 1) + b][1] = rd_frag<8192, 512>(t + (toff ^ 32));
        }
        #pragma unroll
        for (int b = 0; b < 4; ++b) A_PIN(QW, 4 * (n >> 1) + b);
      }
      LBARRIER();
      A_GRP(n + 2);
    }
  }
  const unsigned cb = comm_base(ws);
  u32x2 f0 = {0u, 0u};
  #pragma unroll
  for (int rd = 0; rd < 2; ++rd) {
    if (rd == 0) VMW(8);
    else {
      if (wid >= 6) f0 = ld8(rs, FLG_OFF + (wid == 6 ? F_LN0(lane) : F_CAL(lane)) * 128);
      const unsigned fv = wid == 3 ? FLG_OFF + F_PRG(min((lane >> 1) & 3, 2), h) * 128 + (lane & 1) * 4 : WS_QF + j * (16u << 20) + h * 8192 + lane * 128;
      if (QW) asm volatile("s_mov_b32 m0, %2\n\ts_nop 0\n\tbuffer_load_dword %0, %1, 0 offen sc1 lds" :: "v"(fv), "s"(rs), "s"(lbase + LA_P + (wid == 3 ? 640 : 1024)) : "memory");
      get_tag(P);
      VMWAIT();
    }
    BARRIER();
    u32x8 f[2][2];
    #pragma unroll
    for (int i = 0; i < 2; ++i) {
      f[i][0] = rd_frag<8192, 512>(lds + SLOT((QW ? 0 : 2) + 4 * rd + i) + toff);
      f[i][1] = rd_frag<8192, 512>(lds + SLOT((QW ? 0 : 2) + 4 * rd + i) + (toff ^ 32));
    }
    asm volatile("" : "+v"(f[0][0]), "+v"(f[0][1]), "+v"(f[1][0]), "+v"(f[1][1]));
    LBARRIER();
    #pragma unroll
    for (int i = 0; i < 2; ++i)
      #pragma unroll
      for (int t = 0; t < 2; ++t) {
        LAS unsigned char* p = lds + SLOT((QW ? 0 : 2) + 4 * rd + i) + j * 4096 + t * 2048 + lane * 16;
        *(LAS u32x4*)p = (u32x4){f[i][t][0], f[i][t][1], f[i][t][2], f[i][t][3]};
        *(LAS u32x4*)(p + 1024) = (u32x4){f[i][t][4], f[i][t][5], f[i][t][6], f[i][t][7]};
      }
  }
  const unsigned mask = rec_go(lds + LA_P + 128, P, f0, wid, lane);
  const int idx = (lane & 1) ? 0xEEEE : 0x4444;
  const unsigned ab_a = lbase + LA_MEM + lane * 16, am_a = ab_a + j * 1024;
  const unsigned t0_a = lbase + SLOT((QW ? 0 : 4) + j) + j * 4096 + lane * 16;
  const unsigned iw_a = lbase + (QW ? LA_Q : LA_K) + j * 512 + q4 * 128 + g * 32 + p4 * 4;
  const unsigned sw_a = lbase + LA_KS + (j >> 1) * 1024 + ((j & 1) * 2 + (q4 >> 1)) * 256 + (4 * g + (p4 & 1)) * 16 + ((q4 & 1) * 2 + (p4 >> 1)) * 4;
  const unsigned qr_a = lbase + LA_Q + g * 128 + (lane & 7) * 16;
  const unsigned kr_a = lbase + LA_K + g * 128 + (2 * q4 + (lane & 1)) * 16;
  const unsigned fr_a = lbase + LA_P + p4 * 4;
  if (tid < 12) *(LAS unsigned*)(lds + LA_P + tid * 4) = 0u;
  const unsigned vo_v = VB_AT(h) + j * 512 + lane * 4, md = lbase + LA_VM - VB_AT(h);
  const unsigned oo_v = OB_AT((lane >> 2) & 1, h) + (j * 16 + (g >> 1) * 8 + p4 * 2 + (g & 1)) * 16;
  const unsigned long long onej = 1ull << j;
  const unsigned lkx = lbase + LA_KX + j * 2048;
  u32x8 oxr = {};
  const unsigned xo_v = (lane >> 5) * 2048 + (lane & 31) * 16, vl_a = lbase + LA_VM + (j & 1) * 1024 + lane * 4;
  const int dq = 16 * (j >> 1) + (lane & 15) - 4 * g;
  const unsigned fb = step_base(cb, mask, 0);
  unsigned tgt = (unsigned)__builtin_amdgcn_s_memrealtime() * 4u;
  unsigned long long tp = 0;
  unsigned pt = 0, pd = 0;
  const unsigned long long fmask = __builtin_amdgcn_readfirstlane((16u >> wid) & 1u);
  int known = 0;
  __builtin_amdgcn_s_setprio(QW ? 2 : 1);
  LBARRIER();
  report(P, wid);
  TL(72);
  for (int s = 0; s < NSEG; ++s) {
    const unsigned sb = step_base(cb, mask, s);
    const unsigned sbm = sb - (lbase + LA_MEM);
        const unsigned fo = (unsigned)(s * NH + h) * 8192u + j * 2048 - (lbase + LA_MEM);
    GSDECL;
    RTC(h);
    lim = __builtin_amdgcn_readfirstlane(lim);
    if (QW) {
      if (wid == 3 && s > 0) {
        if (opq(0) == 0) __builtin_amdgcn_raw_buffer_store_b32((unsigned)tp * 4u - tgt, rs, 0, fb + FL_O(h), AUX_SC1);
        plan_wait(rs, fb + FL_T(s), s, lim, pt, pd);
        tgt = pt;
      }
      if (s >= known) {
        const u32x2 v = *(volatile LAS u32x2*)(lds + LA_P + 640 + (opq(0) & 3) * 8);
        if (!__any(v.y != P.tag)) known = rfl(min(min(__builtin_amdgcn_readlane(v.x, 0), __builtin_amdgcn_readlane(v.x, 1)), __builtin_amdgcn_readlane(v.x, 2)));
      }
      kv_wait(P, s, h, known, opq(0));
      const unsigned kxo = WS_KF + fo, vxo = WS_VF + fo;
      dmas(rs, ab_a, kxo, lkx);
      dmas(rs, ab_a, kxo + 1024, lkx + 1024);
      dmas(rs, ab_a, WS_QF + fo, lbase + LA_P + 1024);
      dmas(rs, ab_a, WS_QF + fo + 1024, lbase + LA_P + 1024);
      if (wid == 2 && s + 1 >= known) {
        const int ln = opq(0);
        const unsigned fv = FLG_OFF + F_PRG(min((ln >> 1) & 3, 2), h) * 128 + (ln & 1) * 4;
        asm volatile("s_mov_b32 m0, %2\n\ts_nop 0\n\tbuffer_load_dword %0, %1, 0 offen sc1 lds" :: "v"(fv), "s"(rs), "s"(lbase + LA_P + 640) : "memory");
      }
      unsigned rg = 0x3c000000u;
      if (s == NSEG - 1) { A_LAST_Q(); break; }
      A_MAIN_Q();
      TL(s);
      GSFLUSH(0, 24 + wid); GSFLUSH(1, 4 + wid); GSFLUSH(2, 12 + j); GSFLUSH(3, 16 + j); GSFLUSH(4, 20 + j);
      if (s + PD < NSEG && !(opq(0) & 8)) __builtin_amdgcn_raw_buffer_store_b128(poison4(), rs, oo_v, step_base(cb, mask, s + PD), AUX_SC1);
    } else {
      const unsigned qxo = WS_QF + fo - (j & 1) * 2048, oxl = h * 8192 + (j & 1) * 4096 + (j >> 1) * 1024;
      const unsigned oxo = (s == 0 || s == NSEG - 1 ? WS_DUM : WS_OX + (unsigned)(s - 1) * 262144u) + oxl;
      A_MAIN_K();
      if (s >= NSEG - 2) {
        __builtin_amdgcn_raw_buffer_store_b128((u32x4){oxr[0], oxr[1], oxr[2], oxr[3]}, rs, xo_v, WS_OX + (unsigned)s * 262144u + oxl, AUX_SC1);
        __builtin_amdgcn_raw_buffer_store_b128((u32x4){oxr[4], oxr[5], oxr[6], oxr[7]}, rs, xo_v + 512, WS_OX + (unsigned)s * 262144u + oxl, AUX_SC1);
      }
      if (wid == 7) GSFLUSH(0, 28);
      GSFLUSH(1, 4 + wid);
    }
  }
  VMWAIT();
  BARRIER();
  if (wid == 4) flag_set(P, F_PRG(4, h), NSEG, opq(0));
}

#define TSL 4
#define LB_MEM 0
#define LB_RED 16384
template <bool VW>
__device__ __forceinline__ void b_wave(LAS unsigned char* lds, Ptrs& P, int r, int c, int wid, int bs, int xg) {
  constexpr int NHD = VW ? 2 : 6;
  char* ws = P.ws;
  const int lane = opq(0), tid = wid * 64 + lane;
  const int j = wid & 3, g = lane >> 4, q4 = (lane & 15) >> 2, p4 = lane & 3;
  const int b = r * 32 + c;
  const rsrc_t rs = WS_RSRC(P);
  const rsrc_t rv = __builtin_amdgcn_make_buffer_rsrc((void*)P.Wv, 0, HD * DM * 2, RSRC_W3);
  const rsrc_t ro = __builtin_amdgcn_make_buffer_rsrc((void*)P.Wo, 0, HD * DM * 2, RSRC_W3);
  const unsigned lbase = (unsigned)(size_t)lds;
  int lim = SPIN_BUDGET;
  const int h0 = ((VW ? 2 * j : 8 + 6 * j) + 5 * c) & 31;
  u32x8 wv[16], wo[6][2][2];
  {
    if ((wid & 3) < 2 + MAP && xg < 2) {
      const unsigned nk = 8 + 2 * xg, M = (bs * (2 + MAP) + (wid & 3)) * 64 + lane, row = M / nk, k = M - row * nk;
      if (row < HELP) touch(VW ? P.Wq : P.Wk, HD * DM * 2, (row < 256 ? row + (VW ? 512 : 256) : row + 512) * (HD * 2) + min(xg * 4096 + (k >> 1) * 1024 + 256, 7936u) + (k & 1) * 128, lds);
    }
    const int nl = MAP ? (xg == 2 ? 8 : 4) : xg == 2 ? 6 : 5;
    #pragma unroll
    for (int i = 0; i < 2; ++i) {
      const unsigned L = bs * 1024 + wid * 128 + i * 64 + lane, row = L / nl;
      touch(P.Wo, HD * DM * 2, row * (DM * 2) + (c - (bs >> 1)) * 64 + (L - row * nl) * 128, lds);
    }
    VMWAIT();
    BARRIER();
    const int pos = lane >> 2;
    const int prow = (pos & 9) | ((pos & 2) << 1) | ((pos & 4) >> 1);
    const unsigned dv0 = (unsigned)(prow * (DM * 2) + c * 64 + ((lane & 3) << 4));
    const unsigned dv1 = (unsigned)(prow * (DM * 2) + c * 64 + (((lane & 3) ^ 2) << 4));
    const int toff = wid * 16384 + (g * 16 + q4) * 64 + (((p4 >> 1) ^ ((g & 1) << 1)) << 4) + 8 * (p4 & 1);
    #pragma unroll
    for (int rd = 0; rd < NHD / 2; ++rd) {
      #pragma unroll
      for (int hh = 0; hh < 2; ++hh) {
        const int h = (h0 + 2 * rd + hh) & 31;
        #pragma unroll
        for (int i = 0; i < 8; ++i)
          DMA_O(ro, (i & 1) ? dv1 : dv0, (unsigned)(h * 128 + i * 16) * (DM * 2), lbase + wid * 16384 + hh * 8192 + i * 1024);
      }
      VMWAIT();
      #pragma unroll
      for (int hh = 0; hh < 2; ++hh)
        #pragma unroll
        for (int ks = 0; ks < 2; ++ks)
          #pragma unroll
          for (int t = 0; t < 2; ++t)
            wo[2 * rd + hh][ks][t] = rd_frag<4096, 256>(lds + (toff ^ (t * 32)) + hh * 8192 + ks * 512);
      #pragma unroll
      for (int hh = 0; hh < 2; ++hh)
        WO_PIN(VW, 2 * rd + hh);
      asm volatile("s_waitcnt lgkmcnt(0)" ::: "memory");
    }
    const int lr = lane >> 3;
    #pragma unroll
    for (int i = 0; i < 16; ++i) {
      const int sw = (((lr >> 2) & 1) | ((i & 1) << 1)) << 1;
      const unsigned dv = (unsigned)(lr * (HD * 2) + b * 128 + (((lane & 7) ^ sw) << 4));
      DMA_V(rv, dv, (unsigned)(wid * 128 + i * 8) * (HD * 2), lbase + wid * 16384 + i * 1024);
    }
    VMWAIT();
    BARRIER();
    if (VW) {
      const int trow = 16 * (g >> 1) + 4 * (g & 1) + 8 * (q4 >> 1) + (q4 & 1);
      const int tsw = ((g & 1) | ((q4 >> 1) << 1)) << 1;
      const int tv = j * 32768 + trow * 128 + (((p4 >> 1) ^ tsw) << 4) + 8 * (p4 & 1);
      #pragma unroll
      for (int i = 0; i < 16; ++i) wv[i] = rd_frag<4096, 256>(lds + (tv ^ ((i & 3) * 32)) + (i >> 2) * 8192);
      V_PIN();
    }
    LBARRIER();
  }
  const unsigned cb = comm_base(ws);
  get_tag(P);
  if (wid == 0) touch(P.x, SEQ * DM * 2, (SEQ - 64 + lane) * 2048 + c * 64, lds);
  const unsigned mask = rec_go(lds + LB_RED + 4096 + 64, P, (u32x2){0u, P.tag - 1u}, wid, lane);
  const int idx = (lane & 1) ? 0xEEEE : 0x4444;
  const unsigned fb = step_base(cb, mask, 0);
  volatile LAS unsigned* tsl = (volatile LAS unsigned*)(lds + LB_RED + 4096);
  if (tid < 8) tsl[tid] = tid ? 0u : (unsigned)__builtin_amdgcn_s_memrealtime() * 4u;
  unsigned dn = 600;
  int sn = 210;
  int da = 560 * 16, gb = 350 * 16;
  if (VW) __builtin_amdgcn_s_setprio(2);
  LBARRIER();
  report(P, wid);
  TL(72);
  for (int s = 0; s < NSEG; ++s) {
    const unsigned sb = step_base(cb, mask, s);
    const int ln = opq(lane), gg = ln >> 4;
    GSDECL;
    RTC(32 + b);
    const int l16 = ln * 16;
    const int xc = 32 * (c & 1) + 2 * (ln & 15);
    const unsigned mo_v = MF_AT(c >> 1) + ((xc >> 4) * 16 + (4 * r + gg) * 2 + ((xc >> 3) & 1)) * 16 + (xc & 7) * 2;
    const unsigned tm = __builtin_amdgcn_readfirstlane(*tsl), tgt = tm + DLB;
    if (!VW) {
      LBARRIER();
      if (wid == 7) {
        const int d = (int)((unsigned)__builtin_amdgcn_s_memrealtime() * 4u - tgt);
        const bool late = rfl(tsl[3]) == tgt || d > sn + 100;
        if (!late) sn = (sn + d) >> 1;
        const unsigned to = tm + dn + (late ? max(d - sn + SN0, 0) : 0);
        if (ln == 0) tsl[1] = to;
        if (b == 0) {
          dn = (da >> 4) + DL1;
          if (ln == 0) { *tsl = to + (gb >> 4) + DL2; st8(rs, fb + FL_T(s + 1), (u32x2){to + (gb >> 4) + DL2, (dn << 8) | (s + 1)}); }
          __builtin_amdgcn_s_sleep(4);
          const unsigned va = ld4(rs, fb + FL_O(ln & 31)), vb = ld4(rs, fb + FL_B(ln));
          if (s > 2) {
            track(da, rfl(wmax(va)), s < 8 ? 5 : DCA);
            track(gb, rfl(wmax(vb)), s < 8 ? 5 : DCA);
          }
        }
      }
      if (wid == 7 && s > 0 && s + 2 < NSEG) __builtin_amdgcn_raw_buffer_store_b32(~0u, rs, mo_v, step_base(cb, mask, s + 2), AUX_SC1);
    } else {
      const unsigned mo = sb + MF_AT(4 * j), pw_a = lbase + LB_MEM + j * 512 + ln * 8, pr_a = lbase + LB_MEM + j * 2048 + ln * 8;
      lim = __builtin_amdgcn_readfirstlane(lim);
      const unsigned vo = VB_AT(b >> 1) + ((b & 1) * 4 + j) * 256;
      V_PROJ();
      GS(0); GSFLUSH(0, 4 + j);
      if (s + PD < NSEG && ln < 16) st16(rs, step_base(cb, mask, s + PD) + vo + l16, poison4());
    }
    GSFLUSH(3, 24 + wid);
    if (s + 1 == NSEG) break;
    unsigned so[6];
    #pragma unroll
    for (int i = 0; i < NHD; ++i) { const int h = (h0 + i) & 31; so[i] = __builtin_amdgcn_readfirstlane(sb + OB_AT(r, h)); }
    const unsigned sn = __builtin_amdgcn_readfirstlane(step_base(cb, mask, s + 1));
    const unsigned rw_a = lbase + LB_RED + (wid * 128 + (gg & 1) * 64 + (gg >> 1) * 16 + (ln & 15)) * 4;
    const unsigned rr_a = lbase + LB_RED + ln * 8;
    if (wid == 7) {
      if (b != 0) {
        __builtin_amdgcn_s_sleep(4);
        unsigned tm = 0;
        dn = 0;
        plan_wait(rs, fb + FL_T(s + 1), s + 1, lim, tm, dn);
        if (ln == 0) *tsl = tm;
      }
      const unsigned tgt = rfl(tsl[1]);
      T_WAIT();
    }
    lim = __builtin_amdgcn_readfirstlane(lim);
    unsigned long long t1;
    if (VW) WO_V(); else WO_W();
    if (wid == 4) {
      if (ln == 0) st4(rs, fb + FL_B(b), (unsigned)t1 * 4u - tsl[1]);
    }
    if (wid == 4) GSFLUSH(2, 16);
    TL(s);
    GSFLUSH(1, 8 + wid);
    if (wid == 0 || wid == 4) { GSFLUSH(4, 17 + (wid >> 2)); GSFLUSH(0, 19 + (wid >> 2)); }
  }
  __builtin_amdgcn_s_setprio(0);
  const int ln = opq(0);
  TL(64);
  volatile LAS int* rdy = (volatile LAS int*)(lds + 98304 + 3 * 16384);
  if (tid == 0) *rdy = 61;
  LBARRIER();
#define T_SRC(sg, i) (WS_OX + (unsigned)((sg) * NH + ((h0 + (i)) & 31)) * 8192u + r * 1024)
#define T_MMA(i, k, f) do { const u32x8 w0 = wo[i][(k) & 1][0], w1 = wo[i][(k) & 1][1]; const int e = ((k) >> 1) * 4; \
    a0 = MFMA32(((u32x4){w0[e], w0[e + 1], w0[e + 2], w0[e + 3]}), f, a0); a1 = MFMA32(((u32x4){w1[e], w1[e + 1], w1[e + 2], w1[e + 3]}), f, a1); } while (0)
#define T_DMA(sg, i) do { _Pragma("unroll") for (int k = 0; k < 4; ++k) dmas(rs, ln * 16, T_SRC(sg, i) + k * 2048, lbase + j * 24576 + (i) * 4096 + k * 1024); } while (0)
  const int cb2 = 16 * ((ln >> 4) & 1) + 8 * (ln >> 5);
  const unsigned eo = (unsigned)((NSEG - 1 - j) * 32 + r * 16 + (ln & 15)) * 2048u + (unsigned)(c * 32 + cb2) * 2u;
  u32x4 gv, xv;
  bool ahead = false;
  f32x4 k0, k1;
  #pragma unroll 1
  for (int sg = XS3; sg < NSEG; ++sg) {
    f32x4 a0 = zero4(), a1 = zero4();
    if (VW) {
      if (wid == 3) {
        for (int w = __builtin_amdgcn_readfirstlane(*rdy); w <= sg;) {
          w = max(w + 1, -wmax(-(int)flag_wait<TSL>(P, F_PRG(4, ln & 31), w + 1, ln)));
          if (ln == 0) *rdy = w;
        }
      } else while (__builtin_amdgcn_readfirstlane(*rdy) <= sg) __builtin_amdgcn_s_sleep(1);
      u32x4 of[8];
      #pragma unroll
      for (int i = 0; i < 8; ++i) of[i] = __builtin_amdgcn_raw_buffer_load_b128(rs, ln * 16 + (i & 3) * 2048, T_SRC(sg, i >> 2), AUX_SC1);
      #pragma unroll
      for (int i = 0; i < 8; ++i) T_MMA(i >> 2, i & 3, of[i]);
      if (sg == XS3 && wid < NSEG - XS3) {
        xv = *(const u32x4*)((const char*)P.x + eo);
        cnt_wait(P, C_W4(15), 4, ln);
        gv = ld16(rs, WS_G + (unsigned)((NSEG - 1 - j) * 2 + r) * 32768u + c * 1024 + blk_in(ln & 15, cb2));
      }
    } else {
      if (!ahead) {
        while (__builtin_amdgcn_readfirstlane(*rdy) <= sg) __builtin_amdgcn_s_sleep(1);
        #pragma unroll
        for (int i = 0; i < NHD; ++i) T_DMA(sg, i);
      }
      VMW(20);
      ahead = __builtin_amdgcn_readfirstlane(*rdy) > sg + 1;
      u32x4 of[2][4];
#define T_RD(i) do { _Pragma("unroll") for (int k = 0; k < 4; ++k) of[(i) & 1][k] = *(const LAS u32x4*)(lds + j * 24576 + (i) * 4096 + k * 1024 + ln * 16); } while (0)
      T_RD(0);
      #pragma unroll
      for (int i = 0; i < NHD; ++i) {
        asm volatile("s_waitcnt lgkmcnt(0)" : "+v"(of[i & 1][0]), "+v"(of[i & 1][1]), "+v"(of[i & 1][2]), "+v"(of[i & 1][3]) :: "memory");
        if (i + 1 < NHD) {
          if (ahead) VMW(16); else G_WAIT_V(16 - 4 * (i < 4 ? i : 4));
          T_RD(i + 1);
        }
        #pragma unroll
        for (int k = 0; k < 4; ++k) {
          if (ahead) dmas(rs, ln * 16, T_SRC(sg + 1, i) + k * 2048, lbase + j * 24576 + i * 4096 + k * 1024);
          T_MMA(i, k, of[i & 1][k]);
          __builtin_amdgcn_sched_barrier(0);
        }
      }
    }
    LAS f32x4* pw = (LAS f32x4*)(lds + 98304 + ((sg & 3) * 8 + wid) * 2048) + ln;
    if (wid == 0 && sg == NSEG - 1) { k0 = a0; k1 = a1; } else { pw[0] = a0; pw[64] = a1; }
  }
  if (VW && wid < NSEG - XS3) {
    LBARRIER();
    const LAS f32x4* pr = (const LAS f32x4*)(lds + 98304 + (3 - j) * 16384) + ln;
    f32x4 t[2];
    #pragma unroll
    for (int k = 0; k < 2; ++k)
      t[k] = (((wid ? pr[k * 64] : k ? k1 : k0) + pr[128 + k * 64]) + (pr[256 + k * 64] + pr[384 + k * 64])) + ((pr[512 + k * 64] + pr[640 + k * 64]) + (pr[768 + k * 64] + pr[896 + k * 64]));
    fin_out(P, eo, epi_pack(t[0], t[1]), gv, xv);
  } else LBARRIER();
}

#define DMA_S dman
#define XPC 20
#define LS_P 131072
template <int MODE>
__device__ __forceinline__ void s_role(LAS unsigned char* lds, Ptrs& P, const u16* W, int h, unsigned obase, int nseg, int fl, int wid, int s0) {
  constexpr bool XM = MODE == 2;
  constexpr unsigned RS = XM ? DM * 2 : HD * 2, SS = XM ? 262144u : 65536u;
  const int lane = opq(0);
  const int kh = wid >> 2, tp = wid & 3;
  const rsrc_t rs = WS_RSRC(P);
  const rsrc_t rw = __builtin_amdgcn_make_buffer_rsrc((void*)W, 0, HD * DM * 2, RSRC_W3);
  const unsigned lbase = (unsigned)(size_t)lds;
  u32x8 w[8][2];
  unsigned f0 = 0;
  {
    const int g = lane >> 4, q4 = (lane & 15) >> 2, p4 = lane & 3;
    const int trow = XM ? 16 * g + 4 * (q4 >> 1) + (q4 & 1) : 8 * g + q4;
#define S_SW(row) ((((row) & 1) | ((((row) >> (XM ? 2 : 1)) & 1) << 1) | ((((row) >> (XM ? 4 : 3)) & 1) << 2)) << 1)
    const int toff = trow * 256 + (((4 * tp + (p4 >> 1)) ^ S_SW(trow)) << 4) + 8 * (p4 & 1);
    unsigned dv[2];
    #pragma unroll
    for (int x = 0; x < 2; ++x) {
      const int row = 8 * wid + 4 * x + (lane >> 4);
      dv[x] = (unsigned)row * RS + (XM ? (h >> 3) * 1024 * RS + (h & 7) * 256 : h * 256) + (((lane & 15) ^ S_SW(row)) << 4);
    }
#define S_IMG(i) do { DMA_S(rw, dv[0], (i) * 64 * RS, lbase + ((i) & 7) * 16384 + wid * 2048); \
                      DMA_S(rw, dv[1], (i) * 64 * RS, lbase + ((i) & 7) * 16384 + wid * 2048 + 1024); if (XM && XPC) __builtin_amdgcn_s_sleep(XPC); } while (0)
    #pragma unroll
    for (int i = 0; i < 8; ++i) S_IMG(i);
    #pragma unroll
    for (int b = 0; b < 4; ++b) {
      if (b == 3 && !XM) { f0 = ld4(rs, FLG_OFF + F_LN0(lane & 7) * 128 + 4); get_tag(P); }
      if (b < 3) VMW(8); else VMWAIT();
      BARRIER();
      if (kh == (b >> 1)) {
        #pragma unroll
        for (int i = 4 * (b & 1); i < 4 * (b & 1) + 4; ++i) {
          w[i][0] = rd_frag<XM ? 2048 : 8192, XM ? 512 : 1024>(lds + toff + i * 16384);
          w[i][1] = rd_frag<XM ? 2048 : 8192, XM ? 512 : 1024>(lds + (toff ^ 32) + i * 16384);
        }
      }
      LBARRIER();
      if (b < 2) {
        #pragma unroll
        for (int i = 4 * b + 8; i < 4 * b + 12; ++i) S_IMG(i);
      }
    }
  }
  TL(XM ? 74 : 72);
  const unsigned l16 = lane * 16;
  volatile LAS int* rd = (volatile LAS int*)(lds + LS_P + 20480);
#define X_READY(want) do { \
    if (wid == 0) { \
      const int n_ = -wmax(-(int)flag_wait(P, F_PRG(4, (h & 24) + (lane & 7)), s0 + (want), lane)) - s0; \
      if (lane == 0) *rd = max(n_, want); \
    } \
    LBARRIER(); } while (0)
  if (MODE == 2) X_READY(2);
  else if (!XM) {
    if (__any(f0 != P.tag)) flag_wait(P, F_LN0(lane & 7), 0, lane);
  }
  unsigned sx = (XM ? WS_OX + s0 * SS + (h >> 3) * 65536 : WS_XN) + kh * 32768 + tp * 4096;
  const unsigned sd = lbase + kh * 65536 + tp * 4096;
  const unsigned xl = kh * 65536 + (XM ? l16 : blk_in(lane & 15, (lane >> 4) * 8));
  const unsigned xp = lbase + xl, pa = lbase + LS_P + tp * 4096 + l16;
  LAS f32x4* const pq = (LAS f32x4*)(lds + LS_P + tp * 4096 + l16);
  if (XM) {
    #pragma unroll
    for (int i = 0; i < 12; ++i) dmas(rs, l16, sx + (i >> 3) * SS + ((i >> 2) & 1) * 16384 + (i & 3) * 1024, sd + (i >> 2) * 16384 + (i & 3) * 1024);
    VMWAIT();
    BARRIER();
  } else {
    #pragma unroll
    for (int i = 0; i < 16; ++i) dmas(rs, l16, sx + (i >> 3) * SS + ((i >> 2) & 1) * 16384 + (i & 3) * 1024, sd + ((i >> 2) ^ 2) * 16384 + (i & 3) * 1024);
    VMW(12);
    BARRIER();
    f32x4 a[4];
    if (MODE) ONE_V(xp + 32768); else ONE_K(xp + 32768);
    pq[kh * 128] = kh ? a[0] : a[MODE ? 1 : 2]; pq[kh * 128 + 64] = kh ? a[MODE ? 2 : 1] : a[3];
    LBARRIER();
    const f32x4 a0 = (kh ? a[MODE ? 1 : 2] : a[0]) + pq[128 - kh * 128], a1 = (kh ? a[3] : a[MODE ? 2 : 1]) + pq[192 - kh * 128];
    if (MODE) st16(rs, obase + h * 8192 + (2 * tp + kh) * 1024 + l16, (u32x4){pack2(a0[0], a0[1]), pack2(a0[2], a0[3]), pack2(a1[0], a1[1]), pack2(a1[2], a1[3])});
    else st16(rs, obase + h * 8192 + kh * 4096 + tp * 1024 + l16, (u32x4){pack2(a0[0], a1[0]), pack2(a0[1], a1[1]), pack2(a0[2], a1[2]), pack2(a0[3], a1[3])});
    sx += SS;
    #pragma unroll
    for (int i = 0; i < 4; ++i) dmas(rs, l16, sx + SS + i * 1024, sd + 32768 + i * 1024);
    VMW(4);
    BARRIER();
    if (wid == 1) flag_set(P, fl, 1, lane);
    TL(62);
  }
  u32x8 xf[3], ac[2] = {};
  #pragma unroll
  for (int i = 0; i < 3; ++i) {
    const u32x4 a = *(const LAS u32x4*)(lds + xl + i * 2048), b = *(const LAS u32x4*)(lds + xl + i * 2048 + 1024);
    xf[i] = (u32x8){a[0], a[1], a[2], a[3], b[0], b[1], b[2], b[3]};
  }
  unsigned long long fm, pm;
  asm volatile("s_cmp_eq_u32 %2, 1\n\ts_cselect_b64 %0, 1, 0\n\ts_cmp_eq_u32 %3, 1\n\ts_cselect_b64 %1, -1, 0" : "=&s"(fm), "=&s"(pm) : "s"(wid), "s"(XM ? 0 : wid >> 1) : "scc");
  const unsigned fo = FLG_OFF + fl * 128, pd = lbase + LS_P + 16384;
  const unsigned po = 4 * 65536 + h * 2048 + (wid & 1) * 1024 - tp * 4096 - kh * 32768;
  unsigned oo = obase + (unsigned)h * 8192u + tp * (MODE == 1 ? 2048 : 1024) + (XM ? (s0 - 1) * 0x40000u : 0u), tmp, tmp2;
  unsigned long long em;
  int t = XM ? 0 : 1;
  if (MODE < 2) {
    #pragma unroll 1
    for (int part = 0; part < 2; ++part) {
      if (part == 1) { TL(63); if (wid == 0) setup_wait(P, lane); }
      if (MODE) S_LOOP_V(part ? nseg + 1 : 3, 0); else S_LOOP_K(part ? nseg + 1 : 3, 0);
    }
  } else {
    #pragma unroll 1
    while (t < nseg) {
      X_READY(min(t + 4, nseg));
      const int n = __builtin_amdgcn_readfirstlane(*rd), te = n >= nseg ? nseg : (n - 2) & ~1;
      TL(t);
      S_LOOP_X(te, nseg);
    }
  }
  TL(XM ? 65 : 64);
  VMWAIT();
  BARRIER();
  if (wid == 5) flag_set(P, fl, nseg, lane);
  if (XM && s0 == 0) {
    sx = WS_OX + (h >> 3) * 65536 + tp * 4096;
#define X_GET(t) do { _Pragma("unroll") for (int i = 0; i < 16; ++i) \
      dmas(rs, l16, sx + (t) * SS + ((i >> 2) & 1) * 32768 + (i >> 3) * 16384 + (i & 3) * 1024, sd + ((i >> 2) & 1) * 65536 + ((t) & 1) * 32768 + (i >> 3) * 16384 + (i & 3) * 1024); } while (0)
    bool have = false;
    const unsigned pv = FLG_OFF + F_PRG(kh ? 7 : 4, kh ? h : (h & 24) + (lane & 7)) * 128;
    if (!kh) __builtin_amdgcn_s_setprio(1);
    #pragma unroll 1
    for (int t = XS2; t < XS3; ++t) {
      if (!have && !kh) {
        flag_wait<2>(P, F_PRG(4, (h & 24) + (lane & 7)), t + 1, lane);
        X_GET(t);
        VMW(8);
      }
      BARRIER();
      f32x4 a[4];
      unsigned pg;
      ONE_X(xp + (t & 1) * 32768);
      if (wid == 5) flag_set(P, fl, t - XS2 + XS0, lane);
      have = t + 1 < XS3 && !__any(pg < (unsigned)(t + 2));
      if (!kh && lane == 0) rd[tp] = have;
      pq[kh * 128] = kh ? a[0] : a[2]; pq[kh * 128 + 64] = kh ? a[1] : a[3];
      if (have && !kh) X_GET(t + 1);
      LBARRIER();
      const f32x4 a0 = (kh ? a[2] : a[0]) + pq[128 - kh * 128], a1 = (kh ? a[3] : a[1]) + pq[192 - kh * 128];
      st16(rs, obase + (unsigned)(t * NH + h) * 8192u + kh * 4096 + tp * 1024 + l16, (u32x4){pack2(a0[0], a1[0]), pack2(a0[1], a1[1]), pack2(a0[2], a1[2]), pack2(a0[3], a1[3])});
      if (__any(rd[lane & 3] == 0)) {
        VMWAIT();
        BARRIER();
        if (wid == 5) flag_set(P, fl, t + 1 - XS2 + XS0, lane);
      } else if (!kh) VMW(9);
      asm volatile("s_waitcnt lgkmcnt(0)" ::: "memory");
      TL(t - XS2 + XS0);
    }
    __builtin_amdgcn_s_setprio(0);
    TL(65);
  }
}

#define LND 1
#define CALN 6
__device__ __forceinline__ void setup_role(LAS unsigned char* lds, Ptrs& P, int cu, int wid) {
  const int lane = opq(0), tid = wid * 64 + lane;
  const rsrc_t rs = WS_RSRC(P);
  u32x4 v[8];
  const u32x4* px = (const u32x4*)(P.x + (size_t)(cu * 32 + wid * 4 + (lane >> 4)) * DM) + (lane & 15);
  #pragma unroll
  for (int i = 0; i < 8; ++i) v[i] = __builtin_nontemporal_load(px + i * 16);
  const unsigned epo = __hip_atomic_load((gu32*)(P.ws + EPO_OFF), RLX_AGENT);
  const unsigned cb = comm_base(P.ws);
  const u32x4 z = {0u, 0u, 0u, 0u};
  const int cr = 63 - cu;
  if (wid == 0 && cr < 16) st16(rs, CNT_OFF + cr * 1024 + lane * 16, z);
  if (cr < 4) st16(rs, cb + FL_B(0) + cr * 8192 + tid * 16, z);
  if (cr == 4 && tid < 256) {
    const int g4 = tid >> 6, k = (tid >> 5) & 1 & (g4 >> 1);
    st16(rs, WS_H1 + ((g4 & 1) ? 127u * 32768u : 0u) + (g4 * 8 + ((tid >> 2) & 7)) * 1024 + blk_in((g4 & 1) ? 15 - k : k, (tid & 3) * 8), z);
  }
  #pragma unroll
  for (int i = 0; i < 2; ++i) {
    const int f = i * 54 + cr;
    if (cr < PD * NCHUNK) {
      u32x4 m = {~0u, ~0u, ~0u, ~0u};
      if (f < 4) {
        const int ks = (f >> 1) * 8 + wid;
        m = *(const u32x4*)(P.mem0 + ((lane >> 1) & 7) * DM + 64 * ks + 16 * (lane >> 4) + 8 * (lane & 1));
        m.x = canon1(m.x); m.y = canon1(m.y); m.z = canon1(m.z); m.w = canon1(m.w);
      }
      st16(rs, cb + (unsigned)(f / (NCHUNK * 2)) * CB_STEP + (f % (NCHUNK * 2)) * 8192u + tid * 16, m);
    }
  }
  const unsigned bi = blk_in((wid & 3) * 4 + (lane >> 4), (lane & 3) * 8);
  const int c0 = (lane >> 2) & 3;
  ln_rows(v);
  TL(69);
  if (cu >= 8) for (int i = 0; i < LND; ++i) __builtin_amdgcn_s_sleep(32);
  #pragma unroll
  for (int i = 0; i < 8; ++i) st16(rs, WS_XN + xn_blk(cu * 2 + (wid >> 2), 4 * i + c0) + bi, v[i]);
  P.tag = __builtin_amdgcn_readfirstlane(epo) + 1u;
  ln_rows(v);
  if (cu < 8) {
    VMWAIT();
    BARRIER();
    if (wid == 0) flag_set(P, F_LN0(cu), 1, lane);
    TL(66);
  }
  asm volatile("" ::: "memory");
  #pragma unroll
  for (int i = 0; i < 8; ++i) st16(rs, WS_XN2 + (unsigned)(cu * 2 + (wid >> 2)) * 32768u + (4 * i + c0) * 1024 + bi, v[i]);
  touch(P.w1, DM * DM * 2, ((cu & 31) * 512 + tid) * 128, lds);
  VMW(9);
  BARRIER();
  if (wid == 0) flag_set(P, F_LN0(cu), 1, lane);
  TL(70);
  if (wid == 0) VMW(2); else VMW(1);
  BARRIER();
  if (wid == 0) flag_set(P, F_LN1(cu), 1, lane);
  TL(71);
  if (cu < 32 && (wid >> 1) == 1) {
    const int id = cu * 2 + (wid & 1);
    unsigned best[2] = {65535u, 65535u};
    #pragma unroll
    for (int it = 0; it < CALN; ++it) {
      const unsigned long long c0 = __builtin_amdgcn_s_memtime();
      asm volatile("s_waitcnt lgkmcnt(0)" ::: "memory");
      if (lane == 0) st4(rs, cb + (id & 3) * CB_STEP + (30 + ((id >> 2) & 1)) * 16384u + (it & 1) * 8192u + (id >> 3) * 1024u, 0u);
      VMWAIT();
      const unsigned d = (unsigned)(__builtin_amdgcn_s_memtime() - c0);
      if (it >= 2) best[it & 1] = min(best[it & 1], d);
    }
    flag_set(P, F_CAL(id), (best[0] << 16) | best[1], lane);
  }
}

#define CPF 1
__global__ __launch_bounds__(512, 2) void mega_kernel(const u16* x, const u16* mem0, const u16* Wq, const u16* Wk, const u16* Wv, const u16* Wo,
                                                     const u16* w1, const u16* w2, const u16* w3, const u16* w4, u16* out, char* ws) {
  __shared__ __attribute__((aligned(16))) unsigned char smem[163840];
  LAS unsigned char* lds = (LAS unsigned char*)smem;
  Ptrs P;
  P.x = x; P.mem0 = mem0; P.Wq = Wq; P.Wk = Wk; P.Wv = Wv; P.Wo = Wo; P.w1 = w1; P.w2 = w2; P.w3 = w3; P.w4 = w4; P.out = out; P.ws = ws;
  const int xcd = blockIdx.x & 7, slot = blockIdx.x >> 3;
  const int wid = __builtin_amdgcn_readfirstlane(threadIdx.x >> 6);
  TL(79);
  P.tag = 0;
  if (wid == 7) {
    unsigned long long pc;
    unsigned len;
    asm volatile("s_getpc_b64 %0\nMK_BEG:\n\ts_mov_b32 %1, MK_END-MK_BEG" : "=s"(pc), "=s"(len));
    touch((const void*)pc, len, (slot * 64 + opq(0)) * 128, lds);
  }
  const int na = xcd == 2 ? 8 : 12;
  const bool isa = xcd < 3 && slot < na;
  const int ah = xcd == 2 ? slot * 4 + 2 : xcd * 16 + (slot & 3) * 4 + (slot >> 2) + (slot >> 3), bs = slot - na, br = bs & 1, bc = (xcd == 1 ? 22 : xcd * 5) + (bs >> 1);
  if (isa) {
    if (wid < 4) a_wave<true>(lds, P, ah, wid); else a_wave<false>(lds, P, ah, wid);
  } else if (xcd < 3) {
    if (wid < 4) b_wave<true>(lds, P, br, bc, wid, bs, xcd); else b_wave<false>(lds, P, br, bc, wid, bs, xcd);
  } else {
    if ((xcd & 3) == 3) {
      setup_role(lds, P, (xcd >> 2) * 32 + slot, wid);
    } else if (xcd == 6) s_role<1>(lds, P, Wv, slot, WS_VF, NSEG, F_PRG(2, slot), wid, 0);
    else s_role<0>(lds, P, xcd == 4 ? Wk : Wq, slot, xcd == 4 ? WS_KF : WS_QF, NSEG, F_PRG(xcd - 4, slot), wid, 0);
    if ((xcd & 3) == 3) mlp_role(lds, P, xcd >> 2, slot, wid);
    else s_role<2>(lds, P, Wo, slot, WS_PT, xcd == 4 ? XS1 - XS0 : xcd == 5 ? XS2 - XS1 : XS0, F_PRG(xcd + 1, slot), wid, xcd == 4 ? XS0 : xcd == 5 ? XS1 : 0);
    report(P, wid);
    fin_role(lds, P, wid);
    if (blockIdx.x == 3 && wid == 0) {
      cnt_wait(P, C_ARR, 256, threadIdx.x);
      if (threadIdx.x == 0) __hip_atomic_store((gu32*)(ws + EPO_OFF), P.tag, RLX_AGENT);
    }
  }
  asm volatile("MK_END:");
  TL(78);
}

extern "C" void kernel_launch(void* const* d_in, const int* in_sizes, int n_in, void* d_out, int out_size,
                              void* d_ws, size_t ws_size, hipStream_t stream) {
  mega_kernel<<<256, 512, 0, stream>>>((const u16*)d_in[0], (const u16*)d_in[1], (const u16*)d_in[2], (const u16*)d_in[3], (const u16*)d_in[4],
                                       (const u16*)d_in[5], (const u16*)d_in[8], (const u16*)d_in[9], (const u16*)d_in[10], (const u16*)d_in[11],
                                       (u16*)d_out, (char*)d_ws);
}
```
